```python
import torch

def _softmax(x, axis=-1):
    return torch.softmax(x, dim=axis)

def _gelu(x):
    return torch.nn.functional.gelu(x, approximate='tanh')

def _silu(x):
    return torch.nn.functional.silu(x)

def _sigmoid(x):
    return torch.sigmoid(x)

def _layernorm(x, axis=-1, eps=1e-05):
    m = x.mean(dim=axis, keepdim=True)
    v = x.var(dim=axis, keepdim=True, unbiased=False)
    return (x - m) / torch.sqrt(v + eps)

def _scatter_add(tgt, idx, src, axis=0):
    return tgt.clone().index_add_(axis, idx, src)

def _segment_sum(src, seg, n):
    out = torch.zeros((n,) + src.shape[1:], dtype=src.dtype)
    return out.index_add_(0, seg, src)

def _pad(x, pw):
    flat = tuple((v for pair in reversed(pw) for v in pair))
    return torch.nn.functional.pad(x, flat)
kernel_name = 'gated_mix__ssm_conv_h3__memory_aug_recurrent_mem_tr__prompt'

def setup_inputs():

    _w = lambda a: (lambda t: t.to(torch.bfloat16) if t.is_floating_point() else t)(torch.as_tensor(a))

    def _A_setup():
        B, S, D = (1, 2048, 1024)
        ks = 4
        return {'q': _w((lambda *s: torch.randn(*s, dtype=torch.bfloat16) * 0.05)(B, S, D)), 'k': _w((lambda *s: torch.randn(*s, dtype=torch.bfloat16) * 0.4)(B, S, D)), 'v': _w((lambda *s: torch.randn(*s, dtype=torch.bfloat16) * 0.4)(B, S, D)), 'shift_k': _w((lambda *s: torch.randn(*s, dtype=torch.bfloat16) * 0.4)(D, ks) * 0.5), 'ssm_k': _w((lambda *s: torch.randn(*s, dtype=torch.bfloat16) * 0.4)(D, S) * (1.0 / S ** 0.5)), 'ssm_D': _w((lambda *s: torch.randn(*s, dtype=torch.bfloat16) * 0.4)(D))}
    _A_in = _A_setup()

    def _B_setup():
        B, S, Dm = (1, 2048, 1024)
        H, Dh = (32, 128)
        M, seg = (8, 32)
        seg = min(seg, S)
        S = S // seg * seg
        return {'x': _w((lambda *s: torch.randn(*s, dtype=torch.bfloat16) * 0.05)(B, S, Dm)), 'mem0': _w((lambda *s: torch.randn(*s, dtype=torch.bfloat16) * 0.4)(B, M, Dm)), 'Wq': _w((lambda *s: torch.randn(*s, dtype=torch.bfloat16) * 6.4)(Dm, H, Dh) * Dm ** (-0.5)), 'Wk': _w((lambda *s: torch.randn(*s, dtype=torch.bfloat16) * 6.4)(Dm, H, Dh) * Dm ** (-0.5)), 'Wv': _w((lambda *s: torch.randn(*s, dtype=torch.bfloat16) * 0.4)(Dm, H, Dh) * Dm ** (-0.5)), 'Wo': _w((lambda *s: torch.randn(*s, dtype=torch.bfloat16) * 0.4)(H, Dh, Dm) * (H * Dh) ** (-0.5)), 'seg_len': seg}
    _B_in = _B_setup()
    return {'A_q': _A_in['q'], 'A_k': _A_in['k'], 'A_v': _A_in['v'], 'A_shift_k': _A_in['shift_k'], 'A_ssm_k': _A_in['ssm_k'], 'A_ssm_D': _A_in['ssm_D'], 'B_x': _B_in['x'], 'B_mem0': _B_in['mem0'], 'B_Wq': _B_in['Wq'], 'B_Wk': _B_in['Wk'], 'B_Wv': _B_in['Wv'], 'B_Wo': _B_in['Wo'], 'B_seg_len': _B_in['seg_len'], 'gate_w': _w((lambda *s: torch.randn(*s, dtype=torch.bfloat16) * 0.05)(1024) * 16.0)}

def reference(A_q, A_k, A_v, A_shift_k, A_ssm_k, A_ssm_D, B_x, B_mem0, B_Wq, B_Wk, B_Wv, B_Wo, B_seg_len, gate_w):

    def _A_ref(q, k, v, shift_k, ssm_k, ssm_D):
        B, S, D = q.shape
        ks = shift_k.shape[-1]
        k_pad = _pad(k, ((0, 0), (ks - 1, 0), (0, 0)))
        k_shift = torch.zeros_like(k)
        for t in range(ks):
            k_shift = k_shift + k_pad[:, t:t + S, :] * shift_k[:, ks - 1 - t][None, None, :]
        kv = k_shift * v
        L = 2 * S
        kv_pad = _pad(kv, ((0, 0), (0, S), (0, 0)))
        kf_pad = _pad(ssm_k, ((0, 0), (0, S)))
        kv_f = (lambda x, axes=(-2, -1): torch.fft.rfft2(x.float(), dim=axes))(kv_pad[:, None, :, :], axes=(1, 2))
        kf_f = (lambda x, axes=(-2, -1): torch.fft.rfft2(x.float(), dim=axes))(kf_pad[:, None, :], axes=(1, 2))
        kf_f = (lambda x, *d: x.permute(*d))(kf_f, 1, 2, 0)
        y_f = kv_f * kf_f[None, :, :, :]
        y = (lambda x, s, axes=(-2, -1): torch.fft.irfft2(x, s=s, dim=axes).to(torch.bfloat16))(y_f, (1, L), axes=(1, 2))[:, 0, :S, :]
        y = y + kv * ssm_D[None, None, :]
        return q * y

    def _B_ref(x, mem0, Wq, Wk, Wv, Wo, seg_len):
        B, S, Dm = x.shape
        H, Dh = (Wq.shape[1], Wq.shape[2])
        M = mem0.shape[1]
        scale = Dh ** (-0.5)
        n_seg = S // seg_len
        L = M + seg_len
        idx = torch.arange(L)
        mask = (idx[:, None] < M) | (idx[:, None] >= idx[None, :])
        mem = mem0
        outs = []
        for s in range(n_seg):
            xs = x[:, s * seg_len:(s + 1) * seg_len, :].clone()
            h = torch.concatenate([mem, xs], axis=1)
            q = torch.einsum('btd,dhe->bhte', h, Wq)
            k = torch.einsum('btd,dhe->bhte', h, Wk)
            v = torch.einsum('btd,dhe->bhte', h, Wv)
            scores = torch.einsum('bhqe,bhke->bhqk', q, k) * scale
            scores = torch.where(mask[None, None], scores, -1000000000.0)
            p = _softmax(scores, axis=-1)
            o = torch.einsum('bhqk,bhke->bhqe', p, v)
            o = torch.einsum('bhte,hed->btd', o, Wo)
            mem = o[:, :M, :].clone()
            outs.append(o[:, M:, :])
        return torch.concatenate(outs, axis=1)
    x = A_q
    g = _sigmoid((x * gate_w).sum(-1, keepdims=True))
    a = _A_ref(x, A_k, A_v, A_shift_k, A_ssm_k, A_ssm_D)
    b = _B_ref(x, B_mem0, B_Wq, B_Wk, B_Wv, B_Wo, B_seg_len)
    return g * a + (1 - g) * b
```

```cpp
#include <hip/hip_runtime.h>
#include <stdint.h>
#include <type_traits>
typedef unsigned short bf16_t;
using bf16x8 = __attribute__((ext_vector_type(8))) short;
using f32x4 = __attribute__((ext_vector_type(4))) float;
typedef unsigned v4u __attribute__((ext_vector_type(4)));
typedef unsigned v2u __attribute__((ext_vector_type(2)));
typedef __amdgpu_buffer_rsrc_t rsrc_t;

typedef __bf16 hbf2 __attribute__((ext_vector_type(2)));
typedef float f32x2 __attribute__((ext_vector_type(2)));
__device__ __forceinline__ unsigned pack2(float a, float b) { f32x2 v = {a, b}; return __builtin_bit_cast(unsigned, __builtin_convertvector(v, hbf2)); }
__device__ __forceinline__ float rbfh(float f) { return __uint_as_float(pack2(0.f, f)); }
__device__ __forceinline__ f32x2 rbf2(f32x2 f) { return (f32x2){rbfh(f.x), rbfh(f.y)}; }
__device__ __forceinline__ unsigned nosent(unsigned x) {
    typedef unsigned short us2 __attribute__((ext_vector_type(2)));
    const us2 a = __builtin_bit_cast(us2, x), c = {(unsigned short)0xFFFE, (unsigned short)0xFFFE}; return __builtin_bit_cast(unsigned, __builtin_elementwise_min(a, c)); }

constexpr int S = 2048, D = 1024, H = 32, DH = 128, M = 8, SEG = 32, NSEG = 64;

constexpr unsigned W_FLG = 0;
constexpr unsigned F_KVQ = W_FLG;
constexpr unsigned F_EP = W_FLG + 32768;
constexpr unsigned F_ERR = W_FLG + 32768 + 256;
constexpr unsigned F_PREP = W_FLG + 32768 + 512;
constexpr unsigned F_CAL = W_FLG + 73728;
constexpr unsigned F_CLM = W_FLG + 77824;
constexpr unsigned F_CONV = W_FLG + 33792;
constexpr unsigned F_PROG = W_FLG + 36864;
constexpr unsigned F_ATT = W_FLG + 40960;
constexpr unsigned F_RDY = W_FLG + 49152;
constexpr unsigned FLG_BYTES = 94208;
constexpr unsigned HO_KVM = W_FLG + FLG_BYTES;
constexpr unsigned W_A = HO_KVM + 64 * 32 * 4096;
constexpr unsigned W_GATE = W_A + S * D * 2;
constexpr unsigned W_QX = W_GATE + 8192;
constexpr unsigned W_KX = W_QX + S * 4096 * 2;
constexpr unsigned W_VF = W_KX + S * 4096 * 2;
constexpr unsigned W_OX = W_VF + 64 * 32 * 8192;
constexpr unsigned DBG_BYTES = 0;
constexpr unsigned W_DBG = W_OX + S * 4096 * 2;
constexpr unsigned W_U = W_DBG + DBG_BYTES;
constexpr unsigned W_TOTAL = W_U + S * D * 2;

__device__ __forceinline__ v4u ld16(rsrc_t r, unsigned off) { return __builtin_amdgcn_raw_buffer_load_b128(r, off, 0, 16); }
__device__ __forceinline__ v2u ld8(rsrc_t r, unsigned off) { return __builtin_amdgcn_raw_buffer_load_b64(r, off, 0, 16); }
__device__ __forceinline__ unsigned ld4(rsrc_t r, unsigned off) { return __builtin_amdgcn_raw_buffer_load_b32(r, off, 0, 16); }
__device__ __forceinline__ void st16(rsrc_t r, unsigned off, v4u v) { __builtin_amdgcn_raw_buffer_store_b128(v, r, off, 0, 16); }
__device__ __forceinline__ void st8(rsrc_t r, unsigned off, v2u v) { __builtin_amdgcn_raw_buffer_store_b64(v, r, off, 0, 16); }
__device__ __forceinline__ void st4(rsrc_t r, unsigned off, unsigned v) { __builtin_amdgcn_raw_buffer_store_b32(v, r, off, 0, 16); }
__device__ __forceinline__ bf16x8 as_frag(v4u v) { return __builtin_bit_cast(bf16x8, v); }
#define D1 130
#define D2 90
#define D3 64
#define LB0 16
#define LC0 18
#define LA0 20
#define TR_UP 4
#define TR_DN 2
#define L_DEC 3
#define STAG 1
#define UP_SH 2
#define EARLY_FROM 58
#define OUT_WARM 0
#define AROT 0
#define P_SLP 2
#define O_SLP 2
#define W_NT 0
#define TOL -4
#define A_SLP 0
#define PEN2 20
#define A_UP 48
#define CT0 2
#define CPF 1
#define PH_M 3
#define SKEW 0
#define SKEW1 0
#define XT 1
#define TRK 3
#define WO_NT 0
#define WO_TRK 1
#define WIN 3
#define ROT0 3
#define ROT1 2
#define ROT2 2
#define B_NT 0
#define CALM 15
#define R4M 1
#define HX_SH 0u
#define FINE_T 1
#define FINE_N 0
#define FINE_A 0
#define O_ND 12
#define PEN0 32
#define X0C 20
#define X0A 16
#define X1A 8
#define PEN 60
#define MEMBAR() asm volatile("" ::: "memory")
#define ENDPGM() asm volatile("s_endpgm" ::: "memory")
#define DRAIN() asm volatile("s_waitcnt vmcnt(0)" ::: "memory")

__device__ __forceinline__ float xor16_max(float x) { auto r = __builtin_amdgcn_permlane16_swap(__float_as_uint(x), __float_as_uint(x), false, false); return fmaxf(__uint_as_float(r[0]), __uint_as_float(r[1])); }
__device__ __forceinline__ float xor32_max(float x) { auto r = __builtin_amdgcn_permlane32_swap(__float_as_uint(x), __float_as_uint(x), false, false); return fmaxf(__uint_as_float(r[0]), __uint_as_float(r[1])); }
__device__ __forceinline__ float xor16_add(float x) { auto r = __builtin_amdgcn_permlane16_swap(__float_as_uint(x), __float_as_uint(x), false, false); return __uint_as_float(r[0]) + __uint_as_float(r[1]); }
__device__ __forceinline__ float xor32_add(float x) { auto r = __builtin_amdgcn_permlane32_swap(__float_as_uint(x), __float_as_uint(x), false, false); return __uint_as_float(r[0]) + __uint_as_float(r[1]); }

typedef short v4i16_t __attribute__((ext_vector_type(4)));
__device__ __forceinline__ v2u trb(unsigned lds_addr) { return __builtin_bit_cast(v2u, __builtin_amdgcn_ds_read_tr16_b64_v4i16((__attribute__((address_space(3))) v4i16_t*)(size_t)lds_addr)); }
__device__ __forceinline__ int otid_(int wv) { int t; asm volatile("v_mbcnt_lo_u32_b32 %0, -1, 0\n\tv_mbcnt_hi_u32_b32 %0, -1, %0" : "=v"(t)); return wv * 64 + t; }
#define otid() otid_(wv)
__device__ __forceinline__ bool wait_word(rsrc_t r, unsigned off, unsigned want, unsigned code) {
    for (unsigned spin = 0;; ++spin) { MEMBAR();
        const unsigned v = __builtin_amdgcn_readfirstlane(ld4(r, off));
        if (v >= want) return true;
        if (spin > (1u << 20)) { st4(r, F_ERR, code); return false; }
        __builtin_amdgcn_s_sleep(8); }
}

__device__ __forceinline__ bool wait_prep(rsrc_t r, int l, unsigned tag) {
    for (unsigned spin = 0;; ++spin) { MEMBAR();
        const v2u v = ld8(r, F_PREP + l * 8);
        if (__all(v.x == tag && v.y == tag)) return true;
        if (spin > (1u << 20)) { st4(r, F_ERR, 512u); return false; }
        __builtin_amdgcn_s_sleep(8); }
}
__device__ __forceinline__ bool wait_cal(rsrc_t r, int l, unsigned tag, v4u (&q)[4]) {
    for (unsigned spin = 0;; ++spin) { MEMBAR();
        bool ok = true;
#pragma unroll
        for (int i = 0; i < 4; ++i) { q[i] = ld16(r, F_CAL + l * 64 + i * 16); }
#pragma unroll
        for (int i = 0; i < 4; ++i) ok &= (q[i].x == tag) & (q[i].z == tag);
        if (__all(ok)) return true;
        if (spin > (1u << 20)) { st4(r, F_ERR, 8u); return false; }
        __builtin_amdgcn_s_sleep(4); }
}
__device__ __forceinline__ unsigned rtnow() { return (unsigned)__builtin_amdgcn_s_memrealtime(); }
__device__ __forceinline__ void wait_long(unsigned target) { for (int i = 0; i < 4096; ++i) { if ((int)(rtnow() - target) >= 0) break; __builtin_amdgcn_s_sleep(8); } }
constexpr int L_TAB = 20480;
constexpr int L_QK = 24576;
__device__ __forceinline__ unsigned img(int f) { return (unsigned)(L_QK + f * 16 + (f >> 4) * 128); }
constexpr int L_FL = 30720, L_Z = L_FL + 128, L_LOG = 31744, L_W = 32768;
constexpr int L_TOTAL = 163840;
constexpr unsigned U_END = 128;
#define PMIN 92
#define V_SLP 4
#define R_SLP 4
constexpr int WO_IN = 52;
#define HELP_D 1200
#define CAL_O 0u
#define H_SLP 0
#define CAL_N 2
#define STEP_MIN 180u

struct Params {
    const bf16_t *Aq, *Ak, *Av, *shift_k, *ssm_k, *ssm_D, *mem0, *Wq, *Wk, *Wv, *Wo, *gate_w;
    bf16_t* out; unsigned char* ws;
};
__host__ __device__ __forceinline__ unsigned hx_base(const void* ws) { const size_t a = (size_t)ws; return (unsigned)((((a + W_TOTAL + 0x7FFFFFu) >> 23) << 23) - a) + HX_SH; }

typedef short v4s __attribute__((ext_vector_type(4)));
typedef unsigned v32u __attribute__((ext_vector_type(32)));
typedef unsigned v16u __attribute__((ext_vector_type(16)));
typedef unsigned v8u __attribute__((ext_vector_type(8)));
typedef unsigned v12u __attribute__((ext_vector_type(12)));
__device__ __forceinline__ v4u mk_srd(const void* p, unsigned bytes) { const size_t a = (size_t)p; v4u d;
    d.x = __builtin_amdgcn_readfirstlane((unsigned)a); d.y = __builtin_amdgcn_readfirstlane((unsigned)(a >> 32) & 0xFFFFu); d.z = __builtin_amdgcn_readfirstlane(bytes); d.w = 0x00020000u; return d; }
__device__ __forceinline__ bf16x8 ldsa(unsigned addr) { return *(const __attribute__((address_space(3))) bf16x8*)(size_t)addr; }
__device__ __forceinline__ bool rec_block(const Params& p, rsrc_t r, unsigned char* lds, const int wv, const int b, const unsigned near, const unsigned tag) {
    const int w = wv;
    int tid = otid(), l = tid & 63, n16 = l & 15, g = l >> 4;
    const int x = (b & 7) >> 1, i32 = (b >> 3) * 2 + (b & 1);
    const unsigned hxo = hx_base(p.ws), OOB = 0xF0000000u;
    const v4u rhd = mk_srd(p.ws + hxo, U_END * 16384), wsd = mk_srd(p.ws, W_TOTAL);
    const rsrc_t rh = __builtin_amdgcn_make_buffer_rsrc(p.ws + hxo, 0, U_END * 16384, 0x00020000);
    const int G = x - 1, vi = G * 32 + i32, h = x == 0 ? i32 : vi >> 1;
    const unsigned pr_a = x == 3 ? OOB : F_PROG + h * 128 + (x == 0 ? 0 : 8 + (vi & 1) * 8);
    {
        const v4u ones = {0xFFFFFFFFu, 0xFFFFFFFFu, 0xFFFFFFFFu, 0xFFFFFFFFu};
#pragma unroll
        for (int i = 0; i < 4; ++i) st16(rh, b * 16384 + (w * 4 + i) * 1024 + l * 16, ones);
        st4(r, tid == 192 ? pr_a : OOB, 0u);
    }
    const unsigned cal_a = l == 0 ? F_CAL + b * 32 + w * 8 : OOB;
    if (x == 3) { DRAIN(); st8(r, cal_a, (v2u){tag, near}); return true; }
    v4u cpf;
    if (CPF) {
        unsigned lo, hi, l2; asm volatile("s_getpc_b64 s[98:99]\n.Lpc%=:\n\ts_add_u32 %2, s98, rec_v_code-.Lpc%=\n\ts_add_u32 %0, s98, rec_s_code-.Lpc%=\n\ts_addc_u32 %1, s99, 0" : "=s"(lo), "=s"(hi), "=s"(l2) :: "s98", "s99", "scc");
        const size_t a = (((size_t)hi << 32) | lo) + (size_t)(long long)(x ? (int)(l2 - lo) : 0) + (size_t)(((i32 * 4 + w) % 6) * 1024 + l * 16);
        asm volatile("global_load_dwordx4 %0, %1, off" : "=v"(cpf) : "v"(a & ~(size_t)15) : "memory");
    }
    const int c = n16 >> 2;
    v32u wa[8], wg[4];
    auto votes = [&](unsigned& nm) -> bool {
        v4u q[4]; if (!wait_cal(r, l, tag, q)) return false;
        typedef __attribute__((address_space(3))) int li;
        li* cnt = (li*)(size_t)(w * 256);
        cnt[l] = 0;
        __builtin_amdgcn_wave_barrier();
#pragma unroll
        for (int i = 0; i < 8; ++i) { const unsigned y = (i & 1) ? q[i >> 1].w : q[i >> 1].y; __hip_atomic_fetch_add(cnt + ((y >> 1) & 63u), (y & 1u) ? 1 : -1, __ATOMIC_RELAXED, __HIP_MEMORY_SCOPE_WAVEFRONT); }
        __builtin_amdgcn_wave_barrier();
        const int c0 = cnt[l], c4 = cnt[0] + cnt[1] + cnt[2] + cnt[3];
        nm = (unsigned)__ballot((abs(c0) >= 64 ? c0 : c4) > 0);
        return true; };
    typedef __attribute__((address_space(3))) volatile unsigned lu32;
    const unsigned kvo = HO_KVM + h * 4096 + (x ? 2048 : 0);
    if (x == 0) {
        {
            const v4u sq = mk_srd(p.Wq, 1024 * 4096 * 2), sk = mk_srd(p.Wk, 1024 * 4096 * 2);
            const unsigned m0b = L_W + w * 1024, f = (c & 1) | ((g & 1) << 1) | ((c >> 1) << 2);
            const unsigned t0 = L_W + (16 * (g >> 1) + 4 * (g & 1) + (c & 1) + 8 * (c >> 1)) * 256 + (((2 * w) ^ f) << 5) + (n16 & 3) * 8;
            v16u pa;
            pa[0] = (4 * w + g) * 8192 + h * 256 + ((n16 ^ (((g & 1) | (w << 1)) << 1)) << 4);
#pragma unroll
            for (int q = 0; q < 8; ++q) pa[1 + q] = (t0 + ((w + (q >> 1)) & 3) * 32768) ^ ((q & 1) << 5);
            pa[9] = cal_a; pa[10] = tag; pa[11] = near; pa[12] = L_W + w * 32768 + l * 16; pa[13] = 0u; pa[14] = 0u; pa[15] = 0u;
    asm volatile(
        "s_add_u32 s80, %[m0b], 0\n\t"
        "s_mov_b32 s81, 0\n\t"
        "s_mov_b32 s82, 4\n\t"
        ".Ld0%=:\n\t"
        "s_mov_b32 m0, s80\n\t"
        "s_add_u32 s80, s80, 0x8000\n\t"
        "buffer_load_dwordx4 v112, %[sq], s81 offen lds\n\t"
        "s_add_u32 m0, m0, 0x1000\n\t"
        "s_add_u32 s81, s81, 0x20000\n\t"
        "buffer_load_dwordx4 v112, %[sq], s81 offen lds\n\t"
        "s_add_u32 m0, m0, 0x1000\n\t"
        "s_add_u32 s81, s81, 0x20000\n\t"
        "buffer_load_dwordx4 v112, %[sq], s81 offen lds\n\t"
        "s_add_u32 m0, m0, 0x1000\n\t"
        "s_add_u32 s81, s81, 0x20000\n\t"
        "buffer_load_dwordx4 v112, %[sq], s81 offen lds\n\t"
        "s_add_u32 m0, m0, 0x1000\n\t"
        "s_add_u32 s81, s81, 0x20000\n\t"
        "s_sub_u32 s82, s82, 1\n\t"
        "s_cmp_lg_u32 s82, 0\n\t"
        "s_cbranch_scc1 .Ld0%=\n\t"
        "s_add_u32 s80, %[m0b], 16384\n\t"
        "s_mov_b32 s81, 0\n\t"
        "s_mov_b32 s82, 4\n\t"
        ".Ld1%=:\n\t"
        "s_mov_b32 m0, s80\n\t"
        "s_add_u32 s80, s80, 0x8000\n\t"
        "buffer_load_dwordx4 v112, %[sk], s81 offen lds\n\t"
        "s_add_u32 m0, m0, 0x1000\n\t"
        "s_add_u32 s81, s81, 0x20000\n\t"
        "buffer_load_dwordx4 v112, %[sk], s81 offen lds\n\t"
        "s_add_u32 m0, m0, 0x1000\n\t"
        "s_add_u32 s81, s81, 0x20000\n\t"
        "buffer_load_dwordx4 v112, %[sk], s81 offen lds\n\t"
        "s_add_u32 m0, m0, 0x1000\n\t"
        "s_add_u32 s81, s81, 0x20000\n\t"
        "buffer_load_dwordx4 v112, %[sk], s81 offen lds\n\t"
        "s_add_u32 m0, m0, 0x1000\n\t"
        "s_add_u32 s81, s81, 0x20000\n\t"
        "s_sub_u32 s82, s82, 1\n\t"
        "s_cmp_lg_u32 s82, 0\n\t"
        "s_cbranch_scc1 .Ld1%=\n\t"
        "s_waitcnt vmcnt(32)\n\t"
        "buffer_store_dwordx2 v[122:123], v121, %[wsd], 0 offen sc1\n\t"
        "s_waitcnt vmcnt(17)\n\t"
        "s_barrier\n\t"
        "ds_read_b64_tr_b16 v[0:1], v113 offset:0\n\t"
        "ds_read_b64_tr_b16 v[2:3], v113 offset:512\n\t"
        "ds_read_b64_tr_b16 v[4:5], v113 offset:8192\n\t"
        "ds_read_b64_tr_b16 v[6:7], v113 offset:8704\n\t"
        "ds_read_b64_tr_b16 v[8:9], v114 offset:0\n\t"
        "ds_read_b64_tr_b16 v[10:11], v114 offset:512\n\t"
        "ds_read_b64_tr_b16 v[12:13], v114 offset:8192\n\t"
        "ds_read_b64_tr_b16 v[14:15], v114 offset:8704\n\t"
        "ds_read_b64_tr_b16 a[16:17], v115 offset:0\n\t"
        "ds_read_b64_tr_b16 a[18:19], v115 offset:512\n\t"
        "ds_read_b64_tr_b16 a[20:21], v115 offset:8192\n\t"
        "ds_read_b64_tr_b16 a[22:23], v115 offset:8704\n\t"
        "ds_read_b64_tr_b16 a[24:25], v116 offset:0\n\t"
        "ds_read_b64_tr_b16 a[26:27], v116 offset:512\n\t"
        "ds_read_b64_tr_b16 a[28:29], v116 offset:8192\n\t"
        "ds_read_b64_tr_b16 a[30:31], v116 offset:8704\n\t"
        "ds_read_b64_tr_b16 a[32:33], v117 offset:0\n\t"
        "ds_read_b64_tr_b16 a[34:35], v117 offset:512\n\t"
        "ds_read_b64_tr_b16 a[36:37], v117 offset:8192\n\t"
        "ds_read_b64_tr_b16 a[38:39], v117 offset:8704\n\t"
        "ds_read_b64_tr_b16 a[40:41], v118 offset:0\n\t"
        "ds_read_b64_tr_b16 a[42:43], v118 offset:512\n\t"
        "ds_read_b64_tr_b16 a[44:45], v118 offset:8192\n\t"
        "ds_read_b64_tr_b16 a[46:47], v118 offset:8704\n\t"
        "ds_read_b64_tr_b16 a[48:49], v119 offset:0\n\t"
        "ds_read_b64_tr_b16 a[50:51], v119 offset:512\n\t"
        "ds_read_b64_tr_b16 a[52:53], v119 offset:8192\n\t"
        "ds_read_b64_tr_b16 a[54:55], v119 offset:8704\n\t"
        "ds_read_b64_tr_b16 a[56:57], v120 offset:0\n\t"
        "ds_read_b64_tr_b16 a[58:59], v120 offset:512\n\t"
        "ds_read_b64_tr_b16 a[60:61], v120 offset:8192\n\t"
        "ds_read_b64_tr_b16 a[62:63], v120 offset:8704\n\t"
        "s_waitcnt lgkmcnt(0)\n\t"
        "s_barrier\n\t"
        "s_add_u32 s80, %[m0b], 0\n\t"
        "s_mov_b32 s81, 2097152\n\t"
        "s_mov_b32 s82, 4\n\t"
        ".Ld2%=:\n\t"
        "s_mov_b32 m0, s80\n\t"
        "s_add_u32 s80, s80, 0x8000\n\t"
        "buffer_load_dwordx4 v112, %[sq], s81 offen lds\n\t"
        "s_add_u32 m0, m0, 0x1000\n\t"
        "s_add_u32 s81, s81, 0x20000\n\t"
        "buffer_load_dwordx4 v112, %[sq], s81 offen lds\n\t"
        "s_add_u32 m0, m0, 0x1000\n\t"
        "s_add_u32 s81, s81, 0x20000\n\t"
        "buffer_load_dwordx4 v112, %[sq], s81 offen lds\n\t"
        "s_add_u32 m0, m0, 0x1000\n\t"
        "s_add_u32 s81, s81, 0x20000\n\t"
        "buffer_load_dwordx4 v112, %[sq], s81 offen lds\n\t"
        "s_add_u32 m0, m0, 0x1000\n\t"
        "s_add_u32 s81, s81, 0x20000\n\t"
        "s_sub_u32 s82, s82, 1\n\t"
        "s_cmp_lg_u32 s82, 0\n\t"
        "s_cbranch_scc1 .Ld2%=\n\t"
        "s_waitcnt vmcnt(17)\n\t"
        "s_barrier\n\t"
        "ds_read_b64_tr_b16 v[16:17], v113 offset:16384\n\t"
        "ds_read_b64_tr_b16 v[18:19], v113 offset:16896\n\t"
        "ds_read_b64_tr_b16 v[20:21], v113 offset:24576\n\t"
        "ds_read_b64_tr_b16 v[22:23], v113 offset:25088\n\t"
        "ds_read_b64_tr_b16 v[24:25], v114 offset:16384\n\t"
        "ds_read_b64_tr_b16 v[26:27], v114 offset:16896\n\t"
        "ds_read_b64_tr_b16 v[28:29], v114 offset:24576\n\t"
        "ds_read_b64_tr_b16 v[30:31], v114 offset:25088\n\t"
        "ds_read_b64_tr_b16 a[80:81], v115 offset:16384\n\t"
        "ds_read_b64_tr_b16 a[82:83], v115 offset:16896\n\t"
        "ds_read_b64_tr_b16 a[84:85], v115 offset:24576\n\t"
        "ds_read_b64_tr_b16 a[86:87], v115 offset:25088\n\t"
        "ds_read_b64_tr_b16 a[88:89], v116 offset:16384\n\t"
        "ds_read_b64_tr_b16 a[90:91], v116 offset:16896\n\t"
        "ds_read_b64_tr_b16 a[92:93], v116 offset:24576\n\t"
        "ds_read_b64_tr_b16 a[94:95], v116 offset:25088\n\t"
        "ds_read_b64_tr_b16 a[96:97], v117 offset:16384\n\t"
        "ds_read_b64_tr_b16 a[98:99], v117 offset:16896\n\t"
        "ds_read_b64_tr_b16 a[100:101], v117 offset:24576\n\t"
        "ds_read_b64_tr_b16 a[102:103], v117 offset:25088\n\t"
        "ds_read_b64_tr_b16 a[104:105], v118 offset:16384\n\t"
        "ds_read_b64_tr_b16 a[106:107], v118 offset:16896\n\t"
        "ds_read_b64_tr_b16 a[108:109], v118 offset:24576\n\t"
        "ds_read_b64_tr_b16 a[110:111], v118 offset:25088\n\t"
        "ds_read_b64_tr_b16 a[112:113], v119 offset:16384\n\t"
        "ds_read_b64_tr_b16 a[114:115], v119 offset:16896\n\t"
        "ds_read_b64_tr_b16 a[116:117], v119 offset:24576\n\t"
        "ds_read_b64_tr_b16 a[118:119], v119 offset:25088\n\t"
        "ds_read_b64_tr_b16 a[120:121], v120 offset:16384\n\t"
        "ds_read_b64_tr_b16 a[122:123], v120 offset:16896\n\t"
        "ds_read_b64_tr_b16 a[124:125], v120 offset:24576\n\t"
        "ds_read_b64_tr_b16 a[126:127], v120 offset:25088\n\t"
        "s_waitcnt lgkmcnt(0)\n\t"
        "s_barrier\n\t"
        "s_add_u32 s80, %[m0b], 16384\n\t"
        "s_mov_b32 s81, 2097152\n\t"
        "s_mov_b32 s82, 4\n\t"
        ".Ld3%=:\n\t"
        "s_mov_b32 m0, s80\n\t"
        "s_add_u32 s80, s80, 0x8000\n\t"
        "buffer_load_dwordx4 v112, %[sk], s81 offen lds\n\t"
        "s_add_u32 m0, m0, 0x1000\n\t"
        "s_add_u32 s81, s81, 0x20000\n\t"
        "buffer_load_dwordx4 v112, %[sk], s81 offen lds\n\t"
        "s_add_u32 m0, m0, 0x1000\n\t"
        "s_add_u32 s81, s81, 0x20000\n\t"
        "buffer_load_dwordx4 v112, %[sk], s81 offen lds\n\t"
        "s_add_u32 m0, m0, 0x1000\n\t"
        "s_add_u32 s81, s81, 0x20000\n\t"
        "buffer_load_dwordx4 v112, %[sk], s81 offen lds\n\t"
        "s_add_u32 m0, m0, 0x1000\n\t"
        "s_add_u32 s81, s81, 0x20000\n\t"
        "s_sub_u32 s82, s82, 1\n\t"
        "s_cmp_lg_u32 s82, 0\n\t"
        "s_cbranch_scc1 .Ld3%=\n\t"
        "s_waitcnt vmcnt(16)\n\t"
        "s_barrier\n\t"
        "ds_read_b64_tr_b16 v[32:33], v113 offset:0\n\t"
        "ds_read_b64_tr_b16 v[34:35], v113 offset:512\n\t"
        "ds_read_b64_tr_b16 v[36:37], v113 offset:8192\n\t"
        "ds_read_b64_tr_b16 v[38:39], v113 offset:8704\n\t"
        "ds_read_b64_tr_b16 v[40:41], v114 offset:0\n\t"
        "ds_read_b64_tr_b16 v[42:43], v114 offset:512\n\t"
        "ds_read_b64_tr_b16 v[44:45], v114 offset:8192\n\t"
        "ds_read_b64_tr_b16 v[46:47], v114 offset:8704\n\t"
        "ds_read_b64_tr_b16 a[144:145], v115 offset:0\n\t"
        "ds_read_b64_tr_b16 a[146:147], v115 offset:512\n\t"
        "ds_read_b64_tr_b16 a[148:149], v115 offset:8192\n\t"
        "ds_read_b64_tr_b16 a[150:151], v115 offset:8704\n\t"
        "ds_read_b64_tr_b16 a[152:153], v116 offset:0\n\t"
        "ds_read_b64_tr_b16 a[154:155], v116 offset:512\n\t"
        "ds_read_b64_tr_b16 a[156:157], v116 offset:8192\n\t"
        "ds_read_b64_tr_b16 a[158:159], v116 offset:8704\n\t"
        "ds_read_b64_tr_b16 a[160:161], v117 offset:0\n\t"
        "ds_read_b64_tr_b16 a[162:163], v117 offset:512\n\t"
        "ds_read_b64_tr_b16 a[164:165], v117 offset:8192\n\t"
        "ds_read_b64_tr_b16 a[166:167], v117 offset:8704\n\t"
        "ds_read_b64_tr_b16 a[168:169], v118 offset:0\n\t"
        "ds_read_b64_tr_b16 a[170:171], v118 offset:512\n\t"
        "ds_read_b64_tr_b16 a[172:173], v118 offset:8192\n\t"
        "ds_read_b64_tr_b16 a[174:175], v118 offset:8704\n\t"
        "ds_read_b64_tr_b16 a[176:177], v119 offset:0\n\t"
        "ds_read_b64_tr_b16 a[178:179], v119 offset:512\n\t"
        "ds_read_b64_tr_b16 a[180:181], v119 offset:8192\n\t"
        "ds_read_b64_tr_b16 a[182:183], v119 offset:8704\n\t"
        "ds_read_b64_tr_b16 a[184:185], v120 offset:0\n\t"
        "ds_read_b64_tr_b16 a[186:187], v120 offset:512\n\t"
        "ds_read_b64_tr_b16 a[188:189], v120 offset:8192\n\t"
        "ds_read_b64_tr_b16 a[190:191], v120 offset:8704\n\t"
        "s_waitcnt lgkmcnt(0)\n\t"
        "s_barrier\n\t"
        "s_add_u32 s80, %[m0b], 0\n\t"
        "s_mov_b32 s81, 4194304\n\t"
        "s_mov_b32 s82, 4\n\t"
        ".Ld4%=:\n\t"
        "s_mov_b32 m0, s80\n\t"
        "s_add_u32 s80, s80, 0x8000\n\t"
        "buffer_load_dwordx4 v112, %[sq], s81 offen lds\n\t"
        "s_add_u32 m0, m0, 0x1000\n\t"
        "s_add_u32 s81, s81, 0x20000\n\t"
        "buffer_load_dwordx4 v112, %[sq], s81 offen lds\n\t"
        "s_add_u32 m0, m0, 0x1000\n\t"
        "s_add_u32 s81, s81, 0x20000\n\t"
        "buffer_load_dwordx4 v112, %[sq], s81 offen lds\n\t"
        "s_add_u32 m0, m0, 0x1000\n\t"
        "s_add_u32 s81, s81, 0x20000\n\t"
        "buffer_load_dwordx4 v112, %[sq], s81 offen lds\n\t"
        "s_add_u32 m0, m0, 0x1000\n\t"
        "s_add_u32 s81, s81, 0x20000\n\t"
        "s_sub_u32 s82, s82, 1\n\t"
        "s_cmp_lg_u32 s82, 0\n\t"
        "s_cbranch_scc1 .Ld4%=\n\t"
        "s_waitcnt vmcnt(16)\n\t"
        "s_barrier\n\t"
        "ds_read_b64_tr_b16 v[48:49], v113 offset:16384\n\t"
        "ds_read_b64_tr_b16 v[50:51], v113 offset:16896\n\t"
        "ds_read_b64_tr_b16 v[52:53], v113 offset:24576\n\t"
        "ds_read_b64_tr_b16 v[54:55], v113 offset:25088\n\t"
        "ds_read_b64_tr_b16 v[56:57], v114 offset:16384\n\t"
        "ds_read_b64_tr_b16 v[58:59], v114 offset:16896\n\t"
        "ds_read_b64_tr_b16 v[60:61], v114 offset:24576\n\t"
        "ds_read_b64_tr_b16 v[62:63], v114 offset:25088\n\t"
        "ds_read_b64_tr_b16 a[208:209], v115 offset:16384\n\t"
        "ds_read_b64_tr_b16 a[210:211], v115 offset:16896\n\t"
        "ds_read_b64_tr_b16 a[212:213], v115 offset:24576\n\t"
        "ds_read_b64_tr_b16 a[214:215], v115 offset:25088\n\t"
        "ds_read_b64_tr_b16 a[216:217], v116 offset:16384\n\t"
        "ds_read_b64_tr_b16 a[218:219], v116 offset:16896\n\t"
        "ds_read_b64_tr_b16 a[220:221], v116 offset:24576\n\t"
        "ds_read_b64_tr_b16 a[222:223], v116 offset:25088\n\t"
        "ds_read_b64_tr_b16 a[224:225], v117 offset:16384\n\t"
        "ds_read_b64_tr_b16 a[226:227], v117 offset:16896\n\t"
        "ds_read_b64_tr_b16 a[228:229], v117 offset:24576\n\t"
        "ds_read_b64_tr_b16 a[230:231], v117 offset:25088\n\t"
        "ds_read_b64_tr_b16 a[232:233], v118 offset:16384\n\t"
        "ds_read_b64_tr_b16 a[234:235], v118 offset:16896\n\t"
        "ds_read_b64_tr_b16 a[236:237], v118 offset:24576\n\t"
        "ds_read_b64_tr_b16 a[238:239], v118 offset:25088\n\t"
        "ds_read_b64_tr_b16 a[240:241], v119 offset:16384\n\t"
        "ds_read_b64_tr_b16 a[242:243], v119 offset:16896\n\t"
        "ds_read_b64_tr_b16 a[244:245], v119 offset:24576\n\t"
        "ds_read_b64_tr_b16 a[246:247], v119 offset:25088\n\t"
        "ds_read_b64_tr_b16 a[248:249], v120 offset:16384\n\t"
        "ds_read_b64_tr_b16 a[250:251], v120 offset:16896\n\t"
        "ds_read_b64_tr_b16 a[252:253], v120 offset:24576\n\t"
        "ds_read_b64_tr_b16 a[254:255], v120 offset:25088\n\t"
        "s_waitcnt lgkmcnt(0)\n\t"
        "s_barrier\n\t"
        "s_add_u32 s80, %[m0b], 16384\n\t"
        "s_mov_b32 s81, 4194304\n\t"
        "s_mov_b32 s82, 4\n\t"
        ".Ld5%=:\n\t"
        "s_mov_b32 m0, s80\n\t"
        "s_add_u32 s80, s80, 0x8000\n\t"
        "buffer_load_dwordx4 v112, %[sk], s81 offen lds\n\t"
        "s_add_u32 m0, m0, 0x1000\n\t"
        "s_add_u32 s81, s81, 0x20000\n\t"
        "buffer_load_dwordx4 v112, %[sk], s81 offen lds\n\t"
        "s_add_u32 m0, m0, 0x1000\n\t"
        "s_add_u32 s81, s81, 0x20000\n\t"
        "buffer_load_dwordx4 v112, %[sk], s81 offen lds\n\t"
        "s_add_u32 m0, m0, 0x1000\n\t"
        "s_add_u32 s81, s81, 0x20000\n\t"
        "buffer_load_dwordx4 v112, %[sk], s81 offen lds\n\t"
        "s_add_u32 m0, m0, 0x1000\n\t"
        "s_add_u32 s81, s81, 0x20000\n\t"
        "s_sub_u32 s82, s82, 1\n\t"
        "s_cmp_lg_u32 s82, 0\n\t"
        "s_cbranch_scc1 .Ld5%=\n\t"
        "s_waitcnt vmcnt(16)\n\t"
        "s_barrier\n\t"
        "ds_read_b64_tr_b16 v[128:129], v113 offset:0\n\t"
        "ds_read_b64_tr_b16 v[130:131], v113 offset:512\n\t"
        "ds_read_b64_tr_b16 v[132:133], v113 offset:8192\n\t"
        "ds_read_b64_tr_b16 v[134:135], v113 offset:8704\n\t"
        "ds_read_b64_tr_b16 v[136:137], v114 offset:0\n\t"
        "ds_read_b64_tr_b16 v[138:139], v114 offset:512\n\t"
        "ds_read_b64_tr_b16 v[140:141], v114 offset:8192\n\t"
        "ds_read_b64_tr_b16 v[142:143], v114 offset:8704\n\t"
        "ds_read_b64_tr_b16 v[144:145], v115 offset:0\n\t"
        "ds_read_b64_tr_b16 v[146:147], v115 offset:512\n\t"
        "ds_read_b64_tr_b16 v[148:149], v115 offset:8192\n\t"
        "ds_read_b64_tr_b16 v[150:151], v115 offset:8704\n\t"
        "ds_read_b64_tr_b16 v[152:153], v116 offset:0\n\t"
        "ds_read_b64_tr_b16 v[154:155], v116 offset:512\n\t"
        "ds_read_b64_tr_b16 v[156:157], v116 offset:8192\n\t"
        "ds_read_b64_tr_b16 v[158:159], v116 offset:8704\n\t"
        "ds_read_b64_tr_b16 v[160:161], v117 offset:0\n\t"
        "ds_read_b64_tr_b16 v[162:163], v117 offset:512\n\t"
        "ds_read_b64_tr_b16 v[164:165], v117 offset:8192\n\t"
        "ds_read_b64_tr_b16 v[166:167], v117 offset:8704\n\t"
        "ds_read_b64_tr_b16 v[168:169], v118 offset:0\n\t"
        "ds_read_b64_tr_b16 v[170:171], v118 offset:512\n\t"
        "ds_read_b64_tr_b16 v[172:173], v118 offset:8192\n\t"
        "ds_read_b64_tr_b16 v[174:175], v118 offset:8704\n\t"
        "ds_read_b64_tr_b16 v[176:177], v119 offset:0\n\t"
        "ds_read_b64_tr_b16 v[178:179], v119 offset:512\n\t"
        "ds_read_b64_tr_b16 v[180:181], v119 offset:8192\n\t"
        "ds_read_b64_tr_b16 v[182:183], v119 offset:8704\n\t"
        "ds_read_b64_tr_b16 v[184:185], v120 offset:0\n\t"
        "ds_read_b64_tr_b16 v[186:187], v120 offset:512\n\t"
        "ds_read_b64_tr_b16 v[188:189], v120 offset:8192\n\t"
        "ds_read_b64_tr_b16 v[190:191], v120 offset:8704\n\t"
        "s_waitcnt lgkmcnt(0)\n\t"
        "s_barrier\n\t"
        "s_add_u32 s80, %[m0b], 0\n\t"
        "s_mov_b32 s81, 6291456\n\t"
        "s_mov_b32 s82, 4\n\t"
        ".Ld6%=:\n\t"
        "s_mov_b32 m0, s80\n\t"
        "s_add_u32 s80, s80, 0x8000\n\t"
        "buffer_load_dwordx4 v112, %[sq], s81 offen lds\n\t"
        "s_add_u32 m0, m0, 0x1000\n\t"
        "s_add_u32 s81, s81, 0x20000\n\t"
        "buffer_load_dwordx4 v112, %[sq], s81 offen lds\n\t"
        "s_add_u32 m0, m0, 0x1000\n\t"
        "s_add_u32 s81, s81, 0x20000\n\t"
        "buffer_load_dwordx4 v112, %[sq], s81 offen lds\n\t"
        "s_add_u32 m0, m0, 0x1000\n\t"
        "s_add_u32 s81, s81, 0x20000\n\t"
        "buffer_load_dwordx4 v112, %[sq], s81 offen lds\n\t"
        "s_add_u32 m0, m0, 0x1000\n\t"
        "s_add_u32 s81, s81, 0x20000\n\t"
        "s_sub_u32 s82, s82, 1\n\t"
        "s_cmp_lg_u32 s82, 0\n\t"
        "s_cbranch_scc1 .Ld6%=\n\t"
        "s_waitcnt vmcnt(16)\n\t"
        "s_barrier\n\t"
        "ds_read_b64_tr_b16 v[192:193], v113 offset:16384\n\t"
        "ds_read_b64_tr_b16 v[194:195], v113 offset:16896\n\t"
        "ds_read_b64_tr_b16 v[196:197], v113 offset:24576\n\t"
        "ds_read_b64_tr_b16 v[198:199], v113 offset:25088\n\t"
        "ds_read_b64_tr_b16 v[200:201], v114 offset:16384\n\t"
        "ds_read_b64_tr_b16 v[202:203], v114 offset:16896\n\t"
        "ds_read_b64_tr_b16 v[204:205], v114 offset:24576\n\t"
        "ds_read_b64_tr_b16 v[206:207], v114 offset:25088\n\t"
        "ds_read_b64_tr_b16 v[208:209], v115 offset:16384\n\t"
        "ds_read_b64_tr_b16 v[210:211], v115 offset:16896\n\t"
        "ds_read_b64_tr_b16 v[212:213], v115 offset:24576\n\t"
        "ds_read_b64_tr_b16 v[214:215], v115 offset:25088\n\t"
        "ds_read_b64_tr_b16 v[216:217], v116 offset:16384\n\t"
        "ds_read_b64_tr_b16 v[218:219], v116 offset:16896\n\t"
        "ds_read_b64_tr_b16 v[220:221], v116 offset:24576\n\t"
        "ds_read_b64_tr_b16 v[222:223], v116 offset:25088\n\t"
        "ds_read_b64_tr_b16 v[224:225], v117 offset:16384\n\t"
        "ds_read_b64_tr_b16 v[226:227], v117 offset:16896\n\t"
        "ds_read_b64_tr_b16 v[228:229], v117 offset:24576\n\t"
        "ds_read_b64_tr_b16 v[230:231], v117 offset:25088\n\t"
        "ds_read_b64_tr_b16 v[232:233], v118 offset:16384\n\t"
        "ds_read_b64_tr_b16 v[234:235], v118 offset:16896\n\t"
        "ds_read_b64_tr_b16 v[236:237], v118 offset:24576\n\t"
        "ds_read_b64_tr_b16 v[238:239], v118 offset:25088\n\t"
        "ds_read_b64_tr_b16 v[240:241], v119 offset:16384\n\t"
        "ds_read_b64_tr_b16 v[242:243], v119 offset:16896\n\t"
        "ds_read_b64_tr_b16 v[244:245], v119 offset:24576\n\t"
        "ds_read_b64_tr_b16 v[246:247], v119 offset:25088\n\t"
        "ds_read_b64_tr_b16 v[248:249], v120 offset:16384\n\t"
        "ds_read_b64_tr_b16 v[250:251], v120 offset:16896\n\t"
        "ds_read_b64_tr_b16 v[252:253], v120 offset:24576\n\t"
        "ds_read_b64_tr_b16 v[254:255], v120 offset:25088\n\t"
        "s_waitcnt lgkmcnt(0)\n\t"
        "s_barrier\n\t"
        "s_add_u32 s80, %[m0b], 16384\n\t"
        "s_mov_b32 s81, 6291456\n\t"
        "s_mov_b32 s82, 4\n\t"
        ".Ld7%=:\n\t"
        "s_mov_b32 m0, s80\n\t"
        "s_add_u32 s80, s80, 0x8000\n\t"
        "buffer_load_dwordx4 v112, %[sk], s81 offen lds\n\t"
        "s_add_u32 m0, m0, 0x1000\n\t"
        "s_add_u32 s81, s81, 0x20000\n\t"
        "buffer_load_dwordx4 v112, %[sk], s81 offen lds\n\t"
        "s_add_u32 m0, m0, 0x1000\n\t"
        "s_add_u32 s81, s81, 0x20000\n\t"
        "buffer_load_dwordx4 v112, %[sk], s81 offen lds\n\t"
        "s_add_u32 m0, m0, 0x1000\n\t"
        "s_add_u32 s81, s81, 0x20000\n\t"
        "buffer_load_dwordx4 v112, %[sk], s81 offen lds\n\t"
        "s_add_u32 m0, m0, 0x1000\n\t"
        "s_add_u32 s81, s81, 0x20000\n\t"
        "s_sub_u32 s82, s82, 1\n\t"
        "s_cmp_lg_u32 s82, 0\n\t"
        "s_cbranch_scc1 .Ld7%=\n\t"
        "s_waitcnt vmcnt(16)\n\t"
        "s_barrier\n\t"
        "ds_read_b64_tr_b16 a[0:1], v113 offset:0\n\t"
        "ds_read_b64_tr_b16 a[2:3], v113 offset:512\n\t"
        "ds_read_b64_tr_b16 a[4:5], v113 offset:8192\n\t"
        "ds_read_b64_tr_b16 a[6:7], v113 offset:8704\n\t"
        "ds_read_b64_tr_b16 a[8:9], v114 offset:0\n\t"
        "ds_read_b64_tr_b16 a[10:11], v114 offset:512\n\t"
        "ds_read_b64_tr_b16 a[12:13], v114 offset:8192\n\t"
        "ds_read_b64_tr_b16 a[14:15], v114 offset:8704\n\t"
        "ds_read_b64_tr_b16 v[64:65], v115 offset:0\n\t"
        "ds_read_b64_tr_b16 v[66:67], v115 offset:512\n\t"
        "ds_read_b64_tr_b16 v[68:69], v115 offset:8192\n\t"
        "ds_read_b64_tr_b16 v[70:71], v115 offset:8704\n\t"
        "ds_read_b64_tr_b16 v[72:73], v116 offset:0\n\t"
        "ds_read_b64_tr_b16 v[74:75], v116 offset:512\n\t"
        "ds_read_b64_tr_b16 v[76:77], v116 offset:8192\n\t"
        "ds_read_b64_tr_b16 v[78:79], v116 offset:8704\n\t"
        "ds_read_b64_tr_b16 v[80:81], v117 offset:0\n\t"
        "ds_read_b64_tr_b16 v[82:83], v117 offset:512\n\t"
        "ds_read_b64_tr_b16 v[84:85], v117 offset:8192\n\t"
        "ds_read_b64_tr_b16 v[86:87], v117 offset:8704\n\t"
        "ds_read_b64_tr_b16 v[88:89], v118 offset:0\n\t"
        "ds_read_b64_tr_b16 v[90:91], v118 offset:512\n\t"
        "ds_read_b64_tr_b16 v[92:93], v118 offset:8192\n\t"
        "ds_read_b64_tr_b16 v[94:95], v118 offset:8704\n\t"
        "ds_read_b64_tr_b16 a[128:129], v119 offset:0\n\t"
        "ds_read_b64_tr_b16 a[130:131], v119 offset:512\n\t"
        "ds_read_b64_tr_b16 a[132:133], v119 offset:8192\n\t"
        "ds_read_b64_tr_b16 a[134:135], v119 offset:8704\n\t"
        "ds_read_b64_tr_b16 a[136:137], v120 offset:0\n\t"
        "ds_read_b64_tr_b16 a[138:139], v120 offset:512\n\t"
        "ds_read_b64_tr_b16 a[140:141], v120 offset:8192\n\t"
        "ds_read_b64_tr_b16 a[142:143], v120 offset:8704\n\t"
        "s_waitcnt lgkmcnt(0)\n\t"
        "s_barrier\n\t"
        "ds_write_b128 v124, v[0:3] offset:0\n\t"
        "ds_write_b128 v124, v[4:7] offset:1024\n\t"
        "ds_write_b128 v124, v[8:11] offset:2048\n\t"
        "ds_write_b128 v124, v[12:15] offset:3072\n\t"
        "ds_write_b128 v124, v[16:19] offset:4096\n\t"
        "ds_write_b128 v124, v[20:23] offset:5120\n\t"
        "ds_write_b128 v124, v[24:27] offset:6144\n\t"
        "ds_write_b128 v124, v[28:31] offset:7168\n\t"
        "ds_write_b128 v124, v[32:35] offset:8192\n\t"
        "ds_write_b128 v124, v[36:39] offset:9216\n\t"
        "ds_write_b128 v124, v[40:43] offset:10240\n\t"
        "ds_write_b128 v124, v[44:47] offset:11264\n\t"
        "ds_write_b128 v124, v[48:51] offset:12288\n\t"
        "ds_write_b128 v124, v[52:55] offset:13312\n\t"
        "ds_write_b128 v124, v[56:59] offset:14336\n\t"
        "ds_write_b128 v124, v[60:63] offset:15360\n\t"
        "s_waitcnt lgkmcnt(0)\n\t"
        "s_waitcnt vmcnt(0)\n\t"
        "s_barrier\n\t"
        "ds_read_b64_tr_b16 a[64:65], v113 offset:16384\n\t"
        "ds_read_b64_tr_b16 a[66:67], v113 offset:16896\n\t"
        "ds_read_b64_tr_b16 a[68:69], v113 offset:24576\n\t"
        "ds_read_b64_tr_b16 a[70:71], v113 offset:25088\n\t"
        "ds_read_b64_tr_b16 a[72:73], v114 offset:16384\n\t"
        "ds_read_b64_tr_b16 a[74:75], v114 offset:16896\n\t"
        "ds_read_b64_tr_b16 a[76:77], v114 offset:24576\n\t"
        "ds_read_b64_tr_b16 a[78:79], v114 offset:25088\n\t"
        "ds_read_b64_tr_b16 v[0:1], v115 offset:16384\n\t"
        "ds_read_b64_tr_b16 v[2:3], v115 offset:16896\n\t"
        "ds_read_b64_tr_b16 v[4:5], v115 offset:24576\n\t"
        "ds_read_b64_tr_b16 v[6:7], v115 offset:25088\n\t"
        "ds_read_b64_tr_b16 v[8:9], v116 offset:16384\n\t"
        "ds_read_b64_tr_b16 v[10:11], v116 offset:16896\n\t"
        "ds_read_b64_tr_b16 v[12:13], v116 offset:24576\n\t"
        "ds_read_b64_tr_b16 v[14:15], v116 offset:25088\n\t"
        "ds_read_b64_tr_b16 v[16:17], v117 offset:16384\n\t"
        "ds_read_b64_tr_b16 v[18:19], v117 offset:16896\n\t"
        "ds_read_b64_tr_b16 v[20:21], v117 offset:24576\n\t"
        "ds_read_b64_tr_b16 v[22:23], v117 offset:25088\n\t"
        "ds_read_b64_tr_b16 v[24:25], v118 offset:16384\n\t"
        "ds_read_b64_tr_b16 v[26:27], v118 offset:16896\n\t"
        "ds_read_b64_tr_b16 v[28:29], v118 offset:24576\n\t"
        "ds_read_b64_tr_b16 v[30:31], v118 offset:25088\n\t"
        "ds_read_b64_tr_b16 a[192:193], v119 offset:16384\n\t"
        "ds_read_b64_tr_b16 a[194:195], v119 offset:16896\n\t"
        "ds_read_b64_tr_b16 a[196:197], v119 offset:24576\n\t"
        "ds_read_b64_tr_b16 a[198:199], v119 offset:25088\n\t"
        "ds_read_b64_tr_b16 a[200:201], v120 offset:16384\n\t"
        "ds_read_b64_tr_b16 a[202:203], v120 offset:16896\n\t"
        "ds_read_b64_tr_b16 a[204:205], v120 offset:24576\n\t"
        "ds_read_b64_tr_b16 a[206:207], v120 offset:25088\n\t"
        "s_waitcnt lgkmcnt(0)\n\t"
        "s_barrier\n\t"
        "ds_write_b128 v124, v[64:67] offset:16384\n\t"
        "ds_write_b128 v124, v[68:71] offset:17408\n\t"
        "ds_write_b128 v124, v[72:75] offset:18432\n\t"
        "ds_write_b128 v124, v[76:79] offset:19456\n\t"
        "ds_write_b128 v124, v[80:83] offset:20480\n\t"
        "ds_write_b128 v124, v[84:87] offset:21504\n\t"
        "ds_write_b128 v124, v[88:91] offset:22528\n\t"
        "ds_write_b128 v124, v[92:95] offset:23552\n\t"
        "ds_write_b128 v124, v[0:3] offset:24576\n\t"
        "ds_write_b128 v124, v[4:7] offset:25600\n\t"
        "ds_write_b128 v124, v[8:11] offset:26624\n\t"
        "ds_write_b128 v124, v[12:15] offset:27648\n\t"
        "ds_write_b128 v124, v[16:19] offset:28672\n\t"
        "ds_write_b128 v124, v[20:23] offset:29696\n\t"
        "ds_write_b128 v124, v[24:27] offset:30720\n\t"
        "ds_write_b128 v124, v[28:31] offset:31744\n\t"
        "s_waitcnt lgkmcnt(0)"
        : "={a[0:31]}"(wa[0]), "={a[32:63]}"(wa[1]), "={a[64:95]}"(wa[2]), "={a[96:127]}"(wa[3]), "={a[128:159]}"(wa[4]), "={a[160:191]}"(wa[5]), "={a[192:223]}"(wa[6]), "={a[224:255]}"(wa[7]), "={v[128:159]}"(wg[0]), "={v[160:191]}"(wg[1]), "={v[192:223]}"(wg[2]), "={v[224:255]}"(wg[3])
        : [sq] "s"(sq), [sk] "s"(sk), [wsd] "s"(wsd), [m0b] "s"(m0b), "{v[112:127]}"(pa)
        : "memory", "scc", "m0", "s80", "s81", "s82", "v0", "v1", "v2", "v3", "v4", "v5", "v6", "v7", "v8", "v9", "v10", "v11", "v12", "v13", "v14", "v15", "v16", "v17", "v18", "v19", "v20", "v21", "v22", "v23", "v24", "v25", "v26", "v27", "v28", "v29", "v30", "v31", "v32", "v33", "v34", "v35", "v36", "v37", "v38", "v39", "v40", "v41", "v42", "v43", "v44", "v45", "v46", "v47", "v48", "v49", "v50", "v51", "v52", "v53", "v54", "v55", "v56", "v57", "v58", "v59", "v60", "v61", "v62", "v63", "v64", "v65", "v66", "v67", "v68", "v69", "v70", "v71", "v72", "v73", "v74", "v75", "v76", "v77", "v78", "v79", "v80", "v81", "v82", "v83", "v84", "v85", "v86", "v87", "v88", "v89", "v90", "v91", "v92", "v93", "v94", "v95");
        }
        asm volatile("" : "+v"(cpf));
        tid = otid(); l = tid & 63; n16 = l & 15; g = l >> 4;
        unsigned nm; if (!votes(nm)) return false;
        const unsigned nW = (nm & 1u) << 13, nA = nW, nS = nW + 6 * 16384 - w * 1024, nC = 32 * 16384 + ((nm & 2u) << 12);
        const unsigned b0 = (64 + (h >> 2)) * 16384 + ((nm & 4u) << 11) + (h & 3) * 2048 - w * 256 - L_QK, b1 = b0 + 8 * 16384;
        const unsigned kx = W_KX + h * 8192, vx = W_VF + h * 8192 + w * 1024, kf = F_KVQ + h * 64, swo = 6 * 16384 + h * 8;
        const unsigned aq = img(8 * g + (n16 >> 2)) + (n16 & 1) * 8;
        v8u pc = {(unsigned)tid * 16, (unsigned)(L_W + w * 32768 + l * 16), (n16 & 2) ? 0xEEEEu : 0x4444u, aq,
                  (unsigned)(((w + 1) & 3) * 1024 + l * 16), (unsigned)(((w + 2) & 3) * 1024 + l * 16), (unsigned)(((w + 3) & 3) * 1024 + l * 16), img(32 * w + n16) + g * 4};
        v2u pd = {(n16 & 2) ? (unsigned)L_Z : aq + w * 768, (n16 & 2) ? aq + w * 768 : (unsigned)L_Z};
        *(__attribute__((address_space(3))) v4u*)(size_t)(L_TAB + tid * 16) = (v4u){(unsigned)(L_QK + 3072 + g * 384 + n16 * 8),
                  n16 < 8 ? (unsigned)(((n16 >> 2) * 4 + (h >> 3)) * 16384 + (h & 7) * 1024 + w * 256 + (g >> 1) * 128 + (n16 & 2) * 32 + (g & 1) * 32 + (n16 & 1) * 16) : OOB,
                  (unsigned)((32 * w + 16 * (g & 1) + n16) * 16 + (g >> 1) * 8), (unsigned)(L_QK + 3072 + w * 768 + ((n16 & 1) + 2 * (n16 >> 2)) * 32 + g * 8 + (n16 & 2) * 2)};
        if (tid < 64) ((lu32*)(size_t)L_FL)[tid] = 0u;
    asm volatile(
        "rec_s_code:\n\t"
        "s_mov_b32 s36, 0xf0000000\n\t"
        "s_mov_b32 s38, 0xff00ff00\n\t"
        "s_mov_b32 s39, 0xff00ff00\n\t"
        "s_mov_b32 s40, 0\n\t"
        "s_mov_b32 s37, 0\n\t"
        "s_brev_b32 s54, 1\n\t"
        "s_mov_b32 s46, %c[iD1]\n\t"
        "s_mov_b32 s47, %c[iD2]\n\t"
        "s_mov_b32 s48, 1\n\t"
        "s_mov_b32 s50, %c[LC]\n\t"
        "s_mov_b32 s51, %c[LA]\n\t"
        "s_mov_b32 s52, %c[E1]\n\t"
        "s_mov_b32 s53, %c[E2]\n\t"
        "s_mov_b32 s43, 0\n\t"
        "s_mov_b32 s61, %[b0]\n\t"
        "s_mov_b32 s62, %[b1]\n\t"
        "s_mov_b32 s75, 31744\n\t"
        "s_and_b32 s80, s40, 3\n\t"
        "s_lshl_b32 s80, s80, 17\n\t"
        "s_add_u32 s57, s80, %[nA]\n\t"
        "s_add_u32 s58, s57, 0x1000\n\t"
        "s_add_u32 s59, s57, 0x4000\n\t"
        "s_add_u32 s60, s57, 0x5000\n\t"
        "s_and_b32 s80, s40, 3\n\t"
        "s_lshl_b32 s80, s80, 17\n\t"
        "s_add_u32 s63, s80, %[nC]\n\t"
        "s_add_u32 s66, s80, %[nS]\n\t"
        "s_xor_b32 s81, s80, 0x40000\n\t"
        "s_add_u32 s68, s81, %[nW]\n\t"
        "s_add_u32 s81, s80, 0x20000\n\t"
        "s_and_b32 s81, s81, 0x60000\n\t"
        "s_add_u32 s67, s81, %[nW]\n\t"
        "s_xor_b32 s81, s81, 0x40000\n\t"
        "s_add_u32 s65, s81, %[nC]\n\t"
        "s_add_u32 s74, s81, %[nW]\n\t"
        "s_lshl_b32 s72, s40, 17\n\t"
        "s_add_u32 s72, s72, %[kvo]\n\t"
        "s_mov_b32 s64, %[kf]\n\t"
        "buffer_load_dwordx2 v[48:49], off, %[wsd], s64 sc1\n\t"
        "s_waitcnt vmcnt(0)\n\t"
        "s_cmp_eq_u32 s64, s36\n\t"
        "s_cbranch_scc1 .Lk10%=\n\t"
        "s_mov_b32 s97, 0\n\t"
        ".Lk00%=:\n\t"
        "v_readfirstlane_b32 s82, v48\n\t"
        "v_readfirstlane_b32 s83, v49\n\t"
        "s_xor_b32 s82, s82, %[tag]\n\t"
        "s_xor_b32 s83, s83, %[tag]\n\t"
        "s_or_b32 s82, s82, s83\n\t"
        "s_cbranch_scc0 .Lk10%=\n\t"
        "s_sleep 3\n\t"
        "buffer_load_dwordx2 v[48:49], off, %[wsd], s64 sc1\n\t"
        "s_add_u32 s97, s97, 1\n\t"
        "s_cmp_lt_u32 s97, 0x200000\n\t"
        "s_cbranch_scc0 .Lgu%=\n\t"
        "s_waitcnt vmcnt(0)\n\t"
        "s_branch .Lk00%=\n\t"
        ".Lk10%=:\n\t"
        "s_add_u32 s80, s40, 0\n\t"
        "s_min_u32 s80, s80, 63\n\t"
        "s_lshl_b32 s80, s80, 18\n\t"
        "s_add_u32 s69, s80, %[kx]\n\t"
        "s_add_u32 s70, s69, 0x1000\n\t"
        "s_add_u32 s71, s80, %[vx]\n\t"
        "s_add_u32 s80, s40, 1\n\t"
        "s_lshl_b32 s81, s80, 9\n\t"
        "s_add_u32 s81, s81, %[kf]\n\t"
        "s_and_b32 s80, s80, 0x43\n\t"
        "s_cselect_b32 s64, s36, s81\n\t"
        "buffer_load_dwordx4 v[104:107], v120, %[wsd], s69 offen sc1\n\t"
        "buffer_load_dwordx4 v[108:111], v120, %[wsd], s70 offen sc1\n\t"
        "buffer_load_dwordx4 v[112:115], v120, %[wsd], s71 offen offset:0 sc1\n\t"
        "buffer_load_dwordx4 v[116:119], v120, %[wsd], s71 offen offset:1024 sc1\n\t"
        "ds_read_b128 v[16:19], v121 offset:0\n\t"
        "ds_read_b128 v[20:23], v121 offset:1024\n\t"
        "ds_read_b128 v[24:27], v121 offset:2048\n\t"
        "ds_read_b128 v[28:31], v121 offset:3072\n\t"
        "ds_read_b128 v[32:35], v121 offset:4096\n\t"
        "ds_read_b128 v[36:39], v121 offset:5120\n\t"
        "ds_read_b128 v[40:43], v121 offset:6144\n\t"
        "ds_read_b128 v[44:47], v121 offset:7168\n\t"
        "ds_read_b128 v[56:59], v121 offset:8192\n\t"
        "ds_read_b128 v[60:63], v121 offset:9216\n\t"
        "ds_read_b128 v[64:67], v121 offset:10240\n\t"
        "ds_read_b128 v[68:71], v121 offset:11264\n\t"
        "ds_read_b128 v[72:75], v121 offset:12288\n\t"
        "ds_read_b128 v[76:79], v121 offset:13312\n\t"
        "ds_read_b128 v[80:83], v121 offset:14336\n\t"
        "ds_read_b128 v[84:87], v121 offset:15360\n\t"
        "s_add_u32 s80, s40, 1\n\t"
        "s_min_u32 s80, s80, 63\n\t"
        "s_lshl_b32 s80, s80, 18\n\t"
        "s_add_u32 s69, s80, %[kx]\n\t"
        "s_add_u32 s70, s69, 0x1000\n\t"
        "s_add_u32 s71, s80, %[vx]\n\t"
        "s_add_u32 s80, s40, 2\n\t"
        "s_lshl_b32 s81, s80, 9\n\t"
        "s_add_u32 s81, s81, %[kf]\n\t"
        "s_and_b32 s80, s80, 0x43\n\t"
        "s_cselect_b32 s64, s36, s81\n\t"
        "v_mov_b32_e32 v88, 0\n\t"
        "v_mov_b32_e32 v89, 0\n\t"
        "v_mov_b32_e32 v90, 0\n\t"
        "v_mov_b32_e32 v91, 0\n\t"
        "v_mov_b32_e32 v92, 0\n\t"
        "v_mov_b32_e32 v93, 0\n\t"
        "v_mov_b32_e32 v94, 0\n\t"
        "v_mov_b32_e32 v95, 0\n\t"
        "v_mov_b32_e32 v96, 0\n\t"
        "v_mov_b32_e32 v97, 0\n\t"
        "v_mov_b32_e32 v98, 0\n\t"
        "v_mov_b32_e32 v99, 0\n\t"
        "v_mov_b32_e32 v100, 0\n\t"
        "v_mov_b32_e32 v101, 0\n\t"
        "v_mov_b32_e32 v102, 0\n\t"
        "v_mov_b32_e32 v103, 0\n\t"
        "s_waitcnt vmcnt(0) lgkmcnt(0)\n\t"
        "s_barrier\n\t"
        "s_memrealtime s[98:99]\n\t"
        "v_mov_b32_e32 v49, 0\n\t"
        "v_mov_b32_e32 v50, 0\n\t"
        "v_lshlrev_b32_e32 v51, 6, v120\n\t"
        "s_add_u32 s80, %[nW], %[swo]\n\t"
        "s_cmp_eq_u32 %[w], 0\n\t"
        "s_cselect_b32 s73, s80, s36\n\t"
        "s_cselect_b32 s74, %[pra], s36\n\t"
        "s_waitcnt lgkmcnt(0)\n\t"
        "s_mov_b32 s41, s98\n\t"
        "v_mov_b32_e32 v48, s98\n\t"
        ".Lloop%=:\n\t"
        "s_mov_b32 s97, 0\n\t"
        "s_movk_i32 s96, 0x1000\n\t"
        ".Lwa%=:\n\t"
        "s_memrealtime s[98:99]\n\t"
        "s_addk_i32 s96, -1\n\t"
        "s_cmp_lt_i32 s96, 0\n\t"
        "s_cbranch_scc1 .Lta%=\n\t"
        "s_waitcnt lgkmcnt(0)\n\t"
        "s_sub_i32 s98, s98, s41\n\t"
        "s_cmp_lt_i32 s98, %c[iFT]\n\t"
        "s_cbranch_scc1 .Lwa%=\n\t"
        "s_cmp_ge_i32 s98, 0\n\t"
        "s_cbranch_scc1 .Lxa%=\n\t"
        ".Lqa%=:\n\t"
        "s_nop %c[iFN]\n\t"
        "s_addk_i32 s98, 1\n\t"
        "s_cmp_lt_i32 s98, 0\n\t"
        "s_cbranch_scc1 .Lqa%=\n\t"
        ".Lxa%=:\n\t"
        "buffer_load_dwordx4 v[0:3], v120, %[srd], s57 offen sc1\n\t"
        "s_nop 7\n\t"
        "buffer_load_dwordx4 v[4:7], v120, %[srd], s58 offen sc1\n\t"
        "s_nop 7\n\t"
        "buffer_load_dwordx4 v[8:11], v120, %[srd], s59 offen sc1\n\t"
        "s_nop 7\n\t"
        "buffer_load_dwordx4 v[12:15], v120, %[srd], s60 offen sc1\n\t"
        "s_nop 15\n\t"
        "s_nop 15\n\t"
        "s_nop 3\n\t"
        "s_mov_b64 exec, 7\n\t"
        "buffer_store_dwordx2 v[48:49], v51, %[srd], s73 offen sc1\n\t"
        "s_mov_b64 exec, 1\n\t"
        "buffer_store_dword v50, off, %[wsd], s74 sc1\n\t"
        "s_mov_b64 exec, -1\n\t"
        ".Lgo%=:\n\t"
        "s_waitcnt vmcnt(5)\n\t"
        "v_smfmac_f32_16x16x64_bf16 v[88:91], v[0:3], v[16:23], v122\n\t"
        "ds_write_b128 v120, v[0:3] offset:0\n\t"
        "v_smfmac_f32_16x16x64_bf16 v[92:95], v[0:3], v[24:31], v122\n\t"
        "v_smfmac_f32_16x16x64_bf16 v[96:99], v[0:3], v[32:39], v122\n\t"
        "v_smfmac_f32_16x16x64_bf16 v[100:103], v[0:3], v[40:47], v122\n\t"
        "s_waitcnt vmcnt(4)\n\t"
        "v_smfmac_f32_16x16x64_bf16 v[88:91], v[4:7], v[56:63], v122\n\t"
        "ds_write_b128 v120, v[4:7] offset:4096\n\t"
        "v_smfmac_f32_16x16x64_bf16 v[92:95], v[4:7], v[64:71], v122\n\t"
        "v_smfmac_f32_16x16x64_bf16 v[96:99], v[4:7], v[72:79], v122\n\t"
        "v_smfmac_f32_16x16x64_bf16 v[100:103], v[4:7], v[80:87], v122\n\t"
        "s_waitcnt vmcnt(3)\n\t"
        "v_smfmac_f32_16x16x64_bf16 v[88:91], v[8:11], v[128:135], v122\n\t"
        "ds_write_b128 v120, v[8:11] offset:8192\n\t"
        "v_smfmac_f32_16x16x64_bf16 v[92:95], v[8:11], v[136:143], v122\n\t"
        "s_waitcnt lgkmcnt(1)\n\t"
        "s_barrier\n\t"
        "v_smfmac_f32_16x16x64_bf16 v[96:99], v[8:11], v[192:199], v122\n\t"
        "ds_read_b128 v[16:19], v124 offset:0\n\t"
        "v_smfmac_f32_16x16x64_bf16 v[100:103], v[8:11], v[200:207], v122\n\t"
        "ds_read_b128 v[20:23], v125 offset:0\n\t"
        "s_waitcnt vmcnt(2)\n\t"
        "v_smfmac_f32_16x16x64_bf16 v[88:91], v[12:15], a[0:7], v122\n\t"
        "ds_write_b128 v120, v[12:15] offset:12288\n\t"
        "v_smfmac_f32_16x16x64_bf16 v[92:95], v[12:15], a[8:15], v122\n\t"
        "v_smfmac_f32_16x16x64_bf16 v[96:99], v[12:15], a[64:71], v122\n\t"
        "ds_read_b128 v[24:27], v126 offset:0\n\t"
        "v_smfmac_f32_16x16x64_bf16 v[100:103], v[12:15], a[72:79], v122\n\t"
        "ds_read_b128 v[28:31], v124 offset:4096\n\t"
        "s_waitcnt lgkmcnt(4)\n\t"
        "v_smfmac_f32_16x16x64_bf16 v[88:91], v[16:19], a[16:23], v122\n\t"
        "ds_read_b128 v[32:35], v125 offset:4096\n\t"
        "v_smfmac_f32_16x16x64_bf16 v[92:95], v[16:19], a[24:31], v122\n\t"
        "ds_read_b128 v[36:39], v126 offset:4096\n\t"
        "s_waitcnt lgkmcnt(5)\n\t"
        "v_smfmac_f32_16x16x64_bf16 v[88:91], v[20:23], a[32:39], v122\n\t"
        "ds_read_b128 v[72:75], v121 offset:16384\n\t"
        "v_smfmac_f32_16x16x64_bf16 v[92:95], v[20:23], a[40:47], v122\n\t"
        "s_waitcnt lgkmcnt(5)\n\t"
        "s_barrier\n\t"
        "ds_read_b128 v[76:79], v121 offset:17408\n\t"
        "s_waitcnt lgkmcnt(5)\n\t"
        "v_smfmac_f32_16x16x64_bf16 v[88:91], v[24:27], a[48:55], v122\n\t"
        "ds_read_b128 v[0:3], v124 offset:12288\n\t"
        "v_smfmac_f32_16x16x64_bf16 v[92:95], v[24:27], a[56:63], v122\n\t"
        "ds_read_b128 v[80:83], v121 offset:18432\n\t"
        "s_waitcnt lgkmcnt(6)\n\t"
        "v_smfmac_f32_16x16x64_bf16 v[88:91], v[28:31], a[144:151], v122\n\t"
        "ds_read_b128 v[84:87], v121 offset:19456\n\t"
        "v_smfmac_f32_16x16x64_bf16 v[92:95], v[28:31], a[152:159], v122\n\t"
        "ds_read_b128 v[40:43], v124 offset:8192\n\t"
        "s_waitcnt lgkmcnt(7)\n\t"
        "v_smfmac_f32_16x16x64_bf16 v[88:91], v[32:35], a[160:167], v122\n\t"
        "ds_read_b128 v[44:47], v125 offset:8192\n\t"
        "v_mov_b32_e32 v12, 0x3f803f80\n\t"
        "v_smfmac_f32_16x16x64_bf16 v[92:95], v[32:35], a[168:175], v122\n\t"
        "ds_read_b128 v[56:59], v121 offset:20480\n\t"
        "v_mov_b32_e32 v13, 0x3f803f80\n\t"
        "s_waitcnt lgkmcnt(8)\n\t"
        "v_smfmac_f32_16x16x64_bf16 v[88:91], v[36:39], a[176:183], v122\n\t"
        "ds_read_b128 v[60:63], v121 offset:21504\n\t"
        "v_mov_b32_e32 v14, 0x3f803f80\n\t"
        "v_smfmac_f32_16x16x64_bf16 v[92:95], v[36:39], a[184:191], v122\n\t"
        "ds_read_b128 v[4:7], v125 offset:12288\n\t"
        "v_mov_b32_e32 v15, 0x3f803f80\n\t"
        "s_waitcnt lgkmcnt(8)\n\t"
        "s_waitcnt lgkmcnt(7)\n\t"
        "v_smfmac_f32_16x16x64_bf16 v[88:91], v[0:3], v[72:79], v122\n\t"
        "ds_read_b128 v[64:67], v121 offset:22528\n\t"
        "s_waitcnt lgkmcnt(6)\n\t"
        "v_smfmac_f32_16x16x64_bf16 v[92:95], v[0:3], v[80:87], v122\n\t"
        "ds_read_b128 v[68:71], v121 offset:23552\n\t"
        "s_waitcnt lgkmcnt(6)\n\t"
        "v_smfmac_f32_16x16x64_bf16 v[88:91], v[40:43], v[144:151], v122\n\t"
        "ds_read_b128 v[48:51], v126 offset:8192\n\t"
        "v_smfmac_f32_16x16x64_bf16 v[92:95], v[40:43], v[152:159], v122\n\t"
        "ds_read_b128 v[8:11], v126 offset:12288\n\t"
        "s_waitcnt lgkmcnt(7)\n\t"
        "v_smfmac_f32_16x16x64_bf16 v[88:91], v[44:47], v[160:167], v122\n\t"
        "ds_read_b128 v[72:75], v121 offset:24576\n\t"
        "v_smfmac_f32_16x16x64_bf16 v[92:95], v[44:47], v[168:175], v122\n\t"
        "ds_read_b128 v[76:79], v121 offset:25600\n\t"
        "s_waitcnt lgkmcnt(7)\n\t"
        "s_waitcnt lgkmcnt(6)\n\t"
        "v_smfmac_f32_16x16x64_bf16 v[88:91], v[4:7], v[56:63], v122\n\t"
        "ds_read_b128 v[80:83], v121 offset:26624\n\t"
        "s_waitcnt lgkmcnt(5)\n\t"
        "v_smfmac_f32_16x16x64_bf16 v[92:95], v[4:7], v[64:71], v122\n\t"
        "ds_read_b128 v[84:87], v121 offset:27648\n\t"
        "s_waitcnt lgkmcnt(5)\n\t"
        "v_smfmac_f32_16x16x64_bf16 v[88:91], v[48:51], v[176:183], v122\n\t"
        "v_mov_b32_e32 v64, 0\n\t"
        "v_smfmac_f32_16x16x64_bf16 v[92:95], v[48:51], v[184:191], v122\n\t"
        "v_mov_b32_e32 v65, 0\n\t"
        "s_waitcnt lgkmcnt(4)\n\t"
        "v_smfmac_f32_16x16x64_bf16 v[88:91], v[8:11], a[128:135], v122\n\t"
        "v_mov_b32_e32 v66, 0\n\t"
        "v_smfmac_f32_16x16x64_bf16 v[92:95], v[8:11], a[136:143], v122\n\t"
        "v_mov_b32_e32 v67, 0\n\t"
        "s_waitcnt lgkmcnt(2)\n\t"
        "v_smfmac_f32_16x16x64_bf16 v[96:99], v[0:3], v[72:79], v122\n\t"
        "ds_read_b128 v[72:75], v121 offset:28672\n\t"
        "s_waitcnt lgkmcnt(1)\n\t"
        "v_smfmac_f32_16x16x64_bf16 v[100:103], v[0:3], v[80:87], v122\n\t"
        "ds_read_b128 v[76:79], v121 offset:29696\n\t"
        "v_smfmac_f32_16x16x64_bf16 v[96:99], v[16:19], a[80:87], v122\n\t"
        "v_add_f32_e32 v88, v88, v90\n\t"
        "v_add_f32_e32 v89, v89, v91\n\t"
        "v_smfmac_f32_16x16x64_bf16 v[100:103], v[16:19], a[88:95], v122\n\t"
        "v_cvt_pk_bf16_f32 v90, v88, v89\n\t"
        "v_add_f32_e32 v92, v92, v94\n\t"
        "v_smfmac_f32_16x16x64_bf16 v[96:99], v[20:23], a[96:103], v122\n\t"
        "ds_write_b32 v127, v90\n\t"
        "v_add_f32_e32 v93, v93, v95\n\t"
        "v_cvt_pk_bf16_f32 v91, v92, v93\n\t"
        "v_smfmac_f32_16x16x64_bf16 v[100:103], v[20:23], a[104:111], v122\n\t"
        "ds_write_b32 v127, v91 offset:384\n\t"
        "v_cmp_u_f32_e32 vcc, v88, v89\n\t"
        "v_smfmac_f32_16x16x64_bf16 v[96:99], v[24:27], a[112:119], v122\n\t"
        "ds_read_b64_tr_b16 v[56:57], v52 offset:0\n\t"
        "ds_read_b64_tr_b16 v[58:59], v52 offset:64\n\t"
        "v_smfmac_f32_16x16x64_bf16 v[100:103], v[24:27], a[120:127], v122\n\t"
        "ds_read_b64_tr_b16 v[60:61], v53 offset:0\n\t"
        "ds_read_b64_tr_b16 v[62:63], v53 offset:64\n\t"
        "v_smfmac_f32_16x16x64_bf16 v[96:99], v[28:31], a[208:215], v122\n\t"
        "s_cbranch_vccnz .Lf1%=\n\t"
        ".Lr1%=:\n\t"
        "ds_read_b128 v[80:83], v121 offset:30720\n\t"
        "v_smfmac_f32_16x16x64_bf16 v[100:103], v[28:31], a[216:223], v122\n\t"
        "ds_read_b128 v[84:87], v121 offset:31744\n\t"
        "v_mov_b32_e32 v91, 0\n\t"
        "v_smfmac_f32_16x16x64_bf16 v[96:99], v[32:35], a[224:231], v122\n\t"
        "v_mov_b32_e32 v94, 0\n\t"
        "v_smfmac_f32_16x16x64_bf16 v[100:103], v[32:35], a[232:239], v122\n\t"
        "ds_read_b128 v[32:35], v120 offset:20480\n\t"
        "v_smfmac_f32_16x16x64_bf16 v[96:99], v[36:39], a[240:247], v122\n\t"
        "s_waitcnt lgkmcnt(3)\n\t"
        "s_barrier\n\t"
        "v_mfma_f32_16x16x32_bf16 v[68:71], v[104:107], v[56:59], 0\n\t"
        "ds_read_b64_tr_b16 v[16:17], v123 offset:0\n\t"
        "v_mfma_f32_16x16x32_bf16 v[68:71], v[108:111], v[60:63], v[68:71]\n\t"
        "ds_read_b64_tr_b16 v[18:19], v123 offset:64\n\t"
        "v_smfmac_f32_16x16x64_bf16 v[100:103], v[36:39], a[248:255], v122\n\t"
        "ds_read_b64_tr_b16 v[20:21], v123 offset:768\n\t"
        "v_smfmac_f32_16x16x64_bf16 v[96:99], v[8:11], a[192:199], v122\n\t"
        "ds_read_b64_tr_b16 v[22:23], v123 offset:832\n\t"
        "v_smfmac_f32_16x16x64_bf16 v[100:103], v[8:11], a[200:207], v122\n\t"
        "s_nop 1\n\t"
        "ds_write_b128 v120, v[68:71] offset:16384\n\t"
        "v_smfmac_f32_16x16x64_bf16 v[96:99], v[4:7], v[72:79], v122\n\t"
        "ds_read_b64_tr_b16 v[24:25], v123 offset:1536\n\t"
        "s_waitcnt lgkmcnt(7)\n\t"
        "v_smfmac_f32_16x16x64_bf16 v[100:103], v[4:7], v[80:87], v122\n\t"
        "ds_read_b64_tr_b16 v[26:27], v123 offset:1600\n\t"
        "v_smfmac_f32_16x16x64_bf16 v[96:99], v[40:43], v[208:215], v122\n\t"
        "buffer_load_dword v88, v127, %[srd], s61 offen offset:0 sc1\n\t"
        "v_smfmac_f32_16x16x64_bf16 v[100:103], v[40:43], v[216:223], v122\n\t"
        "buffer_load_dword v89, v127, %[srd], s61 offen offset:256 sc1\n\t"
        "v_smfmac_f32_16x16x64_bf16 v[96:99], v[44:47], v[224:231], v122\n\t"
        "s_waitcnt lgkmcnt(2)\n\t"
        "s_barrier\n\t"
        "ds_read_b128 v[36:39], v124 offset:16384\n\t"
        "v_smfmac_f32_16x16x64_bf16 v[100:103], v[44:47], v[232:239], v122\n\t"
        "ds_read_b128 v[56:59], v125 offset:16384\n\t"
        "v_smfmac_f32_16x16x64_bf16 v[96:99], v[48:51], v[240:247], v122\n\t"
        "ds_read_b128 v[60:63], v126 offset:16384\n\t"
        "v_smfmac_f32_16x16x64_bf16 v[100:103], v[48:51], v[248:255], v122\n\t"
        "ds_read_b64_tr_b16 v[28:29], v123 offset:2304\n\t"
        "ds_read_b64_tr_b16 v[30:31], v123 offset:2368\n\t"
        "s_waitcnt lgkmcnt(4)\n\t"
        "s_cmp_eq_u32 s40, 63\n\t"
        "v_add_f32_e32 v68, v68, v36\n\t"
        "v_add_f32_e32 v69, v69, v37\n\t"
        "v_add_f32_e32 v70, v70, v38\n\t"
        "v_add_f32_e32 v71, v71, v39\n\t"
        "v_add_f32_e32 v96, v96, v98\n\t"
        "v_add_f32_e32 v97, v97, v99\n\t"
        "s_waitcnt lgkmcnt(2)\n\t"
        "v_cvt_pk_bf16_f32 v50, v96, v97\n\t"
        "v_pk_add_f32 v[100:101], v[100:101], v[102:103]\n\t"
        "ds_write_b32 v35, v50\n\t"
        "v_cvt_pk_bf16_f32 v51, v100, v101\n\t"
        "ds_write_b32 v35, v51 offset:384\n\t"
        "v_pk_add_f32 v[56:57], v[56:57], v[60:61]\n\t"
        "s_cbranch_scc1 .Lend%=\n\t"
        "v_pk_add_f32 v[58:59], v[58:59], v[62:63]\n\t"
        "v_pk_add_f32 v[68:69], v[68:69], v[56:57]\n\t"
        "v_pk_add_f32 v[70:71], v[70:71], v[58:59]\n\t"
        "v_max3_f32 v2, v68, v69, v70\n\t"
        "v_max_f32_e32 v2, v2, v71\n\t"
        "s_nop 1\n\t"
        "v_max_f32_dpp v3, v2, v2 row_ror:8 row_mask:0xf bank_mask:0xf\n\t"
        "v_max_f32_dpp v1, v2, v2 row_ror:8 row_mask:0xf bank_mask:0xf\n\t"
        "s_nop 1\n\t"
        "v_permlane16_swap_b32_e32 v3, v1\n\t"
        "v_max_f32_e32 v2, v3, v1\n\t"
        "v_max_f32_e32 v3, v3, v1\n\t"
        "s_waitcnt lgkmcnt(0)\n\t"
        "s_barrier\n\t"
        "ds_read_b64_tr_b16 v[72:73], v32 offset:0\n\t"
        "v_permlane32_swap_b32_e32 v2, v3\n\t"
        "ds_read_b64_tr_b16 v[74:75], v32 offset:128\n\t"
        "v_max_f32_e32 v0, v2, v3\n\t"
        "ds_read_b64_tr_b16 v[76:77], v32 offset:1536\n\t"
        "v_mul_f32_e32 v2, 0xbe0293ee, v0\n\t"
        "ds_read_b64_tr_b16 v[78:79], v32 offset:1664\n\t"
        "v_fmamk_f32 v68, v68, 0x3e0293ee, v2\n\t"
        "v_fmamk_f32 v69, v69, 0x3e0293ee, v2\n\t"
        "v_fmamk_f32 v70, v70, 0x3e0293ee, v2\n\t"
        "v_fmamk_f32 v71, v71, 0x3e0293ee, v2\n\t"
        "v_exp_f32_e32 v68, v68\n\t"
        "v_exp_f32_e32 v69, v69\n\t"
        "v_exp_f32_e32 v70, v70\n\t"
        "s_waitcnt lgkmcnt(2)\n\t"
        "v_smfmac_f32_16x16x64_bf16 v[64:67], v[72:75], v[16:23], v122\n\t"
        "s_waitcnt lgkmcnt(0)\n\t"
        "v_smfmac_f32_16x16x64_bf16 v[64:67], v[76:79], v[24:31], v122\n\t"
        "v_cvt_pk_bf16_f32 v36, v68, v69\n\t"
        "v_exp_f32_e32 v71, v71\n\t"
        "s_nop 0\n\t"
        "v_mov_b32_dpp v38, v36 row_ror:8 row_mask:0xf bank_mask:0xf\n\t"
        "v_cvt_pk_bf16_f32 v37, v70, v71\n\t"
        "s_nop 1\n\t"
        "v_mov_b32_dpp v39, v37 row_ror:8 row_mask:0xf bank_mask:0xf\n\t"
        "v_pk_add_f32 v[64:65], v[64:65], v[66:67]\n\t"
        "v_max3_f32 v3, v0, v64, v65\n\t"
        "v_max3_f32 v1, v0, v64, v65\n\t"
        "v_mfma_f32_16x16x32_bf16 v[40:43], v[112:115], v[36:39], 0\n\t"
        "s_nop 0\n\t"
        "v_permlane16_swap_b32_e32 v3, v1\n\t"
        "v_max_f32_e32 v2, v3, v1\n\t"
        "v_max_f32_e32 v3, v3, v1\n\t"
        "v_mfma_f32_16x16x32_bf16 v[44:47], v[116:119], v[36:39], 0\n\t"
        "s_nop 0\n\t"
        "v_permlane32_swap_b32_e32 v2, v3\n\t"
        "v_max_f32_e32 v2, v2, v3\n\t"
        "v_mfma_f32_16x16x32_bf16 v[96:99], v[12:15], v[36:39], 0\n\t"
        "v_mul_f32_e32 v2, 0xbe0293ee, v2\n\t"
        "v_fmamk_f32 v10, v0, 0x3e0293ee, v2\n\t"
        "v_fmamk_f32 v64, v64, 0x3e0293ee, v2\n\t"
        "v_exp_f32_e32 v10, v10\n\t"
        "v_fmamk_f32 v65, v65, 0x3e0293ee, v2\n\t"
        "v_exp_f32_e32 v64, v64\n\t"
        "s_nop 1\n\t"
        "v_mul_f32_e32 v96, v96, v10\n\t"
        "v_exp_f32_e32 v65, v65\n\t"
        "v_pk_mul_f32 v[40:41], v[40:41], v[10:11] op_sel_hi:[1,0]\n\t"
        "v_cvt_pk_bf16_f32 v90, v64, v65\n\t"
        "v_pk_mul_f32 v[42:43], v[42:43], v[10:11] op_sel_hi:[1,0]\n\t"
        "v_cvt_pk_bf16_f32 v95, v64, v65\n\t"
        "v_pk_mul_f32 v[44:45], v[44:45], v[10:11] op_sel_hi:[1,0]\n\t"
        "v_mfma_f32_16x16x16_bf16 v[96:99], v[12:13], v[90:91], v[96:99]\n\t"
        "s_waitcnt vmcnt(0)\n\t"
        "v_mul_f32_e32 v46, v46, v10\n\t"
        "v_mul_f32_e32 v47, v47, v10\n\t"
        "v_mfma_f32_16x16x16_bf16 v[40:43], v[88:89], v[90:91], v[40:43]\n\t"
        "v_max_u32_e32 v54, v88, v89\n\t"
        "v_cmp_eq_u32_e32 vcc, -1, v54\n\t"
        "v_mfma_f32_16x16x16_bf16 v[44:47], v[88:89], v[94:95], v[44:47]\n\t"
        "s_nop 0\n\t"
        "v_rcp_f32_e32 v2, v96\n\t"
        "s_nop 0\n\t"
        "s_cbranch_vccnz .Lfb%=\n\t"
        ".Lrb%=:\n\t"
        "s_nop 0\n\t"
        "v_pk_mul_f32 v[40:41], v[40:41], v[2:3] op_sel_hi:[1,0]\n\t"
        "v_pk_mul_f32 v[42:43], v[42:43], v[2:3] op_sel_hi:[1,0]\n\t"
        "v_cvt_pk_bf16_f32 v100, v40, v41\n\t"
        "v_pk_mul_f32 v[44:45], v[44:45], v[2:3] op_sel_hi:[1,0]\n\t"
        "v_pk_mul_f32 v[46:47], v[46:47], v[2:3] op_sel_hi:[1,0]\n\t"
        "v_cvt_pk_bf16_f32 v101, v42, v43\n\t"
        "v_cvt_pk_bf16_f32 v102, v44, v45\n\t"
        "v_min_u32_e32 v100, -2, v100\n\t"
        "v_cvt_pk_bf16_f32 v103, v46, v47\n\t"
        "v_min_u32_e32 v102, -2, v102\n\t"
        "buffer_store_dwordx4 v[100:103], v33, %[srd], s63 offen sc1\n\t"
        "s_memrealtime s[76:77]\n\t"
        "v_mov_b32_e32 v8, -1\n\t"
        "v_mov_b32_e32 v9, -1\n\t"
        "v_mov_b32_e32 v10, -1\n\t"
        "v_mov_b32_e32 v11, -1\n\t"
        "v_permlane16_swap_b32_e32 v50, v51\n\t"
        "s_add_u32 s40, s40, 1\n\t"
        "s_sleep %c[HSLP]\n\t"
        "buffer_load_dwordx4 v[84:87], v120, %[srd], s66 offen sc1\n\t"
        "buffer_load_dwordx2 v[48:49], off, %[wsd], s64 sc1\n\t"
        "buffer_store_dwordx2 v[50:51], v34, %[wsd], s72 offen sc1\n\t"
        "buffer_store_dwordx4 v[8:11], v33, %[srd], s65 offen sc1\n\t"
        "v_lshlrev_b32_e32 v15, 6, v120\n\t"
        "s_add_u32 s80, s68, %[swo]\n\t"
        "s_cmp_eq_u32 %[w], 0\n\t"
        "s_cselect_b32 s80, s80, s36\n\t"
        "s_mov_b64 exec, 7\n\t"
        "buffer_store_dwordx2 v[8:9], v15, %[srd], s80 offen sc1\n\t"
        "s_mov_b64 exec, -1\n\t"
        "buffer_load_dwordx4 v[104:107], v120, %[wsd], s69 offen sc1\n\t"
        "buffer_load_dwordx4 v[108:111], v120, %[wsd], s70 offen sc1\n\t"
        "buffer_load_dwordx4 v[112:115], v120, %[wsd], s71 offen offset:0 sc1\n\t"
        "buffer_load_dwordx4 v[116:119], v120, %[wsd], s71 offen offset:1024 sc1\n\t"
        "v_mov_b32_e32 v88, 0\n\t"
        "v_mov_b32_e32 v89, 0\n\t"
        "v_mov_b32_e32 v90, 0\n\t"
        "v_mov_b32_e32 v91, 0\n\t"
        "v_mov_b32_e32 v92, 0\n\t"
        "v_mov_b32_e32 v93, 0\n\t"
        "v_mov_b32_e32 v94, 0\n\t"
        "v_mov_b32_e32 v95, 0\n\t"
        "v_mov_b32_e32 v96, 0\n\t"
        "v_mov_b32_e32 v97, 0\n\t"
        "v_mov_b32_e32 v98, 0\n\t"
        "v_mov_b32_e32 v99, 0\n\t"
        "v_mov_b32_e32 v100, 0\n\t"
        "v_mov_b32_e32 v101, 0\n\t"
        "v_mov_b32_e32 v102, 0\n\t"
        "v_mov_b32_e32 v103, 0\n\t"
        "s_waitcnt lgkmcnt(0)\n\t"
        "s_waitcnt vmcnt(8)\n\t"
        "s_sub_u32 s89, s40, 1\n\t"
        "s_mov_b32 s97, 0\n\t"
        ".Lsr%=:\n\t"
        "s_mov_b32 exec_hi, 0xffff\n\t"
        "v_subrev_u32_e32 v0, s41, v84\n\t"
        "v_subrev_u32_e32 v1, s41, v86\n\t"
        "v_max_i32_e32 v0, v0, v1\n\t"
        "v_max_u32_e32 v1, v85, v87\n\t"
        "v_cmp_eq_u32_e32 vcc, -1, v1\n\t"
        "v_max_i32_dpp v0, v0, v0 quad_perm:[1,0,3,2] row_mask:0xf bank_mask:0xf\n\t"
        "v_or_b32_e32 v2, v85, v87\n\t"
        "s_nop 0\n\t"
        "v_max_i32_dpp v0, v0, v0 quad_perm:[2,3,0,1] row_mask:0xf bank_mask:0xf\n\t"
        "s_nop 1\n\t"
        "v_max_i32_dpp v0, v0, v0 row_ror:4 row_mask:0xf bank_mask:0xf\n\t"
        "s_nop 1\n\t"
        "v_max_i32_dpp v0, v0, v0 row_ror:8 row_mask:0xf bank_mask:0xf\n\t"
        "s_cbranch_vccnz .Lsf%=\n\t"
        "v_readlane_b32 s80, v0, 0\n\t"
        "v_readlane_b32 s81, v0, 16\n\t"
        "v_readlane_b32 s56, v0, 32\n\t"
        "v_and_b32_e32 v1, 1, v2\n\t"
        "v_cmp_ne_u32_e32 vcc, 0, v1\n\t"
        "s_nop 1\n\t"
        "s_cmp_lg_u64 vcc, 0\n\t"
        "s_cselect_b32 s86, 1, 0\n\t"
        "v_and_b32_e32 v1, 2, v2\n\t"
        "v_cmp_ne_u32_e32 vcc, 0, v1\n\t"
        "s_nop 1\n\t"
        "s_cmp_lg_u64 vcc, 0\n\t"
        "s_cselect_b32 s87, 1, 0\n\t"
        "v_and_b32_e32 v1, 4, v2\n\t"
        "v_cmp_ne_u32_e32 vcc, 0, v1\n\t"
        "s_nop 1\n\t"
        "s_cmp_lg_u64 vcc, 0\n\t"
        "s_cselect_b32 s88, 1, 0\n\t"
        "s_max_i32 s56, s56, s81\n\t"
        "s_mov_b32 s85, s80\n\t"
        "s_add_u32 s92, s80, s41\n\t"
        "s_sub_u32 s92, s92, s43\n\t"
        "s_add_i32 s93, s56, s47\n\t"
        "s_mov_b32 s95, s47\n\t"
        "s_cmp_lt_u32 s89, %c[iCT0]\n\t"
        "s_cbranch_scc1 .Lc0%=\n\t"
        "s_or_b32 s80, s48, s86\n\t"
        "s_or_b32 s80, s80, s87\n\t"
        "s_xor_b32 s90, s80, 1\n\t"
        "s_cmp_le_i32 s92, s46\n\t"
        "s_cselect_b32 s80, 1, 0\n\t"
        "s_andn2_b32 s91, s80, s88\n\t"
        "s_and_b32 s80, s89, %c[LDEC]\n\t"
        "s_cmp_eq_u32 s80, 0\n\t"
        "s_cselect_b32 s99, 1, 0\n\t"
        "s_mov_b32 s80, s88\n\t"
        "s_add_i32 s81, s92, s50\n\t"
        "s_sub_i32 s81, s81, 4\n\t"
        "s_cmp_le_i32 s81, s46\n\t"
        "s_cselect_b32 s80, s80, 0\n\t"
        "s_cmp_lt_i32 s50, %c[LC24]\n\t"
        "s_cselect_b32 s80, s80, 0\n\t"
        "s_lshl_b32 s80, s80, 2\n\t"
        "s_add_i32 s50, s50, s80\n\t"
        "s_sub_i32 s50, s50, s99\n\t"
        "s_max_i32 s50, s50, %c[LC]\n\t"
        "s_lshl_b32 s80, s92, 1\n\t"
        "s_sub_i32 s81, s80, s52\n\t"
        "s_ashr_i32 s82, s81, %c[UPSH]\n\t"
        "s_max_i32 s82, s82, %c[TRUP]\n\t"
        "s_add_i32 s82, s82, s52\n\t"
        "s_min_i32 s82, s82, s80\n\t"
        "s_sub_i32 s83, 0, s81\n\t"
        "s_ashr_i32 s83, s83, 1\n\t"
        "s_max_i32 s83, s83, %c[TRDN]\n\t"
        "s_sub_i32 s83, s52, s83\n\t"
        "s_max_i32 s83, s83, s80\n\t"
        "s_cmp_gt_i32 s81, 0\n\t"
        "s_cselect_b32 s82, s82, s83\n\t"
        "s_cmp_lg_u32 s90, 0\n\t"
        "s_cselect_b32 s52, s82, s52\n\t"
        "s_andn2_b32 s80, s86, s48\n\t"
        "s_cmp_lt_i32 s51, %c[LA24]\n\t"
        "s_cselect_b32 s80, s80, 0\n\t"
        "s_lshl_b32 s80, s80, 2\n\t"
        "s_add_i32 s51, s51, s80\n\t"
        "s_sub_i32 s51, s51, s99\n\t"
        "s_max_i32 s51, s51, %c[LA]\n\t"
        "s_lshl_b32 s80, s93, 1\n\t"
        "s_sub_i32 s81, s80, s53\n\t"
        "s_ashr_i32 s82, s81, %c[UPSH]\n\t"
        "s_max_i32 s82, s82, %c[TRUP]\n\t"
        "s_add_i32 s82, s82, s53\n\t"
        "s_min_i32 s82, s82, s80\n\t"
        "s_sub_i32 s83, 0, s81\n\t"
        "s_ashr_i32 s83, s83, 1\n\t"
        "s_max_i32 s83, s83, %c[TRDN]\n\t"
        "s_sub_i32 s83, s53, s83\n\t"
        "s_max_i32 s83, s83, s80\n\t"
        "s_cmp_gt_i32 s81, 0\n\t"
        "s_cselect_b32 s82, s82, s83\n\t"
        "s_cmp_lg_u32 s91, 0\n\t"
        "s_cselect_b32 s53, s82, s53\n\t"
        ".Lc0%=:\n\t"
        "s_add_i32 s80, s52, 1\n\t"
        "s_ashr_i32 s80, s80, 1\n\t"
        "s_add_i32 s46, s80, s50\n\t"
        "s_add_i32 s80, s53, 1\n\t"
        "s_ashr_i32 s80, s80, 1\n\t"
        "s_add_i32 s47, s80, s51\n\t"
        "s_lshl_b32 s80, s87, 1\n\t"
        "s_or_b32 s80, s80, s86\n\t"
        "s_lshl_b32 s81, s88, 2\n\t"
        "s_or_b32 s94, s80, s81\n\t"
        "s_mov_b32 s43, s41\n\t"
        "s_cmp_gt_i32 s56, s54\n\t"
        "s_cbranch_scc1 .Lmv%=\n\t"
        "s_mov_b32 s37, 0\n\t"
        "s_mov_b32 s48, 0\n\t"
        ".Lsd%=:\n\t"
        "s_mov_b32 exec_hi, -1\n\t"
        "s_add_u32 s42, s43, s46\n\t"
        "s_add_u32 s41, s42, s47\n\t"
        "ds_read_b128 v[16:19], v121 offset:0\n\t"
        "ds_read_b128 v[20:23], v121 offset:1024\n\t"
        "ds_read_b128 v[24:27], v121 offset:2048\n\t"
        "ds_read_b128 v[28:31], v121 offset:3072\n\t"
        "ds_read_b128 v[32:35], v121 offset:4096\n\t"
        "ds_read_b128 v[36:39], v121 offset:5120\n\t"
        "ds_read_b128 v[40:43], v121 offset:6144\n\t"
        "ds_read_b128 v[44:47], v121 offset:7168\n\t"
        "ds_read_b128 v[56:59], v121 offset:8192\n\t"
        "ds_read_b128 v[60:63], v121 offset:9216\n\t"
        "ds_read_b128 v[64:67], v121 offset:10240\n\t"
        "ds_read_b128 v[68:71], v121 offset:11264\n\t"
        "ds_read_b128 v[72:75], v121 offset:12288\n\t"
        "ds_read_b128 v[76:79], v121 offset:13312\n\t"
        "ds_read_b128 v[80:83], v121 offset:14336\n\t"
        "ds_read_b128 v[84:87], v121 offset:15360\n\t"
        "s_waitcnt vmcnt(6)\n\t"
        "s_barrier\n\t"
        "v_mov_b32_e32 v12, 30720\n\t"
        "ds_read_b64 v[12:13], v12\n\t"
        "s_waitcnt lgkmcnt(0)\n\t"
        "v_readfirstlane_b32 s80, v12\n\t"
        "v_readfirstlane_b32 s81, v13\n\t"
        "s_cmp_eq_u32 s80, s40\n\t"
        "s_cselect_b32 s84, 1, 0\n\t"
        "s_cmp_eq_u32 s81, s40\n\t"
        "s_cselect_b32 s81, 2, 0\n\t"
        "s_or_b32 s84, s84, s81\n\t"
        "s_cmp_eq_u32 s64, s36\n\t"
        "s_cbranch_scc1 .Lk11%=\n\t"
        "s_mov_b32 s97, 0\n\t"
        ".Lk01%=:\n\t"
        "v_readfirstlane_b32 s82, v48\n\t"
        "v_readfirstlane_b32 s83, v49\n\t"
        "s_xor_b32 s82, s82, %[tag]\n\t"
        "s_xor_b32 s83, s83, %[tag]\n\t"
        "s_or_b32 s82, s82, s83\n\t"
        "s_cbranch_scc0 .Lk11%=\n\t"
        "s_sleep 3\n\t"
        "buffer_load_dwordx2 v[48:49], off, %[wsd], s64 sc1\n\t"
        "s_add_u32 s97, s97, 1\n\t"
        "s_cmp_lt_u32 s97, 0x200000\n\t"
        "s_cbranch_scc0 .Lgu%=\n\t"
        "s_waitcnt vmcnt(0)\n\t"
        "s_branch .Lk01%=\n\t"
        ".Lk11%=:\n\t"
        "v_mov_b32_e32 v48, s76\n\t"
        "v_mov_b32_e32 v49, s84\n\t"
        "v_mov_b32_e32 v50, s40\n\t"
        "s_mov_b32 s85, s67\n\t"
        "s_and_b32 s80, s40, 3\n\t"
        "s_lshl_b32 s80, s80, 17\n\t"
        "s_add_u32 s57, s80, %[nA]\n\t"
        "s_add_u32 s58, s57, 0x1000\n\t"
        "s_add_u32 s59, s57, 0x4000\n\t"
        "s_add_u32 s60, s57, 0x5000\n\t"
        "s_and_b32 s80, s40, 3\n\t"
        "s_lshl_b32 s80, s80, 17\n\t"
        "s_add_u32 s63, s80, %[nC]\n\t"
        "s_add_u32 s66, s80, %[nS]\n\t"
        "s_xor_b32 s81, s80, 0x40000\n\t"
        "s_add_u32 s68, s81, %[nW]\n\t"
        "s_add_u32 s81, s80, 0x20000\n\t"
        "s_and_b32 s81, s81, 0x60000\n\t"
        "s_add_u32 s67, s81, %[nW]\n\t"
        "s_xor_b32 s81, s81, 0x40000\n\t"
        "s_add_u32 s65, s81, %[nC]\n\t"
        "s_add_u32 s74, s81, %[nW]\n\t"
        "s_lshl_b32 s72, s40, 17\n\t"
        "s_add_u32 s72, s72, %[kvo]\n\t"
        "v_lshlrev_b32_e32 v51, 6, v120\n\t"
        "s_add_u32 s80, s85, %[swo]\n\t"
        "s_cmp_eq_u32 %[w], 0\n\t"
        "s_cselect_b32 s73, s80, s36\n\t"
        "s_cselect_b32 s74, %[pra], s36\n\t"
        "s_add_u32 s80, s40, 1\n\t"
        "s_min_u32 s80, s80, 63\n\t"
        "s_lshl_b32 s80, s80, 18\n\t"
        "s_add_u32 s69, s80, %[kx]\n\t"
        "s_add_u32 s70, s69, 0x1000\n\t"
        "s_add_u32 s71, s80, %[vx]\n\t"
        "s_add_u32 s80, s40, 2\n\t"
        "s_lshl_b32 s81, s80, 9\n\t"
        "s_add_u32 s81, s81, %[kf]\n\t"
        "s_and_b32 s80, s80, 0x43\n\t"
        "s_cselect_b32 s64, s36, s81\n\t"
        "s_mov_b32 s80, s61\n\t"
        "s_mov_b32 s61, s62\n\t"
        "s_mov_b32 s62, s80\n\t"
        "s_waitcnt lgkmcnt(0)\n\t"
        "s_branch .Lloop%=\n\t"
        ".Lmv%=:\n\t"
        "s_mov_b32 s48, 1\n\t"
        "s_cmp_eq_u32 s54, %c[iTOL]\n\t"
        "s_cbranch_scc1 .Lm1%=\n\t"
        "s_max_i32 s80, s85, s56\n\t"
        "s_add_u32 s43, s41, s80\n\t"
        "s_add_u32 s43, s43, %c[iPEN0]\n\t"
        "s_mov_b32 s54, %c[iTOL]\n\t"
        "s_add_u32 s46, s46, %c[iX0C]\n\t"
        "s_add_u32 s47, s47, %c[iX0A]\n\t"
        "v_and_b32_e32 v0, 3, v84\n\t"
        "v_and_b32_e32 v1, 3, v86\n\t"
        "v_lshlrev_b32_e64 v0, v0, 1\n\t"
        "v_lshlrev_b32_e64 v1, v1, 1\n\t"
        "v_or_b32_e32 v0, v0, v1\n\t"
        "s_mov_b32 s80, 0\n\t"
        "v_and_b32_e32 v1, 1, v0\n\t"
        "v_cmp_ne_u32_e32 vcc, 0, v1\n\t"
        "s_nop 1\n\t"
        "s_cmp_lg_u64 vcc, 0\n\t"
        "s_cselect_b32 s81, 2, 0\n\t"
        "s_or_b32 s80, s80, s81\n\t"
        "v_and_b32_e32 v1, 2, v0\n\t"
        "v_cmp_ne_u32_e32 vcc, 0, v1\n\t"
        "s_nop 1\n\t"
        "s_cmp_lg_u64 vcc, 0\n\t"
        "s_cselect_b32 s81, 4, 0\n\t"
        "s_or_b32 s80, s80, s81\n\t"
        "v_and_b32_e32 v1, 4, v0\n\t"
        "v_cmp_ne_u32_e32 vcc, 0, v1\n\t"
        "s_nop 1\n\t"
        "s_cmp_lg_u64 vcc, 0\n\t"
        "s_cselect_b32 s81, 8, 0\n\t"
        "s_or_b32 s80, s80, s81\n\t"
        "v_and_b32_e32 v1, 8, v0\n\t"
        "v_cmp_ne_u32_e32 vcc, 0, v1\n\t"
        "s_nop 1\n\t"
        "s_cmp_lg_u64 vcc, 0\n\t"
        "s_cselect_b32 s81, 16, 0\n\t"
        "s_or_b32 s80, s80, s81\n\t"
        "s_lshr_b32 s80, 0x1adf1210, s80\n\t"
        "s_and_b32 s55, s80, 3\n\t"
        "s_branch .Lal%=\n\t"
        ".Lm1%=:\n\t"
        "s_add_u32 s43, s41, s56\n\t"
        "s_add_u32 s80, s37, %c[iPEN2]\n\t"
        "s_min_i32 s80, s80, %c[iPENM]\n\t"
        "s_cmp_lg_u32 s37, 0\n\t"
        "s_cselect_b32 s37, s80, %c[iPEN]\n\t"
        "s_add_u32 s80, s56, s37\n\t"
        "s_max_i32 s80, s80, %c[iPMIN]\n\t"
        "s_add_u32 s43, s41, s80\n\t"
        "s_add_u32 s80, s56, s95\n\t"
        "s_add_u32 s80, s80, %c[LA]\n\t"
        "s_add_u32 s81, s47, %c[AUP]\n\t"
        "s_min_u32 s80, s80, s81\n\t"
        "s_max_u32 s47, s47, s80\n\t"
        ".Lal%=:\n\t"
        "s_sub_u32 s80, s55, s43\n\t"
        "s_and_b32 s80, s80, %c[iPHM]\n\t"
        "s_add_u32 s43, s43, s80\n\t"
        "s_branch .Lsd%=\n\t"
        ".Lsf%=:\n\t"
        "s_mov_b32 exec_hi, -1\n\t"
        "s_sleep 2\n\t"
        "buffer_load_dwordx4 v[84:87], v120, %[srd], s66 offen sc1\n\t"
        "s_add_u32 s97, s97, 1\n\t"
        "s_cmp_lt_u32 s97, 0x100000\n\t"
        "s_cbranch_scc0 .Lgu%=\n\t"
        "s_waitcnt vmcnt(0)\n\t"
        "s_branch .Lsr%=\n\t"
        ".Lf1%=:\n\t"
        "ds_read_b128 v[92:95], v120 offset:0\n\t"
        "ds_read_b128 v[68:71], v120 offset:4096\n\t"
        "v_max_u32_e32 v54, v16, v18\n\t"
        "v_max3_u32 v54, v54, v17, v19\n\t"
        "v_max3_u32 v54, v54, v20, v22\n\t"
        "v_max3_u32 v54, v54, v21, v23\n\t"
        "v_max3_u32 v54, v54, v24, v26\n\t"
        "v_max3_u32 v54, v54, v25, v27\n\t"
        "v_max3_u32 v54, v54, v28, v30\n\t"
        "v_max3_u32 v54, v54, v29, v31\n\t"
        "v_max3_u32 v54, v54, v32, v34\n\t"
        "v_max3_u32 v54, v54, v33, v35\n\t"
        "v_max3_u32 v54, v54, v36, v38\n\t"
        "v_max3_u32 v54, v54, v37, v39\n\t"
        "v_max3_u32 v54, v54, v40, v42\n\t"
        "v_max3_u32 v54, v54, v41, v43\n\t"
        "v_max3_u32 v54, v54, v44, v46\n\t"
        "v_max3_u32 v54, v54, v45, v47\n\t"
        "v_max3_u32 v54, v54, v48, v50\n\t"
        "v_max3_u32 v54, v54, v49, v51\n\t"
        "v_max3_u32 v54, v54, v0, v2\n\t"
        "v_max3_u32 v54, v54, v1, v3\n\t"
        "v_max3_u32 v54, v54, v4, v6\n\t"
        "v_max3_u32 v54, v54, v5, v7\n\t"
        "v_max3_u32 v54, v54, v8, v10\n\t"
        "v_max3_u32 v54, v54, v9, v11\n\t"
        "s_waitcnt lgkmcnt(0)\n\t"
        "v_max3_u32 v54, v54, v92, v94\n\t"
        "v_max3_u32 v54, v54, v93, v95\n\t"
        "v_max3_u32 v54, v54, v68, v70\n\t"
        "v_max3_u32 v54, v54, v69, v71\n\t"
        "ds_read_b128 v[92:95], v120 offset:8192\n\t"
        "ds_read_b128 v[68:71], v120 offset:12288\n\t"
        "s_waitcnt lgkmcnt(0)\n\t"
        "v_max3_u32 v54, v54, v92, v94\n\t"
        "v_max3_u32 v54, v54, v93, v95\n\t"
        "v_max3_u32 v54, v54, v68, v70\n\t"
        "v_max3_u32 v54, v54, v69, v71\n\t"
        "v_cmp_eq_u32_e32 vcc, -1, v54\n\t"
        "s_nop 1\n\t"
        "v_mov_b32_e32 v55, v55\n\t"
        "s_nop 0\n\t"
        "s_cbranch_vccz .Lr1%=\n\t"
        "buffer_load_dwordx4 v[0:3], v120, %[srd], s57 offen sc1\n\t"
        "buffer_load_dwordx4 v[4:7], v120, %[srd], s58 offen sc1\n\t"
        "buffer_load_dwordx4 v[8:11], v120, %[srd], s59 offen sc1\n\t"
        "buffer_load_dwordx4 v[12:15], v120, %[srd], s60 offen sc1\n\t"
        "s_add_u32 s80, s40, 1\n\t"
        "v_mov_b32_e32 v54, 30720\n\t"
        "v_mov_b32_e32 v55, s80\n\t"
        "ds_write_b32 v54, v55\n\t"
        "ds_read_b128 v[16:19], v121 offset:0\n\t"
        "ds_read_b128 v[20:23], v121 offset:1024\n\t"
        "ds_read_b128 v[24:27], v121 offset:2048\n\t"
        "ds_read_b128 v[28:31], v121 offset:3072\n\t"
        "ds_read_b128 v[32:35], v121 offset:4096\n\t"
        "ds_read_b128 v[36:39], v121 offset:5120\n\t"
        "ds_read_b128 v[40:43], v121 offset:6144\n\t"
        "ds_read_b128 v[44:47], v121 offset:7168\n\t"
        "ds_read_b128 v[56:59], v121 offset:8192\n\t"
        "ds_read_b128 v[60:63], v121 offset:9216\n\t"
        "ds_read_b128 v[64:67], v121 offset:10240\n\t"
        "ds_read_b128 v[68:71], v121 offset:11264\n\t"
        "ds_read_b128 v[72:75], v121 offset:12288\n\t"
        "ds_read_b128 v[76:79], v121 offset:13312\n\t"
        "ds_read_b128 v[80:83], v121 offset:14336\n\t"
        "ds_read_b128 v[84:87], v121 offset:15360\n\t"
        "v_mov_b32_e32 v88, 0\n\t"
        "v_mov_b32_e32 v89, 0\n\t"
        "v_mov_b32_e32 v90, 0\n\t"
        "v_mov_b32_e32 v91, 0\n\t"
        "v_mov_b32_e32 v92, 0\n\t"
        "v_mov_b32_e32 v93, 0\n\t"
        "v_mov_b32_e32 v94, 0\n\t"
        "v_mov_b32_e32 v95, 0\n\t"
        "v_mov_b32_e32 v96, 0\n\t"
        "v_mov_b32_e32 v97, 0\n\t"
        "v_mov_b32_e32 v98, 0\n\t"
        "v_mov_b32_e32 v99, 0\n\t"
        "v_mov_b32_e32 v100, 0\n\t"
        "v_mov_b32_e32 v101, 0\n\t"
        "v_mov_b32_e32 v102, 0\n\t"
        "v_mov_b32_e32 v103, 0\n\t"
        "s_waitcnt lgkmcnt(0)\n\t"
        "s_barrier\n\t"
        ".Lfa%=:\n\t"
        "s_waitcnt vmcnt(0)\n\t"
        "v_max_u32_e32 v54, v0, v2\n\t"
        "v_max3_u32 v54, v54, v1, v3\n\t"
        "v_max3_u32 v54, v54, v4, v6\n\t"
        "v_max3_u32 v54, v54, v5, v7\n\t"
        "v_max3_u32 v54, v54, v8, v10\n\t"
        "v_max3_u32 v54, v54, v9, v11\n\t"
        "v_max3_u32 v54, v54, v12, v14\n\t"
        "v_max3_u32 v54, v54, v13, v15\n\t"
        "v_cmp_eq_u32_e32 vcc, -1, v54\n\t"
        "s_nop 1\n\t"
        "v_mov_b32_e32 v55, v55\n\t"
        "s_nop 0\n\t"
        "s_cbranch_vccz .Lfg%=\n\t"
        "s_sleep %c[ASLP]\n\t"
        "buffer_load_dwordx4 v[0:3], v120, %[srd], s57 offen sc1\n\t"
        "buffer_load_dwordx4 v[4:7], v120, %[srd], s58 offen sc1\n\t"
        "buffer_load_dwordx4 v[8:11], v120, %[srd], s59 offen sc1\n\t"
        "buffer_load_dwordx4 v[12:15], v120, %[srd], s60 offen sc1\n\t"
        "s_add_u32 s97, s97, 1\n\t"
        "s_cmp_lt_u32 s97, 0x100000\n\t"
        "s_cbranch_scc0 .Lgu%=\n\t"
        "s_branch .Lfa%=\n\t"
        ".Lfg%=:\n\t"
        "s_sleep %c[RSLP]\n\t"
        "s_branch .Lgo%=\n\t"
        ".Lfb%=:\n\t"
        "s_nop 7\n\t"
        "s_mov_b32 s97, 0\n\t"
        "s_add_u32 s80, s40, 1\n\t"
        "v_mov_b32_e32 v54, 30724\n\t"
        "v_mov_b32_e32 v55, s80\n\t"
        "ds_write_b32 v54, v55\n\t"
        ".Lfp%=:\n\t"
        "s_sleep %c[ASLP]\n\t"
        "buffer_load_dword v88, v127, %[srd], s61 offen offset:0 sc1\n\t"
        "buffer_load_dword v89, v127, %[srd], s61 offen offset:256 sc1\n\t"
        "s_add_u32 s97, s97, 1\n\t"
        "s_cmp_lt_u32 s97, 0x100000\n\t"
        "s_cbranch_scc0 .Lgu%=\n\t"
        "s_waitcnt vmcnt(0)\n\t"
        "v_max_u32_e32 v54, v88, v89\n\t"
        "v_cmp_eq_u32_e32 vcc, -1, v54\n\t"
        "s_nop 1\n\t"
        "v_mov_b32_e32 v55, v55\n\t"
        "s_nop 0\n\t"
        "s_cbranch_vccnz .Lfp%=\n\t"
        "v_mfma_f32_16x16x32_bf16 v[40:43], v[112:115], v[36:39], 0\n\t"
        "v_mfma_f32_16x16x32_bf16 v[44:47], v[116:119], v[36:39], 0\n\t"
        "s_nop 1\n\t"
        "v_mov_b32_e32 v55, v55\n\t"
        "s_nop 1\n\t"
        "v_mov_b32_e32 v55, v55\n\t"
        "s_nop 0\n\t"
        "v_pk_mul_f32 v[40:41], v[40:41], v[10:11] op_sel_hi:[1,0]\n\t"
        "v_pk_mul_f32 v[42:43], v[42:43], v[10:11] op_sel_hi:[1,0]\n\t"
        "v_pk_mul_f32 v[44:45], v[44:45], v[10:11] op_sel_hi:[1,0]\n\t"
        "v_pk_mul_f32 v[46:47], v[46:47], v[10:11] op_sel_hi:[1,0]\n\t"
        "v_mfma_f32_16x16x16_bf16 v[40:43], v[88:89], v[90:91], v[40:43]\n\t"
        "s_nop 0\n\t"
        "v_mfma_f32_16x16x16_bf16 v[44:47], v[88:89], v[94:95], v[44:47]\n\t"
        "s_nop 7\n\t"
        "s_branch .Lrb%=\n\t"
        ".Lta%=:\n\t"
        "s_waitcnt lgkmcnt(0)\n\t"
        "s_branch .Lxa%=\n\t"
        ".Lgu%=:\n\t"
        "s_mov_b64 exec, -1\n\t"
        "v_mov_b32_e32 v54, %c[ferr]\n\t"
        "v_mov_b32_e32 v55, 1\n\t"
        "buffer_store_dword v55, v54, %[wsd], 0 offen sc1\n\t"
        "s_endpgm\n\t"
        ".Lend%=:\n\t"
        "s_nop 0\n\t"
        "v_permlane16_swap_b32_e32 v50, v51\n\t"
        "v_mov_b32_e32 v0, 64\n\t"
        "buffer_store_dwordx2 v[50:51], v34, %[wsd], s72 offen sc1\n\t"
        "s_cmp_lg_u32 %[w], 0\n\t"
        "s_waitcnt vmcnt(0)\n\t"
        "s_barrier\n\t"
        "s_cbranch_scc1 .Le2%=\n\t"
        "s_mov_b64 exec, 1\n\t"
        "buffer_store_dword v0, off, %[wsd], %[pra] sc1\n\t"
        "s_mov_b64 exec, -1\n\t"
        ".Le2%=:\n\t"
        "s_endpgm"
        :
        : [srd] "s"(rhd), [wsd] "s"(wsd), [w] "s"(w), [tag] "s"(tag), [nA] "s"(nA), [nS] "s"(nS), [nW] "s"(nW), [nC] "s"(nC), [b0] "s"(b0), [b1] "s"(b1), [kx] "s"(kx), [vx] "s"(vx), [kf] "s"(kf), [kvo] "s"(kvo),
          [swo] "s"(swo), [pra] "s"(pr_a), [ferr] "i"(F_ERR), [iD1] "i"(D1), [iD2] "i"(D2), [LC] "i"(LC0), [LA] "i"(LA0), [LC24] "i"(LC0 + 24), [LA24] "i"(LA0 + 24), [E1] "i"(2 * (D1 - LC0)), [E2] "i"(2 * (D2 - LA0)),
          [iPEN] "i"(PEN), [iPEN2] "i"(PEN2), [iPENM] "i"(PEN + 2 * PEN2), [iPEN0] "i"(PEN0), [iTOL] "i"(TOL), [iCT0] "i"(CT0), [iX0C] "i"(X0C), [iX0A] "i"(X0A), [iPHM] "i"(PH_M),
          [iFT] "i"(-3), [iFN] "i"(FINE_N), [AUP] "i"(A_UP), [ASLP] "i"(A_SLP), [LDEC] "i"(L_DEC), [UPSH] "i"(UP_SH), [TRUP] "i"(TR_UP), [TRDN] "i"(TR_DN), [HSLP] "i"(H_SLP), [iPMIN] "i"(PMIN), [VSLP] "i"(V_SLP), [RSLP] "i"(R_SLP),
          "{v[120:127]}"(pc), "{v[52:53]}"(pd), "{a[0:31]}"(wa[0]), "{a[32:63]}"(wa[1]), "{a[64:95]}"(wa[2]), "{a[96:127]}"(wa[3]), "{a[128:159]}"(wa[4]), "{a[160:191]}"(wa[5]), "{a[192:223]}"(wa[6]), "{a[224:255]}"(wa[7]), "{v[128:159]}"(wg[0]), "{v[160:191]}"(wg[1]), "{v[192:223]}"(wg[2]), "{v[224:255]}"(wg[3])
        : "memory", "vcc", "scc", "s36", "s37", "s38", "s39", "s40", "s41", "s42", "s43", "s44", "s45", "s46", "s47", "s48", "s49", "s50", "s51", "s52", "s53", "s54", "s55", "s56", "s57", "s58", "s59", "s60", "s61", "s62", "s63", "s64", "s65", "s66", "s67", "s68", "s69", "s70", "s71", "s72", "s73", "s74", "s75", "s76", "s77", "s78", "s79", "s80", "s81", "s82", "s83", "s84", "s85", "s86", "s87", "s88", "s89", "s90", "s91", "s92", "s93", "s94", "s95", "s96", "s97", "s98", "s99");
    } else {
        const int cb = i32;
        unsigned m0v = *(const unsigned*)(p.mem0 + (4 * G + ((l >> 2) & 1) + ((l >> 3) & 2)) * 1024 + cb * 32 + (l >> 5) * 16 + (l & 8) + (l & 3) * 2);
        {
            const v4u sv = mk_srd(p.Wv, 1024 * 4096 * 2), so = mk_srd(p.Wo, 1024 * 4096 * 2);
            const unsigned m0b = w * 32768, f = (g & 1) | ((c >> 1) << 1);
            const unsigned t0 = m0b + (16 * (g >> 1) + 4 * (g & 1) + (c & 1) + 8 * (c >> 1)) * 128 + (f << 5) + (n16 & 3) * 8, t1 = m0b + (16 * g + 4 * (c >> 1) + (c & 1)) * 64 + ((c >> 1) << 5) + (n16 & 3) * 8;
            v16u pa;
            pa[0] = (64 * w + (l >> 3)) * 8192 + vi * 128 + (((l & 7) ^ ((l >> 5) << 1)) << 4); pa[1] = pa[0] ^ 64u;
            pa[2] = (1024 * w + (l >> 2)) * 2048 + cb * 64 + (((l & 3) ^ ((g & 1) << 1)) << 4);
#pragma unroll
            for (int q = 0; q < 4; ++q) pa[3 + q] = t0 ^ (((w + q) & 3) << 5);
            pa[7] = t1; pa[8] = t1 ^ 32u; pa[9] = cal_a; pa[10] = tag; pa[11] = near; pa[12] = 0u; pa[13] = 0u; pa[14] = 0u; pa[15] = 0u;
    asm volatile(
        "s_add_u32 m0, %[m0b], 0\n\t"
        "s_mov_b32 s81, 0\n\t"
        "s_mov_b32 s82, 4\n\t"
        ".Ld0%=:\n\t"
        "buffer_load_dwordx4 v112, %[sv], s81 offen lds\n\t"
        "s_add_u32 m0, m0, 0x400\n\t"
        "s_add_u32 s81, s81, 65536\n\t"
        "buffer_load_dwordx4 v113, %[sv], s81 offen lds\n\t"
        "s_add_u32 m0, m0, 0x400\n\t"
        "s_add_u32 s81, s81, 65536\n\t"
        "s_sub_u32 s82, s82, 1\n\t"
        "s_cmp_lg_u32 s82, 0\n\t"
        "s_cbranch_scc1 .Ld0%=\n\t"
        "s_add_u32 m0, %[m0b], 8192\n\t"
        "s_mov_b32 s81, 2097152\n\t"
        "s_mov_b32 s82, 4\n\t"
        ".Ld1%=:\n\t"
        "buffer_load_dwordx4 v112, %[sv], s81 offen lds\n\t"
        "s_add_u32 m0, m0, 0x400\n\t"
        "s_add_u32 s81, s81, 65536\n\t"
        "buffer_load_dwordx4 v113, %[sv], s81 offen lds\n\t"
        "s_add_u32 m0, m0, 0x400\n\t"
        "s_add_u32 s81, s81, 65536\n\t"
        "s_sub_u32 s82, s82, 1\n\t"
        "s_cmp_lg_u32 s82, 0\n\t"
        "s_cbranch_scc1 .Ld1%=\n\t"
        "s_add_u32 m0, %[m0b], 16384\n\t"
        "s_mov_b32 s81, 4194304\n\t"
        "s_mov_b32 s82, 4\n\t"
        ".Ld2%=:\n\t"
        "buffer_load_dwordx4 v112, %[sv], s81 offen lds\n\t"
        "s_add_u32 m0, m0, 0x400\n\t"
        "s_add_u32 s81, s81, 65536\n\t"
        "buffer_load_dwordx4 v113, %[sv], s81 offen lds\n\t"
        "s_add_u32 m0, m0, 0x400\n\t"
        "s_add_u32 s81, s81, 65536\n\t"
        "s_sub_u32 s82, s82, 1\n\t"
        "s_cmp_lg_u32 s82, 0\n\t"
        "s_cbranch_scc1 .Ld2%=\n\t"
        "s_add_u32 m0, %[m0b], 24576\n\t"
        "s_mov_b32 s81, 6291456\n\t"
        "s_mov_b32 s82, 4\n\t"
        ".Ld3%=:\n\t"
        "buffer_load_dwordx4 v112, %[sv], s81 offen lds\n\t"
        "s_add_u32 m0, m0, 0x400\n\t"
        "s_add_u32 s81, s81, 65536\n\t"
        "buffer_load_dwordx4 v113, %[sv], s81 offen lds\n\t"
        "s_add_u32 m0, m0, 0x400\n\t"
        "s_add_u32 s81, s81, 65536\n\t"
        "s_sub_u32 s82, s82, 1\n\t"
        "s_cmp_lg_u32 s82, 0\n\t"
        "s_cbranch_scc1 .Ld3%=\n\t"
        "s_waitcnt vmcnt(32)\n\t"
        "buffer_store_dwordx2 v[122:123], v121, %[wsd], 0 offen sc1\n\t"
        "s_waitcnt vmcnt(25)\n\t"
        "ds_read_b64_tr_b16 v[128:129], v115 offset:0\n\t"
        "ds_read_b64_tr_b16 v[130:131], v115 offset:256\n\t"
        "ds_read_b64_tr_b16 v[132:133], v115 offset:4096\n\t"
        "ds_read_b64_tr_b16 v[134:135], v115 offset:4352\n\t"
        "ds_read_b64_tr_b16 v[136:137], v116 offset:0\n\t"
        "ds_read_b64_tr_b16 v[138:139], v116 offset:256\n\t"
        "ds_read_b64_tr_b16 v[140:141], v116 offset:4096\n\t"
        "ds_read_b64_tr_b16 v[142:143], v116 offset:4352\n\t"
        "ds_read_b64_tr_b16 v[144:145], v117 offset:0\n\t"
        "ds_read_b64_tr_b16 v[146:147], v117 offset:256\n\t"
        "ds_read_b64_tr_b16 v[148:149], v117 offset:4096\n\t"
        "ds_read_b64_tr_b16 v[150:151], v117 offset:4352\n\t"
        "ds_read_b64_tr_b16 v[152:153], v118 offset:0\n\t"
        "ds_read_b64_tr_b16 v[154:155], v118 offset:256\n\t"
        "ds_read_b64_tr_b16 v[156:157], v118 offset:4096\n\t"
        "ds_read_b64_tr_b16 v[158:159], v118 offset:4352\n\t"
        "s_waitcnt lgkmcnt(0)\n\t"
        "s_add_u32 m0, %[m0b], 0\n\t"
        "s_mov_b32 s81, 0\n\t"
        "s_mov_b32 s82, 4\n\t"
        ".Ld4%=:\n\t"
        "buffer_load_dwordx4 v114, %[so], s81 offen lds\n\t"
        "s_add_u32 m0, m0, 0x400\n\t"
        "s_add_u32 s81, s81, 32768\n\t"
        "buffer_load_dwordx4 v114, %[so], s81 offen lds\n\t"
        "s_add_u32 m0, m0, 0x400\n\t"
        "s_add_u32 s81, s81, 32768\n\t"
        "s_sub_u32 s82, s82, 1\n\t"
        "s_cmp_lg_u32 s82, 0\n\t"
        "s_cbranch_scc1 .Ld4%=\n\t"
        "s_waitcnt vmcnt(25)\n\t"
        "ds_read_b64_tr_b16 v[160:161], v115 offset:8192\n\t"
        "ds_read_b64_tr_b16 v[162:163], v115 offset:8448\n\t"
        "ds_read_b64_tr_b16 v[164:165], v115 offset:12288\n\t"
        "ds_read_b64_tr_b16 v[166:167], v115 offset:12544\n\t"
        "ds_read_b64_tr_b16 v[168:169], v116 offset:8192\n\t"
        "ds_read_b64_tr_b16 v[170:171], v116 offset:8448\n\t"
        "ds_read_b64_tr_b16 v[172:173], v116 offset:12288\n\t"
        "ds_read_b64_tr_b16 v[174:175], v116 offset:12544\n\t"
        "ds_read_b64_tr_b16 v[176:177], v117 offset:8192\n\t"
        "ds_read_b64_tr_b16 v[178:179], v117 offset:8448\n\t"
        "ds_read_b64_tr_b16 v[180:181], v117 offset:12288\n\t"
        "ds_read_b64_tr_b16 v[182:183], v117 offset:12544\n\t"
        "ds_read_b64_tr_b16 v[184:185], v118 offset:8192\n\t"
        "ds_read_b64_tr_b16 v[186:187], v118 offset:8448\n\t"
        "ds_read_b64_tr_b16 v[188:189], v118 offset:12288\n\t"
        "ds_read_b64_tr_b16 v[190:191], v118 offset:12544\n\t"
        "s_waitcnt lgkmcnt(0)\n\t"
        "s_add_u32 m0, %[m0b], 8192\n\t"
        "s_mov_b32 s81, 262144\n\t"
        "s_mov_b32 s82, 4\n\t"
        ".Ld5%=:\n\t"
        "buffer_load_dwordx4 v114, %[so], s81 offen lds\n\t"
        "s_add_u32 m0, m0, 0x400\n\t"
        "s_add_u32 s81, s81, 32768\n\t"
        "buffer_load_dwordx4 v114, %[so], s81 offen lds\n\t"
        "s_add_u32 m0, m0, 0x400\n\t"
        "s_add_u32 s81, s81, 32768\n\t"
        "s_sub_u32 s82, s82, 1\n\t"
        "s_cmp_lg_u32 s82, 0\n\t"
        "s_cbranch_scc1 .Ld5%=\n\t"
        "s_waitcnt vmcnt(25)\n\t"
        "ds_read_b64_tr_b16 v[192:193], v115 offset:16384\n\t"
        "ds_read_b64_tr_b16 v[194:195], v115 offset:16640\n\t"
        "ds_read_b64_tr_b16 v[196:197], v115 offset:20480\n\t"
        "ds_read_b64_tr_b16 v[198:199], v115 offset:20736\n\t"
        "ds_read_b64_tr_b16 v[200:201], v116 offset:16384\n\t"
        "ds_read_b64_tr_b16 v[202:203], v116 offset:16640\n\t"
        "ds_read_b64_tr_b16 v[204:205], v116 offset:20480\n\t"
        "ds_read_b64_tr_b16 v[206:207], v116 offset:20736\n\t"
        "ds_read_b64_tr_b16 v[208:209], v117 offset:16384\n\t"
        "ds_read_b64_tr_b16 v[210:211], v117 offset:16640\n\t"
        "ds_read_b64_tr_b16 v[212:213], v117 offset:20480\n\t"
        "ds_read_b64_tr_b16 v[214:215], v117 offset:20736\n\t"
        "ds_read_b64_tr_b16 v[216:217], v118 offset:16384\n\t"
        "ds_read_b64_tr_b16 v[218:219], v118 offset:16640\n\t"
        "ds_read_b64_tr_b16 v[220:221], v118 offset:20480\n\t"
        "ds_read_b64_tr_b16 v[222:223], v118 offset:20736\n\t"
        "s_waitcnt lgkmcnt(0)\n\t"
        "s_add_u32 m0, %[m0b], 16384\n\t"
        "s_mov_b32 s81, 524288\n\t"
        "s_mov_b32 s82, 4\n\t"
        ".Ld6%=:\n\t"
        "buffer_load_dwordx4 v114, %[so], s81 offen lds\n\t"
        "s_add_u32 m0, m0, 0x400\n\t"
        "s_add_u32 s81, s81, 32768\n\t"
        "buffer_load_dwordx4 v114, %[so], s81 offen lds\n\t"
        "s_add_u32 m0, m0, 0x400\n\t"
        "s_add_u32 s81, s81, 32768\n\t"
        "s_sub_u32 s82, s82, 1\n\t"
        "s_cmp_lg_u32 s82, 0\n\t"
        "s_cbranch_scc1 .Ld6%=\n\t"
        "s_waitcnt vmcnt(25)\n\t"
        "ds_read_b64_tr_b16 v[224:225], v115 offset:24576\n\t"
        "ds_read_b64_tr_b16 v[226:227], v115 offset:24832\n\t"
        "ds_read_b64_tr_b16 v[228:229], v115 offset:28672\n\t"
        "ds_read_b64_tr_b16 v[230:231], v115 offset:28928\n\t"
        "ds_read_b64_tr_b16 v[232:233], v116 offset:24576\n\t"
        "ds_read_b64_tr_b16 v[234:235], v116 offset:24832\n\t"
        "ds_read_b64_tr_b16 v[236:237], v116 offset:28672\n\t"
        "ds_read_b64_tr_b16 v[238:239], v116 offset:28928\n\t"
        "ds_read_b64_tr_b16 v[240:241], v117 offset:24576\n\t"
        "ds_read_b64_tr_b16 v[242:243], v117 offset:24832\n\t"
        "ds_read_b64_tr_b16 v[244:245], v117 offset:28672\n\t"
        "ds_read_b64_tr_b16 v[246:247], v117 offset:28928\n\t"
        "ds_read_b64_tr_b16 v[248:249], v118 offset:24576\n\t"
        "ds_read_b64_tr_b16 v[250:251], v118 offset:24832\n\t"
        "ds_read_b64_tr_b16 v[252:253], v118 offset:28672\n\t"
        "ds_read_b64_tr_b16 v[254:255], v118 offset:28928\n\t"
        "s_waitcnt lgkmcnt(0)\n\t"
        "s_add_u32 m0, %[m0b], 24576\n\t"
        "s_mov_b32 s81, 786432\n\t"
        "s_mov_b32 s82, 4\n\t"
        ".Ld7%=:\n\t"
        "buffer_load_dwordx4 v114, %[so], s81 offen lds\n\t"
        "s_add_u32 m0, m0, 0x400\n\t"
        "s_add_u32 s81, s81, 32768\n\t"
        "buffer_load_dwordx4 v114, %[so], s81 offen lds\n\t"
        "s_add_u32 m0, m0, 0x400\n\t"
        "s_add_u32 s81, s81, 32768\n\t"
        "s_sub_u32 s82, s82, 1\n\t"
        "s_cmp_lg_u32 s82, 0\n\t"
        "s_cbranch_scc1 .Ld7%=\n\t"
        "s_waitcnt vmcnt(24)\n\t"
        "ds_read_b64_tr_b16 a[0:1], v119 offset:0\n\t"
        "ds_read_b64_tr_b16 a[2:3], v119 offset:128\n\t"
        "ds_read_b64_tr_b16 a[4:5], v119 offset:4096\n\t"
        "ds_read_b64_tr_b16 a[6:7], v119 offset:4224\n\t"
        "ds_read_b64_tr_b16 a[8:9], v120 offset:0\n\t"
        "ds_read_b64_tr_b16 a[10:11], v120 offset:128\n\t"
        "ds_read_b64_tr_b16 a[12:13], v120 offset:4096\n\t"
        "ds_read_b64_tr_b16 a[14:15], v120 offset:4224\n\t"
        "ds_read_b64_tr_b16 a[16:17], v119 offset:512\n\t"
        "ds_read_b64_tr_b16 a[18:19], v119 offset:640\n\t"
        "ds_read_b64_tr_b16 a[20:21], v119 offset:4608\n\t"
        "ds_read_b64_tr_b16 a[22:23], v119 offset:4736\n\t"
        "ds_read_b64_tr_b16 a[24:25], v120 offset:512\n\t"
        "ds_read_b64_tr_b16 a[26:27], v120 offset:640\n\t"
        "ds_read_b64_tr_b16 a[28:29], v120 offset:4608\n\t"
        "ds_read_b64_tr_b16 a[30:31], v120 offset:4736\n\t"
        "s_waitcnt lgkmcnt(0)\n\t"
        "s_add_u32 m0, %[m0b], 0\n\t"
        "s_mov_b32 s81, 1048576\n\t"
        "s_mov_b32 s82, 4\n\t"
        ".Ld8%=:\n\t"
        "buffer_load_dwordx4 v114, %[so], s81 offen lds\n\t"
        "s_add_u32 m0, m0, 0x400\n\t"
        "s_add_u32 s81, s81, 32768\n\t"
        "buffer_load_dwordx4 v114, %[so], s81 offen lds\n\t"
        "s_add_u32 m0, m0, 0x400\n\t"
        "s_add_u32 s81, s81, 32768\n\t"
        "s_sub_u32 s82, s82, 1\n\t"
        "s_cmp_lg_u32 s82, 0\n\t"
        "s_cbranch_scc1 .Ld8%=\n\t"
        "s_waitcnt vmcnt(24)\n\t"
        "ds_read_b64_tr_b16 a[32:33], v119 offset:8192\n\t"
        "ds_read_b64_tr_b16 a[34:35], v119 offset:8320\n\t"
        "ds_read_b64_tr_b16 a[36:37], v119 offset:12288\n\t"
        "ds_read_b64_tr_b16 a[38:39], v119 offset:12416\n\t"
        "ds_read_b64_tr_b16 a[40:41], v120 offset:8192\n\t"
        "ds_read_b64_tr_b16 a[42:43], v120 offset:8320\n\t"
        "ds_read_b64_tr_b16 a[44:45], v120 offset:12288\n\t"
        "ds_read_b64_tr_b16 a[46:47], v120 offset:12416\n\t"
        "ds_read_b64_tr_b16 a[48:49], v119 offset:8704\n\t"
        "ds_read_b64_tr_b16 a[50:51], v119 offset:8832\n\t"
        "ds_read_b64_tr_b16 a[52:53], v119 offset:12800\n\t"
        "ds_read_b64_tr_b16 a[54:55], v119 offset:12928\n\t"
        "ds_read_b64_tr_b16 a[56:57], v120 offset:8704\n\t"
        "ds_read_b64_tr_b16 a[58:59], v120 offset:8832\n\t"
        "ds_read_b64_tr_b16 a[60:61], v120 offset:12800\n\t"
        "ds_read_b64_tr_b16 a[62:63], v120 offset:12928\n\t"
        "s_waitcnt lgkmcnt(0)\n\t"
        "s_add_u32 m0, %[m0b], 8192\n\t"
        "s_mov_b32 s81, 1310720\n\t"
        "s_mov_b32 s82, 4\n\t"
        ".Ld9%=:\n\t"
        "buffer_load_dwordx4 v114, %[so], s81 offen lds\n\t"
        "s_add_u32 m0, m0, 0x400\n\t"
        "s_add_u32 s81, s81, 32768\n\t"
        "buffer_load_dwordx4 v114, %[so], s81 offen lds\n\t"
        "s_add_u32 m0, m0, 0x400\n\t"
        "s_add_u32 s81, s81, 32768\n\t"
        "s_sub_u32 s82, s82, 1\n\t"
        "s_cmp_lg_u32 s82, 0\n\t"
        "s_cbranch_scc1 .Ld9%=\n\t"
        "s_waitcnt vmcnt(24)\n\t"
        "ds_read_b64_tr_b16 a[64:65], v119 offset:16384\n\t"
        "ds_read_b64_tr_b16 a[66:67], v119 offset:16512\n\t"
        "ds_read_b64_tr_b16 a[68:69], v119 offset:20480\n\t"
        "ds_read_b64_tr_b16 a[70:71], v119 offset:20608\n\t"
        "ds_read_b64_tr_b16 a[72:73], v120 offset:16384\n\t"
        "ds_read_b64_tr_b16 a[74:75], v120 offset:16512\n\t"
        "ds_read_b64_tr_b16 a[76:77], v120 offset:20480\n\t"
        "ds_read_b64_tr_b16 a[78:79], v120 offset:20608\n\t"
        "ds_read_b64_tr_b16 a[80:81], v119 offset:16896\n\t"
        "ds_read_b64_tr_b16 a[82:83], v119 offset:17024\n\t"
        "ds_read_b64_tr_b16 a[84:85], v119 offset:20992\n\t"
        "ds_read_b64_tr_b16 a[86:87], v119 offset:21120\n\t"
        "ds_read_b64_tr_b16 a[88:89], v120 offset:16896\n\t"
        "ds_read_b64_tr_b16 a[90:91], v120 offset:17024\n\t"
        "ds_read_b64_tr_b16 a[92:93], v120 offset:20992\n\t"
        "ds_read_b64_tr_b16 a[94:95], v120 offset:21120\n\t"
        "s_waitcnt lgkmcnt(0)\n\t"
        "s_add_u32 m0, %[m0b], 16384\n\t"
        "s_mov_b32 s81, 1572864\n\t"
        "s_mov_b32 s82, 4\n\t"
        ".Ld10%=:\n\t"
        "buffer_load_dwordx4 v114, %[so], s81 offen lds\n\t"
        "s_add_u32 m0, m0, 0x400\n\t"
        "s_add_u32 s81, s81, 32768\n\t"
        "buffer_load_dwordx4 v114, %[so], s81 offen lds\n\t"
        "s_add_u32 m0, m0, 0x400\n\t"
        "s_add_u32 s81, s81, 32768\n\t"
        "s_sub_u32 s82, s82, 1\n\t"
        "s_cmp_lg_u32 s82, 0\n\t"
        "s_cbranch_scc1 .Ld10%=\n\t"
        "s_waitcnt vmcnt(24)\n\t"
        "ds_read_b64_tr_b16 a[96:97], v119 offset:24576\n\t"
        "ds_read_b64_tr_b16 a[98:99], v119 offset:24704\n\t"
        "ds_read_b64_tr_b16 a[100:101], v119 offset:28672\n\t"
        "ds_read_b64_tr_b16 a[102:103], v119 offset:28800\n\t"
        "ds_read_b64_tr_b16 a[104:105], v120 offset:24576\n\t"
        "ds_read_b64_tr_b16 a[106:107], v120 offset:24704\n\t"
        "ds_read_b64_tr_b16 a[108:109], v120 offset:28672\n\t"
        "ds_read_b64_tr_b16 a[110:111], v120 offset:28800\n\t"
        "ds_read_b64_tr_b16 a[112:113], v119 offset:25088\n\t"
        "ds_read_b64_tr_b16 a[114:115], v119 offset:25216\n\t"
        "ds_read_b64_tr_b16 a[116:117], v119 offset:29184\n\t"
        "ds_read_b64_tr_b16 a[118:119], v119 offset:29312\n\t"
        "ds_read_b64_tr_b16 a[120:121], v120 offset:25088\n\t"
        "ds_read_b64_tr_b16 a[122:123], v120 offset:25216\n\t"
        "ds_read_b64_tr_b16 a[124:125], v120 offset:29184\n\t"
        "ds_read_b64_tr_b16 a[126:127], v120 offset:29312\n\t"
        "s_waitcnt lgkmcnt(0)\n\t"
        "s_add_u32 m0, %[m0b], 24576\n\t"
        "s_mov_b32 s81, 1835008\n\t"
        "s_mov_b32 s82, 4\n\t"
        ".Ld11%=:\n\t"
        "buffer_load_dwordx4 v114, %[so], s81 offen lds\n\t"
        "s_add_u32 m0, m0, 0x400\n\t"
        "s_add_u32 s81, s81, 32768\n\t"
        "buffer_load_dwordx4 v114, %[so], s81 offen lds\n\t"
        "s_add_u32 m0, m0, 0x400\n\t"
        "s_add_u32 s81, s81, 32768\n\t"
        "s_sub_u32 s82, s82, 1\n\t"
        "s_cmp_lg_u32 s82, 0\n\t"
        "s_cbranch_scc1 .Ld11%=\n\t"
        "s_waitcnt vmcnt(24)\n\t"
        "ds_read_b64_tr_b16 a[128:129], v119 offset:0\n\t"
        "ds_read_b64_tr_b16 a[130:131], v119 offset:128\n\t"
        "ds_read_b64_tr_b16 a[132:133], v119 offset:4096\n\t"
        "ds_read_b64_tr_b16 a[134:135], v119 offset:4224\n\t"
        "ds_read_b64_tr_b16 a[136:137], v120 offset:0\n\t"
        "ds_read_b64_tr_b16 a[138:139], v120 offset:128\n\t"
        "ds_read_b64_tr_b16 a[140:141], v120 offset:4096\n\t"
        "ds_read_b64_tr_b16 a[142:143], v120 offset:4224\n\t"
        "ds_read_b64_tr_b16 a[144:145], v119 offset:512\n\t"
        "ds_read_b64_tr_b16 a[146:147], v119 offset:640\n\t"
        "ds_read_b64_tr_b16 a[148:149], v119 offset:4608\n\t"
        "ds_read_b64_tr_b16 a[150:151], v119 offset:4736\n\t"
        "ds_read_b64_tr_b16 a[152:153], v120 offset:512\n\t"
        "ds_read_b64_tr_b16 a[154:155], v120 offset:640\n\t"
        "ds_read_b64_tr_b16 a[156:157], v120 offset:4608\n\t"
        "ds_read_b64_tr_b16 a[158:159], v120 offset:4736\n\t"
        "s_waitcnt lgkmcnt(0)\n\t"
        "s_waitcnt vmcnt(16)\n\t"
        "ds_read_b64_tr_b16 a[160:161], v119 offset:8192\n\t"
        "ds_read_b64_tr_b16 a[162:163], v119 offset:8320\n\t"
        "ds_read_b64_tr_b16 a[164:165], v119 offset:12288\n\t"
        "ds_read_b64_tr_b16 a[166:167], v119 offset:12416\n\t"
        "ds_read_b64_tr_b16 a[168:169], v120 offset:8192\n\t"
        "ds_read_b64_tr_b16 a[170:171], v120 offset:8320\n\t"
        "ds_read_b64_tr_b16 a[172:173], v120 offset:12288\n\t"
        "ds_read_b64_tr_b16 a[174:175], v120 offset:12416\n\t"
        "ds_read_b64_tr_b16 a[176:177], v119 offset:8704\n\t"
        "ds_read_b64_tr_b16 a[178:179], v119 offset:8832\n\t"
        "ds_read_b64_tr_b16 a[180:181], v119 offset:12800\n\t"
        "ds_read_b64_tr_b16 a[182:183], v119 offset:12928\n\t"
        "ds_read_b64_tr_b16 a[184:185], v120 offset:8704\n\t"
        "ds_read_b64_tr_b16 a[186:187], v120 offset:8832\n\t"
        "ds_read_b64_tr_b16 a[188:189], v120 offset:12800\n\t"
        "ds_read_b64_tr_b16 a[190:191], v120 offset:12928\n\t"
        "s_waitcnt lgkmcnt(0)\n\t"
        "s_waitcnt vmcnt(8)\n\t"
        "ds_read_b64_tr_b16 a[192:193], v119 offset:16384\n\t"
        "ds_read_b64_tr_b16 a[194:195], v119 offset:16512\n\t"
        "ds_read_b64_tr_b16 a[196:197], v119 offset:20480\n\t"
        "ds_read_b64_tr_b16 a[198:199], v119 offset:20608\n\t"
        "ds_read_b64_tr_b16 a[200:201], v120 offset:16384\n\t"
        "ds_read_b64_tr_b16 a[202:203], v120 offset:16512\n\t"
        "ds_read_b64_tr_b16 a[204:205], v120 offset:20480\n\t"
        "ds_read_b64_tr_b16 a[206:207], v120 offset:20608\n\t"
        "ds_read_b64_tr_b16 a[208:209], v119 offset:16896\n\t"
        "ds_read_b64_tr_b16 a[210:211], v119 offset:17024\n\t"
        "ds_read_b64_tr_b16 a[212:213], v119 offset:20992\n\t"
        "ds_read_b64_tr_b16 a[214:215], v119 offset:21120\n\t"
        "ds_read_b64_tr_b16 a[216:217], v120 offset:16896\n\t"
        "ds_read_b64_tr_b16 a[218:219], v120 offset:17024\n\t"
        "ds_read_b64_tr_b16 a[220:221], v120 offset:20992\n\t"
        "ds_read_b64_tr_b16 a[222:223], v120 offset:21120\n\t"
        "s_waitcnt lgkmcnt(0)\n\t"
        "s_waitcnt vmcnt(0)\n\t"
        "ds_read_b64_tr_b16 a[224:225], v119 offset:24576\n\t"
        "ds_read_b64_tr_b16 a[226:227], v119 offset:24704\n\t"
        "ds_read_b64_tr_b16 a[228:229], v119 offset:28672\n\t"
        "ds_read_b64_tr_b16 a[230:231], v119 offset:28800\n\t"
        "ds_read_b64_tr_b16 a[232:233], v120 offset:24576\n\t"
        "ds_read_b64_tr_b16 a[234:235], v120 offset:24704\n\t"
        "ds_read_b64_tr_b16 a[236:237], v120 offset:28672\n\t"
        "ds_read_b64_tr_b16 a[238:239], v120 offset:28800\n\t"
        "ds_read_b64_tr_b16 a[240:241], v119 offset:25088\n\t"
        "ds_read_b64_tr_b16 a[242:243], v119 offset:25216\n\t"
        "ds_read_b64_tr_b16 a[244:245], v119 offset:29184\n\t"
        "ds_read_b64_tr_b16 a[246:247], v119 offset:29312\n\t"
        "ds_read_b64_tr_b16 a[248:249], v120 offset:25088\n\t"
        "ds_read_b64_tr_b16 a[250:251], v120 offset:25216\n\t"
        "ds_read_b64_tr_b16 a[252:253], v120 offset:29184\n\t"
        "ds_read_b64_tr_b16 a[254:255], v120 offset:29312\n\t"
        "s_waitcnt lgkmcnt(0)"
        : "={a[0:31]}"(wa[0]), "={a[32:63]}"(wa[1]), "={a[64:95]}"(wa[2]), "={a[96:127]}"(wa[3]), "={a[128:159]}"(wa[4]), "={a[160:191]}"(wa[5]), "={a[192:223]}"(wa[6]), "={a[224:255]}"(wa[7]), "={v[128:159]}"(wg[0]), "={v[160:191]}"(wg[1]), "={v[192:223]}"(wg[2]), "={v[224:255]}"(wg[3])
        : [sv] "s"(sv), [so] "s"(so), [wsd] "s"(wsd), [m0b] "s"(m0b), "{v[112:127]}"(pa)
        : "memory", "scc", "m0", "s80", "s81", "s82");
        }
        asm volatile("" : "+v"(cpf), "+v"(m0v));
        __syncthreads();
        tid = otid(); l = tid & 63; n16 = l & 15; g = l >> 4;
        unsigned nm; if (!votes(nm)) return false;
        const unsigned nW = (nm & 1u) << 13, nA = nW + x * 32768, nS = nW + 6 * 16384 + x * 1024 - w * 1024, nC = 32 * 16384 + ((nm & 2u) << 12);
        const unsigned b0 = (64 + (h >> 2)) * 16384 + ((nm & 4u) << 11) + (h & 3) * 2048, b1 = b0 + 8 * 16384;
        const unsigned aw = w < 3 ? (unsigned)(w * 32768 + (cb >> 4) * 16384 + ((cb >> 1) & 7) * 1024 + ((cb & 1) * 2 + (l >> 5)) * 256 + G * 128 + (l & 31) * 4) : OOB;
        v16u pc = {(unsigned)tid * 16, (n16 & 2) ? 0xEEEEu : 0x4444u, (unsigned)((vi & 1) * 1024 + w * 256 + n16 * 16 + g * 4), (unsigned)((G * 4 + w) * 16384 + l * 16), aw,
                   tid < 3 ? (unsigned)(6 * 16384 + l * 1024 + (32 + vi) * 8) : OOB, tid == 192 ? pr_a : OOB, (unsigned)(8192 + w * 512 + (g & 1) * 256 + (g >> 1) * 64 + n16 * 4),
                   (unsigned)((((l >> 2) & 1) + ((l >> 3) & 2)) * 128 + (l >> 5) * 64 + (l & 8) * 4 + (l & 3) * 8), (unsigned)(2048 + (64 * (vi & 1) + 16 * w + n16) * 16 + g * 4),
                   (unsigned)(w * 2048 + l * 8), (unsigned)(((w + 1) & 3) * 2048 + 512 + l * 8), (unsigned)(((w + 2) & 3) * 2048 + 1024 + l * 8), (unsigned)(((w + 3) & 3) * 2048 + 1536 + l * 8), 0u, g ? OOB : (unsigned)((vi & 1) * 1024 + w * 256 + n16 * 16)};
        m0v = min(m0v, 0xFFFFFFFEu);
        st4(rh, aw + nW, m0v);
        if (tid < 2) ((lu32*)(size_t)L_FL)[tid] = ~0u;
        __syncthreads();
    asm volatile(
        "rec_v_code:\n\t"
        "s_mov_b32 s36, 0xf0000000\n\t"
        "s_mov_b32 s38, 0xff00ff00\n\t"
        "s_mov_b32 s39, 0xff00ff00\n\t"
        "s_mov_b32 s40, 0\n\t"
        "s_mov_b32 s37, 0\n\t"
        "s_brev_b32 s54, 1\n\t"
        "s_mov_b32 s46, %c[iD1]\n\t"
        "s_mov_b32 s47, %c[iD2]\n\t"
        "s_mov_b32 s48, 1\n\t"
        "s_mov_b32 s50, %c[LC]\n\t"
        "s_mov_b32 s51, %c[LA]\n\t"
        "s_mov_b32 s52, %c[E1]\n\t"
        "s_mov_b32 s53, %c[E2]\n\t"
        "s_mov_b32 s43, 0\n\t"
        "s_mov_b32 s61, %[b0]\n\t"
        "s_mov_b32 s62, %[b1]\n\t"
        "s_mov_b32 s75, 31744\n\t"
        "s_and_b32 s80, s40, 3\n\t"
        "s_lshl_b32 s80, s80, 17\n\t"
        "s_add_u32 s57, s80, %[nA]\n\t"
        "s_add_u32 s58, s57, 0x1000\n\t"
        "s_add_u32 s59, s57, 0x4000\n\t"
        "s_add_u32 s60, s57, 0x5000\n\t"
        "v_mov_b32_e32 v92, -1\n\t"
        "v_mov_b32_e32 v93, -1\n\t"
        "v_mov_b32_e32 v94, -1\n\t"
        "v_mov_b32_e32 v95, -1\n\t"
        "v_mov_b32_e32 v48, 0\n\t"
        "v_mov_b32_e32 v49, 0\n\t"
        "v_mov_b32_e32 v50, 0\n\t"
        "v_mov_b32_e32 v51, 0\n\t"
        "v_mov_b32_e32 v52, 0\n\t"
        "v_mov_b32_e32 v53, 0\n\t"
        "v_mov_b32_e32 v54, 0\n\t"
        "v_mov_b32_e32 v55, 0\n\t"
        "v_mov_b32_e32 v56, 0\n\t"
        "v_mov_b32_e32 v57, 0\n\t"
        "v_mov_b32_e32 v58, 0\n\t"
        "v_mov_b32_e32 v59, 0\n\t"
        "v_mov_b32_e32 v60, 0\n\t"
        "v_mov_b32_e32 v61, 0\n\t"
        "v_mov_b32_e32 v62, 0\n\t"
        "v_mov_b32_e32 v63, 0\n\t"
        "s_memrealtime s[76:77]\n\t"
        "s_mov_b32 s67, %[nW]\n\t"
        "v_mov_b32_e32 v86, 0\n\t"
        "s_mov_b32 s61, %[b1]\n\t"
        "s_mov_b32 s62, %[b0]\n\t"
        "s_sub_u32 s75, s75, 16\n\t"
        "s_waitcnt lgkmcnt(0)\n\t"
        "s_mov_b32 s41, s76\n\t"
        ".Lloop%=:\n\t"
        "s_mov_b32 s97, 0\n\t"
        "s_movk_i32 s96, 0x1000\n\t"
        ".Lwa%=:\n\t"
        "s_memrealtime s[98:99]\n\t"
        "s_addk_i32 s96, -1\n\t"
        "s_cmp_lt_i32 s96, 0\n\t"
        "s_cbranch_scc1 .Lta%=\n\t"
        "s_waitcnt lgkmcnt(0)\n\t"
        "s_sub_i32 s98, s98, s41\n\t"
        "s_cmp_lt_i32 s98, %c[iFT]\n\t"
        "s_cbranch_scc1 .Lwa%=\n\t"
        "s_cmp_ge_i32 s98, 0\n\t"
        "s_cbranch_scc1 .Lxa%=\n\t"
        ".Lqa%=:\n\t"
        "s_nop %c[iFN]\n\t"
        "s_addk_i32 s98, 1\n\t"
        "s_cmp_lt_i32 s98, 0\n\t"
        "s_cbranch_scc1 .Lqa%=\n\t"
        ".Lxa%=:\n\t"
        "buffer_load_dwordx4 v[0:3], v112, %[srd], s57 offen sc1\n\t"
        "s_nop 7\n\t"
        "buffer_load_dwordx4 v[4:7], v112, %[srd], s58 offen sc1\n\t"
        "s_nop 7\n\t"
        "buffer_load_dwordx4 v[8:11], v112, %[srd], s59 offen sc1\n\t"
        "s_nop 7\n\t"
        "buffer_load_dwordx4 v[12:15], v112, %[srd], s60 offen sc1\n\t"
        "v_mov_b32_e32 v80, 30720\n\t"
        "ds_read_b64 v[80:81], v80\n\t"
        "s_waitcnt lgkmcnt(0)\n\t"
        "v_readfirstlane_b32 s80, v80\n\t"
        "v_readfirstlane_b32 s81, v81\n\t"
        "s_cmp_eq_u32 s80, s40\n\t"
        "s_cselect_b32 s84, 1, 0\n\t"
        "s_cmp_eq_u32 s81, s40\n\t"
        "s_cselect_b32 s81, 4, 0\n\t"
        "s_or_b32 s84, s84, s81\n\t"
        "v_mov_b32_e32 v106, s76\n\t"
        "v_mov_b32_e32 v107, s84\n\t"
        "buffer_store_dwordx2 v[106:107], v117, %[srd], s67 offen sc1\n\t"
        "buffer_store_dword v86, v118, %[wsd], 0 offen sc1\n\t"
        "s_and_b32 s80, s40, 3\n\t"
        "s_lshl_b32 s80, s80, 17\n\t"
        "s_add_u32 s63, s80, %[nC]\n\t"
        "s_add_u32 s66, s80, %[nS]\n\t"
        "s_xor_b32 s81, s80, 0x40000\n\t"
        "s_add_u32 s68, s81, %[nW]\n\t"
        "s_add_u32 s81, s80, 0x20000\n\t"
        "s_and_b32 s81, s81, 0x60000\n\t"
        "s_add_u32 s67, s81, %[nW]\n\t"
        "s_xor_b32 s81, s81, 0x40000\n\t"
        "s_add_u32 s65, s81, %[nC]\n\t"
        "s_add_u32 s74, s81, %[nW]\n\t"
        "s_lshl_b32 s72, s40, 17\n\t"
        "s_add_u32 s72, s72, %[kvo]\n\t"
        "s_mov_b32 s80, s61\n\t"
        "s_mov_b32 s61, s62\n\t"
        "s_mov_b32 s62, s80\n\t"
        ".Lgo%=:\n\t"
        "s_waitcnt vmcnt(5)\n\t"
        "v_smfmac_f32_16x16x64_bf16 v[52:55], v[0:3], v[136:143], v113\n\t"
        "v_smfmac_f32_16x16x64_bf16 v[56:59], v[0:3], v[144:151], v113\n\t"
        "v_smfmac_f32_16x16x64_bf16 v[60:63], v[0:3], v[152:159], v113\n\t"
        "v_smfmac_f32_16x16x64_bf16 v[48:51], v[0:3], v[128:135], v113\n\t"
        "s_waitcnt vmcnt(4)\n\t"
        "v_smfmac_f32_16x16x64_bf16 v[52:55], v[4:7], v[168:175], v113\n\t"
        "v_smfmac_f32_16x16x64_bf16 v[56:59], v[4:7], v[176:183], v113\n\t"
        "v_smfmac_f32_16x16x64_bf16 v[60:63], v[4:7], v[184:191], v113\n\t"
        "v_smfmac_f32_16x16x64_bf16 v[48:51], v[4:7], v[160:167], v113\n\t"
        "s_waitcnt vmcnt(3)\n\t"
        "v_smfmac_f32_16x16x64_bf16 v[52:55], v[8:11], v[200:207], v113\n\t"
        "v_smfmac_f32_16x16x64_bf16 v[56:59], v[8:11], v[208:215], v113\n\t"
        "v_smfmac_f32_16x16x64_bf16 v[60:63], v[8:11], v[216:223], v113\n\t"
        "v_smfmac_f32_16x16x64_bf16 v[48:51], v[8:11], v[192:199], v113\n\t"
        "s_waitcnt vmcnt(2)\n\t"
        "v_smfmac_f32_16x16x64_bf16 v[52:55], v[12:15], v[232:239], v113\n\t"
        "v_smfmac_f32_16x16x64_bf16 v[56:59], v[12:15], v[240:247], v113\n\t"
        "v_smfmac_f32_16x16x64_bf16 v[60:63], v[12:15], v[248:255], v113\n\t"
        "v_smfmac_f32_16x16x64_bf16 v[48:51], v[12:15], v[224:231], v113\n\t"
        "s_nop 1\n\t"
        "v_mov_b32_e32 v104, v104\n\t"
        "s_nop 1\n\t"
        "v_add_f32_e32 v98, v52, v54\n\t"
        "v_add_f32_e32 v99, v53, v55\n\t"
        "ds_write_b64 v123, v[98:99]\n\t"
        "v_add_f32_e32 v100, v56, v58\n\t"
        "v_add_f32_e32 v101, v57, v59\n\t"
        "ds_write_b64 v124, v[100:101]\n\t"
        "v_add_f32_e32 v102, v60, v62\n\t"
        "v_add_f32_e32 v103, v61, v63\n\t"
        "ds_write_b64 v125, v[102:103]\n\t"
        "v_pk_add_f32 v[96:97], v[48:49], v[50:51]\n\t"
        "v_cmp_u_f32_e32 vcc, v96, v97\n\t"
        "s_nop 2\n\t"
        "v_mov_b32_e32 v104, v104\n\t"
        "s_cbranch_vccnz .Lfa%=\n\t"
        ".Lra%=:\n\t"
        "v_mov_b32_e32 v104, v104\n\t"
        "s_nop 2\n\t"
        "v_mov_b32_e32 v104, v104\n\t"
        "s_nop 2\n\t"
        "v_mov_b32_e32 v104, v104\n\t"
        "s_nop 2\n\t"
        "v_mov_b32_e32 v104, v104\n\t"
        "s_waitcnt lgkmcnt(0)\n\t"
        "s_barrier\n\t"
        "v_mov_b32_e32 v104, v104\n\t"
        "ds_read_b64 v[98:99], v122 offset:512\n\t"
        "ds_read_b64 v[100:101], v122 offset:1024\n\t"
        "ds_read_b64 v[102:103], v122 offset:1536\n\t"
        "v_mov_b32_e32 v104, v104\n\t"
        "s_nop 2\n\t"
        "v_mov_b32_e32 v104, v104\n\t"
        "s_nop 2\n\t"
        "v_mov_b32_e32 v104, v104\n\t"
        "s_waitcnt lgkmcnt(2)\n\t"
        "v_pk_add_f32 v[96:97], v[96:97], v[98:99]\n\t"
        "s_waitcnt lgkmcnt(0)\n\t"
        "v_pk_add_f32 v[100:101], v[100:101], v[102:103]\n\t"
        "v_pk_add_f32 v[96:97], v[96:97], v[100:101]\n\t"
        "v_cvt_pk_bf16_f32 v80, v96, v97\n\t"
        "v_min_u32_e32 v80, -2, v80\n\t"
        "buffer_store_dword v80, v114, %[srd], s61 offen sc1\n\t"
        "s_add_u32 s80, s40, 1\n\t"
        "v_mov_b32_e32 v86, s80\n\t"
        "s_cmp_eq_u32 s40, 63\n\t"
        "s_cbranch_scc1 .Lend%=\n\t"
        "buffer_load_dwordx4 v[88:91], v112, %[srd], s66 offen sc1\n\t"
        "v_mov_b32_e32 v64, 0\n\t"
        "v_mov_b32_e32 v65, 0\n\t"
        "v_mov_b32_e32 v66, 0\n\t"
        "v_mov_b32_e32 v67, 0\n\t"
        "v_mov_b32_e32 v68, 0\n\t"
        "v_mov_b32_e32 v69, 0\n\t"
        "v_mov_b32_e32 v70, 0\n\t"
        "v_mov_b32_e32 v71, 0\n\t"
        "v_mov_b32_e32 v72, 0\n\t"
        "v_mov_b32_e32 v73, 0\n\t"
        "v_mov_b32_e32 v74, 0\n\t"
        "v_mov_b32_e32 v75, 0\n\t"
        "v_mov_b32_e32 v76, 0\n\t"
        "v_mov_b32_e32 v77, 0\n\t"
        "v_mov_b32_e32 v78, 0\n\t"
        "v_mov_b32_e32 v79, 0\n\t"
        "s_add_u32 s64, s63, 0x1000\n\t"
        "s_sleep %c[VSLP]\n\t"
        "buffer_store_dword v80, v114, %[wsd], s72 offen sc1\n\t"
        "buffer_store_dword v92, v114, %[srd], s62 offen sc1\n\t"
        "buffer_store_dword v92, v116, %[srd], s74 offen sc1\n\t"
        "buffer_store_dwordx2 v[92:93], v117, %[srd], s68 offen sc1\n\t"
        "s_waitcnt lgkmcnt(0)\n\t"
        "v_mov_b32_e32 v48, 0\n\t"
        "v_mov_b32_e32 v49, 0\n\t"
        "v_mov_b32_e32 v50, 0\n\t"
        "v_mov_b32_e32 v51, 0\n\t"
        "v_mov_b32_e32 v52, 0\n\t"
        "v_mov_b32_e32 v53, 0\n\t"
        "v_mov_b32_e32 v54, 0\n\t"
        "v_mov_b32_e32 v55, 0\n\t"
        "v_mov_b32_e32 v56, 0\n\t"
        "v_mov_b32_e32 v57, 0\n\t"
        "v_mov_b32_e32 v58, 0\n\t"
        "v_mov_b32_e32 v59, 0\n\t"
        "v_mov_b32_e32 v60, 0\n\t"
        "v_mov_b32_e32 v61, 0\n\t"
        "v_mov_b32_e32 v62, 0\n\t"
        "v_mov_b32_e32 v63, 0\n\t"
        "s_add_u32 s81, s40, 1\n\t"
        "s_and_b32 s80, s81, 3\n\t"
        "s_lshl_b32 s80, s80, 17\n\t"
        "s_add_u32 s57, s80, %[nA]\n\t"
        "s_add_u32 s58, s57, 0x1000\n\t"
        "s_add_u32 s59, s57, 0x4000\n\t"
        "s_add_u32 s60, s57, 0x5000\n\t"
        "s_waitcnt vmcnt(4)\n\t"
        "s_mov_b32 s97, 0\n\t"
        ".Lsr%=:\n\t"
        "s_mov_b32 exec_hi, 0xffff\n\t"
        "v_subrev_u32_e32 v4, s41, v88\n\t"
        "v_subrev_u32_e32 v5, s41, v90\n\t"
        "v_max_i32_e32 v4, v4, v5\n\t"
        "v_max_u32_e32 v5, v89, v91\n\t"
        "v_cmp_eq_u32_e32 vcc, -1, v5\n\t"
        "v_max_i32_dpp v4, v4, v4 quad_perm:[1,0,3,2] row_mask:0xf bank_mask:0xf\n\t"
        "v_or_b32_e32 v6, v89, v91\n\t"
        "s_nop 0\n\t"
        "v_max_i32_dpp v4, v4, v4 quad_perm:[2,3,0,1] row_mask:0xf bank_mask:0xf\n\t"
        "s_nop 1\n\t"
        "v_max_i32_dpp v4, v4, v4 row_ror:4 row_mask:0xf bank_mask:0xf\n\t"
        "s_nop 1\n\t"
        "v_max_i32_dpp v4, v4, v4 row_ror:8 row_mask:0xf bank_mask:0xf\n\t"
        "s_cbranch_vccnz .Lsf%=\n\t"
        "v_readlane_b32 s80, v4, 0\n\t"
        "v_readlane_b32 s81, v4, 16\n\t"
        "v_readlane_b32 s56, v4, 32\n\t"
        "v_and_b32_e32 v5, 1, v6\n\t"
        "v_cmp_ne_u32_e32 vcc, 0, v5\n\t"
        "s_nop 1\n\t"
        "s_cmp_lg_u64 vcc, 0\n\t"
        "s_cselect_b32 s86, 1, 0\n\t"
        "v_and_b32_e32 v5, 2, v6\n\t"
        "v_cmp_ne_u32_e32 vcc, 0, v5\n\t"
        "s_nop 1\n\t"
        "s_cmp_lg_u64 vcc, 0\n\t"
        "s_cselect_b32 s87, 1, 0\n\t"
        "v_and_b32_e32 v5, 4, v6\n\t"
        "v_cmp_ne_u32_e32 vcc, 0, v5\n\t"
        "s_nop 1\n\t"
        "s_cmp_lg_u64 vcc, 0\n\t"
        "s_cselect_b32 s88, 1, 0\n\t"
        "s_max_i32 s56, s56, s81\n\t"
        "s_mov_b32 s85, s80\n\t"
        "s_add_u32 s92, s80, s41\n\t"
        "s_sub_u32 s92, s92, s43\n\t"
        "s_add_i32 s93, s56, s47\n\t"
        "s_mov_b32 s95, s47\n\t"
        "s_cmp_lt_u32 s40, %c[iCT0]\n\t"
        "s_cbranch_scc1 .Lc0%=\n\t"
        "s_or_b32 s80, s48, s86\n\t"
        "s_or_b32 s80, s80, s87\n\t"
        "s_xor_b32 s90, s80, 1\n\t"
        "s_cmp_le_i32 s92, s46\n\t"
        "s_cselect_b32 s80, 1, 0\n\t"
        "s_andn2_b32 s91, s80, s88\n\t"
        "s_and_b32 s80, s40, %c[LDEC]\n\t"
        "s_cmp_eq_u32 s80, 0\n\t"
        "s_cselect_b32 s99, 1, 0\n\t"
        "s_mov_b32 s80, s88\n\t"
        "s_add_i32 s81, s92, s50\n\t"
        "s_sub_i32 s81, s81, 4\n\t"
        "s_cmp_le_i32 s81, s46\n\t"
        "s_cselect_b32 s80, s80, 0\n\t"
        "s_cmp_lt_i32 s50, %c[LC24]\n\t"
        "s_cselect_b32 s80, s80, 0\n\t"
        "s_lshl_b32 s80, s80, 2\n\t"
        "s_add_i32 s50, s50, s80\n\t"
        "s_sub_i32 s50, s50, s99\n\t"
        "s_max_i32 s50, s50, %c[LC]\n\t"
        "s_lshl_b32 s80, s92, 1\n\t"
        "s_sub_i32 s81, s80, s52\n\t"
        "s_ashr_i32 s82, s81, %c[UPSH]\n\t"
        "s_max_i32 s82, s82, %c[TRUP]\n\t"
        "s_add_i32 s82, s82, s52\n\t"
        "s_min_i32 s82, s82, s80\n\t"
        "s_sub_i32 s83, 0, s81\n\t"
        "s_ashr_i32 s83, s83, 1\n\t"
        "s_max_i32 s83, s83, %c[TRDN]\n\t"
        "s_sub_i32 s83, s52, s83\n\t"
        "s_max_i32 s83, s83, s80\n\t"
        "s_cmp_gt_i32 s81, 0\n\t"
        "s_cselect_b32 s82, s82, s83\n\t"
        "s_cmp_lg_u32 s90, 0\n\t"
        "s_cselect_b32 s52, s82, s52\n\t"
        "s_andn2_b32 s80, s86, s48\n\t"
        "s_cmp_lt_i32 s51, %c[LA24]\n\t"
        "s_cselect_b32 s80, s80, 0\n\t"
        "s_lshl_b32 s80, s80, 2\n\t"
        "s_add_i32 s51, s51, s80\n\t"
        "s_sub_i32 s51, s51, s99\n\t"
        "s_max_i32 s51, s51, %c[LA]\n\t"
        "s_lshl_b32 s80, s93, 1\n\t"
        "s_sub_i32 s81, s80, s53\n\t"
        "s_ashr_i32 s82, s81, %c[UPSH]\n\t"
        "s_max_i32 s82, s82, %c[TRUP]\n\t"
        "s_add_i32 s82, s82, s53\n\t"
        "s_min_i32 s82, s82, s80\n\t"
        "s_sub_i32 s83, 0, s81\n\t"
        "s_ashr_i32 s83, s83, 1\n\t"
        "s_max_i32 s83, s83, %c[TRDN]\n\t"
        "s_sub_i32 s83, s53, s83\n\t"
        "s_max_i32 s83, s83, s80\n\t"
        "s_cmp_gt_i32 s81, 0\n\t"
        "s_cselect_b32 s82, s82, s83\n\t"
        "s_cmp_lg_u32 s91, 0\n\t"
        "s_cselect_b32 s53, s82, s53\n\t"
        ".Lc0%=:\n\t"
        "s_add_i32 s80, s52, 1\n\t"
        "s_ashr_i32 s80, s80, 1\n\t"
        "s_add_i32 s46, s80, s50\n\t"
        "s_add_i32 s80, s53, 1\n\t"
        "s_ashr_i32 s80, s80, 1\n\t"
        "s_add_i32 s47, s80, s51\n\t"
        "s_lshl_b32 s80, s87, 1\n\t"
        "s_or_b32 s80, s80, s86\n\t"
        "s_lshl_b32 s81, s88, 2\n\t"
        "s_or_b32 s94, s80, s81\n\t"
        "s_mov_b32 s43, s41\n\t"
        "s_cmp_gt_i32 s56, s54\n\t"
        "s_cbranch_scc1 .Lmv%=\n\t"
        "s_mov_b32 s37, 0\n\t"
        "s_mov_b32 s48, 0\n\t"
        ".Lsd%=:\n\t"
        "s_mov_b32 exec_hi, -1\n\t"
        "s_add_u32 s42, s43, s46\n\t"
        "s_add_u32 s41, s42, s47\n\t"
        "s_mov_b32 s97, 0\n\t"
        "s_movk_i32 s96, 0x1000\n\t"
        ".Lwc%=:\n\t"
        "s_memrealtime s[98:99]\n\t"
        "s_addk_i32 s96, -1\n\t"
        "s_cmp_lt_i32 s96, 0\n\t"
        "s_cbranch_scc1 .Ltc%=\n\t"
        "s_waitcnt lgkmcnt(0)\n\t"
        "s_sub_i32 s98, s98, s42\n\t"
        "s_cmp_lt_i32 s98, %c[iFT]\n\t"
        "s_cbranch_scc1 .Lwc%=\n\t"
        "s_cmp_ge_i32 s98, 0\n\t"
        "s_cbranch_scc1 .Lxc%=\n\t"
        ".Lqc%=:\n\t"
        "s_nop %c[iFN]\n\t"
        "s_addk_i32 s98, 1\n\t"
        "s_cmp_lt_i32 s98, 0\n\t"
        "s_cbranch_scc1 .Lqc%=\n\t"
        ".Lxc%=:\n\t"
        "buffer_load_dwordx4 v[16:19], v115, %[srd], s63 offen offset:0 sc1\n\t"
        "s_nop 7\n\t"
        "buffer_load_dwordx4 v[20:23], v115, %[srd], s63 offen offset:1024 sc1\n\t"
        "s_nop 7\n\t"
        "buffer_load_dwordx4 v[24:27], v115, %[srd], s63 offen offset:2048 sc1\n\t"
        "s_nop 7\n\t"
        "buffer_load_dwordx4 v[28:31], v115, %[srd], s63 offen offset:3072 sc1\n\t"
        "s_nop 7\n\t"
        "buffer_load_dwordx4 v[32:35], v115, %[srd], s64 offen offset:0 sc1\n\t"
        "s_nop 7\n\t"
        "buffer_load_dwordx4 v[36:39], v115, %[srd], s64 offen offset:1024 sc1\n\t"
        "s_nop 7\n\t"
        "buffer_load_dwordx4 v[40:43], v115, %[srd], s64 offen offset:2048 sc1\n\t"
        "s_nop 7\n\t"
        "buffer_load_dwordx4 v[44:47], v115, %[srd], s64 offen offset:3072 sc1\n\t"
        ".Lrc%=:\n\t"
        "s_waitcnt vmcnt(7)\n\t"
        "v_smfmac_f32_16x16x64_bf16 v[64:67], v[16:19], a[0:7], v113\n\t"
        "v_smfmac_f32_16x16x64_bf16 v[68:71], v[16:19], a[8:15], v113\n\t"
        "v_smfmac_f32_16x16x64_bf16 v[72:75], v[16:19], a[16:23], v113\n\t"
        "v_smfmac_f32_16x16x64_bf16 v[76:79], v[16:19], a[24:31], v113\n\t"
        "s_waitcnt vmcnt(6)\n\t"
        "v_smfmac_f32_16x16x64_bf16 v[64:67], v[20:23], a[32:39], v113\n\t"
        "v_smfmac_f32_16x16x64_bf16 v[68:71], v[20:23], a[40:47], v113\n\t"
        "v_smfmac_f32_16x16x64_bf16 v[72:75], v[20:23], a[48:55], v113\n\t"
        "v_smfmac_f32_16x16x64_bf16 v[76:79], v[20:23], a[56:63], v113\n\t"
        "s_waitcnt vmcnt(5)\n\t"
        "v_smfmac_f32_16x16x64_bf16 v[64:67], v[24:27], a[64:71], v113\n\t"
        "v_smfmac_f32_16x16x64_bf16 v[68:71], v[24:27], a[72:79], v113\n\t"
        "v_smfmac_f32_16x16x64_bf16 v[72:75], v[24:27], a[80:87], v113\n\t"
        "v_smfmac_f32_16x16x64_bf16 v[76:79], v[24:27], a[88:95], v113\n\t"
        "s_waitcnt vmcnt(4)\n\t"
        "v_smfmac_f32_16x16x64_bf16 v[64:67], v[28:31], a[96:103], v113\n\t"
        "v_smfmac_f32_16x16x64_bf16 v[68:71], v[28:31], a[104:111], v113\n\t"
        "v_smfmac_f32_16x16x64_bf16 v[72:75], v[28:31], a[112:119], v113\n\t"
        "v_smfmac_f32_16x16x64_bf16 v[76:79], v[28:31], a[120:127], v113\n\t"
        "s_waitcnt vmcnt(3)\n\t"
        "v_smfmac_f32_16x16x64_bf16 v[64:67], v[32:35], a[128:135], v113\n\t"
        "v_smfmac_f32_16x16x64_bf16 v[68:71], v[32:35], a[136:143], v113\n\t"
        "v_smfmac_f32_16x16x64_bf16 v[72:75], v[32:35], a[144:151], v113\n\t"
        "v_smfmac_f32_16x16x64_bf16 v[76:79], v[32:35], a[152:159], v113\n\t"
        "s_waitcnt vmcnt(2)\n\t"
        "v_smfmac_f32_16x16x64_bf16 v[64:67], v[36:39], a[160:167], v113\n\t"
        "v_smfmac_f32_16x16x64_bf16 v[68:71], v[36:39], a[168:175], v113\n\t"
        "v_smfmac_f32_16x16x64_bf16 v[72:75], v[36:39], a[176:183], v113\n\t"
        "v_smfmac_f32_16x16x64_bf16 v[76:79], v[36:39], a[184:191], v113\n\t"
        "s_waitcnt vmcnt(1)\n\t"
        "v_smfmac_f32_16x16x64_bf16 v[64:67], v[40:43], a[192:199], v113\n\t"
        "v_smfmac_f32_16x16x64_bf16 v[68:71], v[40:43], a[200:207], v113\n\t"
        "v_smfmac_f32_16x16x64_bf16 v[72:75], v[40:43], a[208:215], v113\n\t"
        "v_smfmac_f32_16x16x64_bf16 v[76:79], v[40:43], a[216:223], v113\n\t"
        "s_waitcnt vmcnt(0)\n\t"
        "v_smfmac_f32_16x16x64_bf16 v[64:67], v[44:47], a[224:231], v113\n\t"
        "s_nop 2\n\t"
        "v_smfmac_f32_16x16x64_bf16 v[68:71], v[44:47], a[232:239], v113\n\t"
        "s_cmp_gt_u32 %[w], 2\n\t"
        "s_nop 1\n\t"
        "v_smfmac_f32_16x16x64_bf16 v[72:75], v[44:47], a[240:247], v113\n\t"
        "v_add_f32_e32 v80, v64, v66\n\t"
        "s_nop 0\n\t"
        "v_smfmac_f32_16x16x64_bf16 v[76:79], v[44:47], a[248:255], v113\n\t"
        "v_add_f32_e32 v81, v65, v67\n\t"
        "v_add_f32_e32 v82, v68, v70\n\t"
        "v_add_f32_e32 v83, v69, v71\n\t"
        "s_mov_b32 exec_lo, 0\n\t"
        "s_nop 0\n\t"
        "v_add_f32_e32 v80, v72, v74\n\t"
        "v_add_f32_e32 v81, v73, v75\n\t"
        "s_nop 0\n\t"
        "v_pk_add_f32 v[82:83], v[76:77], v[78:79]\n\t"
        "s_mov_b32 exec_lo, -1\n\t"
        "s_nop 0\n\t"
        "v_permlane32_swap_b32_e32 v80, v82\n\t"
        "v_add_f32_e32 v80, v80, v82\n\t"
        "ds_write_b32 v119, v80\n\t"
        "v_permlane32_swap_b32_e32 v81, v83\n\t"
        "v_add_f32_e32 v81, v81, v83\n\t"
        "ds_write_b32 v119, v81 offset:128\n\t"
        "v_cmp_u_f32_e32 vcc, v80, v81\n\t"
        "s_nop 2\n\t"
        "v_mov_b32_e32 v104, v104\n\t"
        "s_cbranch_vccnz .Lfc%=\n\t"
        ".Lrd%=:\n\t"
        "v_mov_b32_e32 v104, v104\n\t"
        "s_cbranch_scc1 .Lno%=\n\t"
        "v_mov_b32_e32 v104, v104\n\t"
        "s_nop 2\n\t"
        "v_mov_b32_e32 v104, v104\n\t"
        "s_waitcnt lgkmcnt(0)\n\t"
        "s_barrier\n\t"
        "v_mov_b32_e32 v104, v104\n\t"
        "ds_read_b64 v[0:1], v120 offset:8192\n\t"
        "ds_read_b64 v[2:3], v120 offset:8704\n\t"
        "ds_read_b64 v[4:5], v120 offset:9216\n\t"
        "ds_read_b64 v[6:7], v120 offset:9728\n\t"
        "v_mov_b32_e32 v104, v104\n\t"
        "s_nop 2\n\t"
        "v_mov_b32_e32 v104, v104\n\t"
        "s_nop 2\n\t"
        "v_mov_b32_e32 v104, v104\n\t"
        "s_waitcnt lgkmcnt(2)\n\t"
        "v_pk_add_f32 v[0:1], v[0:1], v[2:3]\n\t"
        "s_waitcnt lgkmcnt(0)\n\t"
        "v_pk_add_f32 v[4:5], v[4:5], v[6:7]\n\t"
        "v_pk_add_f32 v[0:1], v[0:1], v[4:5]\n\t"
        "v_cvt_pk_bf16_f32 v0, v0, v1\n\t"
        "v_min_u32_e32 v0, -2, v0\n\t"
        "buffer_store_dword v0, v116, %[srd], s67 offen sc1\n\t"
        ".Lnp%=:\n\t"
        "s_memrealtime s[76:77]\n\t"
        "s_add_u32 s40, s40, 1\n\t"
        "s_branch .Lloop%=\n\t"
        ".Lmv%=:\n\t"
        "s_mov_b32 s48, 1\n\t"
        "s_cmp_eq_u32 s54, %c[iTOL]\n\t"
        "s_cbranch_scc1 .Lm1%=\n\t"
        "s_max_i32 s80, s85, s56\n\t"
        "s_add_u32 s43, s41, s80\n\t"
        "s_add_u32 s43, s43, %c[iPEN0]\n\t"
        "s_mov_b32 s54, %c[iTOL]\n\t"
        "s_add_u32 s46, s46, %c[iX0C]\n\t"
        "s_add_u32 s47, s47, %c[iX0A]\n\t"
        "v_and_b32_e32 v4, 3, v88\n\t"
        "v_and_b32_e32 v5, 3, v90\n\t"
        "v_lshlrev_b32_e64 v4, v4, 1\n\t"
        "v_lshlrev_b32_e64 v5, v5, 1\n\t"
        "v_or_b32_e32 v4, v4, v5\n\t"
        "s_mov_b32 s80, 0\n\t"
        "v_and_b32_e32 v5, 1, v4\n\t"
        "v_cmp_ne_u32_e32 vcc, 0, v5\n\t"
        "s_nop 1\n\t"
        "s_cmp_lg_u64 vcc, 0\n\t"
        "s_cselect_b32 s81, 2, 0\n\t"
        "s_or_b32 s80, s80, s81\n\t"
        "v_and_b32_e32 v5, 2, v4\n\t"
        "v_cmp_ne_u32_e32 vcc, 0, v5\n\t"
        "s_nop 1\n\t"
        "s_cmp_lg_u64 vcc, 0\n\t"
        "s_cselect_b32 s81, 4, 0\n\t"
        "s_or_b32 s80, s80, s81\n\t"
        "v_and_b32_e32 v5, 4, v4\n\t"
        "v_cmp_ne_u32_e32 vcc, 0, v5\n\t"
        "s_nop 1\n\t"
        "s_cmp_lg_u64 vcc, 0\n\t"
        "s_cselect_b32 s81, 8, 0\n\t"
        "s_or_b32 s80, s80, s81\n\t"
        "v_and_b32_e32 v5, 8, v4\n\t"
        "v_cmp_ne_u32_e32 vcc, 0, v5\n\t"
        "s_nop 1\n\t"
        "s_cmp_lg_u64 vcc, 0\n\t"
        "s_cselect_b32 s81, 16, 0\n\t"
        "s_or_b32 s80, s80, s81\n\t"
        "s_lshr_b32 s80, 0x1adf1210, s80\n\t"
        "s_and_b32 s55, s80, 3\n\t"
        "s_branch .Lal%=\n\t"
        ".Lm1%=:\n\t"
        "s_add_u32 s43, s41, s56\n\t"
        "s_add_u32 s80, s37, %c[iPEN2]\n\t"
        "s_min_i32 s80, s80, %c[iPENM]\n\t"
        "s_cmp_lg_u32 s37, 0\n\t"
        "s_cselect_b32 s37, s80, %c[iPEN]\n\t"
        "s_add_u32 s80, s56, s37\n\t"
        "s_max_i32 s80, s80, %c[iPMIN]\n\t"
        "s_add_u32 s43, s41, s80\n\t"
        "s_add_u32 s80, s56, s95\n\t"
        "s_add_u32 s80, s80, %c[LA]\n\t"
        "s_add_u32 s81, s47, %c[AUP]\n\t"
        "s_min_u32 s80, s80, s81\n\t"
        "s_max_u32 s47, s47, s80\n\t"
        ".Lal%=:\n\t"
        "s_sub_u32 s80, s55, s43\n\t"
        "s_and_b32 s80, s80, %c[iPHM]\n\t"
        "s_add_u32 s43, s43, s80\n\t"
        "s_branch .Lsd%=\n\t"
        ".Lsf%=:\n\t"
        "s_mov_b32 exec_hi, -1\n\t"
        "s_sleep 2\n\t"
        "buffer_load_dwordx4 v[88:91], v112, %[srd], s66 offen sc1\n\t"
        "s_add_u32 s97, s97, 1\n\t"
        "s_cmp_lt_u32 s97, 0x100000\n\t"
        "s_cbranch_scc0 .Lgu%=\n\t"
        "s_waitcnt vmcnt(0)\n\t"
        "s_branch .Lsr%=\n\t"
        ".Lfa%=:\n\t"
        "v_max_u32_e32 v84, v0, v2\n\t"
        "v_max3_u32 v84, v84, v1, v3\n\t"
        "v_max3_u32 v84, v84, v4, v6\n\t"
        "v_max3_u32 v84, v84, v5, v7\n\t"
        "v_max3_u32 v84, v84, v8, v10\n\t"
        "v_max3_u32 v84, v84, v9, v11\n\t"
        "v_max3_u32 v84, v84, v12, v14\n\t"
        "v_max3_u32 v84, v84, v13, v15\n\t"
        "v_cmp_eq_u32_e32 vcc, -1, v84\n\t"
        "s_nop 1\n\t"
        "v_mov_b32_e32 v104, v104\n\t"
        "s_nop 0\n\t"
        "s_cbranch_vccz .Lra%=\n\t"
        "s_add_u32 s80, s40, 1\n\t"
        "v_mov_b32_e32 v80, 30720\n\t"
        "v_mov_b32_e32 v81, s80\n\t"
        "ds_write_b32 v80, v81\n\t"
        ".Lfp%=:\n\t"
        "s_sleep %c[ASLP]\n\t"
        "buffer_load_dwordx4 v[0:3], v112, %[srd], s57 offen sc1\n\t"
        "buffer_load_dwordx4 v[4:7], v112, %[srd], s58 offen sc1\n\t"
        "buffer_load_dwordx4 v[8:11], v112, %[srd], s59 offen sc1\n\t"
        "buffer_load_dwordx4 v[12:15], v112, %[srd], s60 offen sc1\n\t"
        "s_add_u32 s97, s97, 1\n\t"
        "s_cmp_lt_u32 s97, 0x100000\n\t"
        "s_cbranch_scc0 .Lgu%=\n\t"
        "s_waitcnt vmcnt(0)\n\t"
        "v_max_u32_e32 v84, v0, v2\n\t"
        "v_max3_u32 v84, v84, v1, v3\n\t"
        "v_max3_u32 v84, v84, v4, v6\n\t"
        "v_max3_u32 v84, v84, v5, v7\n\t"
        "v_max3_u32 v84, v84, v8, v10\n\t"
        "v_max3_u32 v84, v84, v9, v11\n\t"
        "v_max3_u32 v84, v84, v12, v14\n\t"
        "v_max3_u32 v84, v84, v13, v15\n\t"
        "v_cmp_eq_u32_e32 vcc, -1, v84\n\t"
        "s_nop 1\n\t"
        "v_mov_b32_e32 v104, v104\n\t"
        "s_nop 0\n\t"
        "s_cbranch_vccnz .Lfp%=\n\t"
        "v_mov_b32_e32 v48, 0\n\t"
        "v_mov_b32_e32 v49, 0\n\t"
        "v_mov_b32_e32 v50, 0\n\t"
        "v_mov_b32_e32 v51, 0\n\t"
        "v_mov_b32_e32 v52, 0\n\t"
        "v_mov_b32_e32 v53, 0\n\t"
        "v_mov_b32_e32 v54, 0\n\t"
        "v_mov_b32_e32 v55, 0\n\t"
        "v_mov_b32_e32 v56, 0\n\t"
        "v_mov_b32_e32 v57, 0\n\t"
        "v_mov_b32_e32 v58, 0\n\t"
        "v_mov_b32_e32 v59, 0\n\t"
        "v_mov_b32_e32 v60, 0\n\t"
        "v_mov_b32_e32 v61, 0\n\t"
        "v_mov_b32_e32 v62, 0\n\t"
        "v_mov_b32_e32 v63, 0\n\t"
        "s_nop 1\n\t"
        "s_branch .Lgo%=\n\t"
        ".Lfc%=:\n\t"
        "v_max_u32_e32 v84, v16, v18\n\t"
        "v_max3_u32 v84, v84, v20, v22\n\t"
        "v_max3_u32 v84, v84, v24, v26\n\t"
        "v_max3_u32 v84, v84, v28, v30\n\t"
        "v_max3_u32 v84, v84, v32, v34\n\t"
        "v_max3_u32 v84, v84, v36, v38\n\t"
        "v_max3_u32 v84, v84, v40, v42\n\t"
        "v_max3_u32 v84, v84, v44, v46\n\t"
        "v_cmp_eq_u32_e32 vcc, -1, v84\n\t"
        "s_nop 1\n\t"
        "v_mov_b32_e32 v104, v104\n\t"
        "s_nop 0\n\t"
        "s_cbranch_vccz .Lrd%=\n\t"
        "s_add_u32 s80, s40, 1\n\t"
        "v_mov_b32_e32 v80, 30724\n\t"
        "v_mov_b32_e32 v81, s80\n\t"
        "ds_write_b32 v80, v81\n\t"
        ".Lfq%=:\n\t"
        "s_sleep %c[ASLP]\n\t"
        "buffer_load_dwordx4 v[16:19], v115, %[srd], s63 offen offset:0 sc1\n\t"
        "buffer_load_dwordx4 v[20:23], v115, %[srd], s63 offen offset:1024 sc1\n\t"
        "buffer_load_dwordx4 v[24:27], v115, %[srd], s63 offen offset:2048 sc1\n\t"
        "buffer_load_dwordx4 v[28:31], v115, %[srd], s63 offen offset:3072 sc1\n\t"
        "buffer_load_dwordx4 v[32:35], v115, %[srd], s64 offen offset:0 sc1\n\t"
        "buffer_load_dwordx4 v[36:39], v115, %[srd], s64 offen offset:1024 sc1\n\t"
        "buffer_load_dwordx4 v[40:43], v115, %[srd], s64 offen offset:2048 sc1\n\t"
        "buffer_load_dwordx4 v[44:47], v115, %[srd], s64 offen offset:3072 sc1\n\t"
        "s_add_u32 s97, s97, 1\n\t"
        "s_cmp_lt_u32 s97, 0x100000\n\t"
        "s_cbranch_scc0 .Lgu%=\n\t"
        "s_waitcnt vmcnt(0)\n\t"
        "v_max_u32_e32 v84, v16, v18\n\t"
        "v_max3_u32 v84, v84, v20, v22\n\t"
        "v_max3_u32 v84, v84, v24, v26\n\t"
        "v_max3_u32 v84, v84, v28, v30\n\t"
        "v_max3_u32 v84, v84, v32, v34\n\t"
        "v_max3_u32 v84, v84, v36, v38\n\t"
        "v_max3_u32 v84, v84, v40, v42\n\t"
        "v_max3_u32 v84, v84, v44, v46\n\t"
        "v_cmp_eq_u32_e32 vcc, -1, v84\n\t"
        "s_nop 1\n\t"
        "v_mov_b32_e32 v104, v104\n\t"
        "s_nop 0\n\t"
        "s_cbranch_vccnz .Lfq%=\n\t"
        "v_mov_b32_e32 v64, 0\n\t"
        "v_mov_b32_e32 v65, 0\n\t"
        "v_mov_b32_e32 v66, 0\n\t"
        "v_mov_b32_e32 v67, 0\n\t"
        "v_mov_b32_e32 v68, 0\n\t"
        "v_mov_b32_e32 v69, 0\n\t"
        "v_mov_b32_e32 v70, 0\n\t"
        "v_mov_b32_e32 v71, 0\n\t"
        "v_mov_b32_e32 v72, 0\n\t"
        "v_mov_b32_e32 v73, 0\n\t"
        "v_mov_b32_e32 v74, 0\n\t"
        "v_mov_b32_e32 v75, 0\n\t"
        "v_mov_b32_e32 v76, 0\n\t"
        "v_mov_b32_e32 v77, 0\n\t"
        "v_mov_b32_e32 v78, 0\n\t"
        "v_mov_b32_e32 v79, 0\n\t"
        "s_nop 1\n\t"
        "s_branch .Lrc%=\n\t"
        ".Lno%=:\n\t"
        "s_waitcnt lgkmcnt(0)\n\t"
        "s_barrier\n\t"
        "s_branch .Lnp%=\n\t"
        ".Lta%=:\n\t"
        "s_waitcnt lgkmcnt(0)\n\t"
        "s_branch .Lxa%=\n\t"
        ".Ltc%=:\n\t"
        "s_waitcnt lgkmcnt(0)\n\t"
        "s_branch .Lxc%=\n\t"
        ".Lgu%=:\n\t"
        "s_mov_b64 exec, -1\n\t"
        "v_mov_b32_e32 v80, %c[ferr]\n\t"
        "v_mov_b32_e32 v81, 2\n\t"
        "buffer_store_dword v81, v80, %[wsd], 0 offen sc1\n\t"
        "s_endpgm\n\t"
        ".Lend%=:\n\t"
        "buffer_store_dword v80, v114, %[wsd], s72 offen sc1\n\t"
        "s_waitcnt vmcnt(0)\n\t"
        "s_barrier\n\t"
        "buffer_store_dword v86, v118, %[wsd], 0 offen sc1\n\t"
        "s_endpgm"
        :
        : [srd] "s"(rhd), [wsd] "s"(wsd), [w] "s"(w), [nA] "s"(nA), [nS] "s"(nS), [nW] "s"(nW), [nC] "s"(nC), [b0] "s"(b0), [b1] "s"(b1), [kvo] "s"(kvo),
          [ferr] "i"(F_ERR), [iD1] "i"(D1), [iD2] "i"(D2), [LC] "i"(LC0), [LA] "i"(LA0), [LC24] "i"(LC0 + 24), [LA24] "i"(LA0 + 24), [E1] "i"(2 * (D1 - LC0)), [E2] "i"(2 * (D2 - LA0)),
          [iPEN] "i"(PEN), [iPEN2] "i"(PEN2), [iPENM] "i"(PEN + 2 * PEN2), [iPEN0] "i"(PEN0), [iTOL] "i"(TOL), [iCT0] "i"(CT0), [iX0C] "i"(X0C), [iX0A] "i"(X0A), [iPHM] "i"(PH_M),
          [iFT] "i"(-3), [iFN] "i"(FINE_N), [AUP] "i"(A_UP), [ASLP] "i"(A_SLP), [LDEC] "i"(L_DEC), [UPSH] "i"(UP_SH), [TRUP] "i"(TR_UP), [TRDN] "i"(TR_DN), [HSLP] "i"(H_SLP), [iPMIN] "i"(PMIN), [VSLP] "i"(V_SLP), [RSLP] "i"(R_SLP),
          "{v[112:127]}"(pc), "{a[0:31]}"(wa[0]), "{a[32:63]}"(wa[1]), "{a[64:95]}"(wa[2]), "{a[96:127]}"(wa[3]), "{a[128:159]}"(wa[4]), "{a[160:191]}"(wa[5]), "{a[192:223]}"(wa[6]), "{a[224:255]}"(wa[7]), "{v[128:159]}"(wg[0]), "{v[160:191]}"(wg[1]), "{v[192:223]}"(wg[2]), "{v[224:255]}"(wg[3])
        : "memory", "vcc", "scc", "s36", "s37", "s38", "s39", "s40", "s41", "s42", "s43", "s44", "s45", "s46", "s47", "s48", "s49", "s50", "s51", "s52", "s53", "s54", "s55", "s56", "s57", "s58", "s59", "s60", "s61", "s62", "s63", "s64", "s65", "s66", "s67", "s68", "s69", "s70", "s71", "s72", "s73", "s74", "s75", "s76", "s77", "s78", "s79", "s80", "s81", "s82", "s83", "s84", "s85", "s86", "s87", "s88", "s89", "s90", "s91", "s92", "s93", "s94", "s95", "s96", "s97", "s98", "s99");
    }
    return false;
}

using f32x16 = __attribute__((ext_vector_type(16))) float;
constexpr int CV_RP = 80;
constexpr int CV_U = 96 * CV_RP;
constexpr int CV_KP = 4416;
constexpr int CV_K0 = 8 * CV_U;
__device__ __forceinline__ float lo16(unsigned x) { return __uint_as_float(x << 16); }
__device__ __forceinline__ float hi16(unsigned x) { return __uint_as_float(x & 0xFFFF0000u); }
__device__ __forceinline__ void prep_block(const Params& p, rsrc_t r, int bb, const int wv, const unsigned tag) {
    const int tid = otid(), w = wv, l = tid & 63;
    const int cu = (bb & 3) * 32 + w * 8 + (l >> 3), oc = (bb >> 2) * 8 + (l & 7);
    const rsrc_t rk = __builtin_amdgcn_make_buffer_rsrc((void*)p.Ak, 0, S * D * 2, 0x00020000);
    v4u sk[4], kr[11], vr[8], xg[4][2], gw[2];
#pragma unroll
    for (int j = 0; j < 11; ++j) kr[j] = __builtin_amdgcn_raw_buffer_load_b128(rk, (unsigned)((oc * 8 + j - 3) * 2048 + cu * 16), 0, TRK == 3 ? 2 : 0);
#pragma unroll
    for (int q = 0; q < 4; ++q) sk[q] = *(const v4u*)(p.shift_k + cu * 32 + q * 8);
#pragma unroll
    for (int i = 0; i < 8; ++i) vr[i] = TRK == 3 ? __builtin_nontemporal_load((const v4u*)(p.Av + (size_t)(oc * 8 + i) * D + cu * 8)) : *(const v4u*)(p.Av + (size_t)(oc * 8 + i) * D + cu * 8);
#pragma unroll
    for (int q = 0; q < 2; ++q) gw[q] = *(const v4u*)(p.gate_w + q * 512 + l * 8);
#pragma unroll
    for (int i = 0; i < 4; ++i)
#pragma unroll
        for (int q = 0; q < 2; ++q) xg[i][q] = *(const v4u*)(p.Aq + (size_t)(bb * 16 + 4 * i + w) * D + q * 512 + l * 8);
    if (OUT_WARM) {
        const rsrc_t ro = __builtin_amdgcn_make_buffer_rsrc(p.out, 0, S * D * 2, 0x00020000); const v4u z = {0u, 0u, 0u, 0u};
#pragma unroll
        for (int i = 0; i < 8; ++i) st16(ro, (unsigned)(bb * 32768 + i * 4096 + tid * 16), z);
    }
#pragma unroll
    for (int c8 = 0; c8 < 8; ++c8) {
        const unsigned s01 = sk[c8 >> 1][(c8 & 1) * 2], s23 = sk[c8 >> 1][(c8 & 1) * 2 + 1];
        const float sv[4] = {hi16(s23), lo16(s23), hi16(s01), lo16(s01)};
        float kk[11], u[8];
#pragma unroll
        for (int j = 0; j < 11; ++j) kk[j] = (c8 & 1) ? hi16(kr[j][c8 >> 1]) : lo16(kr[j][c8 >> 1]);
        f32x2 acc[4];
#pragma unroll
        for (int j = 0; j < 4; ++j)
#pragma unroll
            for (int i = 0; i < 4; ++i) { const f32x2 t = rbf2((f32x2){kk[i + j], kk[i + j + 4]} * sv[j]); acc[i] = j == 0 ? t : rbf2(acc[i] + t); }
#pragma unroll
        for (int i = 0; i < 8; ++i) u[i] = acc[i & 3][i >> 2] * ((c8 & 1) ? hi16(vr[i][c8 >> 1]) : lo16(vr[i][c8 >> 1]));
        st16(r, W_U + (cu * 8 + c8) * 4096 + oc * 16, (v4u){pack2(u[7], u[6]), pack2(u[5], u[4]), pack2(u[3], u[2]), pack2(u[1], u[0])});
    }
#pragma unroll
    for (int i = 0; i < 4; ++i) { float acc = 0.f;
#pragma unroll
        for (int q = 0; q < 2; ++q)
#pragma unroll
            for (int e = 0; e < 4; ++e) { const unsigned xa = xg[i][q][e], ga = gw[q][e]; acc += rbfh(lo16(xa) * lo16(ga)); acc += rbfh(hi16(xa) * hi16(ga)); }
        for (int o = 32; o > 0; o >>= 1) acc += __shfl_xor(acc, o);
        if (l == 0) { const float z = rbfh(acc); st4(r, W_GATE + (bb * 16 + 4 * i + w) * 4, __float_as_uint(rbfh(1.f / (1.f + expf(-z))))); } }
    st4(r, tid < 32 ? F_RDY + bb * 128 + l * 4 : (tid == 128 && bb == 0) ? F_CONV : 0xF0000000u, 0u);
    DRAIN(); __syncthreads();
    if (tid == 0) st4(r, F_PREP + bb * 4, tag);
}
template <int HK, class HOOK>
__device__ __forceinline__ bool conv_block(const Params& p, rsrc_t r, int bb, unsigned char* lds, const int wv, const int j, const unsigned tag, HOOK&& hook) {
    (void)j;
    const int tid = otid(), d0 = bb * 8, w = wv, l = tid & 63;
    v4u kraw[2][4];
#pragma unroll
    for (int ci = 0; ci < 2; ++ci)
#pragma unroll
        for (int i = 0; i < 4; ++i) kraw[ci][i] = TRK == 3 ? __builtin_nontemporal_load((const v4u*)(p.ssm_k + (size_t)(d0 + 2 * w + ci) * S + (i * 64 + l) * 8)) : *(const v4u*)(p.ssm_k + (size_t)(d0 + 2 * w + ci) * S + (i * 64 + l) * 8);
    const unsigned dd = *(const unsigned*)(p.ssm_D + d0 + 2 * w);
    { const v4u z = {0u, 0u, 0u, 0u};
#pragma unroll
      for (int i = 0; i < 5; ++i) { const int q = tid + 256 * i; *(v4u*)(lds + (q / 160) * CV_U + (q % 160) * 16) = z; } }
    if (!wait_prep(r, l, tag)) return false;
    v4u uv[8], qpre[8];
#pragma unroll
    for (int i = 0; i < 8; ++i) uv[i] = ld16(r, W_U + (d0 + i) * 4096 + tid * 16);
#pragma unroll
    for (int i = 0; i < 8; ++i) qpre[i] = *(const v4u*)(p.Aq + (size_t)(tid + 256 * i) * D + d0);
#pragma unroll
    for (int i = 0; i < 8; ++i) *(v4u*)(lds + i * CV_U + (32 + (tid >> 2)) * CV_RP + (tid & 3) * 16) = uv[i];
    unsigned char* kc = lds + CV_K0 + w * 4 * CV_KP;
    auto copies = [&](int ci) {
        if (l < 32) *(unsigned*)(kc + l * 4) = 0u;
        if (l < 48) *(unsigned*)(kc + 128 + 4096 + l * 4) = 0u;
#pragma unroll
        for (int i = 0; i < 4; ++i) *(v4u*)(kc + 128 + (i * 64 + l) * 16) = kraw[ci][i];
        __builtin_amdgcn_wave_barrier();
#pragma unroll
        for (int it = 0; it < 9; ++it) { const int i = min(l + 64 * it, CV_KP / 8 - 2);
            const v2u e01 = *(const v2u*)(kc + i * 8), e23 = *(const v2u*)(kc + i * 8 + 8);
            const unsigned s0 = (e01.x >> 16) | (e01.y << 16), s1 = (e01.y >> 16) | (e23.x << 16), s2 = (e23.x >> 16) | (e23.y << 16);
            *(v2u*)(kc + CV_KP + i * 8) = (v2u){s0, s1};
            *(v2u*)(kc + 2 * CV_KP + i * 8) = (v2u){e01.y, e23.x};
            *(v2u*)(kc + 3 * CV_KP + i * 8) = (v2u){s1, s2}; }
        __builtin_amdgcn_wave_barrier(); };
    copies(0);
    __syncthreads();
    const int b = l & 31, kg = l >> 5, cc = (b + 1) & 3;
    float ys[32];
#pragma unroll
    for (int ci = 0; ci < 2; ++ci) {
        const int c8 = 2 * w + ci;
        if (ci == 1) copies(1);
        f32x16 acc0, acc1, ad0, ad1;
#pragma unroll
        for (int i = 0; i < 16; ++i) { acc0[i] = 0.f; acc1[i] = 0.f; ad0[i] = 0.f; ad1[i] = 0.f; }
        typedef const __attribute__((address_space(3))) v2u* l2p;
        unsigned kb = (unsigned)(size_t)(kc + cc * CV_KP + (b - 8 * kg - 7 + 64 - cc) * 2) - 32, kb8 = kb + 8;
        asm volatile("" : "+v"(kb), "+v"(kb8));
        const unsigned char* ub = lds + c8 * CV_U + (32 + b) * CV_RP + kg * 16;
        v2u ka[4][2], kz[4][2]; bf16x8 fa[4], fz[4];
        auto ld_k = [&](int set, int un, int hi) { const int dl = un >> 1, kh = un & 1; const unsigned o = (unsigned)((32 * dl - 16 * kh + 16) * 2 + hi * 2048);
            if (hi) { kz[set][0] = *(l2p)(size_t)(kb + o); kz[set][1] = *(l2p)(size_t)(kb8 + o); } else { ka[set][0] = *(l2p)(size_t)(kb + o); ka[set][1] = *(l2p)(size_t)(kb8 + o); } };
        auto ld_f = [&](int set, int un) { const int dl = un >> 1, kh = un & 1;
            fa[set] = *(const bf16x8*)(ub + (32 - dl) * CV_RP + kh * 32); fz[set] = *(const bf16x8*)(ub - dl * CV_RP + kh * 32); };
#pragma unroll
        for (int q = 0; q < 2; ++q) { ld_k(q, q, 0); ld_f(q, q); ld_k(q, q, 1); }
        {
            const unsigned dv = ci ? dd >> 16 : dd & 0xFFFFu;
#pragma unroll
            for (int kh = 0; kh < 2; ++kh) { const int e = 7 - (b - 16 * kh - 8 * kg); const unsigned x = dv << ((e & 1) * 16); const int q = e >> 1;
                const v4u di = {q == 0 ? x : 0u, q == 1 ? x : 0u, q == 2 ? x : 0u, q == 3 ? x : 0u};
                asm volatile("s_nop 1\n\tv_mfma_f32_32x32x16_bf16 %0, %2, %4, %0\n\tv_mfma_f32_32x32x16_bf16 %1, %3, %4, %1" : "+v"(ad1), "+v"(ad0) : "v"(fa[kh]), "v"(fz[kh]), "v"(di)); }
        }
#pragma unroll HK ? 16 : 1
        for (int u0 = 0; u0 < 64; u0 += 4) {
#pragma unroll
            for (int q = 0; q < 4; ++q) {
                if (HK == 1 && (q & 1)) hook(ci * 32 + (u0 >> 1) + (q >> 1));
                if (HK == 2 && q == 1) hook(ci * 16 + (u0 >> 2));
                const int un = u0 + q + 2, ns = (q + 2) & 3;
                const v4u kav = {ka[q][0].x, ka[q][0].y, ka[q][1].x, ka[q][1].y}, kzv = {kz[q][0].x, kz[q][0].y, kz[q][1].x, kz[q][1].y};
                __builtin_amdgcn_sched_barrier(0);
                asm volatile("s_nop 1\n\tv_mfma_f32_32x32x16_bf16 %0, %1, %2, %0" : "+v"(acc1) : "v"(fa[q]), "v"(kav));
                ld_k(ns, un, 0);
                __builtin_amdgcn_sched_barrier(0);
                asm volatile("v_mfma_f32_32x32x16_bf16 %0, %1, %2, %0" : "+v"(acc0) : "v"(fz[q]), "v"(kav));
                ld_f(ns, un);
                __builtin_amdgcn_sched_barrier(0);
                asm volatile("v_mfma_f32_32x32x16_bf16 %0, %1, %2, %0" : "+v"(acc1) : "v"(fz[q]), "v"(kzv));
                ld_k(ns, un, 1);
            }
        }
        asm volatile("s_nop 7\n\ts_nop 7" : "+v"(acc0), "+v"(acc1), "+v"(ad0), "+v"(ad1));
#pragma unroll
        for (int i = 0; i < 32; ++i) { const float y = rbfh(i < 16 ? acc0[i] : acc1[i - 16]) + rbfh(i < 16 ? ad0[i] : ad1[i - 16]);
            ys[i] = ci == 0 ? y : __uint_as_float(pack2(ys[i], y)); }
    }
    __syncthreads();
#pragma unroll
    for (int i = 0; i < 32; ++i) { const int row = (i >> 4) * 32 + (i & 3) + 8 * ((i & 15) >> 2) + 4 * kg; *(float*)(lds + (row * 32 + b) * 16 + w * 4) = ys[i]; }
    __syncthreads();
#pragma unroll
    for (int i = 0; i < 8; ++i) { const int t = tid + 256 * i;
        const v4u qv = qpre[i], yv = *(const v4u*)(lds + t * 16); v4u o;
#pragma unroll
        for (int c2 = 0; c2 < 4; ++c2) o[c2] = pack2(lo16(qv[c2]) * lo16(yv[c2]), hi16(qv[c2]) * hi16(yv[c2]));
        st16(r, W_A + bb * 32768 + t * 16, o);
    }
    DRAIN(); __syncthreads();
    if (tid == 0) __hip_atomic_fetch_add((unsigned*)(p.ws + F_CONV), 1u, __ATOMIC_RELAXED, __HIP_MEMORY_SCOPE_AGENT);
    return true;
}

__device__ __forceinline__ void dma16(unsigned ldsdst, unsigned voff, const v4u& srd, unsigned soff) {
    asm volatile("s_mov_b32 m0, %0\n\ts_nop 0\n\tbuffer_load_dwordx4 %1, %2, %3 offen lds" :: "s"(ldsdst), "v"(voff), "s"(srd), "s"(soff) : "m0", "memory"); }
__device__ __forceinline__ void dma16n(unsigned ldsdst, unsigned voff, const v4u& srd, unsigned soff) {
    asm volatile("s_mov_b32 m0, %0\n\ts_nop 0\n\tbuffer_load_dwordx4 %1, %2, %3 offen nt lds" :: "s"(ldsdst), "v"(voff), "s"(srd), "s"(soff) : "m0", "memory"); }
__device__ __forceinline__ v4u pack_acc(const f32x4& a, const f32x4& b) {
    float x0, x1, x2, x3, y0, y1, y2, y3;
    asm volatile("v_accvgpr_read_b32 %0, %8\n\tv_accvgpr_read_b32 %1, %9\n\tv_accvgpr_read_b32 %2, %10\n\tv_accvgpr_read_b32 %3, %11\n\t"
                 "v_accvgpr_read_b32 %4, %12\n\tv_accvgpr_read_b32 %5, %13\n\tv_accvgpr_read_b32 %6, %14\n\tv_accvgpr_read_b32 %7, %15"
                 : "=v"(x0), "=v"(x1), "=v"(x2), "=v"(x3), "=v"(y0), "=v"(y1), "=v"(y2), "=v"(y3)
                 : "a"(a[0]), "a"(a[1]), "a"(a[2]), "a"(a[3]), "a"(b[0]), "a"(b[1]), "a"(b[2]), "a"(b[3]));
    return (v4u){pack2(x0, x1), pack2(x2, x3), pack2(y0, y1), pack2(y2, y3)}; }
#define SB() __builtin_amdgcn_sched_barrier(0)
struct NoHook { __device__ void operator()(int) const {} };
template <int MODE, int MI, int NJ, int NS, int HX = 0, class HOOK = NoHook, int MN = MI>
__device__ __forceinline__ void gemm(rsrc_t r, const bf16_t* __restrict__ A, const bf16_t* __restrict__ B, int m0, int n0, unsigned char* lds, unsigned coff, const int wv,
                                     const int rot, const void* tk = nullptr,
                                     const bool have = false, const bf16_t* Bn = nullptr, int m0n = 0, int n0n = 0, HOOK&& hook = NoHook()) {
    constexpr int AB = MN * 4096, BB = NJ * 2048, B0 = (NS / 2) * AB, NB = NJ / 2, NM = MI * NJ, NR = MI + NJ, BP = NJ * 64;
    static_assert(NS % 2 == 0 && 32 % NS == 0 && B0 <= 65536 && NS * BB <= 65536 && (NJ == 4 || NJ == 8), "ring");
    const int tid = otid();
    const int w = wv, l = tid & 63, n16 = l & 15, g = l >> 4;
    const int wm = w >> 1, wn = w & 1;
    const unsigned lb = __builtin_amdgcn_readfirstlane((unsigned)(size_t)lds);
    const v4u sa = mk_srd(A, 2048 * 1024 * 2); v4u sb = mk_srd(B, 1024 * 4096 * 2);
    f32x4 acc[MI][NJ];
#pragma unroll
    for (int i = 0; i < MI; ++i)
#pragma unroll
        for (int j = 0; j < NJ; ++j) acc[i][j] = (f32x4){0.f, 0.f, 0.f, 0.f};
    unsigned va = (unsigned)((m0 + 8 * MI * w + (l >> 3)) * 2048 + (((l & 7) ^ (l >> 3)) << 4));
    unsigned vb[2];
    if (NJ == 8) { vb[0] = (unsigned)((8 * w + (l >> 5)) * 8192 + n0 * 2) + (unsigned)(((l & 31) << 4) ^ ((l >> 5) << 5) ^ (wn << 7)); vb[1] = vb[0] ^ 64u; }
    else { vb[0] = (unsigned)((8 * w + g) * 8192 + n0 * 2) + (unsigned)((n16 << 4) ^ (g << 5) ^ (wn << 7)); vb[1] = vb[0]; }
    auto rk = [&](int kt) { return (kt & ~31) | ((kt + rot) & 31); };
    unsigned la = lb + w * MI * 1024;
    auto st_a = [&](int buf, int kt, int q) { dma16(la + buf * AB + q * 1024, va, sa, (unsigned)(rk(kt) * 64 + q * 16384)); };
    auto st_b = [&](int buf, int kt, int x) { if ((B_NT >> (NJ == 4 ? 0 : MI == 4 ? 1 : 2)) & 1) dma16n(lb + B0 + buf * BB + (w * NB + x) * 1024, vb[x & 1], sb, (unsigned)((rk(kt) * 32 + x * (8 / NB)) * 8192));
        else dma16(lb + B0 + buf * BB + (w * NB + x) * 1024, vb[x & 1], sb, (unsigned)((rk(kt) * 32 + x * (8 / NB)) * 8192)); };
    const unsigned f = (unsigned)((n16 >> 2) | ((g & 1) << 2));
    constexpr int NQ = MODE == 1 ? NJ : NJ / 2;
    unsigned ra[2], rb[NQ];
    ra[0] = lb + (16 * MI * wm + n16) * 128 + ((g ^ (n16 & 7)) << 4); ra[1] = ra[0] ^ 64u;
    {   const unsigned row = lb + B0 + (8 * g + (n16 >> 2)) * BP + (NJ == 8 ? 256 * wn : 0), g0 = NJ == 8 ? 0u : 4u * wn;
#pragma unroll
        for (int j = 0; j < NQ; ++j) {
            if (MODE == 1) rb[j] = row + (((g0 + j) ^ f) << 5) + 8 * (n16 & 3);
            else rb[j] = row + (((g0 + 2 * j + ((n16 & 3) >> 1)) ^ f) << 5) + (n16 & 1) * 16;
            asm volatile("" : "+v"(rb[j])); } }
    bf16x8 af[2][MI], bfr[2][NJ];
    auto rd = [&](int set, int buf, int x) {
        if (x < NJ) { const unsigned a = rb[MODE == 1 ? x : (x >> 1)] + (MODE == 1 ? 0 : (x & 1) * 8) + buf * BB;
            const v2u lo = trb(a), hi = trb(a + 4 * BP); v4u v = {lo.x, lo.y, hi.x, hi.y}; bfr[set][x] = as_frag(v); }
        else af[set][x - NJ] = ldsa(ra[buf & 1] + (buf >> 1) * AB + (x - NJ) * 2048); };
    auto mf = [&](int set, int i, int j) {
        if (MODE == 1) asm volatile("v_mfma_f32_16x16x32_bf16 %0, %1, %2, %0" : "+a"(acc[i][j]) : "v"(af[set][i]), "v"(bfr[set][j]));
        else asm volatile("v_mfma_f32_16x16x32_bf16 %0, %1, %2, %0" : "+a"(acc[i][j]) : "v"(bfr[set][j]), "v"(af[set][i])); };
    if (!have) {
#pragma unroll
        for (int t = 0; t < NS; ++t) {
            if (t & 1) {
#pragma unroll
                for (int q = 0; q < MI; ++q) st_a(t >> 1, t - 1, q); }
#pragma unroll
            for (int q = 0; q < NB; ++q) st_b(t, t, q);
            if (HX) hook(t); }
    }
#pragma unroll
    for (int i = 0; i < MI; ++i)
#pragma unroll
        for (int j = 0; j < NJ; ++j) asm volatile("" : "+a"(acc[i][j]));
    asm volatile("s_nop 1" ::: "memory");
    if (!have) {
        asm volatile("s_waitcnt vmcnt(%c0)\n\ts_barrier" :: "i"((NS - 1) * (NB + HX) + (NS / 2 - 1) * MI) : "memory");
    }
#pragma unroll
    for (int x = 0; x < NR; ++x) rd(0, 0, x);
    constexpr int HEAD = NM / 8;
    const v4u st = mk_srd(tk, tk ? 32768 : 0); unsigned tdum = 0u;
#pragma unroll HX ? 32 / NS : 1
    for (int k0 = 0; k0 < 32; k0 += NS) {
        int kb = k0 + NS;
        if (kb == 32) {
            kb = Bn ? 0 : 0x10100;
            if (Bn) { va += (unsigned)((m0n - m0 + 8 * (MN - MI) * w) * 2048); la = lb + w * MN * 1024; vb[0] += (unsigned)((n0n - n0) * 2); vb[1] += (unsigned)((n0n - n0) * 2); sb = mk_srd(Bn, 1024 * 4096 * 2); } }
        if (NS == 4) asm volatile("buffer_load_dword %0, %1, %2, %3 offen" : "+v"(tdum) : "v"(l < 32 ? (unsigned)l * 128u : 0xF0000000u), "s"(st), "s"((unsigned)k0 * 1024u) : "memory");
#pragma unroll
        for (int u = 0; u < NS; ++u) {
            SB();
#pragma unroll
            for (int q = 0; q < HEAD; ++q) mf(u & 1, q / NJ, q % NJ);
            SB();
            asm volatile("s_waitcnt vmcnt(%c0) lgkmcnt(0)\n\ts_barrier" :: "i"((NS - 2) * (NB + HX) + (NS / 2 - 1 - (u & 1)) * MI) : "memory");
            SB();
            constexpr int span = NM - HEAD;
#pragma unroll
            for (int q = HEAD; q < NM; ++q) {
                mf(u & 1, q / NJ, q % NJ);
                const int x = q - HEAD, nd = NB + ((u & 1) ? MI : 0);
#pragma unroll
                for (int y = 0; y < NR; ++y) if ((y * (span - HEAD)) / NR == x) rd((u & 1) ^ 1, (u + 1) % NS, y);
#pragma unroll
                for (int y = 0; y < nd; ++y) if ((y * span + span / 2) / nd == x) { if ((u & 1) && y < MI) st_a(u >> 1, kb + u - 1, y); else st_b(u, kb + u, y - ((u & 1) ? MI : 0)); }
                SB(); }
            if (MN > MI && (u & 1) && k0 == 32 - NS) {
#pragma unroll
                for (int y = MI; y < MN; ++y) st_a(u >> 1, kb + u - 1, y); }
            if (HX) hook(NS + k0 + u);
        }
    }
    asm volatile("s_nop 7\n\ts_nop 3" : "+v"(tdum) :: "memory");
    SB();
    const int col = n0 + 16 * NJ * wn, head = col >> 7, sub = (col & 127) >> 4, seg0 = (m0 >> 5) + (MI / 2) * wm;
#pragma unroll
    for (int sg = 0; sg < MI / 2; ++sg) {
        const unsigned base = coff + (unsigned)(((seg0 + sg) * 32 + head) * 8192 + l * 16);
#pragma unroll
        for (int q = 0; q < NJ; ++q) {
            if (MODE == 0) { const int tt = q / (NJ / 2), kk = q % (NJ / 2); st16(r, base + (tt * 4 + (sub >> 1) + kk) * 1024, pack_acc(acc[2 * sg + tt][2 * kk], acc[2 * sg + tt][2 * kk + 1])); }
            else st16(r, base + (sub + q) * 1024, pack_acc(acc[2 * sg][q], acc[2 * sg + 1][q])); }
    }
}

__device__ __forceinline__ unsigned min4(unsigned v) { return min(min(__builtin_amdgcn_readlane(v, 0), __builtin_amdgcn_readlane(v, 1)), min(__builtin_amdgcn_readlane(v, 2), __builtin_amdgcn_readlane(v, 3))); }
__device__ __forceinline__ void attn_block(const Params& p, rsrc_t r, unsigned char* lds, const int wv, const int h, const int j, const unsigned tag) {
    (void)j;
    const int l = otid() & 63, n16 = l & 15, g = l >> 4;
    { v4u q[4]; if (!wait_cal(r, l, tag, q)) return; }
    v4u qv[2][4], kv[2][4], va[8];
    int nev = 0; (void)nev;
    auto fetch = [&](int seg) -> bool {
        for (unsigned spin = 0;; ++spin) { MEMBAR();
            const unsigned fv = ld4(r, F_KVQ + ((seg >> 2) * 32 + h) * 64 + min(l, 2) * 4);
            if (__all(fv == tag)) break;
            if (spin > (1u << 20)) { st4(r, F_ERR, 64u); return false; }
            __builtin_amdgcn_s_sleep(16); }
        MEMBAR();
        const unsigned o = (unsigned)((seg * 32 + h) * 8192 + l * 16);
#pragma unroll
        for (int i = 0; i < 8; ++i) qv[i >> 2][i & 3] = ld16(r, W_QX + o + i * 1024);
#pragma unroll
        for (int i = 0; i < 8; ++i) kv[i >> 2][i & 3] = ld16(r, W_KX + o + i * 1024);
#pragma unroll
        for (int i = 0; i < 8; ++i) va[i] = ld16(r, W_VF + o + i * 1024);
        return true; };
    if (!fetch(wv)) return;
    unsigned prog = 0u, tseen = rtnow() - 100000u;
#pragma unroll 1
    for (int seg = wv; seg < NSEG; seg += 4) {
        for (unsigned spin = 0; prog <= (unsigned)seg; ++spin) {
            const unsigned now = rtnow();
            if (spin > (1u << 22)) { st4(r, F_ERR, 32u); return; }
            if (prog + (seg >= EARLY_FROM ? 2u : 1u) + (now - tseen) / STEP_MIN <= (unsigned)seg) { __builtin_amdgcn_s_sleep(16); continue; }
            MEMBAR();
            const unsigned pg = min4(ld4(r, F_PROG + h * 128 + min(l, 2) * 8));
            if (pg != prog) { prog = pg; tseen = now; }
            if (prog <= (unsigned)seg) __builtin_amdgcn_s_sleep(P_SLP);
        }
        MEMBAR();
        const unsigned km = HO_KVM + (seg * 32 + h) * 4096;
        v4u kmv[4]; v2u pm[8];
        const v4u k0 = ld16(r, km + l * 16), k1 = ld16(r, km + 1024 + l * 16);
#pragma unroll
        for (int t = 0; t < 8; ++t) pm[t] = ld8(r, km + 2048 + t * 256 + n16 * 16 + (g & 1) * 8);
        *(v4u*)(lds + wv * 2048 + l * 16) = k0; *(v4u*)(lds + wv * 2048 + 1024 + l * 16) = k1;
        __builtin_amdgcn_wave_barrier();
#pragma unroll
        for (int ks = 0; ks < 4; ++ks) { const unsigned a = (unsigned)(size_t)lds + wv * 2048 + (32 * ks + 8 * g + (n16 >> 2)) * 16 + (n16 & 1) * 8; const v2u x0 = trb(a), x1 = trb(a + 64); kmv[ks] = (v4u){x0.x, x0.y, x1.x, x1.y}; }
#pragma unroll
        for (int qt = 0; qt < 2; ++qt) {
            f32x4 sc[3];
            asm volatile("s_nop 1\n\t"
                "v_mfma_f32_16x16x32_bf16 %0, %3, %15, 0\n\tv_mfma_f32_16x16x32_bf16 %1, %7, %15, 0\n\tv_mfma_f32_16x16x32_bf16 %2, %11, %15, 0\n\t"
                "v_mfma_f32_16x16x32_bf16 %0, %4, %16, %0\n\tv_mfma_f32_16x16x32_bf16 %1, %8, %16, %1\n\tv_mfma_f32_16x16x32_bf16 %2, %12, %16, %2\n\t"
                "v_mfma_f32_16x16x32_bf16 %0, %5, %17, %0\n\tv_mfma_f32_16x16x32_bf16 %1, %9, %17, %1\n\tv_mfma_f32_16x16x32_bf16 %2, %13, %17, %2\n\t"
                "v_mfma_f32_16x16x32_bf16 %0, %6, %18, %0\n\tv_mfma_f32_16x16x32_bf16 %1, %10, %18, %1\n\tv_mfma_f32_16x16x32_bf16 %2, %14, %18, %2\n\t"
                "s_nop 7"
                : "=&v"(sc[0]), "=&v"(sc[1]), "=&v"(sc[2])
                : "v"(kv[0][0]), "v"(kv[0][1]), "v"(kv[0][2]), "v"(kv[0][3]), "v"(kv[1][0]), "v"(kv[1][1]), "v"(kv[1][2]), "v"(kv[1][3]),
                  "v"(kmv[0]), "v"(kmv[1]), "v"(kmv[2]), "v"(kmv[3]), "v"(qv[qt][0]), "v"(qv[qt][1]), "v"(qv[qt][2]), "v"(qv[qt][3]));
            const int qi = 16 * qt + n16; float mx = -INFINITY;
#pragma unroll
            for (int t = 0; t < 3; ++t)
#pragma unroll
                for (int rr = 0; rr < 4; ++rr) { float v = rbfh(rbfh(sc[t][rr]) * 0.08838834764831845f);
                    if (t < 2) v = (16 * t + 4 * g + rr <= qi) ? v : -1000000000.0f; else if (g >= 2) v = -INFINITY;
                    sc[t][rr] = v; mx = fmaxf(mx, v); }
            mx = xor16_max(mx); mx = xor32_max(mx); float sum = 0.f;
#pragma unroll
            for (int t = 0; t < 3; ++t)
#pragma unroll
                for (int rr = 0; rr < 4; ++rr) { const float e = __expf(sc[t][rr] - mx); sc[t][rr] = e; sum += e; }
            sum = xor16_add(sum); sum = xor32_add(sum); const float inv = 1.0f / sum;
            const v4u p0 = {pack2(sc[0][0] * inv, sc[0][1] * inv), pack2(sc[0][2] * inv, sc[0][3] * inv), pack2(sc[1][0] * inv, sc[1][1] * inv), pack2(sc[1][2] * inv, sc[1][3] * inv)};
            const v2u p1 = {pack2(sc[2][0] * inv, sc[2][1] * inv), pack2(sc[2][2] * inv, sc[2][3] * inv)};
            f32x4 oa[8];
            asm volatile("s_nop 1\n\t"
                "v_mfma_f32_16x16x32_bf16 %0, %8, %24, 0\n\tv_mfma_f32_16x16x32_bf16 %1, %9, %24, 0\n\tv_mfma_f32_16x16x32_bf16 %2, %10, %24, 0\n\tv_mfma_f32_16x16x32_bf16 %3, %11, %24, 0\n\t"
                "v_mfma_f32_16x16x32_bf16 %4, %12, %24, 0\n\tv_mfma_f32_16x16x32_bf16 %5, %13, %24, 0\n\tv_mfma_f32_16x16x32_bf16 %6, %14, %24, 0\n\tv_mfma_f32_16x16x32_bf16 %7, %15, %24, 0\n\t"
                "v_mfma_f32_16x16x16_bf16 %0, %16, %25, %0\n\tv_mfma_f32_16x16x16_bf16 %1, %17, %25, %1\n\tv_mfma_f32_16x16x16_bf16 %2, %18, %25, %2\n\tv_mfma_f32_16x16x16_bf16 %3, %19, %25, %3\n\t"
                "v_mfma_f32_16x16x16_bf16 %4, %20, %25, %4\n\tv_mfma_f32_16x16x16_bf16 %5, %21, %25, %5\n\tv_mfma_f32_16x16x16_bf16 %6, %22, %25, %6\n\tv_mfma_f32_16x16x16_bf16 %7, %23, %25, %7\n\t"
                "s_nop 7"
                : "=&v"(oa[0]), "=&v"(oa[1]), "=&v"(oa[2]), "=&v"(oa[3]), "=&v"(oa[4]), "=&v"(oa[5]), "=&v"(oa[6]), "=&v"(oa[7])
                : "v"(va[0]), "v"(va[1]), "v"(va[2]), "v"(va[3]), "v"(va[4]), "v"(va[5]), "v"(va[6]), "v"(va[7]),
                  "v"(pm[0]), "v"(pm[1]), "v"(pm[2]), "v"(pm[3]), "v"(pm[4]), "v"(pm[5]), "v"(pm[6]), "v"(pm[7]), "v"(p0), "v"(p1));
#pragma unroll
            for (int u = 0; u < 4; ++u) {
                const f32x4 o0 = oa[2 * u], o1 = oa[2 * u + 1];
                const v4u pk = {pack2(o0[0], o0[1]), pack2(o0[2], o0[3]), pack2(o1[0], o1[1]), pack2(o1[2], o1[3])};
                st16(r, W_OX + (((seg * 2 + qt) * 32 + h) * 4 + u) * 1024 + l * 16, pk);
            }
        }
        DRAIN();
        { const int tt = 2 * seg + (l >> 5);
            __hip_atomic_fetch_add((unsigned*)(p.ws + F_RDY + (((tt & 3) * 32 + (l & 31)) * 32 + (tt >> 2)) * 4), 1u, __ATOMIC_RELAXED, __HIP_MEMORY_SCOPE_AGENT); }
        if (seg + 4 < NSEG && !fetch(seg + 4)) return;
    }
}

constexpr int OP_P = 80;
__device__ __forceinline__ void oproj_block(const Params& p, rsrc_t r, unsigned char* lds, const int wv, const int nt, const int rho, const int j, v4u (&wraw)[64], const unsigned tag
    ) {
    (void)j;
    const int w = wv, l = otid() & 63, n16 = l & 15, g = l >> 4;
    const unsigned ldsbase = (unsigned)(size_t)lds;
    bf16x8 wof[32][2];
#pragma unroll
    for (int ks = 0; ks < 32; ++ks) {
        if (2 * ks == WO_IN) DRAIN();
        const unsigned st = ldsbase + w * 8192 + (ks & 1) * 4096;
        const unsigned sa = st + (l >> 2) * OP_P + (l & 3) * 16;
        switch (ks) {
            case 0: asm volatile("ds_write_b128 %0, %1\n\tds_write_b128 %0, %2 offset:%c3" :: "v"(sa), "{a[128:131]}"(wraw[0]), "{a[132:135]}"(wraw[1]), "i"(16 * OP_P) : "memory"); break;
            case 1: asm volatile("ds_write_b128 %0, %1\n\tds_write_b128 %0, %2 offset:%c3" :: "v"(sa), "{a[136:139]}"(wraw[2]), "{a[140:143]}"(wraw[3]), "i"(16 * OP_P) : "memory"); break;
            case 2: asm volatile("ds_write_b128 %0, %1\n\tds_write_b128 %0, %2 offset:%c3" :: "v"(sa), "{a[144:147]}"(wraw[4]), "{a[148:151]}"(wraw[5]), "i"(16 * OP_P) : "memory"); break;
            case 3: asm volatile("ds_write_b128 %0, %1\n\tds_write_b128 %0, %2 offset:%c3" :: "v"(sa), "{a[152:155]}"(wraw[6]), "{a[156:159]}"(wraw[7]), "i"(16 * OP_P) : "memory"); break;
            case 4: asm volatile("ds_write_b128 %0, %1\n\tds_write_b128 %0, %2 offset:%c3" :: "v"(sa), "{a[160:163]}"(wraw[8]), "{a[164:167]}"(wraw[9]), "i"(16 * OP_P) : "memory"); break;
            case 5: asm volatile("ds_write_b128 %0, %1\n\tds_write_b128 %0, %2 offset:%c3" :: "v"(sa), "{a[168:171]}"(wraw[10]), "{a[172:175]}"(wraw[11]), "i"(16 * OP_P) : "memory"); break;
            case 6: asm volatile("ds_write_b128 %0, %1\n\tds_write_b128 %0, %2 offset:%c3" :: "v"(sa), "{a[176:179]}"(wraw[12]), "{a[180:183]}"(wraw[13]), "i"(16 * OP_P) : "memory"); break;
            case 7: asm volatile("ds_write_b128 %0, %1\n\tds_write_b128 %0, %2 offset:%c3" :: "v"(sa), "{a[184:187]}"(wraw[14]), "{a[188:191]}"(wraw[15]), "i"(16 * OP_P) : "memory"); break;
            case 8: asm volatile("ds_write_b128 %0, %1\n\tds_write_b128 %0, %2 offset:%c3" :: "v"(sa), "{a[192:195]}"(wraw[16]), "{a[196:199]}"(wraw[17]), "i"(16 * OP_P) : "memory"); break;
            case 9: asm volatile("ds_write_b128 %0, %1\n\tds_write_b128 %0, %2 offset:%c3" :: "v"(sa), "{a[200:203]}"(wraw[18]), "{a[204:207]}"(wraw[19]), "i"(16 * OP_P) : "memory"); break;
            case 10: asm volatile("ds_write_b128 %0, %1\n\tds_write_b128 %0, %2 offset:%c3" :: "v"(sa), "{a[208:211]}"(wraw[20]), "{a[212:215]}"(wraw[21]), "i"(16 * OP_P) : "memory"); break;
            case 11: asm volatile("ds_write_b128 %0, %1\n\tds_write_b128 %0, %2 offset:%c3" :: "v"(sa), "{a[216:219]}"(wraw[22]), "{a[220:223]}"(wraw[23]), "i"(16 * OP_P) : "memory"); break;
            case 12: asm volatile("ds_write_b128 %0, %1\n\tds_write_b128 %0, %2 offset:%c3" :: "v"(sa), "{a[224:227]}"(wraw[24]), "{a[228:231]}"(wraw[25]), "i"(16 * OP_P) : "memory"); break;
            case 13: asm volatile("ds_write_b128 %0, %1\n\tds_write_b128 %0, %2 offset:%c3" :: "v"(sa), "{a[232:235]}"(wraw[26]), "{a[236:239]}"(wraw[27]), "i"(16 * OP_P) : "memory"); break;
            case 14: asm volatile("ds_write_b128 %0, %1\n\tds_write_b128 %0, %2 offset:%c3" :: "v"(sa), "{a[240:243]}"(wraw[28]), "{a[244:247]}"(wraw[29]), "i"(16 * OP_P) : "memory"); break;
            case 15: asm volatile("ds_write_b128 %0, %1\n\tds_write_b128 %0, %2 offset:%c3" :: "v"(sa), "{a[248:251]}"(wraw[30]), "{a[252:255]}"(wraw[31]), "i"(16 * OP_P) : "memory"); break;
            case 16: asm volatile("ds_write_b128 %0, %1\n\tds_write_b128 %0, %2 offset:%c3" :: "v"(sa), "{v[176:179]}"(wraw[32]), "{v[180:183]}"(wraw[33]), "i"(16 * OP_P) : "memory"); break;
            case 17: asm volatile("ds_write_b128 %0, %1\n\tds_write_b128 %0, %2 offset:%c3" :: "v"(sa), "{v[184:187]}"(wraw[34]), "{v[188:191]}"(wraw[35]), "i"(16 * OP_P) : "memory"); break;
            case 18: asm volatile("ds_write_b128 %0, %1\n\tds_write_b128 %0, %2 offset:%c3" :: "v"(sa), "{v[192:195]}"(wraw[36]), "{v[196:199]}"(wraw[37]), "i"(16 * OP_P) : "memory"); break;
            case 19: asm volatile("ds_write_b128 %0, %1\n\tds_write_b128 %0, %2 offset:%c3" :: "v"(sa), "{v[200:203]}"(wraw[38]), "{v[204:207]}"(wraw[39]), "i"(16 * OP_P) : "memory"); break;
            case 20: asm volatile("ds_write_b128 %0, %1\n\tds_write_b128 %0, %2 offset:%c3" :: "v"(sa), "{v[208:211]}"(wraw[40]), "{v[212:215]}"(wraw[41]), "i"(16 * OP_P) : "memory"); break;
            case 21: asm volatile("ds_write_b128 %0, %1\n\tds_write_b128 %0, %2 offset:%c3" :: "v"(sa), "{v[216:219]}"(wraw[42]), "{v[220:223]}"(wraw[43]), "i"(16 * OP_P) : "memory"); break;
            case 22: asm volatile("ds_write_b128 %0, %1\n\tds_write_b128 %0, %2 offset:%c3" :: "v"(sa), "{v[224:227]}"(wraw[44]), "{v[228:231]}"(wraw[45]), "i"(16 * OP_P) : "memory"); break;
            case 23: asm volatile("ds_write_b128 %0, %1\n\tds_write_b128 %0, %2 offset:%c3" :: "v"(sa), "{v[232:235]}"(wraw[46]), "{v[236:239]}"(wraw[47]), "i"(16 * OP_P) : "memory"); break;
            case 24: asm volatile("ds_write_b128 %0, %1\n\tds_write_b128 %0, %2 offset:%c3" :: "v"(sa), "{v[240:243]}"(wraw[48]), "{v[244:247]}"(wraw[49]), "i"(16 * OP_P) : "memory"); break;
            case 25: asm volatile("ds_write_b128 %0, %1\n\tds_write_b128 %0, %2 offset:%c3" :: "v"(sa), "{v[248:251]}"(wraw[50]), "{v[252:255]}"(wraw[51]), "i"(16 * OP_P) : "memory"); break;
            case 26: asm volatile("ds_write_b128 %0, %1\n\tds_write_b128 %0, %2 offset:%c3" :: "v"(sa), "{a[80:83]}"(wraw[52]), "{a[84:87]}"(wraw[53]), "i"(16 * OP_P) : "memory"); break;
            case 27: asm volatile("ds_write_b128 %0, %1\n\tds_write_b128 %0, %2 offset:%c3" :: "v"(sa), "{a[88:91]}"(wraw[54]), "{a[92:95]}"(wraw[55]), "i"(16 * OP_P) : "memory"); break;
            case 28: asm volatile("ds_write_b128 %0, %1\n\tds_write_b128 %0, %2 offset:%c3" :: "v"(sa), "{a[96:99]}"(wraw[56]), "{a[100:103]}"(wraw[57]), "i"(16 * OP_P) : "memory"); break;
            case 29: asm volatile("ds_write_b128 %0, %1\n\tds_write_b128 %0, %2 offset:%c3" :: "v"(sa), "{a[104:107]}"(wraw[58]), "{a[108:111]}"(wraw[59]), "i"(16 * OP_P) : "memory"); break;
            case 30: asm volatile("ds_write_b128 %0, %1\n\tds_write_b128 %0, %2 offset:%c3" :: "v"(sa), "{a[112:115]}"(wraw[60]), "{a[116:119]}"(wraw[61]), "i"(16 * OP_P) : "memory"); break;
            case 31: asm volatile("ds_write_b128 %0, %1\n\tds_write_b128 %0, %2 offset:%c3" :: "v"(sa), "{a[120:123]}"(wraw[62]), "{a[124:127]}"(wraw[63]), "i"(16 * OP_P) : "memory"); break;
        }
#pragma unroll
        for (int jn = 0; jn < 2; ++jn) {
            const unsigned a0 = st + (4 * g + (n16 >> 2)) * OP_P + (16 * jn + 4 * (n16 & 3)) * 2;
            const v2u lo = trb(a0), hi = trb(a0 + 16 * OP_P);
            v4u v = {lo.x, lo.y, hi.x, hi.y}; asm volatile("" : "=a"(wof[ks][jn]) : "0"(as_frag(v))); }
    }
    __syncthreads();
    const v4u rd = mk_srd(p.ws, W_TOTAL), ro = mk_srd(p.out, S * D * 2);
    const unsigned OOB = 0xF0000000u;
    const int NT = 32, jn = w & 1;
    auto tile = [&](int i) -> unsigned { return (unsigned)(4 * i + rho); };
    const unsigned r_off = F_RDY + (rho * 32 + nt) * 128 + (l & 31) * 4;
    const unsigned a_b = W_OX + 8 * w * 4096 + l * 16;
    const int n0 = nt * 32 + 16 * jn + 4 * g;
    const unsigned c_b = W_A + (n0 >> 3) * 32768 + n16 * 16 + (g & 1) * 8, g_b = W_GATE + n16 * 4, o_b = (unsigned)((n16 * 1024 + n0) * 2);
    auto mix = [&](const v2u& av, unsigned gv, const f32x4& bs) -> v2u {
        const float gg = __uint_as_float(gv), og = rbfh(1.f - gg); float e[4];
#pragma unroll
        for (int k = 0; k < 4; ++k) { const unsigned aw = k < 2 ? av.x : av.y; e[k] = rbfh(gg * ((k & 1) ? hi16(aw) : lo16(aw))) + rbfh(og * rbfh(bs[k])); }
        return (v2u){pack2(e[0], e[1]), pack2(e[2], e[3])}; };
    const unsigned s_w = ldsbase + w * 2048 + l * 16, s_r = ldsbase + jn * 1024 + l * 16;
    unsigned long long known = 0ull; int nev = 0; (void)nev;
    bool have = false, pend = false; v4u af[32]; v2u pa = {0u, 0u}; unsigned pg = 0u, cf2 = 0u;
    auto finish = [&](int i, bool on) {
        v2u o = {0u, 0u};
        if (on) {
            const __attribute__((address_space(3))) f32x4* q = (const __attribute__((address_space(3))) f32x4*)(size_t)(s_r + (unsigned)(i & 1) * 8192u);
            const f32x4 bs = (q[0] + q[128]) + (q[256] + q[384]);
            o = mix(pa, pg, bs);
        }
        asm volatile("buffer_store_dwordx2 %0, %1, %2, 0 offen" :: "v"(o), "v"(on ? o_b + tile(i) * 32768u : OOB), "s"(ro) : "memory"); };
    if (!wait_prep(r, l, tag) || !wait_word(r, F_CONV, 128u, 256u)) ENDPGM();
#pragma unroll 1
    for (int i = 0; i < NT; ++i) {
        if (!have) {
            for (unsigned spin = 0; !((known >> i) & 1ull); ++spin) { MEMBAR();
                known = __ballot(ld4(r, r_off) == 32u);
                if ((known >> i) & 1ull) break;
                if (spin > (1u << 21)) { st4(r, F_ERR, 128u); ENDPGM(); }
                __builtin_amdgcn_s_sleep(O_SLP); }
            const unsigned a0 = a_b + tile(i) * 131072u;
#pragma unroll
            for (int q = 0; q < 8; ++q)
                asm volatile("buffer_load_dwordx4 %0, %4, %5, %6 offen sc1\n\tbuffer_load_dwordx4 %1, %4, %5, %6 offen offset:1024 sc1\n\t"
                             "buffer_load_dwordx4 %2, %4, %5, %6 offen offset:2048 sc1\n\tbuffer_load_dwordx4 %3, %4, %5, %6 offen offset:3072 sc1"
                             : "=&v"(af[4 * q]), "=&v"(af[4 * q + 1]), "=&v"(af[4 * q + 2]), "=&v"(af[4 * q + 3]) : "v"(a0), "s"(rd), "s"(q * 4096) : "memory");
            asm volatile("buffer_store_dword %0, %0, %1, 0 offen\n\tbuffer_store_dword %0, %0, %1, 0 offen" :: "v"(OOB), "s"(ro) : "memory");
        }
        const bool fin = (w >> 1) == (i & 1);
        const bool more = i + 1 < NT && ((known >> (i + 1)) & 1ull);
        const unsigned an = more ? a_b + tile(i + 1) * 131072u : OOB;
        unsigned cfl, fg; v2u fa;
        asm volatile("buffer_load_dword %0, %3, %6, 0 offen sc1\n\tbuffer_load_dwordx2 %1, %4, %6, 0 offen sc1\n\tbuffer_load_dword %2, %5, %6, 0 offen sc1"
                     : "=&v"(cfl), "=&v"(fa), "=&v"(fg) : "v"(((known >> (i + 2)) & 1ull) ? OOB : r_off), "v"(fin ? c_b + tile(i) * 256u : OOB), "v"(fin ? g_b + tile(i) * 64u : OOB), "s"(rd) : "memory");
        f32x4 acc0 = {0.f, 0.f, 0.f, 0.f}, acc1 = {0.f, 0.f, 0.f, 0.f};
        asm volatile("s_nop 1" : "+v"(acc0), "+v"(acc1));
#pragma unroll
        for (int q = 0; q < 8; ++q) {
            if (q == 2) finish(i - 1, pend);
            if (q == 6) asm volatile("buffer_load_dword %0, %1, %2, 0 offen sc1" : "+v"(cf2) : "v"(more ? OOB : r_off), "s"(rd) : "memory");
            asm volatile(
                "s_waitcnt vmcnt(36)\n\tv_mfma_f32_16x16x32_bf16 %4, %6, %0, %4\n\tv_mfma_f32_16x16x32_bf16 %5, %7, %0, %5\n\tbuffer_load_dwordx4 %0, %14, %15, %16 offen sc1\n\t"
                "s_waitcnt vmcnt(36)\n\tv_mfma_f32_16x16x32_bf16 %4, %8, %1, %4\n\tv_mfma_f32_16x16x32_bf16 %5, %9, %1, %5\n\tbuffer_load_dwordx4 %1, %14, %15, %16 offen offset:1024 sc1\n\t"
                "s_waitcnt vmcnt(36)\n\tv_mfma_f32_16x16x32_bf16 %4, %10, %2, %4\n\tv_mfma_f32_16x16x32_bf16 %5, %11, %2, %5\n\tbuffer_load_dwordx4 %2, %14, %15, %16 offen offset:2048 sc1\n\t"
                "s_waitcnt vmcnt(36)\n\tv_mfma_f32_16x16x32_bf16 %4, %12, %3, %4\n\tv_mfma_f32_16x16x32_bf16 %5, %13, %3, %5\n\tbuffer_load_dwordx4 %3, %14, %15, %16 offen offset:3072 sc1"
                : "+v"(af[4 * q]), "+v"(af[4 * q + 1]), "+v"(af[4 * q + 2]), "+v"(af[4 * q + 3]), "+v"(acc0), "+v"(acc1)
                : "a"(wof[4 * q][0]), "a"(wof[4 * q][1]), "a"(wof[4 * q + 1][0]), "a"(wof[4 * q + 1][1]), "a"(wof[4 * q + 2][0]), "a"(wof[4 * q + 2][1]), "a"(wof[4 * q + 3][0]), "a"(wof[4 * q + 3][1]),
                  "v"(an), "s"(rd), "s"(q * 4096) : "memory"); }
        const unsigned po = (unsigned)(i & 1) * 8192u;
        asm volatile("s_nop 7\n\tds_write_b128 %5, %3\n\tds_write_b128 %5, %4 offset:1024\n\ts_waitcnt vmcnt(34) lgkmcnt(0)\n\ts_barrier"
                     : "+v"(cfl), "+v"(fa), "+v"(fg) : "v"(acc0), "v"(acc1), "v"(s_w + po) : "memory");
        known |= __ballot(cfl == 32u);
        pa = fa; pg = fg; pend = fin && more;
        if (!more) finish(i, fin);
        have = more;
        if (!more) {
            asm volatile("s_waitcnt vmcnt(1)" : "+v"(cf2), "+v"(af[0]), "+v"(af[1]), "+v"(af[2]), "+v"(af[3]), "+v"(af[4]), "+v"(af[5]), "+v"(af[6]), "+v"(af[7]), "+v"(af[8]), "+v"(af[9]), "+v"(af[10]), "+v"(af[11]),
                         "+v"(af[12]), "+v"(af[13]), "+v"(af[14]), "+v"(af[15]) :: "memory");
            known |= __ballot(cf2 == 32u);
        }
    }
    if (nt == 0 && rho == 0 && w == 0) st4(r, l == 0 ? F_EP : OOB, tag);
}

__device__ __forceinline__ void bulk_block(const Params& p, rsrc_t r, unsigned char* lds, const int wv, const int j, const unsigned tag
    ) {
    const int x = (j & 7) >> 1, i32 = (j >> 3) * 2 + (j & 1);
    auto flags = [&](int mt, int nmt, int h0, int nh, int mat) {
        DRAIN(); __syncthreads();
        const int tid = otid(); if (tid < nmt * nh) st4(r, F_KVQ + ((mt + tid / nh) * 32 + h0 + tid % nh) * 64 + mat * 4, tag); };
#pragma unroll 1
    for (int t = 0; t < 2; ++t) {
        const int mt = 2 * t + (i32 & 1), mat = (i32 >> 1) & 1, hd = 8 * x + (i32 >> 2), rot = ((i32 >> 1) & 7) * 2;
        const bf16_t* Bm = mat ? p.Wv : p.Wk;
        if (mat) gemm<1, 4, 4, 8>(r, p.Aq, Bm, mt * 128, hd * 128, lds, W_VF, wv, rot, nullptr, t == 1, t == 0 ? Bm : nullptr, (mt + 2) * 128, hd * 128);
        else gemm<0, 4, 4, 8>(r, p.Aq, Bm, mt * 128, hd * 128, lds, W_KX, wv, rot, nullptr, t == 1, t == 0 ? Bm : nullptr, (mt + 2) * 128, hd * 128);
        flags(mt, 1, hd, 1, mat);
    }
    const int hp = 4 * x + (i32 >> 3);
    auto tile = [&](int t, int& m, int& mat) { const int q = i32 & 7;
        if (t == 0) { m = 2 + (q & 3); mat = q >> 2; } else if (q < 4) { m = 6 + (q & 1); mat = q >> 1; } else { m = q - 4; mat = 2; } };
#pragma unroll 1
    for (int t = 0; t < 2; ++t) {
        int m, mat, mn, matn;
        tile(t, m, mat); tile(1, mn, matn);
        const bf16_t* Bn = t == 0 ? (matn == 2 ? p.Wq : matn == 1 ? p.Wv : p.Wk) : nullptr;
        const bf16_t* tk = (t == 0 && wv == 2 && j < 32) ? p.Aq + (96 + j) * 16384 : nullptr;
        if (mat == 1) gemm<1, 8, 8, 4>(r, p.Aq, p.Wv, m * 256, hp * 256, lds, W_VF, wv, ROT2 * (i32 >> 2), tk, t == 1, Bn, mn * 256, hp * 256);
        else gemm<0, 8, 8, 4>(r, p.Aq, mat == 2 ? p.Wq : p.Wk, m * 256, hp * 256, lds, mat == 2 ? W_QX : W_KX, wv, ROT2 * (i32 >> 2), tk, t == 1, Bn, mn * 256, hp * 256);
        flags(2 * m, 2, 2 * hp, 2, mat);
    }
    {
        v4u wraw[64]; unsigned wd = 0u;
        const int l = otid() & 63; const v4u sw = mk_srd(p.Wo, 4096 * 1024 * 2); const unsigned wa = (unsigned)((wv * 1024 + (l >> 2)) * 2048 + i32 * 64 + (l & 3) * 16);
        auto wo_load = [&](int i) { switch (i) {
            case 0: asm volatile("buffer_load_dwordx4 %0, %1, %2, %3 offen" : "={a[128:131]}"(wraw[0]) : "v"(wa), "s"(sw), "s"(0) : "memory"); break;
            case 1: asm volatile("buffer_load_dwordx4 %0, %1, %2, %3 offen" : "={a[132:135]}"(wraw[1]) : "v"(wa), "s"(sw), "s"(32768) : "memory"); break;
            case 2: asm volatile("buffer_load_dwordx4 %0, %1, %2, %3 offen" : "={a[136:139]}"(wraw[2]) : "v"(wa), "s"(sw), "s"(65536) : "memory"); break;
            case 3: asm volatile("buffer_load_dwordx4 %0, %1, %2, %3 offen" : "={a[140:143]}"(wraw[3]) : "v"(wa), "s"(sw), "s"(98304) : "memory"); break;
            case 4: asm volatile("buffer_load_dwordx4 %0, %1, %2, %3 offen" : "={a[144:147]}"(wraw[4]) : "v"(wa), "s"(sw), "s"(131072) : "memory"); break;
            case 5: asm volatile("buffer_load_dwordx4 %0, %1, %2, %3 offen" : "={a[148:151]}"(wraw[5]) : "v"(wa), "s"(sw), "s"(163840) : "memory"); break;
            case 6: asm volatile("buffer_load_dwordx4 %0, %1, %2, %3 offen" : "={a[152:155]}"(wraw[6]) : "v"(wa), "s"(sw), "s"(196608) : "memory"); break;
            case 7: asm volatile("buffer_load_dwordx4 %0, %1, %2, %3 offen" : "={a[156:159]}"(wraw[7]) : "v"(wa), "s"(sw), "s"(229376) : "memory"); break;
            case 8: asm volatile("buffer_load_dwordx4 %0, %1, %2, %3 offen" : "={a[160:163]}"(wraw[8]) : "v"(wa), "s"(sw), "s"(262144) : "memory"); break;
            case 9: asm volatile("buffer_load_dwordx4 %0, %1, %2, %3 offen" : "={a[164:167]}"(wraw[9]) : "v"(wa), "s"(sw), "s"(294912) : "memory"); break;
            case 10: asm volatile("buffer_load_dwordx4 %0, %1, %2, %3 offen" : "={a[168:171]}"(wraw[10]) : "v"(wa), "s"(sw), "s"(327680) : "memory"); break;
            case 11: asm volatile("buffer_load_dwordx4 %0, %1, %2, %3 offen" : "={a[172:175]}"(wraw[11]) : "v"(wa), "s"(sw), "s"(360448) : "memory"); break;
            case 12: asm volatile("buffer_load_dwordx4 %0, %1, %2, %3 offen" : "={a[176:179]}"(wraw[12]) : "v"(wa), "s"(sw), "s"(393216) : "memory"); break;
            case 13: asm volatile("buffer_load_dwordx4 %0, %1, %2, %3 offen" : "={a[180:183]}"(wraw[13]) : "v"(wa), "s"(sw), "s"(425984) : "memory"); break;
            case 14: asm volatile("buffer_load_dwordx4 %0, %1, %2, %3 offen" : "={a[184:187]}"(wraw[14]) : "v"(wa), "s"(sw), "s"(458752) : "memory"); break;
            case 15: asm volatile("buffer_load_dwordx4 %0, %1, %2, %3 offen" : "={a[188:191]}"(wraw[15]) : "v"(wa), "s"(sw), "s"(491520) : "memory"); break;
            case 16: asm volatile("buffer_load_dwordx4 %0, %1, %2, %3 offen" : "={a[192:195]}"(wraw[16]) : "v"(wa), "s"(sw), "s"(524288) : "memory"); break;
            case 17: asm volatile("buffer_load_dwordx4 %0, %1, %2, %3 offen" : "={a[196:199]}"(wraw[17]) : "v"(wa), "s"(sw), "s"(557056) : "memory"); break;
            case 18: asm volatile("buffer_load_dwordx4 %0, %1, %2, %3 offen" : "={a[200:203]}"(wraw[18]) : "v"(wa), "s"(sw), "s"(589824) : "memory"); break;
            case 19: asm volatile("buffer_load_dwordx4 %0, %1, %2, %3 offen" : "={a[204:207]}"(wraw[19]) : "v"(wa), "s"(sw), "s"(622592) : "memory"); break;
            case 20: asm volatile("buffer_load_dwordx4 %0, %1, %2, %3 offen" : "={a[208:211]}"(wraw[20]) : "v"(wa), "s"(sw), "s"(655360) : "memory"); break;
            case 21: asm volatile("buffer_load_dwordx4 %0, %1, %2, %3 offen" : "={a[212:215]}"(wraw[21]) : "v"(wa), "s"(sw), "s"(688128) : "memory"); break;
            case 22: asm volatile("buffer_load_dwordx4 %0, %1, %2, %3 offen" : "={a[216:219]}"(wraw[22]) : "v"(wa), "s"(sw), "s"(720896) : "memory"); break;
            case 23: asm volatile("buffer_load_dwordx4 %0, %1, %2, %3 offen" : "={a[220:223]}"(wraw[23]) : "v"(wa), "s"(sw), "s"(753664) : "memory"); break;
            case 24: asm volatile("buffer_load_dwordx4 %0, %1, %2, %3 offen" : "={a[224:227]}"(wraw[24]) : "v"(wa), "s"(sw), "s"(786432) : "memory"); break;
            case 25: asm volatile("buffer_load_dwordx4 %0, %1, %2, %3 offen" : "={a[228:231]}"(wraw[25]) : "v"(wa), "s"(sw), "s"(819200) : "memory"); break;
            case 26: asm volatile("buffer_load_dwordx4 %0, %1, %2, %3 offen" : "={a[232:235]}"(wraw[26]) : "v"(wa), "s"(sw), "s"(851968) : "memory"); break;
            case 27: asm volatile("buffer_load_dwordx4 %0, %1, %2, %3 offen" : "={a[236:239]}"(wraw[27]) : "v"(wa), "s"(sw), "s"(884736) : "memory"); break;
            case 28: asm volatile("buffer_load_dwordx4 %0, %1, %2, %3 offen" : "={a[240:243]}"(wraw[28]) : "v"(wa), "s"(sw), "s"(917504) : "memory"); break;
            case 29: asm volatile("buffer_load_dwordx4 %0, %1, %2, %3 offen" : "={a[244:247]}"(wraw[29]) : "v"(wa), "s"(sw), "s"(950272) : "memory"); break;
            case 30: asm volatile("buffer_load_dwordx4 %0, %1, %2, %3 offen" : "={a[248:251]}"(wraw[30]) : "v"(wa), "s"(sw), "s"(983040) : "memory"); break;
            case 31: asm volatile("buffer_load_dwordx4 %0, %1, %2, %3 offen" : "={a[252:255]}"(wraw[31]) : "v"(wa), "s"(sw), "s"(1015808) : "memory"); break;
            case 32: asm volatile("buffer_load_dwordx4 %0, %1, %2, %3 offen" : "={v[176:179]}"(wraw[32]) : "v"(wa), "s"(sw), "s"(1048576) : "memory"); break;
            case 33: asm volatile("buffer_load_dwordx4 %0, %1, %2, %3 offen" : "={v[180:183]}"(wraw[33]) : "v"(wa), "s"(sw), "s"(1081344) : "memory"); break;
            case 34: asm volatile("buffer_load_dwordx4 %0, %1, %2, %3 offen" : "={v[184:187]}"(wraw[34]) : "v"(wa), "s"(sw), "s"(1114112) : "memory"); break;
            case 35: asm volatile("buffer_load_dwordx4 %0, %1, %2, %3 offen" : "={v[188:191]}"(wraw[35]) : "v"(wa), "s"(sw), "s"(1146880) : "memory"); break;
            case 36: asm volatile("buffer_load_dwordx4 %0, %1, %2, %3 offen" : "={v[192:195]}"(wraw[36]) : "v"(wa), "s"(sw), "s"(1179648) : "memory"); break;
            case 37: asm volatile("buffer_load_dwordx4 %0, %1, %2, %3 offen" : "={v[196:199]}"(wraw[37]) : "v"(wa), "s"(sw), "s"(1212416) : "memory"); break;
            case 38: asm volatile("buffer_load_dwordx4 %0, %1, %2, %3 offen" : "={v[200:203]}"(wraw[38]) : "v"(wa), "s"(sw), "s"(1245184) : "memory"); break;
            case 39: asm volatile("buffer_load_dwordx4 %0, %1, %2, %3 offen" : "={v[204:207]}"(wraw[39]) : "v"(wa), "s"(sw), "s"(1277952) : "memory"); break;
            case 40: asm volatile("buffer_load_dwordx4 %0, %1, %2, %3 offen" : "={v[208:211]}"(wraw[40]) : "v"(wa), "s"(sw), "s"(1310720) : "memory"); break;
            case 41: asm volatile("buffer_load_dwordx4 %0, %1, %2, %3 offen" : "={v[212:215]}"(wraw[41]) : "v"(wa), "s"(sw), "s"(1343488) : "memory"); break;
            case 42: asm volatile("buffer_load_dwordx4 %0, %1, %2, %3 offen" : "={v[216:219]}"(wraw[42]) : "v"(wa), "s"(sw), "s"(1376256) : "memory"); break;
            case 43: asm volatile("buffer_load_dwordx4 %0, %1, %2, %3 offen" : "={v[220:223]}"(wraw[43]) : "v"(wa), "s"(sw), "s"(1409024) : "memory"); break;
            case 44: asm volatile("buffer_load_dwordx4 %0, %1, %2, %3 offen" : "={v[224:227]}"(wraw[44]) : "v"(wa), "s"(sw), "s"(1441792) : "memory"); break;
            case 45: asm volatile("buffer_load_dwordx4 %0, %1, %2, %3 offen" : "={v[228:231]}"(wraw[45]) : "v"(wa), "s"(sw), "s"(1474560) : "memory"); break;
            case 46: asm volatile("buffer_load_dwordx4 %0, %1, %2, %3 offen" : "={v[232:235]}"(wraw[46]) : "v"(wa), "s"(sw), "s"(1507328) : "memory"); break;
            case 47: asm volatile("buffer_load_dwordx4 %0, %1, %2, %3 offen" : "={v[236:239]}"(wraw[47]) : "v"(wa), "s"(sw), "s"(1540096) : "memory"); break;
            case 48: asm volatile("buffer_load_dwordx4 %0, %1, %2, %3 offen" : "={v[240:243]}"(wraw[48]) : "v"(wa), "s"(sw), "s"(1572864) : "memory"); break;
            case 49: asm volatile("buffer_load_dwordx4 %0, %1, %2, %3 offen" : "={v[244:247]}"(wraw[49]) : "v"(wa), "s"(sw), "s"(1605632) : "memory"); break;
            case 50: asm volatile("buffer_load_dwordx4 %0, %1, %2, %3 offen" : "={v[248:251]}"(wraw[50]) : "v"(wa), "s"(sw), "s"(1638400) : "memory"); break;
            case 51: asm volatile("buffer_load_dwordx4 %0, %1, %2, %3 offen" : "={v[252:255]}"(wraw[51]) : "v"(wa), "s"(sw), "s"(1671168) : "memory"); break;
            case 52: asm volatile("buffer_load_dwordx4 %0, %1, %2, %3 offen" : "={a[80:83]}"(wraw[52]) : "v"(wa), "s"(sw), "s"(1703936) : "memory"); break;
            case 53: asm volatile("buffer_load_dwordx4 %0, %1, %2, %3 offen" : "={a[84:87]}"(wraw[53]) : "v"(wa), "s"(sw), "s"(1736704) : "memory"); break;
            case 54: asm volatile("buffer_load_dwordx4 %0, %1, %2, %3 offen" : "={a[88:91]}"(wraw[54]) : "v"(wa), "s"(sw), "s"(1769472) : "memory"); break;
            case 55: asm volatile("buffer_load_dwordx4 %0, %1, %2, %3 offen" : "={a[92:95]}"(wraw[55]) : "v"(wa), "s"(sw), "s"(1802240) : "memory"); break;
            case 56: asm volatile("buffer_load_dwordx4 %0, %1, %2, %3 offen" : "={a[96:99]}"(wraw[56]) : "v"(wa), "s"(sw), "s"(1835008) : "memory"); break;
            case 57: asm volatile("buffer_load_dwordx4 %0, %1, %2, %3 offen" : "={a[100:103]}"(wraw[57]) : "v"(wa), "s"(sw), "s"(1867776) : "memory"); break;
            case 58: asm volatile("buffer_load_dwordx4 %0, %1, %2, %3 offen" : "={a[104:107]}"(wraw[58]) : "v"(wa), "s"(sw), "s"(1900544) : "memory"); break;
            case 59: asm volatile("buffer_load_dwordx4 %0, %1, %2, %3 offen" : "={a[108:111]}"(wraw[59]) : "v"(wa), "s"(sw), "s"(1933312) : "memory"); break;
            case 60: asm volatile("buffer_load_dwordx4 %0, %1, %2, %3 offen" : "={a[112:115]}"(wraw[60]) : "v"(wa), "s"(sw), "s"(1966080) : "memory"); break;
            case 61: asm volatile("buffer_load_dwordx4 %0, %1, %2, %3 offen" : "={a[116:119]}"(wraw[61]) : "v"(wa), "s"(sw), "s"(1998848) : "memory"); break;
            case 62: asm volatile("buffer_load_dwordx4 %0, %1, %2, %3 offen" : "={a[120:123]}"(wraw[62]) : "v"(wa), "s"(sw), "s"(2031616) : "memory"); break;
            case 63: asm volatile("buffer_load_dwordx4 %0, %1, %2, %3 offen" : "={a[124:127]}"(wraw[63]) : "v"(wa), "s"(sw), "s"(2064384) : "memory"); break;
        } };
        const int mt = 8 + (i32 & 7);
        gemm<0, 4, 8, 4, 2>(r, p.Aq, p.Wq, mt * 128, hp * 256, lds, W_QX, wv, ROT1 * (i32 >> 2), nullptr, false, nullptr, 0, 0,
                            [&](int i) {
#pragma unroll
                                for (int q = 2 * i; q < 2 * i + 2; ++q) { if (q < WO_IN) wo_load(q); else asm volatile("buffer_load_dword %0, %1, %2, 0 offen" : "+v"(wd) : "v"(0xF0000000u), "s"(sw) : "memory"); } });
        flags(mt, 1, 2 * hp, 2, 2);
        asm volatile("" : "+v"(wd));
#pragma unroll
        for (int i = WO_IN; i < 64; ++i) wo_load(i);
        oproj_block(p, r, lds, wv, i32, x, j, wraw, tag);
    }
}

__device__ __forceinline__ void help_block(const Params& p, rsrc_t r, unsigned char* lds, const int wv, const int k, const unsigned tag) {
    const int j = -32 + k; (void)j;
    if (HELP_D) wait_long(rtnow() + HELP_D);
#pragma unroll 1
    for (int i = 0; i < 4; ++i) prep_block(p, r, i * 32 + k, wv, tag);
#pragma unroll 1
    for (int i = 0; i < 4; ++i) if (!conv_block<0>(p, r, k * 4 + i, lds, wv, j, tag, [](int) {})) return;
    attn_block(p, r, lds, wv, k, j, tag);
}

__global__ void __launch_bounds__(256, 1) k_main(Params p) {
    extern __shared__ __attribute__((aligned(16))) unsigned char lds[];
    rsrc_t r = __builtin_amdgcn_make_buffer_rsrc(p.ws, 0, W_TOTAL, 0x00020000);
    const int wv = __builtin_amdgcn_readfirstlane(threadIdx.x >> 6);
    int role = blockIdx.x;
    unsigned xcc; asm volatile("s_getreg_b32 %0, hwreg(HW_REG_XCC_ID)" : "=s"(xcc)); xcc &= 7u;
    unsigned near = 0u, old = 0u, slot = (xcc >> 2) * 128u + (blockIdx.x >> 4) * 8u + (xcc & 3u) * 2u + ((blockIdx.x >> 3) & 1u);
    const unsigned tag = __builtin_amdgcn_readfirstlane(ld4(r, (threadIdx.x & 63) == 1 ? F_CLM + slot * 64 + 4 : F_EP)) * 2654435761u + 0x9E3779B9u;
    if (threadIdx.x == 0) old = __hip_atomic_exchange((unsigned*)(p.ws + F_CLM + slot * 64), tag, __ATOMIC_RELAXED, __HIP_MEMORY_SCOPE_AGENT);
    near = 126u;
    if (xcc < 4u && ((CALM >> (slot >> 5)) & 1u)) {
        const unsigned gr = (slot >> 5) & 3u;
        const rsrc_t rh = __builtin_amdgcn_make_buffer_rsrc(p.ws + hx_base(p.ws), 0, U_END * 16384, 0x00020000);
        const unsigned ca = (threadIdx.x & 63) != 0 ? 0xF0000000u : (slot & 127u) * 16384u + (unsigned)wv * 2048u + CAL_O;
        const v4u z = {~0u, ~0u, ~0u, ~0u}; unsigned best0 = ~0u, best1 = ~0u;
        for (int i = 0; i < CAL_N; ++i) {
            const unsigned t0 = (unsigned)__builtin_amdgcn_s_memtime(); __builtin_amdgcn_s_waitcnt(0);
            st16(rh, ca, z); DRAIN();
            const unsigned t1 = (unsigned)__builtin_amdgcn_s_memtime(); __builtin_amdgcn_s_waitcnt(0);
            st16(rh, ca + 8192, z); DRAIN();
            const unsigned t2 = (unsigned)__builtin_amdgcn_s_memtime(); __builtin_amdgcn_s_waitcnt(0);
            best0 = min(best0, t1 - t0); best1 = min(best1, t2 - t1); }
        near = gr * 2u + (best1 < best0 ? 1u : 0u);
    }
    if (threadIdx.x == 0) {
        for (int i = 0; i < 256 && old == tag; ++i) { slot = (slot + 1u) & 255u; old = __hip_atomic_exchange((unsigned*)(p.ws + F_CLM + slot * 64), tag, __ATOMIC_RELAXED, __HIP_MEMORY_SCOPE_AGENT); }
        *(volatile __attribute__((address_space(3))) int*)(size_t)(unsigned)(size_t)lds = (int)slot;
    }
    __syncthreads();
    role = __builtin_amdgcn_readfirstlane(*(volatile __attribute__((address_space(3))) int*)(size_t)(unsigned)(size_t)lds) & 255;
    __syncthreads();
    if (role >= 128) bulk_block(p, r, lds, wv, role - 128, tag
        ); else if (rec_block(p, r, lds, wv, role, near, tag)) help_block(p, r, lds, wv, (role >> 3) * 2 + (role & 1), tag);
}

extern "C" void kernel_launch(void* const* d_in, const int* in_sizes, int n_in, void* d_out, int out_size, void* d_ws, size_t ws_size, hipStream_t stream) {
    Params p;
    p.Aq = (const bf16_t*)d_in[0]; p.Ak = (const bf16_t*)d_in[1]; p.Av = (const bf16_t*)d_in[2];
    p.shift_k = (const bf16_t*)d_in[3]; p.ssm_k = (const bf16_t*)d_in[4]; p.ssm_D = (const bf16_t*)d_in[5];
    p.mem0 = (const bf16_t*)d_in[7]; p.Wq = (const bf16_t*)d_in[8]; p.Wk = (const bf16_t*)d_in[9];
    p.Wv = (const bf16_t*)d_in[10]; p.Wo = (const bf16_t*)d_in[11]; p.gate_w = (const bf16_t*)d_in[13];
    p.out = (bf16_t*)d_out; p.ws = (unsigned char*)d_ws;
    (void)hipFuncSetAttribute((const void*)k_main, hipFuncAttributeMaxDynamicSharedMemorySize, L_TOTAL);
    k_main<<<256, 256, L_TOTAL, stream>>>(p);
}
```

```cpp
#include <hip/hip_runtime.h>
#include <stdint.h>
#include <type_traits>
typedef unsigned short bf16_t;
using bf16x8 = __attribute__((ext_vector_type(8))) short;
using f32x4 = __attribute__((ext_vector_type(4))) float;
typedef unsigned v4u __attribute__((ext_vector_type(4)));
typedef unsigned v2u __attribute__((ext_vector_type(2)));
typedef __amdgpu_buffer_rsrc_t rsrc_t;

typedef __bf16 hbf2 __attribute__((ext_vector_type(2)));
typedef float f32x2 __attribute__((ext_vector_type(2)));
__device__ __forceinline__ unsigned pack2(float a, float b) { f32x2 v = {a, b}; return __builtin_bit_cast(unsigned, __builtin_convertvector(v, hbf2)); }
__device__ __forceinline__ float rbfh(float f) { return __uint_as_float(pack2(0.f, f)); }
__device__ __forceinline__ f32x2 rbf2(f32x2 f) { return (f32x2){rbfh(f.x), rbfh(f.y)}; }
__device__ __forceinline__ unsigned nosent(unsigned x) {
    typedef unsigned short us2 __attribute__((ext_vector_type(2)));
    const us2 a = __builtin_bit_cast(us2, x), c = {(unsigned short)0xFFFE, (unsigned short)0xFFFE}; return __builtin_bit_cast(unsigned, __builtin_elementwise_min(a, c)); }

constexpr int S = 2048, D = 1024, H = 32, DH = 128, M = 8, SEG = 32, NSEG = 64;

constexpr unsigned W_FLG = 0;
constexpr unsigned F_KVQ = W_FLG;
constexpr unsigned F_EP = W_FLG + 32768;
constexpr unsigned F_ERR = W_FLG + 32768 + 256;
constexpr unsigned F_PREP = W_FLG + 32768 + 512;
constexpr unsigned F_CAL = W_FLG + 73728;
constexpr unsigned F_CLM = W_FLG + 77824;
constexpr unsigned F_CONV = W_FLG + 33792;
constexpr unsigned F_PROG = W_FLG + 36864;
constexpr unsigned F_ATT = W_FLG + 40960;
constexpr unsigned F_RDY = W_FLG + 49152;
constexpr unsigned FLG_BYTES = 94208;
constexpr unsigned HO_KVM = W_FLG + FLG_BYTES;
constexpr unsigned W_A = HO_KVM + 64 * 32 * 4096;
constexpr unsigned W_GATE = W_A + S * D * 2;
constexpr unsigned W_QX = W_GATE + 8192;
constexpr unsigned W_KX = W_QX + S * 4096 * 2;
constexpr unsigned W_VF = W_KX + S * 4096 * 2;
constexpr unsigned W_OX = W_VF + 64 * 32 * 8192;
constexpr unsigned DBG_BYTES = 0;
constexpr unsigned W_DBG = W_OX + S * 4096 * 2;
constexpr unsigned W_U = W_DBG + DBG_BYTES;
constexpr unsigned W_TOTAL = W_U + S * D * 2;

__device__ __forceinline__ v4u ld16(rsrc_t r, unsigned off) { return __builtin_amdgcn_raw_buffer_load_b128(r, off, 0, 16); }
__device__ __forceinline__ v2u ld8(rsrc_t r, unsigned off) { return __builtin_amdgcn_raw_buffer_load_b64(r, off, 0, 16); }
__device__ __forceinline__ unsigned ld4(rsrc_t r, unsigned off) { return __builtin_amdgcn_raw_buffer_load_b32(r, off, 0, 16); }
__device__ __forceinline__ void st16(rsrc_t r, unsigned off, v4u v) { __builtin_amdgcn_raw_buffer_store_b128(v, r, off, 0, 16); }
__device__ __forceinline__ void st8(rsrc_t r, unsigned off, v2u v) { __builtin_amdgcn_raw_buffer_store_b64(v, r, off, 0, 16); }
__device__ __forceinline__ void st4(rsrc_t r, unsigned off, unsigned v) { __builtin_amdgcn_raw_buffer_store_b32(v, r, off, 0, 16); }
__device__ __forceinline__ bf16x8 as_frag(v4u v) { return __builtin_bit_cast(bf16x8, v); }
#define D1 134
#define D2 90
#define D3 64
#define LB0 16
#define LC0 9
#define LA0 10
#define TR_UP 4
#define TR_DN 2
#define L_DEC 3
#define STAG 1
#define UP_SH 2
#define EARLY_FROM 58
#define OUT_WARM 0
#define AROT 0
#define P_SLP 2
#define O_SLP 2
#define W_NT 0
#define TOL 0
#define A_SLP 0
#define PEN2 20
#define A_UP 48
#define CT0 2
#define CPF 1
#define PH_M 3
#define SKEW 0
#define SKEW1 0
#define XT 1
#define TRK 3
#define WO_NT 0
#define WO_TRK 1
#define WIN 3
#define ROT0 3
#define ROT1 2
#define ROT2 2
#define B_NT 0
#define CALM 15
#define R4M 1
#define HX_SH 0u
#define FINE_T 1
#define FINE_N 0
#define FINE_A 0
#define O_ND 12
#define PEN0 32
#define X0C 20
#define X0A 16
#define X1A 8
#define PEN 60
#define MEMBAR() asm volatile("" ::: "memory")
#define ENDPGM() asm volatile("s_endpgm" ::: "memory")
#define DRAIN() asm volatile("s_waitcnt vmcnt(0)" ::: "memory")

__device__ __forceinline__ float xor16_max(float x) { auto r = __builtin_amdgcn_permlane16_swap(__float_as_uint(x), __float_as_uint(x), false, false); return fmaxf(__uint_as_float(r[0]), __uint_as_float(r[1])); }
__device__ __forceinline__ float xor32_max(float x) { auto r = __builtin_amdgcn_permlane32_swap(__float_as_uint(x), __float_as_uint(x), false, false); return fmaxf(__uint_as_float(r[0]), __uint_as_float(r[1])); }
__device__ __forceinline__ float xor16_add(float x) { auto r = __builtin_amdgcn_permlane16_swap(__float_as_uint(x), __float_as_uint(x), false, false); return __uint_as_float(r[0]) + __uint_as_float(r[1]); }
__device__ __forceinline__ float xor32_add(float x) { auto r = __builtin_amdgcn_permlane32_swap(__float_as_uint(x), __float_as_uint(x), false, false); return __uint_as_float(r[0]) + __uint_as_float(r[1]); }

typedef short v4i16_t __attribute__((ext_vector_type(4)));
__device__ __forceinline__ v2u trb(unsigned lds_addr) { return __builtin_bit_cast(v2u, __builtin_amdgcn_ds_read_tr16_b64_v4i16((__attribute__((address_space(3))) v4i16_t*)(size_t)lds_addr)); }
__device__ __forceinline__ int otid_(int wv) { int t; asm volatile("v_mbcnt_lo_u32_b32 %0, -1, 0\n\tv_mbcnt_hi_u32_b32 %0, -1, %0" : "=v"(t)); return wv * 64 + t; }
#define otid() otid_(wv)
__device__ __forceinline__ bool wait_word(rsrc_t r, unsigned off, unsigned want, unsigned code) {
    for (unsigned spin = 0;; ++spin) { MEMBAR();
        const unsigned v = __builtin_amdgcn_readfirstlane(ld4(r, off));
        if (v >= want) return true;
        if (spin > (1u << 20)) { st4(r, F_ERR, code); return false; }
        __builtin_amdgcn_s_sleep(8); }
}

__device__ __forceinline__ bool wait_prep(rsrc_t r, int l, unsigned tag) {
    for (unsigned spin = 0;; ++spin) { MEMBAR();
        const unsigned v = ld4(r, F_PREP + (l & 31) * 4);
        if (__all(v == tag)) return true;
        if (spin > (1u << 20)) { st4(r, F_ERR, 512u); return false; }
        __builtin_amdgcn_s_sleep(8); }
}
__device__ __forceinline__ bool wait_cal(rsrc_t r, int l, unsigned tag, v4u (&q)[4]) {
    for (unsigned spin = 0;; ++spin) { MEMBAR();
        bool ok = true;
#pragma unroll
        for (int i = 0; i < 4; ++i) { q[i] = ld16(r, F_CAL + l * 64 + i * 16); }
#pragma unroll
        for (int i = 0; i < 4; ++i) ok &= (q[i].x == tag) & (q[i].z == tag);
        if (__all(ok)) return true;
        if (spin > (1u << 20)) { st4(r, F_ERR, 8u); return false; }
        __builtin_amdgcn_s_sleep(4); }
}
__device__ __forceinline__ unsigned rtnow() { return (unsigned)__builtin_amdgcn_s_memrealtime(); }
__device__ __forceinline__ void wait_long(unsigned target) { for (int i = 0; i < 4096; ++i) { if ((int)(rtnow() - target) >= 0) break; __builtin_amdgcn_s_sleep(8); } }
constexpr int L_TAB = 20480;
constexpr int L_QK = 24576;
__device__ __forceinline__ unsigned img(int f) { return (unsigned)(L_QK + f * 16 + (f >> 4) * 128); }
constexpr int L_FL = 30720, L_Z = L_FL + 128, L_LOG = 31744, L_W = 32768;
constexpr int L_TOTAL = 163840;
constexpr unsigned U_END = 128;
#define PMIN 92
#define V_SLP 2
#define R_SLP 4
constexpr int WO_IN = 52;
#define HELP_D 300
#define CAL_O 0u
#define H_SLP 0
#define CAL_N 2
#define STEP_MIN 180u

struct Params {
    const bf16_t *Aq, *Ak, *Av, *shift_k, *ssm_k, *ssm_D, *mem0, *Wq, *Wk, *Wv, *Wo, *gate_w;
    bf16_t* out; unsigned char* ws;
};
__host__ __device__ __forceinline__ unsigned hx_base(const void* ws) { const size_t a = (size_t)ws; return (unsigned)((((a + W_TOTAL + 0x7FFFFFu) >> 23) << 23) - a) + HX_SH; }

typedef short v4s __attribute__((ext_vector_type(4)));
typedef unsigned v32u __attribute__((ext_vector_type(32)));
typedef unsigned v16u __attribute__((ext_vector_type(16)));
typedef unsigned v8u __attribute__((ext_vector_type(8)));
typedef unsigned v12u __attribute__((ext_vector_type(12)));
__device__ __forceinline__ v4u mk_srd(const void* p, unsigned bytes) { const size_t a = (size_t)p; v4u d;
    d.x = __builtin_amdgcn_readfirstlane((unsigned)a); d.y = __builtin_amdgcn_readfirstlane((unsigned)(a >> 32) & 0xFFFFu); d.z = __builtin_amdgcn_readfirstlane(bytes); d.w = 0x00020000u; return d; }
__device__ __forceinline__ bf16x8 ldsa(unsigned addr) { return *(const __attribute__((address_space(3))) bf16x8*)(size_t)addr; }
__device__ __forceinline__ bool rec_block(const Params& p, rsrc_t r, unsigned char* lds, const int wv, const int b, const unsigned near, const unsigned tag) {
    const int w = wv;
    int tid = otid(), l = tid & 63, n16 = l & 15, g = l >> 4;
    const int x = (b & 7) >> 1, i32 = (b >> 3) * 2 + (b & 1);
    const unsigned hxo = hx_base(p.ws), OOB = 0xF0000000u;
    const v4u rhd = mk_srd(p.ws + hxo, U_END * 16384), wsd = mk_srd(p.ws, W_TOTAL);
    const rsrc_t rh = __builtin_amdgcn_make_buffer_rsrc(p.ws + hxo, 0, U_END * 16384, 0x00020000);
    const int G = x - 1, vi = G * 32 + i32, h = x == 0 ? i32 : vi >> 1;
    const unsigned pr_a = x == 3 ? OOB : F_PROG + h * 128 + (x == 0 ? 0 : 8 + (vi & 1) * 8);
    {
        const v4u ones = {0xFFFFFFFFu, 0xFFFFFFFFu, 0xFFFFFFFFu, 0xFFFFFFFFu};
#pragma unroll
        for (int i = 0; i < 4; ++i) st16(rh, b * 16384 + (w * 4 + i) * 1024 + l * 16, ones);
        st4(r, tid == 192 ? pr_a : OOB, x ? 63u : 0u);
    }
    const unsigned cal_a = l == 0 ? F_CAL + b * 32 + w * 8 : OOB;
    if (x == 3) { DRAIN(); st8(r, cal_a, (v2u){tag, near}); return true; }
    v4u cpf;
    if (CPF) {
        unsigned lo, hi, l2; asm volatile("s_getpc_b64 s[98:99]\n.Lpc%=:\n\ts_add_u32 %2, s98, rec_v_code-.Lpc%=\n\ts_add_u32 %0, s98, rec_s_code-.Lpc%=\n\ts_addc_u32 %1, s99, 0" : "=s"(lo), "=s"(hi), "=s"(l2) :: "s98", "s99", "scc");
        const size_t a = (((size_t)hi << 32) | lo) + (size_t)(long long)(x ? (int)(l2 - lo) : 0) + (size_t)(((i32 * 4 + w) % 6) * 1024 + l * 16);
        asm volatile("global_load_dwordx4 %0, %1, off" : "=v"(cpf) : "v"(a & ~(size_t)15) : "memory");
    }
    const int c = n16 >> 2;
    v32u wa[8], wg[4];
    auto votes = [&](unsigned& nm) -> bool {
        v4u q[4]; if (!wait_cal(r, l, tag, q)) return false;
        typedef __attribute__((address_space(3))) int li;
        li* cnt = (li*)(size_t)(w * 256);
        cnt[l] = 0;
        __builtin_amdgcn_wave_barrier();
#pragma unroll
        for (int i = 0; i < 8; ++i) { const unsigned y = (i & 1) ? q[i >> 1].w : q[i >> 1].y; __hip_atomic_fetch_add(cnt + ((y >> 1) & 63u), (y & 1u) ? 1 : -1, __ATOMIC_RELAXED, __HIP_MEMORY_SCOPE_WAVEFRONT); }
        __builtin_amdgcn_wave_barrier();
        const int c0 = cnt[l], c4 = cnt[0] + cnt[1] + cnt[2] + cnt[3];
        nm = (unsigned)__ballot((abs(c0) >= 64 ? c0 : c4) > 0);
        return true; };
    typedef __attribute__((address_space(3))) volatile unsigned lu32;
    const unsigned kvo = HO_KVM + h * 4096 + (x ? 2048 : 0);
    if (x == 0) {
        {
            const v4u sq = mk_srd(p.Wq, 1024 * 4096 * 2), sk = mk_srd(p.Wk, 1024 * 4096 * 2);
            const unsigned m0b = L_W + w * 1024, f = (c & 1) | ((g & 1) << 1) | ((c >> 1) << 2);
            const unsigned t0 = L_W + (16 * (g >> 1) + 4 * (g & 1) + (c & 1) + 8 * (c >> 1)) * 256 + (((2 * w) ^ f) << 5) + (n16 & 3) * 8;
            v16u pa;
            pa[0] = (4 * w + g) * 8192 + h * 256 + ((n16 ^ (((g & 1) | (w << 1)) << 1)) << 4);
#pragma unroll
            for (int q = 0; q < 8; ++q) pa[1 + q] = (t0 + ((w + (q >> 1)) & 3) * 32768) ^ ((q & 1) << 5);
            pa[9] = cal_a; pa[10] = tag; pa[11] = near; pa[12] = L_W + w * 32768 + l * 16; pa[13] = 0u; pa[14] = 0u; pa[15] = 0u;
    asm volatile(
        "s_add_u32 s80, %[m0b], 0\n\t"
        "s_mov_b32 s81, 0\n\t"
        "s_mov_b32 s82, 4\n\t"
        ".Ld0%=:\n\t"
        "s_mov_b32 m0, s80\n\t"
        "s_add_u32 s80, s80, 0x8000\n\t"
        "buffer_load_dwordx4 v112, %[sq], s81 offen lds\n\t"
        "s_add_u32 m0, m0, 0x1000\n\t"
        "s_add_u32 s81, s81, 0x20000\n\t"
        "buffer_load_dwordx4 v112, %[sq], s81 offen lds\n\t"
        "s_add_u32 m0, m0, 0x1000\n\t"
        "s_add_u32 s81, s81, 0x20000\n\t"
        "buffer_load_dwordx4 v112, %[sq], s81 offen lds\n\t"
        "s_add_u32 m0, m0, 0x1000\n\t"
        "s_add_u32 s81, s81, 0x20000\n\t"
        "buffer_load_dwordx4 v112, %[sq], s81 offen lds\n\t"
        "s_add_u32 m0, m0, 0x1000\n\t"
        "s_add_u32 s81, s81, 0x20000\n\t"
        "s_sub_u32 s82, s82, 1\n\t"
        "s_cmp_lg_u32 s82, 0\n\t"
        "s_cbranch_scc1 .Ld0%=\n\t"
        "s_add_u32 s80, %[m0b], 16384\n\t"
        "s_mov_b32 s81, 0\n\t"
        "s_mov_b32 s82, 4\n\t"
        ".Ld1%=:\n\t"
        "s_mov_b32 m0, s80\n\t"
        "s_add_u32 s80, s80, 0x8000\n\t"
        "buffer_load_dwordx4 v112, %[sk], s81 offen lds\n\t"
        "s_add_u32 m0, m0, 0x1000\n\t"
        "s_add_u32 s81, s81, 0x20000\n\t"
        "buffer_load_dwordx4 v112, %[sk], s81 offen lds\n\t"
        "s_add_u32 m0, m0, 0x1000\n\t"
        "s_add_u32 s81, s81, 0x20000\n\t"
        "buffer_load_dwordx4 v112, %[sk], s81 offen lds\n\t"
        "s_add_u32 m0, m0, 0x1000\n\t"
        "s_add_u32 s81, s81, 0x20000\n\t"
        "buffer_load_dwordx4 v112, %[sk], s81 offen lds\n\t"
        "s_add_u32 m0, m0, 0x1000\n\t"
        "s_add_u32 s81, s81, 0x20000\n\t"
        "s_sub_u32 s82, s82, 1\n\t"
        "s_cmp_lg_u32 s82, 0\n\t"
        "s_cbranch_scc1 .Ld1%=\n\t"
        "s_waitcnt vmcnt(32)\n\t"
        "buffer_store_dwordx2 v[122:123], v121, %[wsd], 0 offen sc1\n\t"
        "s_waitcnt vmcnt(17)\n\t"
        "s_barrier\n\t"
        "ds_read_b64_tr_b16 v[0:1], v113 offset:0\n\t"
        "ds_read_b64_tr_b16 v[2:3], v113 offset:512\n\t"
        "ds_read_b64_tr_b16 v[4:5], v113 offset:8192\n\t"
        "ds_read_b64_tr_b16 v[6:7], v113 offset:8704\n\t"
        "ds_read_b64_tr_b16 v[8:9], v114 offset:0\n\t"
        "ds_read_b64_tr_b16 v[10:11], v114 offset:512\n\t"
        "ds_read_b64_tr_b16 v[12:13], v114 offset:8192\n\t"
        "ds_read_b64_tr_b16 v[14:15], v114 offset:8704\n\t"
        "ds_read_b64_tr_b16 a[16:17], v115 offset:0\n\t"
        "ds_read_b64_tr_b16 a[18:19], v115 offset:512\n\t"
        "ds_read_b64_tr_b16 a[20:21], v115 offset:8192\n\t"
        "ds_read_b64_tr_b16 a[22:23], v115 offset:8704\n\t"
        "ds_read_b64_tr_b16 a[24:25], v116 offset:0\n\t"
        "ds_read_b64_tr_b16 a[26:27], v116 offset:512\n\t"
        "ds_read_b64_tr_b16 a[28:29], v116 offset:8192\n\t"
        "ds_read_b64_tr_b16 a[30:31], v116 offset:8704\n\t"
        "ds_read_b64_tr_b16 a[32:33], v117 offset:0\n\t"
        "ds_read_b64_tr_b16 a[34:35], v117 offset:512\n\t"
        "ds_read_b64_tr_b16 a[36:37], v117 offset:8192\n\t"
        "ds_read_b64_tr_b16 a[38:39], v117 offset:8704\n\t"
        "ds_read_b64_tr_b16 a[40:41], v118 offset:0\n\t"
        "ds_read_b64_tr_b16 a[42:43], v118 offset:512\n\t"
        "ds_read_b64_tr_b16 a[44:45], v118 offset:8192\n\t"
        "ds_read_b64_tr_b16 a[46:47], v118 offset:8704\n\t"
        "ds_read_b64_tr_b16 a[48:49], v119 offset:0\n\t"
        "ds_read_b64_tr_b16 a[50:51], v119 offset:512\n\t"
        "ds_read_b64_tr_b16 a[52:53], v119 offset:8192\n\t"
        "ds_read_b64_tr_b16 a[54:55], v119 offset:8704\n\t"
        "ds_read_b64_tr_b16 a[56:57], v120 offset:0\n\t"
        "ds_read_b64_tr_b16 a[58:59], v120 offset:512\n\t"
        "ds_read_b64_tr_b16 a[60:61], v120 offset:8192\n\t"
        "ds_read_b64_tr_b16 a[62:63], v120 offset:8704\n\t"
        "s_waitcnt lgkmcnt(0)\n\t"
        "s_barrier\n\t"
        "s_add_u32 s80, %[m0b], 0\n\t"
        "s_mov_b32 s81, 2097152\n\t"
        "s_mov_b32 s82, 4\n\t"
        ".Ld2%=:\n\t"
        "s_mov_b32 m0, s80\n\t"
        "s_add_u32 s80, s80, 0x8000\n\t"
        "buffer_load_dwordx4 v112, %[sq], s81 offen lds\n\t"
        "s_add_u32 m0, m0, 0x1000\n\t"
        "s_add_u32 s81, s81, 0x20000\n\t"
        "buffer_load_dwordx4 v112, %[sq], s81 offen lds\n\t"
        "s_add_u32 m0, m0, 0x1000\n\t"
        "s_add_u32 s81, s81, 0x20000\n\t"
        "buffer_load_dwordx4 v112, %[sq], s81 offen lds\n\t"
        "s_add_u32 m0, m0, 0x1000\n\t"
        "s_add_u32 s81, s81, 0x20000\n\t"
        "buffer_load_dwordx4 v112, %[sq], s81 offen lds\n\t"
        "s_add_u32 m0, m0, 0x1000\n\t"
        "s_add_u32 s81, s81, 0x20000\n\t"
        "s_sub_u32 s82, s82, 1\n\t"
        "s_cmp_lg_u32 s82, 0\n\t"
        "s_cbranch_scc1 .Ld2%=\n\t"
        "s_waitcnt vmcnt(17)\n\t"
        "s_barrier\n\t"
        "ds_read_b64_tr_b16 v[16:17], v113 offset:16384\n\t"
        "ds_read_b64_tr_b16 v[18:19], v113 offset:16896\n\t"
        "ds_read_b64_tr_b16 v[20:21], v113 offset:24576\n\t"
        "ds_read_b64_tr_b16 v[22:23], v113 offset:25088\n\t"
        "ds_read_b64_tr_b16 v[24:25], v114 offset:16384\n\t"
        "ds_read_b64_tr_b16 v[26:27], v114 offset:16896\n\t"
        "ds_read_b64_tr_b16 v[28:29], v114 offset:24576\n\t"
        "ds_read_b64_tr_b16 v[30:31], v114 offset:25088\n\t"
        "ds_read_b64_tr_b16 a[80:81], v115 offset:16384\n\t"
        "ds_read_b64_tr_b16 a[82:83], v115 offset:16896\n\t"
        "ds_read_b64_tr_b16 a[84:85], v115 offset:24576\n\t"
        "ds_read_b64_tr_b16 a[86:87], v115 offset:25088\n\t"
        "ds_read_b64_tr_b16 a[88:89], v116 offset:16384\n\t"
        "ds_read_b64_tr_b16 a[90:91], v116 offset:16896\n\t"
        "ds_read_b64_tr_b16 a[92:93], v116 offset:24576\n\t"
        "ds_read_b64_tr_b16 a[94:95], v116 offset:25088\n\t"
        "ds_read_b64_tr_b16 a[96:97], v117 offset:16384\n\t"
        "ds_read_b64_tr_b16 a[98:99], v117 offset:16896\n\t"
        "ds_read_b64_tr_b16 a[100:101], v117 offset:24576\n\t"
        "ds_read_b64_tr_b16 a[102:103], v117 offset:25088\n\t"
        "ds_read_b64_tr_b16 a[104:105], v118 offset:16384\n\t"
        "ds_read_b64_tr_b16 a[106:107], v118 offset:16896\n\t"
        "ds_read_b64_tr_b16 a[108:109], v118 offset:24576\n\t"
        "ds_read_b64_tr_b16 a[110:111], v118 offset:25088\n\t"
        "ds_read_b64_tr_b16 a[112:113], v119 offset:16384\n\t"
        "ds_read_b64_tr_b16 a[114:115], v119 offset:16896\n\t"
        "ds_read_b64_tr_b16 a[116:117], v119 offset:24576\n\t"
        "ds_read_b64_tr_b16 a[118:119], v119 offset:25088\n\t"
        "ds_read_b64_tr_b16 a[120:121], v120 offset:16384\n\t"
        "ds_read_b64_tr_b16 a[122:123], v120 offset:16896\n\t"
        "ds_read_b64_tr_b16 a[124:125], v120 offset:24576\n\t"
        "ds_read_b64_tr_b16 a[126:127], v120 offset:25088\n\t"
        "s_waitcnt lgkmcnt(0)\n\t"
        "s_barrier\n\t"
        "s_add_u32 s80, %[m0b], 16384\n\t"
        "s_mov_b32 s81, 2097152\n\t"
        "s_mov_b32 s82, 4\n\t"
        ".Ld3%=:\n\t"
        "s_mov_b32 m0, s80\n\t"
        "s_add_u32 s80, s80, 0x8000\n\t"
        "buffer_load_dwordx4 v112, %[sk], s81 offen lds\n\t"
        "s_add_u32 m0, m0, 0x1000\n\t"
        "s_add_u32 s81, s81, 0x20000\n\t"
        "buffer_load_dwordx4 v112, %[sk], s81 offen lds\n\t"
        "s_add_u32 m0, m0, 0x1000\n\t"
        "s_add_u32 s81, s81, 0x20000\n\t"
        "buffer_load_dwordx4 v112, %[sk], s81 offen lds\n\t"
        "s_add_u32 m0, m0, 0x1000\n\t"
        "s_add_u32 s81, s81, 0x20000\n\t"
        "buffer_load_dwordx4 v112, %[sk], s81 offen lds\n\t"
        "s_add_u32 m0, m0, 0x1000\n\t"
        "s_add_u32 s81, s81, 0x20000\n\t"
        "s_sub_u32 s82, s82, 1\n\t"
        "s_cmp_lg_u32 s82, 0\n\t"
        "s_cbranch_scc1 .Ld3%=\n\t"
        "s_waitcnt vmcnt(16)\n\t"
        "s_barrier\n\t"
        "ds_read_b64_tr_b16 v[32:33], v113 offset:0\n\t"
        "ds_read_b64_tr_b16 v[34:35], v113 offset:512\n\t"
        "ds_read_b64_tr_b16 v[36:37], v113 offset:8192\n\t"
        "ds_read_b64_tr_b16 v[38:39], v113 offset:8704\n\t"
        "ds_read_b64_tr_b16 v[40:41], v114 offset:0\n\t"
        "ds_read_b64_tr_b16 v[42:43], v114 offset:512\n\t"
        "ds_read_b64_tr_b16 v[44:45], v114 offset:8192\n\t"
        "ds_read_b64_tr_b16 v[46:47], v114 offset:8704\n\t"
        "ds_read_b64_tr_b16 a[144:145], v115 offset:0\n\t"
        "ds_read_b64_tr_b16 a[146:147], v115 offset:512\n\t"
        "ds_read_b64_tr_b16 a[148:149], v115 offset:8192\n\t"
        "ds_read_b64_tr_b16 a[150:151], v115 offset:8704\n\t"
        "ds_read_b64_tr_b16 a[152:153], v116 offset:0\n\t"
        "ds_read_b64_tr_b16 a[154:155], v116 offset:512\n\t"
        "ds_read_b64_tr_b16 a[156:157], v116 offset:8192\n\t"
        "ds_read_b64_tr_b16 a[158:159], v116 offset:8704\n\t"
        "ds_read_b64_tr_b16 a[160:161], v117 offset:0\n\t"
        "ds_read_b64_tr_b16 a[162:163], v117 offset:512\n\t"
        "ds_read_b64_tr_b16 a[164:165], v117 offset:8192\n\t"
        "ds_read_b64_tr_b16 a[166:167], v117 offset:8704\n\t"
        "ds_read_b64_tr_b16 a[168:169], v118 offset:0\n\t"
        "ds_read_b64_tr_b16 a[170:171], v118 offset:512\n\t"
        "ds_read_b64_tr_b16 a[172:173], v118 offset:8192\n\t"
        "ds_read_b64_tr_b16 a[174:175], v118 offset:8704\n\t"
        "ds_read_b64_tr_b16 a[176:177], v119 offset:0\n\t"
        "ds_read_b64_tr_b16 a[178:179], v119 offset:512\n\t"
        "ds_read_b64_tr_b16 a[180:181], v119 offset:8192\n\t"
        "ds_read_b64_tr_b16 a[182:183], v119 offset:8704\n\t"
        "ds_read_b64_tr_b16 a[184:185], v120 offset:0\n\t"
        "ds_read_b64_tr_b16 a[186:187], v120 offset:512\n\t"
        "ds_read_b64_tr_b16 a[188:189], v120 offset:8192\n\t"
        "ds_read_b64_tr_b16 a[190:191], v120 offset:8704\n\t"
        "s_waitcnt lgkmcnt(0)\n\t"
        "s_barrier\n\t"
        "s_add_u32 s80, %[m0b], 0\n\t"
        "s_mov_b32 s81, 4194304\n\t"
        "s_mov_b32 s82, 4\n\t"
        ".Ld4%=:\n\t"
        "s_mov_b32 m0, s80\n\t"
        "s_add_u32 s80, s80, 0x8000\n\t"
        "buffer_load_dwordx4 v112, %[sq], s81 offen lds\n\t"
        "s_add_u32 m0, m0, 0x1000\n\t"
        "s_add_u32 s81, s81, 0x20000\n\t"
        "buffer_load_dwordx4 v112, %[sq], s81 offen lds\n\t"
        "s_add_u32 m0, m0, 0x1000\n\t"
        "s_add_u32 s81, s81, 0x20000\n\t"
        "buffer_load_dwordx4 v112, %[sq], s81 offen lds\n\t"
        "s_add_u32 m0, m0, 0x1000\n\t"
        "s_add_u32 s81, s81, 0x20000\n\t"
        "buffer_load_dwordx4 v112, %[sq], s81 offen lds\n\t"
        "s_add_u32 m0, m0, 0x1000\n\t"
        "s_add_u32 s81, s81, 0x20000\n\t"
        "s_sub_u32 s82, s82, 1\n\t"
        "s_cmp_lg_u32 s82, 0\n\t"
        "s_cbranch_scc1 .Ld4%=\n\t"
        "s_waitcnt vmcnt(16)\n\t"
        "s_barrier\n\t"
        "ds_read_b64_tr_b16 v[48:49], v113 offset:16384\n\t"
        "ds_read_b64_tr_b16 v[50:51], v113 offset:16896\n\t"
        "ds_read_b64_tr_b16 v[52:53], v113 offset:24576\n\t"
        "ds_read_b64_tr_b16 v[54:55], v113 offset:25088\n\t"
        "ds_read_b64_tr_b16 v[56:57], v114 offset:16384\n\t"
        "ds_read_b64_tr_b16 v[58:59], v114 offset:16896\n\t"
        "ds_read_b64_tr_b16 v[60:61], v114 offset:24576\n\t"
        "ds_read_b64_tr_b16 v[62:63], v114 offset:25088\n\t"
        "ds_read_b64_tr_b16 a[208:209], v115 offset:16384\n\t"
        "ds_read_b64_tr_b16 a[210:211], v115 offset:16896\n\t"
        "ds_read_b64_tr_b16 a[212:213], v115 offset:24576\n\t"
        "ds_read_b64_tr_b16 a[214:215], v115 offset:25088\n\t"
        "ds_read_b64_tr_b16 a[216:217], v116 offset:16384\n\t"
        "ds_read_b64_tr_b16 a[218:219], v116 offset:16896\n\t"
        "ds_read_b64_tr_b16 a[220:221], v116 offset:24576\n\t"
        "ds_read_b64_tr_b16 a[222:223], v116 offset:25088\n\t"
        "ds_read_b64_tr_b16 a[224:225], v117 offset:16384\n\t"
        "ds_read_b64_tr_b16 a[226:227], v117 offset:16896\n\t"
        "ds_read_b64_tr_b16 a[228:229], v117 offset:24576\n\t"
        "ds_read_b64_tr_b16 a[230:231], v117 offset:25088\n\t"
        "ds_read_b64_tr_b16 a[232:233], v118 offset:16384\n\t"
        "ds_read_b64_tr_b16 a[234:235], v118 offset:16896\n\t"
        "ds_read_b64_tr_b16 a[236:237], v118 offset:24576\n\t"
        "ds_read_b64_tr_b16 a[238:239], v118 offset:25088\n\t"
        "ds_read_b64_tr_b16 a[240:241], v119 offset:16384\n\t"
        "ds_read_b64_tr_b16 a[242:243], v119 offset:16896\n\t"
        "ds_read_b64_tr_b16 a[244:245], v119 offset:24576\n\t"
        "ds_read_b64_tr_b16 a[246:247], v119 offset:25088\n\t"
        "ds_read_b64_tr_b16 a[248:249], v120 offset:16384\n\t"
        "ds_read_b64_tr_b16 a[250:251], v120 offset:16896\n\t"
        "ds_read_b64_tr_b16 a[252:253], v120 offset:24576\n\t"
        "ds_read_b64_tr_b16 a[254:255], v120 offset:25088\n\t"
        "s_waitcnt lgkmcnt(0)\n\t"
        "s_barrier\n\t"
        "s_add_u32 s80, %[m0b], 16384\n\t"
        "s_mov_b32 s81, 4194304\n\t"
        "s_mov_b32 s82, 4\n\t"
        ".Ld5%=:\n\t"
        "s_mov_b32 m0, s80\n\t"
        "s_add_u32 s80, s80, 0x8000\n\t"
        "buffer_load_dwordx4 v112, %[sk], s81 offen lds\n\t"
        "s_add_u32 m0, m0, 0x1000\n\t"
        "s_add_u32 s81, s81, 0x20000\n\t"
        "buffer_load_dwordx4 v112, %[sk], s81 offen lds\n\t"
        "s_add_u32 m0, m0, 0x1000\n\t"
        "s_add_u32 s81, s81, 0x20000\n\t"
        "buffer_load_dwordx4 v112, %[sk], s81 offen lds\n\t"
        "s_add_u32 m0, m0, 0x1000\n\t"
        "s_add_u32 s81, s81, 0x20000\n\t"
        "buffer_load_dwordx4 v112, %[sk], s81 offen lds\n\t"
        "s_add_u32 m0, m0, 0x1000\n\t"
        "s_add_u32 s81, s81, 0x20000\n\t"
        "s_sub_u32 s82, s82, 1\n\t"
        "s_cmp_lg_u32 s82, 0\n\t"
        "s_cbranch_scc1 .Ld5%=\n\t"
        "s_waitcnt vmcnt(16)\n\t"
        "s_barrier\n\t"
        "ds_read_b64_tr_b16 v[128:129], v113 offset:0\n\t"
        "ds_read_b64_tr_b16 v[130:131], v113 offset:512\n\t"
        "ds_read_b64_tr_b16 v[132:133], v113 offset:8192\n\t"
        "ds_read_b64_tr_b16 v[134:135], v113 offset:8704\n\t"
        "ds_read_b64_tr_b16 v[136:137], v114 offset:0\n\t"
        "ds_read_b64_tr_b16 v[138:139], v114 offset:512\n\t"
        "ds_read_b64_tr_b16 v[140:141], v114 offset:8192\n\t"
        "ds_read_b64_tr_b16 v[142:143], v114 offset:8704\n\t"
        "ds_read_b64_tr_b16 v[144:145], v115 offset:0\n\t"
        "ds_read_b64_tr_b16 v[146:147], v115 offset:512\n\t"
        "ds_read_b64_tr_b16 v[148:149], v115 offset:8192\n\t"
        "ds_read_b64_tr_b16 v[150:151], v115 offset:8704\n\t"
        "ds_read_b64_tr_b16 v[152:153], v116 offset:0\n\t"
        "ds_read_b64_tr_b16 v[154:155], v116 offset:512\n\t"
        "ds_read_b64_tr_b16 v[156:157], v116 offset:8192\n\t"
        "ds_read_b64_tr_b16 v[158:159], v116 offset:8704\n\t"
        "ds_read_b64_tr_b16 v[160:161], v117 offset:0\n\t"
        "ds_read_b64_tr_b16 v[162:163], v117 offset:512\n\t"
        "ds_read_b64_tr_b16 v[164:165], v117 offset:8192\n\t"
        "ds_read_b64_tr_b16 v[166:167], v117 offset:8704\n\t"
        "ds_read_b64_tr_b16 v[168:169], v118 offset:0\n\t"
        "ds_read_b64_tr_b16 v[170:171], v118 offset:512\n\t"
        "ds_read_b64_tr_b16 v[172:173], v118 offset:8192\n\t"
        "ds_read_b64_tr_b16 v[174:175], v118 offset:8704\n\t"
        "ds_read_b64_tr_b16 v[176:177], v119 offset:0\n\t"
        "ds_read_b64_tr_b16 v[178:179], v119 offset:512\n\t"
        "ds_read_b64_tr_b16 v[180:181], v119 offset:8192\n\t"
        "ds_read_b64_tr_b16 v[182:183], v119 offset:8704\n\t"
        "ds_read_b64_tr_b16 v[184:185], v120 offset:0\n\t"
        "ds_read_b64_tr_b16 v[186:187], v120 offset:512\n\t"
        "ds_read_b64_tr_b16 v[188:189], v120 offset:8192\n\t"
        "ds_read_b64_tr_b16 v[190:191], v120 offset:8704\n\t"
        "s_waitcnt lgkmcnt(0)\n\t"
        "s_barrier\n\t"
        "s_add_u32 s80, %[m0b], 0\n\t"
        "s_mov_b32 s81, 6291456\n\t"
        "s_mov_b32 s82, 4\n\t"
        ".Ld6%=:\n\t"
        "s_mov_b32 m0, s80\n\t"
        "s_add_u32 s80, s80, 0x8000\n\t"
        "buffer_load_dwordx4 v112, %[sq], s81 offen lds\n\t"
        "s_add_u32 m0, m0, 0x1000\n\t"
        "s_add_u32 s81, s81, 0x20000\n\t"
        "buffer_load_dwordx4 v112, %[sq], s81 offen lds\n\t"
        "s_add_u32 m0, m0, 0x1000\n\t"
        "s_add_u32 s81, s81, 0x20000\n\t"
        "buffer_load_dwordx4 v112, %[sq], s81 offen lds\n\t"
        "s_add_u32 m0, m0, 0x1000\n\t"
        "s_add_u32 s81, s81, 0x20000\n\t"
        "buffer_load_dwordx4 v112, %[sq], s81 offen lds\n\t"
        "s_add_u32 m0, m0, 0x1000\n\t"
        "s_add_u32 s81, s81, 0x20000\n\t"
        "s_sub_u32 s82, s82, 1\n\t"
        "s_cmp_lg_u32 s82, 0\n\t"
        "s_cbranch_scc1 .Ld6%=\n\t"
        "s_waitcnt vmcnt(16)\n\t"
        "s_barrier\n\t"
        "ds_read_b64_tr_b16 v[192:193], v113 offset:16384\n\t"
        "ds_read_b64_tr_b16 v[194:195], v113 offset:16896\n\t"
        "ds_read_b64_tr_b16 v[196:197], v113 offset:24576\n\t"
        "ds_read_b64_tr_b16 v[198:199], v113 offset:25088\n\t"
        "ds_read_b64_tr_b16 v[200:201], v114 offset:16384\n\t"
        "ds_read_b64_tr_b16 v[202:203], v114 offset:16896\n\t"
        "ds_read_b64_tr_b16 v[204:205], v114 offset:24576\n\t"
        "ds_read_b64_tr_b16 v[206:207], v114 offset:25088\n\t"
        "ds_read_b64_tr_b16 v[208:209], v115 offset:16384\n\t"
        "ds_read_b64_tr_b16 v[210:211], v115 offset:16896\n\t"
        "ds_read_b64_tr_b16 v[212:213], v115 offset:24576\n\t"
        "ds_read_b64_tr_b16 v[214:215], v115 offset:25088\n\t"
        "ds_read_b64_tr_b16 v[216:217], v116 offset:16384\n\t"
        "ds_read_b64_tr_b16 v[218:219], v116 offset:16896\n\t"
        "ds_read_b64_tr_b16 v[220:221], v116 offset:24576\n\t"
        "ds_read_b64_tr_b16 v[222:223], v116 offset:25088\n\t"
        "ds_read_b64_tr_b16 v[224:225], v117 offset:16384\n\t"
        "ds_read_b64_tr_b16 v[226:227], v117 offset:16896\n\t"
        "ds_read_b64_tr_b16 v[228:229], v117 offset:24576\n\t"
        "ds_read_b64_tr_b16 v[230:231], v117 offset:25088\n\t"
        "ds_read_b64_tr_b16 v[232:233], v118 offset:16384\n\t"
        "ds_read_b64_tr_b16 v[234:235], v118 offset:16896\n\t"
        "ds_read_b64_tr_b16 v[236:237], v118 offset:24576\n\t"
        "ds_read_b64_tr_b16 v[238:239], v118 offset:25088\n\t"
        "ds_read_b64_tr_b16 v[240:241], v119 offset:16384\n\t"
        "ds_read_b64_tr_b16 v[242:243], v119 offset:16896\n\t"
        "ds_read_b64_tr_b16 v[244:245], v119 offset:24576\n\t"
        "ds_read_b64_tr_b16 v[246:247], v119 offset:25088\n\t"
        "ds_read_b64_tr_b16 v[248:249], v120 offset:16384\n\t"
        "ds_read_b64_tr_b16 v[250:251], v120 offset:16896\n\t"
        "ds_read_b64_tr_b16 v[252:253], v120 offset:24576\n\t"
        "ds_read_b64_tr_b16 v[254:255], v120 offset:25088\n\t"
        "s_waitcnt lgkmcnt(0)\n\t"
        "s_barrier\n\t"
        "s_add_u32 s80, %[m0b], 16384\n\t"
        "s_mov_b32 s81, 6291456\n\t"
        "s_mov_b32 s82, 4\n\t"
        ".Ld7%=:\n\t"
        "s_mov_b32 m0, s80\n\t"
        "s_add_u32 s80, s80, 0x8000\n\t"
        "buffer_load_dwordx4 v112, %[sk], s81 offen lds\n\t"
        "s_add_u32 m0, m0, 0x1000\n\t"
        "s_add_u32 s81, s81, 0x20000\n\t"
        "buffer_load_dwordx4 v112, %[sk], s81 offen lds\n\t"
        "s_add_u32 m0, m0, 0x1000\n\t"
        "s_add_u32 s81, s81, 0x20000\n\t"
        "buffer_load_dwordx4 v112, %[sk], s81 offen lds\n\t"
        "s_add_u32 m0, m0, 0x1000\n\t"
        "s_add_u32 s81, s81, 0x20000\n\t"
        "buffer_load_dwordx4 v112, %[sk], s81 offen lds\n\t"
        "s_add_u32 m0, m0, 0x1000\n\t"
        "s_add_u32 s81, s81, 0x20000\n\t"
        "s_sub_u32 s82, s82, 1\n\t"
        "s_cmp_lg_u32 s82, 0\n\t"
        "s_cbranch_scc1 .Ld7%=\n\t"
        "s_waitcnt vmcnt(16)\n\t"
        "s_barrier\n\t"
        "ds_read_b64_tr_b16 a[0:1], v113 offset:0\n\t"
        "ds_read_b64_tr_b16 a[2:3], v113 offset:512\n\t"
        "ds_read_b64_tr_b16 a[4:5], v113 offset:8192\n\t"
        "ds_read_b64_tr_b16 a[6:7], v113 offset:8704\n\t"
        "ds_read_b64_tr_b16 a[8:9], v114 offset:0\n\t"
        "ds_read_b64_tr_b16 a[10:11], v114 offset:512\n\t"
        "ds_read_b64_tr_b16 a[12:13], v114 offset:8192\n\t"
        "ds_read_b64_tr_b16 a[14:15], v114 offset:8704\n\t"
        "ds_read_b64_tr_b16 v[64:65], v115 offset:0\n\t"
        "ds_read_b64_tr_b16 v[66:67], v115 offset:512\n\t"
        "ds_read_b64_tr_b16 v[68:69], v115 offset:8192\n\t"
        "ds_read_b64_tr_b16 v[70:71], v115 offset:8704\n\t"
        "ds_read_b64_tr_b16 v[72:73], v116 offset:0\n\t"
        "ds_read_b64_tr_b16 v[74:75], v116 offset:512\n\t"
        "ds_read_b64_tr_b16 v[76:77], v116 offset:8192\n\t"
        "ds_read_b64_tr_b16 v[78:79], v116 offset:8704\n\t"
        "ds_read_b64_tr_b16 v[80:81], v117 offset:0\n\t"
        "ds_read_b64_tr_b16 v[82:83], v117 offset:512\n\t"
        "ds_read_b64_tr_b16 v[84:85], v117 offset:8192\n\t"
        "ds_read_b64_tr_b16 v[86:87], v117 offset:8704\n\t"
        "ds_read_b64_tr_b16 v[88:89], v118 offset:0\n\t"
        "ds_read_b64_tr_b16 v[90:91], v118 offset:512\n\t"
        "ds_read_b64_tr_b16 v[92:93], v118 offset:8192\n\t"
        "ds_read_b64_tr_b16 v[94:95], v118 offset:8704\n\t"
        "ds_read_b64_tr_b16 a[128:129], v119 offset:0\n\t"
        "ds_read_b64_tr_b16 a[130:131], v119 offset:512\n\t"
        "ds_read_b64_tr_b16 a[132:133], v119 offset:8192\n\t"
        "ds_read_b64_tr_b16 a[134:135], v119 offset:8704\n\t"
        "ds_read_b64_tr_b16 a[136:137], v120 offset:0\n\t"
        "ds_read_b64_tr_b16 a[138:139], v120 offset:512\n\t"
        "ds_read_b64_tr_b16 a[140:141], v120 offset:8192\n\t"
        "ds_read_b64_tr_b16 a[142:143], v120 offset:8704\n\t"
        "s_waitcnt lgkmcnt(0)\n\t"
        "s_barrier\n\t"
        "ds_write_b128 v124, v[0:3] offset:0\n\t"
        "ds_write_b128 v124, v[4:7] offset:1024\n\t"
        "ds_write_b128 v124, v[8:11] offset:2048\n\t"
        "ds_write_b128 v124, v[12:15] offset:3072\n\t"
        "ds_write_b128 v124, v[16:19] offset:4096\n\t"
        "ds_write_b128 v124, v[20:23] offset:5120\n\t"
        "ds_write_b128 v124, v[24:27] offset:6144\n\t"
        "ds_write_b128 v124, v[28:31] offset:7168\n\t"
        "ds_write_b128 v124, v[32:35] offset:8192\n\t"
        "ds_write_b128 v124, v[36:39] offset:9216\n\t"
        "ds_write_b128 v124, v[40:43] offset:10240\n\t"
        "ds_write_b128 v124, v[44:47] offset:11264\n\t"
        "ds_write_b128 v124, v[48:51] offset:12288\n\t"
        "ds_write_b128 v124, v[52:55] offset:13312\n\t"
        "ds_write_b128 v124, v[56:59] offset:14336\n\t"
        "ds_write_b128 v124, v[60:63] offset:15360\n\t"
        "s_waitcnt lgkmcnt(0)\n\t"
        "s_waitcnt vmcnt(0)\n\t"
        "s_barrier\n\t"
        "ds_read_b64_tr_b16 a[64:65], v113 offset:16384\n\t"
        "ds_read_b64_tr_b16 a[66:67], v113 offset:16896\n\t"
        "ds_read_b64_tr_b16 a[68:69], v113 offset:24576\n\t"
        "ds_read_b64_tr_b16 a[70:71], v113 offset:25088\n\t"
        "ds_read_b64_tr_b16 a[72:73], v114 offset:16384\n\t"
        "ds_read_b64_tr_b16 a[74:75], v114 offset:16896\n\t"
        "ds_read_b64_tr_b16 a[76:77], v114 offset:24576\n\t"
        "ds_read_b64_tr_b16 a[78:79], v114 offset:25088\n\t"
        "ds_read_b64_tr_b16 v[0:1], v115 offset:16384\n\t"
        "ds_read_b64_tr_b16 v[2:3], v115 offset:16896\n\t"
        "ds_read_b64_tr_b16 v[4:5], v115 offset:24576\n\t"
        "ds_read_b64_tr_b16 v[6:7], v115 offset:25088\n\t"
        "ds_read_b64_tr_b16 v[8:9], v116 offset:16384\n\t"
        "ds_read_b64_tr_b16 v[10:11], v116 offset:16896\n\t"
        "ds_read_b64_tr_b16 v[12:13], v116 offset:24576\n\t"
        "ds_read_b64_tr_b16 v[14:15], v116 offset:25088\n\t"
        "ds_read_b64_tr_b16 v[16:17], v117 offset:16384\n\t"
        "ds_read_b64_tr_b16 v[18:19], v117 offset:16896\n\t"
        "ds_read_b64_tr_b16 v[20:21], v117 offset:24576\n\t"
        "ds_read_b64_tr_b16 v[22:23], v117 offset:25088\n\t"
        "ds_read_b64_tr_b16 v[24:25], v118 offset:16384\n\t"
        "ds_read_b64_tr_b16 v[26:27], v118 offset:16896\n\t"
        "ds_read_b64_tr_b16 v[28:29], v118 offset:24576\n\t"
        "ds_read_b64_tr_b16 v[30:31], v118 offset:25088\n\t"
        "ds_read_b64_tr_b16 a[192:193], v119 offset:16384\n\t"
        "ds_read_b64_tr_b16 a[194:195], v119 offset:16896\n\t"
        "ds_read_b64_tr_b16 a[196:197], v119 offset:24576\n\t"
        "ds_read_b64_tr_b16 a[198:199], v119 offset:25088\n\t"
        "ds_read_b64_tr_b16 a[200:201], v120 offset:16384\n\t"
        "ds_read_b64_tr_b16 a[202:203], v120 offset:16896\n\t"
        "ds_read_b64_tr_b16 a[204:205], v120 offset:24576\n\t"
        "ds_read_b64_tr_b16 a[206:207], v120 offset:25088\n\t"
        "s_waitcnt lgkmcnt(0)\n\t"
        "s_barrier\n\t"
        "ds_write_b128 v124, v[64:67] offset:16384\n\t"
        "ds_write_b128 v124, v[68:71] offset:17408\n\t"
        "ds_write_b128 v124, v[72:75] offset:18432\n\t"
        "ds_write_b128 v124, v[76:79] offset:19456\n\t"
        "ds_write_b128 v124, v[80:83] offset:20480\n\t"
        "ds_write_b128 v124, v[84:87] offset:21504\n\t"
        "ds_write_b128 v124, v[88:91] offset:22528\n\t"
        "ds_write_b128 v124, v[92:95] offset:23552\n\t"
        "ds_write_b128 v124, v[0:3] offset:24576\n\t"
        "ds_write_b128 v124, v[4:7] offset:25600\n\t"
        "ds_write_b128 v124, v[8:11] offset:26624\n\t"
        "ds_write_b128 v124, v[12:15] offset:27648\n\t"
        "ds_write_b128 v124, v[16:19] offset:28672\n\t"
        "ds_write_b128 v124, v[20:23] offset:29696\n\t"
        "ds_write_b128 v124, v[24:27] offset:30720\n\t"
        "ds_write_b128 v124, v[28:31] offset:31744\n\t"
        "s_waitcnt lgkmcnt(0)"
        : "={a[0:31]}"(wa[0]), "={a[32:63]}"(wa[1]), "={a[64:95]}"(wa[2]), "={a[96:127]}"(wa[3]), "={a[128:159]}"(wa[4]), "={a[160:191]}"(wa[5]), "={a[192:223]}"(wa[6]), "={a[224:255]}"(wa[7]), "={v[128:159]}"(wg[0]), "={v[160:191]}"(wg[1]), "={v[192:223]}"(wg[2]), "={v[224:255]}"(wg[3])
        : [sq] "s"(sq), [sk] "s"(sk), [wsd] "s"(wsd), [m0b] "s"(m0b), "{v[112:127]}"(pa)
        : "memory", "scc", "m0", "s80", "s81", "s82", "v0", "v1", "v2", "v3", "v4", "v5", "v6", "v7", "v8", "v9", "v10", "v11", "v12", "v13", "v14", "v15", "v16", "v17", "v18", "v19", "v20", "v21", "v22", "v23", "v24", "v25", "v26", "v27", "v28", "v29", "v30", "v31", "v32", "v33", "v34", "v35", "v36", "v37", "v38", "v39", "v40", "v41", "v42", "v43", "v44", "v45", "v46", "v47", "v48", "v49", "v50", "v51", "v52", "v53", "v54", "v55", "v56", "v57", "v58", "v59", "v60", "v61", "v62", "v63", "v64", "v65", "v66", "v67", "v68", "v69", "v70", "v71", "v72", "v73", "v74", "v75", "v76", "v77", "v78", "v79", "v80", "v81", "v82", "v83", "v84", "v85", "v86", "v87", "v88", "v89", "v90", "v91", "v92", "v93", "v94", "v95");
        }
        asm volatile("" : "+v"(cpf));
        tid = otid(); l = tid & 63; n16 = l & 15; g = l >> 4;
        unsigned nm; if (!votes(nm)) return false;
        const unsigned nW = (nm & 1u) << 13, nA = nW, nS = nW + 6 * 16384 - w * 1024, nC = 32 * 16384 + ((nm & 2u) << 12);
        const unsigned b0 = (64 + (h >> 2)) * 16384 + ((nm & 4u) << 11) + (h & 3) * 2048 - w * 256 - L_QK, b1 = b0 + 8 * 16384;
        const unsigned pob = w == 0 ? nA + (i32 >> 4) * 16384 + (i32 & 15) * 512 : (w == 2 && i32 == 0) ? nS : OOB;
        const unsigned kx = W_KX + h * 8192, vx = W_VF + h * 8192 + w * 1024, kf = F_KVQ + h * 64, swo = 6 * 16384 + h * 8;
        const unsigned aq = img(8 * g + (n16 >> 2)) + (n16 & 1) * 8;
        v8u pc = {(unsigned)tid * 16, (unsigned)(L_W + w * 32768 + l * 16), (n16 & 2) ? 0xEEEEu : 0x4444u, aq,
                  (unsigned)(((w + 1) & 3) * 1024 + l * 16), (unsigned)(((w + 2) & 3) * 1024 + l * 16), (unsigned)(((w + 3) & 3) * 1024 + l * 16), img(32 * w + n16) + g * 4};
        v2u pd = {(n16 & 2) ? (unsigned)L_Z : aq + w * 768, (n16 & 2) ? aq + w * 768 : (unsigned)L_Z};
        *(__attribute__((address_space(3))) v4u*)(size_t)(L_TAB + tid * 16) = (v4u){(unsigned)(L_QK + 3072 + g * 384 + n16 * 8),
                  n16 < 8 ? (unsigned)(((n16 >> 2) * 4 + (h >> 3)) * 16384 + (h & 7) * 1024 + w * 256 + (g >> 1) * 128 + (n16 & 2) * 32 + (g & 1) * 32 + (n16 & 1) * 16) : OOB,
                  (unsigned)((32 * w + 16 * (g & 1) + n16) * 16 + (g >> 1) * 8), (unsigned)(L_QK + 3072 + w * 768 + ((n16 & 1) + 2 * (n16 >> 2)) * 32 + g * 8 + (n16 & 2) * 2)};
        if (tid < 64) ((lu32*)(size_t)L_FL)[tid] = 0u;
    asm volatile(
        "rec_s_code:\n\t"
        "s_mov_b32 s36, 0xf0000000\n\t"
        "s_mov_b32 s38, 0xff00ff00\n\t"
        "s_mov_b32 s39, 0xff00ff00\n\t"
        "s_mov_b32 s40, 0\n\t"
        "s_mov_b32 s37, 0\n\t"
        "s_brev_b32 s54, 1\n\t"
        "s_mov_b32 s46, %c[iD1]\n\t"
        "s_mov_b32 s45, %c[iD1]\n\t"
        "s_mov_b32 s47, %c[iD2]\n\t"
        "s_mov_b32 s48, 1\n\t"
        "s_mov_b32 s50, %c[LC]\n\t"
        "s_mov_b32 s51, %c[LA]\n\t"
        "s_mov_b32 s52, %c[E1]\n\t"
        "s_mov_b32 s53, %c[E2]\n\t"
        "s_mov_b32 s43, 0\n\t"
        "s_mov_b32 s61, %[b0]\n\t"
        "s_mov_b32 s62, %[b1]\n\t"
        "s_mov_b32 s75, 31744\n\t"
        "s_and_b32 s80, s40, 3\n\t"
        "s_lshl_b32 s80, s80, 17\n\t"
        "s_add_u32 s57, s80, %[nA]\n\t"
        "s_add_u32 s58, s57, 0x1000\n\t"
        "s_add_u32 s59, s57, 0x4000\n\t"
        "s_add_u32 s60, s57, 0x5000\n\t"
        "s_and_b32 s80, s40, 3\n\t"
        "s_lshl_b32 s80, s80, 17\n\t"
        "s_add_u32 s63, s80, %[nC]\n\t"
        "s_add_u32 s66, s80, %[nS]\n\t"
        "s_add_u32 s81, s80, 0x20000\n\t"
        "s_and_b32 s81, s81, 0x60000\n\t"
        "s_add_u32 s67, s81, %[nW]\n\t"
        "s_cmp_eq_u32 %[w], 2\n\t"
        "s_cselect_b32 s81, s80, s81\n\t"
        "s_xor_b32 s81, s81, 0x40000\n\t"
        "s_add_u32 s68, s81, %[pob]\n\t"
        "s_lshl_b32 s72, s40, 17\n\t"
        "s_add_u32 s72, s72, %[kvo]\n\t"
        "s_mov_b32 s64, %[kf]\n\t"
        "buffer_load_dwordx2 v[48:49], off, %[wsd], s64 sc1\n\t"
        "s_waitcnt vmcnt(0)\n\t"
        "s_cmp_eq_u32 s64, s36\n\t"
        "s_cbranch_scc1 .Lk10%=\n\t"
        "s_mov_b32 s97, 0\n\t"
        ".Lk00%=:\n\t"
        "v_readfirstlane_b32 s82, v48\n\t"
        "v_readfirstlane_b32 s83, v49\n\t"
        "s_xor_b32 s82, s82, %[tag]\n\t"
        "s_xor_b32 s83, s83, %[tag]\n\t"
        "s_or_b32 s82, s82, s83\n\t"
        "s_cbranch_scc0 .Lk10%=\n\t"
        "s_sleep 3\n\t"
        "buffer_load_dwordx2 v[48:49], off, %[wsd], s64 sc1\n\t"
        "s_add_u32 s97, s97, 1\n\t"
        "s_cmp_lt_u32 s97, 0x200000\n\t"
        "s_cbranch_scc0 .Lgu%=\n\t"
        "s_waitcnt vmcnt(0)\n\t"
        "s_branch .Lk00%=\n\t"
        ".Lk10%=:\n\t"
        "s_add_u32 s80, s40, 0\n\t"
        "s_min_u32 s80, s80, 63\n\t"
        "s_lshl_b32 s80, s80, 18\n\t"
        "s_add_u32 s69, s80, %[kx]\n\t"
        "s_add_u32 s70, s69, 0x1000\n\t"
        "s_lshl_b32 s80, s40, 18\n\t"
        "s_add_u32 s71, s80, %[vx]\n\t"
        "s_add_u32 s80, s40, 1\n\t"
        "s_lshl_b32 s81, s80, 9\n\t"
        "s_add_u32 s81, s81, %[kf]\n\t"
        "s_and_b32 s80, s80, 0x43\n\t"
        "s_cselect_b32 s64, s36, s81\n\t"
        "buffer_load_dwordx4 v[104:107], v120, %[wsd], s69 offen sc1\n\t"
        "buffer_load_dwordx4 v[108:111], v120, %[wsd], s70 offen sc1\n\t"
        "ds_read_b128 v[16:19], v121 offset:0\n\t"
        "ds_read_b128 v[20:23], v121 offset:1024\n\t"
        "ds_read_b128 v[24:27], v121 offset:2048\n\t"
        "ds_read_b128 v[28:31], v121 offset:3072\n\t"
        "ds_read_b128 v[32:35], v121 offset:4096\n\t"
        "ds_read_b128 v[36:39], v121 offset:5120\n\t"
        "ds_read_b128 v[40:43], v121 offset:6144\n\t"
        "ds_read_b128 v[44:47], v121 offset:7168\n\t"
        "ds_read_b128 v[56:59], v121 offset:8192\n\t"
        "ds_read_b128 v[60:63], v121 offset:9216\n\t"
        "ds_read_b128 v[64:67], v121 offset:10240\n\t"
        "ds_read_b128 v[68:71], v121 offset:11264\n\t"
        "ds_read_b128 v[72:75], v121 offset:12288\n\t"
        "ds_read_b128 v[76:79], v121 offset:13312\n\t"
        "ds_read_b128 v[80:83], v121 offset:14336\n\t"
        "ds_read_b128 v[84:87], v121 offset:15360\n\t"
        "s_add_u32 s80, s40, 1\n\t"
        "s_min_u32 s80, s80, 63\n\t"
        "s_lshl_b32 s80, s80, 18\n\t"
        "s_add_u32 s69, s80, %[kx]\n\t"
        "s_add_u32 s70, s69, 0x1000\n\t"
        "s_lshl_b32 s80, s40, 18\n\t"
        "s_add_u32 s71, s80, %[vx]\n\t"
        "s_add_u32 s80, s40, 2\n\t"
        "s_lshl_b32 s81, s80, 9\n\t"
        "s_add_u32 s81, s81, %[kf]\n\t"
        "s_and_b32 s80, s80, 0x43\n\t"
        "s_cselect_b32 s64, s36, s81\n\t"
        "v_mov_b32_e32 v88, 0\n\t"
        "v_mov_b32_e32 v89, 0\n\t"
        "v_mov_b32_e32 v90, 0\n\t"
        "v_mov_b32_e32 v91, 0\n\t"
        "v_mov_b32_e32 v92, 0\n\t"
        "v_mov_b32_e32 v93, 0\n\t"
        "v_mov_b32_e32 v94, 0\n\t"
        "v_mov_b32_e32 v95, 0\n\t"
        "v_mov_b32_e32 v96, 0\n\t"
        "v_mov_b32_e32 v97, 0\n\t"
        "v_mov_b32_e32 v98, 0\n\t"
        "v_mov_b32_e32 v99, 0\n\t"
        "v_mov_b32_e32 v100, 0\n\t"
        "v_mov_b32_e32 v101, 0\n\t"
        "v_mov_b32_e32 v102, 0\n\t"
        "v_mov_b32_e32 v103, 0\n\t"
        "s_waitcnt vmcnt(0) lgkmcnt(0)\n\t"
        "s_barrier\n\t"
        "s_memrealtime s[98:99]\n\t"
        "v_mov_b32_e32 v49, 0\n\t"
        "v_lshlrev_b32_e32 v51, 6, v120\n\t"
        "s_add_u32 s80, %[nW], %[swo]\n\t"
        "s_cmp_eq_u32 %[w], 0\n\t"
        "s_cselect_b32 s73, s80, s36\n\t"
        "s_cselect_b32 s74, %[pra], s36\n\t"
        "s_waitcnt lgkmcnt(0)\n\t"
        "s_mov_b32 s41, s98\n\t"
        "v_mov_b32_e32 v48, s98\n\t"
        ".Lloop%=:\n\t"
        "s_mov_b32 s97, 0\n\t"
        "s_movk_i32 s96, 0x1000\n\t"
        ".Lwa%=:\n\t"
        "s_memrealtime s[98:99]\n\t"
        "s_addk_i32 s96, -1\n\t"
        "s_cmp_lt_i32 s96, 0\n\t"
        "s_cbranch_scc1 .Lta%=\n\t"
        "s_waitcnt lgkmcnt(0)\n\t"
        "s_sub_i32 s98, s98, s41\n\t"
        "s_cmp_lt_i32 s98, %c[iFT]\n\t"
        "s_cbranch_scc1 .Lwa%=\n\t"
        "s_cmp_ge_i32 s98, 0\n\t"
        "s_cbranch_scc1 .Lxa%=\n\t"
        ".Lqa%=:\n\t"
        "s_nop %c[iFN]\n\t"
        "s_addk_i32 s98, 1\n\t"
        "s_cmp_lt_i32 s98, 0\n\t"
        "s_cbranch_scc1 .Lqa%=\n\t"
        ".Lxa%=:\n\t"
        "buffer_load_dwordx4 v[0:3], v120, %[srd], s57 offen sc1\n\t"
        "s_nop 7\n\t"
        "buffer_load_dwordx4 v[4:7], v120, %[srd], s58 offen sc1\n\t"
        "s_nop 7\n\t"
        "buffer_load_dwordx4 v[8:11], v120, %[srd], s59 offen sc1\n\t"
        "s_nop 7\n\t"
        "buffer_load_dwordx4 v[12:15], v120, %[srd], s60 offen sc1\n\t"
        "s_nop 15\n\t"
        "s_nop 15\n\t"
        "s_nop 3\n\t"
        "s_mov_b64 exec, 7\n\t"
        "buffer_store_dwordx2 v[48:49], v51, %[srd], s73 offen sc1\n\t"
        "s_mov_b64 exec, -1\n\t"
        "buffer_load_dwordx4 v[112:115], v120, %[wsd], s71 offen offset:0 sc1\n\t"
        "buffer_load_dwordx4 v[116:119], v120, %[wsd], s71 offen offset:1024 sc1\n\t"
        ".Lgo%=:\n\t"
        "s_waitcnt vmcnt(6)\n\t"
        "v_smfmac_f32_16x16x64_bf16 v[88:91], v[0:3], v[16:23], v122\n\t"
        "ds_write_b128 v120, v[0:3] offset:0\n\t"
        "v_smfmac_f32_16x16x64_bf16 v[92:95], v[0:3], v[24:31], v122\n\t"
        "v_smfmac_f32_16x16x64_bf16 v[96:99], v[0:3], v[32:39], v122\n\t"
        "v_smfmac_f32_16x16x64_bf16 v[100:103], v[0:3], v[40:47], v122\n\t"
        "s_waitcnt vmcnt(5)\n\t"
        "v_smfmac_f32_16x16x64_bf16 v[88:91], v[4:7], v[56:63], v122\n\t"
        "ds_write_b128 v120, v[4:7] offset:4096\n\t"
        "v_smfmac_f32_16x16x64_bf16 v[92:95], v[4:7], v[64:71], v122\n\t"
        "v_smfmac_f32_16x16x64_bf16 v[96:99], v[4:7], v[72:79], v122\n\t"
        "v_smfmac_f32_16x16x64_bf16 v[100:103], v[4:7], v[80:87], v122\n\t"
        "s_waitcnt vmcnt(4)\n\t"
        "v_smfmac_f32_16x16x64_bf16 v[88:91], v[8:11], v[128:135], v122\n\t"
        "ds_write_b128 v120, v[8:11] offset:8192\n\t"
        "v_smfmac_f32_16x16x64_bf16 v[92:95], v[8:11], v[136:143], v122\n\t"
        "s_waitcnt lgkmcnt(1)\n\t"
        "s_barrier\n\t"
        "v_smfmac_f32_16x16x64_bf16 v[96:99], v[8:11], v[192:199], v122\n\t"
        "ds_read_b128 v[16:19], v124 offset:0\n\t"
        "v_smfmac_f32_16x16x64_bf16 v[100:103], v[8:11], v[200:207], v122\n\t"
        "ds_read_b128 v[20:23], v125 offset:0\n\t"
        "s_waitcnt vmcnt(3)\n\t"
        "v_smfmac_f32_16x16x64_bf16 v[88:91], v[12:15], a[0:7], v122\n\t"
        "ds_write_b128 v120, v[12:15] offset:12288\n\t"
        "v_smfmac_f32_16x16x64_bf16 v[92:95], v[12:15], a[8:15], v122\n\t"
        "v_smfmac_f32_16x16x64_bf16 v[96:99], v[12:15], a[64:71], v122\n\t"
        "ds_read_b128 v[24:27], v126 offset:0\n\t"
        "v_smfmac_f32_16x16x64_bf16 v[100:103], v[12:15], a[72:79], v122\n\t"
        "ds_read_b128 v[28:31], v124 offset:4096\n\t"
        "s_waitcnt lgkmcnt(4)\n\t"
        "v_smfmac_f32_16x16x64_bf16 v[88:91], v[16:19], a[16:23], v122\n\t"
        "ds_read_b128 v[32:35], v125 offset:4096\n\t"
        "v_smfmac_f32_16x16x64_bf16 v[92:95], v[16:19], a[24:31], v122\n\t"
        "ds_read_b128 v[36:39], v126 offset:4096\n\t"
        "s_waitcnt lgkmcnt(5)\n\t"
        "v_smfmac_f32_16x16x64_bf16 v[88:91], v[20:23], a[32:39], v122\n\t"
        "ds_read_b128 v[72:75], v121 offset:16384\n\t"
        "v_smfmac_f32_16x16x64_bf16 v[92:95], v[20:23], a[40:47], v122\n\t"
        "s_waitcnt lgkmcnt(5)\n\t"
        "s_barrier\n\t"
        "ds_read_b128 v[76:79], v121 offset:17408\n\t"
        "s_waitcnt lgkmcnt(5)\n\t"
        "v_smfmac_f32_16x16x64_bf16 v[88:91], v[24:27], a[48:55], v122\n\t"
        "ds_read_b128 v[0:3], v124 offset:12288\n\t"
        "v_smfmac_f32_16x16x64_bf16 v[92:95], v[24:27], a[56:63], v122\n\t"
        "ds_read_b128 v[80:83], v121 offset:18432\n\t"
        "s_waitcnt lgkmcnt(6)\n\t"
        "v_smfmac_f32_16x16x64_bf16 v[88:91], v[28:31], a[144:151], v122\n\t"
        "ds_read_b128 v[84:87], v121 offset:19456\n\t"
        "v_smfmac_f32_16x16x64_bf16 v[92:95], v[28:31], a[152:159], v122\n\t"
        "ds_read_b128 v[40:43], v124 offset:8192\n\t"
        "s_waitcnt lgkmcnt(7)\n\t"
        "v_smfmac_f32_16x16x64_bf16 v[88:91], v[32:35], a[160:167], v122\n\t"
        "ds_read_b128 v[44:47], v125 offset:8192\n\t"
        "v_mov_b32_e32 v12, 0x3f803f80\n\t"
        "v_smfmac_f32_16x16x64_bf16 v[92:95], v[32:35], a[168:175], v122\n\t"
        "ds_read_b128 v[56:59], v121 offset:20480\n\t"
        "v_mov_b32_e32 v13, 0x3f803f80\n\t"
        "s_waitcnt lgkmcnt(8)\n\t"
        "v_smfmac_f32_16x16x64_bf16 v[88:91], v[36:39], a[176:183], v122\n\t"
        "ds_read_b128 v[60:63], v121 offset:21504\n\t"
        "v_mov_b32_e32 v14, 0x3f803f80\n\t"
        "v_smfmac_f32_16x16x64_bf16 v[92:95], v[36:39], a[184:191], v122\n\t"
        "ds_read_b128 v[4:7], v125 offset:12288\n\t"
        "v_mov_b32_e32 v15, 0x3f803f80\n\t"
        "s_waitcnt lgkmcnt(8)\n\t"
        "s_waitcnt lgkmcnt(7)\n\t"
        "v_smfmac_f32_16x16x64_bf16 v[88:91], v[0:3], v[72:79], v122\n\t"
        "ds_read_b128 v[64:67], v121 offset:22528\n\t"
        "s_waitcnt lgkmcnt(6)\n\t"
        "v_smfmac_f32_16x16x64_bf16 v[92:95], v[0:3], v[80:87], v122\n\t"
        "ds_read_b128 v[68:71], v121 offset:23552\n\t"
        "s_waitcnt lgkmcnt(6)\n\t"
        "v_smfmac_f32_16x16x64_bf16 v[88:91], v[40:43], v[144:151], v122\n\t"
        "ds_read_b128 v[48:51], v126 offset:8192\n\t"
        "v_smfmac_f32_16x16x64_bf16 v[92:95], v[40:43], v[152:159], v122\n\t"
        "ds_read_b128 v[8:11], v126 offset:12288\n\t"
        "s_waitcnt lgkmcnt(7)\n\t"
        "v_smfmac_f32_16x16x64_bf16 v[88:91], v[44:47], v[160:167], v122\n\t"
        "ds_read_b128 v[72:75], v121 offset:24576\n\t"
        "v_smfmac_f32_16x16x64_bf16 v[92:95], v[44:47], v[168:175], v122\n\t"
        "ds_read_b128 v[76:79], v121 offset:25600\n\t"
        "s_waitcnt lgkmcnt(7)\n\t"
        "s_waitcnt lgkmcnt(6)\n\t"
        "v_smfmac_f32_16x16x64_bf16 v[88:91], v[4:7], v[56:63], v122\n\t"
        "ds_read_b128 v[80:83], v121 offset:26624\n\t"
        "s_waitcnt lgkmcnt(5)\n\t"
        "v_smfmac_f32_16x16x64_bf16 v[92:95], v[4:7], v[64:71], v122\n\t"
        "ds_read_b128 v[84:87], v121 offset:27648\n\t"
        "s_waitcnt lgkmcnt(5)\n\t"
        "v_smfmac_f32_16x16x64_bf16 v[88:91], v[48:51], v[176:183], v122\n\t"
        "v_mov_b32_e32 v64, 0\n\t"
        "v_smfmac_f32_16x16x64_bf16 v[92:95], v[48:51], v[184:191], v122\n\t"
        "v_mov_b32_e32 v65, 0\n\t"
        "s_waitcnt lgkmcnt(4)\n\t"
        "v_smfmac_f32_16x16x64_bf16 v[88:91], v[8:11], a[128:135], v122\n\t"
        "v_mov_b32_e32 v66, 0\n\t"
        "v_smfmac_f32_16x16x64_bf16 v[92:95], v[8:11], a[136:143], v122\n\t"
        "v_mov_b32_e32 v67, 0\n\t"
        "s_waitcnt lgkmcnt(2)\n\t"
        "v_smfmac_f32_16x16x64_bf16 v[96:99], v[0:3], v[72:79], v122\n\t"
        "ds_read_b128 v[72:75], v121 offset:28672\n\t"
        "s_waitcnt lgkmcnt(1)\n\t"
        "v_smfmac_f32_16x16x64_bf16 v[100:103], v[0:3], v[80:87], v122\n\t"
        "ds_read_b128 v[76:79], v121 offset:29696\n\t"
        "v_smfmac_f32_16x16x64_bf16 v[96:99], v[16:19], a[80:87], v122\n\t"
        "v_add_f32_e32 v88, v88, v90\n\t"
        "v_add_f32_e32 v89, v89, v91\n\t"
        "v_smfmac_f32_16x16x64_bf16 v[100:103], v[16:19], a[88:95], v122\n\t"
        "v_cvt_pk_bf16_f32 v90, v88, v89\n\t"
        "v_add_f32_e32 v92, v92, v94\n\t"
        "v_smfmac_f32_16x16x64_bf16 v[96:99], v[20:23], a[96:103], v122\n\t"
        "ds_write_b32 v127, v90\n\t"
        "v_add_f32_e32 v93, v93, v95\n\t"
        "v_cvt_pk_bf16_f32 v91, v92, v93\n\t"
        "v_smfmac_f32_16x16x64_bf16 v[100:103], v[20:23], a[104:111], v122\n\t"
        "ds_write_b32 v127, v91 offset:384\n\t"
        "v_cmp_u_f32_e32 vcc, v88, v89\n\t"
        "v_smfmac_f32_16x16x64_bf16 v[96:99], v[24:27], a[112:119], v122\n\t"
        "ds_read_b64_tr_b16 v[56:57], v52 offset:0\n\t"
        "ds_read_b64_tr_b16 v[58:59], v52 offset:64\n\t"
        "v_smfmac_f32_16x16x64_bf16 v[100:103], v[24:27], a[120:127], v122\n\t"
        "ds_read_b64_tr_b16 v[60:61], v53 offset:0\n\t"
        "ds_read_b64_tr_b16 v[62:63], v53 offset:64\n\t"
        "v_smfmac_f32_16x16x64_bf16 v[96:99], v[28:31], a[208:215], v122\n\t"
        "s_cbranch_vccnz .Lf1%=\n\t"
        ".Lr1%=:\n\t"
        "ds_read_b128 v[80:83], v121 offset:30720\n\t"
        "v_smfmac_f32_16x16x64_bf16 v[100:103], v[28:31], a[216:223], v122\n\t"
        "ds_read_b128 v[84:87], v121 offset:31744\n\t"
        "v_mov_b32_e32 v91, 0\n\t"
        "v_smfmac_f32_16x16x64_bf16 v[96:99], v[32:35], a[224:231], v122\n\t"
        "v_mov_b32_e32 v94, 0\n\t"
        "v_smfmac_f32_16x16x64_bf16 v[100:103], v[32:35], a[232:239], v122\n\t"
        "ds_read_b128 v[32:35], v120 offset:20480\n\t"
        "v_smfmac_f32_16x16x64_bf16 v[96:99], v[36:39], a[240:247], v122\n\t"
        "s_waitcnt lgkmcnt(3)\n\t"
        "s_barrier\n\t"
        "v_mfma_f32_16x16x32_bf16 v[68:71], v[104:107], v[56:59], 0\n\t"
        "ds_read_b64_tr_b16 v[16:17], v123 offset:0\n\t"
        "v_mfma_f32_16x16x32_bf16 v[68:71], v[108:111], v[60:63], v[68:71]\n\t"
        "ds_read_b64_tr_b16 v[18:19], v123 offset:64\n\t"
        "v_smfmac_f32_16x16x64_bf16 v[100:103], v[36:39], a[248:255], v122\n\t"
        "ds_read_b64_tr_b16 v[20:21], v123 offset:768\n\t"
        "v_smfmac_f32_16x16x64_bf16 v[96:99], v[8:11], a[192:199], v122\n\t"
        "ds_read_b64_tr_b16 v[22:23], v123 offset:832\n\t"
        "v_smfmac_f32_16x16x64_bf16 v[100:103], v[8:11], a[200:207], v122\n\t"
        "s_nop 1\n\t"
        "ds_write_b128 v120, v[68:71] offset:16384\n\t"
        "v_smfmac_f32_16x16x64_bf16 v[96:99], v[4:7], v[72:79], v122\n\t"
        "buffer_load_dword v88, v127, %[srd], s61 offen offset:0 sc1\n\t"
        "s_waitcnt lgkmcnt(6)\n\t"
        "v_smfmac_f32_16x16x64_bf16 v[100:103], v[4:7], v[80:87], v122\n\t"
        "buffer_load_dword v89, v127, %[srd], s61 offen offset:256 sc1\n\t"
        "v_smfmac_f32_16x16x64_bf16 v[96:99], v[40:43], v[208:215], v122\n\t"
        "ds_read_b64_tr_b16 v[24:25], v123 offset:1536\n\t"
        "v_smfmac_f32_16x16x64_bf16 v[100:103], v[40:43], v[216:223], v122\n\t"
        "ds_read_b64_tr_b16 v[26:27], v123 offset:1600\n\t"
        "v_smfmac_f32_16x16x64_bf16 v[96:99], v[44:47], v[224:231], v122\n\t"
        "s_waitcnt lgkmcnt(2)\n\t"
        "s_barrier\n\t"
        "ds_read_b128 v[36:39], v124 offset:16384\n\t"
        "v_smfmac_f32_16x16x64_bf16 v[100:103], v[44:47], v[232:239], v122\n\t"
        "ds_read_b128 v[56:59], v125 offset:16384\n\t"
        "v_smfmac_f32_16x16x64_bf16 v[96:99], v[48:51], v[240:247], v122\n\t"
        "ds_read_b128 v[60:63], v126 offset:16384\n\t"
        "v_smfmac_f32_16x16x64_bf16 v[100:103], v[48:51], v[248:255], v122\n\t"
        "ds_read_b64_tr_b16 v[28:29], v123 offset:2304\n\t"
        "ds_read_b64_tr_b16 v[30:31], v123 offset:2368\n\t"
        "buffer_load_dwordx4 v[84:87], v120, %[srd], s66 offen sc1\n\t"
        "s_waitcnt lgkmcnt(4)\n\t"
        "s_cmp_eq_u32 s40, 63\n\t"
        "v_add_f32_e32 v68, v68, v36\n\t"
        "v_add_f32_e32 v69, v69, v37\n\t"
        "v_add_f32_e32 v70, v70, v38\n\t"
        "v_add_f32_e32 v71, v71, v39\n\t"
        "v_add_f32_e32 v96, v96, v98\n\t"
        "v_add_f32_e32 v97, v97, v99\n\t"
        "s_waitcnt lgkmcnt(2)\n\t"
        "v_cvt_pk_bf16_f32 v50, v96, v97\n\t"
        "v_pk_add_f32 v[100:101], v[100:101], v[102:103]\n\t"
        "ds_write_b32 v35, v50\n\t"
        "v_cvt_pk_bf16_f32 v51, v100, v101\n\t"
        "ds_write_b32 v35, v51 offset:384\n\t"
        "v_pk_add_f32 v[56:57], v[56:57], v[60:61]\n\t"
        "s_cbranch_scc1 .Lend%=\n\t"
        "v_pk_add_f32 v[58:59], v[58:59], v[62:63]\n\t"
        "v_pk_add_f32 v[68:69], v[68:69], v[56:57]\n\t"
        "v_pk_add_f32 v[70:71], v[70:71], v[58:59]\n\t"
        "v_max3_f32 v2, v68, v69, v70\n\t"
        "v_max_f32_e32 v2, v2, v71\n\t"
        "s_nop 1\n\t"
        "v_max_f32_dpp v3, v2, v2 row_ror:8 row_mask:0xf bank_mask:0xf\n\t"
        "v_max_f32_dpp v1, v2, v2 row_ror:8 row_mask:0xf bank_mask:0xf\n\t"
        "s_nop 1\n\t"
        "v_permlane16_swap_b32_e32 v3, v1\n\t"
        "v_max_f32_e32 v2, v3, v1\n\t"
        "v_max_f32_e32 v3, v3, v1\n\t"
        "s_waitcnt lgkmcnt(0)\n\t"
        "s_barrier\n\t"
        "ds_read_b64_tr_b16 v[72:73], v32 offset:0\n\t"
        "v_permlane32_swap_b32_e32 v2, v3\n\t"
        "ds_read_b64_tr_b16 v[74:75], v32 offset:128\n\t"
        "v_max_f32_e32 v0, v2, v3\n\t"
        "ds_read_b64_tr_b16 v[76:77], v32 offset:1536\n\t"
        "v_mul_f32_e32 v2, 0xbe0293ee, v0\n\t"
        "ds_read_b64_tr_b16 v[78:79], v32 offset:1664\n\t"
        "v_fmamk_f32 v68, v68, 0x3e0293ee, v2\n\t"
        "v_fmamk_f32 v69, v69, 0x3e0293ee, v2\n\t"
        "v_fmamk_f32 v70, v70, 0x3e0293ee, v2\n\t"
        "v_fmamk_f32 v71, v71, 0x3e0293ee, v2\n\t"
        "v_exp_f32_e32 v68, v68\n\t"
        "v_exp_f32_e32 v69, v69\n\t"
        "v_exp_f32_e32 v70, v70\n\t"
        "s_waitcnt lgkmcnt(2)\n\t"
        "v_smfmac_f32_16x16x64_bf16 v[64:67], v[72:75], v[16:23], v122\n\t"
        "s_waitcnt lgkmcnt(0)\n\t"
        "v_smfmac_f32_16x16x64_bf16 v[64:67], v[76:79], v[24:31], v122\n\t"
        "s_waitcnt vmcnt(3)\n\t"
        "v_cvt_pk_bf16_f32 v36, v68, v69\n\t"
        "v_exp_f32_e32 v71, v71\n\t"
        "s_nop 0\n\t"
        "v_mov_b32_dpp v38, v36 row_ror:8 row_mask:0xf bank_mask:0xf\n\t"
        "v_cvt_pk_bf16_f32 v37, v70, v71\n\t"
        "s_nop 1\n\t"
        "v_mov_b32_dpp v39, v37 row_ror:8 row_mask:0xf bank_mask:0xf\n\t"
        "v_pk_add_f32 v[64:65], v[64:65], v[66:67]\n\t"
        "v_max3_f32 v3, v0, v64, v65\n\t"
        "v_max3_f32 v1, v0, v64, v65\n\t"
        "v_mfma_f32_16x16x32_bf16 v[40:43], v[112:115], v[36:39], 0\n\t"
        "s_nop 0\n\t"
        "v_permlane16_swap_b32_e32 v3, v1\n\t"
        "v_max_f32_e32 v2, v3, v1\n\t"
        "v_max_f32_e32 v3, v3, v1\n\t"
        "v_mfma_f32_16x16x32_bf16 v[44:47], v[116:119], v[36:39], 0\n\t"
        "s_nop 0\n\t"
        "v_permlane32_swap_b32_e32 v2, v3\n\t"
        "v_max_f32_e32 v2, v2, v3\n\t"
        "v_mfma_f32_16x16x32_bf16 v[96:99], v[12:15], v[36:39], 0\n\t"
        "v_mul_f32_e32 v2, 0xbe0293ee, v2\n\t"
        "v_fmamk_f32 v10, v0, 0x3e0293ee, v2\n\t"
        "v_fmamk_f32 v64, v64, 0x3e0293ee, v2\n\t"
        "v_exp_f32_e32 v10, v10\n\t"
        "v_fmamk_f32 v65, v65, 0x3e0293ee, v2\n\t"
        "v_exp_f32_e32 v64, v64\n\t"
        "s_nop 1\n\t"
        "v_mul_f32_e32 v96, v96, v10\n\t"
        "v_exp_f32_e32 v65, v65\n\t"
        "v_pk_mul_f32 v[40:41], v[40:41], v[10:11] op_sel_hi:[1,0]\n\t"
        "v_cvt_pk_bf16_f32 v90, v64, v65\n\t"
        "v_pk_mul_f32 v[42:43], v[42:43], v[10:11] op_sel_hi:[1,0]\n\t"
        "v_cvt_pk_bf16_f32 v95, v64, v65\n\t"
        "v_pk_mul_f32 v[44:45], v[44:45], v[10:11] op_sel_hi:[1,0]\n\t"
        "v_mfma_f32_16x16x16_bf16 v[96:99], v[12:13], v[90:91], v[96:99]\n\t"
        "s_waitcnt vmcnt(1)\n\t"
        "v_mul_f32_e32 v46, v46, v10\n\t"
        "v_mul_f32_e32 v47, v47, v10\n\t"
        "v_mfma_f32_16x16x16_bf16 v[40:43], v[88:89], v[90:91], v[40:43]\n\t"
        "v_max_u32_e32 v54, v88, v89\n\t"
        "v_cmp_eq_u32_e32 vcc, -1, v54\n\t"
        "v_mfma_f32_16x16x16_bf16 v[44:47], v[88:89], v[94:95], v[44:47]\n\t"
        "s_nop 0\n\t"
        "v_rcp_f32_e32 v2, v96\n\t"
        "s_nop 0\n\t"
        "s_cbranch_vccnz .Lfb%=\n\t"
        ".Lrb%=:\n\t"
        "s_nop 0\n\t"
        "v_pk_mul_f32 v[40:41], v[40:41], v[2:3] op_sel_hi:[1,0]\n\t"
        "v_pk_mul_f32 v[42:43], v[42:43], v[2:3] op_sel_hi:[1,0]\n\t"
        "v_cvt_pk_bf16_f32 v100, v40, v41\n\t"
        "v_pk_mul_f32 v[44:45], v[44:45], v[2:3] op_sel_hi:[1,0]\n\t"
        "v_pk_mul_f32 v[46:47], v[46:47], v[2:3] op_sel_hi:[1,0]\n\t"
        "v_cvt_pk_bf16_f32 v101, v42, v43\n\t"
        "v_cvt_pk_bf16_f32 v102, v44, v45\n\t"
        "v_min_u32_e32 v100, -2, v100\n\t"
        "v_cvt_pk_bf16_f32 v103, v46, v47\n\t"
        "v_min_u32_e32 v102, -2, v102\n\t"
        "buffer_store_dwordx4 v[100:103], v33, %[srd], s63 offen sc1\n\t"
        "s_memrealtime s[76:77]\n\t"
        "v_mov_b32_e32 v8, -1\n\t"
        "v_mov_b32_e32 v9, -1\n\t"
        "v_mov_b32_e32 v10, -1\n\t"
        "v_mov_b32_e32 v11, -1\n\t"
        "v_permlane16_swap_b32_e32 v50, v51\n\t"
        "v_permlane16_swap_b32_e32 v88, v89\n\t"
        "v_mov_b32_e32 v14, s40\n\t"
        "s_waitcnt lgkmcnt(0)\n\t"
        "s_waitcnt vmcnt(1)\n\t"
        "s_mov_b32 s97, 0\n\t"
        ".Lsr%=:\n\t"
        "s_mov_b32 exec_hi, 0xffff\n\t"
        "v_subrev_u32_e32 v0, s41, v84\n\t"
        "v_subrev_u32_e32 v1, s41, v86\n\t"
        "v_max_i32_e32 v0, v0, v1\n\t"
        "v_max_u32_e32 v1, v85, v87\n\t"
        "v_cmp_eq_u32_e32 vcc, -1, v1\n\t"
        "v_max_i32_dpp v0, v0, v0 quad_perm:[1,0,3,2] row_mask:0xf bank_mask:0xf\n\t"
        "v_or_b32_e32 v2, v85, v87\n\t"
        "s_nop 0\n\t"
        "v_max_i32_dpp v0, v0, v0 quad_perm:[2,3,0,1] row_mask:0xf bank_mask:0xf\n\t"
        "s_nop 1\n\t"
        "v_max_i32_dpp v0, v0, v0 row_ror:4 row_mask:0xf bank_mask:0xf\n\t"
        "s_nop 1\n\t"
        "v_max_i32_dpp v0, v0, v0 row_ror:8 row_mask:0xf bank_mask:0xf\n\t"
        "s_cbranch_vccnz .Lsf%=\n\t"
        "v_readlane_b32 s85, v0, 0\n\t"
        "v_readlane_b32 s81, v0, 16\n\t"
        "v_readlane_b32 s56, v0, 32\n\t"
        "v_and_b32_e32 v1, 1, v2\n\t"
        "v_cmp_ne_u32_e64 s[86:87], 0, v1\n\t"
        "v_and_b32_e32 v1, 2, v2\n\t"
        "v_cmp_ne_u32_e64 s[88:89], 0, v1\n\t"
        "v_and_b32_e32 v1, 4, v2\n\t"
        "v_cmp_ne_u32_e64 s[90:91], 0, v1\n\t"
        "s_max_i32 s56, s56, s81\n\t"
        "s_add_u32 s92, s85, s41\n\t"
        "s_sub_u32 s92, s92, s43\n\t"
        "s_add_i32 s93, s56, s47\n\t"
        "s_mov_b32 s44, s46\n\t"
        "s_mov_b32 s46, s45\n\t"
        "s_mov_b32 s94, s48\n\t"
        "s_mov_b32 s43, s41\n\t"
        "s_cmp_gt_i32 s56, s54\n\t"
        "s_cbranch_scc1 .Lmv%=\n\t"
        "s_mov_b32 s37, 0\n\t"
        "s_mov_b32 s48, 0\n\t"
        "s_mov_b32 s95, 0\n\t"
        ".Lsd%=:\n\t"
        "s_mov_b32 exec_hi, -1\n\t"
        "s_add_u32 s42, s43, s46\n\t"
        "s_sleep %c[HSLP]\n\t"
        "buffer_load_dwordx2 v[48:49], off, %[wsd], s64 sc1\n\t"
        "buffer_load_dwordx4 v[104:107], v120, %[wsd], s69 offen sc1\n\t"
        "buffer_load_dwordx4 v[108:111], v120, %[wsd], s70 offen sc1\n\t"
        "buffer_store_dwordx2 v[50:51], v34, %[wsd], s72 offen sc1\n\t"
        "buffer_store_dwordx2 v[88:89], v34, %[wsd], s72 offen offset:2048 sc1\n\t"
        "s_lshl_b32 s80, %[w], 8\n\t"
        "s_add_u32 s80, s80, s61\n\t"
        "s_add_u32 s80, s80, 24576\n\t"
        "buffer_store_dwordx2 v[8:9], v34, %[srd], s80 offen sc1\n\t"
        "s_cmp_eq_u32 %[w], 0\n\t"
        "s_cselect_b32 exec_hi, 0, -1\n\t"
        "buffer_store_dwordx4 v[8:11], v120, %[srd], s68 offen sc1\n\t"
        "s_mov_b32 exec_hi, -1\n\t"
        "s_mov_b64 exec, 1\n\t"
        "buffer_store_dword v14, off, %[wsd], s74 sc1\n\t"
        "s_mov_b64 exec, -1\n\t"
        "s_cmp_ge_u32 s40, %c[iCT0]\n\t"
        "s_cselect_b32 s85, 1, 0\n\t"
        "s_cmp_lg_u64 s[86:87], 0\n\t"
        "s_cselect_b32 s86, s85, 0\n\t"
        "s_cmp_lg_u64 s[88:89], 0\n\t"
        "s_cselect_b32 s87, s85, 0\n\t"
        "s_cmp_lg_u64 s[90:91], 0\n\t"
        "s_cselect_b32 s88, s85, 0\n\t"
        "s_or_b32 s80, s94, s86\n\t"
        "s_or_b32 s80, s80, s87\n\t"
        "s_andn2_b32 s90, s85, s80\n\t"
        "s_cmp_le_i32 s92, s44\n\t"
        "s_cselect_b32 s80, s85, 0\n\t"
        "s_andn2_b32 s91, s80, s88\n\t"
        "s_and_b32 s80, s40, %c[LDEC]\n\t"
        "s_cselect_b32 s89, 0, 1\n\t"
        "s_mov_b32 s80, s88\n\t"
        "s_add_i32 s81, s92, s50\n\t"
        "s_sub_i32 s81, s81, 4\n\t"
        "s_cmp_le_i32 s81, s44\n\t"
        "s_cselect_b32 s80, s80, 0\n\t"
        "s_cmp_lt_i32 s50, %c[LC24]\n\t"
        "s_cselect_b32 s80, s80, 0\n\t"
        "s_lshl2_add_u32 s50, s80, s50\n\t"
        "s_sub_i32 s50, s50, s89\n\t"
        "s_max_i32 s50, s50, %c[LC]\n\t"
        "s_lshl_b32 s80, s92, 1\n\t"
        "s_sub_i32 s81, s80, s52\n\t"
        "s_ashr_i32 s82, s81, %c[UPSH]\n\t"
        "s_max_i32 s82, s82, %c[TRUP]\n\t"
        "s_add_i32 s82, s82, s52\n\t"
        "s_min_i32 s82, s82, s80\n\t"
        "s_ashr_i32 s83, s81, 1\n\t"
        "s_min_i32 s83, s83, -%c[TRDN]\n\t"
        "s_add_i32 s83, s83, s52\n\t"
        "s_max_i32 s83, s83, s80\n\t"
        "s_cmp_gt_i32 s81, 0\n\t"
        "s_cselect_b32 s82, s82, s83\n\t"
        "s_cmp_lg_u32 s90, 0\n\t"
        "s_cselect_b32 s52, s82, s52\n\t"
        "s_add_i32 s80, s52, 1\n\t"
        "s_ashr_i32 s80, s80, 1\n\t"
        "s_add_i32 s45, s80, s50\n\t"
        "s_andn2_b32 s80, s86, s94\n\t"
        "s_cmp_lt_i32 s51, %c[LA24]\n\t"
        "s_cselect_b32 s80, s80, 0\n\t"
        "s_lshl2_add_u32 s51, s80, s51\n\t"
        "s_sub_i32 s51, s51, s89\n\t"
        "s_max_i32 s51, s51, %c[LA]\n\t"
        "s_lshl_b32 s80, s93, 1\n\t"
        "s_sub_i32 s81, s80, s53\n\t"
        "s_ashr_i32 s82, s81, %c[UPSH]\n\t"
        "s_max_i32 s82, s82, %c[TRUP]\n\t"
        "s_add_i32 s82, s82, s53\n\t"
        "s_min_i32 s82, s82, s80\n\t"
        "s_ashr_i32 s83, s81, 1\n\t"
        "s_min_i32 s83, s83, -%c[TRDN]\n\t"
        "s_add_i32 s83, s83, s53\n\t"
        "s_max_i32 s83, s83, s80\n\t"
        "s_cmp_gt_i32 s81, 0\n\t"
        "s_cselect_b32 s82, s82, s83\n\t"
        "s_cmp_lg_u32 s91, 0\n\t"
        "s_cselect_b32 s53, s82, s53\n\t"
        "s_add_i32 s80, s53, 1\n\t"
        "s_ashr_i32 s80, s80, 1\n\t"
        "s_add_i32 s47, s80, s51\n\t"
        "s_add_u32 s81, s47, %c[AUP]\n\t"
        "s_min_i32 s80, s95, s81\n\t"
        "s_max_i32 s47, s47, s80\n\t"
        "s_add_u32 s41, s42, s47\n\t"
        "s_add_u32 s40, s40, 1\n\t"
        "v_mov_b32_e32 v88, 0\n\t"
        "v_mov_b32_e32 v89, 0\n\t"
        "v_mov_b32_e32 v90, 0\n\t"
        "v_mov_b32_e32 v91, 0\n\t"
        "v_mov_b32_e32 v92, 0\n\t"
        "v_mov_b32_e32 v93, 0\n\t"
        "v_mov_b32_e32 v94, 0\n\t"
        "v_mov_b32_e32 v95, 0\n\t"
        "v_mov_b32_e32 v96, 0\n\t"
        "v_mov_b32_e32 v97, 0\n\t"
        "v_mov_b32_e32 v98, 0\n\t"
        "v_mov_b32_e32 v99, 0\n\t"
        "v_mov_b32_e32 v100, 0\n\t"
        "v_mov_b32_e32 v101, 0\n\t"
        "v_mov_b32_e32 v102, 0\n\t"
        "v_mov_b32_e32 v103, 0\n\t"
        "ds_read_b128 v[16:19], v121 offset:0\n\t"
        "ds_read_b128 v[20:23], v121 offset:1024\n\t"
        "ds_read_b128 v[24:27], v121 offset:2048\n\t"
        "ds_read_b128 v[28:31], v121 offset:3072\n\t"
        "ds_read_b128 v[32:35], v121 offset:4096\n\t"
        "ds_read_b128 v[36:39], v121 offset:5120\n\t"
        "ds_read_b128 v[40:43], v121 offset:6144\n\t"
        "ds_read_b128 v[44:47], v121 offset:7168\n\t"
        "ds_read_b128 v[56:59], v121 offset:8192\n\t"
        "ds_read_b128 v[60:63], v121 offset:9216\n\t"
        "ds_read_b128 v[64:67], v121 offset:10240\n\t"
        "ds_read_b128 v[68:71], v121 offset:11264\n\t"
        "ds_read_b128 v[72:75], v121 offset:12288\n\t"
        "ds_read_b128 v[76:79], v121 offset:13312\n\t"
        "ds_read_b128 v[80:83], v121 offset:14336\n\t"
        "ds_read_b128 v[84:87], v121 offset:15360\n\t"
        "v_mov_b32_e32 v6, 30720\n\t"
        "ds_read_b64 v[6:7], v6\n\t"
        "s_waitcnt lgkmcnt(0)\n\t"
        "v_readfirstlane_b32 s80, v6\n\t"
        "v_readfirstlane_b32 s81, v7\n\t"
        "s_cmp_eq_u32 s80, s40\n\t"
        "s_cselect_b32 s84, 1, 0\n\t"
        "s_cmp_eq_u32 s81, s40\n\t"
        "s_cselect_b32 s81, 2, 0\n\t"
        "s_or_b32 s84, s84, s81\n\t"
        "s_waitcnt vmcnt(7)\n\t"
        "s_cmp_eq_u32 s64, s36\n\t"
        "s_cbranch_scc1 .Lk11%=\n\t"
        "s_mov_b32 s97, 0\n\t"
        ".Lk01%=:\n\t"
        "v_readfirstlane_b32 s82, v48\n\t"
        "v_readfirstlane_b32 s83, v49\n\t"
        "s_xor_b32 s82, s82, %[tag]\n\t"
        "s_xor_b32 s83, s83, %[tag]\n\t"
        "s_or_b32 s82, s82, s83\n\t"
        "s_cbranch_scc0 .Lk11%=\n\t"
        "s_sleep 3\n\t"
        "buffer_load_dwordx2 v[48:49], off, %[wsd], s64 sc1\n\t"
        "s_add_u32 s97, s97, 1\n\t"
        "s_cmp_lt_u32 s97, 0x200000\n\t"
        "s_cbranch_scc0 .Lgu%=\n\t"
        "s_waitcnt vmcnt(0)\n\t"
        "s_branch .Lk01%=\n\t"
        ".Lk11%=:\n\t"
        "v_mov_b32_e32 v48, s76\n\t"
        "v_mov_b32_e32 v49, s84\n\t"
        "s_mov_b32 s85, s67\n\t"
        "s_and_b32 s80, s40, 3\n\t"
        "s_lshl_b32 s80, s80, 17\n\t"
        "s_add_u32 s57, s80, %[nA]\n\t"
        "s_add_u32 s58, s57, 0x1000\n\t"
        "s_add_u32 s59, s57, 0x4000\n\t"
        "s_add_u32 s60, s57, 0x5000\n\t"
        "s_and_b32 s80, s40, 3\n\t"
        "s_lshl_b32 s80, s80, 17\n\t"
        "s_add_u32 s63, s80, %[nC]\n\t"
        "s_add_u32 s66, s80, %[nS]\n\t"
        "s_add_u32 s81, s80, 0x20000\n\t"
        "s_and_b32 s81, s81, 0x60000\n\t"
        "s_add_u32 s67, s81, %[nW]\n\t"
        "s_cmp_eq_u32 %[w], 2\n\t"
        "s_cselect_b32 s81, s80, s81\n\t"
        "s_xor_b32 s81, s81, 0x40000\n\t"
        "s_add_u32 s68, s81, %[pob]\n\t"
        "s_lshl_b32 s72, s40, 17\n\t"
        "s_add_u32 s72, s72, %[kvo]\n\t"
        "v_lshlrev_b32_e32 v51, 6, v120\n\t"
        "s_add_u32 s80, s85, %[swo]\n\t"
        "s_cmp_eq_u32 %[w], 0\n\t"
        "s_cselect_b32 s73, s80, s36\n\t"
        "s_cselect_b32 s74, %[pra], s36\n\t"
        "s_add_u32 s80, s40, 1\n\t"
        "s_min_u32 s80, s80, 63\n\t"
        "s_lshl_b32 s80, s80, 18\n\t"
        "s_add_u32 s69, s80, %[kx]\n\t"
        "s_add_u32 s70, s69, 0x1000\n\t"
        "s_lshl_b32 s80, s40, 18\n\t"
        "s_add_u32 s71, s80, %[vx]\n\t"
        "s_add_u32 s80, s40, 2\n\t"
        "s_lshl_b32 s81, s80, 9\n\t"
        "s_add_u32 s81, s81, %[kf]\n\t"
        "s_and_b32 s80, s80, 0x43\n\t"
        "s_cselect_b32 s64, s36, s81\n\t"
        "s_mov_b32 s80, s61\n\t"
        "s_mov_b32 s61, s62\n\t"
        "s_mov_b32 s62, s80\n\t"
        "s_waitcnt lgkmcnt(0)\n\t"
        "s_branch .Lloop%=\n\t"
        ".Lmv%=:\n\t"
        "s_mov_b32 s48, 1\n\t"
        "s_cmp_eq_u32 s54, %c[iTOL]\n\t"
        "s_cbranch_scc1 .Lm1%=\n\t"
        "s_max_i32 s80, s85, s56\n\t"
        "s_add_u32 s43, s41, s80\n\t"
        "s_add_u32 s43, s43, %c[iPEN0]\n\t"
        "s_mov_b32 s54, %c[iTOL]\n\t"
        "s_add_u32 s46, s46, %c[iX0C]\n\t"
        "s_add_u32 s95, s47, %c[iX0A]\n\t"
        "v_and_b32_e32 v0, 3, v84\n\t"
        "v_and_b32_e32 v1, 3, v86\n\t"
        "v_lshlrev_b32_e64 v0, v0, 1\n\t"
        "v_lshlrev_b32_e64 v1, v1, 1\n\t"
        "v_or_b32_e32 v0, v0, v1\n\t"
        "s_mov_b32 s80, 0\n\t"
        "v_and_b32_e32 v1, 1, v0\n\t"
        "v_cmp_ne_u32_e32 vcc, 0, v1\n\t"
        "s_nop 1\n\t"
        "s_cmp_lg_u64 vcc, 0\n\t"
        "s_cselect_b32 s81, 2, 0\n\t"
        "s_or_b32 s80, s80, s81\n\t"
        "v_and_b32_e32 v1, 2, v0\n\t"
        "v_cmp_ne_u32_e32 vcc, 0, v1\n\t"
        "s_nop 1\n\t"
        "s_cmp_lg_u64 vcc, 0\n\t"
        "s_cselect_b32 s81, 4, 0\n\t"
        "s_or_b32 s80, s80, s81\n\t"
        "v_and_b32_e32 v1, 4, v0\n\t"
        "v_cmp_ne_u32_e32 vcc, 0, v1\n\t"
        "s_nop 1\n\t"
        "s_cmp_lg_u64 vcc, 0\n\t"
        "s_cselect_b32 s81, 8, 0\n\t"
        "s_or_b32 s80, s80, s81\n\t"
        "v_and_b32_e32 v1, 8, v0\n\t"
        "v_cmp_ne_u32_e32 vcc, 0, v1\n\t"
        "s_nop 1\n\t"
        "s_cmp_lg_u64 vcc, 0\n\t"
        "s_cselect_b32 s81, 16, 0\n\t"
        "s_or_b32 s80, s80, s81\n\t"
        "s_lshr_b32 s80, 0x1adf1210, s80\n\t"
        "s_and_b32 s55, s80, 3\n\t"
        "s_branch .Lal%=\n\t"
        ".Lm1%=:\n\t"
        "s_add_u32 s80, s37, %c[iPEN2]\n\t"
        "s_min_i32 s80, s80, %c[iPENM]\n\t"
        "s_cmp_lg_u32 s37, 0\n\t"
        "s_cselect_b32 s37, s80, %c[iPEN]\n\t"
        "s_add_u32 s80, s56, s37\n\t"
        "s_max_i32 s80, s80, %c[iPMIN]\n\t"
        "s_add_u32 s43, s41, s80\n\t"
        "s_add_u32 s95, s93, %c[LA]\n\t"
        ".Lal%=:\n\t"
        "s_sub_u32 s80, s55, s43\n\t"
        "s_and_b32 s80, s80, %c[iPHM]\n\t"
        "s_add_u32 s43, s43, s80\n\t"
        "s_branch .Lsd%=\n\t"
        ".Lsf%=:\n\t"
        "s_mov_b32 exec_hi, -1\n\t"
        "s_sleep 2\n\t"
        "buffer_load_dwordx4 v[84:87], v120, %[srd], s66 offen sc1\n\t"
        "s_add_u32 s97, s97, 1\n\t"
        "s_cmp_lt_u32 s97, 0x100000\n\t"
        "s_cbranch_scc0 .Lgu%=\n\t"
        "s_waitcnt vmcnt(0)\n\t"
        "s_branch .Lsr%=\n\t"
        ".Lf1%=:\n\t"
        "ds_read_b128 v[92:95], v120 offset:0\n\t"
        "ds_read_b128 v[68:71], v120 offset:4096\n\t"
        "v_max_u32_e32 v54, v16, v18\n\t"
        "v_max3_u32 v54, v54, v17, v19\n\t"
        "v_max3_u32 v54, v54, v20, v22\n\t"
        "v_max3_u32 v54, v54, v21, v23\n\t"
        "v_max3_u32 v54, v54, v24, v26\n\t"
        "v_max3_u32 v54, v54, v25, v27\n\t"
        "v_max3_u32 v54, v54, v28, v30\n\t"
        "v_max3_u32 v54, v54, v29, v31\n\t"
        "v_max3_u32 v54, v54, v32, v34\n\t"
        "v_max3_u32 v54, v54, v33, v35\n\t"
        "v_max3_u32 v54, v54, v36, v38\n\t"
        "v_max3_u32 v54, v54, v37, v39\n\t"
        "v_max3_u32 v54, v54, v40, v42\n\t"
        "v_max3_u32 v54, v54, v41, v43\n\t"
        "v_max3_u32 v54, v54, v44, v46\n\t"
        "v_max3_u32 v54, v54, v45, v47\n\t"
        "v_max3_u32 v54, v54, v48, v50\n\t"
        "v_max3_u32 v54, v54, v49, v51\n\t"
        "v_max3_u32 v54, v54, v0, v2\n\t"
        "v_max3_u32 v54, v54, v1, v3\n\t"
        "v_max3_u32 v54, v54, v4, v6\n\t"
        "v_max3_u32 v54, v54, v5, v7\n\t"
        "v_max3_u32 v54, v54, v8, v10\n\t"
        "v_max3_u32 v54, v54, v9, v11\n\t"
        "s_waitcnt lgkmcnt(0)\n\t"
        "v_max3_u32 v54, v54, v92, v94\n\t"
        "v_max3_u32 v54, v54, v93, v95\n\t"
        "v_max3_u32 v54, v54, v68, v70\n\t"
        "v_max3_u32 v54, v54, v69, v71\n\t"
        "ds_read_b128 v[92:95], v120 offset:8192\n\t"
        "ds_read_b128 v[68:71], v120 offset:12288\n\t"
        "s_waitcnt lgkmcnt(0)\n\t"
        "v_max3_u32 v54, v54, v92, v94\n\t"
        "v_max3_u32 v54, v54, v93, v95\n\t"
        "v_max3_u32 v54, v54, v68, v70\n\t"
        "v_max3_u32 v54, v54, v69, v71\n\t"
        "v_cmp_eq_u32_e32 vcc, -1, v54\n\t"
        "s_nop 1\n\t"
        "v_mov_b32_e32 v55, v55\n\t"
        "s_nop 0\n\t"
        "s_cbranch_vccz .Lr1%=\n\t"
        "buffer_load_dwordx4 v[0:3], v120, %[srd], s57 offen sc1\n\t"
        "buffer_load_dwordx4 v[4:7], v120, %[srd], s58 offen sc1\n\t"
        "buffer_load_dwordx4 v[8:11], v120, %[srd], s59 offen sc1\n\t"
        "buffer_load_dwordx4 v[12:15], v120, %[srd], s60 offen sc1\n\t"
        "s_add_u32 s80, s40, 1\n\t"
        "v_mov_b32_e32 v54, 30720\n\t"
        "v_mov_b32_e32 v55, s80\n\t"
        "ds_write_b32 v54, v55\n\t"
        "ds_read_b128 v[16:19], v121 offset:0\n\t"
        "ds_read_b128 v[20:23], v121 offset:1024\n\t"
        "ds_read_b128 v[24:27], v121 offset:2048\n\t"
        "ds_read_b128 v[28:31], v121 offset:3072\n\t"
        "ds_read_b128 v[32:35], v121 offset:4096\n\t"
        "ds_read_b128 v[36:39], v121 offset:5120\n\t"
        "ds_read_b128 v[40:43], v121 offset:6144\n\t"
        "ds_read_b128 v[44:47], v121 offset:7168\n\t"
        "ds_read_b128 v[56:59], v121 offset:8192\n\t"
        "ds_read_b128 v[60:63], v121 offset:9216\n\t"
        "ds_read_b128 v[64:67], v121 offset:10240\n\t"
        "ds_read_b128 v[68:71], v121 offset:11264\n\t"
        "ds_read_b128 v[72:75], v121 offset:12288\n\t"
        "ds_read_b128 v[76:79], v121 offset:13312\n\t"
        "ds_read_b128 v[80:83], v121 offset:14336\n\t"
        "ds_read_b128 v[84:87], v121 offset:15360\n\t"
        "v_mov_b32_e32 v88, 0\n\t"
        "v_mov_b32_e32 v89, 0\n\t"
        "v_mov_b32_e32 v90, 0\n\t"
        "v_mov_b32_e32 v91, 0\n\t"
        "v_mov_b32_e32 v92, 0\n\t"
        "v_mov_b32_e32 v93, 0\n\t"
        "v_mov_b32_e32 v94, 0\n\t"
        "v_mov_b32_e32 v95, 0\n\t"
        "v_mov_b32_e32 v96, 0\n\t"
        "v_mov_b32_e32 v97, 0\n\t"
        "v_mov_b32_e32 v98, 0\n\t"
        "v_mov_b32_e32 v99, 0\n\t"
        "v_mov_b32_e32 v100, 0\n\t"
        "v_mov_b32_e32 v101, 0\n\t"
        "v_mov_b32_e32 v102, 0\n\t"
        "v_mov_b32_e32 v103, 0\n\t"
        "s_waitcnt lgkmcnt(0)\n\t"
        "s_barrier\n\t"
        ".Lfa%=:\n\t"
        "s_waitcnt vmcnt(0)\n\t"
        "v_max_u32_e32 v54, v0, v2\n\t"
        "v_max3_u32 v54, v54, v1, v3\n\t"
        "v_max3_u32 v54, v54, v4, v6\n\t"
        "v_max3_u32 v54, v54, v5, v7\n\t"
        "v_max3_u32 v54, v54, v8, v10\n\t"
        "v_max3_u32 v54, v54, v9, v11\n\t"
        "v_max3_u32 v54, v54, v12, v14\n\t"
        "v_max3_u32 v54, v54, v13, v15\n\t"
        "v_cmp_eq_u32_e32 vcc, -1, v54\n\t"
        "s_nop 1\n\t"
        "v_mov_b32_e32 v55, v55\n\t"
        "s_nop 0\n\t"
        "s_cbranch_vccz .Lfg%=\n\t"
        "s_sleep %c[ASLP]\n\t"
        "buffer_load_dwordx4 v[0:3], v120, %[srd], s57 offen sc1\n\t"
        "buffer_load_dwordx4 v[4:7], v120, %[srd], s58 offen sc1\n\t"
        "buffer_load_dwordx4 v[8:11], v120, %[srd], s59 offen sc1\n\t"
        "buffer_load_dwordx4 v[12:15], v120, %[srd], s60 offen sc1\n\t"
        "s_add_u32 s97, s97, 1\n\t"
        "s_cmp_lt_u32 s97, 0x100000\n\t"
        "s_cbranch_scc0 .Lgu%=\n\t"
        "s_branch .Lfa%=\n\t"
        ".Lfg%=:\n\t"
        "s_sleep %c[RSLP]\n\t"
        "s_branch .Lgo%=\n\t"
        ".Lfb%=:\n\t"
        "s_nop 7\n\t"
        "s_mov_b32 s97, 0\n\t"
        "s_add_u32 s80, s40, 1\n\t"
        "v_mov_b32_e32 v54, 30724\n\t"
        "v_mov_b32_e32 v55, s80\n\t"
        "ds_write_b32 v54, v55\n\t"
        ".Lfp%=:\n\t"
        "s_sleep %c[ASLP]\n\t"
        "buffer_load_dword v88, v127, %[srd], s61 offen offset:0 sc1\n\t"
        "buffer_load_dword v89, v127, %[srd], s61 offen offset:256 sc1\n\t"
        "s_add_u32 s97, s97, 1\n\t"
        "s_cmp_lt_u32 s97, 0x100000\n\t"
        "s_cbranch_scc0 .Lgu%=\n\t"
        "s_waitcnt vmcnt(0)\n\t"
        "v_max_u32_e32 v54, v88, v89\n\t"
        "v_cmp_eq_u32_e32 vcc, -1, v54\n\t"
        "s_nop 1\n\t"
        "v_mov_b32_e32 v55, v55\n\t"
        "s_nop 0\n\t"
        "s_cbranch_vccnz .Lfp%=\n\t"
        "v_mfma_f32_16x16x32_bf16 v[40:43], v[112:115], v[36:39], 0\n\t"
        "v_mfma_f32_16x16x32_bf16 v[44:47], v[116:119], v[36:39], 0\n\t"
        "s_nop 1\n\t"
        "v_mov_b32_e32 v55, v55\n\t"
        "s_nop 1\n\t"
        "v_mov_b32_e32 v55, v55\n\t"
        "s_nop 0\n\t"
        "v_pk_mul_f32 v[40:41], v[40:41], v[10:11] op_sel_hi:[1,0]\n\t"
        "v_pk_mul_f32 v[42:43], v[42:43], v[10:11] op_sel_hi:[1,0]\n\t"
        "v_pk_mul_f32 v[44:45], v[44:45], v[10:11] op_sel_hi:[1,0]\n\t"
        "v_pk_mul_f32 v[46:47], v[46:47], v[10:11] op_sel_hi:[1,0]\n\t"
        "v_mfma_f32_16x16x16_bf16 v[40:43], v[88:89], v[90:91], v[40:43]\n\t"
        "s_nop 0\n\t"
        "v_mfma_f32_16x16x16_bf16 v[44:47], v[88:89], v[94:95], v[44:47]\n\t"
        "s_nop 7\n\t"
        "s_branch .Lrb%=\n\t"
        ".Lta%=:\n\t"
        "s_waitcnt lgkmcnt(0)\n\t"
        "s_branch .Lxa%=\n\t"
        ".Lgu%=:\n\t"
        "s_mov_b64 exec, -1\n\t"
        "v_mov_b32_e32 v54, %c[ferr]\n\t"
        "v_mov_b32_e32 v55, 1\n\t"
        "buffer_store_dword v55, v54, %[wsd], 0 offen sc1\n\t"
        "s_endpgm\n\t"
        ".Lend%=:\n\t"
        "s_nop 0\n\t"
        "v_permlane16_swap_b32_e32 v50, v51\n\t"
        "v_mov_b32_e32 v0, 64\n\t"
        "buffer_store_dwordx2 v[50:51], v34, %[wsd], s72 offen sc1\n\t"
        "s_cmp_lg_u32 %[w], 0\n\t"
        "s_waitcnt vmcnt(0)\n\t"
        "s_barrier\n\t"
        "s_cbranch_scc1 .Le2%=\n\t"
        "s_mov_b64 exec, 1\n\t"
        "buffer_store_dword v0, off, %[wsd], %[pra] sc1\n\t"
        "s_mov_b64 exec, -1\n\t"
        ".Le2%=:\n\t"
        "s_endpgm"
        :
        : [srd] "s"(rhd), [wsd] "s"(wsd), [w] "s"(w), [tag] "s"(tag), [nA] "s"(nA), [nS] "s"(nS), [nW] "s"(nW), [nC] "s"(nC), [b0] "s"(b0), [b1] "s"(b1), [kx] "s"(kx), [vx] "s"(vx), [kf] "s"(kf), [kvo] "s"(kvo), [pob] "s"(pob),
          [swo] "s"(swo), [pra] "s"(pr_a), [ferr] "i"(F_ERR), [iD1] "i"(D1), [iD2] "i"(D2), [LC] "i"(LC0), [LA] "i"(LA0), [LC24] "i"(LC0 + 24), [LA24] "i"(LA0 + 24), [E1] "i"(2 * (D1 - LC0)), [E2] "i"(2 * (D2 - LA0)),
          [iPEN] "i"(PEN), [iPEN2] "i"(PEN2), [iPENM] "i"(PEN + 2 * PEN2), [iPEN0] "i"(PEN0), [iTOL] "i"(TOL), [iCT0] "i"(CT0), [iX0C] "i"(X0C), [iX0A] "i"(X0A), [iPHM] "i"(PH_M),
          [iFT] "i"(-3), [iFN] "i"(FINE_N), [AUP] "i"(A_UP), [ASLP] "i"(A_SLP), [LDEC] "i"(L_DEC), [UPSH] "i"(UP_SH), [TRUP] "i"(TR_UP), [TRDN] "i"(TR_DN), [HSLP] "i"(H_SLP), [iPMIN] "i"(PMIN), [VSLP] "i"(V_SLP), [RSLP] "i"(R_SLP),
          "{v[120:127]}"(pc), "{v[52:53]}"(pd), "{a[0:31]}"(wa[0]), "{a[32:63]}"(wa[1]), "{a[64:95]}"(wa[2]), "{a[96:127]}"(wa[3]), "{a[128:159]}"(wa[4]), "{a[160:191]}"(wa[5]), "{a[192:223]}"(wa[6]), "{a[224:255]}"(wa[7]), "{v[128:159]}"(wg[0]), "{v[160:191]}"(wg[1]), "{v[192:223]}"(wg[2]), "{v[224:255]}"(wg[3])
        : "memory", "vcc", "scc", "s36", "s37", "s38", "s39", "s40", "s41", "s42", "s43", "s44", "s45", "s46", "s47", "s48", "s49", "s50", "s51", "s52", "s53", "s54", "s55", "s56", "s57", "s58", "s59", "s60", "s61", "s62", "s63", "s64", "s65", "s66", "s67", "s68", "s69", "s70", "s71", "s72", "s73", "s74", "s75", "s76", "s77", "s78", "s79", "s80", "s81", "s82", "s83", "s84", "s85", "s86", "s87", "s88", "s89", "s90", "s91", "s92", "s93", "s94", "s95", "s96", "s97", "s98", "s99");
    } else {
        const int cb = i32;
        unsigned m0v = *(const unsigned*)(p.mem0 + (4 * G + ((l >> 2) & 1) + ((l >> 3) & 2)) * 1024 + cb * 32 + (l >> 5) * 16 + (l & 8) + (l & 3) * 2);
        {
            const v4u sv = mk_srd(p.Wv, 1024 * 4096 * 2), so = mk_srd(p.Wo, 1024 * 4096 * 2);
            const unsigned m0b = w * 32768, f = (g & 1) | ((c >> 1) << 1);
            const unsigned t0 = m0b + (16 * (g >> 1) + 4 * (g & 1) + (c & 1) + 8 * (c >> 1)) * 128 + (f << 5) + (n16 & 3) * 8, t1 = m0b + (16 * g + 4 * (c >> 1) + (c & 1)) * 64 + ((c >> 1) << 5) + (n16 & 3) * 8;
            v16u pa;
            pa[0] = (64 * w + (l >> 3)) * 8192 + vi * 128 + (((l & 7) ^ ((l >> 5) << 1)) << 4); pa[1] = pa[0] ^ 64u;
            pa[2] = (1024 * w + (l >> 2)) * 2048 + cb * 64 + (((l & 3) ^ ((g & 1) << 1)) << 4);
#pragma unroll
            for (int q = 0; q < 4; ++q) pa[3 + q] = t0 ^ (((w + q) & 3) << 5);
            pa[7] = t1; pa[8] = t1 ^ 32u; pa[9] = cal_a; pa[10] = tag; pa[11] = near; pa[12] = 0u; pa[13] = 0u; pa[14] = 0u; pa[15] = 0u;
    asm volatile(
        "s_add_u32 m0, %[m0b], 0\n\t"
        "s_mov_b32 s81, 0\n\t"
        "s_mov_b32 s82, 4\n\t"
        ".Ld0%=:\n\t"
        "buffer_load_dwordx4 v112, %[sv], s81 offen lds\n\t"
        "s_add_u32 m0, m0, 0x400\n\t"
        "s_add_u32 s81, s81, 65536\n\t"
        "buffer_load_dwordx4 v113, %[sv], s81 offen lds\n\t"
        "s_add_u32 m0, m0, 0x400\n\t"
        "s_add_u32 s81, s81, 65536\n\t"
        "s_sub_u32 s82, s82, 1\n\t"
        "s_cmp_lg_u32 s82, 0\n\t"
        "s_cbranch_scc1 .Ld0%=\n\t"
        "s_add_u32 m0, %[m0b], 8192\n\t"
        "s_mov_b32 s81, 2097152\n\t"
        "s_mov_b32 s82, 4\n\t"
        ".Ld1%=:\n\t"
        "buffer_load_dwordx4 v112, %[sv], s81 offen lds\n\t"
        "s_add_u32 m0, m0, 0x400\n\t"
        "s_add_u32 s81, s81, 65536\n\t"
        "buffer_load_dwordx4 v113, %[sv], s81 offen lds\n\t"
        "s_add_u32 m0, m0, 0x400\n\t"
        "s_add_u32 s81, s81, 65536\n\t"
        "s_sub_u32 s82, s82, 1\n\t"
        "s_cmp_lg_u32 s82, 0\n\t"
        "s_cbranch_scc1 .Ld1%=\n\t"
        "s_add_u32 m0, %[m0b], 16384\n\t"
        "s_mov_b32 s81, 4194304\n\t"
        "s_mov_b32 s82, 4\n\t"
        ".Ld2%=:\n\t"
        "buffer_load_dwordx4 v112, %[sv], s81 offen lds\n\t"
        "s_add_u32 m0, m0, 0x400\n\t"
        "s_add_u32 s81, s81, 65536\n\t"
        "buffer_load_dwordx4 v113, %[sv], s81 offen lds\n\t"
        "s_add_u32 m0, m0, 0x400\n\t"
        "s_add_u32 s81, s81, 65536\n\t"
        "s_sub_u32 s82, s82, 1\n\t"
        "s_cmp_lg_u32 s82, 0\n\t"
        "s_cbranch_scc1 .Ld2%=\n\t"
        "s_add_u32 m0, %[m0b], 24576\n\t"
        "s_mov_b32 s81, 6291456\n\t"
        "s_mov_b32 s82, 4\n\t"
        ".Ld3%=:\n\t"
        "buffer_load_dwordx4 v112, %[sv], s81 offen lds\n\t"
        "s_add_u32 m0, m0, 0x400\n\t"
        "s_add_u32 s81, s81, 65536\n\t"
        "buffer_load_dwordx4 v113, %[sv], s81 offen lds\n\t"
        "s_add_u32 m0, m0, 0x400\n\t"
        "s_add_u32 s81, s81, 65536\n\t"
        "s_sub_u32 s82, s82, 1\n\t"
        "s_cmp_lg_u32 s82, 0\n\t"
        "s_cbranch_scc1 .Ld3%=\n\t"
        "s_waitcnt vmcnt(32)\n\t"
        "buffer_store_dwordx2 v[122:123], v121, %[wsd], 0 offen sc1\n\t"
        "s_waitcnt vmcnt(25)\n\t"
        "ds_read_b64_tr_b16 v[128:129], v115 offset:0\n\t"
        "ds_read_b64_tr_b16 v[130:131], v115 offset:256\n\t"
        "ds_read_b64_tr_b16 v[132:133], v115 offset:4096\n\t"
        "ds_read_b64_tr_b16 v[134:135], v115 offset:4352\n\t"
        "ds_read_b64_tr_b16 v[136:137], v116 offset:0\n\t"
        "ds_read_b64_tr_b16 v[138:139], v116 offset:256\n\t"
        "ds_read_b64_tr_b16 v[140:141], v116 offset:4096\n\t"
        "ds_read_b64_tr_b16 v[142:143], v116 offset:4352\n\t"
        "ds_read_b64_tr_b16 v[144:145], v117 offset:0\n\t"
        "ds_read_b64_tr_b16 v[146:147], v117 offset:256\n\t"
        "ds_read_b64_tr_b16 v[148:149], v117 offset:4096\n\t"
        "ds_read_b64_tr_b16 v[150:151], v117 offset:4352\n\t"
        "ds_read_b64_tr_b16 v[152:153], v118 offset:0\n\t"
        "ds_read_b64_tr_b16 v[154:155], v118 offset:256\n\t"
        "ds_read_b64_tr_b16 v[156:157], v118 offset:4096\n\t"
        "ds_read_b64_tr_b16 v[158:159], v118 offset:4352\n\t"
        "s_waitcnt lgkmcnt(0)\n\t"
        "s_add_u32 m0, %[m0b], 0\n\t"
        "s_mov_b32 s81, 0\n\t"
        "s_mov_b32 s82, 4\n\t"
        ".Ld4%=:\n\t"
        "buffer_load_dwordx4 v114, %[so], s81 offen lds\n\t"
        "s_add_u32 m0, m0, 0x400\n\t"
        "s_add_u32 s81, s81, 32768\n\t"
        "buffer_load_dwordx4 v114, %[so], s81 offen lds\n\t"
        "s_add_u32 m0, m0, 0x400\n\t"
        "s_add_u32 s81, s81, 32768\n\t"
        "s_sub_u32 s82, s82, 1\n\t"
        "s_cmp_lg_u32 s82, 0\n\t"
        "s_cbranch_scc1 .Ld4%=\n\t"
        "s_waitcnt vmcnt(25)\n\t"
        "ds_read_b64_tr_b16 v[160:161], v115 offset:8192\n\t"
        "ds_read_b64_tr_b16 v[162:163], v115 offset:8448\n\t"
        "ds_read_b64_tr_b16 v[164:165], v115 offset:12288\n\t"
        "ds_read_b64_tr_b16 v[166:167], v115 offset:12544\n\t"
        "ds_read_b64_tr_b16 v[168:169], v116 offset:8192\n\t"
        "ds_read_b64_tr_b16 v[170:171], v116 offset:8448\n\t"
        "ds_read_b64_tr_b16 v[172:173], v116 offset:12288\n\t"
        "ds_read_b64_tr_b16 v[174:175], v116 offset:12544\n\t"
        "ds_read_b64_tr_b16 v[176:177], v117 offset:8192\n\t"
        "ds_read_b64_tr_b16 v[178:179], v117 offset:8448\n\t"
        "ds_read_b64_tr_b16 v[180:181], v117 offset:12288\n\t"
        "ds_read_b64_tr_b16 v[182:183], v117 offset:12544\n\t"
        "ds_read_b64_tr_b16 v[184:185], v118 offset:8192\n\t"
        "ds_read_b64_tr_b16 v[186:187], v118 offset:8448\n\t"
        "ds_read_b64_tr_b16 v[188:189], v118 offset:12288\n\t"
        "ds_read_b64_tr_b16 v[190:191], v118 offset:12544\n\t"
        "s_waitcnt lgkmcnt(0)\n\t"
        "s_add_u32 m0, %[m0b], 8192\n\t"
        "s_mov_b32 s81, 262144\n\t"
        "s_mov_b32 s82, 4\n\t"
        ".Ld5%=:\n\t"
        "buffer_load_dwordx4 v114, %[so], s81 offen lds\n\t"
        "s_add_u32 m0, m0, 0x400\n\t"
        "s_add_u32 s81, s81, 32768\n\t"
        "buffer_load_dwordx4 v114, %[so], s81 offen lds\n\t"
        "s_add_u32 m0, m0, 0x400\n\t"
        "s_add_u32 s81, s81, 32768\n\t"
        "s_sub_u32 s82, s82, 1\n\t"
        "s_cmp_lg_u32 s82, 0\n\t"
        "s_cbranch_scc1 .Ld5%=\n\t"
        "s_waitcnt vmcnt(25)\n\t"
        "ds_read_b64_tr_b16 v[192:193], v115 offset:16384\n\t"
        "ds_read_b64_tr_b16 v[194:195], v115 offset:16640\n\t"
        "ds_read_b64_tr_b16 v[196:197], v115 offset:20480\n\t"
        "ds_read_b64_tr_b16 v[198:199], v115 offset:20736\n\t"
        "ds_read_b64_tr_b16 v[200:201], v116 offset:16384\n\t"
        "ds_read_b64_tr_b16 v[202:203], v116 offset:16640\n\t"
        "ds_read_b64_tr_b16 v[204:205], v116 offset:20480\n\t"
        "ds_read_b64_tr_b16 v[206:207], v116 offset:20736\n\t"
        "ds_read_b64_tr_b16 v[208:209], v117 offset:16384\n\t"
        "ds_read_b64_tr_b16 v[210:211], v117 offset:16640\n\t"
        "ds_read_b64_tr_b16 v[212:213], v117 offset:20480\n\t"
        "ds_read_b64_tr_b16 v[214:215], v117 offset:20736\n\t"
        "ds_read_b64_tr_b16 v[216:217], v118 offset:16384\n\t"
        "ds_read_b64_tr_b16 v[218:219], v118 offset:16640\n\t"
        "ds_read_b64_tr_b16 v[220:221], v118 offset:20480\n\t"
        "ds_read_b64_tr_b16 v[222:223], v118 offset:20736\n\t"
        "s_waitcnt lgkmcnt(0)\n\t"
        "s_add_u32 m0, %[m0b], 16384\n\t"
        "s_mov_b32 s81, 524288\n\t"
        "s_mov_b32 s82, 4\n\t"
        ".Ld6%=:\n\t"
        "buffer_load_dwordx4 v114, %[so], s81 offen lds\n\t"
        "s_add_u32 m0, m0, 0x400\n\t"
        "s_add_u32 s81, s81, 32768\n\t"
        "buffer_load_dwordx4 v114, %[so], s81 offen lds\n\t"
        "s_add_u32 m0, m0, 0x400\n\t"
        "s_add_u32 s81, s81, 32768\n\t"
        "s_sub_u32 s82, s82, 1\n\t"
        "s_cmp_lg_u32 s82, 0\n\t"
        "s_cbranch_scc1 .Ld6%=\n\t"
        "s_waitcnt vmcnt(25)\n\t"
        "ds_read_b64_tr_b16 v[224:225], v115 offset:24576\n\t"
        "ds_read_b64_tr_b16 v[226:227], v115 offset:24832\n\t"
        "ds_read_b64_tr_b16 v[228:229], v115 offset:28672\n\t"
        "ds_read_b64_tr_b16 v[230:231], v115 offset:28928\n\t"
        "ds_read_b64_tr_b16 v[232:233], v116 offset:24576\n\t"
        "ds_read_b64_tr_b16 v[234:235], v116 offset:24832\n\t"
        "ds_read_b64_tr_b16 v[236:237], v116 offset:28672\n\t"
        "ds_read_b64_tr_b16 v[238:239], v116 offset:28928\n\t"
        "ds_read_b64_tr_b16 v[240:241], v117 offset:24576\n\t"
        "ds_read_b64_tr_b16 v[242:243], v117 offset:24832\n\t"
        "ds_read_b64_tr_b16 v[244:245], v117 offset:28672\n\t"
        "ds_read_b64_tr_b16 v[246:247], v117 offset:28928\n\t"
        "ds_read_b64_tr_b16 v[248:249], v118 offset:24576\n\t"
        "ds_read_b64_tr_b16 v[250:251], v118 offset:24832\n\t"
        "ds_read_b64_tr_b16 v[252:253], v118 offset:28672\n\t"
        "ds_read_b64_tr_b16 v[254:255], v118 offset:28928\n\t"
        "s_waitcnt lgkmcnt(0)\n\t"
        "s_add_u32 m0, %[m0b], 24576\n\t"
        "s_mov_b32 s81, 786432\n\t"
        "s_mov_b32 s82, 4\n\t"
        ".Ld7%=:\n\t"
        "buffer_load_dwordx4 v114, %[so], s81 offen lds\n\t"
        "s_add_u32 m0, m0, 0x400\n\t"
        "s_add_u32 s81, s81, 32768\n\t"
        "buffer_load_dwordx4 v114, %[so], s81 offen lds\n\t"
        "s_add_u32 m0, m0, 0x400\n\t"
        "s_add_u32 s81, s81, 32768\n\t"
        "s_sub_u32 s82, s82, 1\n\t"
        "s_cmp_lg_u32 s82, 0\n\t"
        "s_cbranch_scc1 .Ld7%=\n\t"
        "s_waitcnt vmcnt(24)\n\t"
        "ds_read_b64_tr_b16 a[0:1], v119 offset:0\n\t"
        "ds_read_b64_tr_b16 a[2:3], v119 offset:128\n\t"
        "ds_read_b64_tr_b16 a[4:5], v119 offset:4096\n\t"
        "ds_read_b64_tr_b16 a[6:7], v119 offset:4224\n\t"
        "ds_read_b64_tr_b16 a[8:9], v120 offset:0\n\t"
        "ds_read_b64_tr_b16 a[10:11], v120 offset:128\n\t"
        "ds_read_b64_tr_b16 a[12:13], v120 offset:4096\n\t"
        "ds_read_b64_tr_b16 a[14:15], v120 offset:4224\n\t"
        "ds_read_b64_tr_b16 a[16:17], v119 offset:512\n\t"
        "ds_read_b64_tr_b16 a[18:19], v119 offset:640\n\t"
        "ds_read_b64_tr_b16 a[20:21], v119 offset:4608\n\t"
        "ds_read_b64_tr_b16 a[22:23], v119 offset:4736\n\t"
        "ds_read_b64_tr_b16 a[24:25], v120 offset:512\n\t"
        "ds_read_b64_tr_b16 a[26:27], v120 offset:640\n\t"
        "ds_read_b64_tr_b16 a[28:29], v120 offset:4608\n\t"
        "ds_read_b64_tr_b16 a[30:31], v120 offset:4736\n\t"
        "s_waitcnt lgkmcnt(0)\n\t"
        "s_add_u32 m0, %[m0b], 0\n\t"
        "s_mov_b32 s81, 1048576\n\t"
        "s_mov_b32 s82, 4\n\t"
        ".Ld8%=:\n\t"
        "buffer_load_dwordx4 v114, %[so], s81 offen lds\n\t"
        "s_add_u32 m0, m0, 0x400\n\t"
        "s_add_u32 s81, s81, 32768\n\t"
        "buffer_load_dwordx4 v114, %[so], s81 offen lds\n\t"
        "s_add_u32 m0, m0, 0x400\n\t"
        "s_add_u32 s81, s81, 32768\n\t"
        "s_sub_u32 s82, s82, 1\n\t"
        "s_cmp_lg_u32 s82, 0\n\t"
        "s_cbranch_scc1 .Ld8%=\n\t"
        "s_waitcnt vmcnt(24)\n\t"
        "ds_read_b64_tr_b16 a[32:33], v119 offset:8192\n\t"
        "ds_read_b64_tr_b16 a[34:35], v119 offset:8320\n\t"
        "ds_read_b64_tr_b16 a[36:37], v119 offset:12288\n\t"
        "ds_read_b64_tr_b16 a[38:39], v119 offset:12416\n\t"
        "ds_read_b64_tr_b16 a[40:41], v120 offset:8192\n\t"
        "ds_read_b64_tr_b16 a[42:43], v120 offset:8320\n\t"
        "ds_read_b64_tr_b16 a[44:45], v120 offset:12288\n\t"
        "ds_read_b64_tr_b16 a[46:47], v120 offset:12416\n\t"
        "ds_read_b64_tr_b16 a[48:49], v119 offset:8704\n\t"
        "ds_read_b64_tr_b16 a[50:51], v119 offset:8832\n\t"
        "ds_read_b64_tr_b16 a[52:53], v119 offset:12800\n\t"
        "ds_read_b64_tr_b16 a[54:55], v119 offset:12928\n\t"
        "ds_read_b64_tr_b16 a[56:57], v120 offset:8704\n\t"
        "ds_read_b64_tr_b16 a[58:59], v120 offset:8832\n\t"
        "ds_read_b64_tr_b16 a[60:61], v120 offset:12800\n\t"
        "ds_read_b64_tr_b16 a[62:63], v120 offset:12928\n\t"
        "s_waitcnt lgkmcnt(0)\n\t"
        "s_add_u32 m0, %[m0b], 8192\n\t"
        "s_mov_b32 s81, 1310720\n\t"
        "s_mov_b32 s82, 4\n\t"
        ".Ld9%=:\n\t"
        "buffer_load_dwordx4 v114, %[so], s81 offen lds\n\t"
        "s_add_u32 m0, m0, 0x400\n\t"
        "s_add_u32 s81, s81, 32768\n\t"
        "buffer_load_dwordx4 v114, %[so], s81 offen lds\n\t"
        "s_add_u32 m0, m0, 0x400\n\t"
        "s_add_u32 s81, s81, 32768\n\t"
        "s_sub_u32 s82, s82, 1\n\t"
        "s_cmp_lg_u32 s82, 0\n\t"
        "s_cbranch_scc1 .Ld9%=\n\t"
        "s_waitcnt vmcnt(24)\n\t"
        "ds_read_b64_tr_b16 a[64:65], v119 offset:16384\n\t"
        "ds_read_b64_tr_b16 a[66:67], v119 offset:16512\n\t"
        "ds_read_b64_tr_b16 a[68:69], v119 offset:20480\n\t"
        "ds_read_b64_tr_b16 a[70:71], v119 offset:20608\n\t"
        "ds_read_b64_tr_b16 a[72:73], v120 offset:16384\n\t"
        "ds_read_b64_tr_b16 a[74:75], v120 offset:16512\n\t"
        "ds_read_b64_tr_b16 a[76:77], v120 offset:20480\n\t"
        "ds_read_b64_tr_b16 a[78:79], v120 offset:20608\n\t"
        "ds_read_b64_tr_b16 a[80:81], v119 offset:16896\n\t"
        "ds_read_b64_tr_b16 a[82:83], v119 offset:17024\n\t"
        "ds_read_b64_tr_b16 a[84:85], v119 offset:20992\n\t"
        "ds_read_b64_tr_b16 a[86:87], v119 offset:21120\n\t"
        "ds_read_b64_tr_b16 a[88:89], v120 offset:16896\n\t"
        "ds_read_b64_tr_b16 a[90:91], v120 offset:17024\n\t"
        "ds_read_b64_tr_b16 a[92:93], v120 offset:20992\n\t"
        "ds_read_b64_tr_b16 a[94:95], v120 offset:21120\n\t"
        "s_waitcnt lgkmcnt(0)\n\t"
        "s_add_u32 m0, %[m0b], 16384\n\t"
        "s_mov_b32 s81, 1572864\n\t"
        "s_mov_b32 s82, 4\n\t"
        ".Ld10%=:\n\t"
        "buffer_load_dwordx4 v114, %[so], s81 offen lds\n\t"
        "s_add_u32 m0, m0, 0x400\n\t"
        "s_add_u32 s81, s81, 32768\n\t"
        "buffer_load_dwordx4 v114, %[so], s81 offen lds\n\t"
        "s_add_u32 m0, m0, 0x400\n\t"
        "s_add_u32 s81, s81, 32768\n\t"
        "s_sub_u32 s82, s82, 1\n\t"
        "s_cmp_lg_u32 s82, 0\n\t"
        "s_cbranch_scc1 .Ld10%=\n\t"
        "s_waitcnt vmcnt(24)\n\t"
        "ds_read_b64_tr_b16 a[96:97], v119 offset:24576\n\t"
        "ds_read_b64_tr_b16 a[98:99], v119 offset:24704\n\t"
        "ds_read_b64_tr_b16 a[100:101], v119 offset:28672\n\t"
        "ds_read_b64_tr_b16 a[102:103], v119 offset:28800\n\t"
        "ds_read_b64_tr_b16 a[104:105], v120 offset:24576\n\t"
        "ds_read_b64_tr_b16 a[106:107], v120 offset:24704\n\t"
        "ds_read_b64_tr_b16 a[108:109], v120 offset:28672\n\t"
        "ds_read_b64_tr_b16 a[110:111], v120 offset:28800\n\t"
        "ds_read_b64_tr_b16 a[112:113], v119 offset:25088\n\t"
        "ds_read_b64_tr_b16 a[114:115], v119 offset:25216\n\t"
        "ds_read_b64_tr_b16 a[116:117], v119 offset:29184\n\t"
        "ds_read_b64_tr_b16 a[118:119], v119 offset:29312\n\t"
        "ds_read_b64_tr_b16 a[120:121], v120 offset:25088\n\t"
        "ds_read_b64_tr_b16 a[122:123], v120 offset:25216\n\t"
        "ds_read_b64_tr_b16 a[124:125], v120 offset:29184\n\t"
        "ds_read_b64_tr_b16 a[126:127], v120 offset:29312\n\t"
        "s_waitcnt lgkmcnt(0)\n\t"
        "s_add_u32 m0, %[m0b], 24576\n\t"
        "s_mov_b32 s81, 1835008\n\t"
        "s_mov_b32 s82, 4\n\t"
        ".Ld11%=:\n\t"
        "buffer_load_dwordx4 v114, %[so], s81 offen lds\n\t"
        "s_add_u32 m0, m0, 0x400\n\t"
        "s_add_u32 s81, s81, 32768\n\t"
        "buffer_load_dwordx4 v114, %[so], s81 offen lds\n\t"
        "s_add_u32 m0, m0, 0x400\n\t"
        "s_add_u32 s81, s81, 32768\n\t"
        "s_sub_u32 s82, s82, 1\n\t"
        "s_cmp_lg_u32 s82, 0\n\t"
        "s_cbranch_scc1 .Ld11%=\n\t"
        "s_waitcnt vmcnt(24)\n\t"
        "ds_read_b64_tr_b16 a[128:129], v119 offset:0\n\t"
        "ds_read_b64_tr_b16 a[130:131], v119 offset:128\n\t"
        "ds_read_b64_tr_b16 a[132:133], v119 offset:4096\n\t"
        "ds_read_b64_tr_b16 a[134:135], v119 offset:4224\n\t"
        "ds_read_b64_tr_b16 a[136:137], v120 offset:0\n\t"
        "ds_read_b64_tr_b16 a[138:139], v120 offset:128\n\t"
        "ds_read_b64_tr_b16 a[140:141], v120 offset:4096\n\t"
        "ds_read_b64_tr_b16 a[142:143], v120 offset:4224\n\t"
        "ds_read_b64_tr_b16 a[144:145], v119 offset:512\n\t"
        "ds_read_b64_tr_b16 a[146:147], v119 offset:640\n\t"
        "ds_read_b64_tr_b16 a[148:149], v119 offset:4608\n\t"
        "ds_read_b64_tr_b16 a[150:151], v119 offset:4736\n\t"
        "ds_read_b64_tr_b16 a[152:153], v120 offset:512\n\t"
        "ds_read_b64_tr_b16 a[154:155], v120 offset:640\n\t"
        "ds_read_b64_tr_b16 a[156:157], v120 offset:4608\n\t"
        "ds_read_b64_tr_b16 a[158:159], v120 offset:4736\n\t"
        "s_waitcnt lgkmcnt(0)\n\t"
        "s_waitcnt vmcnt(16)\n\t"
        "ds_read_b64_tr_b16 a[160:161], v119 offset:8192\n\t"
        "ds_read_b64_tr_b16 a[162:163], v119 offset:8320\n\t"
        "ds_read_b64_tr_b16 a[164:165], v119 offset:12288\n\t"
        "ds_read_b64_tr_b16 a[166:167], v119 offset:12416\n\t"
        "ds_read_b64_tr_b16 a[168:169], v120 offset:8192\n\t"
        "ds_read_b64_tr_b16 a[170:171], v120 offset:8320\n\t"
        "ds_read_b64_tr_b16 a[172:173], v120 offset:12288\n\t"
        "ds_read_b64_tr_b16 a[174:175], v120 offset:12416\n\t"
        "ds_read_b64_tr_b16 a[176:177], v119 offset:8704\n\t"
        "ds_read_b64_tr_b16 a[178:179], v119 offset:8832\n\t"
        "ds_read_b64_tr_b16 a[180:181], v119 offset:12800\n\t"
        "ds_read_b64_tr_b16 a[182:183], v119 offset:12928\n\t"
        "ds_read_b64_tr_b16 a[184:185], v120 offset:8704\n\t"
        "ds_read_b64_tr_b16 a[186:187], v120 offset:8832\n\t"
        "ds_read_b64_tr_b16 a[188:189], v120 offset:12800\n\t"
        "ds_read_b64_tr_b16 a[190:191], v120 offset:12928\n\t"
        "s_waitcnt lgkmcnt(0)\n\t"
        "s_waitcnt vmcnt(8)\n\t"
        "ds_read_b64_tr_b16 a[192:193], v119 offset:16384\n\t"
        "ds_read_b64_tr_b16 a[194:195], v119 offset:16512\n\t"
        "ds_read_b64_tr_b16 a[196:197], v119 offset:20480\n\t"
        "ds_read_b64_tr_b16 a[198:199], v119 offset:20608\n\t"
        "ds_read_b64_tr_b16 a[200:201], v120 offset:16384\n\t"
        "ds_read_b64_tr_b16 a[202:203], v120 offset:16512\n\t"
        "ds_read_b64_tr_b16 a[204:205], v120 offset:20480\n\t"
        "ds_read_b64_tr_b16 a[206:207], v120 offset:20608\n\t"
        "ds_read_b64_tr_b16 a[208:209], v119 offset:16896\n\t"
        "ds_read_b64_tr_b16 a[210:211], v119 offset:17024\n\t"
        "ds_read_b64_tr_b16 a[212:213], v119 offset:20992\n\t"
        "ds_read_b64_tr_b16 a[214:215], v119 offset:21120\n\t"
        "ds_read_b64_tr_b16 a[216:217], v120 offset:16896\n\t"
        "ds_read_b64_tr_b16 a[218:219], v120 offset:17024\n\t"
        "ds_read_b64_tr_b16 a[220:221], v120 offset:20992\n\t"
        "ds_read_b64_tr_b16 a[222:223], v120 offset:21120\n\t"
        "s_waitcnt lgkmcnt(0)\n\t"
        "s_waitcnt vmcnt(0)\n\t"
        "ds_read_b64_tr_b16 a[224:225], v119 offset:24576\n\t"
        "ds_read_b64_tr_b16 a[226:227], v119 offset:24704\n\t"
        "ds_read_b64_tr_b16 a[228:229], v119 offset:28672\n\t"
        "ds_read_b64_tr_b16 a[230:231], v119 offset:28800\n\t"
        "ds_read_b64_tr_b16 a[232:233], v120 offset:24576\n\t"
        "ds_read_b64_tr_b16 a[234:235], v120 offset:24704\n\t"
        "ds_read_b64_tr_b16 a[236:237], v120 offset:28672\n\t"
        "ds_read_b64_tr_b16 a[238:239], v120 offset:28800\n\t"
        "ds_read_b64_tr_b16 a[240:241], v119 offset:25088\n\t"
        "ds_read_b64_tr_b16 a[242:243], v119 offset:25216\n\t"
        "ds_read_b64_tr_b16 a[244:245], v119 offset:29184\n\t"
        "ds_read_b64_tr_b16 a[246:247], v119 offset:29312\n\t"
        "ds_read_b64_tr_b16 a[248:249], v120 offset:25088\n\t"
        "ds_read_b64_tr_b16 a[250:251], v120 offset:25216\n\t"
        "ds_read_b64_tr_b16 a[252:253], v120 offset:29184\n\t"
        "ds_read_b64_tr_b16 a[254:255], v120 offset:29312\n\t"
        "s_waitcnt lgkmcnt(0)"
        : "={a[0:31]}"(wa[0]), "={a[32:63]}"(wa[1]), "={a[64:95]}"(wa[2]), "={a[96:127]}"(wa[3]), "={a[128:159]}"(wa[4]), "={a[160:191]}"(wa[5]), "={a[192:223]}"(wa[6]), "={a[224:255]}"(wa[7]), "={v[128:159]}"(wg[0]), "={v[160:191]}"(wg[1]), "={v[192:223]}"(wg[2]), "={v[224:255]}"(wg[3])
        : [sv] "s"(sv), [so] "s"(so), [wsd] "s"(wsd), [m0b] "s"(m0b), "{v[112:127]}"(pa)
        : "memory", "scc", "m0", "s80", "s81", "s82");
        }
        asm volatile("" : "+v"(cpf), "+v"(m0v));
        __syncthreads();
        tid = otid(); l = tid & 63; n16 = l & 15; g = l >> 4;
        unsigned nm; if (!votes(nm)) return false;
        const unsigned nW = (nm & 1u) << 13, nA = nW + x * 32768, nS = nW + 6 * 16384 + x * 1024 - w * 1024, nC = 32 * 16384 + ((nm & 2u) << 12);
        const unsigned b0 = (64 + (h >> 2)) * 16384 + ((nm & 4u) << 11) + (h & 3) * 2048, b1 = b0 + 8 * 16384;
        const unsigned pob = w == 0 ? nA + (i32 >> 4) * 16384 + (i32 & 15) * 512 : w == 1 ? nC + (G * 4 + (cb >> 3)) * 16384 + (cb & 7) * 1024 - 1024 : (w == 2 && i32 == 0) ? nS : OOB;
        const unsigned aw = w < 3 ? (unsigned)(w * 32768 + (cb >> 4) * 16384 + ((cb >> 1) & 7) * 1024 + ((cb & 1) * 2 + (l >> 5)) * 256 + G * 128 + (l & 31) * 4) : OOB;
        v16u pc = {(unsigned)tid * 16, (n16 & 2) ? 0xEEEEu : 0x4444u, (unsigned)((vi & 1) * 1024 + w * 256 + n16 * 16 + g * 4), (unsigned)((G * 4 + w) * 16384 + l * 16), aw,
                   tid < 3 ? (unsigned)(6 * 16384 + l * 1024 + (32 + vi) * 8) : OOB, tid == 192 ? pr_a : OOB, (unsigned)(8192 + w * 512 + (g & 1) * 256 + (g >> 1) * 64 + n16 * 4),
                   (unsigned)((((l >> 2) & 1) + ((l >> 3) & 2)) * 128 + (l >> 5) * 64 + (l & 8) * 4 + (l & 3) * 8), (unsigned)(2048 + (64 * (vi & 1) + 16 * w + n16) * 16 + g * 4),
                   (unsigned)(w * 2048 + l * 8), (unsigned)(((w + 1) & 3) * 2048 + 512 + l * 8), (unsigned)(((w + 2) & 3) * 2048 + 1024 + l * 8), (unsigned)(((w + 3) & 3) * 2048 + 1536 + l * 8), 0u, g ? OOB : (unsigned)((vi & 1) * 1024 + w * 256 + n16 * 16)};
        m0v = min(m0v, 0xFFFFFFFEu);
        st4(rh, aw + nW, m0v);
        if (tid < 2) ((lu32*)(size_t)L_FL)[tid] = ~0u;
        __syncthreads();
    asm volatile(
        "rec_v_code:\n\t"
        "s_mov_b32 s36, 0xf0000000\n\t"
        "s_mov_b32 s38, 0xff00ff00\n\t"
        "s_mov_b32 s39, 0xff00ff00\n\t"
        "s_mov_b32 s40, 0\n\t"
        "s_mov_b32 s37, 0\n\t"
        "s_brev_b32 s54, 1\n\t"
        "s_mov_b32 s46, %c[iD1]\n\t"
        "s_mov_b32 s45, %c[iD1]\n\t"
        "s_mov_b32 s47, %c[iD2]\n\t"
        "s_mov_b32 s48, 1\n\t"
        "s_mov_b32 s50, %c[LC]\n\t"
        "s_mov_b32 s51, %c[LA]\n\t"
        "s_mov_b32 s52, %c[E1]\n\t"
        "s_mov_b32 s53, %c[E2]\n\t"
        "s_mov_b32 s43, 0\n\t"
        "s_mov_b32 s61, %[b0]\n\t"
        "s_mov_b32 s62, %[b1]\n\t"
        "s_mov_b32 s75, 31744\n\t"
        "s_and_b32 s80, s40, 3\n\t"
        "s_lshl_b32 s80, s80, 17\n\t"
        "s_add_u32 s57, s80, %[nA]\n\t"
        "s_add_u32 s58, s57, 0x1000\n\t"
        "s_add_u32 s59, s57, 0x4000\n\t"
        "s_add_u32 s60, s57, 0x5000\n\t"
        "v_mov_b32_e32 v92, -1\n\t"
        "v_mov_b32_e32 v93, -1\n\t"
        "v_mov_b32_e32 v94, -1\n\t"
        "v_mov_b32_e32 v95, -1\n\t"
        "v_mov_b32_e32 v48, 0\n\t"
        "v_mov_b32_e32 v49, 0\n\t"
        "v_mov_b32_e32 v50, 0\n\t"
        "v_mov_b32_e32 v51, 0\n\t"
        "v_mov_b32_e32 v52, 0\n\t"
        "v_mov_b32_e32 v53, 0\n\t"
        "v_mov_b32_e32 v54, 0\n\t"
        "v_mov_b32_e32 v55, 0\n\t"
        "v_mov_b32_e32 v56, 0\n\t"
        "v_mov_b32_e32 v57, 0\n\t"
        "v_mov_b32_e32 v58, 0\n\t"
        "v_mov_b32_e32 v59, 0\n\t"
        "v_mov_b32_e32 v60, 0\n\t"
        "v_mov_b32_e32 v61, 0\n\t"
        "v_mov_b32_e32 v62, 0\n\t"
        "v_mov_b32_e32 v63, 0\n\t"
        "s_memrealtime s[76:77]\n\t"
        "s_mov_b32 s67, %[nW]\n\t"
        "v_mov_b32_e32 v86, 64\n\t"
        "s_mov_b32 s61, %[b1]\n\t"
        "s_mov_b32 s62, %[b0]\n\t"
        "s_sub_u32 s75, s75, 16\n\t"
        "s_waitcnt lgkmcnt(0)\n\t"
        "s_mov_b32 s41, s76\n\t"
        ".Lloop%=:\n\t"
        "s_mov_b32 s97, 0\n\t"
        "s_movk_i32 s96, 0x1000\n\t"
        ".Lwa%=:\n\t"
        "s_memrealtime s[98:99]\n\t"
        "s_addk_i32 s96, -1\n\t"
        "s_cmp_lt_i32 s96, 0\n\t"
        "s_cbranch_scc1 .Lta%=\n\t"
        "s_waitcnt lgkmcnt(0)\n\t"
        "s_sub_i32 s98, s98, s41\n\t"
        "s_cmp_lt_i32 s98, %c[iFT]\n\t"
        "s_cbranch_scc1 .Lwa%=\n\t"
        "s_cmp_ge_i32 s98, 0\n\t"
        "s_cbranch_scc1 .Lxa%=\n\t"
        ".Lqa%=:\n\t"
        "s_nop %c[iFN]\n\t"
        "s_addk_i32 s98, 1\n\t"
        "s_cmp_lt_i32 s98, 0\n\t"
        "s_cbranch_scc1 .Lqa%=\n\t"
        ".Lxa%=:\n\t"
        "buffer_load_dwordx4 v[0:3], v112, %[srd], s57 offen sc1\n\t"
        "s_nop 7\n\t"
        "buffer_load_dwordx4 v[4:7], v112, %[srd], s58 offen sc1\n\t"
        "s_nop 7\n\t"
        "buffer_load_dwordx4 v[8:11], v112, %[srd], s59 offen sc1\n\t"
        "s_nop 7\n\t"
        "buffer_load_dwordx4 v[12:15], v112, %[srd], s60 offen sc1\n\t"
        "v_mov_b32_e32 v80, 30720\n\t"
        "ds_read_b64 v[80:81], v80\n\t"
        "s_waitcnt lgkmcnt(0)\n\t"
        "v_readfirstlane_b32 s80, v80\n\t"
        "v_readfirstlane_b32 s81, v81\n\t"
        "s_cmp_eq_u32 s80, s40\n\t"
        "s_cselect_b32 s84, 1, 0\n\t"
        "s_cmp_eq_u32 s81, s40\n\t"
        "s_cselect_b32 s81, 4, 0\n\t"
        "s_or_b32 s84, s84, s81\n\t"
        "v_mov_b32_e32 v106, s76\n\t"
        "v_mov_b32_e32 v107, s84\n\t"
        "buffer_store_dwordx2 v[106:107], v117, %[srd], s67 offen sc1\n\t"
        "s_and_b32 s80, s40, 3\n\t"
        "s_lshl_b32 s80, s80, 17\n\t"
        "s_add_u32 s63, s80, %[nC]\n\t"
        "s_add_u32 s66, s80, %[nS]\n\t"
        "s_add_u32 s81, s80, 0x20000\n\t"
        "s_and_b32 s81, s81, 0x60000\n\t"
        "s_add_u32 s67, s81, %[nW]\n\t"
        "s_cmp_eq_u32 %[w], 2\n\t"
        "s_cselect_b32 s81, s80, s81\n\t"
        "s_xor_b32 s81, s81, 0x40000\n\t"
        "s_add_u32 s68, s81, %[pob]\n\t"
        "s_lshl_b32 s72, s40, 17\n\t"
        "s_add_u32 s72, s72, %[kvo]\n\t"
        "s_mov_b32 s80, s61\n\t"
        "s_mov_b32 s61, s62\n\t"
        "s_mov_b32 s62, s80\n\t"
        ".Lgo%=:\n\t"
        "s_waitcnt vmcnt(4)\n\t"
        "v_smfmac_f32_16x16x64_bf16 v[52:55], v[0:3], v[136:143], v113\n\t"
        "v_smfmac_f32_16x16x64_bf16 v[56:59], v[0:3], v[144:151], v113\n\t"
        "v_smfmac_f32_16x16x64_bf16 v[60:63], v[0:3], v[152:159], v113\n\t"
        "v_smfmac_f32_16x16x64_bf16 v[48:51], v[0:3], v[128:135], v113\n\t"
        "s_waitcnt vmcnt(3)\n\t"
        "v_smfmac_f32_16x16x64_bf16 v[52:55], v[4:7], v[168:175], v113\n\t"
        "v_smfmac_f32_16x16x64_bf16 v[56:59], v[4:7], v[176:183], v113\n\t"
        "v_smfmac_f32_16x16x64_bf16 v[60:63], v[4:7], v[184:191], v113\n\t"
        "v_smfmac_f32_16x16x64_bf16 v[48:51], v[4:7], v[160:167], v113\n\t"
        "s_waitcnt vmcnt(2)\n\t"
        "v_smfmac_f32_16x16x64_bf16 v[52:55], v[8:11], v[200:207], v113\n\t"
        "v_smfmac_f32_16x16x64_bf16 v[56:59], v[8:11], v[208:215], v113\n\t"
        "v_smfmac_f32_16x16x64_bf16 v[60:63], v[8:11], v[216:223], v113\n\t"
        "v_smfmac_f32_16x16x64_bf16 v[48:51], v[8:11], v[192:199], v113\n\t"
        "s_waitcnt vmcnt(1)\n\t"
        "v_smfmac_f32_16x16x64_bf16 v[52:55], v[12:15], v[232:239], v113\n\t"
        "v_smfmac_f32_16x16x64_bf16 v[56:59], v[12:15], v[240:247], v113\n\t"
        "v_smfmac_f32_16x16x64_bf16 v[60:63], v[12:15], v[248:255], v113\n\t"
        "v_smfmac_f32_16x16x64_bf16 v[48:51], v[12:15], v[224:231], v113\n\t"
        "s_nop 1\n\t"
        "v_mov_b32_e32 v104, v104\n\t"
        "s_nop 1\n\t"
        "v_add_f32_e32 v98, v52, v54\n\t"
        "v_add_f32_e32 v99, v53, v55\n\t"
        "ds_write_b64 v123, v[98:99]\n\t"
        "v_add_f32_e32 v100, v56, v58\n\t"
        "v_add_f32_e32 v101, v57, v59\n\t"
        "ds_write_b64 v124, v[100:101]\n\t"
        "v_add_f32_e32 v102, v60, v62\n\t"
        "v_add_f32_e32 v103, v61, v63\n\t"
        "ds_write_b64 v125, v[102:103]\n\t"
        "v_pk_add_f32 v[96:97], v[48:49], v[50:51]\n\t"
        "v_cmp_u_f32_e32 vcc, v96, v97\n\t"
        "s_nop 2\n\t"
        "v_mov_b32_e32 v104, v104\n\t"
        "s_cbranch_vccnz .Lfa%=\n\t"
        ".Lra%=:\n\t"
        "v_mov_b32_e32 v104, v104\n\t"
        "s_nop 2\n\t"
        "v_mov_b32_e32 v104, v104\n\t"
        "s_nop 2\n\t"
        "v_mov_b32_e32 v104, v104\n\t"
        "s_nop 2\n\t"
        "v_mov_b32_e32 v104, v104\n\t"
        "s_waitcnt lgkmcnt(0)\n\t"
        "s_barrier\n\t"
        "v_mov_b32_e32 v104, v104\n\t"
        "ds_read_b64 v[98:99], v122 offset:512\n\t"
        "ds_read_b64 v[100:101], v122 offset:1024\n\t"
        "ds_read_b64 v[102:103], v122 offset:1536\n\t"
        "v_mov_b32_e32 v104, v104\n\t"
        "s_nop 2\n\t"
        "v_mov_b32_e32 v104, v104\n\t"
        "s_nop 2\n\t"
        "v_mov_b32_e32 v104, v104\n\t"
        "s_waitcnt lgkmcnt(2)\n\t"
        "v_pk_add_f32 v[96:97], v[96:97], v[98:99]\n\t"
        "s_waitcnt lgkmcnt(0)\n\t"
        "v_pk_add_f32 v[100:101], v[100:101], v[102:103]\n\t"
        "v_pk_add_f32 v[96:97], v[96:97], v[100:101]\n\t"
        "v_cvt_pk_bf16_f32 v80, v96, v97\n\t"
        "v_min_u32_e32 v80, -2, v80\n\t"
        "buffer_store_dword v80, v114, %[srd], s61 offen sc1\n\t"
        "s_cmp_eq_u32 s40, 63\n\t"
        "s_cbranch_scc1 .Lend%=\n\t"
        "v_mov_b32_e32 v64, 0\n\t"
        "v_mov_b32_e32 v65, 0\n\t"
        "v_mov_b32_e32 v66, 0\n\t"
        "v_mov_b32_e32 v67, 0\n\t"
        "v_mov_b32_e32 v68, 0\n\t"
        "v_mov_b32_e32 v69, 0\n\t"
        "v_mov_b32_e32 v70, 0\n\t"
        "v_mov_b32_e32 v71, 0\n\t"
        "v_mov_b32_e32 v72, 0\n\t"
        "v_mov_b32_e32 v73, 0\n\t"
        "v_mov_b32_e32 v74, 0\n\t"
        "v_mov_b32_e32 v75, 0\n\t"
        "v_mov_b32_e32 v76, 0\n\t"
        "v_mov_b32_e32 v77, 0\n\t"
        "v_mov_b32_e32 v78, 0\n\t"
        "v_mov_b32_e32 v79, 0\n\t"
        "s_add_u32 s64, s63, 0x1000\n\t"
        "s_sleep %c[VSLP]\n\t"
        "buffer_load_dwordx4 v[88:91], v112, %[srd], s66 offen sc1\n\t"
        "s_cmp_eq_u32 %[w], 0\n\t"
        "s_cselect_b32 exec_hi, 0, -1\n\t"
        "buffer_store_dwordx4 v[92:95], v112, %[srd], s68 offen sc1\n\t"
        "s_mov_b32 exec_hi, -1\n\t"
        "s_waitcnt lgkmcnt(0)\n\t"
        "v_mov_b32_e32 v48, 0\n\t"
        "v_mov_b32_e32 v49, 0\n\t"
        "v_mov_b32_e32 v50, 0\n\t"
        "v_mov_b32_e32 v51, 0\n\t"
        "v_mov_b32_e32 v52, 0\n\t"
        "v_mov_b32_e32 v53, 0\n\t"
        "v_mov_b32_e32 v54, 0\n\t"
        "v_mov_b32_e32 v55, 0\n\t"
        "v_mov_b32_e32 v56, 0\n\t"
        "v_mov_b32_e32 v57, 0\n\t"
        "v_mov_b32_e32 v58, 0\n\t"
        "v_mov_b32_e32 v59, 0\n\t"
        "v_mov_b32_e32 v60, 0\n\t"
        "v_mov_b32_e32 v61, 0\n\t"
        "v_mov_b32_e32 v62, 0\n\t"
        "v_mov_b32_e32 v63, 0\n\t"
        "s_add_u32 s81, s40, 1\n\t"
        "s_and_b32 s80, s81, 3\n\t"
        "s_lshl_b32 s80, s80, 17\n\t"
        "s_add_u32 s57, s80, %[nA]\n\t"
        "s_add_u32 s58, s57, 0x1000\n\t"
        "s_add_u32 s59, s57, 0x4000\n\t"
        "s_add_u32 s60, s57, 0x5000\n\t"
        "s_waitcnt vmcnt(1)\n\t"
        "s_mov_b32 s97, 0\n\t"
        ".Lsr%=:\n\t"
        "s_mov_b32 exec_hi, 0xffff\n\t"
        "v_subrev_u32_e32 v4, s41, v88\n\t"
        "v_subrev_u32_e32 v5, s41, v90\n\t"
        "v_max_i32_e32 v4, v4, v5\n\t"
        "v_max_u32_e32 v5, v89, v91\n\t"
        "v_cmp_eq_u32_e32 vcc, -1, v5\n\t"
        "v_max_i32_dpp v4, v4, v4 quad_perm:[1,0,3,2] row_mask:0xf bank_mask:0xf\n\t"
        "v_or_b32_e32 v6, v89, v91\n\t"
        "s_nop 0\n\t"
        "v_max_i32_dpp v4, v4, v4 quad_perm:[2,3,0,1] row_mask:0xf bank_mask:0xf\n\t"
        "s_nop 1\n\t"
        "v_max_i32_dpp v4, v4, v4 row_ror:4 row_mask:0xf bank_mask:0xf\n\t"
        "s_nop 1\n\t"
        "v_max_i32_dpp v4, v4, v4 row_ror:8 row_mask:0xf bank_mask:0xf\n\t"
        "s_cbranch_vccnz .Lsf%=\n\t"
        "v_readlane_b32 s85, v4, 0\n\t"
        "v_readlane_b32 s81, v4, 16\n\t"
        "v_readlane_b32 s56, v4, 32\n\t"
        "v_and_b32_e32 v5, 1, v6\n\t"
        "v_cmp_ne_u32_e64 s[86:87], 0, v5\n\t"
        "v_and_b32_e32 v5, 2, v6\n\t"
        "v_cmp_ne_u32_e64 s[88:89], 0, v5\n\t"
        "v_and_b32_e32 v5, 4, v6\n\t"
        "v_cmp_ne_u32_e64 s[90:91], 0, v5\n\t"
        "s_max_i32 s56, s56, s81\n\t"
        "s_add_u32 s92, s85, s41\n\t"
        "s_sub_u32 s92, s92, s43\n\t"
        "s_add_i32 s93, s56, s47\n\t"
        "s_mov_b32 s44, s46\n\t"
        "s_mov_b32 s46, s45\n\t"
        "s_mov_b32 s94, s48\n\t"
        "s_mov_b32 s43, s41\n\t"
        "s_cmp_gt_i32 s56, s54\n\t"
        "s_cbranch_scc1 .Lmv%=\n\t"
        "s_mov_b32 s37, 0\n\t"
        "s_mov_b32 s48, 0\n\t"
        "s_mov_b32 s95, 0\n\t"
        ".Lsd%=:\n\t"
        "s_mov_b32 exec_hi, -1\n\t"
        "s_add_u32 s42, s43, s46\n\t"
        "s_mov_b32 s97, 0\n\t"
        "s_movk_i32 s96, 0x1000\n\t"
        ".Lwc%=:\n\t"
        "s_memrealtime s[98:99]\n\t"
        "s_addk_i32 s96, -1\n\t"
        "s_cmp_lt_i32 s96, 0\n\t"
        "s_cbranch_scc1 .Ltc%=\n\t"
        "s_waitcnt lgkmcnt(0)\n\t"
        "s_sub_i32 s98, s98, s42\n\t"
        "s_cmp_lt_i32 s98, %c[iFT]\n\t"
        "s_cbranch_scc1 .Lwc%=\n\t"
        "s_cmp_ge_i32 s98, 0\n\t"
        "s_cbranch_scc1 .Lxc%=\n\t"
        ".Lqc%=:\n\t"
        "s_nop %c[iFN]\n\t"
        "s_addk_i32 s98, 1\n\t"
        "s_cmp_lt_i32 s98, 0\n\t"
        "s_cbranch_scc1 .Lqc%=\n\t"
        ".Lxc%=:\n\t"
        "buffer_load_dwordx4 v[16:19], v115, %[srd], s63 offen offset:0 sc1\n\t"
        "s_cmp_ge_u32 s40, %c[iCT0]\n\t"
        "s_cselect_b32 s85, 1, 0\n\t"
        "s_cmp_lg_u64 s[86:87], 0\n\t"
        "s_cselect_b32 s86, s85, 0\n\t"
        "s_cmp_lg_u64 s[88:89], 0\n\t"
        "s_cselect_b32 s87, s85, 0\n\t"
        "s_cmp_lg_u64 s[90:91], 0\n\t"
        "s_cselect_b32 s88, s85, 0\n\t"
        "buffer_load_dwordx4 v[20:23], v115, %[srd], s63 offen offset:1024 sc1\n\t"
        "s_or_b32 s80, s94, s86\n\t"
        "s_or_b32 s80, s80, s87\n\t"
        "s_andn2_b32 s90, s85, s80\n\t"
        "s_cmp_le_i32 s92, s44\n\t"
        "s_cselect_b32 s80, s85, 0\n\t"
        "s_andn2_b32 s91, s80, s88\n\t"
        "s_and_b32 s80, s40, %c[LDEC]\n\t"
        "s_cselect_b32 s89, 0, 1\n\t"
        "buffer_load_dwordx4 v[24:27], v115, %[srd], s63 offen offset:2048 sc1\n\t"
        "s_mov_b32 s80, s88\n\t"
        "s_add_i32 s81, s92, s50\n\t"
        "s_sub_i32 s81, s81, 4\n\t"
        "s_cmp_le_i32 s81, s44\n\t"
        "s_cselect_b32 s80, s80, 0\n\t"
        "s_cmp_lt_i32 s50, %c[LC24]\n\t"
        "s_cselect_b32 s80, s80, 0\n\t"
        "s_lshl2_add_u32 s50, s80, s50\n\t"
        "buffer_load_dwordx4 v[28:31], v115, %[srd], s63 offen offset:3072 sc1\n\t"
        "s_sub_i32 s50, s50, s89\n\t"
        "s_max_i32 s50, s50, %c[LC]\n\t"
        "s_lshl_b32 s80, s92, 1\n\t"
        "s_sub_i32 s81, s80, s52\n\t"
        "s_ashr_i32 s82, s81, %c[UPSH]\n\t"
        "s_max_i32 s82, s82, %c[TRUP]\n\t"
        "s_add_i32 s82, s82, s52\n\t"
        "s_min_i32 s82, s82, s80\n\t"
        "buffer_load_dwordx4 v[32:35], v115, %[srd], s64 offen offset:0 sc1\n\t"
        "s_ashr_i32 s83, s81, 1\n\t"
        "s_min_i32 s83, s83, -%c[TRDN]\n\t"
        "s_add_i32 s83, s83, s52\n\t"
        "s_max_i32 s83, s83, s80\n\t"
        "s_cmp_gt_i32 s81, 0\n\t"
        "s_cselect_b32 s82, s82, s83\n\t"
        "s_cmp_lg_u32 s90, 0\n\t"
        "s_cselect_b32 s52, s82, s52\n\t"
        "buffer_load_dwordx4 v[36:39], v115, %[srd], s64 offen offset:1024 sc1\n\t"
        "s_add_i32 s80, s52, 1\n\t"
        "s_ashr_i32 s80, s80, 1\n\t"
        "s_add_i32 s45, s80, s50\n\t"
        "s_andn2_b32 s80, s86, s94\n\t"
        "s_cmp_lt_i32 s51, %c[LA24]\n\t"
        "s_cselect_b32 s80, s80, 0\n\t"
        "s_lshl2_add_u32 s51, s80, s51\n\t"
        "s_sub_i32 s51, s51, s89\n\t"
        "buffer_load_dwordx4 v[40:43], v115, %[srd], s64 offen offset:2048 sc1\n\t"
        "s_max_i32 s51, s51, %c[LA]\n\t"
        "s_lshl_b32 s80, s93, 1\n\t"
        "s_sub_i32 s81, s80, s53\n\t"
        "s_ashr_i32 s82, s81, %c[UPSH]\n\t"
        "s_max_i32 s82, s82, %c[TRUP]\n\t"
        "s_add_i32 s82, s82, s53\n\t"
        "s_min_i32 s82, s82, s80\n\t"
        "s_ashr_i32 s83, s81, 1\n\t"
        "buffer_load_dwordx4 v[44:47], v115, %[srd], s64 offen offset:3072 sc1\n\t"
        "s_min_i32 s83, s83, -%c[TRDN]\n\t"
        "s_add_i32 s83, s83, s53\n\t"
        "s_max_i32 s83, s83, s80\n\t"
        "s_cmp_gt_i32 s81, 0\n\t"
        "s_cselect_b32 s82, s82, s83\n\t"
        "s_cmp_lg_u32 s91, 0\n\t"
        "s_cselect_b32 s53, s82, s53\n\t"
        "s_add_i32 s80, s53, 1\n\t"
        "s_ashr_i32 s80, s80, 1\n\t"
        "s_add_i32 s47, s80, s51\n\t"
        "s_add_u32 s81, s47, %c[AUP]\n\t"
        "s_min_i32 s80, s95, s81\n\t"
        "s_max_i32 s47, s47, s80\n\t"
        "s_add_u32 s41, s42, s47\n\t"
        ".Lrc%=:\n\t"
        "s_waitcnt vmcnt(7)\n\t"
        "v_smfmac_f32_16x16x64_bf16 v[64:67], v[16:19], a[0:7], v113\n\t"
        "v_smfmac_f32_16x16x64_bf16 v[68:71], v[16:19], a[8:15], v113\n\t"
        "v_smfmac_f32_16x16x64_bf16 v[72:75], v[16:19], a[16:23], v113\n\t"
        "v_smfmac_f32_16x16x64_bf16 v[76:79], v[16:19], a[24:31], v113\n\t"
        "s_waitcnt vmcnt(6)\n\t"
        "v_smfmac_f32_16x16x64_bf16 v[64:67], v[20:23], a[32:39], v113\n\t"
        "v_smfmac_f32_16x16x64_bf16 v[68:71], v[20:23], a[40:47], v113\n\t"
        "v_smfmac_f32_16x16x64_bf16 v[72:75], v[20:23], a[48:55], v113\n\t"
        "v_smfmac_f32_16x16x64_bf16 v[76:79], v[20:23], a[56:63], v113\n\t"
        "s_waitcnt vmcnt(5)\n\t"
        "v_smfmac_f32_16x16x64_bf16 v[64:67], v[24:27], a[64:71], v113\n\t"
        "v_smfmac_f32_16x16x64_bf16 v[68:71], v[24:27], a[72:79], v113\n\t"
        "v_smfmac_f32_16x16x64_bf16 v[72:75], v[24:27], a[80:87], v113\n\t"
        "v_smfmac_f32_16x16x64_bf16 v[76:79], v[24:27], a[88:95], v113\n\t"
        "s_waitcnt vmcnt(4)\n\t"
        "v_smfmac_f32_16x16x64_bf16 v[64:67], v[28:31], a[96:103], v113\n\t"
        "v_smfmac_f32_16x16x64_bf16 v[68:71], v[28:31], a[104:111], v113\n\t"
        "v_smfmac_f32_16x16x64_bf16 v[72:75], v[28:31], a[112:119], v113\n\t"
        "v_smfmac_f32_16x16x64_bf16 v[76:79], v[28:31], a[120:127], v113\n\t"
        "s_waitcnt vmcnt(3)\n\t"
        "v_smfmac_f32_16x16x64_bf16 v[64:67], v[32:35], a[128:135], v113\n\t"
        "v_smfmac_f32_16x16x64_bf16 v[68:71], v[32:35], a[136:143], v113\n\t"
        "v_smfmac_f32_16x16x64_bf16 v[72:75], v[32:35], a[144:151], v113\n\t"
        "v_smfmac_f32_16x16x64_bf16 v[76:79], v[32:35], a[152:159], v113\n\t"
        "s_waitcnt vmcnt(2)\n\t"
        "v_smfmac_f32_16x16x64_bf16 v[64:67], v[36:39], a[160:167], v113\n\t"
        "v_smfmac_f32_16x16x64_bf16 v[68:71], v[36:39], a[168:175], v113\n\t"
        "v_smfmac_f32_16x16x64_bf16 v[72:75], v[36:39], a[176:183], v113\n\t"
        "v_smfmac_f32_16x16x64_bf16 v[76:79], v[36:39], a[184:191], v113\n\t"
        "s_waitcnt vmcnt(1)\n\t"
        "v_smfmac_f32_16x16x64_bf16 v[64:67], v[40:43], a[192:199], v113\n\t"
        "v_smfmac_f32_16x16x64_bf16 v[68:71], v[40:43], a[200:207], v113\n\t"
        "v_smfmac_f32_16x16x64_bf16 v[72:75], v[40:43], a[208:215], v113\n\t"
        "v_smfmac_f32_16x16x64_bf16 v[76:79], v[40:43], a[216:223], v113\n\t"
        "s_waitcnt vmcnt(0)\n\t"
        "v_smfmac_f32_16x16x64_bf16 v[64:67], v[44:47], a[224:231], v113\n\t"
        "s_nop 2\n\t"
        "v_smfmac_f32_16x16x64_bf16 v[68:71], v[44:47], a[232:239], v113\n\t"
        "s_cmp_gt_u32 %[w], 2\n\t"
        "s_nop 1\n\t"
        "v_smfmac_f32_16x16x64_bf16 v[72:75], v[44:47], a[240:247], v113\n\t"
        "v_add_f32_e32 v80, v64, v66\n\t"
        "s_nop 0\n\t"
        "v_smfmac_f32_16x16x64_bf16 v[76:79], v[44:47], a[248:255], v113\n\t"
        "v_add_f32_e32 v81, v65, v67\n\t"
        "v_add_f32_e32 v82, v68, v70\n\t"
        "v_add_f32_e32 v83, v69, v71\n\t"
        "s_mov_b32 exec_lo, 0\n\t"
        "s_nop 0\n\t"
        "v_add_f32_e32 v80, v72, v74\n\t"
        "v_add_f32_e32 v81, v73, v75\n\t"
        "s_nop 0\n\t"
        "v_pk_add_f32 v[82:83], v[76:77], v[78:79]\n\t"
        "s_mov_b32 exec_lo, -1\n\t"
        "s_nop 0\n\t"
        "v_permlane32_swap_b32_e32 v80, v82\n\t"
        "v_add_f32_e32 v80, v80, v82\n\t"
        "ds_write_b32 v119, v80\n\t"
        "v_permlane32_swap_b32_e32 v81, v83\n\t"
        "v_add_f32_e32 v81, v81, v83\n\t"
        "ds_write_b32 v119, v81 offset:128\n\t"
        "v_cmp_u_f32_e32 vcc, v80, v81\n\t"
        "s_nop 2\n\t"
        "v_mov_b32_e32 v104, v104\n\t"
        "s_cbranch_vccnz .Lfc%=\n\t"
        ".Lrd%=:\n\t"
        "v_mov_b32_e32 v104, v104\n\t"
        "s_cbranch_scc1 .Lno%=\n\t"
        "v_mov_b32_e32 v104, v104\n\t"
        "s_nop 2\n\t"
        "v_mov_b32_e32 v104, v104\n\t"
        "s_waitcnt lgkmcnt(0)\n\t"
        "s_barrier\n\t"
        "v_mov_b32_e32 v104, v104\n\t"
        "ds_read_b64 v[0:1], v120 offset:8192\n\t"
        "ds_read_b64 v[2:3], v120 offset:8704\n\t"
        "ds_read_b64 v[4:5], v120 offset:9216\n\t"
        "ds_read_b64 v[6:7], v120 offset:9728\n\t"
        "v_mov_b32_e32 v104, v104\n\t"
        "s_nop 2\n\t"
        "v_mov_b32_e32 v104, v104\n\t"
        "s_nop 2\n\t"
        "v_mov_b32_e32 v104, v104\n\t"
        "s_waitcnt lgkmcnt(2)\n\t"
        "v_pk_add_f32 v[0:1], v[0:1], v[2:3]\n\t"
        "s_waitcnt lgkmcnt(0)\n\t"
        "v_pk_add_f32 v[4:5], v[4:5], v[6:7]\n\t"
        "v_pk_add_f32 v[0:1], v[0:1], v[4:5]\n\t"
        "v_cvt_pk_bf16_f32 v0, v0, v1\n\t"
        "v_min_u32_e32 v0, -2, v0\n\t"
        "buffer_store_dword v0, v116, %[srd], s67 offen sc1\n\t"
        ".Lnp%=:\n\t"
        "s_memrealtime s[76:77]\n\t"
        "s_add_u32 s40, s40, 1\n\t"
        "s_branch .Lloop%=\n\t"
        ".Lmv%=:\n\t"
        "s_mov_b32 s48, 1\n\t"
        "s_cmp_eq_u32 s54, %c[iTOL]\n\t"
        "s_cbranch_scc1 .Lm1%=\n\t"
        "s_max_i32 s80, s85, s56\n\t"
        "s_add_u32 s43, s41, s80\n\t"
        "s_add_u32 s43, s43, %c[iPEN0]\n\t"
        "s_mov_b32 s54, %c[iTOL]\n\t"
        "s_add_u32 s46, s46, %c[iX0C]\n\t"
        "s_add_u32 s95, s47, %c[iX0A]\n\t"
        "v_and_b32_e32 v4, 3, v88\n\t"
        "v_and_b32_e32 v5, 3, v90\n\t"
        "v_lshlrev_b32_e64 v4, v4, 1\n\t"
        "v_lshlrev_b32_e64 v5, v5, 1\n\t"
        "v_or_b32_e32 v4, v4, v5\n\t"
        "s_mov_b32 s80, 0\n\t"
        "v_and_b32_e32 v5, 1, v4\n\t"
        "v_cmp_ne_u32_e32 vcc, 0, v5\n\t"
        "s_nop 1\n\t"
        "s_cmp_lg_u64 vcc, 0\n\t"
        "s_cselect_b32 s81, 2, 0\n\t"
        "s_or_b32 s80, s80, s81\n\t"
        "v_and_b32_e32 v5, 2, v4\n\t"
        "v_cmp_ne_u32_e32 vcc, 0, v5\n\t"
        "s_nop 1\n\t"
        "s_cmp_lg_u64 vcc, 0\n\t"
        "s_cselect_b32 s81, 4, 0\n\t"
        "s_or_b32 s80, s80, s81\n\t"
        "v_and_b32_e32 v5, 4, v4\n\t"
        "v_cmp_ne_u32_e32 vcc, 0, v5\n\t"
        "s_nop 1\n\t"
        "s_cmp_lg_u64 vcc, 0\n\t"
        "s_cselect_b32 s81, 8, 0\n\t"
        "s_or_b32 s80, s80, s81\n\t"
        "v_and_b32_e32 v5, 8, v4\n\t"
        "v_cmp_ne_u32_e32 vcc, 0, v5\n\t"
        "s_nop 1\n\t"
        "s_cmp_lg_u64 vcc, 0\n\t"
        "s_cselect_b32 s81, 16, 0\n\t"
        "s_or_b32 s80, s80, s81\n\t"
        "s_lshr_b32 s80, 0x1adf1210, s80\n\t"
        "s_and_b32 s55, s80, 3\n\t"
        "s_branch .Lal%=\n\t"
        ".Lm1%=:\n\t"
        "s_add_u32 s80, s37, %c[iPEN2]\n\t"
        "s_min_i32 s80, s80, %c[iPENM]\n\t"
        "s_cmp_lg_u32 s37, 0\n\t"
        "s_cselect_b32 s37, s80, %c[iPEN]\n\t"
        "s_add_u32 s80, s56, s37\n\t"
        "s_max_i32 s80, s80, %c[iPMIN]\n\t"
        "s_add_u32 s43, s41, s80\n\t"
        "s_add_u32 s95, s93, %c[LA]\n\t"
        ".Lal%=:\n\t"
        "s_sub_u32 s80, s55, s43\n\t"
        "s_and_b32 s80, s80, %c[iPHM]\n\t"
        "s_add_u32 s43, s43, s80\n\t"
        "s_branch .Lsd%=\n\t"
        ".Lsf%=:\n\t"
        "s_mov_b32 exec_hi, -1\n\t"
        "s_sleep 2\n\t"
        "buffer_load_dwordx4 v[88:91], v112, %[srd], s66 offen sc1\n\t"
        "s_add_u32 s97, s97, 1\n\t"
        "s_cmp_lt_u32 s97, 0x100000\n\t"
        "s_cbranch_scc0 .Lgu%=\n\t"
        "s_waitcnt vmcnt(0)\n\t"
        "s_branch .Lsr%=\n\t"
        ".Lfa%=:\n\t"
        "v_max_u32_e32 v84, v0, v2\n\t"
        "v_max3_u32 v84, v84, v1, v3\n\t"
        "v_max3_u32 v84, v84, v4, v6\n\t"
        "v_max3_u32 v84, v84, v5, v7\n\t"
        "v_max3_u32 v84, v84, v8, v10\n\t"
        "v_max3_u32 v84, v84, v9, v11\n\t"
        "v_max3_u32 v84, v84, v12, v14\n\t"
        "v_max3_u32 v84, v84, v13, v15\n\t"
        "v_cmp_eq_u32_e32 vcc, -1, v84\n\t"
        "s_nop 1\n\t"
        "v_mov_b32_e32 v104, v104\n\t"
        "s_nop 0\n\t"
        "s_cbranch_vccz .Lra%=\n\t"
        "s_add_u32 s80, s40, 1\n\t"
        "v_mov_b32_e32 v80, 30720\n\t"
        "v_mov_b32_e32 v81, s80\n\t"
        "ds_write_b32 v80, v81\n\t"
        ".Lfp%=:\n\t"
        "s_sleep %c[ASLP]\n\t"
        "buffer_load_dwordx4 v[0:3], v112, %[srd], s57 offen sc1\n\t"
        "buffer_load_dwordx4 v[4:7], v112, %[srd], s58 offen sc1\n\t"
        "buffer_load_dwordx4 v[8:11], v112, %[srd], s59 offen sc1\n\t"
        "buffer_load_dwordx4 v[12:15], v112, %[srd], s60 offen sc1\n\t"
        "s_add_u32 s97, s97, 1\n\t"
        "s_cmp_lt_u32 s97, 0x100000\n\t"
        "s_cbranch_scc0 .Lgu%=\n\t"
        "s_waitcnt vmcnt(0)\n\t"
        "v_max_u32_e32 v84, v0, v2\n\t"
        "v_max3_u32 v84, v84, v1, v3\n\t"
        "v_max3_u32 v84, v84, v4, v6\n\t"
        "v_max3_u32 v84, v84, v5, v7\n\t"
        "v_max3_u32 v84, v84, v8, v10\n\t"
        "v_max3_u32 v84, v84, v9, v11\n\t"
        "v_max3_u32 v84, v84, v12, v14\n\t"
        "v_max3_u32 v84, v84, v13, v15\n\t"
        "v_cmp_eq_u32_e32 vcc, -1, v84\n\t"
        "s_nop 1\n\t"
        "v_mov_b32_e32 v104, v104\n\t"
        "s_nop 0\n\t"
        "s_cbranch_vccnz .Lfp%=\n\t"
        "v_mov_b32_e32 v48, 0\n\t"
        "v_mov_b32_e32 v49, 0\n\t"
        "v_mov_b32_e32 v50, 0\n\t"
        "v_mov_b32_e32 v51, 0\n\t"
        "v_mov_b32_e32 v52, 0\n\t"
        "v_mov_b32_e32 v53, 0\n\t"
        "v_mov_b32_e32 v54, 0\n\t"
        "v_mov_b32_e32 v55, 0\n\t"
        "v_mov_b32_e32 v56, 0\n\t"
        "v_mov_b32_e32 v57, 0\n\t"
        "v_mov_b32_e32 v58, 0\n\t"
        "v_mov_b32_e32 v59, 0\n\t"
        "v_mov_b32_e32 v60, 0\n\t"
        "v_mov_b32_e32 v61, 0\n\t"
        "v_mov_b32_e32 v62, 0\n\t"
        "v_mov_b32_e32 v63, 0\n\t"
        "s_nop 1\n\t"
        "s_branch .Lgo%=\n\t"
        ".Lfc%=:\n\t"
        "v_max_u32_e32 v84, v16, v18\n\t"
        "v_max3_u32 v84, v84, v20, v22\n\t"
        "v_max3_u32 v84, v84, v24, v26\n\t"
        "v_max3_u32 v84, v84, v28, v30\n\t"
        "v_max3_u32 v84, v84, v32, v34\n\t"
        "v_max3_u32 v84, v84, v36, v38\n\t"
        "v_max3_u32 v84, v84, v40, v42\n\t"
        "v_max3_u32 v84, v84, v44, v46\n\t"
        "v_cmp_eq_u32_e32 vcc, -1, v84\n\t"
        "s_nop 1\n\t"
        "v_mov_b32_e32 v104, v104\n\t"
        "s_nop 0\n\t"
        "s_cbranch_vccz .Lrd%=\n\t"
        "s_add_u32 s80, s40, 1\n\t"
        "v_mov_b32_e32 v80, 30724\n\t"
        "v_mov_b32_e32 v81, s80\n\t"
        "ds_write_b32 v80, v81\n\t"
        ".Lfq%=:\n\t"
        "s_sleep %c[ASLP]\n\t"
        "buffer_load_dwordx4 v[16:19], v115, %[srd], s63 offen offset:0 sc1\n\t"
        "buffer_load_dwordx4 v[20:23], v115, %[srd], s63 offen offset:1024 sc1\n\t"
        "buffer_load_dwordx4 v[24:27], v115, %[srd], s63 offen offset:2048 sc1\n\t"
        "buffer_load_dwordx4 v[28:31], v115, %[srd], s63 offen offset:3072 sc1\n\t"
        "buffer_load_dwordx4 v[32:35], v115, %[srd], s64 offen offset:0 sc1\n\t"
        "buffer_load_dwordx4 v[36:39], v115, %[srd], s64 offen offset:1024 sc1\n\t"
        "buffer_load_dwordx4 v[40:43], v115, %[srd], s64 offen offset:2048 sc1\n\t"
        "buffer_load_dwordx4 v[44:47], v115, %[srd], s64 offen offset:3072 sc1\n\t"
        "s_add_u32 s97, s97, 1\n\t"
        "s_cmp_lt_u32 s97, 0x100000\n\t"
        "s_cbranch_scc0 .Lgu%=\n\t"
        "s_waitcnt vmcnt(0)\n\t"
        "v_max_u32_e32 v84, v16, v18\n\t"
        "v_max3_u32 v84, v84, v20, v22\n\t"
        "v_max3_u32 v84, v84, v24, v26\n\t"
        "v_max3_u32 v84, v84, v28, v30\n\t"
        "v_max3_u32 v84, v84, v32, v34\n\t"
        "v_max3_u32 v84, v84, v36, v38\n\t"
        "v_max3_u32 v84, v84, v40, v42\n\t"
        "v_max3_u32 v84, v84, v44, v46\n\t"
        "v_cmp_eq_u32_e32 vcc, -1, v84\n\t"
        "s_nop 1\n\t"
        "v_mov_b32_e32 v104, v104\n\t"
        "s_nop 0\n\t"
        "s_cbranch_vccnz .Lfq%=\n\t"
        "v_mov_b32_e32 v64, 0\n\t"
        "v_mov_b32_e32 v65, 0\n\t"
        "v_mov_b32_e32 v66, 0\n\t"
        "v_mov_b32_e32 v67, 0\n\t"
        "v_mov_b32_e32 v68, 0\n\t"
        "v_mov_b32_e32 v69, 0\n\t"
        "v_mov_b32_e32 v70, 0\n\t"
        "v_mov_b32_e32 v71, 0\n\t"
        "v_mov_b32_e32 v72, 0\n\t"
        "v_mov_b32_e32 v73, 0\n\t"
        "v_mov_b32_e32 v74, 0\n\t"
        "v_mov_b32_e32 v75, 0\n\t"
        "v_mov_b32_e32 v76, 0\n\t"
        "v_mov_b32_e32 v77, 0\n\t"
        "v_mov_b32_e32 v78, 0\n\t"
        "v_mov_b32_e32 v79, 0\n\t"
        "s_nop 1\n\t"
        "s_branch .Lrc%=\n\t"
        ".Lno%=:\n\t"
        "s_waitcnt lgkmcnt(0)\n\t"
        "s_barrier\n\t"
        "s_branch .Lnp%=\n\t"
        ".Lta%=:\n\t"
        "s_waitcnt lgkmcnt(0)\n\t"
        "s_branch .Lxa%=\n\t"
        ".Ltc%=:\n\t"
        "s_waitcnt lgkmcnt(0)\n\t"
        "s_branch .Lxc%=\n\t"
        ".Lgu%=:\n\t"
        "s_mov_b64 exec, -1\n\t"
        "v_mov_b32_e32 v80, %c[ferr]\n\t"
        "v_mov_b32_e32 v81, 2\n\t"
        "buffer_store_dword v81, v80, %[wsd], 0 offen sc1\n\t"
        "s_endpgm\n\t"
        ".Lend%=:\n\t"
        "buffer_store_dword v80, v114, %[wsd], s72 offen sc1\n\t"
        "s_waitcnt vmcnt(0)\n\t"
        "s_barrier\n\t"
        "buffer_store_dword v86, v118, %[wsd], 0 offen sc1\n\t"
        "s_endpgm"
        :
        : [srd] "s"(rhd), [wsd] "s"(wsd), [w] "s"(w), [nA] "s"(nA), [nS] "s"(nS), [nW] "s"(nW), [nC] "s"(nC), [b0] "s"(b0), [b1] "s"(b1), [kvo] "s"(kvo), [pob] "s"(pob),
          [ferr] "i"(F_ERR), [iD1] "i"(D1), [iD2] "i"(D2), [LC] "i"(LC0), [LA] "i"(LA0), [LC24] "i"(LC0 + 24), [LA24] "i"(LA0 + 24), [E1] "i"(2 * (D1 - LC0)), [E2] "i"(2 * (D2 - LA0)),
          [iPEN] "i"(PEN), [iPEN2] "i"(PEN2), [iPENM] "i"(PEN + 2 * PEN2), [iPEN0] "i"(PEN0), [iTOL] "i"(TOL), [iCT0] "i"(CT0), [iX0C] "i"(X0C), [iX0A] "i"(X0A), [iPHM] "i"(PH_M),
          [iFT] "i"(-3), [iFN] "i"(FINE_N), [AUP] "i"(A_UP), [ASLP] "i"(A_SLP), [LDEC] "i"(L_DEC), [UPSH] "i"(UP_SH), [TRUP] "i"(TR_UP), [TRDN] "i"(TR_DN), [HSLP] "i"(H_SLP), [iPMIN] "i"(PMIN), [VSLP] "i"(V_SLP), [RSLP] "i"(R_SLP),
          "{v[112:127]}"(pc), "{a[0:31]}"(wa[0]), "{a[32:63]}"(wa[1]), "{a[64:95]}"(wa[2]), "{a[96:127]}"(wa[3]), "{a[128:159]}"(wa[4]), "{a[160:191]}"(wa[5]), "{a[192:223]}"(wa[6]), "{a[224:255]}"(wa[7]), "{v[128:159]}"(wg[0]), "{v[160:191]}"(wg[1]), "{v[192:223]}"(wg[2]), "{v[224:255]}"(wg[3])
        : "memory", "vcc", "scc", "s36", "s37", "s38", "s39", "s40", "s41", "s42", "s43", "s44", "s45", "s46", "s47", "s48", "s49", "s50", "s51", "s52", "s53", "s54", "s55", "s56", "s57", "s58", "s59", "s60", "s61", "s62", "s63", "s64", "s65", "s66", "s67", "s68", "s69", "s70", "s71", "s72", "s73", "s74", "s75", "s76", "s77", "s78", "s79", "s80", "s81", "s82", "s83", "s84", "s85", "s86", "s87", "s88", "s89", "s90", "s91", "s92", "s93", "s94", "s95", "s96", "s97", "s98", "s99");
    }
    return false;
}

using f32x16 = __attribute__((ext_vector_type(16))) float;
constexpr int CV_RP = 80;
constexpr int CV_U = 96 * CV_RP;
constexpr int CV_KP = 4416;
constexpr int CV_K0 = 8 * CV_U;
__device__ __forceinline__ float lo16(unsigned x) { return __uint_as_float(x << 16); }
__device__ __forceinline__ float hi16(unsigned x) { return __uint_as_float(x & 0xFFFF0000u); }
__device__ __forceinline__ void prep_block(const Params& p, rsrc_t r, int k4, const int wv, const unsigned tag) {
    const int tid = otid(), w = wv, l = tid & 63;
    const int cu = (k4 & 3) * 32 + w * 8 + (l >> 3), oc0 = (k4 >> 2) * 8 + (l & 7);
    const rsrc_t rk = __builtin_amdgcn_make_buffer_rsrc((void*)p.Ak, 0, S * D * 2, 0x00020000), rv = __builtin_amdgcn_make_buffer_rsrc((void*)p.Av, 0, S * D * 2, 0x00020000);
    v4u sk[4], kn[11], vn[8], gw[2];
    auto fetch = [&](int oc) {
#pragma unroll
        for (int j = 0; j < 11; ++j) kn[j] = __builtin_amdgcn_raw_buffer_load_b128(rk, (unsigned)((oc * 8 + j - 3) * 2048 + cu * 16), 0, 2);
#pragma unroll
        for (int i = 0; i < 8; ++i) vn[i] = __builtin_amdgcn_raw_buffer_load_b128(rv, (unsigned)((oc * 8 + i) * 2048 + cu * 16), 0, 2); };
    fetch(oc0);
#pragma unroll
    for (int q = 0; q < 4; ++q) sk[q] = *(const v4u*)(p.shift_k + cu * 32 + q * 8);
#pragma unroll
    for (int q = 0; q < 2; ++q) gw[q] = *(const v4u*)(p.gate_w + q * 512 + l * 8);
    st4(r, tid < 128 ? F_RDY + k4 * 512 + tid * 4 : (tid == 128 && k4 == 0) ? F_CONV : 0xF0000000u, 0u);
#pragma unroll 1
    for (int it = 0; it < 4; ++it) {
        const int oc = oc0 + 64 * it, tk = (it * 32 + k4) * 16 + w;
        v4u kr[11], vr[8], xg[4][2];
#pragma unroll
        for (int j = 0; j < 11; ++j) kr[j] = kn[j];
#pragma unroll
        for (int i = 0; i < 8; ++i) vr[i] = vn[i];
        __builtin_amdgcn_sched_barrier(0);
#pragma unroll
        for (int i = 0; i < 4; ++i)
#pragma unroll
            for (int q = 0; q < 2; ++q) xg[i][q] = *(const v4u*)(p.Aq + (size_t)(tk + 4 * i) * D + q * 512 + l * 8);
        fetch(it < 3 ? oc + 64 : 4096);
        __builtin_amdgcn_sched_barrier(0);
#pragma unroll
        for (int c8 = 0; c8 < 8; ++c8) {
            const unsigned s01 = sk[c8 >> 1][(c8 & 1) * 2], s23 = sk[c8 >> 1][(c8 & 1) * 2 + 1];
            const float sv[4] = {hi16(s23), lo16(s23), hi16(s01), lo16(s01)};
            float kk[11], u[8];
#pragma unroll
            for (int j = 0; j < 11; ++j) kk[j] = (c8 & 1) ? hi16(kr[j][c8 >> 1]) : lo16(kr[j][c8 >> 1]);
            f32x2 acc[4];
#pragma unroll
            for (int j = 0; j < 4; ++j)
#pragma unroll
                for (int i = 0; i < 4; ++i) { const f32x2 t = rbf2((f32x2){kk[i + j], kk[i + j + 4]} * sv[j]); acc[i] = j == 0 ? t : rbf2(acc[i] + t); }
#pragma unroll
            for (int i = 0; i < 8; ++i) u[i] = acc[i & 3][i >> 2] * ((c8 & 1) ? hi16(vr[i][c8 >> 1]) : lo16(vr[i][c8 >> 1]));
            st16(r, W_U + (cu * 8 + c8) * 4096 + oc * 16, (v4u){pack2(u[7], u[6]), pack2(u[5], u[4]), pack2(u[3], u[2]), pack2(u[1], u[0])});
        }
        float ga[4];
#pragma unroll
        for (int i = 0; i < 4; ++i) { float acc = 0.f;
#pragma unroll
            for (int q = 0; q < 2; ++q)
#pragma unroll
                for (int e = 0; e < 4; ++e) { const unsigned xa = xg[i][q][e], gb = gw[q][e]; acc += rbfh(lo16(xa) * lo16(gb)); acc += rbfh(hi16(xa) * hi16(gb)); }
            ga[i] = acc; }
        for (int o = 32; o > 0; o >>= 1)
#pragma unroll
            for (int i = 0; i < 4; ++i) ga[i] += __shfl_xor(ga[i], o);
        const float z = rbfh(l == 0 ? ga[0] : l == 1 ? ga[1] : l == 2 ? ga[2] : ga[3]);
        st4(r, l < 4 ? W_GATE + (tk + 4 * l) * 4 : 0xF0000000u, __float_as_uint(rbfh(1.f / (1.f + expf(-z)))));
    }
    DRAIN(); __syncthreads();
    if (tid == 0) st4(r, F_PREP + k4 * 4, tag);
}
__device__ __forceinline__ bool conv_block(const Params& p, rsrc_t r, int k4, unsigned char* lds, const int wv, const int j, const unsigned tag) {
    (void)j;
    const int tid = otid(), w = wv, l = tid & 63;
    const rsrc_t rs = __builtin_amdgcn_make_buffer_rsrc((void*)p.ssm_k, 0, S * D * 2, 0x00020000);
    v4u kraw[2][4], uv[8]; unsigned dn;
    auto fetch_k = [&](int d0) {
#pragma unroll
        for (int ci = 0; ci < 2; ++ci)
#pragma unroll
            for (int i = 0; i < 4; ++i) kraw[ci][i] = __builtin_amdgcn_raw_buffer_load_b128(rs, (unsigned)((d0 + 2 * w + ci) * 4096 + (i * 64 + l) * 16), 0, 2);
        dn = *(const unsigned*)(p.ssm_D + (d0 & 1023) + 2 * w); };
    auto fetch_u = [&](unsigned o) {
#pragma unroll
        for (int i = 0; i < 8; ++i) uv[i] = ld16(r, o + i * 4096 + tid * 16); };
    fetch_k(k4 * 32);
    if (!wait_prep(r, l, tag)) return false;
    fetch_u(W_U + k4 * 32 * 4096);
#pragma unroll 1
    for (int it = 0; it < 4; ++it) {
    const int bb = k4 * 4 + it; const unsigned dd = dn;
    { const v4u z = {0u, 0u, 0u, 0u};
#pragma unroll
      for (int i = 0; i < 5; ++i) { const int q = tid + 256 * i; *(v4u*)(lds + (q / 160) * CV_U + (q % 160) * 16) = z; } }
#pragma unroll
    for (int i = 0; i < 8; ++i) *(v4u*)(lds + i * CV_U + (32 + (tid >> 2)) * CV_RP + (tid & 3) * 16) = uv[i];
    unsigned char* kc = lds + CV_K0 + w * 4 * CV_KP;
    auto copies = [&](int ci) {
        if (l < 32) *(unsigned*)(kc + l * 4) = 0u;
        if (l < 48) *(unsigned*)(kc + 128 + 4096 + l * 4) = 0u;
#pragma unroll
        for (int i = 0; i < 4; ++i) *(v4u*)(kc + 128 + (i * 64 + l) * 16) = kraw[ci][i];
        __builtin_amdgcn_wave_barrier();
#pragma unroll
        for (int it = 0; it < 9; ++it) { const int i = min(l + 64 * it, CV_KP / 8 - 2);
            const v2u e01 = *(const v2u*)(kc + i * 8), e23 = *(const v2u*)(kc + i * 8 + 8);
            const unsigned s0 = (e01.x >> 16) | (e01.y << 16), s1 = (e01.y >> 16) | (e23.x << 16), s2 = (e23.x >> 16) | (e23.y << 16);
            *(v2u*)(kc + CV_KP + i * 8) = (v2u){s0, s1};
            *(v2u*)(kc + 2 * CV_KP + i * 8) = (v2u){e01.y, e23.x};
            *(v2u*)(kc + 3 * CV_KP + i * 8) = (v2u){s1, s2}; }
        __builtin_amdgcn_wave_barrier(); };
    copies(0);
    __syncthreads();
    const int b = l & 31, kg = l >> 5, cc = (b + 1) & 3;
    float ys[32];
#pragma unroll
    for (int ci = 0; ci < 2; ++ci) {
        const int c8 = 2 * w + ci;
        if (ci == 1) { copies(1); __builtin_amdgcn_sched_barrier(0); fetch_k(it < 3 ? bb * 8 + 8 : 1024); fetch_u(it < 3 ? W_U + (bb * 8 + 8) * 4096 : 0xF0000000u); __builtin_amdgcn_sched_barrier(0); }
        f32x16 acc0, acc1, ad0, ad1;
#pragma unroll
        for (int i = 0; i < 16; ++i) { acc0[i] = 0.f; acc1[i] = 0.f; ad0[i] = 0.f; ad1[i] = 0.f; }
        typedef const __attribute__((address_space(3))) v2u* l2p;
        unsigned kb = (unsigned)(size_t)(kc + cc * CV_KP + (b - 8 * kg - 7 + 64 - cc) * 2) - 32, kb8 = kb + 8;
        asm volatile("" : "+v"(kb), "+v"(kb8));
        const unsigned char* ub = lds + c8 * CV_U + (32 + b) * CV_RP + kg * 16;
        v2u ka[4][2], kz[4][2]; bf16x8 fa[4], fz[4];
        auto ld_k = [&](int set, int un, int hi) { const int dl = un >> 1, kh = un & 1; const unsigned o = (unsigned)((32 * dl - 16 * kh + 16) * 2 + hi * 2048);
            if (hi) { kz[set][0] = *(l2p)(size_t)(kb + o); kz[set][1] = *(l2p)(size_t)(kb8 + o); } else { ka[set][0] = *(l2p)(size_t)(kb + o); ka[set][1] = *(l2p)(size_t)(kb8 + o); } };
        auto ld_f = [&](int set, int un) { const int dl = un >> 1, kh = un & 1;
            fa[set] = *(const bf16x8*)(ub + (32 - dl) * CV_RP + kh * 32); fz[set] = *(const bf16x8*)(ub - dl * CV_RP + kh * 32); };
#pragma unroll
        for (int q = 0; q < 2; ++q) { ld_k(q, q, 0); ld_f(q, q); ld_k(q, q, 1); }
        {
            const unsigned dv = ci ? dd >> 16 : dd & 0xFFFFu;
#pragma unroll
            for (int kh = 0; kh < 2; ++kh) { const int e = 7 - (b - 16 * kh - 8 * kg); const unsigned x = dv << ((e & 1) * 16); const int q = e >> 1;
                const v4u di = {q == 0 ? x : 0u, q == 1 ? x : 0u, q == 2 ? x : 0u, q == 3 ? x : 0u};
                asm volatile("s_nop 1\n\tv_mfma_f32_32x32x16_bf16 %0, %2, %4, %0\n\tv_mfma_f32_32x32x16_bf16 %1, %3, %4, %1" : "+v"(ad1), "+v"(ad0) : "v"(fa[kh]), "v"(fz[kh]), "v"(di)); }
        }
#pragma unroll 1
        for (int u0 = 0; u0 < 64; u0 += 4) {
#pragma unroll
            for (int q = 0; q < 4; ++q) {
                const int un = u0 + q + 2, ns = (q + 2) & 3;
                const v4u kav = {ka[q][0].x, ka[q][0].y, ka[q][1].x, ka[q][1].y}, kzv = {kz[q][0].x, kz[q][0].y, kz[q][1].x, kz[q][1].y};
                __builtin_amdgcn_sched_barrier(0);
                asm volatile("s_nop 1\n\tv_mfma_f32_32x32x16_bf16 %0, %1, %2, %0" : "+v"(acc1) : "v"(fa[q]), "v"(kav));
                ld_k(ns, un, 0);
                __builtin_amdgcn_sched_barrier(0);
                asm volatile("v_mfma_f32_32x32x16_bf16 %0, %1, %2, %0" : "+v"(acc0) : "v"(fz[q]), "v"(kav));
                ld_f(ns, un);
                __builtin_amdgcn_sched_barrier(0);
                asm volatile("v_mfma_f32_32x32x16_bf16 %0, %1, %2, %0" : "+v"(acc1) : "v"(fz[q]), "v"(kzv));
                ld_k(ns, un, 1);
            }
        }
        asm volatile("s_nop 7\n\ts_nop 7" : "+v"(acc0), "+v"(acc1), "+v"(ad0), "+v"(ad1));
#pragma unroll
        for (int i = 0; i < 32; ++i) { const float y = rbfh(i < 16 ? acc0[i] : acc1[i - 16]) + rbfh(i < 16 ? ad0[i] : ad1[i - 16]);
            ys[i] = ci == 0 ? y : __uint_as_float(pack2(ys[i], y)); }
    }
    __syncthreads();
#pragma unroll
    for (int i = 0; i < 32; ++i) { const int row = (i >> 4) * 32 + (i & 3) + 8 * ((i & 15) >> 2) + 4 * kg; *(float*)(lds + (row * 32 + b) * 16 + w * 4) = ys[i]; }
    __syncthreads();
#pragma unroll
    for (int i = 0; i < 8; ++i) { const int t = tid + 256 * i; st16(r, W_A + bb * 32768 + t * 16, *(const v4u*)(lds + t * 16)); }
    __syncthreads();
    }
    DRAIN(); __syncthreads();
    if (tid == 0) __hip_atomic_fetch_add((unsigned*)(p.ws + F_CONV), 1u, __ATOMIC_RELAXED, __HIP_MEMORY_SCOPE_AGENT);
    return true;
}

__device__ __forceinline__ void dma16(unsigned ldsdst, unsigned voff, const v4u& srd, unsigned soff) {
    asm volatile("s_mov_b32 m0, %0\n\ts_nop 0\n\tbuffer_load_dwordx4 %1, %2, %3 offen lds" :: "s"(ldsdst), "v"(voff), "s"(srd), "s"(soff) : "m0", "memory"); }
__device__ __forceinline__ void dma16n(unsigned ldsdst, unsigned voff, const v4u& srd, unsigned soff) {
    asm volatile("s_mov_b32 m0, %0\n\ts_nop 0\n\tbuffer_load_dwordx4 %1, %2, %3 offen nt lds" :: "s"(ldsdst), "v"(voff), "s"(srd), "s"(soff) : "m0", "memory"); }
__device__ __forceinline__ v4u pack_acc(const f32x4& a, const f32x4& b) {
    float x0, x1, x2, x3, y0, y1, y2, y3;
    asm volatile("v_accvgpr_read_b32 %0, %8\n\tv_accvgpr_read_b32 %1, %9\n\tv_accvgpr_read_b32 %2, %10\n\tv_accvgpr_read_b32 %3, %11\n\t"
                 "v_accvgpr_read_b32 %4, %12\n\tv_accvgpr_read_b32 %5, %13\n\tv_accvgpr_read_b32 %6, %14\n\tv_accvgpr_read_b32 %7, %15"
                 : "=v"(x0), "=v"(x1), "=v"(x2), "=v"(x3), "=v"(y0), "=v"(y1), "=v"(y2), "=v"(y3)
                 : "a"(a[0]), "a"(a[1]), "a"(a[2]), "a"(a[3]), "a"(b[0]), "a"(b[1]), "a"(b[2]), "a"(b[3]));
    return (v4u){pack2(x0, x1), pack2(x2, x3), pack2(y0, y1), pack2(y2, y3)}; }
#define SB() __builtin_amdgcn_sched_barrier(0)
struct NoHook { __device__ void operator()(int) const {} };
template <int MODE, int MI, int NJ, int NS, int HX = 0, class HOOK = NoHook, int MN = MI>
__device__ __forceinline__ void gemm(rsrc_t r, const bf16_t* __restrict__ A, const bf16_t* __restrict__ B, int m0, int n0, unsigned char* lds, unsigned coff, const int wv,
                                     const int rot, const void* tk = nullptr,
                                     const bool have = false, const bf16_t* Bn = nullptr, int m0n = 0, int n0n = 0, HOOK&& hook = NoHook()) {
    constexpr int AB = MN * 4096, BB = NJ * 2048, B0 = (NS / 2) * AB, NB = NJ / 2, NM = MI * NJ, NR = MI + NJ, BP = NJ * 64;
    static_assert(NS % 2 == 0 && 32 % NS == 0 && B0 <= 65536 && NS * BB <= 65536 && (NJ == 4 || NJ == 8), "ring");
    const int tid = otid();
    const int w = wv, l = tid & 63, n16 = l & 15, g = l >> 4;
    const int wm = w >> 1, wn = w & 1;
    const unsigned lb = __builtin_amdgcn_readfirstlane((unsigned)(size_t)lds);
    const v4u sa = mk_srd(A, 2048 * 1024 * 2); v4u sb = mk_srd(B, 1024 * 4096 * 2);
    f32x4 acc[MI][NJ];
#pragma unroll
    for (int i = 0; i < MI; ++i)
#pragma unroll
        for (int j = 0; j < NJ; ++j) acc[i][j] = (f32x4){0.f, 0.f, 0.f, 0.f};
    unsigned va = (unsigned)((m0 + 8 * MI * w + (l >> 3)) * 2048 + (((l & 7) ^ (l >> 3)) << 4));
    unsigned vb[2];
    if (NJ == 8) { vb[0] = (unsigned)((8 * w + (l >> 5)) * 8192 + n0 * 2) + (unsigned)(((l & 31) << 4) ^ ((l >> 5) << 5) ^ (wn << 7)); vb[1] = vb[0] ^ 64u; }
    else { vb[0] = (unsigned)((8 * w + g) * 8192 + n0 * 2) + (unsigned)((n16 << 4) ^ (g << 5) ^ (wn << 7)); vb[1] = vb[0]; }
    auto rk = [&](int kt) { return (kt & ~31) | ((kt + rot) & 31); };
    unsigned la = lb + w * MI * 1024;
    auto st_a = [&](int buf, int kt, int q) { dma16(la + buf * AB + q * 1024, va, sa, (unsigned)(rk(kt) * 64 + q * 16384)); };
    auto st_b = [&](int buf, int kt, int x) { if ((B_NT >> (NJ == 4 ? 0 : MI == 4 ? 1 : 2)) & 1) dma16n(lb + B0 + buf * BB + (w * NB + x) * 1024, vb[x & 1], sb, (unsigned)((rk(kt) * 32 + x * (8 / NB)) * 8192));
        else dma16(lb + B0 + buf * BB + (w * NB + x) * 1024, vb[x & 1], sb, (unsigned)((rk(kt) * 32 + x * (8 / NB)) * 8192)); };
    const unsigned f = (unsigned)((n16 >> 2) | ((g & 1) << 2));
    constexpr int NQ = MODE == 1 ? NJ : NJ / 2;
    unsigned ra[2], rb[NQ];
    ra[0] = lb + (16 * MI * wm + n16) * 128 + ((g ^ (n16 & 7)) << 4); ra[1] = ra[0] ^ 64u;
    {   const unsigned row = lb + B0 + (8 * g + (n16 >> 2)) * BP + (NJ == 8 ? 256 * wn : 0), g0 = NJ == 8 ? 0u : 4u * wn;
#pragma unroll
        for (int j = 0; j < NQ; ++j) {
            if (MODE == 1) rb[j] = row + (((g0 + j) ^ f) << 5) + 8 * (n16 & 3);
            else rb[j] = row + (((g0 + 2 * j + ((n16 & 3) >> 1)) ^ f) << 5) + (n16 & 1) * 16;
            asm volatile("" : "+v"(rb[j])); } }
    bf16x8 af[2][MI], bfr[2][NJ];
    auto rd = [&](int set, int buf, int x) {
        if (x < NJ) { const unsigned a = rb[MODE == 1 ? x : (x >> 1)] + (MODE == 1 ? 0 : (x & 1) * 8) + buf * BB;
            const v2u lo = trb(a), hi = trb(a + 4 * BP); v4u v = {lo.x, lo.y, hi.x, hi.y}; bfr[set][x] = as_frag(v); }
        else af[set][x - NJ] = ldsa(ra[buf & 1] + (buf >> 1) * AB + (x - NJ) * 2048); };
    auto mf = [&](int set, int i, int j) {
        if (MODE == 1) asm volatile("v_mfma_f32_16x16x32_bf16 %0, %1, %2, %0" : "+a"(acc[i][j]) : "v"(af[set][i]), "v"(bfr[set][j]));
        else asm volatile("v_mfma_f32_16x16x32_bf16 %0, %1, %2, %0" : "+a"(acc[i][j]) : "v"(bfr[set][j]), "v"(af[set][i])); };
    if (!have) {
#pragma unroll
        for (int t = 0; t < NS; ++t) {
            if (t & 1) {
#pragma unroll
                for (int q = 0; q < MI; ++q) st_a(t >> 1, t - 1, q); }
#pragma unroll
            for (int q = 0; q < NB; ++q) st_b(t, t, q);
            if (HX) hook(t); }
    }
#pragma unroll
    for (int i = 0; i < MI; ++i)
#pragma unroll
        for (int j = 0; j < NJ; ++j) asm volatile("" : "+a"(acc[i][j]));
    asm volatile("s_nop 1" ::: "memory");
    if (!have) {
        asm volatile("s_waitcnt vmcnt(%c0)\n\ts_barrier" :: "i"((NS - 1) * (NB + HX) + (NS / 2 - 1) * MI) : "memory");
    }
#pragma unroll
    for (int x = 0; x < NR; ++x) rd(0, 0, x);
    constexpr int HEAD = NM / 8;
    const v4u st = mk_srd(tk, tk ? 32768 : 0); unsigned tdum = 0u;
#pragma unroll HX ? 32 / NS : 1
    for (int k0 = 0; k0 < 32; k0 += NS) {
        int kb = k0 + NS;
        if (kb == 32) {
            kb = Bn ? 0 : 0x10100;
            if (Bn) { va += (unsigned)((m0n - m0 + 8 * (MN - MI) * w) * 2048); la = lb + w * MN * 1024; vb[0] += (unsigned)((n0n - n0) * 2); vb[1] += (unsigned)((n0n - n0) * 2); sb = mk_srd(Bn, 1024 * 4096 * 2); } }
        if (NS == 4) asm volatile("buffer_load_dword %0, %1, %2, %3 offen" : "+v"(tdum) : "v"(l < 32 ? (unsigned)l * 128u : 0xF0000000u), "s"(st), "s"((unsigned)k0 * 1024u) : "memory");
#pragma unroll
        for (int u = 0; u < NS; ++u) {
            SB();
#pragma unroll
            for (int q = 0; q < HEAD; ++q) mf(u & 1, q / NJ, q % NJ);
            SB();
            asm volatile("s_waitcnt vmcnt(%c0) lgkmcnt(0)\n\ts_barrier" :: "i"((NS - 2) * (NB + HX) + (NS / 2 - 1 - (u & 1)) * MI) : "memory");
            SB();
            constexpr int span = NM - HEAD;
#pragma unroll
            for (int q = HEAD; q < NM; ++q) {
                mf(u & 1, q / NJ, q % NJ);
                const int x = q - HEAD, nd = NB + ((u & 1) ? MI : 0);
#pragma unroll
                for (int y = 0; y < NR; ++y) if ((y * (span - HEAD)) / NR == x) rd((u & 1) ^ 1, (u + 1) % NS, y);
#pragma unroll
                for (int y = 0; y < nd; ++y) if ((y * span + span / 2) / nd == x) { if ((u & 1) && y < MI) st_a(u >> 1, kb + u - 1, y); else st_b(u, kb + u, y - ((u & 1) ? MI : 0)); }
                SB(); }
            if (MN > MI && (u & 1) && k0 == 32 - NS) {
#pragma unroll
                for (int y = MI; y < MN; ++y) st_a(u >> 1, kb + u - 1, y); }
            if (HX) hook(NS + k0 + u);
        }
    }
    asm volatile("s_nop 7\n\ts_nop 3" : "+v"(tdum) :: "memory");
    SB();
    const int col = n0 + 16 * NJ * wn, head = col >> 7, sub = (col & 127) >> 4, seg0 = (m0 >> 5) + (MI / 2) * wm;
#pragma unroll
    for (int sg = 0; sg < MI / 2; ++sg) {
        const unsigned base = coff + (unsigned)(((seg0 + sg) * 32 + head) * 8192 + l * 16);
#pragma unroll
        for (int q = 0; q < NJ; ++q) {
            if (MODE == 0) { const int tt = q / (NJ / 2), kk = q % (NJ / 2); st16(r, base + (tt * 4 + (sub >> 1) + kk) * 1024, pack_acc(acc[2 * sg + tt][2 * kk], acc[2 * sg + tt][2 * kk + 1])); }
            else st16(r, base + (sub + q) * 1024, pack_acc(acc[2 * sg][q], acc[2 * sg + 1][q])); }
    }
}

__device__ __forceinline__ unsigned min4(unsigned v) { return min(min(__builtin_amdgcn_readlane(v, 0), __builtin_amdgcn_readlane(v, 1)), min(__builtin_amdgcn_readlane(v, 2), __builtin_amdgcn_readlane(v, 3))); }
__device__ __forceinline__ void attn_block(const Params& p, rsrc_t r, unsigned char* lds, const int wv, const int h, const int j, const unsigned tag) {
    (void)j;
    const int l = otid() & 63, n16 = l & 15, g = l >> 4;
    { v4u q[4]; if (!wait_cal(r, l, tag, q)) return; }
    v4u qv[2][4], kv[2][4], va[8];
    int nev = 0; (void)nev;
    auto fetch = [&](int seg) -> bool {
        for (unsigned spin = 0;; ++spin) { MEMBAR();
            const unsigned fv = ld4(r, F_KVQ + ((seg >> 2) * 32 + h) * 64 + min(l, 2) * 4);
            if (__all(fv == tag)) break;
            if (spin > (1u << 20)) { st4(r, F_ERR, 64u); return false; }
            __builtin_amdgcn_s_sleep(16); }
        MEMBAR();
        const unsigned o = (unsigned)((seg * 32 + h) * 8192 + l * 16);
#pragma unroll
        for (int i = 0; i < 8; ++i) qv[i >> 2][i & 3] = ld16(r, W_QX + o + i * 1024);
#pragma unroll
        for (int i = 0; i < 8; ++i) kv[i >> 2][i & 3] = ld16(r, W_KX + o + i * 1024);
#pragma unroll
        for (int i = 0; i < 8; ++i) va[i] = ld16(r, W_VF + o + i * 1024);
        return true; };
    if (!fetch(wv)) return;
    unsigned prog = 0u, tseen = rtnow() - 100000u;
#pragma unroll 1
    for (int seg = wv; seg < NSEG; seg += 4) {
        for (unsigned spin = 0; prog <= (unsigned)seg; ++spin) {
            const unsigned now = rtnow();
            if (spin > (1u << 22)) { st4(r, F_ERR, 32u); return; }
            if (prog + (seg >= EARLY_FROM ? 2u : 1u) + (now - tseen) / STEP_MIN <= (unsigned)seg) { __builtin_amdgcn_s_sleep(16); continue; }
            MEMBAR();
            const unsigned pg = min4(ld4(r, F_PROG + h * 128 + min(l, 2) * 8));
            if (pg != prog) { prog = pg; tseen = now; }
            if (prog <= (unsigned)seg) __builtin_amdgcn_s_sleep(P_SLP);
        }
        MEMBAR();
        const unsigned km = HO_KVM + (seg * 32 + h) * 4096;
        v4u kmv[4]; v2u pm[8];
        const v4u k0 = ld16(r, km + l * 16), k1 = ld16(r, km + 1024 + l * 16);
#pragma unroll
        for (int t = 0; t < 8; ++t) pm[t] = ld8(r, km + 2048 + t * 256 + n16 * 16 + (g & 1) * 8);
        *(v4u*)(lds + wv * 2048 + l * 16) = k0; *(v4u*)(lds + wv * 2048 + 1024 + l * 16) = k1;
        __builtin_amdgcn_wave_barrier();
#pragma unroll
        for (int ks = 0; ks < 4; ++ks) { const unsigned a = (unsigned)(size_t)lds + wv * 2048 + (32 * ks + 8 * g + (n16 >> 2)) * 16 + (n16 & 1) * 8; const v2u x0 = trb(a), x1 = trb(a + 64); kmv[ks] = (v4u){x0.x, x0.y, x1.x, x1.y}; }
#pragma unroll
        for (int qt = 0; qt < 2; ++qt) {
            f32x4 sc[3];
            asm volatile("s_nop 1\n\t"
                "v_mfma_f32_16x16x32_bf16 %0, %3, %15, 0\n\tv_mfma_f32_16x16x32_bf16 %1, %7, %15, 0\n\tv_mfma_f32_16x16x32_bf16 %2, %11, %15, 0\n\t"
                "v_mfma_f32_16x16x32_bf16 %0, %4, %16, %0\n\tv_mfma_f32_16x16x32_bf16 %1, %8, %16, %1\n\tv_mfma_f32_16x16x32_bf16 %2, %12, %16, %2\n\t"
                "v_mfma_f32_16x16x32_bf16 %0, %5, %17, %0\n\tv_mfma_f32_16x16x32_bf16 %1, %9, %17, %1\n\tv_mfma_f32_16x16x32_bf16 %2, %13, %17, %2\n\t"
                "v_mfma_f32_16x16x32_bf16 %0, %6, %18, %0\n\tv_mfma_f32_16x16x32_bf16 %1, %10, %18, %1\n\tv_mfma_f32_16x16x32_bf16 %2, %14, %18, %2\n\t"
                "s_nop 7"
                : "=&v"(sc[0]), "=&v"(sc[1]), "=&v"(sc[2])
                : "v"(kv[0][0]), "v"(kv[0][1]), "v"(kv[0][2]), "v"(kv[0][3]), "v"(kv[1][0]), "v"(kv[1][1]), "v"(kv[1][2]), "v"(kv[1][3]),
                  "v"(kmv[0]), "v"(kmv[1]), "v"(kmv[2]), "v"(kmv[3]), "v"(qv[qt][0]), "v"(qv[qt][1]), "v"(qv[qt][2]), "v"(qv[qt][3]));
            const int qi = 16 * qt + n16; float mx = -INFINITY;
#pragma unroll
            for (int t = 0; t < 3; ++t)
#pragma unroll
                for (int rr = 0; rr < 4; ++rr) { float v = rbfh(rbfh(sc[t][rr]) * 0.08838834764831845f);
                    if (t < 2) v = (16 * t + 4 * g + rr <= qi) ? v : -1000000000.0f; else if (g >= 2) v = -INFINITY;
                    sc[t][rr] = v; mx = fmaxf(mx, v); }
            mx = xor16_max(mx); mx = xor32_max(mx); float sum = 0.f;
#pragma unroll
            for (int t = 0; t < 3; ++t)
#pragma unroll
                for (int rr = 0; rr < 4; ++rr) { const float e = __expf(sc[t][rr] - mx); sc[t][rr] = e; sum += e; }
            sum = xor16_add(sum); sum = xor32_add(sum); const float inv = 1.0f / sum;
            const v4u p0 = {pack2(sc[0][0] * inv, sc[0][1] * inv), pack2(sc[0][2] * inv, sc[0][3] * inv), pack2(sc[1][0] * inv, sc[1][1] * inv), pack2(sc[1][2] * inv, sc[1][3] * inv)};
            const v2u p1 = {pack2(sc[2][0] * inv, sc[2][1] * inv), pack2(sc[2][2] * inv, sc[2][3] * inv)};
            f32x4 oa[8];
            asm volatile("s_nop 1\n\t"
                "v_mfma_f32_16x16x32_bf16 %0, %8, %24, 0\n\tv_mfma_f32_16x16x32_bf16 %1, %9, %24, 0\n\tv_mfma_f32_16x16x32_bf16 %2, %10, %24, 0\n\tv_mfma_f32_16x16x32_bf16 %3, %11, %24, 0\n\t"
                "v_mfma_f32_16x16x32_bf16 %4, %12, %24, 0\n\tv_mfma_f32_16x16x32_bf16 %5, %13, %24, 0\n\tv_mfma_f32_16x16x32_bf16 %6, %14, %24, 0\n\tv_mfma_f32_16x16x32_bf16 %7, %15, %24, 0\n\t"
                "v_mfma_f32_16x16x16_bf16 %0, %16, %25, %0\n\tv_mfma_f32_16x16x16_bf16 %1, %17, %25, %1\n\tv_mfma_f32_16x16x16_bf16 %2, %18, %25, %2\n\tv_mfma_f32_16x16x16_bf16 %3, %19, %25, %3\n\t"
                "v_mfma_f32_16x16x16_bf16 %4, %20, %25, %4\n\tv_mfma_f32_16x16x16_bf16 %5, %21, %25, %5\n\tv_mfma_f32_16x16x16_bf16 %6, %22, %25, %6\n\tv_mfma_f32_16x16x16_bf16 %7, %23, %25, %7\n\t"
                "s_nop 7"
                : "=&v"(oa[0]), "=&v"(oa[1]), "=&v"(oa[2]), "=&v"(oa[3]), "=&v"(oa[4]), "=&v"(oa[5]), "=&v"(oa[6]), "=&v"(oa[7])
                : "v"(va[0]), "v"(va[1]), "v"(va[2]), "v"(va[3]), "v"(va[4]), "v"(va[5]), "v"(va[6]), "v"(va[7]),
                  "v"(pm[0]), "v"(pm[1]), "v"(pm[2]), "v"(pm[3]), "v"(pm[4]), "v"(pm[5]), "v"(pm[6]), "v"(pm[7]), "v"(p0), "v"(p1));
#pragma unroll
            for (int u = 0; u < 4; ++u) {
                const f32x4 o0 = oa[2 * u], o1 = oa[2 * u + 1];
                const v4u pk = {pack2(o0[0], o0[1]), pack2(o0[2], o0[3]), pack2(o1[0], o1[1]), pack2(o1[2], o1[3])};
                st16(r, W_OX + (((seg * 2 + qt) * 32 + h) * 4 + u) * 1024 + l * 16, pk);
            }
        }
        DRAIN();
        { const int tt = 2 * seg + (l >> 5);
            __hip_atomic_fetch_add((unsigned*)(p.ws + F_RDY + (((tt & 3) * 32 + (l & 31)) * 32 + (tt >> 2)) * 4), 1u, __ATOMIC_RELAXED, __HIP_MEMORY_SCOPE_AGENT); }
        if (seg + 4 < NSEG && !fetch(seg + 4)) return;
    }
}

constexpr int OP_P = 80;
__device__ __forceinline__ void oproj_block(const Params& p, rsrc_t r, unsigned char* lds, const int wv, const int nt, const int rho, const int j, v4u (&wraw)[64], const unsigned tag
    ) {
    (void)j;
    const int w = wv, l = otid() & 63, n16 = l & 15, g = l >> 4;
    const unsigned ldsbase = (unsigned)(size_t)lds;
    bf16x8 wof[32][2];
#pragma unroll
    for (int ks = 0; ks < 32; ++ks) {
        if (2 * ks == WO_IN) DRAIN();
        const unsigned st = ldsbase + w * 8192 + (ks & 1) * 4096;
        const unsigned sa = st + (l >> 2) * OP_P + (l & 3) * 16;
        switch (ks) {
            case 0: asm volatile("ds_write_b128 %0, %1\n\tds_write_b128 %0, %2 offset:%c3" :: "v"(sa), "{a[128:131]}"(wraw[0]), "{a[132:135]}"(wraw[1]), "i"(16 * OP_P) : "memory"); break;
            case 1: asm volatile("ds_write_b128 %0, %1\n\tds_write_b128 %0, %2 offset:%c3" :: "v"(sa), "{a[136:139]}"(wraw[2]), "{a[140:143]}"(wraw[3]), "i"(16 * OP_P) : "memory"); break;
            case 2: asm volatile("ds_write_b128 %0, %1\n\tds_write_b128 %0, %2 offset:%c3" :: "v"(sa), "{a[144:147]}"(wraw[4]), "{a[148:151]}"(wraw[5]), "i"(16 * OP_P) : "memory"); break;
            case 3: asm volatile("ds_write_b128 %0, %1\n\tds_write_b128 %0, %2 offset:%c3" :: "v"(sa), "{a[152:155]}"(wraw[6]), "{a[156:159]}"(wraw[7]), "i"(16 * OP_P) : "memory"); break;
            case 4: asm volatile("ds_write_b128 %0, %1\n\tds_write_b128 %0, %2 offset:%c3" :: "v"(sa), "{a[160:163]}"(wraw[8]), "{a[164:167]}"(wraw[9]), "i"(16 * OP_P) : "memory"); break;
            case 5: asm volatile("ds_write_b128 %0, %1\n\tds_write_b128 %0, %2 offset:%c3" :: "v"(sa), "{a[168:171]}"(wraw[10]), "{a[172:175]}"(wraw[11]), "i"(16 * OP_P) : "memory"); break;
            case 6: asm volatile("ds_write_b128 %0, %1\n\tds_write_b128 %0, %2 offset:%c3" :: "v"(sa), "{a[176:179]}"(wraw[12]), "{a[180:183]}"(wraw[13]), "i"(16 * OP_P) : "memory"); break;
            case 7: asm volatile("ds_write_b128 %0, %1\n\tds_write_b128 %0, %2 offset:%c3" :: "v"(sa), "{a[184:187]}"(wraw[14]), "{a[188:191]}"(wraw[15]), "i"(16 * OP_P) : "memory"); break;
            case 8: asm volatile("ds_write_b128 %0, %1\n\tds_write_b128 %0, %2 offset:%c3" :: "v"(sa), "{a[192:195]}"(wraw[16]), "{a[196:199]}"(wraw[17]), "i"(16 * OP_P) : "memory"); break;
            case 9: asm volatile("ds_write_b128 %0, %1\n\tds_write_b128 %0, %2 offset:%c3" :: "v"(sa), "{a[200:203]}"(wraw[18]), "{a[204:207]}"(wraw[19]), "i"(16 * OP_P) : "memory"); break;
            case 10: asm volatile("ds_write_b128 %0, %1\n\tds_write_b128 %0, %2 offset:%c3" :: "v"(sa), "{a[208:211]}"(wraw[20]), "{a[212:215]}"(wraw[21]), "i"(16 * OP_P) : "memory"); break;
            case 11: asm volatile("ds_write_b128 %0, %1\n\tds_write_b128 %0, %2 offset:%c3" :: "v"(sa), "{a[216:219]}"(wraw[22]), "{a[220:223]}"(wraw[23]), "i"(16 * OP_P) : "memory"); break;
            case 12: asm volatile("ds_write_b128 %0, %1\n\tds_write_b128 %0, %2 offset:%c3" :: "v"(sa), "{a[224:227]}"(wraw[24]), "{a[228:231]}"(wraw[25]), "i"(16 * OP_P) : "memory"); break;
            case 13: asm volatile("ds_write_b128 %0, %1\n\tds_write_b128 %0, %2 offset:%c3" :: "v"(sa), "{a[232:235]}"(wraw[26]), "{a[236:239]}"(wraw[27]), "i"(16 * OP_P) : "memory"); break;
            case 14: asm volatile("ds_write_b128 %0, %1\n\tds_write_b128 %0, %2 offset:%c3" :: "v"(sa), "{a[240:243]}"(wraw[28]), "{a[244:247]}"(wraw[29]), "i"(16 * OP_P) : "memory"); break;
            case 15: asm volatile("ds_write_b128 %0, %1\n\tds_write_b128 %0, %2 offset:%c3" :: "v"(sa), "{a[248:251]}"(wraw[30]), "{a[252:255]}"(wraw[31]), "i"(16 * OP_P) : "memory"); break;
            case 16: asm volatile("ds_write_b128 %0, %1\n\tds_write_b128 %0, %2 offset:%c3" :: "v"(sa), "{v[176:179]}"(wraw[32]), "{v[180:183]}"(wraw[33]), "i"(16 * OP_P) : "memory"); break;
            case 17: asm volatile("ds_write_b128 %0, %1\n\tds_write_b128 %0, %2 offset:%c3" :: "v"(sa), "{v[184:187]}"(wraw[34]), "{v[188:191]}"(wraw[35]), "i"(16 * OP_P) : "memory"); break;
            case 18: asm volatile("ds_write_b128 %0, %1\n\tds_write_b128 %0, %2 offset:%c3" :: "v"(sa), "{v[192:195]}"(wraw[36]), "{v[196:199]}"(wraw[37]), "i"(16 * OP_P) : "memory"); break;
            case 19: asm volatile("ds_write_b128 %0, %1\n\tds_write_b128 %0, %2 offset:%c3" :: "v"(sa), "{v[200:203]}"(wraw[38]), "{v[204:207]}"(wraw[39]), "i"(16 * OP_P) : "memory"); break;
            case 20: asm volatile("ds_write_b128 %0, %1\n\tds_write_b128 %0, %2 offset:%c3" :: "v"(sa), "{v[208:211]}"(wraw[40]), "{v[212:215]}"(wraw[41]), "i"(16 * OP_P) : "memory"); break;
            case 21: asm volatile("ds_write_b128 %0, %1\n\tds_write_b128 %0, %2 offset:%c3" :: "v"(sa), "{v[216:219]}"(wraw[42]), "{v[220:223]}"(wraw[43]), "i"(16 * OP_P) : "memory"); break;
            case 22: asm volatile("ds_write_b128 %0, %1\n\tds_write_b128 %0, %2 offset:%c3" :: "v"(sa), "{v[224:227]}"(wraw[44]), "{v[228:231]}"(wraw[45]), "i"(16 * OP_P) : "memory"); break;
            case 23: asm volatile("ds_write_b128 %0, %1\n\tds_write_b128 %0, %2 offset:%c3" :: "v"(sa), "{v[232:235]}"(wraw[46]), "{v[236:239]}"(wraw[47]), "i"(16 * OP_P) : "memory"); break;
            case 24: asm volatile("ds_write_b128 %0, %1\n\tds_write_b128 %0, %2 offset:%c3" :: "v"(sa), "{v[240:243]}"(wraw[48]), "{v[244:247]}"(wraw[49]), "i"(16 * OP_P) : "memory"); break;
            case 25: asm volatile("ds_write_b128 %0, %1\n\tds_write_b128 %0, %2 offset:%c3" :: "v"(sa), "{v[248:251]}"(wraw[50]), "{v[252:255]}"(wraw[51]), "i"(16 * OP_P) : "memory"); break;
            case 26: asm volatile("ds_write_b128 %0, %1\n\tds_write_b128 %0, %2 offset:%c3" :: "v"(sa), "{a[80:83]}"(wraw[52]), "{a[84:87]}"(wraw[53]), "i"(16 * OP_P) : "memory"); break;
            case 27: asm volatile("ds_write_b128 %0, %1\n\tds_write_b128 %0, %2 offset:%c3" :: "v"(sa), "{a[88:91]}"(wraw[54]), "{a[92:95]}"(wraw[55]), "i"(16 * OP_P) : "memory"); break;
            case 28: asm volatile("ds_write_b128 %0, %1\n\tds_write_b128 %0, %2 offset:%c3" :: "v"(sa), "{a[96:99]}"(wraw[56]), "{a[100:103]}"(wraw[57]), "i"(16 * OP_P) : "memory"); break;
            case 29: asm volatile("ds_write_b128 %0, %1\n\tds_write_b128 %0, %2 offset:%c3" :: "v"(sa), "{a[104:107]}"(wraw[58]), "{a[108:111]}"(wraw[59]), "i"(16 * OP_P) : "memory"); break;
            case 30: asm volatile("ds_write_b128 %0, %1\n\tds_write_b128 %0, %2 offset:%c3" :: "v"(sa), "{a[112:115]}"(wraw[60]), "{a[116:119]}"(wraw[61]), "i"(16 * OP_P) : "memory"); break;
            case 31: asm volatile("ds_write_b128 %0, %1\n\tds_write_b128 %0, %2 offset:%c3" :: "v"(sa), "{a[120:123]}"(wraw[62]), "{a[124:127]}"(wraw[63]), "i"(16 * OP_P) : "memory"); break;
        }
#pragma unroll
        for (int jn = 0; jn < 2; ++jn) {
            const unsigned a0 = st + (4 * g + (n16 >> 2)) * OP_P + (16 * jn + 4 * (n16 & 3)) * 2;
            const v2u lo = trb(a0), hi = trb(a0 + 16 * OP_P);
            v4u v = {lo.x, lo.y, hi.x, hi.y}; asm volatile("" : "=a"(wof[ks][jn]) : "0"(as_frag(v))); }
    }
    __syncthreads();
    const v4u rd = mk_srd(p.ws, W_TOTAL), ro = mk_srd(p.out, S * D * 2), rx = mk_srd(p.Aq, S * D * 2);
    const unsigned OOB = 0xF0000000u;
    const int NT = 32, jn = w & 1;
    auto tile = [&](int i) -> unsigned { return (unsigned)(4 * i + rho); };
    const unsigned r_off = F_RDY + (rho * 32 + nt) * 128 + (l & 31) * 4;
    const unsigned a_b = W_OX + 8 * w * 4096 + l * 16;
    const int n0 = nt * 32 + 16 * jn + 4 * g;
    const unsigned c_b = W_A + (n0 >> 3) * 32768 + n16 * 16 + (g & 1) * 8, g_b = W_GATE + n16 * 4, o_b = (unsigned)((n16 * 1024 + n0) * 2);
    auto mix = [&](const v2u& av, const v2u& xv, unsigned gv, const f32x4& bs) -> v2u {
        const float gg = __uint_as_float(gv), og = rbfh(1.f - gg); float e[4];
#pragma unroll
        for (int k = 0; k < 4; ++k) { const unsigned aw = k < 2 ? av.x : av.y, xw = k < 2 ? xv.x : xv.y; e[k] = rbfh(gg * rbfh((k & 1) ? hi16(aw) * hi16(xw) : lo16(aw) * lo16(xw))) + rbfh(og * rbfh(bs[k])); }
        return (v2u){pack2(e[0], e[1]), pack2(e[2], e[3])}; };
    const unsigned s_w = ldsbase + w * 2048 + l * 16, s_r = ldsbase + jn * 1024 + l * 16;
    unsigned long long known = 0ull; int nev = 0; (void)nev;
    bool have = false, pend = false; v4u af[32]; v2u pa = {0u, 0u}, px = {0u, 0u}; unsigned pg = 0u, cf2 = 0u;
    auto finish = [&](int i, bool on) {
        v2u o = {0u, 0u};
        if (on) {
            const __attribute__((address_space(3))) f32x4* q = (const __attribute__((address_space(3))) f32x4*)(size_t)(s_r + (unsigned)(i & 1) * 8192u);
            const f32x4 bs = (q[0] + q[128]) + (q[256] + q[384]);
            o = mix(pa, px, pg, bs);
        }
        asm volatile("buffer_store_dwordx2 %0, %1, %2, 0 offen" :: "v"(o), "v"(on ? o_b + tile(i) * 32768u : OOB), "s"(ro) : "memory"); };
    if (!wait_prep(r, l, tag) || !wait_word(r, F_CONV, 32u, 256u)) ENDPGM();
#pragma unroll 1
    for (int i = 0; i < NT; ++i) {
        if (!have) {
            for (unsigned spin = 0; !((known >> i) & 1ull); ++spin) { MEMBAR();
                known = __ballot(ld4(r, r_off) == 32u);
                if ((known >> i) & 1ull) break;
                if (spin > (1u << 21)) { st4(r, F_ERR, 128u); ENDPGM(); }
                __builtin_amdgcn_s_sleep(O_SLP); }
            const unsigned a0 = a_b + tile(i) * 131072u;
#pragma unroll
            for (int q = 0; q < 8; ++q)
                asm volatile("buffer_load_dwordx4 %0, %4, %5, %6 offen sc1\n\tbuffer_load_dwordx4 %1, %4, %5, %6 offen offset:1024 sc1\n\t"
                             "buffer_load_dwordx4 %2, %4, %5, %6 offen offset:2048 sc1\n\tbuffer_load_dwordx4 %3, %4, %5, %6 offen offset:3072 sc1"
                             : "=&v"(af[4 * q]), "=&v"(af[4 * q + 1]), "=&v"(af[4 * q + 2]), "=&v"(af[4 * q + 3]) : "v"(a0), "s"(rd), "s"(q * 4096) : "memory");
            asm volatile("buffer_store_dword %0, %0, %1, 0 offen\n\tbuffer_store_dword %0, %0, %1, 0 offen" :: "v"(OOB), "s"(ro) : "memory");
        }
        const bool fin = (w >> 1) == (i & 1);
        const bool more = i + 1 < NT && ((known >> (i + 1)) & 1ull);
        const unsigned an = more ? a_b + tile(i + 1) * 131072u : OOB;
        unsigned cfl, fg; v2u fa, fx;
        asm volatile("buffer_load_dwordx2 %0, %1, %2, 0 offen" : "=&v"(fx) : "v"(fin ? o_b + tile(i) * 32768u : OOB), "s"(rx) : "memory");
        asm volatile("buffer_load_dword %0, %3, %6, 0 offen sc1\n\tbuffer_load_dwordx2 %1, %4, %6, 0 offen sc1\n\tbuffer_load_dword %2, %5, %6, 0 offen sc1"
                     : "=&v"(cfl), "=&v"(fa), "=&v"(fg) : "v"(((known >> (i + 2)) & 1ull) ? OOB : r_off), "v"(fin ? c_b + tile(i) * 256u : OOB), "v"(fin ? g_b + tile(i) * 64u : OOB), "s"(rd) : "memory");
        f32x4 acc0 = {0.f, 0.f, 0.f, 0.f}, acc1 = {0.f, 0.f, 0.f, 0.f};
        asm volatile("s_nop 1" : "+v"(acc0), "+v"(acc1));
#pragma unroll
        for (int q = 0; q < 8; ++q) {
            if (q == 2) finish(i - 1, pend);
            if (q == 6) asm volatile("buffer_load_dword %0, %1, %2, 0 offen sc1" : "+v"(cf2) : "v"(more ? OOB : r_off), "s"(rd) : "memory");
            asm volatile(
                "s_waitcnt vmcnt(37)\n\tv_mfma_f32_16x16x32_bf16 %4, %6, %0, %4\n\tv_mfma_f32_16x16x32_bf16 %5, %7, %0, %5\n\tbuffer_load_dwordx4 %0, %14, %15, %16 offen sc1\n\t"
                "s_waitcnt vmcnt(37)\n\tv_mfma_f32_16x16x32_bf16 %4, %8, %1, %4\n\tv_mfma_f32_16x16x32_bf16 %5, %9, %1, %5\n\tbuffer_load_dwordx4 %1, %14, %15, %16 offen offset:1024 sc1\n\t"
                "s_waitcnt vmcnt(37)\n\tv_mfma_f32_16x16x32_bf16 %4, %10, %2, %4\n\tv_mfma_f32_16x16x32_bf16 %5, %11, %2, %5\n\tbuffer_load_dwordx4 %2, %14, %15, %16 offen offset:2048 sc1\n\t"
                "s_waitcnt vmcnt(37)\n\tv_mfma_f32_16x16x32_bf16 %4, %12, %3, %4\n\tv_mfma_f32_16x16x32_bf16 %5, %13, %3, %5\n\tbuffer_load_dwordx4 %3, %14, %15, %16 offen offset:3072 sc1"
                : "+v"(af[4 * q]), "+v"(af[4 * q + 1]), "+v"(af[4 * q + 2]), "+v"(af[4 * q + 3]), "+v"(acc0), "+v"(acc1)
                : "a"(wof[4 * q][0]), "a"(wof[4 * q][1]), "a"(wof[4 * q + 1][0]), "a"(wof[4 * q + 1][1]), "a"(wof[4 * q + 2][0]), "a"(wof[4 * q + 2][1]), "a"(wof[4 * q + 3][0]), "a"(wof[4 * q + 3][1]),
                  "v"(an), "s"(rd), "s"(q * 4096) : "memory"); }
        const unsigned po = (unsigned)(i & 1) * 8192u;
        asm volatile("s_nop 7\n\tds_write_b128 %6, %4\n\tds_write_b128 %6, %5 offset:1024\n\ts_waitcnt vmcnt(34) lgkmcnt(0)\n\ts_barrier"
                     : "+v"(cfl), "+v"(fa), "+v"(fg), "+v"(fx) : "v"(acc0), "v"(acc1), "v"(s_w + po) : "memory");
        known |= __ballot(cfl == 32u);
        pa = fa; px = fx; pg = fg; pend = fin && more;
        if (!more) finish(i, fin);
        have = more;
        if (!more) {
            asm volatile("s_waitcnt vmcnt(1)" : "+v"(cf2), "+v"(af[0]), "+v"(af[1]), "+v"(af[2]), "+v"(af[3]), "+v"(af[4]), "+v"(af[5]), "+v"(af[6]), "+v"(af[7]), "+v"(af[8]), "+v"(af[9]), "+v"(af[10]), "+v"(af[11]),
                         "+v"(af[12]), "+v"(af[13]), "+v"(af[14]), "+v"(af[15]) :: "memory");
            known |= __ballot(cf2 == 32u);
        }
    }
    if (nt == 0 && rho == 0 && w == 0) st4(r, l == 0 ? F_EP : OOB, tag);
}

__device__ __forceinline__ void bulk_block(const Params& p, rsrc_t r, unsigned char* lds, const int wv, const int j, const unsigned tag
    ) {
    const int x = (j & 7) >> 1, i32 = (j >> 3) * 2 + (j & 1);
    auto flags = [&](int mt, int nmt, int h0, int nh, int mat) {
        DRAIN(); __syncthreads();
        const int tid = otid(); if (tid < nmt * nh) st4(r, F_KVQ + ((mt + tid / nh) * 32 + h0 + tid % nh) * 64 + mat * 4, tag); };
#pragma unroll 1
    for (int t = 0; t < 2; ++t) {
        const int mt = 2 * t + (i32 & 1), mat = (i32 >> 1) & 1, hd = 8 * x + (i32 >> 2), rot = ((i32 >> 1) & 7) * 2;
        const bf16_t* Bm = mat ? p.Wv : p.Wk;
        if (mat) gemm<1, 4, 4, 8>(r, p.Aq, Bm, mt * 128, hd * 128, lds, W_VF, wv, rot, nullptr, t == 1, t == 0 ? Bm : nullptr, (mt + 2) * 128, hd * 128);
        else gemm<0, 4, 4, 8>(r, p.Aq, Bm, mt * 128, hd * 128, lds, W_KX, wv, rot, nullptr, t == 1, t == 0 ? Bm : nullptr, (mt + 2) * 128, hd * 128);
        flags(mt, 1, hd, 1, mat);
    }
    const int hp = 4 * x + (i32 >> 3), q8 = i32 & 7, rot = ROT2 * (i32 >> 2);
    {
        const int m = q8 < 4 ? 4 + (q8 & 1) : q8 - 4, mat = q8 < 4 ? q8 >> 1 : 2;
        const bf16_t* Bn = mat == 2 ? p.Wq : mat == 1 ? p.Wv : p.Wk;
#pragma unroll 1
        for (int t = 0; t < 2; ++t) {
            const int mt = 4 + 8 * t + (q8 & 3), mv = q8 >> 2;
            const bf16_t* tk = (t == 0 && wv == 1 && j < 32) ? p.Aq + (64 + j) * 16384 : nullptr;
            if (mv) gemm<1, 4, 8, 4, 0, NoHook, 8>(r, p.Aq, p.Wv, mt * 128, hp * 256, lds, W_VF, wv, rot, tk, false, t == 0 ? Bn : nullptr, m * 256, hp * 256);
            else gemm<0, 4, 8, 4, 0, NoHook, 8>(r, p.Aq, p.Wk, mt * 128, hp * 256, lds, W_KX, wv, rot, tk, false, t == 0 ? Bn : nullptr, m * 256, hp * 256);
            flags(mt, 1, 2 * hp, 2, mv);
            if (t == 1) break;
            const bf16_t* tl = (wv == 1 && j < 32) ? p.Aq + (96 + j) * 16384 : nullptr;
            if (mat == 1) gemm<1, 8, 8, 4>(r, p.Aq, Bn, m * 256, hp * 256, lds, W_VF, wv, rot, tl, true);
            else gemm<0, 8, 8, 4>(r, p.Aq, Bn, m * 256, hp * 256, lds, mat == 2 ? W_QX : W_KX, wv, rot, tl, true);
            flags(2 * m, 2, 2 * hp, 2, mat);
        }
    }
    {
        v4u wraw[64]; unsigned wd = 0u;
        const int l = otid() & 63; const v4u sw = mk_srd(p.Wo, 4096 * 1024 * 2); const unsigned wa = (unsigned)((wv * 1024 + (l >> 2)) * 2048 + i32 * 64 + (l & 3) * 16);
        auto wo_load = [&](int i) { switch (i) {
            case 0: asm volatile("buffer_load_dwordx4 %0, %1, %2, %3 offen" : "={a[128:131]}"(wraw[0]) : "v"(wa), "s"(sw), "s"(0) : "memory"); break;
            case 1: asm volatile("buffer_load_dwordx4 %0, %1, %2, %3 offen" : "={a[132:135]}"(wraw[1]) : "v"(wa), "s"(sw), "s"(32768) : "memory"); break;
            case 2: asm volatile("buffer_load_dwordx4 %0, %1, %2, %3 offen" : "={a[136:139]}"(wraw[2]) : "v"(wa), "s"(sw), "s"(65536) : "memory"); break;
            case 3: asm volatile("buffer_load_dwordx4 %0, %1, %2, %3 offen" : "={a[140:143]}"(wraw[3]) : "v"(wa), "s"(sw), "s"(98304) : "memory"); break;
            case 4: asm volatile("buffer_load_dwordx4 %0, %1, %2, %3 offen" : "={a[144:147]}"(wraw[4]) : "v"(wa), "s"(sw), "s"(131072) : "memory"); break;
            case 5: asm volatile("buffer_load_dwordx4 %0, %1, %2, %3 offen" : "={a[148:151]}"(wraw[5]) : "v"(wa), "s"(sw), "s"(163840) : "memory"); break;
            case 6: asm volatile("buffer_load_dwordx4 %0, %1, %2, %3 offen" : "={a[152:155]}"(wraw[6]) : "v"(wa), "s"(sw), "s"(196608) : "memory"); break;
            case 7: asm volatile("buffer_load_dwordx4 %0, %1, %2, %3 offen" : "={a[156:159]}"(wraw[7]) : "v"(wa), "s"(sw), "s"(229376) : "memory"); break;
            case 8: asm volatile("buffer_load_dwordx4 %0, %1, %2, %3 offen" : "={a[160:163]}"(wraw[8]) : "v"(wa), "s"(sw), "s"(262144) : "memory"); break;
            case 9: asm volatile("buffer_load_dwordx4 %0, %1, %2, %3 offen" : "={a[164:167]}"(wraw[9]) : "v"(wa), "s"(sw), "s"(294912) : "memory"); break;
            case 10: asm volatile("buffer_load_dwordx4 %0, %1, %2, %3 offen" : "={a[168:171]}"(wraw[10]) : "v"(wa), "s"(sw), "s"(327680) : "memory"); break;
            case 11: asm volatile("buffer_load_dwordx4 %0, %1, %2, %3 offen" : "={a[172:175]}"(wraw[11]) : "v"(wa), "s"(sw), "s"(360448) : "memory"); break;
            case 12: asm volatile("buffer_load_dwordx4 %0, %1, %2, %3 offen" : "={a[176:179]}"(wraw[12]) : "v"(wa), "s"(sw), "s"(393216) : "memory"); break;
            case 13: asm volatile("buffer_load_dwordx4 %0, %1, %2, %3 offen" : "={a[180:183]}"(wraw[13]) : "v"(wa), "s"(sw), "s"(425984) : "memory"); break;
            case 14: asm volatile("buffer_load_dwordx4 %0, %1, %2, %3 offen" : "={a[184:187]}"(wraw[14]) : "v"(wa), "s"(sw), "s"(458752) : "memory"); break;
            case 15: asm volatile("buffer_load_dwordx4 %0, %1, %2, %3 offen" : "={a[188:191]}"(wraw[15]) : "v"(wa), "s"(sw), "s"(491520) : "memory"); break;
            case 16: asm volatile("buffer_load_dwordx4 %0, %1, %2, %3 offen" : "={a[192:195]}"(wraw[16]) : "v"(wa), "s"(sw), "s"(524288) : "memory"); break;
            case 17: asm volatile("buffer_load_dwordx4 %0, %1, %2, %3 offen" : "={a[196:199]}"(wraw[17]) : "v"(wa), "s"(sw), "s"(557056) : "memory"); break;
            case 18: asm volatile("buffer_load_dwordx4 %0, %1, %2, %3 offen" : "={a[200:203]}"(wraw[18]) : "v"(wa), "s"(sw), "s"(589824) : "memory"); break;
            case 19: asm volatile("buffer_load_dwordx4 %0, %1, %2, %3 offen" : "={a[204:207]}"(wraw[19]) : "v"(wa), "s"(sw), "s"(622592) : "memory"); break;
            case 20: asm volatile("buffer_load_dwordx4 %0, %1, %2, %3 offen" : "={a[208:211]}"(wraw[20]) : "v"(wa), "s"(sw), "s"(655360) : "memory"); break;
            case 21: asm volatile("buffer_load_dwordx4 %0, %1, %2, %3 offen" : "={a[212:215]}"(wraw[21]) : "v"(wa), "s"(sw), "s"(688128) : "memory"); break;
            case 22: asm volatile("buffer_load_dwordx4 %0, %1, %2, %3 offen" : "={a[216:219]}"(wraw[22]) : "v"(wa), "s"(sw), "s"(720896) : "memory"); break;
            case 23: asm volatile("buffer_load_dwordx4 %0, %1, %2, %3 offen" : "={a[220:223]}"(wraw[23]) : "v"(wa), "s"(sw), "s"(753664) : "memory"); break;
            case 24: asm volatile("buffer_load_dwordx4 %0, %1, %2, %3 offen" : "={a[224:227]}"(wraw[24]) : "v"(wa), "s"(sw), "s"(786432) : "memory"); break;
            case 25: asm volatile("buffer_load_dwordx4 %0, %1, %2, %3 offen" : "={a[228:231]}"(wraw[25]) : "v"(wa), "s"(sw), "s"(819200) : "memory"); break;
            case 26: asm volatile("buffer_load_dwordx4 %0, %1, %2, %3 offen" : "={a[232:235]}"(wraw[26]) : "v"(wa), "s"(sw), "s"(851968) : "memory"); break;
            case 27: asm volatile("buffer_load_dwordx4 %0, %1, %2, %3 offen" : "={a[236:239]}"(wraw[27]) : "v"(wa), "s"(sw), "s"(884736) : "memory"); break;
            case 28: asm volatile("buffer_load_dwordx4 %0, %1, %2, %3 offen" : "={a[240:243]}"(wraw[28]) : "v"(wa), "s"(sw), "s"(917504) : "memory"); break;
            case 29: asm volatile("buffer_load_dwordx4 %0, %1, %2, %3 offen" : "={a[244:247]}"(wraw[29]) : "v"(wa), "s"(sw), "s"(950272) : "memory"); break;
            case 30: asm volatile("buffer_load_dwordx4 %0, %1, %2, %3 offen" : "={a[248:251]}"(wraw[30]) : "v"(wa), "s"(sw), "s"(983040) : "memory"); break;
            case 31: asm volatile("buffer_load_dwordx4 %0, %1, %2, %3 offen" : "={a[252:255]}"(wraw[31]) : "v"(wa), "s"(sw), "s"(1015808) : "memory"); break;
            case 32: asm volatile("buffer_load_dwordx4 %0, %1, %2, %3 offen" : "={v[176:179]}"(wraw[32]) : "v"(wa), "s"(sw), "s"(1048576) : "memory"); break;
            case 33: asm volatile("buffer_load_dwordx4 %0, %1, %2, %3 offen" : "={v[180:183]}"(wraw[33]) : "v"(wa), "s"(sw), "s"(1081344) : "memory"); break;
            case 34: asm volatile("buffer_load_dwordx4 %0, %1, %2, %3 offen" : "={v[184:187]}"(wraw[34]) : "v"(wa), "s"(sw), "s"(1114112) : "memory"); break;
            case 35: asm volatile("buffer_load_dwordx4 %0, %1, %2, %3 offen" : "={v[188:191]}"(wraw[35]) : "v"(wa), "s"(sw), "s"(1146880) : "memory"); break;
            case 36: asm volatile("buffer_load_dwordx4 %0, %1, %2, %3 offen" : "={v[192:195]}"(wraw[36]) : "v"(wa), "s"(sw), "s"(1179648) : "memory"); break;
            case 37: asm volatile("buffer_load_dwordx4 %0, %1, %2, %3 offen" : "={v[196:199]}"(wraw[37]) : "v"(wa), "s"(sw), "s"(1212416) : "memory"); break;
            case 38: asm volatile("buffer_load_dwordx4 %0, %1, %2, %3 offen" : "={v[200:203]}"(wraw[38]) : "v"(wa), "s"(sw), "s"(1245184) : "memory"); break;
            case 39: asm volatile("buffer_load_dwordx4 %0, %1, %2, %3 offen" : "={v[204:207]}"(wraw[39]) : "v"(wa), "s"(sw), "s"(1277952) : "memory"); break;
            case 40: asm volatile("buffer_load_dwordx4 %0, %1, %2, %3 offen" : "={v[208:211]}"(wraw[40]) : "v"(wa), "s"(sw), "s"(1310720) : "memory"); break;
            case 41: asm volatile("buffer_load_dwordx4 %0, %1, %2, %3 offen" : "={v[212:215]}"(wraw[41]) : "v"(wa), "s"(sw), "s"(1343488) : "memory"); break;
            case 42: asm volatile("buffer_load_dwordx4 %0, %1, %2, %3 offen" : "={v[216:219]}"(wraw[42]) : "v"(wa), "s"(sw), "s"(1376256) : "memory"); break;
            case 43: asm volatile("buffer_load_dwordx4 %0, %1, %2, %3 offen" : "={v[220:223]}"(wraw[43]) : "v"(wa), "s"(sw), "s"(1409024) : "memory"); break;
            case 44: asm volatile("buffer_load_dwordx4 %0, %1, %2, %3 offen" : "={v[224:227]}"(wraw[44]) : "v"(wa), "s"(sw), "s"(1441792) : "memory"); break;
            case 45: asm volatile("buffer_load_dwordx4 %0, %1, %2, %3 offen" : "={v[228:231]}"(wraw[45]) : "v"(wa), "s"(sw), "s"(1474560) : "memory"); break;
            case 46: asm volatile("buffer_load_dwordx4 %0, %1, %2, %3 offen" : "={v[232:235]}"(wraw[46]) : "v"(wa), "s"(sw), "s"(1507328) : "memory"); break;
            case 47: asm volatile("buffer_load_dwordx4 %0, %1, %2, %3 offen" : "={v[236:239]}"(wraw[47]) : "v"(wa), "s"(sw), "s"(1540096) : "memory"); break;
            case 48: asm volatile("buffer_load_dwordx4 %0, %1, %2, %3 offen" : "={v[240:243]}"(wraw[48]) : "v"(wa), "s"(sw), "s"(1572864) : "memory"); break;
            case 49: asm volatile("buffer_load_dwordx4 %0, %1, %2, %3 offen" : "={v[244:247]}"(wraw[49]) : "v"(wa), "s"(sw), "s"(1605632) : "memory"); break;
            case 50: asm volatile("buffer_load_dwordx4 %0, %1, %2, %3 offen" : "={v[248:251]}"(wraw[50]) : "v"(wa), "s"(sw), "s"(1638400) : "memory"); break;
            case 51: asm volatile("buffer_load_dwordx4 %0, %1, %2, %3 offen" : "={v[252:255]}"(wraw[51]) : "v"(wa), "s"(sw), "s"(1671168) : "memory"); break;
            case 52: asm volatile("buffer_load_dwordx4 %0, %1, %2, %3 offen" : "={a[80:83]}"(wraw[52]) : "v"(wa), "s"(sw), "s"(1703936) : "memory"); break;
            case 53: asm volatile("buffer_load_dwordx4 %0, %1, %2, %3 offen" : "={a[84:87]}"(wraw[53]) : "v"(wa), "s"(sw), "s"(1736704) : "memory"); break;
            case 54: asm volatile("buffer_load_dwordx4 %0, %1, %2, %3 offen" : "={a[88:91]}"(wraw[54]) : "v"(wa), "s"(sw), "s"(1769472) : "memory"); break;
            case 55: asm volatile("buffer_load_dwordx4 %0, %1, %2, %3 offen" : "={a[92:95]}"(wraw[55]) : "v"(wa), "s"(sw), "s"(1802240) : "memory"); break;
            case 56: asm volatile("buffer_load_dwordx4 %0, %1, %2, %3 offen" : "={a[96:99]}"(wraw[56]) : "v"(wa), "s"(sw), "s"(1835008) : "memory"); break;
            case 57: asm volatile("buffer_load_dwordx4 %0, %1, %2, %3 offen" : "={a[100:103]}"(wraw[57]) : "v"(wa), "s"(sw), "s"(1867776) : "memory"); break;
            case 58: asm volatile("buffer_load_dwordx4 %0, %1, %2, %3 offen" : "={a[104:107]}"(wraw[58]) : "v"(wa), "s"(sw), "s"(1900544) : "memory"); break;
            case 59: asm volatile("buffer_load_dwordx4 %0, %1, %2, %3 offen" : "={a[108:111]}"(wraw[59]) : "v"(wa), "s"(sw), "s"(1933312) : "memory"); break;
            case 60: asm volatile("buffer_load_dwordx4 %0, %1, %2, %3 offen" : "={a[112:115]}"(wraw[60]) : "v"(wa), "s"(sw), "s"(1966080) : "memory"); break;
            case 61: asm volatile("buffer_load_dwordx4 %0, %1, %2, %3 offen" : "={a[116:119]}"(wraw[61]) : "v"(wa), "s"(sw), "s"(1998848) : "memory"); break;
            case 62: asm volatile("buffer_load_dwordx4 %0, %1, %2, %3 offen" : "={a[120:123]}"(wraw[62]) : "v"(wa), "s"(sw), "s"(2031616) : "memory"); break;
            case 63: asm volatile("buffer_load_dwordx4 %0, %1, %2, %3 offen" : "={a[124:127]}"(wraw[63]) : "v"(wa), "s"(sw), "s"(2064384) : "memory"); break;
        } };
        const int mt = 8 + (i32 & 7);
        gemm<0, 4, 8, 4, 2>(r, p.Aq, p.Wq, mt * 128, hp * 256, lds, W_QX, wv, ROT1 * (i32 >> 2), nullptr, false, nullptr, 0, 0,
                            [&](int i) {
#pragma unroll
                                for (int q = 2 * i; q < 2 * i + 2; ++q) { if (q < WO_IN) wo_load(q); else asm volatile("buffer_load_dword %0, %1, %2, 0 offen" : "+v"(wd) : "v"(0xF0000000u), "s"(sw) : "memory"); } });
        flags(mt, 1, 2 * hp, 2, 2);
        asm volatile("" : "+v"(wd));
#pragma unroll
        for (int i = WO_IN; i < 64; ++i) wo_load(i);
        oproj_block(p, r, lds, wv, i32, x, j, wraw, tag);
    }
}

__device__ __forceinline__ void help_block(const Params& p, rsrc_t r, unsigned char* lds, const int wv, const int k, const unsigned tag) {
    const int j = -32 + k; (void)j;
    if (HELP_D) wait_long(rtnow() + HELP_D);
    prep_block(p, r, k, wv, tag);
    if (!conv_block(p, r, k, lds, wv, j, tag)) return;
    attn_block(p, r, lds, wv, k, j, tag);
}

__global__ void __launch_bounds__(256, 1) k_main(Params p) {
    extern __shared__ __attribute__((aligned(16))) unsigned char lds[];
    rsrc_t r = __builtin_amdgcn_make_buffer_rsrc(p.ws, 0, W_TOTAL, 0x00020000);
    const int wv = __builtin_amdgcn_readfirstlane(threadIdx.x >> 6);
    int role = blockIdx.x;
    unsigned xcc; asm volatile("s_getreg_b32 %0, hwreg(HW_REG_XCC_ID)" : "=s"(xcc)); xcc &= 7u;
    unsigned near = 0u, old = 0u, slot = (xcc >> 2) * 128u + (blockIdx.x >> 4) * 8u + (xcc & 3u) * 2u + ((blockIdx.x >> 3) & 1u);
    const unsigned tag = __builtin_amdgcn_readfirstlane(ld4(r, (threadIdx.x & 63) == 1 ? F_CLM + slot * 64 + 4 : F_EP)) * 2654435761u + 0x9E3779B9u;
    if (threadIdx.x == 0) old = __hip_atomic_exchange((unsigned*)(p.ws + F_CLM + slot * 64), tag, __ATOMIC_RELAXED, __HIP_MEMORY_SCOPE_AGENT);
    near = 126u;
    if (xcc < 4u && ((CALM >> (slot >> 5)) & 1u)) {
        const unsigned gr = (slot >> 5) & 3u;
        const rsrc_t rh = __builtin_amdgcn_make_buffer_rsrc(p.ws + hx_base(p.ws), 0, U_END * 16384, 0x00020000);
        const unsigned ca = (threadIdx.x & 63) != 0 ? 0xF0000000u : (slot & 127u) * 16384u + (unsigned)wv * 2048u + CAL_O;
        const v4u z = {~0u, ~0u, ~0u, ~0u}; unsigned best0 = ~0u, best1 = ~0u;
        for (int i = 0; i < CAL_N; ++i) {
            const unsigned t0 = (unsigned)__builtin_amdgcn_s_memtime(); __builtin_amdgcn_s_waitcnt(0);
            st16(rh, ca, z); DRAIN();
            const unsigned t1 = (unsigned)__builtin_amdgcn_s_memtime(); __builtin_amdgcn_s_waitcnt(0);
            st16(rh, ca + 8192, z); DRAIN();
            const unsigned t2 = (unsigned)__builtin_amdgcn_s_memtime(); __builtin_amdgcn_s_waitcnt(0);
            best0 = min(best0, t1 - t0); best1 = min(best1, t2 - t1); }
        near = gr * 2u + (best1 < best0 ? 1u : 0u);
    }
    if (threadIdx.x == 0) {
        for (int i = 0; i < 256 && old == tag; ++i) { slot = (slot + 1u) & 255u; old = __hip_atomic_exchange((unsigned*)(p.ws + F_CLM + slot * 64), tag, __ATOMIC_RELAXED, __HIP_MEMORY_SCOPE_AGENT); }
        *(volatile __attribute__((address_space(3))) int*)(size_t)(unsigned)(size_t)lds = (int)slot;
    }
    __syncthreads();
    role = __builtin_amdgcn_readfirstlane(*(volatile __attribute__((address_space(3))) int*)(size_t)(unsigned)(size_t)lds) & 255;
    __syncthreads();
    if (role >= 128) bulk_block(p, r, lds, wv, role - 128, tag
        ); else if (rec_block(p, r, lds, wv, role, near, tag)) help_block(p, r, lds, wv, (role >> 3) * 2 + (role & 1), tag);
}

extern "C" void kernel_launch(void* const* d_in, const int* in_sizes, int n_in, void* d_out, int out_size, void* d_ws, size_t ws_size, hipStream_t stream) {
    Params p;
    p.Aq = (const bf16_t*)d_in[0]; p.Ak = (const bf16_t*)d_in[1]; p.Av = (const bf16_t*)d_in[2];
    p.shift_k = (const bf16_t*)d_in[3]; p.ssm_k = (const bf16_t*)d_in[4]; p.ssm_D = (const bf16_t*)d_in[5];
    p.mem0 = (const bf16_t*)d_in[7]; p.Wq = (const bf16_t*)d_in[8]; p.Wk = (const bf16_t*)d_in[9];
    p.Wv = (const bf16_t*)d_in[10]; p.Wo = (const bf16_t*)d_in[11]; p.gate_w = (const bf16_t*)d_in[13];
    p.out = (bf16_t*)d_out; p.ws = (unsigned char*)d_ws;
    (void)hipFuncSetAttribute((const void*)k_main, hipFuncAttributeMaxDynamicSharedMemorySize, L_TOTAL);
    k_main<<<256, 256, L_TOTAL, stream>>>(p);
}
```

```cpp
#include <hip/hip_runtime.h>
#include <stdint.h>
#include <type_traits>
typedef unsigned short bf16_t;
using bf16x8 = __attribute__((ext_vector_type(8))) short;
using f32x4 = __attribute__((ext_vector_type(4))) float;
typedef unsigned v4u __attribute__((ext_vector_type(4)));
typedef unsigned v2u __attribute__((ext_vector_type(2)));
typedef __amdgpu_buffer_rsrc_t rsrc_t;

typedef __bf16 hbf2 __attribute__((ext_vector_type(2)));
typedef float f32x2 __attribute__((ext_vector_type(2)));
__device__ __forceinline__ unsigned pack2(float a, float b) { f32x2 v = {a, b}; return __builtin_bit_cast(unsigned, __builtin_convertvector(v, hbf2)); }
__device__ __forceinline__ float rbfh(float f) { return __uint_as_float(pack2(0.f, f)); }
__device__ __forceinline__ f32x2 rbf2(f32x2 f) { return (f32x2){rbfh(f.x), rbfh(f.y)}; }
__device__ __forceinline__ unsigned nosent(unsigned x) {
    typedef unsigned short us2 __attribute__((ext_vector_type(2)));
    const us2 a = __builtin_bit_cast(us2, x), c = {(unsigned short)0xFFFE, (unsigned short)0xFFFE}; return __builtin_bit_cast(unsigned, __builtin_elementwise_min(a, c)); }

constexpr int S = 2048, D = 1024, H = 32, DH = 128, M = 8, SEG = 32, NSEG = 64;

constexpr unsigned W_FLG = 0;
constexpr unsigned F_KVQ = W_FLG;
constexpr unsigned F_EP = W_FLG + 32768;
constexpr unsigned F_ERR = W_FLG + 32768 + 256;
constexpr unsigned F_PREP = W_FLG + 32768 + 512;
constexpr unsigned F_CAL = W_FLG + 73728;
constexpr unsigned F_CLM = W_FLG + 77824;
constexpr unsigned F_CONV = W_FLG + 33792;
constexpr unsigned F_PROG = W_FLG + 36864;
constexpr unsigned F_ATT = W_FLG + 40960;
constexpr unsigned F_RDY = W_FLG + 49152;
constexpr unsigned FLG_BYTES = 94208;
constexpr unsigned HO_KVM = W_FLG + FLG_BYTES;
constexpr unsigned W_A = HO_KVM + 64 * 32 * 4096;
constexpr unsigned W_GATE = W_A + S * D * 2;
constexpr unsigned W_QX = W_GATE + 8192;
constexpr unsigned W_KX = W_QX + S * 4096 * 2;
constexpr unsigned W_VF = W_KX + S * 4096 * 2;
constexpr unsigned W_OX = W_VF + 64 * 32 * 8192;
constexpr unsigned DBG_BYTES = 0;
constexpr unsigned W_DBG = W_OX + S * 4096 * 2;
constexpr unsigned W_U = W_DBG + DBG_BYTES;
constexpr unsigned W_TOTAL = W_U + S * D * 2;

__device__ __forceinline__ v4u ld16(rsrc_t r, unsigned off) { return __builtin_amdgcn_raw_buffer_load_b128(r, off, 0, 16); }
__device__ __forceinline__ v2u ld8(rsrc_t r, unsigned off) { return __builtin_amdgcn_raw_buffer_load_b64(r, off, 0, 16); }
__device__ __forceinline__ unsigned ld4(rsrc_t r, unsigned off) { return __builtin_amdgcn_raw_buffer_load_b32(r, off, 0, 16); }
__device__ __forceinline__ void st16(rsrc_t r, unsigned off, v4u v) { __builtin_amdgcn_raw_buffer_store_b128(v, r, off, 0, 16); }
__device__ __forceinline__ void st8(rsrc_t r, unsigned off, v2u v) { __builtin_amdgcn_raw_buffer_store_b64(v, r, off, 0, 16); }
__device__ __forceinline__ void st4(rsrc_t r, unsigned off, unsigned v) { __builtin_amdgcn_raw_buffer_store_b32(v, r, off, 0, 16); }
__device__ __forceinline__ bf16x8 as_frag(v4u v) { return __builtin_bit_cast(bf16x8, v); }
#define D1 134
#define D2 90
#define D3 64
#define LB0 16
#define LC0 7
#define LA0 8
#define TR_UP 4
#define TR_DN 2
#define L_DEC 3
#define STAG 1
#define UP_SH 2
#define EARLY_FROM 58
#define OUT_WARM 0
#define AROT 0
#define P_SLP 2
#define O_SLP 2
#define W_NT 0
#define TOL 2
#define A_SLP 0
#define PEN2 20
#define A_UP 24
#define CT0 2
#define CPF 1
#define PH_M 3
#define SKEW 0
#define SKEW1 0
#define XT 1
#define TRK 3
#define WO_NT 0
#define WO_TRK 1
#define WIN 3
#define ROT0 3
#define ROT1 2
#define ROT2 2
#define B_NT 0
#define CALM 15
#define R4M 1
#define HX_SH 0u
#define FINE_T 1
#define FINE_N 0
#define FINE_A 0
#define O_ND 12
#define PEN0 32
#define X0C 20
#define X0A 16
#define X1A 8
#define PEN 24
#define MEMBAR() asm volatile("" ::: "memory")
#define ENDPGM() asm volatile("s_endpgm" ::: "memory")
#define DRAIN() asm volatile("s_waitcnt vmcnt(0)" ::: "memory")

__device__ __forceinline__ float xor16_max(float x) { auto r = __builtin_amdgcn_permlane16_swap(__float_as_uint(x), __float_as_uint(x), false, false); return fmaxf(__uint_as_float(r[0]), __uint_as_float(r[1])); }
__device__ __forceinline__ float xor32_max(float x) { auto r = __builtin_amdgcn_permlane32_swap(__float_as_uint(x), __float_as_uint(x), false, false); return fmaxf(__uint_as_float(r[0]), __uint_as_float(r[1])); }
__device__ __forceinline__ float xor16_add(float x) { auto r = __builtin_amdgcn_permlane16_swap(__float_as_uint(x), __float_as_uint(x), false, false); return __uint_as_float(r[0]) + __uint_as_float(r[1]); }
__device__ __forceinline__ float xor32_add(float x) { auto r = __builtin_amdgcn_permlane32_swap(__float_as_uint(x), __float_as_uint(x), false, false); return __uint_as_float(r[0]) + __uint_as_float(r[1]); }

typedef short v4i16_t __attribute__((ext_vector_type(4)));
__device__ __forceinline__ v2u trb(unsigned lds_addr) { return __builtin_bit_cast(v2u, __builtin_amdgcn_ds_read_tr16_b64_v4i16((__attribute__((address_space(3))) v4i16_t*)(size_t)lds_addr)); }
__device__ __forceinline__ int otid_(int wv) { int t; asm volatile("v_mbcnt_lo_u32_b32 %0, -1, 0\n\tv_mbcnt_hi_u32_b32 %0, -1, %0" : "=v"(t)); return wv * 64 + t; }
#define otid() otid_(wv)
__device__ __forceinline__ bool wait_word(rsrc_t r, unsigned off, unsigned want, unsigned code) {
    for (unsigned spin = 0;; ++spin) { MEMBAR();
        const unsigned v = __builtin_amdgcn_readfirstlane(ld4(r, off));
        if (v >= want) return true;
        if (spin > (1u << 20)) { st4(r, F_ERR, code); return false; }
        __builtin_amdgcn_s_sleep(8); }
}

__device__ __forceinline__ bool wait_prep(rsrc_t r, int l, unsigned tag) {
    for (unsigned spin = 0;; ++spin) { MEMBAR();
        const unsigned v = ld4(r, F_PREP + (l & 31) * 4);
        if (__all(v == tag)) return true;
        if (spin > (1u << 20)) { st4(r, F_ERR, 512u); return false; }
        __builtin_amdgcn_s_sleep(8); }
}
__device__ __forceinline__ bool wait_cal(rsrc_t r, int l, unsigned tag, v4u (&q)[4]) {
    for (unsigned spin = 0;; ++spin) { MEMBAR();
        bool ok = true;
#pragma unroll
        for (int i = 0; i < 4; ++i) { q[i] = ld16(r, F_CAL + l * 64 + i * 16); }
#pragma unroll
        for (int i = 0; i < 4; ++i) ok &= (q[i].x == tag) & (q[i].z == tag);
        if (__all(ok)) return true;
        if (spin > (1u << 20)) { st4(r, F_ERR, 8u); return false; }
        __builtin_amdgcn_s_sleep(4); }
}
__device__ __forceinline__ unsigned rtnow() { return (unsigned)__builtin_amdgcn_s_memrealtime(); }
__device__ __forceinline__ void wait_long(unsigned target) { for (int i = 0; i < 4096; ++i) { if ((int)(rtnow() - target) >= 0) break; __builtin_amdgcn_s_sleep(8); } }
constexpr int L_TAB = 20480;
constexpr int L_QK = 24576;
__device__ __forceinline__ unsigned img(int f) { return (unsigned)(L_QK + f * 16 + (f >> 4) * 128); }
constexpr int L_FL = 30720, L_Z = L_FL + 128, L_LOG = 31744, L_W = 32768;
constexpr int L_TOTAL = 163840;
constexpr unsigned U_END = 128;
#define PMIN 76
#define V_SLP 2
#define R_SLP 1
constexpr unsigned WO_L = 98304;
#define HELP_D 300
#define V_DEL 16
#define XR 12
#define CAL_O 0u
#define H_SLP 0
#define CAL_N 2
#define STEP_MIN 180u

struct Params {
    const bf16_t *Aq, *Ak, *Av, *shift_k, *ssm_k, *ssm_D, *mem0, *Wq, *Wk, *Wv, *Wo, *gate_w;
    bf16_t* out; unsigned char* ws;
};
__host__ __device__ __forceinline__ unsigned hx_base(const void* ws) { const size_t a = (size_t)ws; return (unsigned)((((a + W_TOTAL + 0x7FFFFFu) >> 23) << 23) - a) + HX_SH; }

typedef short v4s __attribute__((ext_vector_type(4)));
typedef unsigned v32u __attribute__((ext_vector_type(32)));
typedef unsigned v16u __attribute__((ext_vector_type(16)));
typedef unsigned v8u __attribute__((ext_vector_type(8)));
typedef unsigned v12u __attribute__((ext_vector_type(12)));
__device__ __forceinline__ v4u mk_srd(const void* p, unsigned bytes) { const size_t a = (size_t)p; v4u d;
    d.x = __builtin_amdgcn_readfirstlane((unsigned)a); d.y = __builtin_amdgcn_readfirstlane((unsigned)(a >> 32) & 0xFFFFu); d.z = __builtin_amdgcn_readfirstlane(bytes); d.w = 0x00020000u; return d; }
__device__ __forceinline__ bf16x8 ldsa(unsigned addr) { return *(const __attribute__((address_space(3))) bf16x8*)(size_t)addr; }
__device__ __forceinline__ bool rec_block(const Params& p, rsrc_t r, unsigned char* lds, const int wv, const int b, const unsigned near, const unsigned tag) {
    const int w = wv;
    int tid = otid(), l = tid & 63, n16 = l & 15, g = l >> 4;
    const int x = (b & 7) >> 1, i32 = (b >> 3) * 2 + (b & 1);
    const unsigned hxo = hx_base(p.ws), OOB = 0xF0000000u;
    const v4u rhd = mk_srd(p.ws + hxo, U_END * 16384), wsd = mk_srd(p.ws, W_TOTAL);
    const rsrc_t rh = __builtin_amdgcn_make_buffer_rsrc(p.ws + hxo, 0, U_END * 16384, 0x00020000);
    const int G = x - 1, vi = G * 32 + i32, h = x == 0 ? i32 : vi >> 1;
    const unsigned pr_a = x == 3 ? OOB : F_PROG + h * 128 + (x == 0 ? 0 : 8 + (vi & 1) * 8);
    {
        const v4u ones = {0xFFFFFFFFu, 0xFFFFFFFFu, 0xFFFFFFFFu, 0xFFFFFFFFu};
#pragma unroll
        for (int i = 0; i < 4; ++i) st16(rh, b * 16384 + (w * 4 + i) * 1024 + l * 16, ones);
        st4(r, tid == 192 ? pr_a : OOB, x ? 63u : 0u);
    }
    const unsigned cal_a = l == 0 ? F_CAL + b * 32 + w * 8 : OOB;
    if (x == 3) { DRAIN(); st8(r, cal_a, (v2u){tag, near}); return true; }
    v4u cpf;
    if (CPF) {
        unsigned lo, hi, l2; asm volatile("s_getpc_b64 s[98:99]\n.Lpc%=:\n\ts_add_u32 %2, s98, rec_v_code-.Lpc%=\n\ts_add_u32 %0, s98, rec_s_code-.Lpc%=\n\ts_addc_u32 %1, s99, 0" : "=s"(lo), "=s"(hi), "=s"(l2) :: "s98", "s99", "scc");
        const size_t a = (((size_t)hi << 32) | lo) + (size_t)(long long)(x ? (int)(l2 - lo) : 0) + (size_t)(((i32 * 4 + w) % 6) * 1024 + l * 16);
        asm volatile("global_load_dwordx4 %0, %1, off" : "=v"(cpf) : "v"(a & ~(size_t)15) : "memory");
    }
    const int c = n16 >> 2;
    v32u wa[8], wg[4];
    auto votes = [&](unsigned& nm) -> bool {
        v4u q[4]; if (!wait_cal(r, l, tag, q)) return false;
        typedef __attribute__((address_space(3))) int li;
        li* cnt = (li*)(size_t)(w * 256);
        cnt[l] = 0;
        __builtin_amdgcn_wave_barrier();
#pragma unroll
        for (int i = 0; i < 8; ++i) { const unsigned y = (i & 1) ? q[i >> 1].w : q[i >> 1].y; __hip_atomic_fetch_add(cnt + ((y >> 1) & 63u), (y & 1u) ? 1 : -1, __ATOMIC_RELAXED, __HIP_MEMORY_SCOPE_WAVEFRONT); }
        __builtin_amdgcn_wave_barrier();
        const int c0 = cnt[l], c4 = cnt[0] + cnt[1] + cnt[2] + cnt[3];
        nm = (unsigned)__ballot((abs(c0) >= 64 ? c0 : c4) > 0);
        return true; };
    typedef __attribute__((address_space(3))) volatile unsigned lu32;
    const unsigned kvo = HO_KVM + h * 4096 + (x ? 2048 : 0);
    if (x == 0) {
        {
            const v4u sq = mk_srd(p.Wq, 1024 * 4096 * 2), sk = mk_srd(p.Wk, 1024 * 4096 * 2);
            const unsigned m0b = L_W + w * 1024, f = (c & 1) | ((g & 1) << 1) | ((c >> 1) << 2);
            const unsigned t0 = L_W + (16 * (g >> 1) + 4 * (g & 1) + (c & 1) + 8 * (c >> 1)) * 256 + (((2 * w) ^ f) << 5) + (n16 & 3) * 8;
            v16u pa;
            pa[0] = (4 * w + g) * 8192 + h * 256 + ((n16 ^ (((g & 1) | (w << 1)) << 1)) << 4);
#pragma unroll
            for (int q = 0; q < 8; ++q) pa[1 + q] = (t0 + ((w + (q >> 1)) & 3) * 32768) ^ ((q & 1) << 5);
            pa[9] = cal_a; pa[10] = tag; pa[11] = near; pa[12] = L_W + w * 32768 + l * 16; pa[13] = 0u; pa[14] = 0u; pa[15] = 0u;
    asm volatile(
        "s_add_u32 s80, %[m0b], 0\n\t"
        "s_mov_b32 s81, 0\n\t"
        "s_mov_b32 s82, 4\n\t"
        ".Ld0%=:\n\t"
        "s_mov_b32 m0, s80\n\t"
        "s_add_u32 s80, s80, 0x8000\n\t"
        "buffer_load_dwordx4 v112, %[sq], s81 offen lds\n\t"
        "s_add_u32 m0, m0, 0x1000\n\t"
        "s_add_u32 s81, s81, 0x20000\n\t"
        "buffer_load_dwordx4 v112, %[sq], s81 offen lds\n\t"
        "s_add_u32 m0, m0, 0x1000\n\t"
        "s_add_u32 s81, s81, 0x20000\n\t"
        "buffer_load_dwordx4 v112, %[sq], s81 offen lds\n\t"
        "s_add_u32 m0, m0, 0x1000\n\t"
        "s_add_u32 s81, s81, 0x20000\n\t"
        "buffer_load_dwordx4 v112, %[sq], s81 offen lds\n\t"
        "s_add_u32 m0, m0, 0x1000\n\t"
        "s_add_u32 s81, s81, 0x20000\n\t"
        "s_sub_u32 s82, s82, 1\n\t"
        "s_cmp_lg_u32 s82, 0\n\t"
        "s_cbranch_scc1 .Ld0%=\n\t"
        "s_add_u32 s80, %[m0b], 16384\n\t"
        "s_mov_b32 s81, 0\n\t"
        "s_mov_b32 s82, 4\n\t"
        ".Ld1%=:\n\t"
        "s_mov_b32 m0, s80\n\t"
        "s_add_u32 s80, s80, 0x8000\n\t"
        "buffer_load_dwordx4 v112, %[sk], s81 offen lds\n\t"
        "s_add_u32 m0, m0, 0x1000\n\t"
        "s_add_u32 s81, s81, 0x20000\n\t"
        "buffer_load_dwordx4 v112, %[sk], s81 offen lds\n\t"
        "s_add_u32 m0, m0, 0x1000\n\t"
        "s_add_u32 s81, s81, 0x20000\n\t"
        "buffer_load_dwordx4 v112, %[sk], s81 offen lds\n\t"
        "s_add_u32 m0, m0, 0x1000\n\t"
        "s_add_u32 s81, s81, 0x20000\n\t"
        "buffer_load_dwordx4 v112, %[sk], s81 offen lds\n\t"
        "s_add_u32 m0, m0, 0x1000\n\t"
        "s_add_u32 s81, s81, 0x20000\n\t"
        "s_sub_u32 s82, s82, 1\n\t"
        "s_cmp_lg_u32 s82, 0\n\t"
        "s_cbranch_scc1 .Ld1%=\n\t"
        "s_waitcnt vmcnt(32)\n\t"
        "buffer_store_dwordx2 v[122:123], v121, %[wsd], 0 offen sc1\n\t"
        "s_waitcnt vmcnt(17)\n\t"
        "s_barrier\n\t"
        "ds_read_b64_tr_b16 v[0:1], v113 offset:0\n\t"
        "ds_read_b64_tr_b16 v[2:3], v113 offset:512\n\t"
        "ds_read_b64_tr_b16 v[4:5], v113 offset:8192\n\t"
        "ds_read_b64_tr_b16 v[6:7], v113 offset:8704\n\t"
        "ds_read_b64_tr_b16 v[8:9], v114 offset:0\n\t"
        "ds_read_b64_tr_b16 v[10:11], v114 offset:512\n\t"
        "ds_read_b64_tr_b16 v[12:13], v114 offset:8192\n\t"
        "ds_read_b64_tr_b16 v[14:15], v114 offset:8704\n\t"
        "ds_read_b64_tr_b16 a[16:17], v115 offset:0\n\t"
        "ds_read_b64_tr_b16 a[18:19], v115 offset:512\n\t"
        "ds_read_b64_tr_b16 a[20:21], v115 offset:8192\n\t"
        "ds_read_b64_tr_b16 a[22:23], v115 offset:8704\n\t"
        "ds_read_b64_tr_b16 a[24:25], v116 offset:0\n\t"
        "ds_read_b64_tr_b16 a[26:27], v116 offset:512\n\t"
        "ds_read_b64_tr_b16 a[28:29], v116 offset:8192\n\t"
        "ds_read_b64_tr_b16 a[30:31], v116 offset:8704\n\t"
        "ds_read_b64_tr_b16 a[32:33], v117 offset:0\n\t"
        "ds_read_b64_tr_b16 a[34:35], v117 offset:512\n\t"
        "ds_read_b64_tr_b16 a[36:37], v117 offset:8192\n\t"
        "ds_read_b64_tr_b16 a[38:39], v117 offset:8704\n\t"
        "ds_read_b64_tr_b16 a[40:41], v118 offset:0\n\t"
        "ds_read_b64_tr_b16 a[42:43], v118 offset:512\n\t"
        "ds_read_b64_tr_b16 a[44:45], v118 offset:8192\n\t"
        "ds_read_b64_tr_b16 a[46:47], v118 offset:8704\n\t"
        "ds_read_b64_tr_b16 a[48:49], v119 offset:0\n\t"
        "ds_read_b64_tr_b16 a[50:51], v119 offset:512\n\t"
        "ds_read_b64_tr_b16 a[52:53], v119 offset:8192\n\t"
        "ds_read_b64_tr_b16 a[54:55], v119 offset:8704\n\t"
        "ds_read_b64_tr_b16 a[56:57], v120 offset:0\n\t"
        "ds_read_b64_tr_b16 a[58:59], v120 offset:512\n\t"
        "ds_read_b64_tr_b16 a[60:61], v120 offset:8192\n\t"
        "ds_read_b64_tr_b16 a[62:63], v120 offset:8704\n\t"
        "s_waitcnt lgkmcnt(0)\n\t"
        "s_barrier\n\t"
        "s_add_u32 s80, %[m0b], 0\n\t"
        "s_mov_b32 s81, 2097152\n\t"
        "s_mov_b32 s82, 4\n\t"
        ".Ld2%=:\n\t"
        "s_mov_b32 m0, s80\n\t"
        "s_add_u32 s80, s80, 0x8000\n\t"
        "buffer_load_dwordx4 v112, %[sq], s81 offen lds\n\t"
        "s_add_u32 m0, m0, 0x1000\n\t"
        "s_add_u32 s81, s81, 0x20000\n\t"
        "buffer_load_dwordx4 v112, %[sq], s81 offen lds\n\t"
        "s_add_u32 m0, m0, 0x1000\n\t"
        "s_add_u32 s81, s81, 0x20000\n\t"
        "buffer_load_dwordx4 v112, %[sq], s81 offen lds\n\t"
        "s_add_u32 m0, m0, 0x1000\n\t"
        "s_add_u32 s81, s81, 0x20000\n\t"
        "buffer_load_dwordx4 v112, %[sq], s81 offen lds\n\t"
        "s_add_u32 m0, m0, 0x1000\n\t"
        "s_add_u32 s81, s81, 0x20000\n\t"
        "s_sub_u32 s82, s82, 1\n\t"
        "s_cmp_lg_u32 s82, 0\n\t"
        "s_cbranch_scc1 .Ld2%=\n\t"
        "s_waitcnt vmcnt(17)\n\t"
        "s_barrier\n\t"
        "ds_read_b64_tr_b16 v[16:17], v113 offset:16384\n\t"
        "ds_read_b64_tr_b16 v[18:19], v113 offset:16896\n\t"
        "ds_read_b64_tr_b16 v[20:21], v113 offset:24576\n\t"
        "ds_read_b64_tr_b16 v[22:23], v113 offset:25088\n\t"
        "ds_read_b64_tr_b16 v[24:25], v114 offset:16384\n\t"
        "ds_read_b64_tr_b16 v[26:27], v114 offset:16896\n\t"
        "ds_read_b64_tr_b16 v[28:29], v114 offset:24576\n\t"
        "ds_read_b64_tr_b16 v[30:31], v114 offset:25088\n\t"
        "ds_read_b64_tr_b16 a[80:81], v115 offset:16384\n\t"
        "ds_read_b64_tr_b16 a[82:83], v115 offset:16896\n\t"
        "ds_read_b64_tr_b16 a[84:85], v115 offset:24576\n\t"
        "ds_read_b64_tr_b16 a[86:87], v115 offset:25088\n\t"
        "ds_read_b64_tr_b16 a[88:89], v116 offset:16384\n\t"
        "ds_read_b64_tr_b16 a[90:91], v116 offset:16896\n\t"
        "ds_read_b64_tr_b16 a[92:93], v116 offset:24576\n\t"
        "ds_read_b64_tr_b16 a[94:95], v116 offset:25088\n\t"
        "ds_read_b64_tr_b16 a[96:97], v117 offset:16384\n\t"
        "ds_read_b64_tr_b16 a[98:99], v117 offset:16896\n\t"
        "ds_read_b64_tr_b16 a[100:101], v117 offset:24576\n\t"
        "ds_read_b64_tr_b16 a[102:103], v117 offset:25088\n\t"
        "ds_read_b64_tr_b16 a[104:105], v118 offset:16384\n\t"
        "ds_read_b64_tr_b16 a[106:107], v118 offset:16896\n\t"
        "ds_read_b64_tr_b16 a[108:109], v118 offset:24576\n\t"
        "ds_read_b64_tr_b16 a[110:111], v118 offset:25088\n\t"
        "ds_read_b64_tr_b16 a[112:113], v119 offset:16384\n\t"
        "ds_read_b64_tr_b16 a[114:115], v119 offset:16896\n\t"
        "ds_read_b64_tr_b16 a[116:117], v119 offset:24576\n\t"
        "ds_read_b64_tr_b16 a[118:119], v119 offset:25088\n\t"
        "ds_read_b64_tr_b16 a[120:121], v120 offset:16384\n\t"
        "ds_read_b64_tr_b16 a[122:123], v120 offset:16896\n\t"
        "ds_read_b64_tr_b16 a[124:125], v120 offset:24576\n\t"
        "ds_read_b64_tr_b16 a[126:127], v120 offset:25088\n\t"
        "s_waitcnt lgkmcnt(0)\n\t"
        "s_barrier\n\t"
        "s_add_u32 s80, %[m0b], 16384\n\t"
        "s_mov_b32 s81, 2097152\n\t"
        "s_mov_b32 s82, 4\n\t"
        ".Ld3%=:\n\t"
        "s_mov_b32 m0, s80\n\t"
        "s_add_u32 s80, s80, 0x8000\n\t"
        "buffer_load_dwordx4 v112, %[sk], s81 offen lds\n\t"
        "s_add_u32 m0, m0, 0x1000\n\t"
        "s_add_u32 s81, s81, 0x20000\n\t"
        "buffer_load_dwordx4 v112, %[sk], s81 offen lds\n\t"
        "s_add_u32 m0, m0, 0x1000\n\t"
        "s_add_u32 s81, s81, 0x20000\n\t"
        "buffer_load_dwordx4 v112, %[sk], s81 offen lds\n\t"
        "s_add_u32 m0, m0, 0x1000\n\t"
        "s_add_u32 s81, s81, 0x20000\n\t"
        "buffer_load_dwordx4 v112, %[sk], s81 offen lds\n\t"
        "s_add_u32 m0, m0, 0x1000\n\t"
        "s_add_u32 s81, s81, 0x20000\n\t"
        "s_sub_u32 s82, s82, 1\n\t"
        "s_cmp_lg_u32 s82, 0\n\t"
        "s_cbranch_scc1 .Ld3%=\n\t"
        "s_waitcnt vmcnt(16)\n\t"
        "s_barrier\n\t"
        "ds_read_b64_tr_b16 v[32:33], v113 offset:0\n\t"
        "ds_read_b64_tr_b16 v[34:35], v113 offset:512\n\t"
        "ds_read_b64_tr_b16 v[36:37], v113 offset:8192\n\t"
        "ds_read_b64_tr_b16 v[38:39], v113 offset:8704\n\t"
        "ds_read_b64_tr_b16 v[40:41], v114 offset:0\n\t"
        "ds_read_b64_tr_b16 v[42:43], v114 offset:512\n\t"
        "ds_read_b64_tr_b16 v[44:45], v114 offset:8192\n\t"
        "ds_read_b64_tr_b16 v[46:47], v114 offset:8704\n\t"
        "ds_read_b64_tr_b16 a[144:145], v115 offset:0\n\t"
        "ds_read_b64_tr_b16 a[146:147], v115 offset:512\n\t"
        "ds_read_b64_tr_b16 a[148:149], v115 offset:8192\n\t"
        "ds_read_b64_tr_b16 a[150:151], v115 offset:8704\n\t"
        "ds_read_b64_tr_b16 a[152:153], v116 offset:0\n\t"
        "ds_read_b64_tr_b16 a[154:155], v116 offset:512\n\t"
        "ds_read_b64_tr_b16 a[156:157], v116 offset:8192\n\t"
        "ds_read_b64_tr_b16 a[158:159], v116 offset:8704\n\t"
        "ds_read_b64_tr_b16 a[160:161], v117 offset:0\n\t"
        "ds_read_b64_tr_b16 a[162:163], v117 offset:512\n\t"
        "ds_read_b64_tr_b16 a[164:165], v117 offset:8192\n\t"
        "ds_read_b64_tr_b16 a[166:167], v117 offset:8704\n\t"
        "ds_read_b64_tr_b16 a[168:169], v118 offset:0\n\t"
        "ds_read_b64_tr_b16 a[170:171], v118 offset:512\n\t"
        "ds_read_b64_tr_b16 a[172:173], v118 offset:8192\n\t"
        "ds_read_b64_tr_b16 a[174:175], v118 offset:8704\n\t"
        "ds_read_b64_tr_b16 a[176:177], v119 offset:0\n\t"
        "ds_read_b64_tr_b16 a[178:179], v119 offset:512\n\t"
        "ds_read_b64_tr_b16 a[180:181], v119 offset:8192\n\t"
        "ds_read_b64_tr_b16 a[182:183], v119 offset:8704\n\t"
        "ds_read_b64_tr_b16 a[184:185], v120 offset:0\n\t"
        "ds_read_b64_tr_b16 a[186:187], v120 offset:512\n\t"
        "ds_read_b64_tr_b16 a[188:189], v120 offset:8192\n\t"
        "ds_read_b64_tr_b16 a[190:191], v120 offset:8704\n\t"
        "s_waitcnt lgkmcnt(0)\n\t"
        "s_barrier\n\t"
        "s_add_u32 s80, %[m0b], 0\n\t"
        "s_mov_b32 s81, 4194304\n\t"
        "s_mov_b32 s82, 4\n\t"
        ".Ld4%=:\n\t"
        "s_mov_b32 m0, s80\n\t"
        "s_add_u32 s80, s80, 0x8000\n\t"
        "buffer_load_dwordx4 v112, %[sq], s81 offen lds\n\t"
        "s_add_u32 m0, m0, 0x1000\n\t"
        "s_add_u32 s81, s81, 0x20000\n\t"
        "buffer_load_dwordx4 v112, %[sq], s81 offen lds\n\t"
        "s_add_u32 m0, m0, 0x1000\n\t"
        "s_add_u32 s81, s81, 0x20000\n\t"
        "buffer_load_dwordx4 v112, %[sq], s81 offen lds\n\t"
        "s_add_u32 m0, m0, 0x1000\n\t"
        "s_add_u32 s81, s81, 0x20000\n\t"
        "buffer_load_dwordx4 v112, %[sq], s81 offen lds\n\t"
        "s_add_u32 m0, m0, 0x1000\n\t"
        "s_add_u32 s81, s81, 0x20000\n\t"
        "s_sub_u32 s82, s82, 1\n\t"
        "s_cmp_lg_u32 s82, 0\n\t"
        "s_cbranch_scc1 .Ld4%=\n\t"
        "s_waitcnt vmcnt(16)\n\t"
        "s_barrier\n\t"
        "ds_read_b64_tr_b16 v[48:49], v113 offset:16384\n\t"
        "ds_read_b64_tr_b16 v[50:51], v113 offset:16896\n\t"
        "ds_read_b64_tr_b16 v[52:53], v113 offset:24576\n\t"
        "ds_read_b64_tr_b16 v[54:55], v113 offset:25088\n\t"
        "ds_read_b64_tr_b16 v[56:57], v114 offset:16384\n\t"
        "ds_read_b64_tr_b16 v[58:59], v114 offset:16896\n\t"
        "ds_read_b64_tr_b16 v[60:61], v114 offset:24576\n\t"
        "ds_read_b64_tr_b16 v[62:63], v114 offset:25088\n\t"
        "ds_read_b64_tr_b16 a[208:209], v115 offset:16384\n\t"
        "ds_read_b64_tr_b16 a[210:211], v115 offset:16896\n\t"
        "ds_read_b64_tr_b16 a[212:213], v115 offset:24576\n\t"
        "ds_read_b64_tr_b16 a[214:215], v115 offset:25088\n\t"
        "ds_read_b64_tr_b16 a[216:217], v116 offset:16384\n\t"
        "ds_read_b64_tr_b16 a[218:219], v116 offset:16896\n\t"
        "ds_read_b64_tr_b16 a[220:221], v116 offset:24576\n\t"
        "ds_read_b64_tr_b16 a[222:223], v116 offset:25088\n\t"
        "ds_read_b64_tr_b16 a[224:225], v117 offset:16384\n\t"
        "ds_read_b64_tr_b16 a[226:227], v117 offset:16896\n\t"
        "ds_read_b64_tr_b16 a[228:229], v117 offset:24576\n\t"
        "ds_read_b64_tr_b16 a[230:231], v117 offset:25088\n\t"
        "ds_read_b64_tr_b16 a[232:233], v118 offset:16384\n\t"
        "ds_read_b64_tr_b16 a[234:235], v118 offset:16896\n\t"
        "ds_read_b64_tr_b16 a[236:237], v118 offset:24576\n\t"
        "ds_read_b64_tr_b16 a[238:239], v118 offset:25088\n\t"
        "ds_read_b64_tr_b16 a[240:241], v119 offset:16384\n\t"
        "ds_read_b64_tr_b16 a[242:243], v119 offset:16896\n\t"
        "ds_read_b64_tr_b16 a[244:245], v119 offset:24576\n\t"
        "ds_read_b64_tr_b16 a[246:247], v119 offset:25088\n\t"
        "ds_read_b64_tr_b16 a[248:249], v120 offset:16384\n\t"
        "ds_read_b64_tr_b16 a[250:251], v120 offset:16896\n\t"
        "ds_read_b64_tr_b16 a[252:253], v120 offset:24576\n\t"
        "ds_read_b64_tr_b16 a[254:255], v120 offset:25088\n\t"
        "s_waitcnt lgkmcnt(0)\n\t"
        "s_barrier\n\t"
        "s_add_u32 s80, %[m0b], 16384\n\t"
        "s_mov_b32 s81, 4194304\n\t"
        "s_mov_b32 s82, 4\n\t"
        ".Ld5%=:\n\t"
        "s_mov_b32 m0, s80\n\t"
        "s_add_u32 s80, s80, 0x8000\n\t"
        "buffer_load_dwordx4 v112, %[sk], s81 offen lds\n\t"
        "s_add_u32 m0, m0, 0x1000\n\t"
        "s_add_u32 s81, s81, 0x20000\n\t"
        "buffer_load_dwordx4 v112, %[sk], s81 offen lds\n\t"
        "s_add_u32 m0, m0, 0x1000\n\t"
        "s_add_u32 s81, s81, 0x20000\n\t"
        "buffer_load_dwordx4 v112, %[sk], s81 offen lds\n\t"
        "s_add_u32 m0, m0, 0x1000\n\t"
        "s_add_u32 s81, s81, 0x20000\n\t"
        "buffer_load_dwordx4 v112, %[sk], s81 offen lds\n\t"
        "s_add_u32 m0, m0, 0x1000\n\t"
        "s_add_u32 s81, s81, 0x20000\n\t"
        "s_sub_u32 s82, s82, 1\n\t"
        "s_cmp_lg_u32 s82, 0\n\t"
        "s_cbranch_scc1 .Ld5%=\n\t"
        "s_waitcnt vmcnt(16)\n\t"
        "s_barrier\n\t"
        "ds_read_b64_tr_b16 v[128:129], v113 offset:0\n\t"
        "ds_read_b64_tr_b16 v[130:131], v113 offset:512\n\t"
        "ds_read_b64_tr_b16 v[132:133], v113 offset:8192\n\t"
        "ds_read_b64_tr_b16 v[134:135], v113 offset:8704\n\t"
        "ds_read_b64_tr_b16 v[136:137], v114 offset:0\n\t"
        "ds_read_b64_tr_b16 v[138:139], v114 offset:512\n\t"
        "ds_read_b64_tr_b16 v[140:141], v114 offset:8192\n\t"
        "ds_read_b64_tr_b16 v[142:143], v114 offset:8704\n\t"
        "ds_read_b64_tr_b16 v[144:145], v115 offset:0\n\t"
        "ds_read_b64_tr_b16 v[146:147], v115 offset:512\n\t"
        "ds_read_b64_tr_b16 v[148:149], v115 offset:8192\n\t"
        "ds_read_b64_tr_b16 v[150:151], v115 offset:8704\n\t"
        "ds_read_b64_tr_b16 v[152:153], v116 offset:0\n\t"
        "ds_read_b64_tr_b16 v[154:155], v116 offset:512\n\t"
        "ds_read_b64_tr_b16 v[156:157], v116 offset:8192\n\t"
        "ds_read_b64_tr_b16 v[158:159], v116 offset:8704\n\t"
        "ds_read_b64_tr_b16 v[160:161], v117 offset:0\n\t"
        "ds_read_b64_tr_b16 v[162:163], v117 offset:512\n\t"
        "ds_read_b64_tr_b16 v[164:165], v117 offset:8192\n\t"
        "ds_read_b64_tr_b16 v[166:167], v117 offset:8704\n\t"
        "ds_read_b64_tr_b16 v[168:169], v118 offset:0\n\t"
        "ds_read_b64_tr_b16 v[170:171], v118 offset:512\n\t"
        "ds_read_b64_tr_b16 v[172:173], v118 offset:8192\n\t"
        "ds_read_b64_tr_b16 v[174:175], v118 offset:8704\n\t"
        "ds_read_b64_tr_b16 v[176:177], v119 offset:0\n\t"
        "ds_read_b64_tr_b16 v[178:179], v119 offset:512\n\t"
        "ds_read_b64_tr_b16 v[180:181], v119 offset:8192\n\t"
        "ds_read_b64_tr_b16 v[182:183], v119 offset:8704\n\t"
        "ds_read_b64_tr_b16 v[184:185], v120 offset:0\n\t"
        "ds_read_b64_tr_b16 v[186:187], v120 offset:512\n\t"
        "ds_read_b64_tr_b16 v[188:189], v120 offset:8192\n\t"
        "ds_read_b64_tr_b16 v[190:191], v120 offset:8704\n\t"
        "s_waitcnt lgkmcnt(0)\n\t"
        "s_barrier\n\t"
        "s_add_u32 s80, %[m0b], 0\n\t"
        "s_mov_b32 s81, 6291456\n\t"
        "s_mov_b32 s82, 4\n\t"
        ".Ld6%=:\n\t"
        "s_mov_b32 m0, s80\n\t"
        "s_add_u32 s80, s80, 0x8000\n\t"
        "buffer_load_dwordx4 v112, %[sq], s81 offen lds\n\t"
        "s_add_u32 m0, m0, 0x1000\n\t"
        "s_add_u32 s81, s81, 0x20000\n\t"
        "buffer_load_dwordx4 v112, %[sq], s81 offen lds\n\t"
        "s_add_u32 m0, m0, 0x1000\n\t"
        "s_add_u32 s81, s81, 0x20000\n\t"
        "buffer_load_dwordx4 v112, %[sq], s81 offen lds\n\t"
        "s_add_u32 m0, m0, 0x1000\n\t"
        "s_add_u32 s81, s81, 0x20000\n\t"
        "buffer_load_dwordx4 v112, %[sq], s81 offen lds\n\t"
        "s_add_u32 m0, m0, 0x1000\n\t"
        "s_add_u32 s81, s81, 0x20000\n\t"
        "s_sub_u32 s82, s82, 1\n\t"
        "s_cmp_lg_u32 s82, 0\n\t"
        "s_cbranch_scc1 .Ld6%=\n\t"
        "s_waitcnt vmcnt(16)\n\t"
        "s_barrier\n\t"
        "ds_read_b64_tr_b16 v[192:193], v113 offset:16384\n\t"
        "ds_read_b64_tr_b16 v[194:195], v113 offset:16896\n\t"
        "ds_read_b64_tr_b16 v[196:197], v113 offset:24576\n\t"
        "ds_read_b64_tr_b16 v[198:199], v113 offset:25088\n\t"
        "ds_read_b64_tr_b16 v[200:201], v114 offset:16384\n\t"
        "ds_read_b64_tr_b16 v[202:203], v114 offset:16896\n\t"
        "ds_read_b64_tr_b16 v[204:205], v114 offset:24576\n\t"
        "ds_read_b64_tr_b16 v[206:207], v114 offset:25088\n\t"
        "ds_read_b64_tr_b16 v[208:209], v115 offset:16384\n\t"
        "ds_read_b64_tr_b16 v[210:211], v115 offset:16896\n\t"
        "ds_read_b64_tr_b16 v[212:213], v115 offset:24576\n\t"
        "ds_read_b64_tr_b16 v[214:215], v115 offset:25088\n\t"
        "ds_read_b64_tr_b16 v[216:217], v116 offset:16384\n\t"
        "ds_read_b64_tr_b16 v[218:219], v116 offset:16896\n\t"
        "ds_read_b64_tr_b16 v[220:221], v116 offset:24576\n\t"
        "ds_read_b64_tr_b16 v[222:223], v116 offset:25088\n\t"
        "ds_read_b64_tr_b16 v[224:225], v117 offset:16384\n\t"
        "ds_read_b64_tr_b16 v[226:227], v117 offset:16896\n\t"
        "ds_read_b64_tr_b16 v[228:229], v117 offset:24576\n\t"
        "ds_read_b64_tr_b16 v[230:231], v117 offset:25088\n\t"
        "ds_read_b64_tr_b16 v[232:233], v118 offset:16384\n\t"
        "ds_read_b64_tr_b16 v[234:235], v118 offset:16896\n\t"
        "ds_read_b64_tr_b16 v[236:237], v118 offset:24576\n\t"
        "ds_read_b64_tr_b16 v[238:239], v118 offset:25088\n\t"
        "ds_read_b64_tr_b16 v[240:241], v119 offset:16384\n\t"
        "ds_read_b64_tr_b16 v[242:243], v119 offset:16896\n\t"
        "ds_read_b64_tr_b16 v[244:245], v119 offset:24576\n\t"
        "ds_read_b64_tr_b16 v[246:247], v119 offset:25088\n\t"
        "ds_read_b64_tr_b16 v[248:249], v120 offset:16384\n\t"
        "ds_read_b64_tr_b16 v[250:251], v120 offset:16896\n\t"
        "ds_read_b64_tr_b16 v[252:253], v120 offset:24576\n\t"
        "ds_read_b64_tr_b16 v[254:255], v120 offset:25088\n\t"
        "s_waitcnt lgkmcnt(0)\n\t"
        "s_barrier\n\t"
        "s_add_u32 s80, %[m0b], 16384\n\t"
        "s_mov_b32 s81, 6291456\n\t"
        "s_mov_b32 s82, 4\n\t"
        ".Ld7%=:\n\t"
        "s_mov_b32 m0, s80\n\t"
        "s_add_u32 s80, s80, 0x8000\n\t"
        "buffer_load_dwordx4 v112, %[sk], s81 offen lds\n\t"
        "s_add_u32 m0, m0, 0x1000\n\t"
        "s_add_u32 s81, s81, 0x20000\n\t"
        "buffer_load_dwordx4 v112, %[sk], s81 offen lds\n\t"
        "s_add_u32 m0, m0, 0x1000\n\t"
        "s_add_u32 s81, s81, 0x20000\n\t"
        "buffer_load_dwordx4 v112, %[sk], s81 offen lds\n\t"
        "s_add_u32 m0, m0, 0x1000\n\t"
        "s_add_u32 s81, s81, 0x20000\n\t"
        "buffer_load_dwordx4 v112, %[sk], s81 offen lds\n\t"
        "s_add_u32 m0, m0, 0x1000\n\t"
        "s_add_u32 s81, s81, 0x20000\n\t"
        "s_sub_u32 s82, s82, 1\n\t"
        "s_cmp_lg_u32 s82, 0\n\t"
        "s_cbranch_scc1 .Ld7%=\n\t"
        "s_waitcnt vmcnt(16)\n\t"
        "s_barrier\n\t"
        "ds_read_b64_tr_b16 a[0:1], v113 offset:0\n\t"
        "ds_read_b64_tr_b16 a[2:3], v113 offset:512\n\t"
        "ds_read_b64_tr_b16 a[4:5], v113 offset:8192\n\t"
        "ds_read_b64_tr_b16 a[6:7], v113 offset:8704\n\t"
        "ds_read_b64_tr_b16 a[8:9], v114 offset:0\n\t"
        "ds_read_b64_tr_b16 a[10:11], v114 offset:512\n\t"
        "ds_read_b64_tr_b16 a[12:13], v114 offset:8192\n\t"
        "ds_read_b64_tr_b16 a[14:15], v114 offset:8704\n\t"
        "ds_read_b64_tr_b16 v[64:65], v115 offset:0\n\t"
        "ds_read_b64_tr_b16 v[66:67], v115 offset:512\n\t"
        "ds_read_b64_tr_b16 v[68:69], v115 offset:8192\n\t"
        "ds_read_b64_tr_b16 v[70:71], v115 offset:8704\n\t"
        "ds_read_b64_tr_b16 v[72:73], v116 offset:0\n\t"
        "ds_read_b64_tr_b16 v[74:75], v116 offset:512\n\t"
        "ds_read_b64_tr_b16 v[76:77], v116 offset:8192\n\t"
        "ds_read_b64_tr_b16 v[78:79], v116 offset:8704\n\t"
        "ds_read_b64_tr_b16 v[80:81], v117 offset:0\n\t"
        "ds_read_b64_tr_b16 v[82:83], v117 offset:512\n\t"
        "ds_read_b64_tr_b16 v[84:85], v117 offset:8192\n\t"
        "ds_read_b64_tr_b16 v[86:87], v117 offset:8704\n\t"
        "ds_read_b64_tr_b16 v[88:89], v118 offset:0\n\t"
        "ds_read_b64_tr_b16 v[90:91], v118 offset:512\n\t"
        "ds_read_b64_tr_b16 v[92:93], v118 offset:8192\n\t"
        "ds_read_b64_tr_b16 v[94:95], v118 offset:8704\n\t"
        "ds_read_b64_tr_b16 a[128:129], v119 offset:0\n\t"
        "ds_read_b64_tr_b16 a[130:131], v119 offset:512\n\t"
        "ds_read_b64_tr_b16 a[132:133], v119 offset:8192\n\t"
        "ds_read_b64_tr_b16 a[134:135], v119 offset:8704\n\t"
        "ds_read_b64_tr_b16 a[136:137], v120 offset:0\n\t"
        "ds_read_b64_tr_b16 a[138:139], v120 offset:512\n\t"
        "ds_read_b64_tr_b16 a[140:141], v120 offset:8192\n\t"
        "ds_read_b64_tr_b16 a[142:143], v120 offset:8704\n\t"
        "s_waitcnt lgkmcnt(0)\n\t"
        "s_barrier\n\t"
        "ds_write_b128 v124, v[0:3] offset:0\n\t"
        "ds_write_b128 v124, v[4:7] offset:1024\n\t"
        "ds_write_b128 v124, v[8:11] offset:2048\n\t"
        "ds_write_b128 v124, v[12:15] offset:3072\n\t"
        "ds_write_b128 v124, v[16:19] offset:4096\n\t"
        "ds_write_b128 v124, v[20:23] offset:5120\n\t"
        "ds_write_b128 v124, v[24:27] offset:6144\n\t"
        "ds_write_b128 v124, v[28:31] offset:7168\n\t"
        "ds_write_b128 v124, v[32:35] offset:8192\n\t"
        "ds_write_b128 v124, v[36:39] offset:9216\n\t"
        "ds_write_b128 v124, v[40:43] offset:10240\n\t"
        "ds_write_b128 v124, v[44:47] offset:11264\n\t"
        "ds_write_b128 v124, v[48:51] offset:12288\n\t"
        "ds_write_b128 v124, v[52:55] offset:13312\n\t"
        "ds_write_b128 v124, v[56:59] offset:14336\n\t"
        "ds_write_b128 v124, v[60:63] offset:15360\n\t"
        "s_waitcnt lgkmcnt(0)\n\t"
        "s_waitcnt vmcnt(0)\n\t"
        "s_barrier\n\t"
        "ds_read_b64_tr_b16 a[64:65], v113 offset:16384\n\t"
        "ds_read_b64_tr_b16 a[66:67], v113 offset:16896\n\t"
        "ds_read_b64_tr_b16 a[68:69], v113 offset:24576\n\t"
        "ds_read_b64_tr_b16 a[70:71], v113 offset:25088\n\t"
        "ds_read_b64_tr_b16 a[72:73], v114 offset:16384\n\t"
        "ds_read_b64_tr_b16 a[74:75], v114 offset:16896\n\t"
        "ds_read_b64_tr_b16 a[76:77], v114 offset:24576\n\t"
        "ds_read_b64_tr_b16 a[78:79], v114 offset:25088\n\t"
        "ds_read_b64_tr_b16 v[0:1], v115 offset:16384\n\t"
        "ds_read_b64_tr_b16 v[2:3], v115 offset:16896\n\t"
        "ds_read_b64_tr_b16 v[4:5], v115 offset:24576\n\t"
        "ds_read_b64_tr_b16 v[6:7], v115 offset:25088\n\t"
        "ds_read_b64_tr_b16 v[8:9], v116 offset:16384\n\t"
        "ds_read_b64_tr_b16 v[10:11], v116 offset:16896\n\t"
        "ds_read_b64_tr_b16 v[12:13], v116 offset:24576\n\t"
        "ds_read_b64_tr_b16 v[14:15], v116 offset:25088\n\t"
        "ds_read_b64_tr_b16 v[16:17], v117 offset:16384\n\t"
        "ds_read_b64_tr_b16 v[18:19], v117 offset:16896\n\t"
        "ds_read_b64_tr_b16 v[20:21], v117 offset:24576\n\t"
        "ds_read_b64_tr_b16 v[22:23], v117 offset:25088\n\t"
        "ds_read_b64_tr_b16 v[24:25], v118 offset:16384\n\t"
        "ds_read_b64_tr_b16 v[26:27], v118 offset:16896\n\t"
        "ds_read_b64_tr_b16 v[28:29], v118 offset:24576\n\t"
        "ds_read_b64_tr_b16 v[30:31], v118 offset:25088\n\t"
        "ds_read_b64_tr_b16 a[192:193], v119 offset:16384\n\t"
        "ds_read_b64_tr_b16 a[194:195], v119 offset:16896\n\t"
        "ds_read_b64_tr_b16 a[196:197], v119 offset:24576\n\t"
        "ds_read_b64_tr_b16 a[198:199], v119 offset:25088\n\t"
        "ds_read_b64_tr_b16 a[200:201], v120 offset:16384\n\t"
        "ds_read_b64_tr_b16 a[202:203], v120 offset:16896\n\t"
        "ds_read_b64_tr_b16 a[204:205], v120 offset:24576\n\t"
        "ds_read_b64_tr_b16 a[206:207], v120 offset:25088\n\t"
        "s_waitcnt lgkmcnt(0)\n\t"
        "s_barrier\n\t"
        "ds_write_b128 v124, v[64:67] offset:16384\n\t"
        "ds_write_b128 v124, v[68:71] offset:17408\n\t"
        "ds_write_b128 v124, v[72:75] offset:18432\n\t"
        "ds_write_b128 v124, v[76:79] offset:19456\n\t"
        "ds_write_b128 v124, v[80:83] offset:20480\n\t"
        "ds_write_b128 v124, v[84:87] offset:21504\n\t"
        "ds_write_b128 v124, v[88:91] offset:22528\n\t"
        "ds_write_b128 v124, v[92:95] offset:23552\n\t"
        "ds_write_b128 v124, v[0:3] offset:24576\n\t"
        "ds_write_b128 v124, v[4:7] offset:25600\n\t"
        "ds_write_b128 v124, v[8:11] offset:26624\n\t"
        "ds_write_b128 v124, v[12:15] offset:27648\n\t"
        "ds_write_b128 v124, v[16:19] offset:28672\n\t"
        "ds_write_b128 v124, v[20:23] offset:29696\n\t"
        "ds_write_b128 v124, v[24:27] offset:30720\n\t"
        "ds_write_b128 v124, v[28:31] offset:31744\n\t"
        "s_waitcnt lgkmcnt(0)"
        : "={a[0:31]}"(wa[0]), "={a[32:63]}"(wa[1]), "={a[64:95]}"(wa[2]), "={a[96:127]}"(wa[3]), "={a[128:159]}"(wa[4]), "={a[160:191]}"(wa[5]), "={a[192:223]}"(wa[6]), "={a[224:255]}"(wa[7]), "={v[128:159]}"(wg[0]), "={v[160:191]}"(wg[1]), "={v[192:223]}"(wg[2]), "={v[224:255]}"(wg[3])
        : [sq] "s"(sq), [sk] "s"(sk), [wsd] "s"(wsd), [m0b] "s"(m0b), "{v[112:127]}"(pa)
        : "memory", "scc", "m0", "s80", "s81", "s82", "v0", "v1", "v2", "v3", "v4", "v5", "v6", "v7", "v8", "v9", "v10", "v11", "v12", "v13", "v14", "v15", "v16", "v17", "v18", "v19", "v20", "v21", "v22", "v23", "v24", "v25", "v26", "v27", "v28", "v29", "v30", "v31", "v32", "v33", "v34", "v35", "v36", "v37", "v38", "v39", "v40", "v41", "v42", "v43", "v44", "v45", "v46", "v47", "v48", "v49", "v50", "v51", "v52", "v53", "v54", "v55", "v56", "v57", "v58", "v59", "v60", "v61", "v62", "v63", "v64", "v65", "v66", "v67", "v68", "v69", "v70", "v71", "v72", "v73", "v74", "v75", "v76", "v77", "v78", "v79", "v80", "v81", "v82", "v83", "v84", "v85", "v86", "v87", "v88", "v89", "v90", "v91", "v92", "v93", "v94", "v95");
        }
        asm volatile("" : "+v"(cpf));
        tid = otid(); l = tid & 63; n16 = l & 15; g = l >> 4;
        unsigned nm; if (!votes(nm)) return false;
        const unsigned nW = (nm & 1u) << 13, nA = nW, nS = nW + 6 * 16384 - w * 1024, nC = 32 * 16384 + ((nm & 2u) << 12);
        const unsigned b0 = (64 + (h >> 2)) * 16384 + ((nm & 4u) << 11) + (h & 3) * 2048 - w * 256 - L_QK, b1 = b0 + 8 * 16384;
        const unsigned pob = w == 0 ? nA + (i32 >> 4) * 16384 + (i32 & 15) * 512 : (w == 2 && i32 == 0) ? nS : OOB;
        const unsigned kx = W_KX + h * 8192, vx = W_VF + h * 8192 + w * 1024, kf = F_KVQ + h * 64, swo = 6 * 16384 + h * 8;
        const unsigned aq = img(8 * g + (n16 >> 2)) + (n16 & 1) * 8;
        v8u pc = {(unsigned)tid * 16, (unsigned)(L_W + w * 32768 + l * 16), (n16 & 2) ? 0xEEEEu : 0x4444u, aq,
                  (unsigned)(((w + 1) & 3) * 1024 + l * 16), (unsigned)(((w + 2) & 3) * 1024 + l * 16), (unsigned)(((w + 3) & 3) * 1024 + l * 16), img(32 * w + n16) + g * 4};
        v2u pd = {(n16 & 2) ? (unsigned)L_Z : aq + w * 768, (n16 & 2) ? aq + w * 768 : (unsigned)L_Z};
        *(__attribute__((address_space(3))) v4u*)(size_t)(L_TAB + tid * 16) = (v4u){(unsigned)(L_QK + 3072 + g * 384 + n16 * 8),
                  n16 < 8 ? (unsigned)(((n16 >> 2) * 4 + (h >> 3)) * 16384 + (h & 7) * 1024 + w * 256 + (g >> 1) * 128 + (n16 & 2) * 32 + (g & 1) * 32 + (n16 & 1) * 16) : OOB,
                  (unsigned)((32 * w + 16 * (g & 1) + n16) * 16 + (g >> 1) * 8), (unsigned)(L_QK + 3072 + w * 768 + ((n16 & 1) + 2 * (n16 >> 2)) * 32 + g * 8 + (n16 & 2) * 2)};
        if (tid < 64) ((lu32*)(size_t)L_FL)[tid] = 0u;
    asm volatile(
        "rec_s_code:\n\t"
        "s_mov_b32 s36, 0xf0000000\n\t"
        "s_mov_b32 s38, 0xff00ff00\n\t"
        "s_mov_b32 s39, 0xff00ff00\n\t"
        "s_mov_b32 s40, 0\n\t"
        "s_mov_b32 s37, 0\n\t"
        "s_brev_b32 s54, 1\n\t"
        "s_mov_b32 s46, %c[iD1]\n\t"
        "s_mov_b32 s45, %c[iD1]\n\t"
        "s_mov_b32 s47, %c[iD2]\n\t"
        "s_mov_b32 s48, 1\n\t"
        "s_mov_b32 s50, %c[LC]\n\t"
        "s_mov_b32 s51, %c[LA]\n\t"
        "s_mov_b32 s52, %c[E1]\n\t"
        "s_mov_b32 s53, %c[E2]\n\t"
        "s_mov_b32 s43, 0\n\t"
        "s_mov_b32 s61, %[b0]\n\t"
        "s_mov_b32 s62, %[b1]\n\t"
        "s_mov_b32 s75, 31744\n\t"
        "s_and_b32 s80, s40, 3\n\t"
        "s_lshl_b32 s80, s80, 17\n\t"
        "s_add_u32 s57, s80, %[nA]\n\t"
        "s_add_u32 s58, s57, 0x1000\n\t"
        "s_add_u32 s59, s57, 0x4000\n\t"
        "s_add_u32 s60, s57, 0x5000\n\t"
        "s_and_b32 s80, s40, 3\n\t"
        "s_lshl_b32 s80, s80, 17\n\t"
        "s_add_u32 s63, s80, %[nC]\n\t"
        "s_add_u32 s66, s80, %[nS]\n\t"
        "s_add_u32 s81, s80, 0x20000\n\t"
        "s_and_b32 s81, s81, 0x60000\n\t"
        "s_add_u32 s67, s81, %[nW]\n\t"
        "s_cmp_eq_u32 %[w], 2\n\t"
        "s_cselect_b32 s81, s80, s81\n\t"
        "s_xor_b32 s81, s81, 0x40000\n\t"
        "s_add_u32 s68, s81, %[pob]\n\t"
        "s_lshl_b32 s72, s40, 17\n\t"
        "s_add_u32 s72, s72, %[kvo]\n\t"
        "s_mov_b32 s64, %[kf]\n\t"
        "buffer_load_dwordx2 v[48:49], off, %[wsd], s64 sc1\n\t"
        "s_waitcnt vmcnt(0)\n\t"
        "s_cmp_eq_u32 s64, s36\n\t"
        "s_cbranch_scc1 .Lk10%=\n\t"
        "s_mov_b32 s97, 0\n\t"
        ".Lk00%=:\n\t"
        "v_readfirstlane_b32 s82, v48\n\t"
        "v_readfirstlane_b32 s83, v49\n\t"
        "s_xor_b32 s82, s82, %[tag]\n\t"
        "s_xor_b32 s83, s83, %[tag]\n\t"
        "s_or_b32 s82, s82, s83\n\t"
        "s_cbranch_scc0 .Lk10%=\n\t"
        "s_sleep 3\n\t"
        "buffer_load_dwordx2 v[48:49], off, %[wsd], s64 sc1\n\t"
        "s_add_u32 s97, s97, 1\n\t"
        "s_cmp_lt_u32 s97, 0x200000\n\t"
        "s_cbranch_scc0 .Lgu%=\n\t"
        "s_waitcnt vmcnt(0)\n\t"
        "s_branch .Lk00%=\n\t"
        ".Lk10%=:\n\t"
        "s_add_u32 s80, s40, 0\n\t"
        "s_min_u32 s80, s80, 63\n\t"
        "s_lshl_b32 s80, s80, 18\n\t"
        "s_add_u32 s69, s80, %[kx]\n\t"
        "s_add_u32 s70, s69, 0x1000\n\t"
        "s_lshl_b32 s80, s40, 18\n\t"
        "s_add_u32 s71, s80, %[vx]\n\t"
        "s_add_u32 s80, s40, 1\n\t"
        "s_lshl_b32 s81, s80, 9\n\t"
        "s_add_u32 s81, s81, %[kf]\n\t"
        "s_and_b32 s80, s80, 0x43\n\t"
        "s_cselect_b32 s64, s36, s81\n\t"
        "buffer_load_dwordx4 v[104:107], v120, %[wsd], s69 offen sc1\n\t"
        "buffer_load_dwordx4 v[108:111], v120, %[wsd], s70 offen sc1\n\t"
        "ds_read_b128 v[16:19], v121 offset:0\n\t"
        "ds_read_b128 v[20:23], v121 offset:1024\n\t"
        "ds_read_b128 v[24:27], v121 offset:2048\n\t"
        "ds_read_b128 v[28:31], v121 offset:3072\n\t"
        "ds_read_b128 v[32:35], v121 offset:4096\n\t"
        "ds_read_b128 v[36:39], v121 offset:5120\n\t"
        "ds_read_b128 v[40:43], v121 offset:6144\n\t"
        "ds_read_b128 v[44:47], v121 offset:7168\n\t"
        "ds_read_b128 v[56:59], v121 offset:8192\n\t"
        "ds_read_b128 v[60:63], v121 offset:9216\n\t"
        "ds_read_b128 v[64:67], v121 offset:10240\n\t"
        "ds_read_b128 v[68:71], v121 offset:11264\n\t"
        "ds_read_b128 v[72:75], v121 offset:12288\n\t"
        "ds_read_b128 v[76:79], v121 offset:13312\n\t"
        "ds_read_b128 v[80:83], v121 offset:14336\n\t"
        "ds_read_b128 v[84:87], v121 offset:15360\n\t"
        "s_add_u32 s80, s40, 1\n\t"
        "s_min_u32 s80, s80, 63\n\t"
        "s_lshl_b32 s80, s80, 18\n\t"
        "s_add_u32 s69, s80, %[kx]\n\t"
        "s_add_u32 s70, s69, 0x1000\n\t"
        "s_lshl_b32 s80, s40, 18\n\t"
        "s_add_u32 s71, s80, %[vx]\n\t"
        "s_add_u32 s80, s40, 2\n\t"
        "s_lshl_b32 s81, s80, 9\n\t"
        "s_add_u32 s81, s81, %[kf]\n\t"
        "s_and_b32 s80, s80, 0x43\n\t"
        "s_cselect_b32 s64, s36, s81\n\t"
        "v_mov_b32_e32 v88, 0\n\t"
        "v_mov_b32_e32 v89, 0\n\t"
        "v_mov_b32_e32 v90, 0\n\t"
        "v_mov_b32_e32 v91, 0\n\t"
        "v_mov_b32_e32 v92, 0\n\t"
        "v_mov_b32_e32 v93, 0\n\t"
        "v_mov_b32_e32 v94, 0\n\t"
        "v_mov_b32_e32 v95, 0\n\t"
        "v_mov_b32_e32 v96, 0\n\t"
        "v_mov_b32_e32 v97, 0\n\t"
        "v_mov_b32_e32 v98, 0\n\t"
        "v_mov_b32_e32 v99, 0\n\t"
        "v_mov_b32_e32 v100, 0\n\t"
        "v_mov_b32_e32 v101, 0\n\t"
        "v_mov_b32_e32 v102, 0\n\t"
        "v_mov_b32_e32 v103, 0\n\t"
        "s_waitcnt vmcnt(0) lgkmcnt(0)\n\t"
        "s_barrier\n\t"
        "s_memrealtime s[98:99]\n\t"
        "v_mov_b32_e32 v49, 0\n\t"
        "v_lshlrev_b32_e32 v51, 6, v120\n\t"
        "s_add_u32 s80, %[nW], %[swo]\n\t"
        "s_cmp_eq_u32 %[w], 0\n\t"
        "s_cselect_b32 s73, s80, s36\n\t"
        "s_cselect_b32 s74, %[pra], s36\n\t"
        "s_waitcnt lgkmcnt(0)\n\t"
        "s_mov_b32 s41, s98\n\t"
        "v_mov_b32_e32 v48, s98\n\t"
        ".p2align 6\n\t"
        ".Lloop%=:\n\t"
        "s_mov_b32 s97, 0\n\t"
        "s_movk_i32 s96, 0x1000\n\t"
        ".Lwa%=:\n\t"
        "s_memrealtime s[98:99]\n\t"
        "s_addk_i32 s96, -1\n\t"
        "s_cmp_lt_i32 s96, 0\n\t"
        "s_cbranch_scc1 .Lta%=\n\t"
        "s_waitcnt lgkmcnt(0)\n\t"
        "s_sub_i32 s98, s98, s41\n\t"
        "s_cmp_lt_i32 s98, %c[iFT]\n\t"
        "s_cbranch_scc1 .Lwa%=\n\t"
        "s_cmp_ge_i32 s98, 0\n\t"
        "s_cbranch_scc1 .Lxa%=\n\t"
        ".Lqa%=:\n\t"
        "s_nop %c[iFN]\n\t"
        "s_addk_i32 s98, 1\n\t"
        "s_cmp_lt_i32 s98, 0\n\t"
        "s_cbranch_scc1 .Lqa%=\n\t"
        ".Lxa%=:\n\t"
        "buffer_load_dwordx4 v[0:3], v120, %[srd], s57 offen sc1\n\t"
        "s_nop 7\n\t"
        "buffer_load_dwordx4 v[4:7], v120, %[srd], s58 offen sc1\n\t"
        "s_nop 7\n\t"
        "buffer_load_dwordx4 v[8:11], v120, %[srd], s59 offen sc1\n\t"
        "s_nop 7\n\t"
        "buffer_load_dwordx4 v[12:15], v120, %[srd], s60 offen sc1\n\t"
        "s_nop 15\n\t"
        "s_nop 15\n\t"
        "s_nop 3\n\t"
        "s_mov_b64 exec, 7\n\t"
        "buffer_store_dwordx2 v[48:49], v51, %[srd], s73 offen sc1\n\t"
        "s_mov_b64 exec, -1\n\t"
        "buffer_load_dwordx4 v[112:115], v120, %[wsd], s71 offen offset:0 sc1\n\t"
        "buffer_load_dwordx4 v[116:119], v120, %[wsd], s71 offen offset:1024 sc1\n\t"
        ".Lgo%=:\n\t"
        "s_waitcnt vmcnt(6)\n\t"
        "v_smfmac_f32_16x16x64_bf16 v[88:91], v[0:3], v[16:23], v122\n\t"
        "ds_write_b128 v120, v[0:3] offset:0\n\t"
        "v_smfmac_f32_16x16x64_bf16 v[92:95], v[0:3], v[24:31], v122\n\t"
        "v_smfmac_f32_16x16x64_bf16 v[96:99], v[0:3], v[32:39], v122\n\t"
        "v_smfmac_f32_16x16x64_bf16 v[100:103], v[0:3], v[40:47], v122\n\t"
        "s_waitcnt vmcnt(5)\n\t"
        "v_smfmac_f32_16x16x64_bf16 v[88:91], v[4:7], v[56:63], v122\n\t"
        "ds_write_b128 v120, v[4:7] offset:4096\n\t"
        "v_smfmac_f32_16x16x64_bf16 v[92:95], v[4:7], v[64:71], v122\n\t"
        "v_smfmac_f32_16x16x64_bf16 v[96:99], v[4:7], v[72:79], v122\n\t"
        "v_smfmac_f32_16x16x64_bf16 v[100:103], v[4:7], v[80:87], v122\n\t"
        "s_waitcnt vmcnt(4)\n\t"
        "v_smfmac_f32_16x16x64_bf16 v[88:91], v[8:11], v[128:135], v122\n\t"
        "ds_write_b128 v120, v[8:11] offset:8192\n\t"
        "v_smfmac_f32_16x16x64_bf16 v[92:95], v[8:11], v[136:143], v122\n\t"
        "s_waitcnt lgkmcnt(1)\n\t"
        "s_barrier\n\t"
        "v_smfmac_f32_16x16x64_bf16 v[96:99], v[8:11], v[192:199], v122\n\t"
        "ds_read_b128 v[16:19], v124 offset:0\n\t"
        "v_smfmac_f32_16x16x64_bf16 v[100:103], v[8:11], v[200:207], v122\n\t"
        "ds_read_b128 v[20:23], v125 offset:0\n\t"
        "s_waitcnt vmcnt(3)\n\t"
        "v_smfmac_f32_16x16x64_bf16 v[88:91], v[12:15], a[0:7], v122\n\t"
        "ds_write_b128 v120, v[12:15] offset:12288\n\t"
        "v_smfmac_f32_16x16x64_bf16 v[92:95], v[12:15], a[8:15], v122\n\t"
        "v_smfmac_f32_16x16x64_bf16 v[96:99], v[12:15], a[64:71], v122\n\t"
        "ds_read_b128 v[24:27], v126 offset:0\n\t"
        "v_smfmac_f32_16x16x64_bf16 v[100:103], v[12:15], a[72:79], v122\n\t"
        "ds_read_b128 v[28:31], v124 offset:4096\n\t"
        "s_waitcnt lgkmcnt(4)\n\t"
        "v_smfmac_f32_16x16x64_bf16 v[88:91], v[16:19], a[16:23], v122\n\t"
        "ds_read_b128 v[32:35], v125 offset:4096\n\t"
        "v_smfmac_f32_16x16x64_bf16 v[92:95], v[16:19], a[24:31], v122\n\t"
        "ds_read_b128 v[36:39], v126 offset:4096\n\t"
        "s_waitcnt lgkmcnt(5)\n\t"
        "v_smfmac_f32_16x16x64_bf16 v[88:91], v[20:23], a[32:39], v122\n\t"
        "ds_read_b128 v[72:75], v121 offset:16384\n\t"
        "v_smfmac_f32_16x16x64_bf16 v[92:95], v[20:23], a[40:47], v122\n\t"
        "s_waitcnt lgkmcnt(5)\n\t"
        "s_barrier\n\t"
        "ds_read_b128 v[76:79], v121 offset:17408\n\t"
        "s_waitcnt lgkmcnt(5)\n\t"
        "v_smfmac_f32_16x16x64_bf16 v[88:91], v[24:27], a[48:55], v122\n\t"
        "ds_read_b128 v[0:3], v124 offset:12288\n\t"
        "v_smfmac_f32_16x16x64_bf16 v[92:95], v[24:27], a[56:63], v122\n\t"
        "ds_read_b128 v[80:83], v121 offset:18432\n\t"
        "s_waitcnt lgkmcnt(6)\n\t"
        "v_smfmac_f32_16x16x64_bf16 v[88:91], v[28:31], a[144:151], v122\n\t"
        "ds_read_b128 v[84:87], v121 offset:19456\n\t"
        "v_smfmac_f32_16x16x64_bf16 v[92:95], v[28:31], a[152:159], v122\n\t"
        "ds_read_b128 v[40:43], v124 offset:8192\n\t"
        "s_waitcnt lgkmcnt(7)\n\t"
        "v_smfmac_f32_16x16x64_bf16 v[88:91], v[32:35], a[160:167], v122\n\t"
        "ds_read_b128 v[44:47], v125 offset:8192\n\t"
        "v_mov_b32_e32 v12, 0x3f803f80\n\t"
        "v_smfmac_f32_16x16x64_bf16 v[92:95], v[32:35], a[168:175], v122\n\t"
        "ds_read_b128 v[56:59], v121 offset:20480\n\t"
        "v_mov_b32_e32 v13, 0x3f803f80\n\t"
        "s_waitcnt lgkmcnt(8)\n\t"
        "v_smfmac_f32_16x16x64_bf16 v[88:91], v[36:39], a[176:183], v122\n\t"
        "ds_read_b128 v[60:63], v121 offset:21504\n\t"
        "v_mov_b32_e32 v14, 0x3f803f80\n\t"
        "v_smfmac_f32_16x16x64_bf16 v[92:95], v[36:39], a[184:191], v122\n\t"
        "ds_read_b128 v[4:7], v125 offset:12288\n\t"
        "v_mov_b32_e32 v15, 0x3f803f80\n\t"
        "s_waitcnt lgkmcnt(8)\n\t"
        "s_waitcnt lgkmcnt(7)\n\t"
        "v_smfmac_f32_16x16x64_bf16 v[88:91], v[0:3], v[72:79], v122\n\t"
        "ds_read_b128 v[64:67], v121 offset:22528\n\t"
        "s_waitcnt lgkmcnt(6)\n\t"
        "v_smfmac_f32_16x16x64_bf16 v[92:95], v[0:3], v[80:87], v122\n\t"
        "ds_read_b128 v[68:71], v121 offset:23552\n\t"
        "s_waitcnt lgkmcnt(6)\n\t"
        "v_smfmac_f32_16x16x64_bf16 v[88:91], v[40:43], v[144:151], v122\n\t"
        "ds_read_b128 v[48:51], v126 offset:8192\n\t"
        "v_smfmac_f32_16x16x64_bf16 v[92:95], v[40:43], v[152:159], v122\n\t"
        "ds_read_b128 v[8:11], v126 offset:12288\n\t"
        "s_waitcnt lgkmcnt(7)\n\t"
        "v_smfmac_f32_16x16x64_bf16 v[88:91], v[44:47], v[160:167], v122\n\t"
        "ds_read_b128 v[72:75], v121 offset:24576\n\t"
        "v_smfmac_f32_16x16x64_bf16 v[92:95], v[44:47], v[168:175], v122\n\t"
        "ds_read_b128 v[76:79], v121 offset:25600\n\t"
        "s_waitcnt lgkmcnt(7)\n\t"
        "s_waitcnt lgkmcnt(6)\n\t"
        "v_smfmac_f32_16x16x64_bf16 v[88:91], v[4:7], v[56:63], v122\n\t"
        "ds_read_b128 v[80:83], v121 offset:26624\n\t"
        "s_waitcnt lgkmcnt(5)\n\t"
        "v_smfmac_f32_16x16x64_bf16 v[92:95], v[4:7], v[64:71], v122\n\t"
        "ds_read_b128 v[84:87], v121 offset:27648\n\t"
        "s_waitcnt lgkmcnt(5)\n\t"
        "v_smfmac_f32_16x16x64_bf16 v[88:91], v[48:51], v[176:183], v122\n\t"
        "v_mov_b32_e32 v64, 0\n\t"
        "v_smfmac_f32_16x16x64_bf16 v[92:95], v[48:51], v[184:191], v122\n\t"
        "v_mov_b32_e32 v65, 0\n\t"
        "s_waitcnt lgkmcnt(4)\n\t"
        "v_smfmac_f32_16x16x64_bf16 v[88:91], v[8:11], a[128:135], v122\n\t"
        "v_mov_b32_e32 v66, 0\n\t"
        "v_smfmac_f32_16x16x64_bf16 v[92:95], v[8:11], a[136:143], v122\n\t"
        "v_mov_b32_e32 v67, 0\n\t"
        "s_waitcnt lgkmcnt(2)\n\t"
        "v_smfmac_f32_16x16x64_bf16 v[96:99], v[0:3], v[72:79], v122\n\t"
        "ds_read_b128 v[72:75], v121 offset:28672\n\t"
        "s_waitcnt lgkmcnt(1)\n\t"
        "v_smfmac_f32_16x16x64_bf16 v[100:103], v[0:3], v[80:87], v122\n\t"
        "ds_read_b128 v[76:79], v121 offset:29696\n\t"
        "v_smfmac_f32_16x16x64_bf16 v[96:99], v[16:19], a[80:87], v122\n\t"
        "v_add_f32_e32 v88, v88, v90\n\t"
        "v_add_f32_e32 v89, v89, v91\n\t"
        "v_smfmac_f32_16x16x64_bf16 v[100:103], v[16:19], a[88:95], v122\n\t"
        "v_cvt_pk_bf16_f32 v90, v88, v89\n\t"
        "v_add_f32_e32 v92, v92, v94\n\t"
        "v_smfmac_f32_16x16x64_bf16 v[96:99], v[20:23], a[96:103], v122\n\t"
        "ds_write_b32 v127, v90\n\t"
        "v_add_f32_e32 v93, v93, v95\n\t"
        "v_cvt_pk_bf16_f32 v91, v92, v93\n\t"
        "v_smfmac_f32_16x16x64_bf16 v[100:103], v[20:23], a[104:111], v122\n\t"
        "ds_write_b32 v127, v91 offset:384\n\t"
        "v_cmp_u_f32_e32 vcc, v88, v89\n\t"
        "v_smfmac_f32_16x16x64_bf16 v[96:99], v[24:27], a[112:119], v122\n\t"
        "ds_read_b64_tr_b16 v[56:57], v52 offset:0\n\t"
        "ds_read_b64_tr_b16 v[58:59], v52 offset:64\n\t"
        "v_smfmac_f32_16x16x64_bf16 v[100:103], v[24:27], a[120:127], v122\n\t"
        "ds_read_b64_tr_b16 v[60:61], v53 offset:0\n\t"
        "ds_read_b64_tr_b16 v[62:63], v53 offset:64\n\t"
        "v_smfmac_f32_16x16x64_bf16 v[96:99], v[28:31], a[208:215], v122\n\t"
        "s_cbranch_vccnz .Lf1%=\n\t"
        ".Lr1%=:\n\t"
        "ds_read_b128 v[80:83], v121 offset:30720\n\t"
        "v_smfmac_f32_16x16x64_bf16 v[100:103], v[28:31], a[216:223], v122\n\t"
        "ds_read_b128 v[84:87], v121 offset:31744\n\t"
        "v_mov_b32_e32 v91, 0\n\t"
        "v_smfmac_f32_16x16x64_bf16 v[96:99], v[32:35], a[224:231], v122\n\t"
        "v_mov_b32_e32 v94, 0\n\t"
        "v_smfmac_f32_16x16x64_bf16 v[100:103], v[32:35], a[232:239], v122\n\t"
        "ds_read_b128 v[32:35], v120 offset:20480\n\t"
        "v_smfmac_f32_16x16x64_bf16 v[96:99], v[36:39], a[240:247], v122\n\t"
        "s_waitcnt lgkmcnt(3)\n\t"
        "s_barrier\n\t"
        "v_mfma_f32_16x16x32_bf16 v[68:71], v[104:107], v[56:59], 0\n\t"
        "ds_read_b64_tr_b16 v[16:17], v123 offset:0\n\t"
        "v_mfma_f32_16x16x32_bf16 v[68:71], v[108:111], v[60:63], v[68:71]\n\t"
        "ds_read_b64_tr_b16 v[18:19], v123 offset:64\n\t"
        "v_smfmac_f32_16x16x64_bf16 v[100:103], v[36:39], a[248:255], v122\n\t"
        "ds_read_b64_tr_b16 v[20:21], v123 offset:768\n\t"
        "v_smfmac_f32_16x16x64_bf16 v[96:99], v[8:11], a[192:199], v122\n\t"
        "ds_read_b64_tr_b16 v[22:23], v123 offset:832\n\t"
        "v_smfmac_f32_16x16x64_bf16 v[100:103], v[8:11], a[200:207], v122\n\t"
        "s_nop 1\n\t"
        "ds_write_b128 v120, v[68:71] offset:16384\n\t"
        "v_smfmac_f32_16x16x64_bf16 v[96:99], v[4:7], v[72:79], v122\n\t"
        "buffer_load_dword v88, v127, %[srd], s61 offen offset:0 sc1\n\t"
        "s_waitcnt lgkmcnt(6)\n\t"
        "v_smfmac_f32_16x16x64_bf16 v[100:103], v[4:7], v[80:87], v122\n\t"
        "buffer_load_dword v89, v127, %[srd], s61 offen offset:256 sc1\n\t"
        "v_smfmac_f32_16x16x64_bf16 v[96:99], v[40:43], v[208:215], v122\n\t"
        "ds_read_b64_tr_b16 v[24:25], v123 offset:1536\n\t"
        "v_smfmac_f32_16x16x64_bf16 v[100:103], v[40:43], v[216:223], v122\n\t"
        "ds_read_b64_tr_b16 v[26:27], v123 offset:1600\n\t"
        "v_smfmac_f32_16x16x64_bf16 v[96:99], v[44:47], v[224:231], v122\n\t"
        "s_waitcnt lgkmcnt(2)\n\t"
        "s_barrier\n\t"
        "ds_read_b128 v[36:39], v124 offset:16384\n\t"
        "v_smfmac_f32_16x16x64_bf16 v[100:103], v[44:47], v[232:239], v122\n\t"
        "ds_read_b128 v[56:59], v125 offset:16384\n\t"
        "v_smfmac_f32_16x16x64_bf16 v[96:99], v[48:51], v[240:247], v122\n\t"
        "ds_read_b128 v[60:63], v126 offset:16384\n\t"
        "v_smfmac_f32_16x16x64_bf16 v[100:103], v[48:51], v[248:255], v122\n\t"
        "ds_read_b64_tr_b16 v[28:29], v123 offset:2304\n\t"
        "ds_read_b64_tr_b16 v[30:31], v123 offset:2368\n\t"
        "buffer_load_dwordx4 v[84:87], v120, %[srd], s66 offen sc1\n\t"
        "s_waitcnt lgkmcnt(4)\n\t"
        "s_cmp_eq_u32 s40, 63\n\t"
        "v_add_f32_e32 v68, v68, v36\n\t"
        "v_add_f32_e32 v69, v69, v37\n\t"
        "v_add_f32_e32 v70, v70, v38\n\t"
        "v_add_f32_e32 v71, v71, v39\n\t"
        "v_add_f32_e32 v96, v96, v98\n\t"
        "v_add_f32_e32 v97, v97, v99\n\t"
        "s_waitcnt lgkmcnt(2)\n\t"
        "v_cvt_pk_bf16_f32 v50, v96, v97\n\t"
        "v_pk_add_f32 v[100:101], v[100:101], v[102:103]\n\t"
        "ds_write_b32 v35, v50\n\t"
        "v_cvt_pk_bf16_f32 v51, v100, v101\n\t"
        "ds_write_b32 v35, v51 offset:384\n\t"
        "v_pk_add_f32 v[56:57], v[56:57], v[60:61]\n\t"
        "s_cbranch_scc1 .Lend%=\n\t"
        "v_pk_add_f32 v[58:59], v[58:59], v[62:63]\n\t"
        "v_pk_add_f32 v[68:69], v[68:69], v[56:57]\n\t"
        "v_pk_add_f32 v[70:71], v[70:71], v[58:59]\n\t"
        "v_max3_f32 v2, v68, v69, v70\n\t"
        "v_max_f32_e32 v2, v2, v71\n\t"
        "s_nop 1\n\t"
        "v_max_f32_dpp v3, v2, v2 row_ror:8 row_mask:0xf bank_mask:0xf\n\t"
        "v_max_f32_dpp v1, v2, v2 row_ror:8 row_mask:0xf bank_mask:0xf\n\t"
        "s_nop 1\n\t"
        "v_permlane16_swap_b32_e32 v3, v1\n\t"
        "v_max_f32_e32 v2, v3, v1\n\t"
        "v_max_f32_e32 v3, v3, v1\n\t"
        "s_waitcnt lgkmcnt(0)\n\t"
        "s_barrier\n\t"
        "ds_read_b64_tr_b16 v[72:73], v32 offset:0\n\t"
        "v_permlane32_swap_b32_e32 v2, v3\n\t"
        "ds_read_b64_tr_b16 v[74:75], v32 offset:128\n\t"
        "v_max_f32_e32 v0, v2, v3\n\t"
        "ds_read_b64_tr_b16 v[76:77], v32 offset:1536\n\t"
        "v_mul_f32_e32 v2, 0xbe0293ee, v0\n\t"
        "ds_read_b64_tr_b16 v[78:79], v32 offset:1664\n\t"
        "v_fmamk_f32 v68, v68, 0x3e0293ee, v2\n\t"
        "v_fmamk_f32 v69, v69, 0x3e0293ee, v2\n\t"
        "v_fmamk_f32 v70, v70, 0x3e0293ee, v2\n\t"
        "v_fmamk_f32 v71, v71, 0x3e0293ee, v2\n\t"
        "v_exp_f32_e32 v68, v68\n\t"
        "v_exp_f32_e32 v69, v69\n\t"
        "v_exp_f32_e32 v70, v70\n\t"
        "s_waitcnt lgkmcnt(2)\n\t"
        "v_smfmac_f32_16x16x64_bf16 v[64:67], v[72:75], v[16:23], v122\n\t"
        "s_waitcnt lgkmcnt(0)\n\t"
        "v_smfmac_f32_16x16x64_bf16 v[64:67], v[76:79], v[24:31], v122\n\t"
        "s_waitcnt vmcnt(3)\n\t"
        "v_cvt_pk_bf16_f32 v36, v68, v69\n\t"
        "v_exp_f32_e32 v71, v71\n\t"
        "s_nop 0\n\t"
        "v_mov_b32_dpp v38, v36 row_ror:8 row_mask:0xf bank_mask:0xf\n\t"
        "v_cvt_pk_bf16_f32 v37, v70, v71\n\t"
        "s_nop 1\n\t"
        "v_mov_b32_dpp v39, v37 row_ror:8 row_mask:0xf bank_mask:0xf\n\t"
        "v_pk_add_f32 v[64:65], v[64:65], v[66:67]\n\t"
        "v_max3_f32 v3, v0, v64, v65\n\t"
        "v_max3_f32 v1, v0, v64, v65\n\t"
        "v_mfma_f32_16x16x32_bf16 v[40:43], v[112:115], v[36:39], 0\n\t"
        "s_nop 0\n\t"
        "v_permlane16_swap_b32_e32 v3, v1\n\t"
        "v_max_f32_e32 v2, v3, v1\n\t"
        "v_max_f32_e32 v3, v3, v1\n\t"
        "v_mfma_f32_16x16x32_bf16 v[44:47], v[116:119], v[36:39], 0\n\t"
        "s_nop 0\n\t"
        "v_permlane32_swap_b32_e32 v2, v3\n\t"
        "v_max_f32_e32 v2, v2, v3\n\t"
        "v_mfma_f32_16x16x32_bf16 v[96:99], v[12:15], v[36:39], 0\n\t"
        "v_mul_f32_e32 v2, 0xbe0293ee, v2\n\t"
        "v_fmamk_f32 v10, v0, 0x3e0293ee, v2\n\t"
        "v_fmamk_f32 v64, v64, 0x3e0293ee, v2\n\t"
        "v_exp_f32_e32 v10, v10\n\t"
        "v_fmamk_f32 v65, v65, 0x3e0293ee, v2\n\t"
        "v_exp_f32_e32 v64, v64\n\t"
        "s_nop 1\n\t"
        "v_mul_f32_e32 v96, v96, v10\n\t"
        "v_exp_f32_e32 v65, v65\n\t"
        "v_pk_mul_f32 v[40:41], v[40:41], v[10:11] op_sel_hi:[1,0]\n\t"
        "v_cvt_pk_bf16_f32 v90, v64, v65\n\t"
        "v_pk_mul_f32 v[42:43], v[42:43], v[10:11] op_sel_hi:[1,0]\n\t"
        "v_cvt_pk_bf16_f32 v95, v64, v65\n\t"
        "v_pk_mul_f32 v[44:45], v[44:45], v[10:11] op_sel_hi:[1,0]\n\t"
        "v_mfma_f32_16x16x16_bf16 v[96:99], v[12:13], v[90:91], v[96:99]\n\t"
        "s_waitcnt vmcnt(1)\n\t"
        "v_mul_f32_e32 v46, v46, v10\n\t"
        "v_mul_f32_e32 v47, v47, v10\n\t"
        "v_mfma_f32_16x16x16_bf16 v[40:43], v[88:89], v[90:91], v[40:43]\n\t"
        "v_max_u32_e32 v54, v88, v89\n\t"
        "v_cmp_eq_u32_e32 vcc, -1, v54\n\t"
        "v_mfma_f32_16x16x16_bf16 v[44:47], v[88:89], v[94:95], v[44:47]\n\t"
        "s_nop 0\n\t"
        "v_rcp_f32_e32 v2, v96\n\t"
        "s_nop 0\n\t"
        "s_cbranch_vccnz .Lfb%=\n\t"
        ".Lrb%=:\n\t"
        "s_nop 0\n\t"
        "v_pk_mul_f32 v[40:41], v[40:41], v[2:3] op_sel_hi:[1,0]\n\t"
        "v_pk_mul_f32 v[42:43], v[42:43], v[2:3] op_sel_hi:[1,0]\n\t"
        "v_cvt_pk_bf16_f32 v100, v40, v41\n\t"
        "v_pk_mul_f32 v[44:45], v[44:45], v[2:3] op_sel_hi:[1,0]\n\t"
        "v_pk_mul_f32 v[46:47], v[46:47], v[2:3] op_sel_hi:[1,0]\n\t"
        "v_cvt_pk_bf16_f32 v101, v42, v43\n\t"
        "v_cvt_pk_bf16_f32 v102, v44, v45\n\t"
        "v_min_u32_e32 v100, -2, v100\n\t"
        "v_cvt_pk_bf16_f32 v103, v46, v47\n\t"
        "v_min_u32_e32 v102, -2, v102\n\t"
        "buffer_store_dwordx4 v[100:103], v33, %[srd], s63 offen sc1\n\t"
        "s_memrealtime s[76:77]\n\t"
        "v_mov_b32_e32 v8, -1\n\t"
        "v_mov_b32_e32 v9, -1\n\t"
        "v_mov_b32_e32 v10, -1\n\t"
        "v_mov_b32_e32 v11, -1\n\t"
        "v_permlane16_swap_b32_e32 v50, v51\n\t"
        "v_permlane16_swap_b32_e32 v88, v89\n\t"
        "v_mov_b32_e32 v14, s40\n\t"
        "s_waitcnt lgkmcnt(0)\n\t"
        "s_waitcnt vmcnt(1)\n\t"
        "s_mov_b32 s97, 0\n\t"
        ".Lsr%=:\n\t"
        "s_mov_b32 exec_hi, 0xffff\n\t"
        "v_subrev_u32_e32 v0, s41, v84\n\t"
        "v_subrev_u32_e32 v1, s41, v86\n\t"
        "v_max_i32_e32 v0, v0, v1\n\t"
        "v_max_u32_e32 v1, v85, v87\n\t"
        "v_cmp_eq_u32_e32 vcc, -1, v1\n\t"
        "v_max_i32_dpp v0, v0, v0 quad_perm:[1,0,3,2] row_mask:0xf bank_mask:0xf\n\t"
        "v_or_b32_e32 v2, v85, v87\n\t"
        "s_nop 0\n\t"
        "v_max_i32_dpp v0, v0, v0 quad_perm:[2,3,0,1] row_mask:0xf bank_mask:0xf\n\t"
        "s_nop 1\n\t"
        "v_max_i32_dpp v0, v0, v0 row_ror:4 row_mask:0xf bank_mask:0xf\n\t"
        "s_nop 1\n\t"
        "v_max_i32_dpp v0, v0, v0 row_ror:8 row_mask:0xf bank_mask:0xf\n\t"
        "s_cbranch_vccnz .Lsf%=\n\t"
        "v_readlane_b32 s85, v0, 0\n\t"
        "v_readlane_b32 s81, v0, 16\n\t"
        "v_readlane_b32 s56, v0, 32\n\t"
        "v_and_b32_e32 v1, 1, v2\n\t"
        "v_cmp_ne_u32_e64 s[86:87], 0, v1\n\t"
        "v_and_b32_e32 v1, 2, v2\n\t"
        "v_cmp_ne_u32_e64 s[88:89], 0, v1\n\t"
        "v_and_b32_e32 v1, 4, v2\n\t"
        "v_cmp_ne_u32_e64 s[90:91], 0, v1\n\t"
        "s_max_i32 s56, s56, s81\n\t"
        "s_add_u32 s92, s85, s41\n\t"
        "s_sub_u32 s92, s92, s43\n\t"
        "s_add_i32 s93, s56, s47\n\t"
        "s_mov_b32 s44, s46\n\t"
        "s_mov_b32 s46, s45\n\t"
        "s_mov_b32 s94, s48\n\t"
        "s_mov_b32 s43, s41\n\t"
        "s_cmp_gt_i32 s56, s54\n\t"
        "s_cbranch_scc1 .Lmv%=\n\t"
        "s_mov_b32 s37, 0\n\t"
        "s_mov_b32 s48, 0\n\t"
        "s_mov_b32 s95, 0\n\t"
        ".Lsd%=:\n\t"
        "s_mov_b32 exec_hi, -1\n\t"
        "s_add_u32 s42, s43, s46\n\t"
        "s_sleep %c[HSLP]\n\t"
        "buffer_load_dwordx2 v[48:49], off, %[wsd], s64 sc1\n\t"
        "buffer_load_dwordx4 v[104:107], v120, %[wsd], s69 offen sc1\n\t"
        "buffer_load_dwordx4 v[108:111], v120, %[wsd], s70 offen sc1\n\t"
        "buffer_store_dwordx2 v[50:51], v34, %[wsd], s72 offen sc1\n\t"
        "buffer_store_dwordx2 v[88:89], v34, %[wsd], s72 offen offset:2048 sc1\n\t"
        "s_lshl_b32 s80, %[w], 8\n\t"
        "s_add_u32 s80, s80, s61\n\t"
        "s_add_u32 s80, s80, 24576\n\t"
        "buffer_store_dwordx2 v[8:9], v34, %[srd], s80 offen sc1\n\t"
        "s_cmp_eq_u32 %[w], 0\n\t"
        "s_cselect_b32 exec_hi, 0, -1\n\t"
        "buffer_store_dwordx4 v[8:11], v120, %[srd], s68 offen sc1\n\t"
        "s_mov_b32 exec_hi, -1\n\t"
        "s_mov_b64 exec, 1\n\t"
        "buffer_store_dword v14, off, %[wsd], s74 sc1\n\t"
        "s_mov_b64 exec, -1\n\t"
        "s_cmp_ge_u32 s40, %c[iCT0]\n\t"
        "s_cselect_b32 s85, 1, 0\n\t"
        "s_cmp_lg_u64 s[86:87], 0\n\t"
        "s_cselect_b32 s86, s85, 0\n\t"
        "s_cmp_lg_u64 s[88:89], 0\n\t"
        "s_cselect_b32 s87, s85, 0\n\t"
        "s_cmp_lg_u64 s[90:91], 0\n\t"
        "s_cselect_b32 s88, s85, 0\n\t"
        "s_or_b32 s80, s94, s86\n\t"
        "s_or_b32 s80, s80, s87\n\t"
        "s_andn2_b32 s90, s85, s80\n\t"
        "s_cmp_le_i32 s92, s44\n\t"
        "s_cselect_b32 s80, s85, 0\n\t"
        "s_andn2_b32 s91, s80, s88\n\t"
        "s_and_b32 s80, s40, %c[LDEC]\n\t"
        "s_cselect_b32 s89, 0, 1\n\t"
        "s_mov_b32 s80, s88\n\t"
        "s_add_i32 s81, s92, s50\n\t"
        "s_sub_i32 s81, s81, 4\n\t"
        "s_cmp_le_i32 s81, s44\n\t"
        "s_cselect_b32 s80, s80, 0\n\t"
        "s_cmp_lt_i32 s50, %c[LC24]\n\t"
        "s_cselect_b32 s80, s80, 0\n\t"
        "s_lshl2_add_u32 s50, s80, s50\n\t"
        "s_sub_i32 s50, s50, s89\n\t"
        "s_max_i32 s50, s50, %c[LC]\n\t"
        "s_lshl_b32 s80, s92, 1\n\t"
        "s_sub_i32 s81, s80, s52\n\t"
        "s_ashr_i32 s82, s81, %c[UPSH]\n\t"
        "s_max_i32 s82, s82, %c[TRUP]\n\t"
        "s_add_i32 s82, s82, s52\n\t"
        "s_min_i32 s82, s82, s80\n\t"
        "s_ashr_i32 s83, s81, 1\n\t"
        "s_min_i32 s83, s83, -%c[TRDN]\n\t"
        "s_add_i32 s83, s83, s52\n\t"
        "s_max_i32 s83, s83, s80\n\t"
        "s_cmp_gt_i32 s81, 0\n\t"
        "s_cselect_b32 s82, s82, s83\n\t"
        "s_cmp_lg_u32 s90, 0\n\t"
        "s_cselect_b32 s52, s82, s52\n\t"
        "s_add_i32 s80, s52, 1\n\t"
        "s_ashr_i32 s80, s80, 1\n\t"
        "s_add_i32 s45, s80, s50\n\t"
        "s_andn2_b32 s80, s86, s94\n\t"
        "s_cmp_lt_i32 s51, %c[LA24]\n\t"
        "s_cselect_b32 s80, s80, 0\n\t"
        "s_lshl2_add_u32 s51, s80, s51\n\t"
        "s_sub_i32 s51, s51, s89\n\t"
        "s_max_i32 s51, s51, %c[LA]\n\t"
        "s_lshl_b32 s80, s93, 1\n\t"
        "s_sub_i32 s81, s80, s53\n\t"
        "s_ashr_i32 s82, s81, %c[UPSH]\n\t"
        "s_max_i32 s82, s82, %c[TRUP]\n\t"
        "s_add_i32 s82, s82, s53\n\t"
        "s_min_i32 s82, s82, s80\n\t"
        "s_ashr_i32 s83, s81, 1\n\t"
        "s_min_i32 s83, s83, -%c[TRDN]\n\t"
        "s_add_i32 s83, s83, s53\n\t"
        "s_max_i32 s83, s83, s80\n\t"
        "s_cmp_gt_i32 s81, 0\n\t"
        "s_cselect_b32 s82, s82, s83\n\t"
        "s_cmp_lg_u32 s91, 0\n\t"
        "s_cselect_b32 s53, s82, s53\n\t"
        "s_add_i32 s80, s53, 1\n\t"
        "s_ashr_i32 s80, s80, 1\n\t"
        "s_add_i32 s47, s80, s51\n\t"
        "s_add_u32 s81, s47, %c[AUP]\n\t"
        "s_min_i32 s80, s95, s81\n\t"
        "s_max_i32 s47, s47, s80\n\t"
        "s_add_u32 s41, s42, s47\n\t"
        "s_add_u32 s40, s40, 1\n\t"
        "v_mov_b32_e32 v88, 0\n\t"
        "v_mov_b32_e32 v89, 0\n\t"
        "v_mov_b32_e32 v90, 0\n\t"
        "v_mov_b32_e32 v91, 0\n\t"
        "v_mov_b32_e32 v92, 0\n\t"
        "v_mov_b32_e32 v93, 0\n\t"
        "v_mov_b32_e32 v94, 0\n\t"
        "v_mov_b32_e32 v95, 0\n\t"
        "v_mov_b32_e32 v96, 0\n\t"
        "v_mov_b32_e32 v97, 0\n\t"
        "v_mov_b32_e32 v98, 0\n\t"
        "v_mov_b32_e32 v99, 0\n\t"
        "v_mov_b32_e32 v100, 0\n\t"
        "v_mov_b32_e32 v101, 0\n\t"
        "v_mov_b32_e32 v102, 0\n\t"
        "v_mov_b32_e32 v103, 0\n\t"
        "ds_read_b128 v[16:19], v121 offset:0\n\t"
        "ds_read_b128 v[20:23], v121 offset:1024\n\t"
        "ds_read_b128 v[24:27], v121 offset:2048\n\t"
        "ds_read_b128 v[28:31], v121 offset:3072\n\t"
        "ds_read_b128 v[32:35], v121 offset:4096\n\t"
        "ds_read_b128 v[36:39], v121 offset:5120\n\t"
        "ds_read_b128 v[40:43], v121 offset:6144\n\t"
        "ds_read_b128 v[44:47], v121 offset:7168\n\t"
        "ds_read_b128 v[56:59], v121 offset:8192\n\t"
        "ds_read_b128 v[60:63], v121 offset:9216\n\t"
        "ds_read_b128 v[64:67], v121 offset:10240\n\t"
        "ds_read_b128 v[68:71], v121 offset:11264\n\t"
        "ds_read_b128 v[72:75], v121 offset:12288\n\t"
        "ds_read_b128 v[76:79], v121 offset:13312\n\t"
        "ds_read_b128 v[80:83], v121 offset:14336\n\t"
        "ds_read_b128 v[84:87], v121 offset:15360\n\t"
        "v_mov_b32_e32 v6, 30720\n\t"
        "ds_read_b64 v[6:7], v6\n\t"
        "s_waitcnt lgkmcnt(0)\n\t"
        "v_readfirstlane_b32 s80, v6\n\t"
        "v_readfirstlane_b32 s81, v7\n\t"
        "s_cmp_eq_u32 s80, s40\n\t"
        "s_cselect_b32 s84, 1, 0\n\t"
        "s_cmp_eq_u32 s81, s40\n\t"
        "s_cselect_b32 s81, 2, 0\n\t"
        "s_or_b32 s84, s84, s81\n\t"
        "s_waitcnt vmcnt(7)\n\t"
        "s_cmp_eq_u32 s64, s36\n\t"
        "s_cbranch_scc1 .Lk11%=\n\t"
        "s_mov_b32 s97, 0\n\t"
        ".Lk01%=:\n\t"
        "v_readfirstlane_b32 s82, v48\n\t"
        "v_readfirstlane_b32 s83, v49\n\t"
        "s_xor_b32 s82, s82, %[tag]\n\t"
        "s_xor_b32 s83, s83, %[tag]\n\t"
        "s_or_b32 s82, s82, s83\n\t"
        "s_cbranch_scc0 .Lk11%=\n\t"
        "s_sleep 3\n\t"
        "buffer_load_dwordx2 v[48:49], off, %[wsd], s64 sc1\n\t"
        "s_add_u32 s97, s97, 1\n\t"
        "s_cmp_lt_u32 s97, 0x200000\n\t"
        "s_cbranch_scc0 .Lgu%=\n\t"
        "s_waitcnt vmcnt(0)\n\t"
        "s_branch .Lk01%=\n\t"
        ".Lk11%=:\n\t"
        "v_mov_b32_e32 v48, s76\n\t"
        "v_mov_b32_e32 v49, s84\n\t"
        "s_mov_b32 s85, s67\n\t"
        "s_and_b32 s80, s40, 3\n\t"
        "s_lshl_b32 s80, s80, 17\n\t"
        "s_add_u32 s57, s80, %[nA]\n\t"
        "s_add_u32 s58, s57, 0x1000\n\t"
        "s_add_u32 s59, s57, 0x4000\n\t"
        "s_add_u32 s60, s57, 0x5000\n\t"
        "s_and_b32 s80, s40, 3\n\t"
        "s_lshl_b32 s80, s80, 17\n\t"
        "s_add_u32 s63, s80, %[nC]\n\t"
        "s_add_u32 s66, s80, %[nS]\n\t"
        "s_add_u32 s81, s80, 0x20000\n\t"
        "s_and_b32 s81, s81, 0x60000\n\t"
        "s_add_u32 s67, s81, %[nW]\n\t"
        "s_cmp_eq_u32 %[w], 2\n\t"
        "s_cselect_b32 s81, s80, s81\n\t"
        "s_xor_b32 s81, s81, 0x40000\n\t"
        "s_add_u32 s68, s81, %[pob]\n\t"
        "s_lshl_b32 s72, s40, 17\n\t"
        "s_add_u32 s72, s72, %[kvo]\n\t"
        "v_lshlrev_b32_e32 v51, 6, v120\n\t"
        "s_add_u32 s80, s85, %[swo]\n\t"
        "s_cmp_eq_u32 %[w], 0\n\t"
        "s_cselect_b32 s73, s80, s36\n\t"
        "s_cselect_b32 s74, %[pra], s36\n\t"
        "s_add_u32 s80, s40, 1\n\t"
        "s_min_u32 s80, s80, 63\n\t"
        "s_lshl_b32 s80, s80, 18\n\t"
        "s_add_u32 s69, s80, %[kx]\n\t"
        "s_add_u32 s70, s69, 0x1000\n\t"
        "s_lshl_b32 s80, s40, 18\n\t"
        "s_add_u32 s71, s80, %[vx]\n\t"
        "s_add_u32 s80, s40, 2\n\t"
        "s_lshl_b32 s81, s80, 9\n\t"
        "s_add_u32 s81, s81, %[kf]\n\t"
        "s_and_b32 s80, s80, 0x43\n\t"
        "s_cselect_b32 s64, s36, s81\n\t"
        "s_mov_b32 s80, s61\n\t"
        "s_mov_b32 s61, s62\n\t"
        "s_mov_b32 s62, s80\n\t"
        "s_waitcnt lgkmcnt(0)\n\t"
        "s_branch .Lloop%=\n\t"
        ".Lmv%=:\n\t"
        "s_mov_b32 s48, 1\n\t"
        "s_cmp_eq_u32 s54, %c[iTOL]\n\t"
        "s_cbranch_scc1 .Lm1%=\n\t"
        "s_max_i32 s80, s85, s56\n\t"
        "s_add_u32 s43, s41, s80\n\t"
        "s_add_u32 s43, s43, %c[iPEN0]\n\t"
        "s_mov_b32 s54, %c[iTOL]\n\t"
        "s_add_u32 s46, s46, %c[iX0C]\n\t"
        "s_add_u32 s95, s47, %c[iX0A]\n\t"
        "v_and_b32_e32 v0, 3, v84\n\t"
        "v_and_b32_e32 v1, 3, v86\n\t"
        "v_lshlrev_b32_e64 v0, v0, 1\n\t"
        "v_lshlrev_b32_e64 v1, v1, 1\n\t"
        "v_or_b32_e32 v0, v0, v1\n\t"
        "s_mov_b32 s80, 0\n\t"
        "v_and_b32_e32 v1, 1, v0\n\t"
        "v_cmp_ne_u32_e32 vcc, 0, v1\n\t"
        "s_nop 1\n\t"
        "s_cmp_lg_u64 vcc, 0\n\t"
        "s_cselect_b32 s81, 2, 0\n\t"
        "s_or_b32 s80, s80, s81\n\t"
        "v_and_b32_e32 v1, 2, v0\n\t"
        "v_cmp_ne_u32_e32 vcc, 0, v1\n\t"
        "s_nop 1\n\t"
        "s_cmp_lg_u64 vcc, 0\n\t"
        "s_cselect_b32 s81, 4, 0\n\t"
        "s_or_b32 s80, s80, s81\n\t"
        "v_and_b32_e32 v1, 4, v0\n\t"
        "v_cmp_ne_u32_e32 vcc, 0, v1\n\t"
        "s_nop 1\n\t"
        "s_cmp_lg_u64 vcc, 0\n\t"
        "s_cselect_b32 s81, 8, 0\n\t"
        "s_or_b32 s80, s80, s81\n\t"
        "v_and_b32_e32 v1, 8, v0\n\t"
        "v_cmp_ne_u32_e32 vcc, 0, v1\n\t"
        "s_nop 1\n\t"
        "s_cmp_lg_u64 vcc, 0\n\t"
        "s_cselect_b32 s81, 16, 0\n\t"
        "s_or_b32 s80, s80, s81\n\t"
        "s_lshr_b32 s80, 0x1adf1210, s80\n\t"
        "s_and_b32 s55, s80, 3\n\t"
        "s_branch .Lal%=\n\t"
        ".Lm1%=:\n\t"
        "s_add_u32 s80, s37, %c[iPEN2]\n\t"
        "s_min_i32 s80, s80, %c[iPENM]\n\t"
        "s_cmp_lg_u32 s37, 0\n\t"
        "s_cselect_b32 s37, s80, %c[iPEN]\n\t"
        "s_add_u32 s80, s56, s37\n\t"
        "s_max_i32 s80, s80, %c[iPMIN]\n\t"
        "s_add_u32 s43, s41, s80\n\t"
        "s_add_u32 s95, s93, %c[LA]\n\t"
        "s_add_u32 s46, s46, %c[iXR]\n\t"
        ".Lal%=:\n\t"
        "s_sub_u32 s80, s55, s43\n\t"
        "s_and_b32 s80, s80, %c[iPHM]\n\t"
        "s_add_u32 s43, s43, s80\n\t"
        "s_branch .Lsd%=\n\t"
        ".Lsf%=:\n\t"
        "s_mov_b32 exec_hi, -1\n\t"
        "s_sleep 2\n\t"
        "buffer_load_dwordx4 v[84:87], v120, %[srd], s66 offen sc1\n\t"
        "s_add_u32 s97, s97, 1\n\t"
        "s_cmp_lt_u32 s97, 0x100000\n\t"
        "s_cbranch_scc0 .Lgu%=\n\t"
        "s_waitcnt vmcnt(0)\n\t"
        "s_branch .Lsr%=\n\t"
        ".Lf1%=:\n\t"
        "ds_read_b128 v[92:95], v120 offset:0\n\t"
        "ds_read_b128 v[68:71], v120 offset:4096\n\t"
        "v_max_u32_e32 v54, v16, v18\n\t"
        "v_max3_u32 v54, v54, v17, v19\n\t"
        "v_max3_u32 v54, v54, v20, v22\n\t"
        "v_max3_u32 v54, v54, v21, v23\n\t"
        "v_max3_u32 v54, v54, v24, v26\n\t"
        "v_max3_u32 v54, v54, v25, v27\n\t"
        "v_max3_u32 v54, v54, v28, v30\n\t"
        "v_max3_u32 v54, v54, v29, v31\n\t"
        "v_max3_u32 v54, v54, v32, v34\n\t"
        "v_max3_u32 v54, v54, v33, v35\n\t"
        "v_max3_u32 v54, v54, v36, v38\n\t"
        "v_max3_u32 v54, v54, v37, v39\n\t"
        "v_max3_u32 v54, v54, v40, v42\n\t"
        "v_max3_u32 v54, v54, v41, v43\n\t"
        "v_max3_u32 v54, v54, v44, v46\n\t"
        "v_max3_u32 v54, v54, v45, v47\n\t"
        "v_max3_u32 v54, v54, v48, v50\n\t"
        "v_max3_u32 v54, v54, v49, v51\n\t"
        "v_max3_u32 v54, v54, v0, v2\n\t"
        "v_max3_u32 v54, v54, v1, v3\n\t"
        "v_max3_u32 v54, v54, v4, v6\n\t"
        "v_max3_u32 v54, v54, v5, v7\n\t"
        "v_max3_u32 v54, v54, v8, v10\n\t"
        "v_max3_u32 v54, v54, v9, v11\n\t"
        "s_waitcnt lgkmcnt(0)\n\t"
        "v_max3_u32 v54, v54, v92, v94\n\t"
        "v_max3_u32 v54, v54, v93, v95\n\t"
        "v_max3_u32 v54, v54, v68, v70\n\t"
        "v_max3_u32 v54, v54, v69, v71\n\t"
        "ds_read_b128 v[92:95], v120 offset:8192\n\t"
        "ds_read_b128 v[68:71], v120 offset:12288\n\t"
        "s_waitcnt lgkmcnt(0)\n\t"
        "v_max3_u32 v54, v54, v92, v94\n\t"
        "v_max3_u32 v54, v54, v93, v95\n\t"
        "v_max3_u32 v54, v54, v68, v70\n\t"
        "v_max3_u32 v54, v54, v69, v71\n\t"
        "v_cmp_eq_u32_e32 vcc, -1, v54\n\t"
        "s_nop 1\n\t"
        "v_mov_b32_e32 v55, v55\n\t"
        "s_nop 0\n\t"
        "s_cbranch_vccz .Lr1%=\n\t"
        "buffer_load_dwordx4 v[0:3], v120, %[srd], s57 offen sc1\n\t"
        "buffer_load_dwordx4 v[4:7], v120, %[srd], s58 offen sc1\n\t"
        "buffer_load_dwordx4 v[8:11], v120, %[srd], s59 offen sc1\n\t"
        "buffer_load_dwordx4 v[12:15], v120, %[srd], s60 offen sc1\n\t"
        "s_add_u32 s80, s40, 1\n\t"
        "v_mov_b32_e32 v54, 30720\n\t"
        "v_mov_b32_e32 v55, s80\n\t"
        "ds_write_b32 v54, v55\n\t"
        "ds_read_b128 v[16:19], v121 offset:0\n\t"
        "ds_read_b128 v[20:23], v121 offset:1024\n\t"
        "ds_read_b128 v[24:27], v121 offset:2048\n\t"
        "ds_read_b128 v[28:31], v121 offset:3072\n\t"
        "ds_read_b128 v[32:35], v121 offset:4096\n\t"
        "ds_read_b128 v[36:39], v121 offset:5120\n\t"
        "ds_read_b128 v[40:43], v121 offset:6144\n\t"
        "ds_read_b128 v[44:47], v121 offset:7168\n\t"
        "ds_read_b128 v[56:59], v121 offset:8192\n\t"
        "ds_read_b128 v[60:63], v121 offset:9216\n\t"
        "ds_read_b128 v[64:67], v121 offset:10240\n\t"
        "ds_read_b128 v[68:71], v121 offset:11264\n\t"
        "ds_read_b128 v[72:75], v121 offset:12288\n\t"
        "ds_read_b128 v[76:79], v121 offset:13312\n\t"
        "ds_read_b128 v[80:83], v121 offset:14336\n\t"
        "ds_read_b128 v[84:87], v121 offset:15360\n\t"
        "v_mov_b32_e32 v88, 0\n\t"
        "v_mov_b32_e32 v89, 0\n\t"
        "v_mov_b32_e32 v90, 0\n\t"
        "v_mov_b32_e32 v91, 0\n\t"
        "v_mov_b32_e32 v92, 0\n\t"
        "v_mov_b32_e32 v93, 0\n\t"
        "v_mov_b32_e32 v94, 0\n\t"
        "v_mov_b32_e32 v95, 0\n\t"
        "v_mov_b32_e32 v96, 0\n\t"
        "v_mov_b32_e32 v97, 0\n\t"
        "v_mov_b32_e32 v98, 0\n\t"
        "v_mov_b32_e32 v99, 0\n\t"
        "v_mov_b32_e32 v100, 0\n\t"
        "v_mov_b32_e32 v101, 0\n\t"
        "v_mov_b32_e32 v102, 0\n\t"
        "v_mov_b32_e32 v103, 0\n\t"
        "s_waitcnt lgkmcnt(0)\n\t"
        "s_barrier\n\t"
        ".Lfa%=:\n\t"
        "s_waitcnt vmcnt(0)\n\t"
        "v_max_u32_e32 v54, v0, v2\n\t"
        "v_max3_u32 v54, v54, v1, v3\n\t"
        "v_max3_u32 v54, v54, v4, v6\n\t"
        "v_max3_u32 v54, v54, v5, v7\n\t"
        "v_max3_u32 v54, v54, v8, v10\n\t"
        "v_max3_u32 v54, v54, v9, v11\n\t"
        "v_max3_u32 v54, v54, v12, v14\n\t"
        "v_max3_u32 v54, v54, v13, v15\n\t"
        "v_cmp_eq_u32_e32 vcc, -1, v54\n\t"
        "s_nop 1\n\t"
        "v_mov_b32_e32 v55, v55\n\t"
        "s_nop 0\n\t"
        "s_cbranch_vccz .Lfg%=\n\t"
        "s_sleep %c[ASLP]\n\t"
        "buffer_load_dwordx4 v[0:3], v120, %[srd], s57 offen sc1\n\t"
        "buffer_load_dwordx4 v[4:7], v120, %[srd], s58 offen sc1\n\t"
        "buffer_load_dwordx4 v[8:11], v120, %[srd], s59 offen sc1\n\t"
        "buffer_load_dwordx4 v[12:15], v120, %[srd], s60 offen sc1\n\t"
        "s_add_u32 s97, s97, 1\n\t"
        "s_cmp_lt_u32 s97, 0x100000\n\t"
        "s_cbranch_scc0 .Lgu%=\n\t"
        "s_branch .Lfa%=\n\t"
        ".Lfg%=:\n\t"
        "s_sleep %c[RSLP]\n\t"
        "s_branch .Lgo%=\n\t"
        ".Lfb%=:\n\t"
        "s_nop 7\n\t"
        "s_mov_b32 s97, 0\n\t"
        "s_add_u32 s80, s40, 1\n\t"
        "v_mov_b32_e32 v54, 30724\n\t"
        "v_mov_b32_e32 v55, s80\n\t"
        "ds_write_b32 v54, v55\n\t"
        ".Lfp%=:\n\t"
        "s_sleep %c[ASLP]\n\t"
        "buffer_load_dword v88, v127, %[srd], s61 offen offset:0 sc1\n\t"
        "buffer_load_dword v89, v127, %[srd], s61 offen offset:256 sc1\n\t"
        "s_add_u32 s97, s97, 1\n\t"
        "s_cmp_lt_u32 s97, 0x100000\n\t"
        "s_cbranch_scc0 .Lgu%=\n\t"
        "s_waitcnt vmcnt(0)\n\t"
        "v_max_u32_e32 v54, v88, v89\n\t"
        "v_cmp_eq_u32_e32 vcc, -1, v54\n\t"
        "s_nop 1\n\t"
        "v_mov_b32_e32 v55, v55\n\t"
        "s_nop 0\n\t"
        "s_cbranch_vccnz .Lfp%=\n\t"
        "v_mfma_f32_16x16x32_bf16 v[40:43], v[112:115], v[36:39], 0\n\t"
        "v_mfma_f32_16x16x32_bf16 v[44:47], v[116:119], v[36:39], 0\n\t"
        "s_nop 1\n\t"
        "v_mov_b32_e32 v55, v55\n\t"
        "s_nop 1\n\t"
        "v_mov_b32_e32 v55, v55\n\t"
        "s_nop 0\n\t"
        "v_pk_mul_f32 v[40:41], v[40:41], v[10:11] op_sel_hi:[1,0]\n\t"
        "v_pk_mul_f32 v[42:43], v[42:43], v[10:11] op_sel_hi:[1,0]\n\t"
        "v_pk_mul_f32 v[44:45], v[44:45], v[10:11] op_sel_hi:[1,0]\n\t"
        "v_pk_mul_f32 v[46:47], v[46:47], v[10:11] op_sel_hi:[1,0]\n\t"
        "v_mfma_f32_16x16x16_bf16 v[40:43], v[88:89], v[90:91], v[40:43]\n\t"
        "s_nop 0\n\t"
        "v_mfma_f32_16x16x16_bf16 v[44:47], v[88:89], v[94:95], v[44:47]\n\t"
        "s_nop 7\n\t"
        "s_branch .Lrb%=\n\t"
        ".Lta%=:\n\t"
        "s_waitcnt lgkmcnt(0)\n\t"
        "s_branch .Lxa%=\n\t"
        ".Lgu%=:\n\t"
        "s_mov_b64 exec, -1\n\t"
        "v_mov_b32_e32 v54, %c[ferr]\n\t"
        "v_mov_b32_e32 v55, 1\n\t"
        "buffer_store_dword v55, v54, %[wsd], 0 offen sc1\n\t"
        "s_endpgm\n\t"
        ".Lend%=:\n\t"
        "s_nop 0\n\t"
        "v_permlane16_swap_b32_e32 v50, v51\n\t"
        "v_mov_b32_e32 v0, 64\n\t"
        "buffer_store_dwordx2 v[50:51], v34, %[wsd], s72 offen sc1\n\t"
        "s_cmp_lg_u32 %[w], 0\n\t"
        "s_waitcnt vmcnt(0)\n\t"
        "s_barrier\n\t"
        "s_cbranch_scc1 .Le2%=\n\t"
        "s_mov_b64 exec, 1\n\t"
        "buffer_store_dword v0, off, %[wsd], %[pra] sc1\n\t"
        "s_mov_b64 exec, -1\n\t"
        ".Le2%=:\n\t"
        "s_endpgm"
        :
        : [srd] "s"(rhd), [wsd] "s"(wsd), [w] "s"(w), [tag] "s"(tag), [nA] "s"(nA), [nS] "s"(nS), [nW] "s"(nW), [nC] "s"(nC), [b0] "s"(b0), [b1] "s"(b1), [kx] "s"(kx), [vx] "s"(vx), [kf] "s"(kf), [kvo] "s"(kvo), [pob] "s"(pob),
          [swo] "s"(swo), [pra] "s"(pr_a), [ferr] "i"(F_ERR), [iD1] "i"(D1), [iD2] "i"(D2), [LC] "i"(LC0), [LA] "i"(LA0), [LC24] "i"(LC0 + 24), [LA24] "i"(LA0 + 24), [E1] "i"(2 * (D1 - LC0)), [E2] "i"(2 * (D2 - LA0)),
          [iPEN] "i"(PEN), [iPEN2] "i"(PEN2), [iPENM] "i"(PEN + 2 * PEN2), [iPEN0] "i"(PEN0), [iTOL] "i"(TOL), [iCT0] "i"(CT0), [iX0C] "i"(X0C), [iX0A] "i"(X0A), [iPHM] "i"(PH_M),
          [iFT] "i"(-3), [iFN] "i"(FINE_N), [AUP] "i"(A_UP), [ASLP] "i"(A_SLP), [LDEC] "i"(L_DEC), [UPSH] "i"(UP_SH), [TRUP] "i"(TR_UP), [TRDN] "i"(TR_DN), [HSLP] "i"(H_SLP), [iPMIN] "i"(PMIN), [VSLP] "i"(V_SLP), [RSLP] "i"(R_SLP), [iVD] "i"(V_DEL), [iXR] "i"(XR),
          "{v[120:127]}"(pc), "{v[52:53]}"(pd), "{a[0:31]}"(wa[0]), "{a[32:63]}"(wa[1]), "{a[64:95]}"(wa[2]), "{a[96:127]}"(wa[3]), "{a[128:159]}"(wa[4]), "{a[160:191]}"(wa[5]), "{a[192:223]}"(wa[6]), "{a[224:255]}"(wa[7]), "{v[128:159]}"(wg[0]), "{v[160:191]}"(wg[1]), "{v[192:223]}"(wg[2]), "{v[224:255]}"(wg[3])
        : "memory", "vcc", "scc", "s36", "s37", "s38", "s39", "s40", "s41", "s42", "s43", "s44", "s45", "s46", "s47", "s48", "s49", "s50", "s51", "s52", "s53", "s54", "s55", "s56", "s57", "s58", "s59", "s60", "s61", "s62", "s63", "s64", "s65", "s66", "s67", "s68", "s69", "s70", "s71", "s72", "s73", "s74", "s75", "s76", "s77", "s78", "s79", "s80", "s81", "s82", "s83", "s84", "s85", "s86", "s87", "s88", "s89", "s90", "s91", "s92", "s93", "s94", "s95", "s96", "s97", "s98", "s99");
    } else {
        const int cb = i32;
        unsigned m0v = *(const unsigned*)(p.mem0 + (4 * G + ((l >> 2) & 1) + ((l >> 3) & 2)) * 1024 + cb * 32 + (l >> 5) * 16 + (l & 8) + (l & 3) * 2);
        {
            const v4u sv = mk_srd(p.Wv, 1024 * 4096 * 2), so = mk_srd(p.Wo, 1024 * 4096 * 2);
            const unsigned m0b = w * 32768, f = (g & 1) | ((c >> 1) << 1);
            const unsigned t0 = m0b + (16 * (g >> 1) + 4 * (g & 1) + (c & 1) + 8 * (c >> 1)) * 128 + (f << 5) + (n16 & 3) * 8, t1 = m0b + (16 * g + 4 * (c >> 1) + (c & 1)) * 64 + ((c >> 1) << 5) + (n16 & 3) * 8;
            v16u pa;
            pa[0] = (64 * w + (l >> 3)) * 8192 + vi * 128 + (((l & 7) ^ ((l >> 5) << 1)) << 4); pa[1] = pa[0] ^ 64u;
            pa[2] = (1024 * w + (l >> 2)) * 2048 + cb * 64 + (((l & 3) ^ ((g & 1) << 1)) << 4);
#pragma unroll
            for (int q = 0; q < 4; ++q) pa[3 + q] = t0 ^ (((w + q) & 3) << 5);
            pa[7] = t1; pa[8] = t1 ^ 32u; pa[9] = cal_a; pa[10] = tag; pa[11] = near; pa[12] = 0u; pa[13] = 0u; pa[14] = 0u; pa[15] = 0u;
    asm volatile(
        "s_add_u32 m0, %[m0b], 0\n\t"
        "s_mov_b32 s81, 0\n\t"
        "s_mov_b32 s82, 4\n\t"
        ".Ld0%=:\n\t"
        "buffer_load_dwordx4 v112, %[sv], s81 offen lds\n\t"
        "s_add_u32 m0, m0, 0x400\n\t"
        "s_add_u32 s81, s81, 65536\n\t"
        "buffer_load_dwordx4 v113, %[sv], s81 offen lds\n\t"
        "s_add_u32 m0, m0, 0x400\n\t"
        "s_add_u32 s81, s81, 65536\n\t"
        "s_sub_u32 s82, s82, 1\n\t"
        "s_cmp_lg_u32 s82, 0\n\t"
        "s_cbranch_scc1 .Ld0%=\n\t"
        "s_add_u32 m0, %[m0b], 8192\n\t"
        "s_mov_b32 s81, 2097152\n\t"
        "s_mov_b32 s82, 4\n\t"
        ".Ld1%=:\n\t"
        "buffer_load_dwordx4 v112, %[sv], s81 offen lds\n\t"
        "s_add_u32 m0, m0, 0x400\n\t"
        "s_add_u32 s81, s81, 65536\n\t"
        "buffer_load_dwordx4 v113, %[sv], s81 offen lds\n\t"
        "s_add_u32 m0, m0, 0x400\n\t"
        "s_add_u32 s81, s81, 65536\n\t"
        "s_sub_u32 s82, s82, 1\n\t"
        "s_cmp_lg_u32 s82, 0\n\t"
        "s_cbranch_scc1 .Ld1%=\n\t"
        "s_add_u32 m0, %[m0b], 16384\n\t"
        "s_mov_b32 s81, 4194304\n\t"
        "s_mov_b32 s82, 4\n\t"
        ".Ld2%=:\n\t"
        "buffer_load_dwordx4 v112, %[sv], s81 offen lds\n\t"
        "s_add_u32 m0, m0, 0x400\n\t"
        "s_add_u32 s81, s81, 65536\n\t"
        "buffer_load_dwordx4 v113, %[sv], s81 offen lds\n\t"
        "s_add_u32 m0, m0, 0x400\n\t"
        "s_add_u32 s81, s81, 65536\n\t"
        "s_sub_u32 s82, s82, 1\n\t"
        "s_cmp_lg_u32 s82, 0\n\t"
        "s_cbranch_scc1 .Ld2%=\n\t"
        "s_add_u32 m0, %[m0b], 24576\n\t"
        "s_mov_b32 s81, 6291456\n\t"
        "s_mov_b32 s82, 4\n\t"
        ".Ld3%=:\n\t"
        "buffer_load_dwordx4 v112, %[sv], s81 offen lds\n\t"
        "s_add_u32 m0, m0, 0x400\n\t"
        "s_add_u32 s81, s81, 65536\n\t"
        "buffer_load_dwordx4 v113, %[sv], s81 offen lds\n\t"
        "s_add_u32 m0, m0, 0x400\n\t"
        "s_add_u32 s81, s81, 65536\n\t"
        "s_sub_u32 s82, s82, 1\n\t"
        "s_cmp_lg_u32 s82, 0\n\t"
        "s_cbranch_scc1 .Ld3%=\n\t"
        "s_waitcnt vmcnt(32)\n\t"
        "buffer_store_dwordx2 v[122:123], v121, %[wsd], 0 offen sc1\n\t"
        "s_waitcnt vmcnt(25)\n\t"
        "ds_read_b64_tr_b16 v[128:129], v115 offset:0\n\t"
        "ds_read_b64_tr_b16 v[130:131], v115 offset:256\n\t"
        "ds_read_b64_tr_b16 v[132:133], v115 offset:4096\n\t"
        "ds_read_b64_tr_b16 v[134:135], v115 offset:4352\n\t"
        "ds_read_b64_tr_b16 v[136:137], v116 offset:0\n\t"
        "ds_read_b64_tr_b16 v[138:139], v116 offset:256\n\t"
        "ds_read_b64_tr_b16 v[140:141], v116 offset:4096\n\t"
        "ds_read_b64_tr_b16 v[142:143], v116 offset:4352\n\t"
        "ds_read_b64_tr_b16 v[144:145], v117 offset:0\n\t"
        "ds_read_b64_tr_b16 v[146:147], v117 offset:256\n\t"
        "ds_read_b64_tr_b16 v[148:149], v117 offset:4096\n\t"
        "ds_read_b64_tr_b16 v[150:151], v117 offset:4352\n\t"
        "ds_read_b64_tr_b16 v[152:153], v118 offset:0\n\t"
        "ds_read_b64_tr_b16 v[154:155], v118 offset:256\n\t"
        "ds_read_b64_tr_b16 v[156:157], v118 offset:4096\n\t"
        "ds_read_b64_tr_b16 v[158:159], v118 offset:4352\n\t"
        "s_waitcnt lgkmcnt(0)\n\t"
        "s_add_u32 m0, %[m0b], 0\n\t"
        "s_mov_b32 s81, 0\n\t"
        "s_mov_b32 s82, 4\n\t"
        ".Ld4%=:\n\t"
        "buffer_load_dwordx4 v114, %[so], s81 offen lds\n\t"
        "s_add_u32 m0, m0, 0x400\n\t"
        "s_add_u32 s81, s81, 32768\n\t"
        "buffer_load_dwordx4 v114, %[so], s81 offen lds\n\t"
        "s_add_u32 m0, m0, 0x400\n\t"
        "s_add_u32 s81, s81, 32768\n\t"
        "s_sub_u32 s82, s82, 1\n\t"
        "s_cmp_lg_u32 s82, 0\n\t"
        "s_cbranch_scc1 .Ld4%=\n\t"
        "s_waitcnt vmcnt(25)\n\t"
        "ds_read_b64_tr_b16 v[160:161], v115 offset:8192\n\t"
        "ds_read_b64_tr_b16 v[162:163], v115 offset:8448\n\t"
        "ds_read_b64_tr_b16 v[164:165], v115 offset:12288\n\t"
        "ds_read_b64_tr_b16 v[166:167], v115 offset:12544\n\t"
        "ds_read_b64_tr_b16 v[168:169], v116 offset:8192\n\t"
        "ds_read_b64_tr_b16 v[170:171], v116 offset:8448\n\t"
        "ds_read_b64_tr_b16 v[172:173], v116 offset:12288\n\t"
        "ds_read_b64_tr_b16 v[174:175], v116 offset:12544\n\t"
        "ds_read_b64_tr_b16 v[176:177], v117 offset:8192\n\t"
        "ds_read_b64_tr_b16 v[178:179], v117 offset:8448\n\t"
        "ds_read_b64_tr_b16 v[180:181], v117 offset:12288\n\t"
        "ds_read_b64_tr_b16 v[182:183], v117 offset:12544\n\t"
        "ds_read_b64_tr_b16 v[184:185], v118 offset:8192\n\t"
        "ds_read_b64_tr_b16 v[186:187], v118 offset:8448\n\t"
        "ds_read_b64_tr_b16 v[188:189], v118 offset:12288\n\t"
        "ds_read_b64_tr_b16 v[190:191], v118 offset:12544\n\t"
        "s_waitcnt lgkmcnt(0)\n\t"
        "s_add_u32 m0, %[m0b], 8192\n\t"
        "s_mov_b32 s81, 262144\n\t"
        "s_mov_b32 s82, 4\n\t"
        ".Ld5%=:\n\t"
        "buffer_load_dwordx4 v114, %[so], s81 offen lds\n\t"
        "s_add_u32 m0, m0, 0x400\n\t"
        "s_add_u32 s81, s81, 32768\n\t"
        "buffer_load_dwordx4 v114, %[so], s81 offen lds\n\t"
        "s_add_u32 m0, m0, 0x400\n\t"
        "s_add_u32 s81, s81, 32768\n\t"
        "s_sub_u32 s82, s82, 1\n\t"
        "s_cmp_lg_u32 s82, 0\n\t"
        "s_cbranch_scc1 .Ld5%=\n\t"
        "s_waitcnt vmcnt(25)\n\t"
        "ds_read_b64_tr_b16 v[192:193], v115 offset:16384\n\t"
        "ds_read_b64_tr_b16 v[194:195], v115 offset:16640\n\t"
        "ds_read_b64_tr_b16 v[196:197], v115 offset:20480\n\t"
        "ds_read_b64_tr_b16 v[198:199], v115 offset:20736\n\t"
        "ds_read_b64_tr_b16 v[200:201], v116 offset:16384\n\t"
        "ds_read_b64_tr_b16 v[202:203], v116 offset:16640\n\t"
        "ds_read_b64_tr_b16 v[204:205], v116 offset:20480\n\t"
        "ds_read_b64_tr_b16 v[206:207], v116 offset:20736\n\t"
        "ds_read_b64_tr_b16 v[208:209], v117 offset:16384\n\t"
        "ds_read_b64_tr_b16 v[210:211], v117 offset:16640\n\t"
        "ds_read_b64_tr_b16 v[212:213], v117 offset:20480\n\t"
        "ds_read_b64_tr_b16 v[214:215], v117 offset:20736\n\t"
        "ds_read_b64_tr_b16 v[216:217], v118 offset:16384\n\t"
        "ds_read_b64_tr_b16 v[218:219], v118 offset:16640\n\t"
        "ds_read_b64_tr_b16 v[220:221], v118 offset:20480\n\t"
        "ds_read_b64_tr_b16 v[222:223], v118 offset:20736\n\t"
        "s_waitcnt lgkmcnt(0)\n\t"
        "s_add_u32 m0, %[m0b], 16384\n\t"
        "s_mov_b32 s81, 524288\n\t"
        "s_mov_b32 s82, 4\n\t"
        ".Ld6%=:\n\t"
        "buffer_load_dwordx4 v114, %[so], s81 offen lds\n\t"
        "s_add_u32 m0, m0, 0x400\n\t"
        "s_add_u32 s81, s81, 32768\n\t"
        "buffer_load_dwordx4 v114, %[so], s81 offen lds\n\t"
        "s_add_u32 m0, m0, 0x400\n\t"
        "s_add_u32 s81, s81, 32768\n\t"
        "s_sub_u32 s82, s82, 1\n\t"
        "s_cmp_lg_u32 s82, 0\n\t"
        "s_cbranch_scc1 .Ld6%=\n\t"
        "s_waitcnt vmcnt(25)\n\t"
        "ds_read_b64_tr_b16 v[224:225], v115 offset:24576\n\t"
        "ds_read_b64_tr_b16 v[226:227], v115 offset:24832\n\t"
        "ds_read_b64_tr_b16 v[228:229], v115 offset:28672\n\t"
        "ds_read_b64_tr_b16 v[230:231], v115 offset:28928\n\t"
        "ds_read_b64_tr_b16 v[232:233], v116 offset:24576\n\t"
        "ds_read_b64_tr_b16 v[234:235], v116 offset:24832\n\t"
        "ds_read_b64_tr_b16 v[236:237], v116 offset:28672\n\t"
        "ds_read_b64_tr_b16 v[238:239], v116 offset:28928\n\t"
        "ds_read_b64_tr_b16 v[240:241], v117 offset:24576\n\t"
        "ds_read_b64_tr_b16 v[242:243], v117 offset:24832\n\t"
        "ds_read_b64_tr_b16 v[244:245], v117 offset:28672\n\t"
        "ds_read_b64_tr_b16 v[246:247], v117 offset:28928\n\t"
        "ds_read_b64_tr_b16 v[248:249], v118 offset:24576\n\t"
        "ds_read_b64_tr_b16 v[250:251], v118 offset:24832\n\t"
        "ds_read_b64_tr_b16 v[252:253], v118 offset:28672\n\t"
        "ds_read_b64_tr_b16 v[254:255], v118 offset:28928\n\t"
        "s_waitcnt lgkmcnt(0)\n\t"
        "s_add_u32 m0, %[m0b], 24576\n\t"
        "s_mov_b32 s81, 786432\n\t"
        "s_mov_b32 s82, 4\n\t"
        ".Ld7%=:\n\t"
        "buffer_load_dwordx4 v114, %[so], s81 offen lds\n\t"
        "s_add_u32 m0, m0, 0x400\n\t"
        "s_add_u32 s81, s81, 32768\n\t"
        "buffer_load_dwordx4 v114, %[so], s81 offen lds\n\t"
        "s_add_u32 m0, m0, 0x400\n\t"
        "s_add_u32 s81, s81, 32768\n\t"
        "s_sub_u32 s82, s82, 1\n\t"
        "s_cmp_lg_u32 s82, 0\n\t"
        "s_cbranch_scc1 .Ld7%=\n\t"
        "s_waitcnt vmcnt(24)\n\t"
        "ds_read_b64_tr_b16 a[0:1], v119 offset:0\n\t"
        "ds_read_b64_tr_b16 a[2:3], v119 offset:128\n\t"
        "ds_read_b64_tr_b16 a[4:5], v119 offset:4096\n\t"
        "ds_read_b64_tr_b16 a[6:7], v119 offset:4224\n\t"
        "ds_read_b64_tr_b16 a[8:9], v120 offset:0\n\t"
        "ds_read_b64_tr_b16 a[10:11], v120 offset:128\n\t"
        "ds_read_b64_tr_b16 a[12:13], v120 offset:4096\n\t"
        "ds_read_b64_tr_b16 a[14:15], v120 offset:4224\n\t"
        "ds_read_b64_tr_b16 a[16:17], v119 offset:512\n\t"
        "ds_read_b64_tr_b16 a[18:19], v119 offset:640\n\t"
        "ds_read_b64_tr_b16 a[20:21], v119 offset:4608\n\t"
        "ds_read_b64_tr_b16 a[22:23], v119 offset:4736\n\t"
        "ds_read_b64_tr_b16 a[24:25], v120 offset:512\n\t"
        "ds_read_b64_tr_b16 a[26:27], v120 offset:640\n\t"
        "ds_read_b64_tr_b16 a[28:29], v120 offset:4608\n\t"
        "ds_read_b64_tr_b16 a[30:31], v120 offset:4736\n\t"
        "s_waitcnt lgkmcnt(0)\n\t"
        "s_add_u32 m0, %[m0b], 0\n\t"
        "s_mov_b32 s81, 1048576\n\t"
        "s_mov_b32 s82, 4\n\t"
        ".Ld8%=:\n\t"
        "buffer_load_dwordx4 v114, %[so], s81 offen lds\n\t"
        "s_add_u32 m0, m0, 0x400\n\t"
        "s_add_u32 s81, s81, 32768\n\t"
        "buffer_load_dwordx4 v114, %[so], s81 offen lds\n\t"
        "s_add_u32 m0, m0, 0x400\n\t"
        "s_add_u32 s81, s81, 32768\n\t"
        "s_sub_u32 s82, s82, 1\n\t"
        "s_cmp_lg_u32 s82, 0\n\t"
        "s_cbranch_scc1 .Ld8%=\n\t"
        "s_waitcnt vmcnt(24)\n\t"
        "ds_read_b64_tr_b16 a[32:33], v119 offset:8192\n\t"
        "ds_read_b64_tr_b16 a[34:35], v119 offset:8320\n\t"
        "ds_read_b64_tr_b16 a[36:37], v119 offset:12288\n\t"
        "ds_read_b64_tr_b16 a[38:39], v119 offset:12416\n\t"
        "ds_read_b64_tr_b16 a[40:41], v120 offset:8192\n\t"
        "ds_read_b64_tr_b16 a[42:43], v120 offset:8320\n\t"
        "ds_read_b64_tr_b16 a[44:45], v120 offset:12288\n\t"
        "ds_read_b64_tr_b16 a[46:47], v120 offset:12416\n\t"
        "ds_read_b64_tr_b16 a[48:49], v119 offset:8704\n\t"
        "ds_read_b64_tr_b16 a[50:51], v119 offset:8832\n\t"
        "ds_read_b64_tr_b16 a[52:53], v119 offset:12800\n\t"
        "ds_read_b64_tr_b16 a[54:55], v119 offset:12928\n\t"
        "ds_read_b64_tr_b16 a[56:57], v120 offset:8704\n\t"
        "ds_read_b64_tr_b16 a[58:59], v120 offset:8832\n\t"
        "ds_read_b64_tr_b16 a[60:61], v120 offset:12800\n\t"
        "ds_read_b64_tr_b16 a[62:63], v120 offset:12928\n\t"
        "s_waitcnt lgkmcnt(0)\n\t"
        "s_add_u32 m0, %[m0b], 8192\n\t"
        "s_mov_b32 s81, 1310720\n\t"
        "s_mov_b32 s82, 4\n\t"
        ".Ld9%=:\n\t"
        "buffer_load_dwordx4 v114, %[so], s81 offen lds\n\t"
        "s_add_u32 m0, m0, 0x400\n\t"
        "s_add_u32 s81, s81, 32768\n\t"
        "buffer_load_dwordx4 v114, %[so], s81 offen lds\n\t"
        "s_add_u32 m0, m0, 0x400\n\t"
        "s_add_u32 s81, s81, 32768\n\t"
        "s_sub_u32 s82, s82, 1\n\t"
        "s_cmp_lg_u32 s82, 0\n\t"
        "s_cbranch_scc1 .Ld9%=\n\t"
        "s_waitcnt vmcnt(24)\n\t"
        "ds_read_b64_tr_b16 a[64:65], v119 offset:16384\n\t"
        "ds_read_b64_tr_b16 a[66:67], v119 offset:16512\n\t"
        "ds_read_b64_tr_b16 a[68:69], v119 offset:20480\n\t"
        "ds_read_b64_tr_b16 a[70:71], v119 offset:20608\n\t"
        "ds_read_b64_tr_b16 a[72:73], v120 offset:16384\n\t"
        "ds_read_b64_tr_b16 a[74:75], v120 offset:16512\n\t"
        "ds_read_b64_tr_b16 a[76:77], v120 offset:20480\n\t"
        "ds_read_b64_tr_b16 a[78:79], v120 offset:20608\n\t"
        "ds_read_b64_tr_b16 a[80:81], v119 offset:16896\n\t"
        "ds_read_b64_tr_b16 a[82:83], v119 offset:17024\n\t"
        "ds_read_b64_tr_b16 a[84:85], v119 offset:20992\n\t"
        "ds_read_b64_tr_b16 a[86:87], v119 offset:21120\n\t"
        "ds_read_b64_tr_b16 a[88:89], v120 offset:16896\n\t"
        "ds_read_b64_tr_b16 a[90:91], v120 offset:17024\n\t"
        "ds_read_b64_tr_b16 a[92:93], v120 offset:20992\n\t"
        "ds_read_b64_tr_b16 a[94:95], v120 offset:21120\n\t"
        "s_waitcnt lgkmcnt(0)\n\t"
        "s_add_u32 m0, %[m0b], 16384\n\t"
        "s_mov_b32 s81, 1572864\n\t"
        "s_mov_b32 s82, 4\n\t"
        ".Ld10%=:\n\t"
        "buffer_load_dwordx4 v114, %[so], s81 offen lds\n\t"
        "s_add_u32 m0, m0, 0x400\n\t"
        "s_add_u32 s81, s81, 32768\n\t"
        "buffer_load_dwordx4 v114, %[so], s81 offen lds\n\t"
        "s_add_u32 m0, m0, 0x400\n\t"
        "s_add_u32 s81, s81, 32768\n\t"
        "s_sub_u32 s82, s82, 1\n\t"
        "s_cmp_lg_u32 s82, 0\n\t"
        "s_cbranch_scc1 .Ld10%=\n\t"
        "s_waitcnt vmcnt(24)\n\t"
        "ds_read_b64_tr_b16 a[96:97], v119 offset:24576\n\t"
        "ds_read_b64_tr_b16 a[98:99], v119 offset:24704\n\t"
        "ds_read_b64_tr_b16 a[100:101], v119 offset:28672\n\t"
        "ds_read_b64_tr_b16 a[102:103], v119 offset:28800\n\t"
        "ds_read_b64_tr_b16 a[104:105], v120 offset:24576\n\t"
        "ds_read_b64_tr_b16 a[106:107], v120 offset:24704\n\t"
        "ds_read_b64_tr_b16 a[108:109], v120 offset:28672\n\t"
        "ds_read_b64_tr_b16 a[110:111], v120 offset:28800\n\t"
        "ds_read_b64_tr_b16 a[112:113], v119 offset:25088\n\t"
        "ds_read_b64_tr_b16 a[114:115], v119 offset:25216\n\t"
        "ds_read_b64_tr_b16 a[116:117], v119 offset:29184\n\t"
        "ds_read_b64_tr_b16 a[118:119], v119 offset:29312\n\t"
        "ds_read_b64_tr_b16 a[120:121], v120 offset:25088\n\t"
        "ds_read_b64_tr_b16 a[122:123], v120 offset:25216\n\t"
        "ds_read_b64_tr_b16 a[124:125], v120 offset:29184\n\t"
        "ds_read_b64_tr_b16 a[126:127], v120 offset:29312\n\t"
        "s_waitcnt lgkmcnt(0)\n\t"
        "s_add_u32 m0, %[m0b], 24576\n\t"
        "s_mov_b32 s81, 1835008\n\t"
        "s_mov_b32 s82, 4\n\t"
        ".Ld11%=:\n\t"
        "buffer_load_dwordx4 v114, %[so], s81 offen lds\n\t"
        "s_add_u32 m0, m0, 0x400\n\t"
        "s_add_u32 s81, s81, 32768\n\t"
        "buffer_load_dwordx4 v114, %[so], s81 offen lds\n\t"
        "s_add_u32 m0, m0, 0x400\n\t"
        "s_add_u32 s81, s81, 32768\n\t"
        "s_sub_u32 s82, s82, 1\n\t"
        "s_cmp_lg_u32 s82, 0\n\t"
        "s_cbranch_scc1 .Ld11%=\n\t"
        "s_waitcnt vmcnt(24)\n\t"
        "ds_read_b64_tr_b16 a[128:129], v119 offset:0\n\t"
        "ds_read_b64_tr_b16 a[130:131], v119 offset:128\n\t"
        "ds_read_b64_tr_b16 a[132:133], v119 offset:4096\n\t"
        "ds_read_b64_tr_b16 a[134:135], v119 offset:4224\n\t"
        "ds_read_b64_tr_b16 a[136:137], v120 offset:0\n\t"
        "ds_read_b64_tr_b16 a[138:139], v120 offset:128\n\t"
        "ds_read_b64_tr_b16 a[140:141], v120 offset:4096\n\t"
        "ds_read_b64_tr_b16 a[142:143], v120 offset:4224\n\t"
        "ds_read_b64_tr_b16 a[144:145], v119 offset:512\n\t"
        "ds_read_b64_tr_b16 a[146:147], v119 offset:640\n\t"
        "ds_read_b64_tr_b16 a[148:149], v119 offset:4608\n\t"
        "ds_read_b64_tr_b16 a[150:151], v119 offset:4736\n\t"
        "ds_read_b64_tr_b16 a[152:153], v120 offset:512\n\t"
        "ds_read_b64_tr_b16 a[154:155], v120 offset:640\n\t"
        "ds_read_b64_tr_b16 a[156:157], v120 offset:4608\n\t"
        "ds_read_b64_tr_b16 a[158:159], v120 offset:4736\n\t"
        "s_waitcnt lgkmcnt(0)\n\t"
        "s_waitcnt vmcnt(16)\n\t"
        "ds_read_b64_tr_b16 a[160:161], v119 offset:8192\n\t"
        "ds_read_b64_tr_b16 a[162:163], v119 offset:8320\n\t"
        "ds_read_b64_tr_b16 a[164:165], v119 offset:12288\n\t"
        "ds_read_b64_tr_b16 a[166:167], v119 offset:12416\n\t"
        "ds_read_b64_tr_b16 a[168:169], v120 offset:8192\n\t"
        "ds_read_b64_tr_b16 a[170:171], v120 offset:8320\n\t"
        "ds_read_b64_tr_b16 a[172:173], v120 offset:12288\n\t"
        "ds_read_b64_tr_b16 a[174:175], v120 offset:12416\n\t"
        "ds_read_b64_tr_b16 a[176:177], v119 offset:8704\n\t"
        "ds_read_b64_tr_b16 a[178:179], v119 offset:8832\n\t"
        "ds_read_b64_tr_b16 a[180:181], v119 offset:12800\n\t"
        "ds_read_b64_tr_b16 a[182:183], v119 offset:12928\n\t"
        "ds_read_b64_tr_b16 a[184:185], v120 offset:8704\n\t"
        "ds_read_b64_tr_b16 a[186:187], v120 offset:8832\n\t"
        "ds_read_b64_tr_b16 a[188:189], v120 offset:12800\n\t"
        "ds_read_b64_tr_b16 a[190:191], v120 offset:12928\n\t"
        "s_waitcnt lgkmcnt(0)\n\t"
        "s_waitcnt vmcnt(8)\n\t"
        "ds_read_b64_tr_b16 a[192:193], v119 offset:16384\n\t"
        "ds_read_b64_tr_b16 a[194:195], v119 offset:16512\n\t"
        "ds_read_b64_tr_b16 a[196:197], v119 offset:20480\n\t"
        "ds_read_b64_tr_b16 a[198:199], v119 offset:20608\n\t"
        "ds_read_b64_tr_b16 a[200:201], v120 offset:16384\n\t"
        "ds_read_b64_tr_b16 a[202:203], v120 offset:16512\n\t"
        "ds_read_b64_tr_b16 a[204:205], v120 offset:20480\n\t"
        "ds_read_b64_tr_b16 a[206:207], v120 offset:20608\n\t"
        "ds_read_b64_tr_b16 a[208:209], v119 offset:16896\n\t"
        "ds_read_b64_tr_b16 a[210:211], v119 offset:17024\n\t"
        "ds_read_b64_tr_b16 a[212:213], v119 offset:20992\n\t"
        "ds_read_b64_tr_b16 a[214:215], v119 offset:21120\n\t"
        "ds_read_b64_tr_b16 a[216:217], v120 offset:16896\n\t"
        "ds_read_b64_tr_b16 a[218:219], v120 offset:17024\n\t"
        "ds_read_b64_tr_b16 a[220:221], v120 offset:20992\n\t"
        "ds_read_b64_tr_b16 a[222:223], v120 offset:21120\n\t"
        "s_waitcnt lgkmcnt(0)\n\t"
        "s_waitcnt vmcnt(0)\n\t"
        "ds_read_b64_tr_b16 a[224:225], v119 offset:24576\n\t"
        "ds_read_b64_tr_b16 a[226:227], v119 offset:24704\n\t"
        "ds_read_b64_tr_b16 a[228:229], v119 offset:28672\n\t"
        "ds_read_b64_tr_b16 a[230:231], v119 offset:28800\n\t"
        "ds_read_b64_tr_b16 a[232:233], v120 offset:24576\n\t"
        "ds_read_b64_tr_b16 a[234:235], v120 offset:24704\n\t"
        "ds_read_b64_tr_b16 a[236:237], v120 offset:28672\n\t"
        "ds_read_b64_tr_b16 a[238:239], v120 offset:28800\n\t"
        "ds_read_b64_tr_b16 a[240:241], v119 offset:25088\n\t"
        "ds_read_b64_tr_b16 a[242:243], v119 offset:25216\n\t"
        "ds_read_b64_tr_b16 a[244:245], v119 offset:29184\n\t"
        "ds_read_b64_tr_b16 a[246:247], v119 offset:29312\n\t"
        "ds_read_b64_tr_b16 a[248:249], v120 offset:25088\n\t"
        "ds_read_b64_tr_b16 a[250:251], v120 offset:25216\n\t"
        "ds_read_b64_tr_b16 a[252:253], v120 offset:29184\n\t"
        "ds_read_b64_tr_b16 a[254:255], v120 offset:29312\n\t"
        "s_waitcnt lgkmcnt(0)"
        : "={a[0:31]}"(wa[0]), "={a[32:63]}"(wa[1]), "={a[64:95]}"(wa[2]), "={a[96:127]}"(wa[3]), "={a[128:159]}"(wa[4]), "={a[160:191]}"(wa[5]), "={a[192:223]}"(wa[6]), "={a[224:255]}"(wa[7]), "={v[128:159]}"(wg[0]), "={v[160:191]}"(wg[1]), "={v[192:223]}"(wg[2]), "={v[224:255]}"(wg[3])
        : [sv] "s"(sv), [so] "s"(so), [wsd] "s"(wsd), [m0b] "s"(m0b), "{v[112:127]}"(pa)
        : "memory", "scc", "m0", "s80", "s81", "s82");
        }
        asm volatile("" : "+v"(cpf), "+v"(m0v));
        __syncthreads();
        tid = otid(); l = tid & 63; n16 = l & 15; g = l >> 4;
        unsigned nm; if (!votes(nm)) return false;
        const unsigned nW = (nm & 1u) << 13, nA = nW + x * 32768, nS = nW + 6 * 16384 + x * 1024 - w * 1024, nC = 32 * 16384 + ((nm & 2u) << 12);
        const unsigned b0 = (64 + (h >> 2)) * 16384 + ((nm & 4u) << 11) + (h & 3) * 2048, b1 = b0 + 8 * 16384;
        const unsigned pob = w == 0 ? nA + (i32 >> 4) * 16384 + (i32 & 15) * 512 : w == 1 ? nC + (G * 4 + (cb >> 3)) * 16384 + (cb & 7) * 1024 - 1024 : (w == 2 && i32 == 0) ? nS : OOB;
        const unsigned aw = w < 3 ? (unsigned)(w * 32768 + (cb >> 4) * 16384 + ((cb >> 1) & 7) * 1024 + ((cb & 1) * 2 + (l >> 5)) * 256 + G * 128 + (l & 31) * 4) : OOB;
        v16u pc = {(unsigned)tid * 16, (n16 & 2) ? 0xEEEEu : 0x4444u, (unsigned)((vi & 1) * 1024 + w * 256 + n16 * 16 + g * 4), (unsigned)((G * 4 + w) * 16384 + l * 16), aw,
                   tid < 3 ? (unsigned)(6 * 16384 + l * 1024 + (32 + vi) * 8) : OOB, tid == 192 ? pr_a : OOB, (unsigned)(8192 + w * 512 + (g & 1) * 256 + (g >> 1) * 64 + n16 * 4),
                   (unsigned)((((l >> 2) & 1) + ((l >> 3) & 2)) * 128 + (l >> 5) * 64 + (l & 8) * 4 + (l & 3) * 8), (unsigned)(2048 + (64 * (vi & 1) + 16 * w + n16) * 16 + g * 4),
                   (unsigned)(w * 2048 + l * 8), (unsigned)(((w + 1) & 3) * 2048 + 512 + l * 8), (unsigned)(((w + 2) & 3) * 2048 + 1024 + l * 8), (unsigned)(((w + 3) & 3) * 2048 + 1536 + l * 8), 0u, g ? OOB : (unsigned)((vi & 1) * 1024 + w * 256 + n16 * 16)};
        m0v = min(m0v, 0xFFFFFFFEu);
        st4(rh, aw + nW, m0v);
        if (tid < 2) ((lu32*)(size_t)L_FL)[tid] = ~0u;
        __syncthreads();
    asm volatile(
        "rec_v_code:\n\t"
        "s_mov_b32 s36, 0xf0000000\n\t"
        "s_mov_b32 s38, 0xff00ff00\n\t"
        "s_mov_b32 s39, 0xff00ff00\n\t"
        "s_mov_b32 s40, 0\n\t"
        "s_mov_b32 s37, 0\n\t"
        "s_brev_b32 s54, 1\n\t"
        "s_mov_b32 s46, %c[iD1]\n\t"
        "s_mov_b32 s45, %c[iD1]\n\t"
        "s_mov_b32 s47, %c[iD2]\n\t"
        "s_mov_b32 s48, 1\n\t"
        "s_mov_b32 s50, %c[LC]\n\t"
        "s_mov_b32 s51, %c[LA]\n\t"
        "s_mov_b32 s52, %c[E1]\n\t"
        "s_mov_b32 s53, %c[E2]\n\t"
        "s_mov_b32 s43, 0\n\t"
        "s_mov_b32 s61, %[b0]\n\t"
        "s_mov_b32 s62, %[b1]\n\t"
        "s_mov_b32 s75, 31744\n\t"
        "s_and_b32 s80, s40, 3\n\t"
        "s_lshl_b32 s80, s80, 17\n\t"
        "s_add_u32 s57, s80, %[nA]\n\t"
        "s_add_u32 s58, s57, 0x1000\n\t"
        "s_add_u32 s59, s57, 0x4000\n\t"
        "s_add_u32 s60, s57, 0x5000\n\t"
        "v_mov_b32_e32 v92, -1\n\t"
        "v_mov_b32_e32 v93, -1\n\t"
        "v_mov_b32_e32 v94, -1\n\t"
        "v_mov_b32_e32 v95, -1\n\t"
        "v_mov_b32_e32 v48, 0\n\t"
        "v_mov_b32_e32 v49, 0\n\t"
        "v_mov_b32_e32 v50, 0\n\t"
        "v_mov_b32_e32 v51, 0\n\t"
        "v_mov_b32_e32 v52, 0\n\t"
        "v_mov_b32_e32 v53, 0\n\t"
        "v_mov_b32_e32 v54, 0\n\t"
        "v_mov_b32_e32 v55, 0\n\t"
        "v_mov_b32_e32 v56, 0\n\t"
        "v_mov_b32_e32 v57, 0\n\t"
        "v_mov_b32_e32 v58, 0\n\t"
        "v_mov_b32_e32 v59, 0\n\t"
        "v_mov_b32_e32 v60, 0\n\t"
        "v_mov_b32_e32 v61, 0\n\t"
        "v_mov_b32_e32 v62, 0\n\t"
        "v_mov_b32_e32 v63, 0\n\t"
        "s_memrealtime s[76:77]\n\t"
        "s_mov_b32 s67, %[nW]\n\t"
        "v_mov_b32_e32 v86, 64\n\t"
        "s_mov_b32 s61, %[b1]\n\t"
        "s_mov_b32 s62, %[b0]\n\t"
        "s_sub_u32 s75, s75, 16\n\t"
        "s_waitcnt lgkmcnt(0)\n\t"
        "s_mov_b32 s41, s76\n\t"
        "s_mov_b32 s69, s76\n\t"
        ".p2align 6\n\t"
        ".Lloop%=:\n\t"
        "s_mov_b32 s97, 0\n\t"
        "s_movk_i32 s96, 0x1000\n\t"
        ".Lwa%=:\n\t"
        "s_memrealtime s[98:99]\n\t"
        "s_addk_i32 s96, -1\n\t"
        "s_cmp_lt_i32 s96, 0\n\t"
        "s_cbranch_scc1 .Lta%=\n\t"
        "s_waitcnt lgkmcnt(0)\n\t"
        "s_sub_i32 s98, s98, s69\n\t"
        "s_cmp_lt_i32 s98, %c[iFT]\n\t"
        "s_cbranch_scc1 .Lwa%=\n\t"
        "s_cmp_ge_i32 s98, 0\n\t"
        "s_cbranch_scc1 .Lxa%=\n\t"
        ".Lqa%=:\n\t"
        "s_nop %c[iFN]\n\t"
        "s_addk_i32 s98, 1\n\t"
        "s_cmp_lt_i32 s98, 0\n\t"
        "s_cbranch_scc1 .Lqa%=\n\t"
        ".Lxa%=:\n\t"
        "buffer_load_dwordx4 v[0:3], v112, %[srd], s57 offen sc1\n\t"
        "s_nop 7\n\t"
        "buffer_load_dwordx4 v[4:7], v112, %[srd], s58 offen sc1\n\t"
        "s_nop 7\n\t"
        "buffer_load_dwordx4 v[8:11], v112, %[srd], s59 offen sc1\n\t"
        "s_nop 7\n\t"
        "buffer_load_dwordx4 v[12:15], v112, %[srd], s60 offen sc1\n\t"
        "v_mov_b32_e32 v80, 30720\n\t"
        "ds_read_b64 v[80:81], v80\n\t"
        "s_waitcnt lgkmcnt(0)\n\t"
        "v_readfirstlane_b32 s80, v80\n\t"
        "v_readfirstlane_b32 s81, v81\n\t"
        "s_cmp_eq_u32 s80, s40\n\t"
        "s_cselect_b32 s84, 1, 0\n\t"
        "s_cmp_eq_u32 s81, s40\n\t"
        "s_cselect_b32 s81, 4, 0\n\t"
        "s_or_b32 s84, s84, s81\n\t"
        "v_mov_b32_e32 v106, s76\n\t"
        "v_mov_b32_e32 v107, s84\n\t"
        "buffer_store_dwordx2 v[106:107], v117, %[srd], s67 offen sc1\n\t"
        "s_and_b32 s80, s40, 3\n\t"
        "s_lshl_b32 s80, s80, 17\n\t"
        "s_add_u32 s63, s80, %[nC]\n\t"
        "s_add_u32 s66, s80, %[nS]\n\t"
        "s_add_u32 s81, s80, 0x20000\n\t"
        "s_and_b32 s81, s81, 0x60000\n\t"
        "s_add_u32 s67, s81, %[nW]\n\t"
        "s_cmp_eq_u32 %[w], 2\n\t"
        "s_cselect_b32 s81, s80, s81\n\t"
        "s_xor_b32 s81, s81, 0x40000\n\t"
        "s_add_u32 s68, s81, %[pob]\n\t"
        "s_lshl_b32 s72, s40, 17\n\t"
        "s_add_u32 s72, s72, %[kvo]\n\t"
        "s_mov_b32 s80, s61\n\t"
        "s_mov_b32 s61, s62\n\t"
        "s_mov_b32 s62, s80\n\t"
        ".Lgo%=:\n\t"
        "s_waitcnt vmcnt(4)\n\t"
        "v_smfmac_f32_16x16x64_bf16 v[52:55], v[0:3], v[136:143], v113\n\t"
        "v_smfmac_f32_16x16x64_bf16 v[56:59], v[0:3], v[144:151], v113\n\t"
        "v_smfmac_f32_16x16x64_bf16 v[60:63], v[0:3], v[152:159], v113\n\t"
        "v_smfmac_f32_16x16x64_bf16 v[48:51], v[0:3], v[128:135], v113\n\t"
        "s_waitcnt vmcnt(3)\n\t"
        "v_smfmac_f32_16x16x64_bf16 v[52:55], v[4:7], v[168:175], v113\n\t"
        "v_smfmac_f32_16x16x64_bf16 v[56:59], v[4:7], v[176:183], v113\n\t"
        "v_smfmac_f32_16x16x64_bf16 v[60:63], v[4:7], v[184:191], v113\n\t"
        "v_smfmac_f32_16x16x64_bf16 v[48:51], v[4:7], v[160:167], v113\n\t"
        "s_waitcnt vmcnt(2)\n\t"
        "v_smfmac_f32_16x16x64_bf16 v[52:55], v[8:11], v[200:207], v113\n\t"
        "v_smfmac_f32_16x16x64_bf16 v[56:59], v[8:11], v[208:215], v113\n\t"
        "v_smfmac_f32_16x16x64_bf16 v[60:63], v[8:11], v[216:223], v113\n\t"
        "v_smfmac_f32_16x16x64_bf16 v[48:51], v[8:11], v[192:199], v113\n\t"
        "s_waitcnt vmcnt(1)\n\t"
        "v_smfmac_f32_16x16x64_bf16 v[52:55], v[12:15], v[232:239], v113\n\t"
        "v_smfmac_f32_16x16x64_bf16 v[56:59], v[12:15], v[240:247], v113\n\t"
        "v_smfmac_f32_16x16x64_bf16 v[60:63], v[12:15], v[248:255], v113\n\t"
        "v_smfmac_f32_16x16x64_bf16 v[48:51], v[12:15], v[224:231], v113\n\t"
        "s_nop 1\n\t"
        "v_mov_b32_e32 v104, v104\n\t"
        "s_nop 1\n\t"
        "v_add_f32_e32 v98, v52, v54\n\t"
        "v_add_f32_e32 v99, v53, v55\n\t"
        "ds_write_b64 v123, v[98:99]\n\t"
        "v_add_f32_e32 v100, v56, v58\n\t"
        "v_add_f32_e32 v101, v57, v59\n\t"
        "ds_write_b64 v124, v[100:101]\n\t"
        "v_add_f32_e32 v102, v60, v62\n\t"
        "v_add_f32_e32 v103, v61, v63\n\t"
        "ds_write_b64 v125, v[102:103]\n\t"
        "v_pk_add_f32 v[96:97], v[48:49], v[50:51]\n\t"
        "v_cmp_u_f32_e32 vcc, v96, v97\n\t"
        "s_nop 2\n\t"
        "v_mov_b32_e32 v104, v104\n\t"
        "s_cbranch_vccnz .Lfa%=\n\t"
        ".Lra%=:\n\t"
        "v_mov_b32_e32 v104, v104\n\t"
        "s_nop 2\n\t"
        "v_mov_b32_e32 v104, v104\n\t"
        "s_nop 2\n\t"
        "v_mov_b32_e32 v104, v104\n\t"
        "s_nop 2\n\t"
        "v_mov_b32_e32 v104, v104\n\t"
        "s_waitcnt lgkmcnt(0)\n\t"
        "s_barrier\n\t"
        "v_mov_b32_e32 v104, v104\n\t"
        "ds_read_b64 v[98:99], v122 offset:512\n\t"
        "ds_read_b64 v[100:101], v122 offset:1024\n\t"
        "ds_read_b64 v[102:103], v122 offset:1536\n\t"
        "v_mov_b32_e32 v104, v104\n\t"
        "s_nop 2\n\t"
        "v_mov_b32_e32 v104, v104\n\t"
        "s_nop 2\n\t"
        "v_mov_b32_e32 v104, v104\n\t"
        "s_waitcnt lgkmcnt(2)\n\t"
        "v_pk_add_f32 v[96:97], v[96:97], v[98:99]\n\t"
        "s_waitcnt lgkmcnt(0)\n\t"
        "v_pk_add_f32 v[100:101], v[100:101], v[102:103]\n\t"
        "v_pk_add_f32 v[96:97], v[96:97], v[100:101]\n\t"
        "v_cvt_pk_bf16_f32 v80, v96, v97\n\t"
        "v_min_u32_e32 v80, -2, v80\n\t"
        "buffer_store_dword v80, v114, %[srd], s61 offen sc1\n\t"
        "s_cmp_eq_u32 s40, 63\n\t"
        "s_cbranch_scc1 .Lend%=\n\t"
        "v_mov_b32_e32 v64, 0\n\t"
        "v_mov_b32_e32 v65, 0\n\t"
        "v_mov_b32_e32 v66, 0\n\t"
        "v_mov_b32_e32 v67, 0\n\t"
        "v_mov_b32_e32 v68, 0\n\t"
        "v_mov_b32_e32 v69, 0\n\t"
        "v_mov_b32_e32 v70, 0\n\t"
        "v_mov_b32_e32 v71, 0\n\t"
        "v_mov_b32_e32 v72, 0\n\t"
        "v_mov_b32_e32 v73, 0\n\t"
        "v_mov_b32_e32 v74, 0\n\t"
        "v_mov_b32_e32 v75, 0\n\t"
        "v_mov_b32_e32 v76, 0\n\t"
        "v_mov_b32_e32 v77, 0\n\t"
        "v_mov_b32_e32 v78, 0\n\t"
        "v_mov_b32_e32 v79, 0\n\t"
        "s_add_u32 s64, s63, 0x1000\n\t"
        "s_sleep %c[VSLP]\n\t"
        "buffer_load_dwordx4 v[88:91], v112, %[srd], s66 offen sc1\n\t"
        "s_cmp_eq_u32 %[w], 0\n\t"
        "s_cselect_b32 exec_hi, 0, -1\n\t"
        "buffer_store_dwordx4 v[92:95], v112, %[srd], s68 offen sc1\n\t"
        "s_mov_b32 exec_hi, -1\n\t"
        "s_waitcnt lgkmcnt(0)\n\t"
        "v_mov_b32_e32 v48, 0\n\t"
        "v_mov_b32_e32 v49, 0\n\t"
        "v_mov_b32_e32 v50, 0\n\t"
        "v_mov_b32_e32 v51, 0\n\t"
        "v_mov_b32_e32 v52, 0\n\t"
        "v_mov_b32_e32 v53, 0\n\t"
        "v_mov_b32_e32 v54, 0\n\t"
        "v_mov_b32_e32 v55, 0\n\t"
        "v_mov_b32_e32 v56, 0\n\t"
        "v_mov_b32_e32 v57, 0\n\t"
        "v_mov_b32_e32 v58, 0\n\t"
        "v_mov_b32_e32 v59, 0\n\t"
        "v_mov_b32_e32 v60, 0\n\t"
        "v_mov_b32_e32 v61, 0\n\t"
        "v_mov_b32_e32 v62, 0\n\t"
        "v_mov_b32_e32 v63, 0\n\t"
        "s_add_u32 s81, s40, 1\n\t"
        "s_and_b32 s80, s81, 3\n\t"
        "s_lshl_b32 s80, s80, 17\n\t"
        "s_add_u32 s57, s80, %[nA]\n\t"
        "s_add_u32 s58, s57, 0x1000\n\t"
        "s_add_u32 s59, s57, 0x4000\n\t"
        "s_add_u32 s60, s57, 0x5000\n\t"
        "s_waitcnt vmcnt(1)\n\t"
        "s_mov_b32 s97, 0\n\t"
        ".Lsr%=:\n\t"
        "s_mov_b32 exec_hi, 0xffff\n\t"
        "v_subrev_u32_e32 v4, s41, v88\n\t"
        "v_subrev_u32_e32 v5, s41, v90\n\t"
        "v_max_i32_e32 v4, v4, v5\n\t"
        "v_max_u32_e32 v5, v89, v91\n\t"
        "v_cmp_eq_u32_e32 vcc, -1, v5\n\t"
        "v_max_i32_dpp v4, v4, v4 quad_perm:[1,0,3,2] row_mask:0xf bank_mask:0xf\n\t"
        "v_or_b32_e32 v6, v89, v91\n\t"
        "s_nop 0\n\t"
        "v_max_i32_dpp v4, v4, v4 quad_perm:[2,3,0,1] row_mask:0xf bank_mask:0xf\n\t"
        "s_nop 1\n\t"
        "v_max_i32_dpp v4, v4, v4 row_ror:4 row_mask:0xf bank_mask:0xf\n\t"
        "s_nop 1\n\t"
        "v_max_i32_dpp v4, v4, v4 row_ror:8 row_mask:0xf bank_mask:0xf\n\t"
        "s_cbranch_vccnz .Lsf%=\n\t"
        "v_readlane_b32 s85, v4, 0\n\t"
        "v_readlane_b32 s81, v4, 16\n\t"
        "v_readlane_b32 s56, v4, 32\n\t"
        "v_and_b32_e32 v5, 1, v6\n\t"
        "v_cmp_ne_u32_e64 s[86:87], 0, v5\n\t"
        "v_and_b32_e32 v5, 2, v6\n\t"
        "v_cmp_ne_u32_e64 s[88:89], 0, v5\n\t"
        "v_and_b32_e32 v5, 4, v6\n\t"
        "v_cmp_ne_u32_e64 s[90:91], 0, v5\n\t"
        "s_max_i32 s56, s56, s81\n\t"
        "s_add_u32 s92, s85, s41\n\t"
        "s_sub_u32 s92, s92, s43\n\t"
        "s_add_i32 s93, s56, s47\n\t"
        "s_mov_b32 s44, s46\n\t"
        "s_mov_b32 s46, s45\n\t"
        "s_mov_b32 s94, s48\n\t"
        "s_mov_b32 s43, s41\n\t"
        "s_cmp_gt_i32 s56, s54\n\t"
        "s_cbranch_scc1 .Lmv%=\n\t"
        "s_mov_b32 s37, 0\n\t"
        "s_mov_b32 s48, 0\n\t"
        "s_mov_b32 s95, 0\n\t"
        ".Lsd%=:\n\t"
        "s_mov_b32 exec_hi, -1\n\t"
        "s_add_u32 s42, s43, s46\n\t"
        "s_mov_b32 s97, 0\n\t"
        "s_movk_i32 s96, 0x1000\n\t"
        ".Lwc%=:\n\t"
        "s_memrealtime s[98:99]\n\t"
        "s_addk_i32 s96, -1\n\t"
        "s_cmp_lt_i32 s96, 0\n\t"
        "s_cbranch_scc1 .Ltc%=\n\t"
        "s_waitcnt lgkmcnt(0)\n\t"
        "s_sub_i32 s98, s98, s42\n\t"
        "s_cmp_lt_i32 s98, %c[iFT]\n\t"
        "s_cbranch_scc1 .Lwc%=\n\t"
        "s_cmp_ge_i32 s98, 0\n\t"
        "s_cbranch_scc1 .Lxc%=\n\t"
        ".Lqc%=:\n\t"
        "s_nop %c[iFN]\n\t"
        "s_addk_i32 s98, 1\n\t"
        "s_cmp_lt_i32 s98, 0\n\t"
        "s_cbranch_scc1 .Lqc%=\n\t"
        ".Lxc%=:\n\t"
        "buffer_load_dwordx4 v[16:19], v115, %[srd], s63 offen offset:0 sc1\n\t"
        "s_cmp_ge_u32 s40, %c[iCT0]\n\t"
        "s_cselect_b32 s85, 1, 0\n\t"
        "s_cmp_lg_u64 s[86:87], 0\n\t"
        "s_cselect_b32 s86, s85, 0\n\t"
        "s_cmp_lg_u64 s[88:89], 0\n\t"
        "s_cselect_b32 s87, s85, 0\n\t"
        "s_cmp_lg_u64 s[90:91], 0\n\t"
        "s_cselect_b32 s88, s85, 0\n\t"
        "buffer_load_dwordx4 v[20:23], v115, %[srd], s63 offen offset:1024 sc1\n\t"
        "s_or_b32 s80, s94, s86\n\t"
        "s_or_b32 s80, s80, s87\n\t"
        "s_andn2_b32 s90, s85, s80\n\t"
        "s_cmp_le_i32 s92, s44\n\t"
        "s_cselect_b32 s80, s85, 0\n\t"
        "s_andn2_b32 s91, s80, s88\n\t"
        "s_and_b32 s80, s40, %c[LDEC]\n\t"
        "s_cselect_b32 s89, 0, 1\n\t"
        "buffer_load_dwordx4 v[24:27], v115, %[srd], s63 offen offset:2048 sc1\n\t"
        "s_mov_b32 s80, s88\n\t"
        "s_add_i32 s81, s92, s50\n\t"
        "s_sub_i32 s81, s81, 4\n\t"
        "s_cmp_le_i32 s81, s44\n\t"
        "s_cselect_b32 s80, s80, 0\n\t"
        "s_cmp_lt_i32 s50, %c[LC24]\n\t"
        "s_cselect_b32 s80, s80, 0\n\t"
        "s_lshl2_add_u32 s50, s80, s50\n\t"
        "buffer_load_dwordx4 v[28:31], v115, %[srd], s63 offen offset:3072 sc1\n\t"
        "s_sub_i32 s50, s50, s89\n\t"
        "s_max_i32 s50, s50, %c[LC]\n\t"
        "s_lshl_b32 s80, s92, 1\n\t"
        "s_sub_i32 s81, s80, s52\n\t"
        "s_ashr_i32 s82, s81, %c[UPSH]\n\t"
        "s_max_i32 s82, s82, %c[TRUP]\n\t"
        "s_add_i32 s82, s82, s52\n\t"
        "s_min_i32 s82, s82, s80\n\t"
        "buffer_load_dwordx4 v[32:35], v115, %[srd], s64 offen offset:0 sc1\n\t"
        "s_ashr_i32 s83, s81, 1\n\t"
        "s_min_i32 s83, s83, -%c[TRDN]\n\t"
        "s_add_i32 s83, s83, s52\n\t"
        "s_max_i32 s83, s83, s80\n\t"
        "s_cmp_gt_i32 s81, 0\n\t"
        "s_cselect_b32 s82, s82, s83\n\t"
        "s_cmp_lg_u32 s90, 0\n\t"
        "s_cselect_b32 s52, s82, s52\n\t"
        "buffer_load_dwordx4 v[36:39], v115, %[srd], s64 offen offset:1024 sc1\n\t"
        "s_add_i32 s80, s52, 1\n\t"
        "s_ashr_i32 s80, s80, 1\n\t"
        "s_add_i32 s45, s80, s50\n\t"
        "s_andn2_b32 s80, s86, s94\n\t"
        "s_cmp_lt_i32 s51, %c[LA24]\n\t"
        "s_cselect_b32 s80, s80, 0\n\t"
        "s_lshl2_add_u32 s51, s80, s51\n\t"
        "s_sub_i32 s51, s51, s89\n\t"
        "buffer_load_dwordx4 v[40:43], v115, %[srd], s64 offen offset:2048 sc1\n\t"
        "s_max_i32 s51, s51, %c[LA]\n\t"
        "s_lshl_b32 s80, s93, 1\n\t"
        "s_sub_i32 s81, s80, s53\n\t"
        "s_ashr_i32 s82, s81, %c[UPSH]\n\t"
        "s_max_i32 s82, s82, %c[TRUP]\n\t"
        "s_add_i32 s82, s82, s53\n\t"
        "s_min_i32 s82, s82, s80\n\t"
        "s_ashr_i32 s83, s81, 1\n\t"
        "buffer_load_dwordx4 v[44:47], v115, %[srd], s64 offen offset:3072 sc1\n\t"
        "s_min_i32 s83, s83, -%c[TRDN]\n\t"
        "s_add_i32 s83, s83, s53\n\t"
        "s_max_i32 s83, s83, s80\n\t"
        "s_cmp_gt_i32 s81, 0\n\t"
        "s_cselect_b32 s82, s82, s83\n\t"
        "s_cmp_lg_u32 s91, 0\n\t"
        "s_cselect_b32 s53, s82, s53\n\t"
        "s_add_i32 s80, s53, 1\n\t"
        "s_ashr_i32 s80, s80, 1\n\t"
        "s_add_i32 s47, s80, s51\n\t"
        "s_add_u32 s81, s47, %c[AUP]\n\t"
        "s_min_i32 s80, s95, s81\n\t"
        "s_max_i32 s47, s47, s80\n\t"
        "s_add_u32 s41, s42, s47\n\t"
        ".Lrc%=:\n\t"
        "s_waitcnt vmcnt(7)\n\t"
        "v_smfmac_f32_16x16x64_bf16 v[64:67], v[16:19], a[0:7], v113\n\t"
        "v_smfmac_f32_16x16x64_bf16 v[68:71], v[16:19], a[8:15], v113\n\t"
        "v_smfmac_f32_16x16x64_bf16 v[72:75], v[16:19], a[16:23], v113\n\t"
        "v_smfmac_f32_16x16x64_bf16 v[76:79], v[16:19], a[24:31], v113\n\t"
        "s_waitcnt vmcnt(6)\n\t"
        "v_smfmac_f32_16x16x64_bf16 v[64:67], v[20:23], a[32:39], v113\n\t"
        "v_smfmac_f32_16x16x64_bf16 v[68:71], v[20:23], a[40:47], v113\n\t"
        "v_smfmac_f32_16x16x64_bf16 v[72:75], v[20:23], a[48:55], v113\n\t"
        "v_smfmac_f32_16x16x64_bf16 v[76:79], v[20:23], a[56:63], v113\n\t"
        "s_waitcnt vmcnt(5)\n\t"
        "v_smfmac_f32_16x16x64_bf16 v[64:67], v[24:27], a[64:71], v113\n\t"
        "v_smfmac_f32_16x16x64_bf16 v[68:71], v[24:27], a[72:79], v113\n\t"
        "v_smfmac_f32_16x16x64_bf16 v[72:75], v[24:27], a[80:87], v113\n\t"
        "v_smfmac_f32_16x16x64_bf16 v[76:79], v[24:27], a[88:95], v113\n\t"
        "s_waitcnt vmcnt(4)\n\t"
        "v_smfmac_f32_16x16x64_bf16 v[64:67], v[28:31], a[96:103], v113\n\t"
        "v_smfmac_f32_16x16x64_bf16 v[68:71], v[28:31], a[104:111], v113\n\t"
        "v_smfmac_f32_16x16x64_bf16 v[72:75], v[28:31], a[112:119], v113\n\t"
        "v_smfmac_f32_16x16x64_bf16 v[76:79], v[28:31], a[120:127], v113\n\t"
        "s_waitcnt vmcnt(3)\n\t"
        "v_smfmac_f32_16x16x64_bf16 v[64:67], v[32:35], a[128:135], v113\n\t"
        "v_smfmac_f32_16x16x64_bf16 v[68:71], v[32:35], a[136:143], v113\n\t"
        "v_smfmac_f32_16x16x64_bf16 v[72:75], v[32:35], a[144:151], v113\n\t"
        "v_smfmac_f32_16x16x64_bf16 v[76:79], v[32:35], a[152:159], v113\n\t"
        "s_waitcnt vmcnt(2)\n\t"
        "v_smfmac_f32_16x16x64_bf16 v[64:67], v[36:39], a[160:167], v113\n\t"
        "v_smfmac_f32_16x16x64_bf16 v[68:71], v[36:39], a[168:175], v113\n\t"
        "v_smfmac_f32_16x16x64_bf16 v[72:75], v[36:39], a[176:183], v113\n\t"
        "v_smfmac_f32_16x16x64_bf16 v[76:79], v[36:39], a[184:191], v113\n\t"
        "s_waitcnt vmcnt(1)\n\t"
        "v_smfmac_f32_16x16x64_bf16 v[64:67], v[40:43], a[192:199], v113\n\t"
        "v_smfmac_f32_16x16x64_bf16 v[68:71], v[40:43], a[200:207], v113\n\t"
        "v_smfmac_f32_16x16x64_bf16 v[72:75], v[40:43], a[208:215], v113\n\t"
        "v_smfmac_f32_16x16x64_bf16 v[76:79], v[40:43], a[216:223], v113\n\t"
        "s_waitcnt vmcnt(0)\n\t"
        "v_smfmac_f32_16x16x64_bf16 v[64:67], v[44:47], a[224:231], v113\n\t"
        "s_nop 2\n\t"
        "v_smfmac_f32_16x16x64_bf16 v[68:71], v[44:47], a[232:239], v113\n\t"
        "s_cmp_gt_u32 %[w], 2\n\t"
        "s_nop 1\n\t"
        "v_smfmac_f32_16x16x64_bf16 v[72:75], v[44:47], a[240:247], v113\n\t"
        "v_add_f32_e32 v80, v64, v66\n\t"
        "s_nop 0\n\t"
        "v_smfmac_f32_16x16x64_bf16 v[76:79], v[44:47], a[248:255], v113\n\t"
        "v_add_f32_e32 v81, v65, v67\n\t"
        "v_add_f32_e32 v82, v68, v70\n\t"
        "v_add_f32_e32 v83, v69, v71\n\t"
        "s_mov_b32 exec_lo, 0\n\t"
        "s_nop 0\n\t"
        "v_add_f32_e32 v80, v72, v74\n\t"
        "v_add_f32_e32 v81, v73, v75\n\t"
        "s_nop 0\n\t"
        "v_pk_add_f32 v[82:83], v[76:77], v[78:79]\n\t"
        "s_mov_b32 exec_lo, -1\n\t"
        "s_nop 0\n\t"
        "v_permlane32_swap_b32_e32 v80, v82\n\t"
        "v_add_f32_e32 v80, v80, v82\n\t"
        "ds_write_b32 v119, v80\n\t"
        "v_permlane32_swap_b32_e32 v81, v83\n\t"
        "v_add_f32_e32 v81, v81, v83\n\t"
        "ds_write_b32 v119, v81 offset:128\n\t"
        "v_cmp_u_f32_e32 vcc, v80, v81\n\t"
        "s_nop 2\n\t"
        "v_mov_b32_e32 v104, v104\n\t"
        "s_cbranch_vccnz .Lfc%=\n\t"
        ".Lrd%=:\n\t"
        "v_mov_b32_e32 v104, v104\n\t"
        "s_cbranch_scc1 .Lno%=\n\t"
        "v_mov_b32_e32 v104, v104\n\t"
        "s_nop 2\n\t"
        "v_mov_b32_e32 v104, v104\n\t"
        "s_waitcnt lgkmcnt(0)\n\t"
        "s_barrier\n\t"
        "v_mov_b32_e32 v104, v104\n\t"
        "ds_read_b64 v[0:1], v120 offset:8192\n\t"
        "ds_read_b64 v[2:3], v120 offset:8704\n\t"
        "ds_read_b64 v[4:5], v120 offset:9216\n\t"
        "ds_read_b64 v[6:7], v120 offset:9728\n\t"
        "v_mov_b32_e32 v104, v104\n\t"
        "s_nop 2\n\t"
        "v_mov_b32_e32 v104, v104\n\t"
        "s_nop 2\n\t"
        "v_mov_b32_e32 v104, v104\n\t"
        "s_waitcnt lgkmcnt(2)\n\t"
        "v_pk_add_f32 v[0:1], v[0:1], v[2:3]\n\t"
        "s_waitcnt lgkmcnt(0)\n\t"
        "v_pk_add_f32 v[4:5], v[4:5], v[6:7]\n\t"
        "v_pk_add_f32 v[0:1], v[0:1], v[4:5]\n\t"
        "v_cvt_pk_bf16_f32 v0, v0, v1\n\t"
        "v_min_u32_e32 v0, -2, v0\n\t"
        "buffer_store_dword v0, v116, %[srd], s67 offen sc1\n\t"
        ".Lnp%=:\n\t"
        "s_memrealtime s[76:77]\n\t"
        "s_add_u32 s40, s40, 1\n\t"
        "s_waitcnt lgkmcnt(0)\n\t"
        "s_add_u32 s81, s76, %c[iVD]\n\t"
        "s_sub_u32 s80, s76, s41\n\t"
        "s_cmp_gt_i32 s80, %c[iTOL]\n\t"
        "s_cselect_b32 s69, s81, s41\n\t"
        "s_branch .Lloop%=\n\t"
        ".Lmv%=:\n\t"
        "s_mov_b32 s48, 1\n\t"
        "s_cmp_eq_u32 s54, %c[iTOL]\n\t"
        "s_cbranch_scc1 .Lm1%=\n\t"
        "s_max_i32 s80, s85, s56\n\t"
        "s_add_u32 s43, s41, s80\n\t"
        "s_add_u32 s43, s43, %c[iPEN0]\n\t"
        "s_mov_b32 s54, %c[iTOL]\n\t"
        "s_add_u32 s46, s46, %c[iX0C]\n\t"
        "s_add_u32 s95, s47, %c[iX0A]\n\t"
        "v_and_b32_e32 v4, 3, v88\n\t"
        "v_and_b32_e32 v5, 3, v90\n\t"
        "v_lshlrev_b32_e64 v4, v4, 1\n\t"
        "v_lshlrev_b32_e64 v5, v5, 1\n\t"
        "v_or_b32_e32 v4, v4, v5\n\t"
        "s_mov_b32 s80, 0\n\t"
        "v_and_b32_e32 v5, 1, v4\n\t"
        "v_cmp_ne_u32_e32 vcc, 0, v5\n\t"
        "s_nop 1\n\t"
        "s_cmp_lg_u64 vcc, 0\n\t"
        "s_cselect_b32 s81, 2, 0\n\t"
        "s_or_b32 s80, s80, s81\n\t"
        "v_and_b32_e32 v5, 2, v4\n\t"
        "v_cmp_ne_u32_e32 vcc, 0, v5\n\t"
        "s_nop 1\n\t"
        "s_cmp_lg_u64 vcc, 0\n\t"
        "s_cselect_b32 s81, 4, 0\n\t"
        "s_or_b32 s80, s80, s81\n\t"
        "v_and_b32_e32 v5, 4, v4\n\t"
        "v_cmp_ne_u32_e32 vcc, 0, v5\n\t"
        "s_nop 1\n\t"
        "s_cmp_lg_u64 vcc, 0\n\t"
        "s_cselect_b32 s81, 8, 0\n\t"
        "s_or_b32 s80, s80, s81\n\t"
        "v_and_b32_e32 v5, 8, v4\n\t"
        "v_cmp_ne_u32_e32 vcc, 0, v5\n\t"
        "s_nop 1\n\t"
        "s_cmp_lg_u64 vcc, 0\n\t"
        "s_cselect_b32 s81, 16, 0\n\t"
        "s_or_b32 s80, s80, s81\n\t"
        "s_lshr_b32 s80, 0x1adf1210, s80\n\t"
        "s_and_b32 s55, s80, 3\n\t"
        "s_branch .Lal%=\n\t"
        ".Lm1%=:\n\t"
        "s_add_u32 s80, s37, %c[iPEN2]\n\t"
        "s_min_i32 s80, s80, %c[iPENM]\n\t"
        "s_cmp_lg_u32 s37, 0\n\t"
        "s_cselect_b32 s37, s80, %c[iPEN]\n\t"
        "s_add_u32 s80, s56, s37\n\t"
        "s_max_i32 s80, s80, %c[iPMIN]\n\t"
        "s_add_u32 s43, s41, s80\n\t"
        "s_add_u32 s95, s93, %c[LA]\n\t"
        "s_add_u32 s46, s46, %c[iXR]\n\t"
        ".Lal%=:\n\t"
        "s_sub_u32 s80, s55, s43\n\t"
        "s_and_b32 s80, s80, %c[iPHM]\n\t"
        "s_add_u32 s43, s43, s80\n\t"
        "s_branch .Lsd%=\n\t"
        ".Lsf%=:\n\t"
        "s_mov_b32 exec_hi, -1\n\t"
        "s_sleep 2\n\t"
        "buffer_load_dwordx4 v[88:91], v112, %[srd], s66 offen sc1\n\t"
        "s_add_u32 s97, s97, 1\n\t"
        "s_cmp_lt_u32 s97, 0x100000\n\t"
        "s_cbranch_scc0 .Lgu%=\n\t"
        "s_waitcnt vmcnt(0)\n\t"
        "s_branch .Lsr%=\n\t"
        ".Lfa%=:\n\t"
        "v_max_u32_e32 v84, v0, v2\n\t"
        "v_max3_u32 v84, v84, v1, v3\n\t"
        "v_max3_u32 v84, v84, v4, v6\n\t"
        "v_max3_u32 v84, v84, v5, v7\n\t"
        "v_max3_u32 v84, v84, v8, v10\n\t"
        "v_max3_u32 v84, v84, v9, v11\n\t"
        "v_max3_u32 v84, v84, v12, v14\n\t"
        "v_max3_u32 v84, v84, v13, v15\n\t"
        "v_cmp_eq_u32_e32 vcc, -1, v84\n\t"
        "s_nop 1\n\t"
        "v_mov_b32_e32 v104, v104\n\t"
        "s_nop 0\n\t"
        "s_cbranch_vccz .Lra%=\n\t"
        "s_add_u32 s80, s40, 1\n\t"
        "v_mov_b32_e32 v80, 30720\n\t"
        "v_mov_b32_e32 v81, s80\n\t"
        "ds_write_b32 v80, v81\n\t"
        ".Lfp%=:\n\t"
        "s_sleep %c[ASLP]\n\t"
        "buffer_load_dwordx4 v[0:3], v112, %[srd], s57 offen sc1\n\t"
        "buffer_load_dwordx4 v[4:7], v112, %[srd], s58 offen sc1\n\t"
        "buffer_load_dwordx4 v[8:11], v112, %[srd], s59 offen sc1\n\t"
        "buffer_load_dwordx4 v[12:15], v112, %[srd], s60 offen sc1\n\t"
        "s_add_u32 s97, s97, 1\n\t"
        "s_cmp_lt_u32 s97, 0x100000\n\t"
        "s_cbranch_scc0 .Lgu%=\n\t"
        "s_waitcnt vmcnt(0)\n\t"
        "v_max_u32_e32 v84, v0, v2\n\t"
        "v_max3_u32 v84, v84, v1, v3\n\t"
        "v_max3_u32 v84, v84, v4, v6\n\t"
        "v_max3_u32 v84, v84, v5, v7\n\t"
        "v_max3_u32 v84, v84, v8, v10\n\t"
        "v_max3_u32 v84, v84, v9, v11\n\t"
        "v_max3_u32 v84, v84, v12, v14\n\t"
        "v_max3_u32 v84, v84, v13, v15\n\t"
        "v_cmp_eq_u32_e32 vcc, -1, v84\n\t"
        "s_nop 1\n\t"
        "v_mov_b32_e32 v104, v104\n\t"
        "s_nop 0\n\t"
        "s_cbranch_vccnz .Lfp%=\n\t"
        "v_mov_b32_e32 v48, 0\n\t"
        "v_mov_b32_e32 v49, 0\n\t"
        "v_mov_b32_e32 v50, 0\n\t"
        "v_mov_b32_e32 v51, 0\n\t"
        "v_mov_b32_e32 v52, 0\n\t"
        "v_mov_b32_e32 v53, 0\n\t"
        "v_mov_b32_e32 v54, 0\n\t"
        "v_mov_b32_e32 v55, 0\n\t"
        "v_mov_b32_e32 v56, 0\n\t"
        "v_mov_b32_e32 v57, 0\n\t"
        "v_mov_b32_e32 v58, 0\n\t"
        "v_mov_b32_e32 v59, 0\n\t"
        "v_mov_b32_e32 v60, 0\n\t"
        "v_mov_b32_e32 v61, 0\n\t"
        "v_mov_b32_e32 v62, 0\n\t"
        "v_mov_b32_e32 v63, 0\n\t"
        "s_nop 1\n\t"
        "s_branch .Lgo%=\n\t"
        ".Lfc%=:\n\t"
        "v_max_u32_e32 v84, v16, v18\n\t"
        "v_max3_u32 v84, v84, v20, v22\n\t"
        "v_max3_u32 v84, v84, v24, v26\n\t"
        "v_max3_u32 v84, v84, v28, v30\n\t"
        "v_max3_u32 v84, v84, v32, v34\n\t"
        "v_max3_u32 v84, v84, v36, v38\n\t"
        "v_max3_u32 v84, v84, v40, v42\n\t"
        "v_max3_u32 v84, v84, v44, v46\n\t"
        "v_cmp_eq_u32_e32 vcc, -1, v84\n\t"
        "s_nop 1\n\t"
        "v_mov_b32_e32 v104, v104\n\t"
        "s_nop 0\n\t"
        "s_cbranch_vccz .Lrd%=\n\t"
        "s_add_u32 s80, s40, 1\n\t"
        "v_mov_b32_e32 v80, 30724\n\t"
        "v_mov_b32_e32 v81, s80\n\t"
        "ds_write_b32 v80, v81\n\t"
        ".Lfq%=:\n\t"
        "s_sleep %c[ASLP]\n\t"
        "buffer_load_dwordx4 v[16:19], v115, %[srd], s63 offen offset:0 sc1\n\t"
        "buffer_load_dwordx4 v[20:23], v115, %[srd], s63 offen offset:1024 sc1\n\t"
        "buffer_load_dwordx4 v[24:27], v115, %[srd], s63 offen offset:2048 sc1\n\t"
        "buffer_load_dwordx4 v[28:31], v115, %[srd], s63 offen offset:3072 sc1\n\t"
        "buffer_load_dwordx4 v[32:35], v115, %[srd], s64 offen offset:0 sc1\n\t"
        "buffer_load_dwordx4 v[36:39], v115, %[srd], s64 offen offset:1024 sc1\n\t"
        "buffer_load_dwordx4 v[40:43], v115, %[srd], s64 offen offset:2048 sc1\n\t"
        "buffer_load_dwordx4 v[44:47], v115, %[srd], s64 offen offset:3072 sc1\n\t"
        "s_add_u32 s97, s97, 1\n\t"
        "s_cmp_lt_u32 s97, 0x100000\n\t"
        "s_cbranch_scc0 .Lgu%=\n\t"
        "s_waitcnt vmcnt(0)\n\t"
        "v_max_u32_e32 v84, v16, v18\n\t"
        "v_max3_u32 v84, v84, v20, v22\n\t"
        "v_max3_u32 v84, v84, v24, v26\n\t"
        "v_max3_u32 v84, v84, v28, v30\n\t"
        "v_max3_u32 v84, v84, v32, v34\n\t"
        "v_max3_u32 v84, v84, v36, v38\n\t"
        "v_max3_u32 v84, v84, v40, v42\n\t"
        "v_max3_u32 v84, v84, v44, v46\n\t"
        "v_cmp_eq_u32_e32 vcc, -1, v84\n\t"
        "s_nop 1\n\t"
        "v_mov_b32_e32 v104, v104\n\t"
        "s_nop 0\n\t"
        "s_cbranch_vccnz .Lfq%=\n\t"
        "v_mov_b32_e32 v64, 0\n\t"
        "v_mov_b32_e32 v65, 0\n\t"
        "v_mov_b32_e32 v66, 0\n\t"
        "v_mov_b32_e32 v67, 0\n\t"
        "v_mov_b32_e32 v68, 0\n\t"
        "v_mov_b32_e32 v69, 0\n\t"
        "v_mov_b32_e32 v70, 0\n\t"
        "v_mov_b32_e32 v71, 0\n\t"
        "v_mov_b32_e32 v72, 0\n\t"
        "v_mov_b32_e32 v73, 0\n\t"
        "v_mov_b32_e32 v74, 0\n\t"
        "v_mov_b32_e32 v75, 0\n\t"
        "v_mov_b32_e32 v76, 0\n\t"
        "v_mov_b32_e32 v77, 0\n\t"
        "v_mov_b32_e32 v78, 0\n\t"
        "v_mov_b32_e32 v79, 0\n\t"
        "s_nop 1\n\t"
        "s_branch .Lrc%=\n\t"
        ".Lno%=:\n\t"
        "s_waitcnt lgkmcnt(0)\n\t"
        "s_barrier\n\t"
        "s_branch .Lnp%=\n\t"
        ".Lta%=:\n\t"
        "s_waitcnt lgkmcnt(0)\n\t"
        "s_branch .Lxa%=\n\t"
        ".Ltc%=:\n\t"
        "s_waitcnt lgkmcnt(0)\n\t"
        "s_branch .Lxc%=\n\t"
        ".Lgu%=:\n\t"
        "s_mov_b64 exec, -1\n\t"
        "v_mov_b32_e32 v80, %c[ferr]\n\t"
        "v_mov_b32_e32 v81, 2\n\t"
        "buffer_store_dword v81, v80, %[wsd], 0 offen sc1\n\t"
        "s_endpgm\n\t"
        ".Lend%=:\n\t"
        "buffer_store_dword v80, v114, %[wsd], s72 offen sc1\n\t"
        "s_waitcnt vmcnt(0)\n\t"
        "s_barrier\n\t"
        "buffer_store_dword v86, v118, %[wsd], 0 offen sc1\n\t"
        "s_endpgm"
        :
        : [srd] "s"(rhd), [wsd] "s"(wsd), [w] "s"(w), [nA] "s"(nA), [nS] "s"(nS), [nW] "s"(nW), [nC] "s"(nC), [b0] "s"(b0), [b1] "s"(b1), [kvo] "s"(kvo), [pob] "s"(pob),
          [ferr] "i"(F_ERR), [iD1] "i"(D1), [iD2] "i"(D2), [LC] "i"(LC0), [LA] "i"(LA0), [LC24] "i"(LC0 + 24), [LA24] "i"(LA0 + 24), [E1] "i"(2 * (D1 - LC0)), [E2] "i"(2 * (D2 - LA0)),
          [iPEN] "i"(PEN), [iPEN2] "i"(PEN2), [iPENM] "i"(PEN + 2 * PEN2), [iPEN0] "i"(PEN0), [iTOL] "i"(TOL), [iCT0] "i"(CT0), [iX0C] "i"(X0C), [iX0A] "i"(X0A), [iPHM] "i"(PH_M),
          [iFT] "i"(-3), [iFN] "i"(FINE_N), [AUP] "i"(A_UP), [ASLP] "i"(A_SLP), [LDEC] "i"(L_DEC), [UPSH] "i"(UP_SH), [TRUP] "i"(TR_UP), [TRDN] "i"(TR_DN), [HSLP] "i"(H_SLP), [iPMIN] "i"(PMIN), [VSLP] "i"(V_SLP), [RSLP] "i"(R_SLP), [iVD] "i"(V_DEL), [iXR] "i"(XR),
          "{v[112:127]}"(pc), "{a[0:31]}"(wa[0]), "{a[32:63]}"(wa[1]), "{a[64:95]}"(wa[2]), "{a[96:127]}"(wa[3]), "{a[128:159]}"(wa[4]), "{a[160:191]}"(wa[5]), "{a[192:223]}"(wa[6]), "{a[224:255]}"(wa[7]), "{v[128:159]}"(wg[0]), "{v[160:191]}"(wg[1]), "{v[192:223]}"(wg[2]), "{v[224:255]}"(wg[3])
        : "memory", "vcc", "scc", "s36", "s37", "s38", "s39", "s40", "s41", "s42", "s43", "s44", "s45", "s46", "s47", "s48", "s49", "s50", "s51", "s52", "s53", "s54", "s55", "s56", "s57", "s58", "s59", "s60", "s61", "s62", "s63", "s64", "s65", "s66", "s67", "s68", "s69", "s70", "s71", "s72", "s73", "s74", "s75", "s76", "s77", "s78", "s79", "s80", "s81", "s82", "s83", "s84", "s85", "s86", "s87", "s88", "s89", "s90", "s91", "s92", "s93", "s94", "s95", "s96", "s97", "s98", "s99");
    }
    return false;
}

using f32x16 = __attribute__((ext_vector_type(16))) float;
constexpr int CV_RP = 80;
constexpr int CV_U = 96 * CV_RP;
constexpr int CV_KP = 4416;
constexpr int CV_K0 = 8 * CV_U;
__device__ __forceinline__ float lo16(unsigned x) { return __uint_as_float(x << 16); }
__device__ __forceinline__ float hi16(unsigned x) { return __uint_as_float(x & 0xFFFF0000u); }
__device__ __forceinline__ void prep_block(const Params& p, rsrc_t r, int k4, const int wv, const unsigned tag) {
    const int tid = otid(), w = wv, l = tid & 63;
    const int cu = (k4 & 3) * 32 + w * 8 + (l >> 3), oc0 = (k4 >> 2) * 8 + (l & 7);
    const rsrc_t rk = __builtin_amdgcn_make_buffer_rsrc((void*)p.Ak, 0, S * D * 2, 0x00020000), rv = __builtin_amdgcn_make_buffer_rsrc((void*)p.Av, 0, S * D * 2, 0x00020000);
    v4u sk[4], kn[11], vn[8], gw[2];
    auto fetch = [&](int oc) {
#pragma unroll
        for (int j = 0; j < 11; ++j) kn[j] = __builtin_amdgcn_raw_buffer_load_b128(rk, (unsigned)((oc * 8 + j - 3) * 2048 + cu * 16), 0, 2);
#pragma unroll
        for (int i = 0; i < 8; ++i) vn[i] = __builtin_amdgcn_raw_buffer_load_b128(rv, (unsigned)((oc * 8 + i) * 2048 + cu * 16), 0, 2); };
    fetch(oc0);
#pragma unroll
    for (int q = 0; q < 4; ++q) sk[q] = *(const v4u*)(p.shift_k + cu * 32 + q * 8);
#pragma unroll
    for (int q = 0; q < 2; ++q) gw[q] = *(const v4u*)(p.gate_w + q * 512 + l * 8);
    st4(r, tid < 128 ? F_RDY + k4 * 512 + tid * 4 : (tid == 128 && k4 == 0) ? F_CONV : 0xF0000000u, 0u);
#pragma unroll 1
    for (int it = 0; it < 4; ++it) {
        const int oc = oc0 + 64 * it, tk = (it * 32 + k4) * 16 + w;
        v4u kr[11], vr[8], xg[4][2];
#pragma unroll
        for (int j = 0; j < 11; ++j) kr[j] = kn[j];
#pragma unroll
        for (int i = 0; i < 8; ++i) vr[i] = vn[i];
        __builtin_amdgcn_sched_barrier(0);
#pragma unroll
        for (int i = 0; i < 4; ++i)
#pragma unroll
            for (int q = 0; q < 2; ++q) xg[i][q] = *(const v4u*)(p.Aq + (size_t)(tk + 4 * i) * D + q * 512 + l * 8);
        fetch(it < 3 ? oc + 64 : 4096);
        __builtin_amdgcn_sched_barrier(0);
#pragma unroll
        for (int c8 = 0; c8 < 8; ++c8) {
            const unsigned s01 = sk[c8 >> 1][(c8 & 1) * 2], s23 = sk[c8 >> 1][(c8 & 1) * 2 + 1];
            const float sv[4] = {hi16(s23), lo16(s23), hi16(s01), lo16(s01)};
            float kk[11], u[8];
#pragma unroll
            for (int j = 0; j < 11; ++j) kk[j] = (c8 & 1) ? hi16(kr[j][c8 >> 1]) : lo16(kr[j][c8 >> 1]);
            f32x2 acc[4];
#pragma unroll
            for (int j = 0; j < 4; ++j)
#pragma unroll
                for (int i = 0; i < 4; ++i) { const f32x2 t = rbf2((f32x2){kk[i + j], kk[i + j + 4]} * sv[j]); acc[i] = j == 0 ? t : rbf2(acc[i] + t); }
#pragma unroll
            for (int i = 0; i < 8; ++i) u[i] = acc[i & 3][i >> 2] * ((c8 & 1) ? hi16(vr[i][c8 >> 1]) : lo16(vr[i][c8 >> 1]));
            st16(r, W_U + (cu * 8 + c8) * 4096 + oc * 16, (v4u){pack2(u[7], u[6]), pack2(u[5], u[4]), pack2(u[3], u[2]), pack2(u[1], u[0])});
        }
        float ga[4];
#pragma unroll
        for (int i = 0; i < 4; ++i) { float acc = 0.f;
#pragma unroll
            for (int q = 0; q < 2; ++q)
#pragma unroll
                for (int e = 0; e < 4; ++e) { const unsigned xa = xg[i][q][e], gb = gw[q][e]; acc += rbfh(lo16(xa) * lo16(gb)); acc += rbfh(hi16(xa) * hi16(gb)); }
            ga[i] = acc; }
        for (int o = 32; o > 0; o >>= 1)
#pragma unroll
            for (int i = 0; i < 4; ++i) ga[i] += __shfl_xor(ga[i], o);
        const float z = rbfh(l == 0 ? ga[0] : l == 1 ? ga[1] : l == 2 ? ga[2] : ga[3]);
        st4(r, l < 4 ? W_GATE + (tk + 4 * l) * 4 : 0xF0000000u, __float_as_uint(rbfh(1.f / (1.f + expf(-z)))));
    }
    DRAIN(); __syncthreads();
    if (tid == 0) st4(r, F_PREP + k4 * 4, tag);
}
__device__ __forceinline__ bool conv_block(const Params& p, rsrc_t r, int k4, unsigned char* lds, const int wv, const int j, const unsigned tag) {
    (void)j;
    const int tid = otid(), w = wv, l = tid & 63;
    const rsrc_t rs = __builtin_amdgcn_make_buffer_rsrc((void*)p.ssm_k, 0, S * D * 2, 0x00020000);
    v4u kraw[2][4], uv[8]; unsigned dn;
    auto fetch_k = [&](int d0) {
#pragma unroll
        for (int ci = 0; ci < 2; ++ci)
#pragma unroll
            for (int i = 0; i < 4; ++i) kraw[ci][i] = __builtin_amdgcn_raw_buffer_load_b128(rs, (unsigned)((d0 + 2 * w + ci) * 4096 + (i * 64 + l) * 16), 0, 2);
        dn = *(const unsigned*)(p.ssm_D + (d0 & 1023) + 2 * w); };
    auto fetch_u = [&](unsigned o) {
#pragma unroll
        for (int i = 0; i < 8; ++i) uv[i] = ld16(r, o + i * 4096 + tid * 16); };
    fetch_k(k4 * 32);
    if (!wait_prep(r, l, tag)) return false;
    fetch_u(W_U + k4 * 32 * 4096);
#pragma unroll 1
    for (int it = 0; it < 4; ++it) {
    const int bb = k4 * 4 + it; const unsigned dd = dn;
    { const v4u z = {0u, 0u, 0u, 0u};
#pragma unroll
      for (int i = 0; i < 5; ++i) { const int q = tid + 256 * i; *(v4u*)(lds + (q / 160) * CV_U + (q % 160) * 16) = z; } }
#pragma unroll
    for (int i = 0; i < 8; ++i) *(v4u*)(lds + i * CV_U + (32 + (tid >> 2)) * CV_RP + (tid & 3) * 16) = uv[i];
    unsigned char* kc = lds + CV_K0 + w * 4 * CV_KP;
    auto copies = [&](int ci) {
        if (l < 32) *(unsigned*)(kc + l * 4) = 0u;
        if (l < 48) *(unsigned*)(kc + 128 + 4096 + l * 4) = 0u;
#pragma unroll
        for (int i = 0; i < 4; ++i) *(v4u*)(kc + 128 + (i * 64 + l) * 16) = kraw[ci][i];
        __builtin_amdgcn_wave_barrier();
#pragma unroll
        for (int it = 0; it < 9; ++it) { const int i = min(l + 64 * it, CV_KP / 8 - 2);
            const v2u e01 = *(const v2u*)(kc + i * 8), e23 = *(const v2u*)(kc + i * 8 + 8);
            const unsigned s0 = (e01.x >> 16) | (e01.y << 16), s1 = (e01.y >> 16) | (e23.x << 16), s2 = (e23.x >> 16) | (e23.y << 16);
            *(v2u*)(kc + CV_KP + i * 8) = (v2u){s0, s1};
            *(v2u*)(kc + 2 * CV_KP + i * 8) = (v2u){e01.y, e23.x};
            *(v2u*)(kc + 3 * CV_KP + i * 8) = (v2u){s1, s2}; }
        __builtin_amdgcn_wave_barrier(); };
    copies(0);
    __syncthreads();
    const int b = l & 31, kg = l >> 5, cc = (b + 1) & 3;
    float ys[32];
#pragma unroll
    for (int ci = 0; ci < 2; ++ci) {
        const int c8 = 2 * w + ci;
        if (ci == 1) { copies(1); __builtin_amdgcn_sched_barrier(0); fetch_k(it < 3 ? bb * 8 + 8 : 1024); fetch_u(it < 3 ? W_U + (bb * 8 + 8) * 4096 : 0xF0000000u); __builtin_amdgcn_sched_barrier(0); }
        f32x16 acc0, acc1, ad0, ad1;
#pragma unroll
        for (int i = 0; i < 16; ++i) { acc0[i] = 0.f; acc1[i] = 0.f; ad0[i] = 0.f; ad1[i] = 0.f; }
        typedef const __attribute__((address_space(3))) v2u* l2p;
        unsigned kb = (unsigned)(size_t)(kc + cc * CV_KP + (b - 8 * kg - 7 + 64 - cc) * 2) - 32, kb8 = kb + 8;
        asm volatile("" : "+v"(kb), "+v"(kb8));
        const unsigned char* ub = lds + c8 * CV_U + (32 + b) * CV_RP + kg * 16;
        v2u ka[4][2], kz[4][2]; bf16x8 fa[4], fz[4];
        auto ld_k = [&](int set, int un, int hi) { const int dl = un >> 1, kh = un & 1; const unsigned o = (unsigned)((32 * dl - 16 * kh + 16) * 2 + hi * 2048);
            if (hi) { kz[set][0] = *(l2p)(size_t)(kb + o); kz[set][1] = *(l2p)(size_t)(kb8 + o); } else { ka[set][0] = *(l2p)(size_t)(kb + o); ka[set][1] = *(l2p)(size_t)(kb8 + o); } };
        auto ld_f = [&](int set, int un) { const int dl = un >> 1, kh = un & 1;
            fa[set] = *(const bf16x8*)(ub + (32 - dl) * CV_RP + kh * 32); fz[set] = *(const bf16x8*)(ub - dl * CV_RP + kh * 32); };
#pragma unroll
        for (int q = 0; q < 2; ++q) { ld_k(q, q, 0); ld_f(q, q); ld_k(q, q, 1); }
        {
            const unsigned dv = ci ? dd >> 16 : dd & 0xFFFFu;
#pragma unroll
            for (int kh = 0; kh < 2; ++kh) { const int e = 7 - (b - 16 * kh - 8 * kg); const unsigned x = dv << ((e & 1) * 16); const int q = e >> 1;
                const v4u di = {q == 0 ? x : 0u, q == 1 ? x : 0u, q == 2 ? x : 0u, q == 3 ? x : 0u};
                asm volatile("s_nop 1\n\tv_mfma_f32_32x32x16_bf16 %0, %2, %4, %0\n\tv_mfma_f32_32x32x16_bf16 %1, %3, %4, %1" : "+v"(ad1), "+v"(ad0) : "v"(fa[kh]), "v"(fz[kh]), "v"(di)); }
        }
#pragma unroll 1
        for (int u0 = 0; u0 < 64; u0 += 4) {
#pragma unroll
            for (int q = 0; q < 4; ++q) {
                const int un = u0 + q + 2, ns = (q + 2) & 3;
                const v4u kav = {ka[q][0].x, ka[q][0].y, ka[q][1].x, ka[q][1].y}, kzv = {kz[q][0].x, kz[q][0].y, kz[q][1].x, kz[q][1].y};
                __builtin_amdgcn_sched_barrier(0);
                asm volatile("s_nop 1\n\tv_mfma_f32_32x32x16_bf16 %0, %1, %2, %0" : "+v"(acc1) : "v"(fa[q]), "v"(kav));
                ld_k(ns, un, 0);
                __builtin_amdgcn_sched_barrier(0);
                asm volatile("v_mfma_f32_32x32x16_bf16 %0, %1, %2, %0" : "+v"(acc0) : "v"(fz[q]), "v"(kav));
                ld_f(ns, un);
                __builtin_amdgcn_sched_barrier(0);
                asm volatile("v_mfma_f32_32x32x16_bf16 %0, %1, %2, %0" : "+v"(acc1) : "v"(fz[q]), "v"(kzv));
                ld_k(ns, un, 1);
            }
        }
        asm volatile("s_nop 7\n\ts_nop 7" : "+v"(acc0), "+v"(acc1), "+v"(ad0), "+v"(ad1));
#pragma unroll
        for (int i = 0; i < 32; ++i) { const float y = rbfh(i < 16 ? acc0[i] : acc1[i - 16]) + rbfh(i < 16 ? ad0[i] : ad1[i - 16]);
            ys[i] = ci == 0 ? y : __uint_as_float(pack2(ys[i], y)); }
    }
    __syncthreads();
#pragma unroll
    for (int i = 0; i < 32; ++i) { const int row = (i >> 4) * 32 + (i & 3) + 8 * ((i & 15) >> 2) + 4 * kg; *(float*)(lds + (row * 32 + b) * 16 + w * 4) = ys[i]; }
    __syncthreads();
#pragma unroll
    for (int i = 0; i < 8; ++i) { const int t = tid + 256 * i; st16(r, W_A + bb * 32768 + t * 16, *(const v4u*)(lds + t * 16)); }
    __syncthreads();
    }
    DRAIN(); __syncthreads();
    if (tid == 0) __hip_atomic_fetch_add((unsigned*)(p.ws + F_CONV), 1u, __ATOMIC_RELAXED, __HIP_MEMORY_SCOPE_AGENT);
    return true;
}

__device__ __forceinline__ void dma16(unsigned ldsdst, unsigned voff, const v4u& srd, unsigned soff) {
    asm volatile("s_mov_b32 m0, %0\n\ts_nop 0\n\tbuffer_load_dwordx4 %1, %2, %3 offen lds" :: "s"(ldsdst), "v"(voff), "s"(srd), "s"(soff) : "m0", "memory"); }
__device__ __forceinline__ void dma16n(unsigned ldsdst, unsigned voff, const v4u& srd, unsigned soff) {
    asm volatile("s_mov_b32 m0, %0\n\ts_nop 0\n\tbuffer_load_dwordx4 %1, %2, %3 offen nt lds" :: "s"(ldsdst), "v"(voff), "s"(srd), "s"(soff) : "m0", "memory"); }
__device__ __forceinline__ v4u pack_acc(const f32x4& a, const f32x4& b) {
    float x0, x1, x2, x3, y0, y1, y2, y3;
    asm volatile("v_accvgpr_read_b32 %0, %8\n\tv_accvgpr_read_b32 %1, %9\n\tv_accvgpr_read_b32 %2, %10\n\tv_accvgpr_read_b32 %3, %11\n\t"
                 "v_accvgpr_read_b32 %4, %12\n\tv_accvgpr_read_b32 %5, %13\n\tv_accvgpr_read_b32 %6, %14\n\tv_accvgpr_read_b32 %7, %15"
                 : "=v"(x0), "=v"(x1), "=v"(x2), "=v"(x3), "=v"(y0), "=v"(y1), "=v"(y2), "=v"(y3)
                 : "a"(a[0]), "a"(a[1]), "a"(a[2]), "a"(a[3]), "a"(b[0]), "a"(b[1]), "a"(b[2]), "a"(b[3]));
    return (v4u){pack2(x0, x1), pack2(x2, x3), pack2(y0, y1), pack2(y2, y3)}; }
#define SB() __builtin_amdgcn_sched_barrier(0)
#define WO_REGS "a128", "a129", "a130", "a131", "a132", "a133", "a134", "a135", "a136", "a137", "a138", "a139", "a140", "a141", "a142", "a143", "a144", "a145", "a146", "a147", "a148", "a149", "a150", "a151", "a152", "a153", "a154", "a155", "a156", "a157", "a158", "a159", "a160", "a161", "a162", "a163", "a164", "a165", "a166", "a167", "a168", "a169", "a170", "a171", "a172", "a173", "a174", "a175", "a176", "a177", "a178", "a179", "a180", "a181", "a182", "a183", "a184", "a185", "a186", "a187", "a188", "a189", "a190", "a191", "a192", "a193", "a194", "a195", "a196", "a197", "a198", "a199", "a200", "a201", "a202", "a203", "a204", "a205", "a206", "a207", "a208", "a209", "a210", "a211", "a212", "a213", "a214", "a215", "a216", "a217", "a218", "a219", "a220", "a221", "a222", "a223", "a224", "a225", "a226", "a227", "a228", "a229", "a230", "a231", "a232", "a233", "a234", "a235", "a236", "a237", "a238", "a239", "a240", "a241", "a242", "a243", "a244", "a245", "a246", "a247", "a248", "a249", "a250", "a251", "a252", "a253", "a254", "a255", "v192", "v193", "v194", "v195", "v196", "v197", "v198", "v199", "v200", "v201", "v202", "v203", "v204", "v205", "v206", "v207", "v208", "v209", "v210", "v211", "v212", "v213", "v214", "v215", "v216", "v217", "v218", "v219", "v220", "v221", "v222", "v223", "v224", "v225", "v226", "v227", "v228", "v229", "v230", "v231", "v232", "v233", "v234", "v235", "v236", "v237", "v238", "v239", "v240", "v241", "v242", "v243", "v244", "v245", "v246", "v247", "v248", "v249", "v250", "v251", "v252", "v253", "v254", "v255"
struct NoHook { __device__ void operator()(int) const {} };
template <int MODE, int MI, int NJ, int NS, int HX = 0, class HOOK = NoHook, int MN = MI>
__device__ __forceinline__ void gemm(rsrc_t r, const bf16_t* __restrict__ A, const bf16_t* __restrict__ B, int m0, int n0, unsigned char* lds, unsigned coff, const int wv,
                                     const int rot, const void* tk = nullptr,
                                     const bool have = false, const bf16_t* Bn = nullptr, int m0n = 0, int n0n = 0, HOOK&& hook = NoHook()) {
    constexpr int AB = MN * 4096, BB = NJ * 2048, B0 = (NS / 2) * AB, NB = NJ / 2, NM = MI * NJ, NR = MI + NJ, BP = NJ * 64;
    static_assert(NS % 2 == 0 && 32 % NS == 0 && B0 <= 65536 && NS * BB <= 65536 && (NJ == 4 || NJ == 8), "ring");
    const int tid = otid();
    const int w = wv, l = tid & 63, n16 = l & 15, g = l >> 4;
    const int wm = w >> 1, wn = w & 1;
    const unsigned lb = __builtin_amdgcn_readfirstlane((unsigned)(size_t)lds);
    const v4u sa = mk_srd(A, 2048 * 1024 * 2); v4u sb = mk_srd(B, 1024 * 4096 * 2);
    f32x4 acc[MI][NJ];
#pragma unroll
    for (int i = 0; i < MI; ++i)
#pragma unroll
        for (int j = 0; j < NJ; ++j) acc[i][j] = (f32x4){0.f, 0.f, 0.f, 0.f};
    unsigned va = (unsigned)((m0 + 8 * MI * w + (l >> 3)) * 2048 + (((l & 7) ^ (l >> 3)) << 4));
    unsigned vb[2];
    if (NJ == 8) { vb[0] = (unsigned)((8 * w + (l >> 5)) * 8192 + n0 * 2) + (unsigned)(((l & 31) << 4) ^ ((l >> 5) << 5) ^ (wn << 7)); vb[1] = vb[0] ^ 64u; }
    else { vb[0] = (unsigned)((8 * w + g) * 8192 + n0 * 2) + (unsigned)((n16 << 4) ^ (g << 5) ^ (wn << 7)); vb[1] = vb[0]; }
    auto rk = [&](int kt) { return (kt & ~31) | ((kt + rot) & 31); };
    unsigned la = lb + w * MI * 1024;
    auto st_a = [&](int buf, int kt, int q) { dma16(la + buf * AB + q * 1024, va, sa, (unsigned)(rk(kt) * 64 + q * 16384)); };
    auto st_b = [&](int buf, int kt, int x) { if ((B_NT >> (NJ == 4 ? 0 : MI == 4 ? 1 : 2)) & 1) dma16n(lb + B0 + buf * BB + (w * NB + x) * 1024, vb[x & 1], sb, (unsigned)((rk(kt) * 32 + x * (8 / NB)) * 8192));
        else dma16(lb + B0 + buf * BB + (w * NB + x) * 1024, vb[x & 1], sb, (unsigned)((rk(kt) * 32 + x * (8 / NB)) * 8192)); };
    const unsigned f = (unsigned)((n16 >> 2) | ((g & 1) << 2));
    constexpr int NQ = MODE == 1 ? NJ : NJ / 2;
    unsigned ra[2], rb[NQ];
    ra[0] = lb + (16 * MI * wm + n16) * 128 + ((g ^ (n16 & 7)) << 4); ra[1] = ra[0] ^ 64u;
    {   const unsigned row = lb + B0 + (8 * g + (n16 >> 2)) * BP + (NJ == 8 ? 256 * wn : 0), g0 = NJ == 8 ? 0u : 4u * wn;
#pragma unroll
        for (int j = 0; j < NQ; ++j) {
            if (MODE == 1) rb[j] = row + (((g0 + j) ^ f) << 5) + 8 * (n16 & 3);
            else rb[j] = row + (((g0 + 2 * j + ((n16 & 3) >> 1)) ^ f) << 5) + (n16 & 1) * 16;
            asm volatile("" : "+v"(rb[j])); } }
    bf16x8 af[2][MI], bfr[2][NJ];
    auto rd = [&](int set, int buf, int x) {
        if (x < NJ) { const unsigned a = rb[MODE == 1 ? x : (x >> 1)] + (MODE == 1 ? 0 : (x & 1) * 8) + buf * BB;
            const v2u lo = trb(a), hi = trb(a + 4 * BP); v4u v = {lo.x, lo.y, hi.x, hi.y}; bfr[set][x] = as_frag(v); }
        else af[set][x - NJ] = ldsa(ra[buf & 1] + (buf >> 1) * AB + (x - NJ) * 2048); };
    auto mf = [&](int set, int i, int j) {
        if (HX) asm volatile("v_mfma_f32_16x16x32_bf16 %0, %1, %2, %0" : "+a"(acc[i][j]) : "v"(MODE == 1 ? af[set][i] : bfr[set][j]), "v"(MODE == 1 ? bfr[set][j] : af[set][i]) : WO_REGS);
        else if (MODE == 1) asm volatile("v_mfma_f32_16x16x32_bf16 %0, %1, %2, %0" : "+a"(acc[i][j]) : "v"(af[set][i]), "v"(bfr[set][j]));
        else asm volatile("v_mfma_f32_16x16x32_bf16 %0, %1, %2, %0" : "+a"(acc[i][j]) : "v"(bfr[set][j]), "v"(af[set][i])); };
    if (!have) {
#pragma unroll
        for (int t = 0; t < NS; ++t) {
            if (t & 1) {
#pragma unroll
                for (int q = 0; q < MI; ++q) st_a(t >> 1, t - 1, q); }
#pragma unroll
            for (int q = 0; q < NB; ++q) st_b(t, t, q);
            if (HX) hook(t); }
    }
#pragma unroll
    for (int i = 0; i < MI; ++i)
#pragma unroll
        for (int j = 0; j < NJ; ++j) asm volatile("" : "+a"(acc[i][j]));
    asm volatile("s_nop 1" ::: "memory");
    if (!have) {
        asm volatile("s_waitcnt vmcnt(%c0)\n\ts_barrier" :: "i"((NS - 1) * (NB + HX) + (NS / 2 - 1) * MI) : "memory");
    }
#pragma unroll
    for (int x = 0; x < NR; ++x) rd(0, 0, x);
    constexpr int HEAD = NM / 8;
    const v4u st = mk_srd(tk, tk ? 32768 : 0); unsigned tdum = 0u;
#pragma unroll 1
    for (int k0 = 0; k0 < 32; k0 += NS) {
        int kb = k0 + NS;
        if (kb == 32) {
            kb = Bn ? 0 : 0x10100;
            if (Bn) { va += (unsigned)((m0n - m0 + 8 * (MN - MI) * w) * 2048); la = lb + w * MN * 1024; vb[0] += (unsigned)((n0n - n0) * 2); vb[1] += (unsigned)((n0n - n0) * 2); sb = mk_srd(Bn, 1024 * 4096 * 2); } }
        if (NS == 4) asm volatile("buffer_load_dword %0, %1, %2, %3 offen" : "+v"(tdum) : "v"(l < 32 ? (unsigned)l * 128u : 0xF0000000u), "s"(st), "s"((unsigned)k0 * 1024u) : "memory");
#pragma unroll
        for (int u = 0; u < NS; ++u) {
            SB();
#pragma unroll
            for (int q = 0; q < HEAD; ++q) mf(u & 1, q / NJ, q % NJ);
            SB();
            asm volatile("s_waitcnt vmcnt(%c0) lgkmcnt(0)\n\ts_barrier" :: "i"((NS - 2) * (NB + HX) + (NS / 2 - 1 - (u & 1)) * MI) : "memory");
            SB();
            constexpr int span = NM - HEAD;
#pragma unroll
            for (int q = HEAD; q < NM; ++q) {
                mf(u & 1, q / NJ, q % NJ);
                const int x = q - HEAD, nd = NB + ((u & 1) ? MI : 0);
#pragma unroll
                for (int y = 0; y < NR; ++y) if ((y * (span - HEAD)) / NR == x) rd((u & 1) ^ 1, (u + 1) % NS, y);
#pragma unroll
                for (int y = 0; y < nd; ++y) if ((y * span + span / 2) / nd == x) { if ((u & 1) && y < MI) st_a(u >> 1, kb + u - 1, y); else st_b(u, kb + u, y - ((u & 1) ? MI : 0)); }
                SB(); }
            if (MN > MI && (u & 1) && k0 == 32 - NS) {
#pragma unroll
                for (int y = MI; y < MN; ++y) st_a(u >> 1, kb + u - 1, y); }
            if (HX) hook(NS + k0 + u);
        }
    }
    asm volatile("s_nop 7\n\ts_nop 3" : "+v"(tdum) :: "memory");
    SB();
    const int col = n0 + 16 * NJ * wn, head = col >> 7, sub = (col & 127) >> 4, seg0 = (m0 >> 5) + (MI / 2) * wm;
#pragma unroll
    for (int sg = 0; sg < MI / 2; ++sg) {
        const unsigned base = coff + (unsigned)(((seg0 + sg) * 32 + head) * 8192 + l * 16);
#pragma unroll
        for (int q = 0; q < NJ; ++q) {
            if (HX) asm volatile("" ::: WO_REGS);
            if (MODE == 0) { const int tt = q / (NJ / 2), kk = q % (NJ / 2); st16(r, base + (tt * 4 + (sub >> 1) + kk) * 1024, pack_acc(acc[2 * sg + tt][2 * kk], acc[2 * sg + tt][2 * kk + 1])); }
            else st16(r, base + (sub + q) * 1024, pack_acc(acc[2 * sg][q], acc[2 * sg + 1][q])); }
    }
}

__device__ __forceinline__ unsigned min4(unsigned v) { return min(min(__builtin_amdgcn_readlane(v, 0), __builtin_amdgcn_readlane(v, 1)), min(__builtin_amdgcn_readlane(v, 2), __builtin_amdgcn_readlane(v, 3))); }
__device__ __forceinline__ void attn_block(const Params& p, rsrc_t r, unsigned char* lds, const int wv, const int h, const int j, const unsigned tag) {
    (void)j;
    const int l = otid() & 63, n16 = l & 15, g = l >> 4;
    { v4u q[4]; if (!wait_cal(r, l, tag, q)) return; }
    v4u qv[2][4], kv[2][4], va[8];
    int nev = 0; (void)nev;
    auto fetch = [&](int seg) -> bool {
        for (unsigned spin = 0;; ++spin) { MEMBAR();
            const unsigned fv = ld4(r, F_KVQ + ((seg >> 2) * 32 + h) * 64 + min(l, 2) * 4);
            if (__all(fv == tag)) break;
            if (spin > (1u << 20)) { st4(r, F_ERR, 64u); return false; }
            __builtin_amdgcn_s_sleep(16); }
        MEMBAR();
        const unsigned o = (unsigned)((seg * 32 + h) * 8192 + l * 16);
#pragma unroll
        for (int i = 0; i < 8; ++i) qv[i >> 2][i & 3] = ld16(r, W_QX + o + i * 1024);
#pragma unroll
        for (int i = 0; i < 8; ++i) kv[i >> 2][i & 3] = ld16(r, W_KX + o + i * 1024);
#pragma unroll
        for (int i = 0; i < 8; ++i) va[i] = ld16(r, W_VF + o + i * 1024);
        return true; };
    if (!fetch(wv)) return;
    unsigned prog = 0u, tseen = rtnow() - 100000u;
#pragma unroll 1
    for (int seg = wv; seg < NSEG; seg += 4) {
        for (unsigned spin = 0; prog <= (unsigned)seg; ++spin) {
            const unsigned now = rtnow();
            if (spin > (1u << 22)) { st4(r, F_ERR, 32u); return; }
            if (prog + (seg >= EARLY_FROM ? 2u : 1u) + (now - tseen) / STEP_MIN <= (unsigned)seg) { __builtin_amdgcn_s_sleep(16); continue; }
            MEMBAR();
            const unsigned pg = min4(ld4(r, F_PROG + h * 128 + min(l, 2) * 8));
            if (pg != prog) { prog = pg; tseen = now; }
            if (prog <= (unsigned)seg) __builtin_amdgcn_s_sleep(P_SLP);
        }
        MEMBAR();
        const unsigned km = HO_KVM + (seg * 32 + h) * 4096;
        v4u kmv[4]; v2u pm[8];
        const v4u k0 = ld16(r, km + l * 16), k1 = ld16(r, km + 1024 + l * 16);
#pragma unroll
        for (int t = 0; t < 8; ++t) pm[t] = ld8(r, km + 2048 + t * 256 + n16 * 16 + (g & 1) * 8);
        *(v4u*)(lds + wv * 2048 + l * 16) = k0; *(v4u*)(lds + wv * 2048 + 1024 + l * 16) = k1;
        __builtin_amdgcn_wave_barrier();
#pragma unroll
        for (int ks = 0; ks < 4; ++ks) { const unsigned a = (unsigned)(size_t)lds + wv * 2048 + (32 * ks + 8 * g + (n16 >> 2)) * 16 + (n16 & 1) * 8; const v2u x0 = trb(a), x1 = trb(a + 64); kmv[ks] = (v4u){x0.x, x0.y, x1.x, x1.y}; }
#pragma unroll
        for (int qt = 0; qt < 2; ++qt) {
            f32x4 sc[3];
            asm volatile("s_nop 1\n\t"
                "v_mfma_f32_16x16x32_bf16 %0, %3, %15, 0\n\tv_mfma_f32_16x16x32_bf16 %1, %7, %15, 0\n\tv_mfma_f32_16x16x32_bf16 %2, %11, %15, 0\n\t"
                "v_mfma_f32_16x16x32_bf16 %0, %4, %16, %0\n\tv_mfma_f32_16x16x32_bf16 %1, %8, %16, %1\n\tv_mfma_f32_16x16x32_bf16 %2, %12, %16, %2\n\t"
                "v_mfma_f32_16x16x32_bf16 %0, %5, %17, %0\n\tv_mfma_f32_16x16x32_bf16 %1, %9, %17, %1\n\tv_mfma_f32_16x16x32_bf16 %2, %13, %17, %2\n\t"
                "v_mfma_f32_16x16x32_bf16 %0, %6, %18, %0\n\tv_mfma_f32_16x16x32_bf16 %1, %10, %18, %1\n\tv_mfma_f32_16x16x32_bf16 %2, %14, %18, %2\n\t"
                "s_nop 7"
                : "=&v"(sc[0]), "=&v"(sc[1]), "=&v"(sc[2])
                : "v"(kv[0][0]), "v"(kv[0][1]), "v"(kv[0][2]), "v"(kv[0][3]), "v"(kv[1][0]), "v"(kv[1][1]), "v"(kv[1][2]), "v"(kv[1][3]),
                  "v"(kmv[0]), "v"(kmv[1]), "v"(kmv[2]), "v"(kmv[3]), "v"(qv[qt][0]), "v"(qv[qt][1]), "v"(qv[qt][2]), "v"(qv[qt][3]));
            const int qi = 16 * qt + n16; float mx = -INFINITY;
#pragma unroll
            for (int t = 0; t < 3; ++t)
#pragma unroll
                for (int rr = 0; rr < 4; ++rr) { float v = rbfh(rbfh(sc[t][rr]) * 0.08838834764831845f);
                    if (t < 2) v = (16 * t + 4 * g + rr <= qi) ? v : -1000000000.0f; else if (g >= 2) v = -INFINITY;
                    sc[t][rr] = v; mx = fmaxf(mx, v); }
            mx = xor16_max(mx); mx = xor32_max(mx); float sum = 0.f;
#pragma unroll
            for (int t = 0; t < 3; ++t)
#pragma unroll
                for (int rr = 0; rr < 4; ++rr) { const float e = __expf(sc[t][rr] - mx); sc[t][rr] = e; sum += e; }
            sum = xor16_add(sum); sum = xor32_add(sum); const float inv = 1.0f / sum;
            const v4u p0 = {pack2(sc[0][0] * inv, sc[0][1] * inv), pack2(sc[0][2] * inv, sc[0][3] * inv), pack2(sc[1][0] * inv, sc[1][1] * inv), pack2(sc[1][2] * inv, sc[1][3] * inv)};
            const v2u p1 = {pack2(sc[2][0] * inv, sc[2][1] * inv), pack2(sc[2][2] * inv, sc[2][3] * inv)};
            f32x4 oa[8];
            asm volatile("s_nop 1\n\t"
                "v_mfma_f32_16x16x32_bf16 %0, %8, %24, 0\n\tv_mfma_f32_16x16x32_bf16 %1, %9, %24, 0\n\tv_mfma_f32_16x16x32_bf16 %2, %10, %24, 0\n\tv_mfma_f32_16x16x32_bf16 %3, %11, %24, 0\n\t"
                "v_mfma_f32_16x16x32_bf16 %4, %12, %24, 0\n\tv_mfma_f32_16x16x32_bf16 %5, %13, %24, 0\n\tv_mfma_f32_16x16x32_bf16 %6, %14, %24, 0\n\tv_mfma_f32_16x16x32_bf16 %7, %15, %24, 0\n\t"
                "v_mfma_f32_16x16x16_bf16 %0, %16, %25, %0\n\tv_mfma_f32_16x16x16_bf16 %1, %17, %25, %1\n\tv_mfma_f32_16x16x16_bf16 %2, %18, %25, %2\n\tv_mfma_f32_16x16x16_bf16 %3, %19, %25, %3\n\t"
                "v_mfma_f32_16x16x16_bf16 %4, %20, %25, %4\n\tv_mfma_f32_16x16x16_bf16 %5, %21, %25, %5\n\tv_mfma_f32_16x16x16_bf16 %6, %22, %25, %6\n\tv_mfma_f32_16x16x16_bf16 %7, %23, %25, %7\n\t"
                "s_nop 7"
                : "=&v"(oa[0]), "=&v"(oa[1]), "=&v"(oa[2]), "=&v"(oa[3]), "=&v"(oa[4]), "=&v"(oa[5]), "=&v"(oa[6]), "=&v"(oa[7])
                : "v"(va[0]), "v"(va[1]), "v"(va[2]), "v"(va[3]), "v"(va[4]), "v"(va[5]), "v"(va[6]), "v"(va[7]),
                  "v"(pm[0]), "v"(pm[1]), "v"(pm[2]), "v"(pm[3]), "v"(pm[4]), "v"(pm[5]), "v"(pm[6]), "v"(pm[7]), "v"(p0), "v"(p1));
#pragma unroll
            for (int u = 0; u < 4; ++u) {
                const f32x4 o0 = oa[2 * u], o1 = oa[2 * u + 1];
                const v4u pk = {pack2(o0[0], o0[1]), pack2(o0[2], o0[3]), pack2(o1[0], o1[1]), pack2(o1[2], o1[3])};
                st16(r, W_OX + (((seg * 2 + qt) * 32 + h) * 4 + u) * 1024 + l * 16, pk);
            }
        }
        DRAIN();
        { const int tt = 2 * seg + (l >> 5);
            __hip_atomic_fetch_add((unsigned*)(p.ws + F_RDY + (((tt & 3) * 32 + (l & 31)) * 32 + (tt >> 2)) * 4), 1u, __ATOMIC_RELAXED, __HIP_MEMORY_SCOPE_AGENT); }
        if (seg + 4 < NSEG && !fetch(seg + 4)) return;
    }
}

constexpr int OP_P = 80;
__device__ __forceinline__ void oproj_block(const Params& p, rsrc_t r, unsigned char* lds, const int wv, const int nt, const int rho, const int j, const unsigned tag
    ) {
    (void)j;
    const int w = wv, l = otid() & 63, n16 = l & 15, g = l >> 4;
    const unsigned ldsbase = (unsigned)(size_t)lds;
    bf16x8 wof[32][2]; v32u wo[6];
    asm volatile("" : "={a[128:159]}"(wo[0]), "={a[160:191]}"(wo[1]), "={a[192:223]}"(wo[2]), "={a[224:255]}"(wo[3]), "={v[192:223]}"(wo[4]), "={v[224:255]}"(wo[5]));
#pragma unroll
    for (int kq = 0; kq < 32; ++kq) {
        const int ks = (kq + 16) & 31;
        const unsigned st = ldsbase + w * 8192 + (ks & 1) * 4096, sa = st + (l >> 2) * OP_P + (l & 3) * 16;
        switch (ks) {
            case 0: asm volatile("ds_write_b128 %0, a[128:131]\n\tds_write_b128 %0, a[132:135] offset:%c2" :: "v"(sa), "{a[128:159]}"(wo[0]), "i"(16 * OP_P) : "memory"); break;
            case 1: asm volatile("ds_write_b128 %0, a[136:139]\n\tds_write_b128 %0, a[140:143] offset:%c2" :: "v"(sa), "{a[128:159]}"(wo[0]), "i"(16 * OP_P) : "memory"); break;
            case 2: asm volatile("ds_write_b128 %0, a[144:147]\n\tds_write_b128 %0, a[148:151] offset:%c2" :: "v"(sa), "{a[128:159]}"(wo[0]), "i"(16 * OP_P) : "memory"); break;
            case 3: asm volatile("ds_write_b128 %0, a[152:155]\n\tds_write_b128 %0, a[156:159] offset:%c2" :: "v"(sa), "{a[128:159]}"(wo[0]), "i"(16 * OP_P) : "memory"); break;
            case 4: asm volatile("ds_write_b128 %0, a[160:163]\n\tds_write_b128 %0, a[164:167] offset:%c2" :: "v"(sa), "{a[160:191]}"(wo[1]), "i"(16 * OP_P) : "memory"); break;
            case 5: asm volatile("ds_write_b128 %0, a[168:171]\n\tds_write_b128 %0, a[172:175] offset:%c2" :: "v"(sa), "{a[160:191]}"(wo[1]), "i"(16 * OP_P) : "memory"); break;
            case 6: asm volatile("ds_write_b128 %0, a[176:179]\n\tds_write_b128 %0, a[180:183] offset:%c2" :: "v"(sa), "{a[160:191]}"(wo[1]), "i"(16 * OP_P) : "memory"); break;
            case 7: asm volatile("ds_write_b128 %0, a[184:187]\n\tds_write_b128 %0, a[188:191] offset:%c2" :: "v"(sa), "{a[160:191]}"(wo[1]), "i"(16 * OP_P) : "memory"); break;
            case 8: asm volatile("ds_write_b128 %0, a[192:195]\n\tds_write_b128 %0, a[196:199] offset:%c2" :: "v"(sa), "{a[192:223]}"(wo[2]), "i"(16 * OP_P) : "memory"); break;
            case 9: asm volatile("ds_write_b128 %0, a[200:203]\n\tds_write_b128 %0, a[204:207] offset:%c2" :: "v"(sa), "{a[192:223]}"(wo[2]), "i"(16 * OP_P) : "memory"); break;
            case 10: asm volatile("ds_write_b128 %0, a[208:211]\n\tds_write_b128 %0, a[212:215] offset:%c2" :: "v"(sa), "{a[192:223]}"(wo[2]), "i"(16 * OP_P) : "memory"); break;
            case 11: asm volatile("ds_write_b128 %0, a[216:219]\n\tds_write_b128 %0, a[220:223] offset:%c2" :: "v"(sa), "{a[192:223]}"(wo[2]), "i"(16 * OP_P) : "memory"); break;
            case 12: asm volatile("ds_write_b128 %0, a[224:227]\n\tds_write_b128 %0, a[228:231] offset:%c2" :: "v"(sa), "{a[224:255]}"(wo[3]), "i"(16 * OP_P) : "memory"); break;
            case 13: asm volatile("ds_write_b128 %0, a[232:235]\n\tds_write_b128 %0, a[236:239] offset:%c2" :: "v"(sa), "{a[224:255]}"(wo[3]), "i"(16 * OP_P) : "memory"); break;
            case 14: asm volatile("ds_write_b128 %0, a[240:243]\n\tds_write_b128 %0, a[244:247] offset:%c2" :: "v"(sa), "{a[224:255]}"(wo[3]), "i"(16 * OP_P) : "memory"); break;
            case 15: asm volatile("ds_write_b128 %0, a[248:251]\n\tds_write_b128 %0, a[252:255] offset:%c2" :: "v"(sa), "{a[224:255]}"(wo[3]), "i"(16 * OP_P) : "memory"); break;
            case 16: asm volatile("ds_write_b128 %0, v[192:195]\n\tds_write_b128 %0, v[196:199] offset:%c2" :: "v"(sa), "{v[192:223]}"(wo[4]), "i"(16 * OP_P) : "memory"); break;
            case 17: asm volatile("ds_write_b128 %0, v[200:203]\n\tds_write_b128 %0, v[204:207] offset:%c2" :: "v"(sa), "{v[192:223]}"(wo[4]), "i"(16 * OP_P) : "memory"); break;
            case 18: asm volatile("ds_write_b128 %0, v[208:211]\n\tds_write_b128 %0, v[212:215] offset:%c2" :: "v"(sa), "{v[192:223]}"(wo[4]), "i"(16 * OP_P) : "memory"); break;
            case 19: asm volatile("ds_write_b128 %0, v[216:219]\n\tds_write_b128 %0, v[220:223] offset:%c2" :: "v"(sa), "{v[192:223]}"(wo[4]), "i"(16 * OP_P) : "memory"); break;
            case 20: asm volatile("ds_write_b128 %0, v[224:227]\n\tds_write_b128 %0, v[228:231] offset:%c2" :: "v"(sa), "{v[224:255]}"(wo[5]), "i"(16 * OP_P) : "memory"); break;
            case 21: asm volatile("ds_write_b128 %0, v[232:235]\n\tds_write_b128 %0, v[236:239] offset:%c2" :: "v"(sa), "{v[224:255]}"(wo[5]), "i"(16 * OP_P) : "memory"); break;
            case 22: asm volatile("ds_write_b128 %0, v[240:243]\n\tds_write_b128 %0, v[244:247] offset:%c2" :: "v"(sa), "{v[224:255]}"(wo[5]), "i"(16 * OP_P) : "memory"); break;
            case 23: asm volatile("ds_write_b128 %0, v[248:251]\n\tds_write_b128 %0, v[252:255] offset:%c2" :: "v"(sa), "{v[224:255]}"(wo[5]), "i"(16 * OP_P) : "memory"); break;
        }
#pragma unroll
        for (int jn = 0; jn < 2; ++jn) {
            const int pt = ks < 24 ? OP_P : 64;
            const unsigned a0 = (ks < 24 ? st : WO_L + w * 16384 + (ks - 24) * 2048) + (4 * g + (n16 >> 2)) * pt + (16 * jn + 4 * (n16 & 3)) * 2;
            const v2u lo = trb(a0), hi = trb(a0 + 16 * pt);
            v4u v = {lo.x, lo.y, hi.x, hi.y}; asm volatile("" : "=a"(wof[ks][jn]) : "0"(as_frag(v))); }
    }
    __syncthreads();
    const v4u rd = mk_srd(p.ws, W_TOTAL), ro = mk_srd(p.out, S * D * 2), rx = mk_srd(p.Aq, S * D * 2);
    const unsigned OOB = 0xF0000000u;
    const int NT = 32, jn = w & 1;
    auto tile = [&](int i) -> unsigned { return (unsigned)(4 * i + rho); };
    const unsigned r_off = F_RDY + (rho * 32 + nt) * 128 + (l & 31) * 4;
    const unsigned a_b = W_OX + 8 * w * 4096 + l * 16;
    const int n0 = nt * 32 + 16 * jn + 4 * g;
    const unsigned c_b = W_A + (n0 >> 3) * 32768 + n16 * 16 + (g & 1) * 8, g_b = W_GATE + n16 * 4, o_b = (unsigned)((n16 * 1024 + n0) * 2);
    auto mix = [&](const v2u& av, const v2u& xv, unsigned gv, const f32x4& bs) -> v2u {
        const float gg = __uint_as_float(gv), og = rbfh(1.f - gg); float e[4];
#pragma unroll
        for (int k = 0; k < 4; ++k) { const unsigned aw = k < 2 ? av.x : av.y, xw = k < 2 ? xv.x : xv.y; e[k] = rbfh(gg * rbfh((k & 1) ? hi16(aw) * hi16(xw) : lo16(aw) * lo16(xw))) + rbfh(og * rbfh(bs[k])); }
        return (v2u){pack2(e[0], e[1]), pack2(e[2], e[3])}; };
    const unsigned s_w = ldsbase + w * 2048 + l * 16, s_r = ldsbase + jn * 1024 + l * 16;
    unsigned long long known = 0ull; int nev = 0; (void)nev;
    bool have = false, pend = false; v4u af[32]; v2u pa = {0u, 0u}, px = {0u, 0u}; unsigned pg = 0u, cf2 = 0u;
    auto finish = [&](int i, bool on) {
        v2u o = {0u, 0u};
        if (on) {
            const __attribute__((address_space(3))) f32x4* q = (const __attribute__((address_space(3))) f32x4*)(size_t)(s_r + (unsigned)(i & 1) * 8192u);
            const f32x4 bs = (q[0] + q[128]) + (q[256] + q[384]);
            o = mix(pa, px, pg, bs);
        }
        asm volatile("buffer_store_dwordx2 %0, %1, %2, 0 offen" :: "v"(o), "v"(on ? o_b + tile(i) * 32768u : OOB), "s"(ro) : "memory"); };
    if (!wait_prep(r, l, tag) || !wait_word(r, F_CONV, 32u, 256u)) ENDPGM();
#pragma unroll 1
    for (int i = 0; i < NT; ++i) {
        if (!have) {
            for (unsigned spin = 0; !((known >> i) & 1ull); ++spin) { MEMBAR();
                known = __ballot(ld4(r, r_off) == 32u);
                if ((known >> i) & 1ull) break;
                if (spin > (1u << 21)) { st4(r, F_ERR, 128u); ENDPGM(); }
                __builtin_amdgcn_s_sleep(O_SLP); }
            const unsigned a0 = a_b + tile(i) * 131072u;
#pragma unroll
            for (int q = 0; q < 8; ++q)
                asm volatile("buffer_load_dwordx4 %0, %4, %5, %6 offen sc1\n\tbuffer_load_dwordx4 %1, %4, %5, %6 offen offset:1024 sc1\n\t"
                             "buffer_load_dwordx4 %2, %4, %5, %6 offen offset:2048 sc1\n\tbuffer_load_dwordx4 %3, %4, %5, %6 offen offset:3072 sc1"
                             : "=&v"(af[4 * q]), "=&v"(af[4 * q + 1]), "=&v"(af[4 * q + 2]), "=&v"(af[4 * q + 3]) : "v"(a0), "s"(rd), "s"(q * 4096) : "memory");
            asm volatile("buffer_store_dword %0, %0, %1, 0 offen\n\tbuffer_store_dword %0, %0, %1, 0 offen" :: "v"(OOB), "s"(ro) : "memory");
        }
        const bool fin = (w >> 1) == (i & 1);
        const bool more = i + 1 < NT && ((known >> (i + 1)) & 1ull);
        const unsigned an = more ? a_b + tile(i + 1) * 131072u : OOB;
        unsigned cfl, fg; v2u fa, fx;
        asm volatile("buffer_load_dwordx2 %0, %1, %2, 0 offen" : "=&v"(fx) : "v"(fin ? o_b + tile(i) * 32768u : OOB), "s"(rx) : "memory");
        asm volatile("buffer_load_dword %0, %3, %6, 0 offen sc1\n\tbuffer_load_dwordx2 %1, %4, %6, 0 offen sc1\n\tbuffer_load_dword %2, %5, %6, 0 offen sc1"
                     : "=&v"(cfl), "=&v"(fa), "=&v"(fg) : "v"(((known >> (i + 2)) & 1ull) ? OOB : r_off), "v"(fin ? c_b + tile(i) * 256u : OOB), "v"(fin ? g_b + tile(i) * 64u : OOB), "s"(rd) : "memory");
        f32x4 acc0 = {0.f, 0.f, 0.f, 0.f}, acc1 = {0.f, 0.f, 0.f, 0.f};
        asm volatile("s_nop 1" : "+v"(acc0), "+v"(acc1));
#pragma unroll
        for (int q = 0; q < 8; ++q) {
            if (q == 2) finish(i - 1, pend);
            if (q == 6) asm volatile("buffer_load_dword %0, %1, %2, 0 offen sc1" : "+v"(cf2) : "v"(more ? OOB : r_off), "s"(rd) : "memory");
            asm volatile(
                "s_waitcnt vmcnt(37)\n\tv_mfma_f32_16x16x32_bf16 %4, %6, %0, %4\n\tv_mfma_f32_16x16x32_bf16 %5, %7, %0, %5\n\tbuffer_load_dwordx4 %0, %14, %15, %16 offen sc1\n\t"
                "s_waitcnt vmcnt(37)\n\tv_mfma_f32_16x16x32_bf16 %4, %8, %1, %4\n\tv_mfma_f32_16x16x32_bf16 %5, %9, %1, %5\n\tbuffer_load_dwordx4 %1, %14, %15, %16 offen offset:1024 sc1\n\t"
                "s_waitcnt vmcnt(37)\n\tv_mfma_f32_16x16x32_bf16 %4, %10, %2, %4\n\tv_mfma_f32_16x16x32_bf16 %5, %11, %2, %5\n\tbuffer_load_dwordx4 %2, %14, %15, %16 offen offset:2048 sc1\n\t"
                "s_waitcnt vmcnt(37)\n\tv_mfma_f32_16x16x32_bf16 %4, %12, %3, %4\n\tv_mfma_f32_16x16x32_bf16 %5, %13, %3, %5\n\tbuffer_load_dwordx4 %3, %14, %15, %16 offen offset:3072 sc1"
                : "+v"(af[4 * q]), "+v"(af[4 * q + 1]), "+v"(af[4 * q + 2]), "+v"(af[4 * q + 3]), "+v"(acc0), "+v"(acc1)
                : "a"(wof[4 * q][0]), "a"(wof[4 * q][1]), "a"(wof[4 * q + 1][0]), "a"(wof[4 * q + 1][1]), "a"(wof[4 * q + 2][0]), "a"(wof[4 * q + 2][1]), "a"(wof[4 * q + 3][0]), "a"(wof[4 * q + 3][1]),
                  "v"(an), "s"(rd), "s"(q * 4096) : "memory"); }
        const unsigned po = (unsigned)(i & 1) * 8192u;
        asm volatile("s_nop 7\n\tds_write_b128 %6, %4\n\tds_write_b128 %6, %5 offset:1024\n\ts_waitcnt vmcnt(34) lgkmcnt(0)\n\ts_barrier"
                     : "+v"(cfl), "+v"(fa), "+v"(fg), "+v"(fx) : "v"(acc0), "v"(acc1), "v"(s_w + po) : "memory");
        known |= __ballot(cfl == 32u);
        pa = fa; px = fx; pg = fg; pend = fin && more;
        if (!more) finish(i, fin);
        have = more;
        if (!more) {
            asm volatile("s_waitcnt vmcnt(1)" : "+v"(cf2), "+v"(af[0]), "+v"(af[1]), "+v"(af[2]), "+v"(af[3]), "+v"(af[4]), "+v"(af[5]), "+v"(af[6]), "+v"(af[7]), "+v"(af[8]), "+v"(af[9]), "+v"(af[10]), "+v"(af[11]),
                         "+v"(af[12]), "+v"(af[13]), "+v"(af[14]), "+v"(af[15]) :: "memory");
            known |= __ballot(cf2 == 32u);
        }
    }
    if (nt == 0 && rho == 0 && w == 0) st4(r, l == 0 ? F_EP : OOB, tag);
}

__device__ __forceinline__ void bulk_block(const Params& p, rsrc_t r, unsigned char* lds, const int wv, const int j, const unsigned tag
    ) {
    const int x = (j & 7) >> 1, i32 = (j >> 3) * 2 + (j & 1);
    auto flags = [&](int mt, int nmt, int h0, int nh, int mat) {
        DRAIN(); __syncthreads();
        const int tid = otid(); if (tid < nmt * nh) st4(r, F_KVQ + ((mt + tid / nh) * 32 + h0 + tid % nh) * 64 + mat * 4, tag); };
#pragma unroll 1
    for (int t = 0; t < 2; ++t) {
        const int mt = 2 * t + (i32 & 1), mat = (i32 >> 1) & 1, hd = 8 * x + (i32 >> 2), rot = ((i32 >> 1) & 7) * 2;
        const bf16_t* Bm = mat ? p.Wv : p.Wk;
        if (mat) gemm<1, 4, 4, 8>(r, p.Aq, Bm, mt * 128, hd * 128, lds, W_VF, wv, rot, nullptr, t == 1, t == 0 ? Bm : nullptr, (mt + 2) * 128, hd * 128);
        else gemm<0, 4, 4, 8>(r, p.Aq, Bm, mt * 128, hd * 128, lds, W_KX, wv, rot, nullptr, t == 1, t == 0 ? Bm : nullptr, (mt + 2) * 128, hd * 128);
        flags(mt, 1, hd, 1, mat);
    }
    const int hp = 4 * x + (i32 >> 3), q8 = i32 & 7, rot = ROT2 * (i32 >> 2);
    {
        const int m = q8 < 4 ? 4 + (q8 & 1) : q8 - 4, mat = q8 < 4 ? q8 >> 1 : 2;
        const bf16_t* Bn = mat == 2 ? p.Wq : mat == 1 ? p.Wv : p.Wk;
        const int mt = 4 + (q8 & 3), mv = q8 >> 2;
        const bf16_t* tk = (wv == 1 && j < 32) ? p.Aq + (64 + j) * 16384 : nullptr;
        if (mv) gemm<1, 4, 8, 4, 0, NoHook, 8>(r, p.Aq, p.Wv, mt * 128, hp * 256, lds, W_VF, wv, rot, tk, false, Bn, m * 256, hp * 256);
        else gemm<0, 4, 8, 4, 0, NoHook, 8>(r, p.Aq, p.Wk, mt * 128, hp * 256, lds, W_KX, wv, rot, tk, false, Bn, m * 256, hp * 256);
        flags(mt, 1, 2 * hp, 2, mv);
        const bf16_t* tl = (wv == 1 && j < 32) ? p.Aq + (96 + j) * 16384 : nullptr;
        if (mat == 1) gemm<1, 8, 8, 4>(r, p.Aq, Bn, m * 256, hp * 256, lds, W_VF, wv, rot, tl, true);
        else gemm<0, 8, 8, 4>(r, p.Aq, Bn, m * 256, hp * 256, lds, mat == 2 ? W_QX : W_KX, wv, rot, tl, true);
        flags(2 * m, 2, 2 * hp, 2, mat);
    }
    {
        unsigned wi = 0u;
        const int l = otid() & 63; const v4u sw = mk_srd(p.Wo, 4096 * 1024 * 2); const unsigned wa = (unsigned)((wv * 1024 + (l >> 2)) * 2048 + i32 * 64 + (l & 3) * 16);
        unsigned long long tab;
        asm volatile("s_getpc_b64 %0\n\ts_branch .Lwo%=\n\t"
                     "buffer_load_dwordx4 a[128:131], v191, s[92:95], s91 offen\n\ts_setpc_b64 s[96:97]\n\t" "buffer_load_dwordx4 a[132:135], v191, s[92:95], s91 offen\n\ts_setpc_b64 s[96:97]\n\t"
                     "buffer_load_dwordx4 a[136:139], v191, s[92:95], s91 offen\n\ts_setpc_b64 s[96:97]\n\t" "buffer_load_dwordx4 a[140:143], v191, s[92:95], s91 offen\n\ts_setpc_b64 s[96:97]\n\t"
                     "buffer_load_dwordx4 a[144:147], v191, s[92:95], s91 offen\n\ts_setpc_b64 s[96:97]\n\t" "buffer_load_dwordx4 a[148:151], v191, s[92:95], s91 offen\n\ts_setpc_b64 s[96:97]\n\t"
                     "buffer_load_dwordx4 a[152:155], v191, s[92:95], s91 offen\n\ts_setpc_b64 s[96:97]\n\t" "buffer_load_dwordx4 a[156:159], v191, s[92:95], s91 offen\n\ts_setpc_b64 s[96:97]\n\t"
                     "buffer_load_dwordx4 a[160:163], v191, s[92:95], s91 offen\n\ts_setpc_b64 s[96:97]\n\t" "buffer_load_dwordx4 a[164:167], v191, s[92:95], s91 offen\n\ts_setpc_b64 s[96:97]\n\t"
                     "buffer_load_dwordx4 a[168:171], v191, s[92:95], s91 offen\n\ts_setpc_b64 s[96:97]\n\t" "buffer_load_dwordx4 a[172:175], v191, s[92:95], s91 offen\n\ts_setpc_b64 s[96:97]\n\t"
                     "buffer_load_dwordx4 a[176:179], v191, s[92:95], s91 offen\n\ts_setpc_b64 s[96:97]\n\t" "buffer_load_dwordx4 a[180:183], v191, s[92:95], s91 offen\n\ts_setpc_b64 s[96:97]\n\t"
                     "buffer_load_dwordx4 a[184:187], v191, s[92:95], s91 offen\n\ts_setpc_b64 s[96:97]\n\t" "buffer_load_dwordx4 a[188:191], v191, s[92:95], s91 offen\n\ts_setpc_b64 s[96:97]\n\t"
                     "buffer_load_dwordx4 a[192:195], v191, s[92:95], s91 offen\n\ts_setpc_b64 s[96:97]\n\t" "buffer_load_dwordx4 a[196:199], v191, s[92:95], s91 offen\n\ts_setpc_b64 s[96:97]\n\t"
                     "buffer_load_dwordx4 a[200:203], v191, s[92:95], s91 offen\n\ts_setpc_b64 s[96:97]\n\t" "buffer_load_dwordx4 a[204:207], v191, s[92:95], s91 offen\n\ts_setpc_b64 s[96:97]\n\t"
                     "buffer_load_dwordx4 a[208:211], v191, s[92:95], s91 offen\n\ts_setpc_b64 s[96:97]\n\t" "buffer_load_dwordx4 a[212:215], v191, s[92:95], s91 offen\n\ts_setpc_b64 s[96:97]\n\t"
                     "buffer_load_dwordx4 a[216:219], v191, s[92:95], s91 offen\n\ts_setpc_b64 s[96:97]\n\t" "buffer_load_dwordx4 a[220:223], v191, s[92:95], s91 offen\n\ts_setpc_b64 s[96:97]\n\t"
                     "buffer_load_dwordx4 a[224:227], v191, s[92:95], s91 offen\n\ts_setpc_b64 s[96:97]\n\t" "buffer_load_dwordx4 a[228:231], v191, s[92:95], s91 offen\n\ts_setpc_b64 s[96:97]\n\t"
                     "buffer_load_dwordx4 a[232:235], v191, s[92:95], s91 offen\n\ts_setpc_b64 s[96:97]\n\t" "buffer_load_dwordx4 a[236:239], v191, s[92:95], s91 offen\n\ts_setpc_b64 s[96:97]\n\t"
                     "buffer_load_dwordx4 a[240:243], v191, s[92:95], s91 offen\n\ts_setpc_b64 s[96:97]\n\t" "buffer_load_dwordx4 a[244:247], v191, s[92:95], s91 offen\n\ts_setpc_b64 s[96:97]\n\t"
                     "buffer_load_dwordx4 a[248:251], v191, s[92:95], s91 offen\n\ts_setpc_b64 s[96:97]\n\t" "buffer_load_dwordx4 a[252:255], v191, s[92:95], s91 offen\n\ts_setpc_b64 s[96:97]\n\t"
                     "buffer_load_dwordx4 v[192:195], v191, s[92:95], s91 offen\n\ts_setpc_b64 s[96:97]\n\t" "buffer_load_dwordx4 v[196:199], v191, s[92:95], s91 offen\n\ts_setpc_b64 s[96:97]\n\t"
                     "buffer_load_dwordx4 v[200:203], v191, s[92:95], s91 offen\n\ts_setpc_b64 s[96:97]\n\t" "buffer_load_dwordx4 v[204:207], v191, s[92:95], s91 offen\n\ts_setpc_b64 s[96:97]\n\t"
                     "buffer_load_dwordx4 v[208:211], v191, s[92:95], s91 offen\n\ts_setpc_b64 s[96:97]\n\t" "buffer_load_dwordx4 v[212:215], v191, s[92:95], s91 offen\n\ts_setpc_b64 s[96:97]\n\t"
                     "buffer_load_dwordx4 v[216:219], v191, s[92:95], s91 offen\n\ts_setpc_b64 s[96:97]\n\t" "buffer_load_dwordx4 v[220:223], v191, s[92:95], s91 offen\n\ts_setpc_b64 s[96:97]\n\t"
                     "buffer_load_dwordx4 v[224:227], v191, s[92:95], s91 offen\n\ts_setpc_b64 s[96:97]\n\t" "buffer_load_dwordx4 v[228:231], v191, s[92:95], s91 offen\n\ts_setpc_b64 s[96:97]\n\t"
                     "buffer_load_dwordx4 v[232:235], v191, s[92:95], s91 offen\n\ts_setpc_b64 s[96:97]\n\t" "buffer_load_dwordx4 v[236:239], v191, s[92:95], s91 offen\n\ts_setpc_b64 s[96:97]\n\t"
                     "buffer_load_dwordx4 v[240:243], v191, s[92:95], s91 offen\n\ts_setpc_b64 s[96:97]\n\t" "buffer_load_dwordx4 v[244:247], v191, s[92:95], s91 offen\n\ts_setpc_b64 s[96:97]\n\t"
                     "buffer_load_dwordx4 v[248:251], v191, s[92:95], s91 offen\n\ts_setpc_b64 s[96:97]\n\t" "buffer_load_dwordx4 v[252:255], v191, s[92:95], s91 offen\n\ts_setpc_b64 s[96:97]\n\t"
                     "buffer_load_dwordx4 v191, s[92:95], s91 offen lds\n\ts_setpc_b64 s[96:97]\n.Lwo%=:" : "=s"(tab));
        auto hook = [&](int) {
            const unsigned q = min(wi, 63u);
            asm volatile("s_mov_b32 m0, %1\n\ts_swappc_b64 s[96:97], %0"
                         :: "s"(tab + (min(q, 48u) * 12u + 4u)), "s"(WO_L + wv * 16384 + (q - 48u) * 1024u), "{v191}"(wa), "{s[92:95]}"(sw), "{s91}"(q * 32768u) : "memory", "m0", "s96", "s97", WO_REGS);
            ++wi; };
#pragma unroll 1
        for (int t = 0; t < 2; ++t) {
            const int mt = t ? 8 + q8 : 12 + (q8 & 3), mv = t ? 2 : q8 >> 2;
            if (mv == 1) gemm<1, 4, 8, 4, 1>(r, p.Aq, p.Wv, mt * 128, hp * 256, lds, W_VF, wv, rot, nullptr, false, p.Wq, (8 + q8) * 128, hp * 256, hook);
            else gemm<0, 4, 8, 4, 1>(r, p.Aq, t ? p.Wq : p.Wk, mt * 128, hp * 256, lds, t ? W_QX : W_KX, wv, rot, nullptr, t == 1, t ? nullptr : p.Wq, (8 + q8) * 128, hp * 256, hook);
            flags(mt, 1, 2 * hp, 2, mv);
        }
        oproj_block(p, r, lds, wv, i32, x, j, tag);
    }
}

__device__ __forceinline__ void help_block(const Params& p, rsrc_t r, unsigned char* lds, const int wv, const int k, const unsigned tag) {
    const int j = -32 + k; (void)j;
    if (HELP_D) wait_long(rtnow() + HELP_D);
    prep_block(p, r, k, wv, tag);
    if (!conv_block(p, r, k, lds, wv, j, tag)) return;
    attn_block(p, r, lds, wv, k, j, tag);
}

__global__ void __launch_bounds__(256, 1) k_main(Params p) {
    extern __shared__ __attribute__((aligned(16))) unsigned char lds[];
    rsrc_t r = __builtin_amdgcn_make_buffer_rsrc(p.ws, 0, W_TOTAL, 0x00020000);
    const int wv = __builtin_amdgcn_readfirstlane(threadIdx.x >> 6);
    int role = blockIdx.x;
    unsigned xcc; asm volatile("s_getreg_b32 %0, hwreg(HW_REG_XCC_ID)" : "=s"(xcc)); xcc &= 7u;
    unsigned near = 0u, old = 0u, slot = (xcc >> 2) * 128u + (blockIdx.x >> 4) * 8u + (xcc & 3u) * 2u + ((blockIdx.x >> 3) & 1u);
    const unsigned tag = __builtin_amdgcn_readfirstlane(ld4(r, (threadIdx.x & 63) == 1 ? F_CLM + slot * 64 + 4 : F_EP)) * 2654435761u + 0x9E3779B9u;
    if (threadIdx.x == 0) old = __hip_atomic_exchange((unsigned*)(p.ws + F_CLM + slot * 64), tag, __ATOMIC_RELAXED, __HIP_MEMORY_SCOPE_AGENT);
    near = 126u;
    if (xcc < 4u && ((CALM >> (slot >> 5)) & 1u)) {
        const unsigned gr = (slot >> 5) & 3u;
        const rsrc_t rh = __builtin_amdgcn_make_buffer_rsrc(p.ws + hx_base(p.ws), 0, U_END * 16384, 0x00020000);
        const unsigned ca = (threadIdx.x & 63) != 0 ? 0xF0000000u : (slot & 127u) * 16384u + (unsigned)wv * 2048u + CAL_O;
        const v4u z = {~0u, ~0u, ~0u, ~0u}; unsigned best0 = ~0u, best1 = ~0u;
        for (int i = 0; i < CAL_N; ++i) {
            const unsigned t0 = (unsigned)__builtin_amdgcn_s_memtime(); __builtin_amdgcn_s_waitcnt(0);
            st16(rh, ca, z); DRAIN();
            const unsigned t1 = (unsigned)__builtin_amdgcn_s_memtime(); __builtin_amdgcn_s_waitcnt(0);
            st16(rh, ca + 8192, z); DRAIN();
            const unsigned t2 = (unsigned)__builtin_amdgcn_s_memtime(); __builtin_amdgcn_s_waitcnt(0);
            best0 = min(best0, t1 - t0); best1 = min(best1, t2 - t1); }
        near = gr * 2u + (best1 < best0 ? 1u : 0u);
    }
    if (threadIdx.x == 0) {
        for (int i = 0; i < 256 && old == tag; ++i) { slot = (slot + 1u) & 255u; old = __hip_atomic_exchange((unsigned*)(p.ws + F_CLM + slot * 64), tag, __ATOMIC_RELAXED, __HIP_MEMORY_SCOPE_AGENT); }
        *(volatile __attribute__((address_space(3))) int*)(size_t)(unsigned)(size_t)lds = (int)slot;
    }
    __syncthreads();
    role = __builtin_amdgcn_readfirstlane(*(volatile __attribute__((address_space(3))) int*)(size_t)(unsigned)(size_t)lds) & 255;
    __syncthreads();
    if (role >= 128) bulk_block(p, r, lds, wv, role - 128, tag
        ); else if (rec_block(p, r, lds, wv, role, near, tag)) help_block(p, r, lds, wv, (role >> 3) * 2 + (role & 1), tag);
}

extern "C" void kernel_launch(void* const* d_in, const int* in_sizes, int n_in, void* d_out, int out_size, void* d_ws, size_t ws_size, hipStream_t stream) {
    Params p;
    p.Aq = (const bf16_t*)d_in[0]; p.Ak = (const bf16_t*)d_in[1]; p.Av = (const bf16_t*)d_in[2];
    p.shift_k = (const bf16_t*)d_in[3]; p.ssm_k = (const bf16_t*)d_in[4]; p.ssm_D = (const bf16_t*)d_in[5];
    p.mem0 = (const bf16_t*)d_in[7]; p.Wq = (const bf16_t*)d_in[8]; p.Wk = (const bf16_t*)d_in[9];
    p.Wv = (const bf16_t*)d_in[10]; p.Wo = (const bf16_t*)d_in[11]; p.gate_w = (const bf16_t*)d_in[13];
    p.out = (bf16_t*)d_out; p.ws = (unsigned char*)d_ws;
    (void)hipFuncSetAttribute((const void*)k_main, hipFuncAttributeMaxDynamicSharedMemorySize, L_TOTAL);
    k_main<<<256, 256, L_TOTAL, stream>>>(p);
}
```

```cpp
#include <hip/hip_runtime.h>
#include <stdint.h>
#include <type_traits>
typedef unsigned short bf16_t;
using bf16x8 = __attribute__((ext_vector_type(8))) short;
using f32x4 = __attribute__((ext_vector_type(4))) float;
typedef unsigned v4u __attribute__((ext_vector_type(4)));
typedef unsigned v2u __attribute__((ext_vector_type(2)));
typedef __amdgpu_buffer_rsrc_t rsrc_t;

typedef __bf16 hbf2 __attribute__((ext_vector_type(2)));
typedef float f32x2 __attribute__((ext_vector_type(2)));
__device__ __forceinline__ unsigned pack2(float a, float b) { f32x2 v = {a, b}; return __builtin_bit_cast(unsigned, __builtin_convertvector(v, hbf2)); }
__device__ __forceinline__ float rbfh(float f) { return __uint_as_float(pack2(0.f, f)); }
__device__ __forceinline__ f32x2 rbf2(f32x2 f) { return (f32x2){rbfh(f.x), rbfh(f.y)}; }
__device__ __forceinline__ unsigned nosent(unsigned x) {
    typedef unsigned short us2 __attribute__((ext_vector_type(2)));
    const us2 a = __builtin_bit_cast(us2, x), c = {(unsigned short)0xFFFE, (unsigned short)0xFFFE}; return __builtin_bit_cast(unsigned, __builtin_elementwise_min(a, c)); }

constexpr int S = 2048, D = 1024, H = 32, DH = 128, M = 8, SEG = 32, NSEG = 64;

constexpr unsigned W_FLG = 0;
constexpr unsigned F_KVQ = W_FLG;
constexpr unsigned F_EP = W_FLG + 32768;
constexpr unsigned F_ERR = W_FLG + 32768 + 256;
constexpr unsigned F_PREP = W_FLG + 32768 + 512;
constexpr unsigned F_CAL = W_FLG + 73728;
constexpr unsigned F_CLM = W_FLG + 77824;
constexpr unsigned F_CONV = W_FLG + 33792;
constexpr unsigned F_PROG = W_FLG + 36864;
constexpr unsigned F_ATT = W_FLG + 40960;
constexpr unsigned F_RDY = W_FLG + 49152;
constexpr unsigned FLG_BYTES = 94208;
constexpr unsigned HO_KVM = W_FLG + FLG_BYTES;
constexpr unsigned W_A = HO_KVM + 64 * 32 * 4096;
constexpr unsigned W_GATE = W_A + S * D * 2;
constexpr unsigned W_QX = W_GATE + 8192;
constexpr unsigned W_KX = W_QX + S * 4096 * 2;
constexpr unsigned W_VF = W_KX + S * 4096 * 2;
constexpr unsigned W_OX = W_VF + 64 * 32 * 8192;
constexpr unsigned DBG_BYTES = 0;
constexpr unsigned W_DBG = W_OX + S * 4096 * 2;
constexpr unsigned W_U = W_DBG + DBG_BYTES;
constexpr unsigned W_TOTAL = W_U + S * D * 2;

__device__ __forceinline__ v4u ld16(rsrc_t r, unsigned off) { return __builtin_amdgcn_raw_buffer_load_b128(r, off, 0, 16); }
__device__ __forceinline__ v2u ld8(rsrc_t r, unsigned off) { return __builtin_amdgcn_raw_buffer_load_b64(r, off, 0, 16); }
__device__ __forceinline__ unsigned ld4(rsrc_t r, unsigned off) { return __builtin_amdgcn_raw_buffer_load_b32(r, off, 0, 16); }
__device__ __forceinline__ void st16(rsrc_t r, unsigned off, v4u v) { __builtin_amdgcn_raw_buffer_store_b128(v, r, off, 0, 16); }
__device__ __forceinline__ void st8(rsrc_t r, unsigned off, v2u v) { __builtin_amdgcn_raw_buffer_store_b64(v, r, off, 0, 16); }
__device__ __forceinline__ void st4(rsrc_t r, unsigned off, unsigned v) { __builtin_amdgcn_raw_buffer_store_b32(v, r, off, 0, 16); }
__device__ __forceinline__ bf16x8 as_frag(v4u v) { return __builtin_bit_cast(bf16x8, v); }
#define D1 134
#define D2 90
#define D3 64
#define LB0 16
#define LC0 7
#define LA0 8
#define TR_UP 4
#define TR_DN 2
#define L_DEC 3
#define STAG 1
#define UP_SH 2
#define EARLY_FROM 58
#define OUT_WARM 0
#define AROT 0
#define P_SLP 2
#define O_SLP 2
#define W_NT 0
#define TOL 2
#define A_SLP 0
#define PEN2 20
#define A_UP 24
#define CT0 2
#define CPF 1
#define PH_M 3
#define SKEW 0
#define SKEW1 0
#define XT 1
#define TRK 3
#define WO_NT 0
#define WO_TRK 1
#define WIN 3
#define ROT0 3
#define ROT1 2
#define ROT2 2
#define B_NT 0
#define CALM 15
#define R4M 1
#define HX_SH 0u
#define FINE_T 1
#define FINE_N 0
#define FINE_A 0
#define O_ND 12
#define PEN0 32
#define X0C 20
#define X0A 16
#define X1A 8
#define PEN 24
#define MEMBAR() asm volatile("" ::: "memory")
#define ENDPGM() asm volatile("s_endpgm" ::: "memory")
#define DRAIN() asm volatile("s_waitcnt vmcnt(0)" ::: "memory")

__device__ __forceinline__ float xor16_max(float x) { auto r = __builtin_amdgcn_permlane16_swap(__float_as_uint(x), __float_as_uint(x), false, false); return fmaxf(__uint_as_float(r[0]), __uint_as_float(r[1])); }
__device__ __forceinline__ float xor32_max(float x) { auto r = __builtin_amdgcn_permlane32_swap(__float_as_uint(x), __float_as_uint(x), false, false); return fmaxf(__uint_as_float(r[0]), __uint_as_float(r[1])); }
__device__ __forceinline__ float xor16_add(float x) { auto r = __builtin_amdgcn_permlane16_swap(__float_as_uint(x), __float_as_uint(x), false, false); return __uint_as_float(r[0]) + __uint_as_float(r[1]); }
__device__ __forceinline__ float xor32_add(float x) { auto r = __builtin_amdgcn_permlane32_swap(__float_as_uint(x), __float_as_uint(x), false, false); return __uint_as_float(r[0]) + __uint_as_float(r[1]); }

typedef short v4i16_t __attribute__((ext_vector_type(4)));
__device__ __forceinline__ v2u trb(unsigned lds_addr) { return __builtin_bit_cast(v2u, __builtin_amdgcn_ds_read_tr16_b64_v4i16((__attribute__((address_space(3))) v4i16_t*)(size_t)lds_addr)); }
__device__ __forceinline__ int otid_(int wv) { int t; asm volatile("v_mbcnt_lo_u32_b32 %0, -1, 0\n\tv_mbcnt_hi_u32_b32 %0, -1, %0" : "=v"(t)); return wv * 64 + t; }
#define otid() otid_(wv)
__device__ __forceinline__ bool wait_word(rsrc_t r, unsigned off, unsigned want, unsigned code) {
    for (unsigned spin = 0;; ++spin) { MEMBAR();
        const unsigned v = __builtin_amdgcn_readfirstlane(ld4(r, off));
        if (v >= want) return true;
        if (spin > (1u << 20)) { st4(r, F_ERR, code); return false; }
        __builtin_amdgcn_s_sleep(8); }
}

__device__ __forceinline__ bool wait_prep(rsrc_t r, int l, unsigned tag) {
    for (unsigned spin = 0;; ++spin) { MEMBAR();
        const unsigned v = ld4(r, F_PREP + (l & 31) * 4);
        if (__all(v == tag)) return true;
        if (spin > (1u << 20)) { st4(r, F_ERR, 512u); return false; }
        __builtin_amdgcn_s_sleep(8); }
}
__device__ __forceinline__ bool wait_cal(rsrc_t r, int l, unsigned tag, v4u (&q)[4]) {
    for (unsigned spin = 0;; ++spin) { MEMBAR();
        bool ok = true;
#pragma unroll
        for (int i = 0; i < 4; ++i) { q[i] = ld16(r, F_CAL + l * 64 + i * 16); }
#pragma unroll
        for (int i = 0; i < 4; ++i) ok &= (q[i].x == tag) & (q[i].z == tag);
        if (__all(ok)) return true;
        if (spin > (1u << 20)) { st4(r, F_ERR, 8u); return false; }
        __builtin_amdgcn_s_sleep(4); }
}
__device__ __forceinline__ unsigned rtnow() { return (unsigned)__builtin_amdgcn_s_memrealtime(); }
__device__ __forceinline__ void wait_long(unsigned target) { for (int i = 0; i < 4096; ++i) { if ((int)(rtnow() - target) >= 0) break; __builtin_amdgcn_s_sleep(8); } }
constexpr int L_TAB = 20480;
constexpr int L_QK = 24576;
__device__ __forceinline__ unsigned img(int f) { return (unsigned)(L_QK + f * 16 + (f >> 4) * 128); }
constexpr int L_FL = 30720, L_Z = L_FL + 128, L_LOG = 31744, L_W = 32768;
constexpr int L_TOTAL = 163840;
constexpr unsigned U_END = 128;
#define PMIN 76
#define V_SLP 2
#define R_SLP 1
constexpr unsigned WO_L = 98304;
#define HELP_D 800
#define V_DEL 16
#define XR 12
#define PFW 0
#define KEYM 3
#define CAL_O 0u
#define H_SLP 0
#define CAL_N 2
#define STEP_MIN 180u

struct Params {
    const bf16_t *Aq, *Ak, *Av, *shift_k, *ssm_k, *ssm_D, *mem0, *Wq, *Wk, *Wv, *Wo, *gate_w;
    bf16_t* out; unsigned char* ws;
};
__host__ __device__ __forceinline__ unsigned hx_base(const void* ws) { const size_t a = (size_t)ws; return (unsigned)((((a + W_TOTAL + 0x7FFFFFu) >> 23) << 23) - a) + HX_SH; }

typedef short v4s __attribute__((ext_vector_type(4)));
typedef unsigned v32u __attribute__((ext_vector_type(32)));
typedef unsigned v16u __attribute__((ext_vector_type(16)));
typedef unsigned v8u __attribute__((ext_vector_type(8)));
typedef unsigned v12u __attribute__((ext_vector_type(12)));
__device__ __forceinline__ v4u mk_srd(const void* p, unsigned bytes) { const size_t a = (size_t)p; v4u d;
    d.x = __builtin_amdgcn_readfirstlane((unsigned)a); d.y = __builtin_amdgcn_readfirstlane((unsigned)(a >> 32) & 0xFFFFu); d.z = __builtin_amdgcn_readfirstlane(bytes); d.w = 0x00020000u; return d; }
__device__ __forceinline__ bf16x8 ldsa(unsigned addr) { return *(const __attribute__((address_space(3))) bf16x8*)(size_t)addr; }
__device__ __forceinline__ bool rec_block(const Params& p, rsrc_t r, unsigned char* lds, const int wv, const int b, const unsigned near, const unsigned tag) {
    const int w = wv;
    int tid = otid(), l = tid & 63, n16 = l & 15, g = l >> 4;
    const int x = (b & 7) >> 1, i32 = (b >> 3) * 2 + (b & 1);
    const unsigned hxo = hx_base(p.ws), OOB = 0xF0000000u;
    const v4u rhd = mk_srd(p.ws + hxo, U_END * 16384), wsd = mk_srd(p.ws, W_TOTAL);
    const rsrc_t rh = __builtin_amdgcn_make_buffer_rsrc(p.ws + hxo, 0, U_END * 16384, 0x00020000);
    const int G = x - 1, vi = G * 32 + i32, h = x == 0 ? i32 : vi >> 1;
    const unsigned pr_a = x == 3 ? OOB : F_PROG + h * 128 + (x == 0 ? 0 : 8 + (vi & 1) * 8);
    {
        const v4u ones = {0xFFFFFFFFu, 0xFFFFFFFFu, 0xFFFFFFFFu, 0xFFFFFFFFu};
#pragma unroll
        for (int i = 0; i < 4; ++i) st16(rh, b * 16384 + (w * 4 + i) * 1024 + l * 16, ones);
        st4(r, tid == 192 ? pr_a : OOB, x ? 63u : 0u);
    }
    const unsigned cal_a = l == 0 ? F_CAL + b * 32 + w * 8 : OOB;
    if (x == 3) { DRAIN(); st8(r, cal_a, (v2u){tag, near}); return true; }
    v4u cpf;
    if (CPF) {
        unsigned lo, hi, l2; asm volatile("s_getpc_b64 s[98:99]\n.Lpc%=:\n\ts_add_u32 %2, s98, rec_v_code-.Lpc%=\n\ts_add_u32 %0, s98, rec_s_code-.Lpc%=\n\ts_addc_u32 %1, s99, 0" : "=s"(lo), "=s"(hi), "=s"(l2) :: "s98", "s99", "scc");
        const size_t a = (((size_t)hi << 32) | lo) + (size_t)(long long)(x ? (int)(l2 - lo) : 0) + (size_t)(((i32 * 4 + w) % 6) * 1024 + l * 16);
        asm volatile("global_load_dwordx4 %0, %1, off" : "=v"(cpf) : "v"(a & ~(size_t)15) : "memory");
    }
    const int c = n16 >> 2;
    v32u wa[8], wg[4];
    auto votes = [&](unsigned& nm) -> bool {
        v4u q[4]; if (!wait_cal(r, l, tag, q)) return false;
        typedef __attribute__((address_space(3))) int li;
        li* cnt = (li*)(size_t)(w * 256);
        cnt[l] = 0;
        __builtin_amdgcn_wave_barrier();
#pragma unroll
        for (int i = 0; i < 8; ++i) { const unsigned y = (i & 1) ? q[i >> 1].w : q[i >> 1].y; __hip_atomic_fetch_add(cnt + ((y >> 1) & 63u), (y & 1u) ? 1 : -1, __ATOMIC_RELAXED, __HIP_MEMORY_SCOPE_WAVEFRONT); }
        __builtin_amdgcn_wave_barrier();
        const int c0 = cnt[l], c4 = cnt[0] + cnt[1] + cnt[2] + cnt[3];
        nm = (unsigned)__ballot((abs(c0) >= 64 ? c0 : c4) > 0);
        return true; };
    typedef __attribute__((address_space(3))) volatile unsigned lu32;
    const unsigned kvo = HO_KVM + h * 4096 + (x ? 2048 : 0);
    if (x == 0) {
        {
            const v4u sq = mk_srd(p.Wq, 1024 * 4096 * 2), sk = mk_srd(p.Wk, 1024 * 4096 * 2);
            const unsigned m0b = L_W + w * 1024, f = (c & 1) | ((g & 1) << 1) | ((c >> 1) << 2);
            const unsigned t0 = L_W + (16 * (g >> 1) + 4 * (g & 1) + (c & 1) + 8 * (c >> 1)) * 256 + (((2 * w) ^ f) << 5) + (n16 & 3) * 8;
            v16u pa;
            pa[0] = (4 * w + g) * 8192 + h * 256 + ((n16 ^ (((g & 1) | (w << 1)) << 1)) << 4);
#pragma unroll
            for (int q = 0; q < 8; ++q) pa[1 + q] = (t0 + ((w + (q >> 1)) & 3) * 32768) ^ ((q & 1) << 5);
            pa[9] = cal_a; pa[10] = tag; pa[11] = near; pa[12] = L_W + w * 32768 + l * 16; pa[13] = 0u; pa[14] = 0u; pa[15] = 0u;
    asm volatile(
        "s_add_u32 s80, %[m0b], 0\n\t"
        "s_mov_b32 s81, 0\n\t"
        "s_mov_b32 s82, 4\n\t"
        ".Ld0%=:\n\t"
        "s_mov_b32 m0, s80\n\t"
        "s_add_u32 s80, s80, 0x8000\n\t"
        "buffer_load_dwordx4 v112, %[sq], s81 offen lds\n\t"
        "s_add_u32 m0, m0, 0x1000\n\t"
        "s_add_u32 s81, s81, 0x20000\n\t"
        "buffer_load_dwordx4 v112, %[sq], s81 offen lds\n\t"
        "s_add_u32 m0, m0, 0x1000\n\t"
        "s_add_u32 s81, s81, 0x20000\n\t"
        "buffer_load_dwordx4 v112, %[sq], s81 offen lds\n\t"
        "s_add_u32 m0, m0, 0x1000\n\t"
        "s_add_u32 s81, s81, 0x20000\n\t"
        "buffer_load_dwordx4 v112, %[sq], s81 offen lds\n\t"
        "s_add_u32 m0, m0, 0x1000\n\t"
        "s_add_u32 s81, s81, 0x20000\n\t"
        "s_sub_u32 s82, s82, 1\n\t"
        "s_cmp_lg_u32 s82, 0\n\t"
        "s_cbranch_scc1 .Ld0%=\n\t"
        "s_add_u32 s80, %[m0b], 16384\n\t"
        "s_mov_b32 s81, 0\n\t"
        "s_mov_b32 s82, 4\n\t"
        ".Ld1%=:\n\t"
        "s_mov_b32 m0, s80\n\t"
        "s_add_u32 s80, s80, 0x8000\n\t"
        "buffer_load_dwordx4 v112, %[sk], s81 offen lds\n\t"
        "s_add_u32 m0, m0, 0x1000\n\t"
        "s_add_u32 s81, s81, 0x20000\n\t"
        "buffer_load_dwordx4 v112, %[sk], s81 offen lds\n\t"
        "s_add_u32 m0, m0, 0x1000\n\t"
        "s_add_u32 s81, s81, 0x20000\n\t"
        "buffer_load_dwordx4 v112, %[sk], s81 offen lds\n\t"
        "s_add_u32 m0, m0, 0x1000\n\t"
        "s_add_u32 s81, s81, 0x20000\n\t"
        "buffer_load_dwordx4 v112, %[sk], s81 offen lds\n\t"
        "s_add_u32 m0, m0, 0x1000\n\t"
        "s_add_u32 s81, s81, 0x20000\n\t"
        "s_sub_u32 s82, s82, 1\n\t"
        "s_cmp_lg_u32 s82, 0\n\t"
        "s_cbranch_scc1 .Ld1%=\n\t"
        "s_waitcnt vmcnt(32)\n\t"
        "buffer_store_dwordx2 v[122:123], v121, %[wsd], 0 offen sc1\n\t"
        "s_waitcnt vmcnt(17)\n\t"
        "s_barrier\n\t"
        "ds_read_b64_tr_b16 v[0:1], v113 offset:0\n\t"
        "ds_read_b64_tr_b16 v[2:3], v113 offset:512\n\t"
        "ds_read_b64_tr_b16 v[4:5], v113 offset:8192\n\t"
        "ds_read_b64_tr_b16 v[6:7], v113 offset:8704\n\t"
        "ds_read_b64_tr_b16 v[8:9], v114 offset:0\n\t"
        "ds_read_b64_tr_b16 v[10:11], v114 offset:512\n\t"
        "ds_read_b64_tr_b16 v[12:13], v114 offset:8192\n\t"
        "ds_read_b64_tr_b16 v[14:15], v114 offset:8704\n\t"
        "ds_read_b64_tr_b16 a[16:17], v115 offset:0\n\t"
        "ds_read_b64_tr_b16 a[18:19], v115 offset:512\n\t"
        "ds_read_b64_tr_b16 a[20:21], v115 offset:8192\n\t"
        "ds_read_b64_tr_b16 a[22:23], v115 offset:8704\n\t"
        "ds_read_b64_tr_b16 a[24:25], v116 offset:0\n\t"
        "ds_read_b64_tr_b16 a[26:27], v116 offset:512\n\t"
        "ds_read_b64_tr_b16 a[28:29], v116 offset:8192\n\t"
        "ds_read_b64_tr_b16 a[30:31], v116 offset:8704\n\t"
        "ds_read_b64_tr_b16 a[32:33], v117 offset:0\n\t"
        "ds_read_b64_tr_b16 a[34:35], v117 offset:512\n\t"
        "ds_read_b64_tr_b16 a[36:37], v117 offset:8192\n\t"
        "ds_read_b64_tr_b16 a[38:39], v117 offset:8704\n\t"
        "ds_read_b64_tr_b16 a[40:41], v118 offset:0\n\t"
        "ds_read_b64_tr_b16 a[42:43], v118 offset:512\n\t"
        "ds_read_b64_tr_b16 a[44:45], v118 offset:8192\n\t"
        "ds_read_b64_tr_b16 a[46:47], v118 offset:8704\n\t"
        "ds_read_b64_tr_b16 a[48:49], v119 offset:0\n\t"
        "ds_read_b64_tr_b16 a[50:51], v119 offset:512\n\t"
        "ds_read_b64_tr_b16 a[52:53], v119 offset:8192\n\t"
        "ds_read_b64_tr_b16 a[54:55], v119 offset:8704\n\t"
        "ds_read_b64_tr_b16 a[56:57], v120 offset:0\n\t"
        "ds_read_b64_tr_b16 a[58:59], v120 offset:512\n\t"
        "ds_read_b64_tr_b16 a[60:61], v120 offset:8192\n\t"
        "ds_read_b64_tr_b16 a[62:63], v120 offset:8704\n\t"
        "s_waitcnt lgkmcnt(0)\n\t"
        "s_barrier\n\t"
        "s_add_u32 s80, %[m0b], 0\n\t"
        "s_mov_b32 s81, 2097152\n\t"
        "s_mov_b32 s82, 4\n\t"
        ".Ld2%=:\n\t"
        "s_mov_b32 m0, s80\n\t"
        "s_add_u32 s80, s80, 0x8000\n\t"
        "buffer_load_dwordx4 v112, %[sq], s81 offen lds\n\t"
        "s_add_u32 m0, m0, 0x1000\n\t"
        "s_add_u32 s81, s81, 0x20000\n\t"
        "buffer_load_dwordx4 v112, %[sq], s81 offen lds\n\t"
        "s_add_u32 m0, m0, 0x1000\n\t"
        "s_add_u32 s81, s81, 0x20000\n\t"
        "buffer_load_dwordx4 v112, %[sq], s81 offen lds\n\t"
        "s_add_u32 m0, m0, 0x1000\n\t"
        "s_add_u32 s81, s81, 0x20000\n\t"
        "buffer_load_dwordx4 v112, %[sq], s81 offen lds\n\t"
        "s_add_u32 m0, m0, 0x1000\n\t"
        "s_add_u32 s81, s81, 0x20000\n\t"
        "s_sub_u32 s82, s82, 1\n\t"
        "s_cmp_lg_u32 s82, 0\n\t"
        "s_cbranch_scc1 .Ld2%=\n\t"
        "s_waitcnt vmcnt(17)\n\t"
        "s_barrier\n\t"
        "ds_read_b64_tr_b16 v[16:17], v113 offset:16384\n\t"
        "ds_read_b64_tr_b16 v[18:19], v113 offset:16896\n\t"
        "ds_read_b64_tr_b16 v[20:21], v113 offset:24576\n\t"
        "ds_read_b64_tr_b16 v[22:23], v113 offset:25088\n\t"
        "ds_read_b64_tr_b16 v[24:25], v114 offset:16384\n\t"
        "ds_read_b64_tr_b16 v[26:27], v114 offset:16896\n\t"
        "ds_read_b64_tr_b16 v[28:29], v114 offset:24576\n\t"
        "ds_read_b64_tr_b16 v[30:31], v114 offset:25088\n\t"
        "ds_read_b64_tr_b16 a[80:81], v115 offset:16384\n\t"
        "ds_read_b64_tr_b16 a[82:83], v115 offset:16896\n\t"
        "ds_read_b64_tr_b16 a[84:85], v115 offset:24576\n\t"
        "ds_read_b64_tr_b16 a[86:87], v115 offset:25088\n\t"
        "ds_read_b64_tr_b16 a[88:89], v116 offset:16384\n\t"
        "ds_read_b64_tr_b16 a[90:91], v116 offset:16896\n\t"
        "ds_read_b64_tr_b16 a[92:93], v116 offset:24576\n\t"
        "ds_read_b64_tr_b16 a[94:95], v116 offset:25088\n\t"
        "ds_read_b64_tr_b16 a[96:97], v117 offset:16384\n\t"
        "ds_read_b64_tr_b16 a[98:99], v117 offset:16896\n\t"
        "ds_read_b64_tr_b16 a[100:101], v117 offset:24576\n\t"
        "ds_read_b64_tr_b16 a[102:103], v117 offset:25088\n\t"
        "ds_read_b64_tr_b16 a[104:105], v118 offset:16384\n\t"
        "ds_read_b64_tr_b16 a[106:107], v118 offset:16896\n\t"
        "ds_read_b64_tr_b16 a[108:109], v118 offset:24576\n\t"
        "ds_read_b64_tr_b16 a[110:111], v118 offset:25088\n\t"
        "ds_read_b64_tr_b16 a[112:113], v119 offset:16384\n\t"
        "ds_read_b64_tr_b16 a[114:115], v119 offset:16896\n\t"
        "ds_read_b64_tr_b16 a[116:117], v119 offset:24576\n\t"
        "ds_read_b64_tr_b16 a[118:119], v119 offset:25088\n\t"
        "ds_read_b64_tr_b16 a[120:121], v120 offset:16384\n\t"
        "ds_read_b64_tr_b16 a[122:123], v120 offset:16896\n\t"
        "ds_read_b64_tr_b16 a[124:125], v120 offset:24576\n\t"
        "ds_read_b64_tr_b16 a[126:127], v120 offset:25088\n\t"
        "s_waitcnt lgkmcnt(0)\n\t"
        "s_barrier\n\t"
        "s_add_u32 s80, %[m0b], 16384\n\t"
        "s_mov_b32 s81, 2097152\n\t"
        "s_mov_b32 s82, 4\n\t"
        ".Ld3%=:\n\t"
        "s_mov_b32 m0, s80\n\t"
        "s_add_u32 s80, s80, 0x8000\n\t"
        "buffer_load_dwordx4 v112, %[sk], s81 offen lds\n\t"
        "s_add_u32 m0, m0, 0x1000\n\t"
        "s_add_u32 s81, s81, 0x20000\n\t"
        "buffer_load_dwordx4 v112, %[sk], s81 offen lds\n\t"
        "s_add_u32 m0, m0, 0x1000\n\t"
        "s_add_u32 s81, s81, 0x20000\n\t"
        "buffer_load_dwordx4 v112, %[sk], s81 offen lds\n\t"
        "s_add_u32 m0, m0, 0x1000\n\t"
        "s_add_u32 s81, s81, 0x20000\n\t"
        "buffer_load_dwordx4 v112, %[sk], s81 offen lds\n\t"
        "s_add_u32 m0, m0, 0x1000\n\t"
        "s_add_u32 s81, s81, 0x20000\n\t"
        "s_sub_u32 s82, s82, 1\n\t"
        "s_cmp_lg_u32 s82, 0\n\t"
        "s_cbranch_scc1 .Ld3%=\n\t"
        "s_waitcnt vmcnt(16)\n\t"
        "s_barrier\n\t"
        "ds_read_b64_tr_b16 v[32:33], v113 offset:0\n\t"
        "ds_read_b64_tr_b16 v[34:35], v113 offset:512\n\t"
        "ds_read_b64_tr_b16 v[36:37], v113 offset:8192\n\t"
        "ds_read_b64_tr_b16 v[38:39], v113 offset:8704\n\t"
        "ds_read_b64_tr_b16 v[40:41], v114 offset:0\n\t"
        "ds_read_b64_tr_b16 v[42:43], v114 offset:512\n\t"
        "ds_read_b64_tr_b16 v[44:45], v114 offset:8192\n\t"
        "ds_read_b64_tr_b16 v[46:47], v114 offset:8704\n\t"
        "ds_read_b64_tr_b16 a[144:145], v115 offset:0\n\t"
        "ds_read_b64_tr_b16 a[146:147], v115 offset:512\n\t"
        "ds_read_b64_tr_b16 a[148:149], v115 offset:8192\n\t"
        "ds_read_b64_tr_b16 a[150:151], v115 offset:8704\n\t"
        "ds_read_b64_tr_b16 a[152:153], v116 offset:0\n\t"
        "ds_read_b64_tr_b16 a[154:155], v116 offset:512\n\t"
        "ds_read_b64_tr_b16 a[156:157], v116 offset:8192\n\t"
        "ds_read_b64_tr_b16 a[158:159], v116 offset:8704\n\t"
        "ds_read_b64_tr_b16 a[160:161], v117 offset:0\n\t"
        "ds_read_b64_tr_b16 a[162:163], v117 offset:512\n\t"
        "ds_read_b64_tr_b16 a[164:165], v117 offset:8192\n\t"
        "ds_read_b64_tr_b16 a[166:167], v117 offset:8704\n\t"
        "ds_read_b64_tr_b16 a[168:169], v118 offset:0\n\t"
        "ds_read_b64_tr_b16 a[170:171], v118 offset:512\n\t"
        "ds_read_b64_tr_b16 a[172:173], v118 offset:8192\n\t"
        "ds_read_b64_tr_b16 a[174:175], v118 offset:8704\n\t"
        "ds_read_b64_tr_b16 a[176:177], v119 offset:0\n\t"
        "ds_read_b64_tr_b16 a[178:179], v119 offset:512\n\t"
        "ds_read_b64_tr_b16 a[180:181], v119 offset:8192\n\t"
        "ds_read_b64_tr_b16 a[182:183], v119 offset:8704\n\t"
        "ds_read_b64_tr_b16 a[184:185], v120 offset:0\n\t"
        "ds_read_b64_tr_b16 a[186:187], v120 offset:512\n\t"
        "ds_read_b64_tr_b16 a[188:189], v120 offset:8192\n\t"
        "ds_read_b64_tr_b16 a[190:191], v120 offset:8704\n\t"
        "s_waitcnt lgkmcnt(0)\n\t"
        "s_barrier\n\t"
        "s_add_u32 s80, %[m0b], 0\n\t"
        "s_mov_b32 s81, 4194304\n\t"
        "s_mov_b32 s82, 4\n\t"
        ".Ld4%=:\n\t"
        "s_mov_b32 m0, s80\n\t"
        "s_add_u32 s80, s80, 0x8000\n\t"
        "buffer_load_dwordx4 v112, %[sq], s81 offen lds\n\t"
        "s_add_u32 m0, m0, 0x1000\n\t"
        "s_add_u32 s81, s81, 0x20000\n\t"
        "buffer_load_dwordx4 v112, %[sq], s81 offen lds\n\t"
        "s_add_u32 m0, m0, 0x1000\n\t"
        "s_add_u32 s81, s81, 0x20000\n\t"
        "buffer_load_dwordx4 v112, %[sq], s81 offen lds\n\t"
        "s_add_u32 m0, m0, 0x1000\n\t"
        "s_add_u32 s81, s81, 0x20000\n\t"
        "buffer_load_dwordx4 v112, %[sq], s81 offen lds\n\t"
        "s_add_u32 m0, m0, 0x1000\n\t"
        "s_add_u32 s81, s81, 0x20000\n\t"
        "s_sub_u32 s82, s82, 1\n\t"
        "s_cmp_lg_u32 s82, 0\n\t"
        "s_cbranch_scc1 .Ld4%=\n\t"
        "s_waitcnt vmcnt(16)\n\t"
        "s_barrier\n\t"
        "ds_read_b64_tr_b16 v[48:49], v113 offset:16384\n\t"
        "ds_read_b64_tr_b16 v[50:51], v113 offset:16896\n\t"
        "ds_read_b64_tr_b16 v[52:53], v113 offset:24576\n\t"
        "ds_read_b64_tr_b16 v[54:55], v113 offset:25088\n\t"
        "ds_read_b64_tr_b16 v[56:57], v114 offset:16384\n\t"
        "ds_read_b64_tr_b16 v[58:59], v114 offset:16896\n\t"
        "ds_read_b64_tr_b16 v[60:61], v114 offset:24576\n\t"
        "ds_read_b64_tr_b16 v[62:63], v114 offset:25088\n\t"
        "ds_read_b64_tr_b16 a[208:209], v115 offset:16384\n\t"
        "ds_read_b64_tr_b16 a[210:211], v115 offset:16896\n\t"
        "ds_read_b64_tr_b16 a[212:213], v115 offset:24576\n\t"
        "ds_read_b64_tr_b16 a[214:215], v115 offset:25088\n\t"
        "ds_read_b64_tr_b16 a[216:217], v116 offset:16384\n\t"
        "ds_read_b64_tr_b16 a[218:219], v116 offset:16896\n\t"
        "ds_read_b64_tr_b16 a[220:221], v116 offset:24576\n\t"
        "ds_read_b64_tr_b16 a[222:223], v116 offset:25088\n\t"
        "ds_read_b64_tr_b16 a[224:225], v117 offset:16384\n\t"
        "ds_read_b64_tr_b16 a[226:227], v117 offset:16896\n\t"
        "ds_read_b64_tr_b16 a[228:229], v117 offset:24576\n\t"
        "ds_read_b64_tr_b16 a[230:231], v117 offset:25088\n\t"
        "ds_read_b64_tr_b16 a[232:233], v118 offset:16384\n\t"
        "ds_read_b64_tr_b16 a[234:235], v118 offset:16896\n\t"
        "ds_read_b64_tr_b16 a[236:237], v118 offset:24576\n\t"
        "ds_read_b64_tr_b16 a[238:239], v118 offset:25088\n\t"
        "ds_read_b64_tr_b16 a[240:241], v119 offset:16384\n\t"
        "ds_read_b64_tr_b16 a[242:243], v119 offset:16896\n\t"
        "ds_read_b64_tr_b16 a[244:245], v119 offset:24576\n\t"
        "ds_read_b64_tr_b16 a[246:247], v119 offset:25088\n\t"
        "ds_read_b64_tr_b16 a[248:249], v120 offset:16384\n\t"
        "ds_read_b64_tr_b16 a[250:251], v120 offset:16896\n\t"
        "ds_read_b64_tr_b16 a[252:253], v120 offset:24576\n\t"
        "ds_read_b64_tr_b16 a[254:255], v120 offset:25088\n\t"
        "s_waitcnt lgkmcnt(0)\n\t"
        "s_barrier\n\t"
        "s_add_u32 s80, %[m0b], 16384\n\t"
        "s_mov_b32 s81, 4194304\n\t"
        "s_mov_b32 s82, 4\n\t"
        ".Ld5%=:\n\t"
        "s_mov_b32 m0, s80\n\t"
        "s_add_u32 s80, s80, 0x8000\n\t"
        "buffer_load_dwordx4 v112, %[sk], s81 offen lds\n\t"
        "s_add_u32 m0, m0, 0x1000\n\t"
        "s_add_u32 s81, s81, 0x20000\n\t"
        "buffer_load_dwordx4 v112, %[sk], s81 offen lds\n\t"
        "s_add_u32 m0, m0, 0x1000\n\t"
        "s_add_u32 s81, s81, 0x20000\n\t"
        "buffer_load_dwordx4 v112, %[sk], s81 offen lds\n\t"
        "s_add_u32 m0, m0, 0x1000\n\t"
        "s_add_u32 s81, s81, 0x20000\n\t"
        "buffer_load_dwordx4 v112, %[sk], s81 offen lds\n\t"
        "s_add_u32 m0, m0, 0x1000\n\t"
        "s_add_u32 s81, s81, 0x20000\n\t"
        "s_sub_u32 s82, s82, 1\n\t"
        "s_cmp_lg_u32 s82, 0\n\t"
        "s_cbranch_scc1 .Ld5%=\n\t"
        "s_waitcnt vmcnt(16)\n\t"
        "s_barrier\n\t"
        "ds_read_b64_tr_b16 v[128:129], v113 offset:0\n\t"
        "ds_read_b64_tr_b16 v[130:131], v113 offset:512\n\t"
        "ds_read_b64_tr_b16 v[132:133], v113 offset:8192\n\t"
        "ds_read_b64_tr_b16 v[134:135], v113 offset:8704\n\t"
        "ds_read_b64_tr_b16 v[136:137], v114 offset:0\n\t"
        "ds_read_b64_tr_b16 v[138:139], v114 offset:512\n\t"
        "ds_read_b64_tr_b16 v[140:141], v114 offset:8192\n\t"
        "ds_read_b64_tr_b16 v[142:143], v114 offset:8704\n\t"
        "ds_read_b64_tr_b16 v[144:145], v115 offset:0\n\t"
        "ds_read_b64_tr_b16 v[146:147], v115 offset:512\n\t"
        "ds_read_b64_tr_b16 v[148:149], v115 offset:8192\n\t"
        "ds_read_b64_tr_b16 v[150:151], v115 offset:8704\n\t"
        "ds_read_b64_tr_b16 v[152:153], v116 offset:0\n\t"
        "ds_read_b64_tr_b16 v[154:155], v116 offset:512\n\t"
        "ds_read_b64_tr_b16 v[156:157], v116 offset:8192\n\t"
        "ds_read_b64_tr_b16 v[158:159], v116 offset:8704\n\t"
        "ds_read_b64_tr_b16 v[160:161], v117 offset:0\n\t"
        "ds_read_b64_tr_b16 v[162:163], v117 offset:512\n\t"
        "ds_read_b64_tr_b16 v[164:165], v117 offset:8192\n\t"
        "ds_read_b64_tr_b16 v[166:167], v117 offset:8704\n\t"
        "ds_read_b64_tr_b16 v[168:169], v118 offset:0\n\t"
        "ds_read_b64_tr_b16 v[170:171], v118 offset:512\n\t"
        "ds_read_b64_tr_b16 v[172:173], v118 offset:8192\n\t"
        "ds_read_b64_tr_b16 v[174:175], v118 offset:8704\n\t"
        "ds_read_b64_tr_b16 v[176:177], v119 offset:0\n\t"
        "ds_read_b64_tr_b16 v[178:179], v119 offset:512\n\t"
        "ds_read_b64_tr_b16 v[180:181], v119 offset:8192\n\t"
        "ds_read_b64_tr_b16 v[182:183], v119 offset:8704\n\t"
        "ds_read_b64_tr_b16 v[184:185], v120 offset:0\n\t"
        "ds_read_b64_tr_b16 v[186:187], v120 offset:512\n\t"
        "ds_read_b64_tr_b16 v[188:189], v120 offset:8192\n\t"
        "ds_read_b64_tr_b16 v[190:191], v120 offset:8704\n\t"
        "s_waitcnt lgkmcnt(0)\n\t"
        "s_barrier\n\t"
        "s_add_u32 s80, %[m0b], 0\n\t"
        "s_mov_b32 s81, 6291456\n\t"
        "s_mov_b32 s82, 4\n\t"
        ".Ld6%=:\n\t"
        "s_mov_b32 m0, s80\n\t"
        "s_add_u32 s80, s80, 0x8000\n\t"
        "buffer_load_dwordx4 v112, %[sq], s81 offen lds\n\t"
        "s_add_u32 m0, m0, 0x1000\n\t"
        "s_add_u32 s81, s81, 0x20000\n\t"
        "buffer_load_dwordx4 v112, %[sq], s81 offen lds\n\t"
        "s_add_u32 m0, m0, 0x1000\n\t"
        "s_add_u32 s81, s81, 0x20000\n\t"
        "buffer_load_dwordx4 v112, %[sq], s81 offen lds\n\t"
        "s_add_u32 m0, m0, 0x1000\n\t"
        "s_add_u32 s81, s81, 0x20000\n\t"
        "buffer_load_dwordx4 v112, %[sq], s81 offen lds\n\t"
        "s_add_u32 m0, m0, 0x1000\n\t"
        "s_add_u32 s81, s81, 0x20000\n\t"
        "s_sub_u32 s82, s82, 1\n\t"
        "s_cmp_lg_u32 s82, 0\n\t"
        "s_cbranch_scc1 .Ld6%=\n\t"
        "s_waitcnt vmcnt(16)\n\t"
        "s_barrier\n\t"
        "ds_read_b64_tr_b16 v[192:193], v113 offset:16384\n\t"
        "ds_read_b64_tr_b16 v[194:195], v113 offset:16896\n\t"
        "ds_read_b64_tr_b16 v[196:197], v113 offset:24576\n\t"
        "ds_read_b64_tr_b16 v[198:199], v113 offset:25088\n\t"
        "ds_read_b64_tr_b16 v[200:201], v114 offset:16384\n\t"
        "ds_read_b64_tr_b16 v[202:203], v114 offset:16896\n\t"
        "ds_read_b64_tr_b16 v[204:205], v114 offset:24576\n\t"
        "ds_read_b64_tr_b16 v[206:207], v114 offset:25088\n\t"
        "ds_read_b64_tr_b16 v[208:209], v115 offset:16384\n\t"
        "ds_read_b64_tr_b16 v[210:211], v115 offset:16896\n\t"
        "ds_read_b64_tr_b16 v[212:213], v115 offset:24576\n\t"
        "ds_read_b64_tr_b16 v[214:215], v115 offset:25088\n\t"
        "ds_read_b64_tr_b16 v[216:217], v116 offset:16384\n\t"
        "ds_read_b64_tr_b16 v[218:219], v116 offset:16896\n\t"
        "ds_read_b64_tr_b16 v[220:221], v116 offset:24576\n\t"
        "ds_read_b64_tr_b16 v[222:223], v116 offset:25088\n\t"
        "ds_read_b64_tr_b16 v[224:225], v117 offset:16384\n\t"
        "ds_read_b64_tr_b16 v[226:227], v117 offset:16896\n\t"
        "ds_read_b64_tr_b16 v[228:229], v117 offset:24576\n\t"
        "ds_read_b64_tr_b16 v[230:231], v117 offset:25088\n\t"
        "ds_read_b64_tr_b16 v[232:233], v118 offset:16384\n\t"
        "ds_read_b64_tr_b16 v[234:235], v118 offset:16896\n\t"
        "ds_read_b64_tr_b16 v[236:237], v118 offset:24576\n\t"
        "ds_read_b64_tr_b16 v[238:239], v118 offset:25088\n\t"
        "ds_read_b64_tr_b16 v[240:241], v119 offset:16384\n\t"
        "ds_read_b64_tr_b16 v[242:243], v119 offset:16896\n\t"
        "ds_read_b64_tr_b16 v[244:245], v119 offset:24576\n\t"
        "ds_read_b64_tr_b16 v[246:247], v119 offset:25088\n\t"
        "ds_read_b64_tr_b16 v[248:249], v120 offset:16384\n\t"
        "ds_read_b64_tr_b16 v[250:251], v120 offset:16896\n\t"
        "ds_read_b64_tr_b16 v[252:253], v120 offset:24576\n\t"
        "ds_read_b64_tr_b16 v[254:255], v120 offset:25088\n\t"
        "s_waitcnt lgkmcnt(0)\n\t"
        "s_barrier\n\t"
        "s_add_u32 s80, %[m0b], 16384\n\t"
        "s_mov_b32 s81, 6291456\n\t"
        "s_mov_b32 s82, 4\n\t"
        ".Ld7%=:\n\t"
        "s_mov_b32 m0, s80\n\t"
        "s_add_u32 s80, s80, 0x8000\n\t"
        "buffer_load_dwordx4 v112, %[sk], s81 offen lds\n\t"
        "s_add_u32 m0, m0, 0x1000\n\t"
        "s_add_u32 s81, s81, 0x20000\n\t"
        "buffer_load_dwordx4 v112, %[sk], s81 offen lds\n\t"
        "s_add_u32 m0, m0, 0x1000\n\t"
        "s_add_u32 s81, s81, 0x20000\n\t"
        "buffer_load_dwordx4 v112, %[sk], s81 offen lds\n\t"
        "s_add_u32 m0, m0, 0x1000\n\t"
        "s_add_u32 s81, s81, 0x20000\n\t"
        "buffer_load_dwordx4 v112, %[sk], s81 offen lds\n\t"
        "s_add_u32 m0, m0, 0x1000\n\t"
        "s_add_u32 s81, s81, 0x20000\n\t"
        "s_sub_u32 s82, s82, 1\n\t"
        "s_cmp_lg_u32 s82, 0\n\t"
        "s_cbranch_scc1 .Ld7%=\n\t"
        "s_waitcnt vmcnt(16)\n\t"
        "s_barrier\n\t"
        "ds_read_b64_tr_b16 a[0:1], v113 offset:0\n\t"
        "ds_read_b64_tr_b16 a[2:3], v113 offset:512\n\t"
        "ds_read_b64_tr_b16 a[4:5], v113 offset:8192\n\t"
        "ds_read_b64_tr_b16 a[6:7], v113 offset:8704\n\t"
        "ds_read_b64_tr_b16 a[8:9], v114 offset:0\n\t"
        "ds_read_b64_tr_b16 a[10:11], v114 offset:512\n\t"
        "ds_read_b64_tr_b16 a[12:13], v114 offset:8192\n\t"
        "ds_read_b64_tr_b16 a[14:15], v114 offset:8704\n\t"
        "ds_read_b64_tr_b16 v[64:65], v115 offset:0\n\t"
        "ds_read_b64_tr_b16 v[66:67], v115 offset:512\n\t"
        "ds_read_b64_tr_b16 v[68:69], v115 offset:8192\n\t"
        "ds_read_b64_tr_b16 v[70:71], v115 offset:8704\n\t"
        "ds_read_b64_tr_b16 v[72:73], v116 offset:0\n\t"
        "ds_read_b64_tr_b16 v[74:75], v116 offset:512\n\t"
        "ds_read_b64_tr_b16 v[76:77], v116 offset:8192\n\t"
        "ds_read_b64_tr_b16 v[78:79], v116 offset:8704\n\t"
        "ds_read_b64_tr_b16 v[80:81], v117 offset:0\n\t"
        "ds_read_b64_tr_b16 v[82:83], v117 offset:512\n\t"
        "ds_read_b64_tr_b16 v[84:85], v117 offset:8192\n\t"
        "ds_read_b64_tr_b16 v[86:87], v117 offset:8704\n\t"
        "ds_read_b64_tr_b16 v[88:89], v118 offset:0\n\t"
        "ds_read_b64_tr_b16 v[90:91], v118 offset:512\n\t"
        "ds_read_b64_tr_b16 v[92:93], v118 offset:8192\n\t"
        "ds_read_b64_tr_b16 v[94:95], v118 offset:8704\n\t"
        "ds_read_b64_tr_b16 a[128:129], v119 offset:0\n\t"
        "ds_read_b64_tr_b16 a[130:131], v119 offset:512\n\t"
        "ds_read_b64_tr_b16 a[132:133], v119 offset:8192\n\t"
        "ds_read_b64_tr_b16 a[134:135], v119 offset:8704\n\t"
        "ds_read_b64_tr_b16 a[136:137], v120 offset:0\n\t"
        "ds_read_b64_tr_b16 a[138:139], v120 offset:512\n\t"
        "ds_read_b64_tr_b16 a[140:141], v120 offset:8192\n\t"
        "ds_read_b64_tr_b16 a[142:143], v120 offset:8704\n\t"
        "s_waitcnt lgkmcnt(0)\n\t"
        "s_barrier\n\t"
        "ds_write_b128 v124, v[0:3] offset:0\n\t"
        "ds_write_b128 v124, v[4:7] offset:1024\n\t"
        "ds_write_b128 v124, v[8:11] offset:2048\n\t"
        "ds_write_b128 v124, v[12:15] offset:3072\n\t"
        "ds_write_b128 v124, v[16:19] offset:4096\n\t"
        "ds_write_b128 v124, v[20:23] offset:5120\n\t"
        "ds_write_b128 v124, v[24:27] offset:6144\n\t"
        "ds_write_b128 v124, v[28:31] offset:7168\n\t"
        "ds_write_b128 v124, v[32:35] offset:8192\n\t"
        "ds_write_b128 v124, v[36:39] offset:9216\n\t"
        "ds_write_b128 v124, v[40:43] offset:10240\n\t"
        "ds_write_b128 v124, v[44:47] offset:11264\n\t"
        "ds_write_b128 v124, v[48:51] offset:12288\n\t"
        "ds_write_b128 v124, v[52:55] offset:13312\n\t"
        "ds_write_b128 v124, v[56:59] offset:14336\n\t"
        "ds_write_b128 v124, v[60:63] offset:15360\n\t"
        "s_waitcnt lgkmcnt(0)\n\t"
        "s_waitcnt vmcnt(0)\n\t"
        "s_barrier\n\t"
        "ds_read_b64_tr_b16 a[64:65], v113 offset:16384\n\t"
        "ds_read_b64_tr_b16 a[66:67], v113 offset:16896\n\t"
        "ds_read_b64_tr_b16 a[68:69], v113 offset:24576\n\t"
        "ds_read_b64_tr_b16 a[70:71], v113 offset:25088\n\t"
        "ds_read_b64_tr_b16 a[72:73], v114 offset:16384\n\t"
        "ds_read_b64_tr_b16 a[74:75], v114 offset:16896\n\t"
        "ds_read_b64_tr_b16 a[76:77], v114 offset:24576\n\t"
        "ds_read_b64_tr_b16 a[78:79], v114 offset:25088\n\t"
        "ds_read_b64_tr_b16 v[0:1], v115 offset:16384\n\t"
        "ds_read_b64_tr_b16 v[2:3], v115 offset:16896\n\t"
        "ds_read_b64_tr_b16 v[4:5], v115 offset:24576\n\t"
        "ds_read_b64_tr_b16 v[6:7], v115 offset:25088\n\t"
        "ds_read_b64_tr_b16 v[8:9], v116 offset:16384\n\t"
        "ds_read_b64_tr_b16 v[10:11], v116 offset:16896\n\t"
        "ds_read_b64_tr_b16 v[12:13], v116 offset:24576\n\t"
        "ds_read_b64_tr_b16 v[14:15], v116 offset:25088\n\t"
        "ds_read_b64_tr_b16 v[16:17], v117 offset:16384\n\t"
        "ds_read_b64_tr_b16 v[18:19], v117 offset:16896\n\t"
        "ds_read_b64_tr_b16 v[20:21], v117 offset:24576\n\t"
        "ds_read_b64_tr_b16 v[22:23], v117 offset:25088\n\t"
        "ds_read_b64_tr_b16 v[24:25], v118 offset:16384\n\t"
        "ds_read_b64_tr_b16 v[26:27], v118 offset:16896\n\t"
        "ds_read_b64_tr_b16 v[28:29], v118 offset:24576\n\t"
        "ds_read_b64_tr_b16 v[30:31], v118 offset:25088\n\t"
        "ds_read_b64_tr_b16 a[192:193], v119 offset:16384\n\t"
        "ds_read_b64_tr_b16 a[194:195], v119 offset:16896\n\t"
        "ds_read_b64_tr_b16 a[196:197], v119 offset:24576\n\t"
        "ds_read_b64_tr_b16 a[198:199], v119 offset:25088\n\t"
        "ds_read_b64_tr_b16 a[200:201], v120 offset:16384\n\t"
        "ds_read_b64_tr_b16 a[202:203], v120 offset:16896\n\t"
        "ds_read_b64_tr_b16 a[204:205], v120 offset:24576\n\t"
        "ds_read_b64_tr_b16 a[206:207], v120 offset:25088\n\t"
        "s_waitcnt lgkmcnt(0)\n\t"
        "s_barrier\n\t"
        "ds_write_b128 v124, v[64:67] offset:16384\n\t"
        "ds_write_b128 v124, v[68:71] offset:17408\n\t"
        "ds_write_b128 v124, v[72:75] offset:18432\n\t"
        "ds_write_b128 v124, v[76:79] offset:19456\n\t"
        "ds_write_b128 v124, v[80:83] offset:20480\n\t"
        "ds_write_b128 v124, v[84:87] offset:21504\n\t"
        "ds_write_b128 v124, v[88:91] offset:22528\n\t"
        "ds_write_b128 v124, v[92:95] offset:23552\n\t"
        "ds_write_b128 v124, v[0:3] offset:24576\n\t"
        "ds_write_b128 v124, v[4:7] offset:25600\n\t"
        "ds_write_b128 v124, v[8:11] offset:26624\n\t"
        "ds_write_b128 v124, v[12:15] offset:27648\n\t"
        "ds_write_b128 v124, v[16:19] offset:28672\n\t"
        "ds_write_b128 v124, v[20:23] offset:29696\n\t"
        "ds_write_b128 v124, v[24:27] offset:30720\n\t"
        "ds_write_b128 v124, v[28:31] offset:31744\n\t"
        "s_waitcnt lgkmcnt(0)"
        : "={a[0:31]}"(wa[0]), "={a[32:63]}"(wa[1]), "={a[64:95]}"(wa[2]), "={a[96:127]}"(wa[3]), "={a[128:159]}"(wa[4]), "={a[160:191]}"(wa[5]), "={a[192:223]}"(wa[6]), "={a[224:255]}"(wa[7]), "={v[128:159]}"(wg[0]), "={v[160:191]}"(wg[1]), "={v[192:223]}"(wg[2]), "={v[224:255]}"(wg[3]), "+{v[112:127]}"(pa)
        : [sq] "s"(sq), [sk] "s"(sk), [wsd] "s"(wsd), [m0b] "s"(m0b)
        : "memory", "scc", "m0", "s80", "s81", "s82", "v0", "v1", "v2", "v3", "v4", "v5", "v6", "v7", "v8", "v9", "v10", "v11", "v12", "v13", "v14", "v15", "v16", "v17", "v18", "v19", "v20", "v21", "v22", "v23", "v24", "v25", "v26", "v27", "v28", "v29", "v30", "v31", "v32", "v33", "v34", "v35", "v36", "v37", "v38", "v39", "v40", "v41", "v42", "v43", "v44", "v45", "v46", "v47", "v48", "v49", "v50", "v51", "v52", "v53", "v54", "v55", "v56", "v57", "v58", "v59", "v60", "v61", "v62", "v63", "v64", "v65", "v66", "v67", "v68", "v69", "v70", "v71", "v72", "v73", "v74", "v75", "v76", "v77", "v78", "v79", "v80", "v81", "v82", "v83", "v84", "v85", "v86", "v87", "v88", "v89", "v90", "v91", "v92", "v93", "v94", "v95");
        }
        asm volatile("" : "+v"(cpf));
        tid = otid(); l = tid & 63; n16 = l & 15; g = l >> 4;
        unsigned nm; if (!votes(nm)) return false;
        const unsigned nW = (nm & 1u) << 13, nA = nW, nS = nW + 6 * 16384 - w * 1024, nC = 32 * 16384 + ((nm & 2u) << 12);
        const unsigned b0 = (64 + (h >> 2)) * 16384 + ((nm & 4u) << 11) + (h & 3) * 2048 - w * 256 - L_QK, b1 = b0 + 8 * 16384;
        const unsigned pob = w == 0 ? nA + (i32 >> 4) * 16384 + (i32 & 15) * 512 : (w == 2 && i32 == 0) ? nS : OOB;
        const unsigned kx = W_KX + h * 8192, vx = W_VF + h * 8192 + w * 1024, kf = F_KVQ + h * 64, swo = 6 * 16384 + h * 8;
        const unsigned aq = img(8 * g + (n16 >> 2)) + (n16 & 1) * 8;
        v8u pc = {(unsigned)tid * 16, (unsigned)(L_W + w * 32768 + l * 16), (n16 & 2) ? 0xEEEEu : 0x4444u, aq,
                  (unsigned)(((w + 1) & 3) * 1024 + l * 16), (unsigned)(((w + 2) & 3) * 1024 + l * 16), (unsigned)(((w + 3) & 3) * 1024 + l * 16), img(32 * w + n16) + g * 4};
        v2u pd = {(n16 & 2) ? (unsigned)L_Z : aq + w * 768, (n16 & 2) ? aq + w * 768 : (unsigned)L_Z};
        *(__attribute__((address_space(3))) v4u*)(size_t)(L_TAB + tid * 16) = (v4u){(unsigned)(L_QK + 3072 + g * 384 + n16 * 8),
                  n16 < 8 ? (unsigned)(((n16 >> 2) * 4 + (h >> 3)) * 16384 + (h & 7) * 1024 + w * 256 + (g >> 1) * 128 + (n16 & 2) * 32 + (g & 1) * 32 + (n16 & 1) * 16) : OOB,
                  (unsigned)((32 * w + 16 * (g & 1) + n16) * 16 + (g >> 1) * 8), (unsigned)(L_QK + 3072 + w * 768 + ((n16 & 1) + 2 * (n16 >> 2)) * 32 + g * 8 + (n16 & 2) * 2)};
        if (tid < 64) ((lu32*)(size_t)L_FL)[tid] = 0u;
    asm volatile(
        "rec_s_code:\n\t"
        "s_mov_b32 s36, 0xf0000000\n\t"
        "s_mov_b32 s38, 0xff00ff00\n\t"
        "s_mov_b32 s39, 0xff00ff00\n\t"
        "s_mov_b32 s40, 0\n\t"
        "s_mov_b32 s37, 0\n\t"
        "s_brev_b32 s54, 1\n\t"
        "s_mov_b32 s46, %c[iD1]\n\t"
        "s_mov_b32 s45, %c[iD1]\n\t"
        "s_mov_b32 s47, %c[iD2]\n\t"
        "s_mov_b32 s48, 1\n\t"
        "s_mov_b32 s50, %c[LC]\n\t"
        "s_mov_b32 s51, %c[LA]\n\t"
        "s_mov_b32 s52, %c[E1]\n\t"
        "s_mov_b32 s53, %c[E2]\n\t"
        "s_mov_b32 s43, 0\n\t"
        "s_mov_b32 s61, %[b0]\n\t"
        "s_mov_b32 s62, %[b1]\n\t"
        "s_mov_b32 s75, 31744\n\t"
        "s_and_b32 s80, s40, 3\n\t"
        "s_lshl_b32 s80, s80, 17\n\t"
        "s_add_u32 s57, s80, %[nA]\n\t"
        "s_add_u32 s58, s57, 0x1000\n\t"
        "s_add_u32 s59, s57, 0x4000\n\t"
        "s_add_u32 s60, s57, 0x5000\n\t"
        "s_and_b32 s80, s40, 3\n\t"
        "s_lshl_b32 s80, s80, 17\n\t"
        "s_add_u32 s63, s80, %[nC]\n\t"
        "s_add_u32 s66, s80, %[nS]\n\t"
        "s_add_u32 s81, s80, 0x20000\n\t"
        "s_and_b32 s81, s81, 0x60000\n\t"
        "s_add_u32 s67, s81, %[nW]\n\t"
        "s_cmp_eq_u32 %[w], 2\n\t"
        "s_cselect_b32 s81, s80, s81\n\t"
        "s_xor_b32 s81, s81, 0x40000\n\t"
        "s_add_u32 s68, s81, %[pob]\n\t"
        "s_lshl_b32 s72, s40, 17\n\t"
        "s_add_u32 s72, s72, %[kvo]\n\t"
        "s_mov_b32 s64, %[kf]\n\t"
        "buffer_load_dwordx2 v[48:49], off, %[wsd], s64 sc1\n\t"
        "s_waitcnt vmcnt(0)\n\t"
        "s_cmp_eq_u32 s64, s36\n\t"
        "s_cbranch_scc1 .Lk10%=\n\t"
        "s_mov_b32 s97, 0\n\t"
        ".Lk00%=:\n\t"
        "v_readfirstlane_b32 s82, v48\n\t"
        "v_readfirstlane_b32 s83, v49\n\t"
        "s_xor_b32 s82, s82, %[tag]\n\t"
        "s_xor_b32 s83, s83, %[tag]\n\t"
        "s_or_b32 s82, s82, s83\n\t"
        "s_cbranch_scc0 .Lk10%=\n\t"
        "s_sleep 3\n\t"
        "buffer_load_dwordx2 v[48:49], off, %[wsd], s64 sc1\n\t"
        "s_add_u32 s97, s97, 1\n\t"
        "s_cmp_lt_u32 s97, 0x200000\n\t"
        "s_cbranch_scc0 .Lgu%=\n\t"
        "s_waitcnt vmcnt(0)\n\t"
        "s_branch .Lk00%=\n\t"
        ".Lk10%=:\n\t"
        "s_add_u32 s80, s40, 0\n\t"
        "s_min_u32 s80, s80, 63\n\t"
        "s_lshl_b32 s80, s80, 18\n\t"
        "s_add_u32 s69, s80, %[kx]\n\t"
        "s_add_u32 s70, s69, 0x1000\n\t"
        "s_lshl_b32 s80, s40, 18\n\t"
        "s_add_u32 s71, s80, %[vx]\n\t"
        "s_add_u32 s80, s40, 1\n\t"
        "s_lshl_b32 s81, s80, 9\n\t"
        "s_add_u32 s81, s81, %[kf]\n\t"
        "s_and_b32 s80, s80, 0x43\n\t"
        "s_cselect_b32 s64, s36, s81\n\t"
        "buffer_load_dwordx4 v[104:107], v120, %[wsd], s69 offen sc1\n\t"
        "buffer_load_dwordx4 v[108:111], v120, %[wsd], s70 offen sc1\n\t"
        "ds_read_b128 v[16:19], v121 offset:0\n\t"
        "ds_read_b128 v[20:23], v121 offset:1024\n\t"
        "ds_read_b128 v[24:27], v121 offset:2048\n\t"
        "ds_read_b128 v[28:31], v121 offset:3072\n\t"
        "ds_read_b128 v[32:35], v121 offset:4096\n\t"
        "ds_read_b128 v[36:39], v121 offset:5120\n\t"
        "ds_read_b128 v[40:43], v121 offset:6144\n\t"
        "ds_read_b128 v[44:47], v121 offset:7168\n\t"
        "ds_read_b128 v[56:59], v121 offset:8192\n\t"
        "ds_read_b128 v[60:63], v121 offset:9216\n\t"
        "ds_read_b128 v[64:67], v121 offset:10240\n\t"
        "ds_read_b128 v[68:71], v121 offset:11264\n\t"
        "ds_read_b128 v[72:75], v121 offset:12288\n\t"
        "ds_read_b128 v[76:79], v121 offset:13312\n\t"
        "ds_read_b128 v[80:83], v121 offset:14336\n\t"
        "ds_read_b128 v[84:87], v121 offset:15360\n\t"
        "s_add_u32 s80, s40, 1\n\t"
        "s_min_u32 s80, s80, 63\n\t"
        "s_lshl_b32 s80, s80, 18\n\t"
        "s_add_u32 s69, s80, %[kx]\n\t"
        "s_add_u32 s70, s69, 0x1000\n\t"
        "s_lshl_b32 s80, s40, 18\n\t"
        "s_add_u32 s71, s80, %[vx]\n\t"
        "s_add_u32 s80, s40, 2\n\t"
        "s_lshl_b32 s81, s80, 9\n\t"
        "s_add_u32 s81, s81, %[kf]\n\t"
        "s_and_b32 s80, s80, 0x43\n\t"
        "s_cselect_b32 s64, s36, s81\n\t"
        "v_mov_b32_e32 v88, 0\n\t"
        "v_mov_b32_e32 v89, 0\n\t"
        "v_mov_b32_e32 v90, 0\n\t"
        "v_mov_b32_e32 v91, 0\n\t"
        "v_mov_b32_e32 v92, 0\n\t"
        "v_mov_b32_e32 v93, 0\n\t"
        "v_mov_b32_e32 v94, 0\n\t"
        "v_mov_b32_e32 v95, 0\n\t"
        "v_mov_b32_e32 v96, 0\n\t"
        "v_mov_b32_e32 v97, 0\n\t"
        "v_mov_b32_e32 v98, 0\n\t"
        "v_mov_b32_e32 v99, 0\n\t"
        "v_mov_b32_e32 v100, 0\n\t"
        "v_mov_b32_e32 v101, 0\n\t"
        "v_mov_b32_e32 v102, 0\n\t"
        "v_mov_b32_e32 v103, 0\n\t"
        "s_waitcnt vmcnt(0) lgkmcnt(0)\n\t"
        "s_barrier\n\t"
        "s_memrealtime s[98:99]\n\t"
        "v_mov_b32_e32 v49, 0\n\t"
        "v_lshlrev_b32_e32 v51, 6, v120\n\t"
        "s_add_u32 s80, %[nW], %[swo]\n\t"
        "s_cmp_eq_u32 %[w], 0\n\t"
        "s_cselect_b32 s73, s80, s36\n\t"
        "s_cselect_b32 s74, %[pra], s36\n\t"
        "s_waitcnt lgkmcnt(0)\n\t"
        "s_mov_b32 s41, s98\n\t"
        "v_mov_b32_e32 v48, s98\n\t"
        ".p2align 6\n\t"
        ".Lloop%=:\n\t"
        "s_mov_b32 s97, 0\n\t"
        "s_movk_i32 s96, 0x1000\n\t"
        ".Lwa%=:\n\t"
        "s_memrealtime s[98:99]\n\t"
        "s_addk_i32 s96, -1\n\t"
        "s_cmp_lt_i32 s96, 0\n\t"
        "s_cbranch_scc1 .Lta%=\n\t"
        "s_waitcnt lgkmcnt(0)\n\t"
        "s_sub_i32 s98, s98, s41\n\t"
        "s_cmp_lt_i32 s98, %c[iFT]\n\t"
        "s_cbranch_scc1 .Lwa%=\n\t"
        "s_cmp_ge_i32 s98, 0\n\t"
        "s_cbranch_scc1 .Lxa%=\n\t"
        ".Lqa%=:\n\t"
        "s_nop %c[iFN]\n\t"
        "s_addk_i32 s98, 1\n\t"
        "s_cmp_lt_i32 s98, 0\n\t"
        "s_cbranch_scc1 .Lqa%=\n\t"
        ".Lxa%=:\n\t"
        "buffer_load_dwordx4 v[0:3], v120, %[srd], s57 offen sc1\n\t"
        "s_nop 7\n\t"
        "buffer_load_dwordx4 v[4:7], v120, %[srd], s58 offen sc1\n\t"
        "s_nop 7\n\t"
        "buffer_load_dwordx4 v[8:11], v120, %[srd], s59 offen sc1\n\t"
        "s_nop 7\n\t"
        "buffer_load_dwordx4 v[12:15], v120, %[srd], s60 offen sc1\n\t"
        "s_nop 15\n\t"
        "s_nop 15\n\t"
        "s_nop 3\n\t"
        "s_mov_b64 exec, 7\n\t"
        "buffer_store_dwordx2 v[48:49], v51, %[srd], s73 offen sc1\n\t"
        "s_mov_b64 exec, -1\n\t"
        "buffer_load_dwordx4 v[112:115], v120, %[wsd], s71 offen offset:0 sc1\n\t"
        "buffer_load_dwordx4 v[116:119], v120, %[wsd], s71 offen offset:1024 sc1\n\t"
        ".Lgo%=:\n\t"
        "s_waitcnt vmcnt(6)\n\t"
        "v_smfmac_f32_16x16x64_bf16 v[88:91], v[0:3], v[16:23], v122\n\t"
        "ds_write_b128 v120, v[0:3] offset:0\n\t"
        "v_smfmac_f32_16x16x64_bf16 v[92:95], v[0:3], v[24:31], v122\n\t"
        "v_smfmac_f32_16x16x64_bf16 v[96:99], v[0:3], v[32:39], v122\n\t"
        "v_smfmac_f32_16x16x64_bf16 v[100:103], v[0:3], v[40:47], v122\n\t"
        "s_waitcnt vmcnt(5)\n\t"
        "v_smfmac_f32_16x16x64_bf16 v[88:91], v[4:7], v[56:63], v122\n\t"
        "ds_write_b128 v120, v[4:7] offset:4096\n\t"
        "v_smfmac_f32_16x16x64_bf16 v[92:95], v[4:7], v[64:71], v122\n\t"
        "v_smfmac_f32_16x16x64_bf16 v[96:99], v[4:7], v[72:79], v122\n\t"
        "v_smfmac_f32_16x16x64_bf16 v[100:103], v[4:7], v[80:87], v122\n\t"
        "s_waitcnt vmcnt(4)\n\t"
        "v_smfmac_f32_16x16x64_bf16 v[88:91], v[8:11], v[128:135], v122\n\t"
        "ds_write_b128 v120, v[8:11] offset:8192\n\t"
        "v_smfmac_f32_16x16x64_bf16 v[92:95], v[8:11], v[136:143], v122\n\t"
        "s_waitcnt lgkmcnt(1)\n\t"
        "s_barrier\n\t"
        "v_smfmac_f32_16x16x64_bf16 v[96:99], v[8:11], v[192:199], v122\n\t"
        "ds_read_b128 v[16:19], v124 offset:0\n\t"
        "v_smfmac_f32_16x16x64_bf16 v[100:103], v[8:11], v[200:207], v122\n\t"
        "ds_read_b128 v[20:23], v125 offset:0\n\t"
        "s_waitcnt vmcnt(3)\n\t"
        "v_smfmac_f32_16x16x64_bf16 v[88:91], v[12:15], a[0:7], v122\n\t"
        "ds_write_b128 v120, v[12:15] offset:12288\n\t"
        "v_smfmac_f32_16x16x64_bf16 v[92:95], v[12:15], a[8:15], v122\n\t"
        "v_smfmac_f32_16x16x64_bf16 v[96:99], v[12:15], a[64:71], v122\n\t"
        "ds_read_b128 v[24:27], v126 offset:0\n\t"
        "v_smfmac_f32_16x16x64_bf16 v[100:103], v[12:15], a[72:79], v122\n\t"
        "ds_read_b128 v[28:31], v124 offset:4096\n\t"
        "s_waitcnt lgkmcnt(4)\n\t"
        "v_smfmac_f32_16x16x64_bf16 v[88:91], v[16:19], a[16:23], v122\n\t"
        "ds_read_b128 v[32:35], v125 offset:4096\n\t"
        "v_smfmac_f32_16x16x64_bf16 v[92:95], v[16:19], a[24:31], v122\n\t"
        "ds_read_b128 v[36:39], v126 offset:4096\n\t"
        "s_waitcnt lgkmcnt(5)\n\t"
        "v_smfmac_f32_16x16x64_bf16 v[88:91], v[20:23], a[32:39], v122\n\t"
        "ds_read_b128 v[72:75], v121 offset:16384\n\t"
        "v_smfmac_f32_16x16x64_bf16 v[92:95], v[20:23], a[40:47], v122\n\t"
        "s_waitcnt lgkmcnt(5)\n\t"
        "s_barrier\n\t"
        "ds_read_b128 v[76:79], v121 offset:17408\n\t"
        "s_waitcnt lgkmcnt(5)\n\t"
        "v_smfmac_f32_16x16x64_bf16 v[88:91], v[24:27], a[48:55], v122\n\t"
        "ds_read_b128 v[0:3], v124 offset:12288\n\t"
        "v_smfmac_f32_16x16x64_bf16 v[92:95], v[24:27], a[56:63], v122\n\t"
        "ds_read_b128 v[80:83], v121 offset:18432\n\t"
        "s_waitcnt lgkmcnt(6)\n\t"
        "v_smfmac_f32_16x16x64_bf16 v[88:91], v[28:31], a[144:151], v122\n\t"
        "ds_read_b128 v[84:87], v121 offset:19456\n\t"
        "v_smfmac_f32_16x16x64_bf16 v[92:95], v[28:31], a[152:159], v122\n\t"
        "ds_read_b128 v[40:43], v124 offset:8192\n\t"
        "s_waitcnt lgkmcnt(7)\n\t"
        "v_smfmac_f32_16x16x64_bf16 v[88:91], v[32:35], a[160:167], v122\n\t"
        "ds_read_b128 v[44:47], v125 offset:8192\n\t"
        "v_mov_b32_e32 v12, 0x3f803f80\n\t"
        "v_smfmac_f32_16x16x64_bf16 v[92:95], v[32:35], a[168:175], v122\n\t"
        "ds_read_b128 v[56:59], v121 offset:20480\n\t"
        "v_mov_b32_e32 v13, 0x3f803f80\n\t"
        "s_waitcnt lgkmcnt(8)\n\t"
        "v_smfmac_f32_16x16x64_bf16 v[88:91], v[36:39], a[176:183], v122\n\t"
        "ds_read_b128 v[60:63], v121 offset:21504\n\t"
        "v_mov_b32_e32 v14, 0x3f803f80\n\t"
        "v_smfmac_f32_16x16x64_bf16 v[92:95], v[36:39], a[184:191], v122\n\t"
        "ds_read_b128 v[4:7], v125 offset:12288\n\t"
        "v_mov_b32_e32 v15, 0x3f803f80\n\t"
        "s_waitcnt lgkmcnt(8)\n\t"
        "s_waitcnt lgkmcnt(7)\n\t"
        "v_smfmac_f32_16x16x64_bf16 v[88:91], v[0:3], v[72:79], v122\n\t"
        "ds_read_b128 v[64:67], v121 offset:22528\n\t"
        "s_waitcnt lgkmcnt(6)\n\t"
        "v_smfmac_f32_16x16x64_bf16 v[92:95], v[0:3], v[80:87], v122\n\t"
        "ds_read_b128 v[68:71], v121 offset:23552\n\t"
        "s_waitcnt lgkmcnt(6)\n\t"
        "v_smfmac_f32_16x16x64_bf16 v[88:91], v[40:43], v[144:151], v122\n\t"
        "ds_read_b128 v[48:51], v126 offset:8192\n\t"
        "v_smfmac_f32_16x16x64_bf16 v[92:95], v[40:43], v[152:159], v122\n\t"
        "ds_read_b128 v[8:11], v126 offset:12288\n\t"
        "s_waitcnt lgkmcnt(7)\n\t"
        "v_smfmac_f32_16x16x64_bf16 v[88:91], v[44:47], v[160:167], v122\n\t"
        "ds_read_b128 v[72:75], v121 offset:24576\n\t"
        "v_smfmac_f32_16x16x64_bf16 v[92:95], v[44:47], v[168:175], v122\n\t"
        "ds_read_b128 v[76:79], v121 offset:25600\n\t"
        "s_waitcnt lgkmcnt(7)\n\t"
        "s_waitcnt lgkmcnt(6)\n\t"
        "v_smfmac_f32_16x16x64_bf16 v[88:91], v[4:7], v[56:63], v122\n\t"
        "ds_read_b128 v[80:83], v121 offset:26624\n\t"
        "s_waitcnt lgkmcnt(5)\n\t"
        "v_smfmac_f32_16x16x64_bf16 v[92:95], v[4:7], v[64:71], v122\n\t"
        "ds_read_b128 v[84:87], v121 offset:27648\n\t"
        "s_waitcnt lgkmcnt(5)\n\t"
        "v_smfmac_f32_16x16x64_bf16 v[88:91], v[48:51], v[176:183], v122\n\t"
        "v_mov_b32_e32 v64, 0\n\t"
        "v_smfmac_f32_16x16x64_bf16 v[92:95], v[48:51], v[184:191], v122\n\t"
        "v_mov_b32_e32 v65, 0\n\t"
        "s_waitcnt lgkmcnt(4)\n\t"
        "v_smfmac_f32_16x16x64_bf16 v[88:91], v[8:11], a[128:135], v122\n\t"
        "v_mov_b32_e32 v66, 0\n\t"
        "v_smfmac_f32_16x16x64_bf16 v[92:95], v[8:11], a[136:143], v122\n\t"
        "v_mov_b32_e32 v67, 0\n\t"
        "s_waitcnt lgkmcnt(2)\n\t"
        "v_smfmac_f32_16x16x64_bf16 v[96:99], v[0:3], v[72:79], v122\n\t"
        "ds_read_b128 v[72:75], v121 offset:28672\n\t"
        "s_waitcnt lgkmcnt(1)\n\t"
        "v_smfmac_f32_16x16x64_bf16 v[100:103], v[0:3], v[80:87], v122\n\t"
        "ds_read_b128 v[76:79], v121 offset:29696\n\t"
        "v_smfmac_f32_16x16x64_bf16 v[96:99], v[16:19], a[80:87], v122\n\t"
        "v_add_f32_e32 v88, v88, v90\n\t"
        "v_add_f32_e32 v89, v89, v91\n\t"
        "v_smfmac_f32_16x16x64_bf16 v[100:103], v[16:19], a[88:95], v122\n\t"
        "v_cvt_pk_bf16_f32 v90, v88, v89\n\t"
        "v_add_f32_e32 v92, v92, v94\n\t"
        "v_smfmac_f32_16x16x64_bf16 v[96:99], v[20:23], a[96:103], v122\n\t"
        "ds_write_b32 v127, v90\n\t"
        "v_add_f32_e32 v93, v93, v95\n\t"
        "v_cvt_pk_bf16_f32 v91, v92, v93\n\t"
        "v_smfmac_f32_16x16x64_bf16 v[100:103], v[20:23], a[104:111], v122\n\t"
        "ds_write_b32 v127, v91 offset:384\n\t"
        "v_cmp_u_f32_e32 vcc, v88, v89\n\t"
        "v_smfmac_f32_16x16x64_bf16 v[96:99], v[24:27], a[112:119], v122\n\t"
        "ds_read_b64_tr_b16 v[56:57], v52 offset:0\n\t"
        "ds_read_b64_tr_b16 v[58:59], v52 offset:64\n\t"
        "v_smfmac_f32_16x16x64_bf16 v[100:103], v[24:27], a[120:127], v122\n\t"
        "ds_read_b64_tr_b16 v[60:61], v53 offset:0\n\t"
        "ds_read_b64_tr_b16 v[62:63], v53 offset:64\n\t"
        "v_smfmac_f32_16x16x64_bf16 v[96:99], v[28:31], a[208:215], v122\n\t"
        "s_cbranch_vccnz .Lf1%=\n\t"
        ".Lr1%=:\n\t"
        "ds_read_b128 v[80:83], v121 offset:30720\n\t"
        "v_smfmac_f32_16x16x64_bf16 v[100:103], v[28:31], a[216:223], v122\n\t"
        "ds_read_b128 v[84:87], v121 offset:31744\n\t"
        "v_mov_b32_e32 v91, 0\n\t"
        "v_smfmac_f32_16x16x64_bf16 v[96:99], v[32:35], a[224:231], v122\n\t"
        "v_mov_b32_e32 v94, 0\n\t"
        "v_smfmac_f32_16x16x64_bf16 v[100:103], v[32:35], a[232:239], v122\n\t"
        "ds_read_b128 v[32:35], v120 offset:20480\n\t"
        "v_smfmac_f32_16x16x64_bf16 v[96:99], v[36:39], a[240:247], v122\n\t"
        "s_waitcnt lgkmcnt(3)\n\t"
        "s_barrier\n\t"
        "v_mfma_f32_16x16x32_bf16 v[68:71], v[104:107], v[56:59], 0\n\t"
        "ds_read_b64_tr_b16 v[16:17], v123 offset:0\n\t"
        "v_mfma_f32_16x16x32_bf16 v[68:71], v[108:111], v[60:63], v[68:71]\n\t"
        "ds_read_b64_tr_b16 v[18:19], v123 offset:64\n\t"
        "v_smfmac_f32_16x16x64_bf16 v[100:103], v[36:39], a[248:255], v122\n\t"
        "ds_read_b64_tr_b16 v[20:21], v123 offset:768\n\t"
        "v_smfmac_f32_16x16x64_bf16 v[96:99], v[8:11], a[192:199], v122\n\t"
        "ds_read_b64_tr_b16 v[22:23], v123 offset:832\n\t"
        "v_smfmac_f32_16x16x64_bf16 v[100:103], v[8:11], a[200:207], v122\n\t"
        "s_nop 1\n\t"
        "ds_write_b128 v120, v[68:71] offset:16384\n\t"
        "v_smfmac_f32_16x16x64_bf16 v[96:99], v[4:7], v[72:79], v122\n\t"
        "buffer_load_dword v88, v127, %[srd], s61 offen offset:0 sc1\n\t"
        "s_waitcnt lgkmcnt(6)\n\t"
        "v_smfmac_f32_16x16x64_bf16 v[100:103], v[4:7], v[80:87], v122\n\t"
        "buffer_load_dword v89, v127, %[srd], s61 offen offset:256 sc1\n\t"
        "v_smfmac_f32_16x16x64_bf16 v[96:99], v[40:43], v[208:215], v122\n\t"
        "ds_read_b64_tr_b16 v[24:25], v123 offset:1536\n\t"
        "v_smfmac_f32_16x16x64_bf16 v[100:103], v[40:43], v[216:223], v122\n\t"
        "ds_read_b64_tr_b16 v[26:27], v123 offset:1600\n\t"
        "v_smfmac_f32_16x16x64_bf16 v[96:99], v[44:47], v[224:231], v122\n\t"
        "s_waitcnt lgkmcnt(2)\n\t"
        "s_barrier\n\t"
        "ds_read_b128 v[36:39], v124 offset:16384\n\t"
        "v_smfmac_f32_16x16x64_bf16 v[100:103], v[44:47], v[232:239], v122\n\t"
        "ds_read_b128 v[56:59], v125 offset:16384\n\t"
        "v_smfmac_f32_16x16x64_bf16 v[96:99], v[48:51], v[240:247], v122\n\t"
        "ds_read_b128 v[60:63], v126 offset:16384\n\t"
        "v_smfmac_f32_16x16x64_bf16 v[100:103], v[48:51], v[248:255], v122\n\t"
        "ds_read_b64_tr_b16 v[28:29], v123 offset:2304\n\t"
        "ds_read_b64_tr_b16 v[30:31], v123 offset:2368\n\t"
        "buffer_load_dwordx4 v[84:87], v120, %[srd], s66 offen sc1\n\t"
        "s_waitcnt lgkmcnt(4)\n\t"
        "s_cmp_eq_u32 s40, 63\n\t"
        "v_add_f32_e32 v68, v68, v36\n\t"
        "v_add_f32_e32 v69, v69, v37\n\t"
        "v_add_f32_e32 v70, v70, v38\n\t"
        "v_add_f32_e32 v71, v71, v39\n\t"
        "v_add_f32_e32 v96, v96, v98\n\t"
        "v_add_f32_e32 v97, v97, v99\n\t"
        "s_waitcnt lgkmcnt(2)\n\t"
        "v_cvt_pk_bf16_f32 v50, v96, v97\n\t"
        "v_pk_add_f32 v[100:101], v[100:101], v[102:103]\n\t"
        "ds_write_b32 v35, v50\n\t"
        "v_cvt_pk_bf16_f32 v51, v100, v101\n\t"
        "ds_write_b32 v35, v51 offset:384\n\t"
        "v_pk_add_f32 v[56:57], v[56:57], v[60:61]\n\t"
        "s_cbranch_scc1 .Lend%=\n\t"
        "v_pk_add_f32 v[58:59], v[58:59], v[62:63]\n\t"
        "v_pk_add_f32 v[68:69], v[68:69], v[56:57]\n\t"
        "v_pk_add_f32 v[70:71], v[70:71], v[58:59]\n\t"
        "v_max3_f32 v2, v68, v69, v70\n\t"
        "v_max_f32_e32 v2, v2, v71\n\t"
        "s_nop 1\n\t"
        "v_max_f32_dpp v3, v2, v2 row_ror:8 row_mask:0xf bank_mask:0xf\n\t"
        "v_max_f32_dpp v1, v2, v2 row_ror:8 row_mask:0xf bank_mask:0xf\n\t"
        "s_nop 1\n\t"
        "v_permlane16_swap_b32_e32 v3, v1\n\t"
        "v_max_f32_e32 v2, v3, v1\n\t"
        "v_max_f32_e32 v3, v3, v1\n\t"
        "s_waitcnt lgkmcnt(0)\n\t"
        "s_barrier\n\t"
        "ds_read_b64_tr_b16 v[72:73], v32 offset:0\n\t"
        "v_permlane32_swap_b32_e32 v2, v3\n\t"
        "ds_read_b64_tr_b16 v[74:75], v32 offset:128\n\t"
        "v_max_f32_e32 v0, v2, v3\n\t"
        "ds_read_b64_tr_b16 v[76:77], v32 offset:1536\n\t"
        "v_mul_f32_e32 v2, 0xbe0293ee, v0\n\t"
        "ds_read_b64_tr_b16 v[78:79], v32 offset:1664\n\t"
        "v_fmamk_f32 v68, v68, 0x3e0293ee, v2\n\t"
        "v_fmamk_f32 v69, v69, 0x3e0293ee, v2\n\t"
        "v_fmamk_f32 v70, v70, 0x3e0293ee, v2\n\t"
        "v_fmamk_f32 v71, v71, 0x3e0293ee, v2\n\t"
        "v_exp_f32_e32 v68, v68\n\t"
        "v_exp_f32_e32 v69, v69\n\t"
        "v_exp_f32_e32 v70, v70\n\t"
        "s_waitcnt lgkmcnt(2)\n\t"
        "v_smfmac_f32_16x16x64_bf16 v[64:67], v[72:75], v[16:23], v122\n\t"
        "s_waitcnt lgkmcnt(0)\n\t"
        "v_smfmac_f32_16x16x64_bf16 v[64:67], v[76:79], v[24:31], v122\n\t"
        "s_waitcnt vmcnt(3)\n\t"
        "v_cvt_pk_bf16_f32 v36, v68, v69\n\t"
        "v_exp_f32_e32 v71, v71\n\t"
        "s_nop 0\n\t"
        "v_mov_b32_dpp v38, v36 row_ror:8 row_mask:0xf bank_mask:0xf\n\t"
        "v_cvt_pk_bf16_f32 v37, v70, v71\n\t"
        "s_nop 1\n\t"
        "v_mov_b32_dpp v39, v37 row_ror:8 row_mask:0xf bank_mask:0xf\n\t"
        "v_pk_add_f32 v[64:65], v[64:65], v[66:67]\n\t"
        "v_max3_f32 v3, v0, v64, v65\n\t"
        "v_max3_f32 v1, v0, v64, v65\n\t"
        "v_mfma_f32_16x16x32_bf16 v[40:43], v[112:115], v[36:39], 0\n\t"
        "s_nop 0\n\t"
        "v_permlane16_swap_b32_e32 v3, v1\n\t"
        "v_max_f32_e32 v2, v3, v1\n\t"
        "v_max_f32_e32 v3, v3, v1\n\t"
        "v_mfma_f32_16x16x32_bf16 v[44:47], v[116:119], v[36:39], 0\n\t"
        "s_nop 0\n\t"
        "v_permlane32_swap_b32_e32 v2, v3\n\t"
        "v_max_f32_e32 v2, v2, v3\n\t"
        "v_mfma_f32_16x16x32_bf16 v[96:99], v[12:15], v[36:39], 0\n\t"
        "v_mul_f32_e32 v2, 0xbe0293ee, v2\n\t"
        "v_fmamk_f32 v10, v0, 0x3e0293ee, v2\n\t"
        "v_fmamk_f32 v64, v64, 0x3e0293ee, v2\n\t"
        "v_exp_f32_e32 v10, v10\n\t"
        "v_fmamk_f32 v65, v65, 0x3e0293ee, v2\n\t"
        "v_exp_f32_e32 v64, v64\n\t"
        "s_nop 1\n\t"
        "v_mul_f32_e32 v96, v96, v10\n\t"
        "v_exp_f32_e32 v65, v65\n\t"
        "v_pk_mul_f32 v[40:41], v[40:41], v[10:11] op_sel_hi:[1,0]\n\t"
        "v_cvt_pk_bf16_f32 v90, v64, v65\n\t"
        "v_pk_mul_f32 v[42:43], v[42:43], v[10:11] op_sel_hi:[1,0]\n\t"
        "v_cvt_pk_bf16_f32 v95, v64, v65\n\t"
        "v_pk_mul_f32 v[44:45], v[44:45], v[10:11] op_sel_hi:[1,0]\n\t"
        "v_mfma_f32_16x16x16_bf16 v[96:99], v[12:13], v[90:91], v[96:99]\n\t"
        "s_waitcnt vmcnt(1)\n\t"
        "v_mul_f32_e32 v46, v46, v10\n\t"
        "v_mul_f32_e32 v47, v47, v10\n\t"
        "v_mfma_f32_16x16x16_bf16 v[40:43], v[88:89], v[90:91], v[40:43]\n\t"
        "v_max_u32_e32 v54, v88, v89\n\t"
        "v_cmp_eq_u32_e32 vcc, -1, v54\n\t"
        "v_mfma_f32_16x16x16_bf16 v[44:47], v[88:89], v[94:95], v[44:47]\n\t"
        "s_nop 0\n\t"
        "v_rcp_f32_e32 v2, v96\n\t"
        "s_nop 0\n\t"
        "s_cbranch_vccnz .Lfb%=\n\t"
        ".Lrb%=:\n\t"
        "s_nop 0\n\t"
        "v_pk_mul_f32 v[40:41], v[40:41], v[2:3] op_sel_hi:[1,0]\n\t"
        "v_pk_mul_f32 v[42:43], v[42:43], v[2:3] op_sel_hi:[1,0]\n\t"
        "v_cvt_pk_bf16_f32 v100, v40, v41\n\t"
        "v_pk_mul_f32 v[44:45], v[44:45], v[2:3] op_sel_hi:[1,0]\n\t"
        "v_pk_mul_f32 v[46:47], v[46:47], v[2:3] op_sel_hi:[1,0]\n\t"
        "v_cvt_pk_bf16_f32 v101, v42, v43\n\t"
        "v_cvt_pk_bf16_f32 v102, v44, v45\n\t"
        "v_min_u32_e32 v100, -2, v100\n\t"
        "v_cvt_pk_bf16_f32 v103, v46, v47\n\t"
        "v_min_u32_e32 v102, -2, v102\n\t"
        "buffer_store_dwordx4 v[100:103], v33, %[srd], s63 offen sc1\n\t"
        "s_memrealtime s[76:77]\n\t"
        "v_mov_b32_e32 v8, -1\n\t"
        "v_mov_b32_e32 v9, -1\n\t"
        "v_mov_b32_e32 v10, -1\n\t"
        "v_mov_b32_e32 v11, -1\n\t"
        "v_permlane16_swap_b32_e32 v50, v51\n\t"
        "v_permlane16_swap_b32_e32 v88, v89\n\t"
        "v_mov_b32_e32 v14, s40\n\t"
        "s_waitcnt lgkmcnt(0)\n\t"
        "s_waitcnt vmcnt(1)\n\t"
        "s_mov_b32 s97, 0\n\t"
        ".Lsr%=:\n\t"
        "s_mov_b32 exec_hi, 0xffff\n\t"
        "v_subrev_u32_e32 v0, s41, v84\n\t"
        "v_subrev_u32_e32 v1, s41, v86\n\t"
        "v_max_i32_e32 v0, v0, v1\n\t"
        "v_max_u32_e32 v1, v85, v87\n\t"
        "v_cmp_eq_u32_e32 vcc, -1, v1\n\t"
        "v_max_i32_dpp v0, v0, v0 quad_perm:[1,0,3,2] row_mask:0xf bank_mask:0xf\n\t"
        "v_or_b32_e32 v2, v85, v87\n\t"
        "s_nop 0\n\t"
        "v_max_i32_dpp v0, v0, v0 quad_perm:[2,3,0,1] row_mask:0xf bank_mask:0xf\n\t"
        "s_nop 1\n\t"
        "v_max_i32_dpp v0, v0, v0 row_ror:4 row_mask:0xf bank_mask:0xf\n\t"
        "s_nop 1\n\t"
        "v_max_i32_dpp v0, v0, v0 row_ror:8 row_mask:0xf bank_mask:0xf\n\t"
        "s_cbranch_vccnz .Lsf%=\n\t"
        "v_readlane_b32 s85, v0, 0\n\t"
        "v_readlane_b32 s81, v0, 16\n\t"
        "v_readlane_b32 s56, v0, 32\n\t"
        "v_and_b32_e32 v1, 1, v2\n\t"
        "v_cmp_ne_u32_e64 s[86:87], 0, v1\n\t"
        "v_and_b32_e32 v1, 2, v2\n\t"
        "v_cmp_ne_u32_e64 s[88:89], 0, v1\n\t"
        "v_and_b32_e32 v1, 4, v2\n\t"
        "v_cmp_ne_u32_e64 s[90:91], 0, v1\n\t"
        "s_max_i32 s56, s56, s81\n\t"
        "s_add_u32 s92, s85, s41\n\t"
        "s_sub_u32 s92, s92, s43\n\t"
        "s_add_i32 s93, s56, s47\n\t"
        "s_mov_b32 s44, s46\n\t"
        "s_mov_b32 s46, s45\n\t"
        "s_mov_b32 s94, s48\n\t"
        "s_mov_b32 s43, s41\n\t"
        "s_cmp_gt_i32 s56, s54\n\t"
        "s_cbranch_scc1 .Lmv%=\n\t"
        "s_mov_b32 s37, 0\n\t"
        "s_mov_b32 s48, 0\n\t"
        "s_mov_b32 s95, 0\n\t"
        ".Lsd%=:\n\t"
        "s_mov_b32 exec_hi, -1\n\t"
        "s_add_u32 s42, s43, s46\n\t"
        "s_sleep %c[HSLP]\n\t"
        "buffer_load_dwordx2 v[48:49], off, %[wsd], s64 sc1\n\t"
        "buffer_load_dwordx4 v[104:107], v120, %[wsd], s69 offen sc1\n\t"
        "buffer_load_dwordx4 v[108:111], v120, %[wsd], s70 offen sc1\n\t"
        "buffer_store_dwordx2 v[50:51], v34, %[wsd], s72 offen sc1\n\t"
        "buffer_store_dwordx2 v[88:89], v34, %[wsd], s72 offen offset:2048 sc1\n\t"
        "s_lshl_b32 s80, %[w], 8\n\t"
        "s_add_u32 s80, s80, s61\n\t"
        "s_add_u32 s80, s80, 24576\n\t"
        "buffer_store_dwordx2 v[8:9], v34, %[srd], s80 offen sc1\n\t"
        "s_cmp_eq_u32 %[w], 0\n\t"
        "s_cselect_b32 exec_hi, 0, -1\n\t"
        "buffer_store_dwordx4 v[8:11], v120, %[srd], s68 offen sc1\n\t"
        "s_mov_b32 exec_hi, -1\n\t"
        "s_mov_b64 exec, 1\n\t"
        "buffer_store_dword v14, off, %[wsd], s74 sc1\n\t"
        "s_mov_b64 exec, -1\n\t"
        "s_cmp_ge_u32 s40, %c[iCT0]\n\t"
        "s_cselect_b32 s85, 1, 0\n\t"
        "s_cmp_lg_u64 s[86:87], 0\n\t"
        "s_cselect_b32 s86, s85, 0\n\t"
        "s_cmp_lg_u64 s[88:89], 0\n\t"
        "s_cselect_b32 s87, s85, 0\n\t"
        "s_cmp_lg_u64 s[90:91], 0\n\t"
        "s_cselect_b32 s88, s85, 0\n\t"
        "s_or_b32 s80, s94, s86\n\t"
        "s_or_b32 s80, s80, s87\n\t"
        "s_andn2_b32 s90, s85, s80\n\t"
        "s_cmp_le_i32 s92, s44\n\t"
        "s_cselect_b32 s80, s85, 0\n\t"
        "s_andn2_b32 s91, s80, s88\n\t"
        "s_and_b32 s80, s40, %c[LDEC]\n\t"
        "s_cselect_b32 s89, 0, 1\n\t"
        "s_mov_b32 s80, s88\n\t"
        "s_add_i32 s81, s92, s50\n\t"
        "s_sub_i32 s81, s81, 4\n\t"
        "s_cmp_le_i32 s81, s44\n\t"
        "s_cselect_b32 s80, s80, 0\n\t"
        "s_cmp_lt_i32 s50, %c[LC24]\n\t"
        "s_cselect_b32 s80, s80, 0\n\t"
        "s_lshl2_add_u32 s50, s80, s50\n\t"
        "s_sub_i32 s50, s50, s89\n\t"
        "s_max_i32 s50, s50, %c[LC]\n\t"
        "s_lshl_b32 s80, s92, 1\n\t"
        "s_sub_i32 s81, s80, s52\n\t"
        "s_ashr_i32 s82, s81, %c[UPSH]\n\t"
        "s_max_i32 s82, s82, %c[TRUP]\n\t"
        "s_add_i32 s82, s82, s52\n\t"
        "s_min_i32 s82, s82, s80\n\t"
        "s_ashr_i32 s83, s81, 1\n\t"
        "s_min_i32 s83, s83, -%c[TRDN]\n\t"
        "s_add_i32 s83, s83, s52\n\t"
        "s_max_i32 s83, s83, s80\n\t"
        "s_cmp_gt_i32 s81, 0\n\t"
        "s_cselect_b32 s82, s82, s83\n\t"
        "s_cmp_lg_u32 s90, 0\n\t"
        "s_cselect_b32 s52, s82, s52\n\t"
        "s_add_i32 s80, s52, 1\n\t"
        "s_ashr_i32 s80, s80, 1\n\t"
        "s_add_i32 s45, s80, s50\n\t"
        "s_andn2_b32 s80, s86, s94\n\t"
        "s_cmp_lt_i32 s51, %c[LA24]\n\t"
        "s_cselect_b32 s80, s80, 0\n\t"
        "s_lshl2_add_u32 s51, s80, s51\n\t"
        "s_sub_i32 s51, s51, s89\n\t"
        "s_max_i32 s51, s51, %c[LA]\n\t"
        "s_lshl_b32 s80, s93, 1\n\t"
        "s_sub_i32 s81, s80, s53\n\t"
        "s_ashr_i32 s82, s81, %c[UPSH]\n\t"
        "s_max_i32 s82, s82, %c[TRUP]\n\t"
        "s_add_i32 s82, s82, s53\n\t"
        "s_min_i32 s82, s82, s80\n\t"
        "s_ashr_i32 s83, s81, 1\n\t"
        "s_min_i32 s83, s83, -%c[TRDN]\n\t"
        "s_add_i32 s83, s83, s53\n\t"
        "s_max_i32 s83, s83, s80\n\t"
        "s_cmp_gt_i32 s81, 0\n\t"
        "s_cselect_b32 s82, s82, s83\n\t"
        "s_cmp_lg_u32 s91, 0\n\t"
        "s_cselect_b32 s53, s82, s53\n\t"
        "s_add_i32 s80, s53, 1\n\t"
        "s_ashr_i32 s80, s80, 1\n\t"
        "s_add_i32 s47, s80, s51\n\t"
        "s_add_u32 s81, s47, %c[AUP]\n\t"
        "s_min_i32 s80, s95, s81\n\t"
        "s_max_i32 s47, s47, s80\n\t"
        "s_add_u32 s41, s42, s47\n\t"
        "s_add_u32 s40, s40, 1\n\t"
        "v_mov_b32_e32 v88, 0\n\t"
        "v_mov_b32_e32 v89, 0\n\t"
        "v_mov_b32_e32 v90, 0\n\t"
        "v_mov_b32_e32 v91, 0\n\t"
        "v_mov_b32_e32 v92, 0\n\t"
        "v_mov_b32_e32 v93, 0\n\t"
        "v_mov_b32_e32 v94, 0\n\t"
        "v_mov_b32_e32 v95, 0\n\t"
        "v_mov_b32_e32 v96, 0\n\t"
        "v_mov_b32_e32 v97, 0\n\t"
        "v_mov_b32_e32 v98, 0\n\t"
        "v_mov_b32_e32 v99, 0\n\t"
        "v_mov_b32_e32 v100, 0\n\t"
        "v_mov_b32_e32 v101, 0\n\t"
        "v_mov_b32_e32 v102, 0\n\t"
        "v_mov_b32_e32 v103, 0\n\t"
        "ds_read_b128 v[16:19], v121 offset:0\n\t"
        "ds_read_b128 v[20:23], v121 offset:1024\n\t"
        "ds_read_b128 v[24:27], v121 offset:2048\n\t"
        "ds_read_b128 v[28:31], v121 offset:3072\n\t"
        "ds_read_b128 v[32:35], v121 offset:4096\n\t"
        "ds_read_b128 v[36:39], v121 offset:5120\n\t"
        "ds_read_b128 v[40:43], v121 offset:6144\n\t"
        "ds_read_b128 v[44:47], v121 offset:7168\n\t"
        "ds_read_b128 v[56:59], v121 offset:8192\n\t"
        "ds_read_b128 v[60:63], v121 offset:9216\n\t"
        "ds_read_b128 v[64:67], v121 offset:10240\n\t"
        "ds_read_b128 v[68:71], v121 offset:11264\n\t"
        "ds_read_b128 v[72:75], v121 offset:12288\n\t"
        "ds_read_b128 v[76:79], v121 offset:13312\n\t"
        "ds_read_b128 v[80:83], v121 offset:14336\n\t"
        "ds_read_b128 v[84:87], v121 offset:15360\n\t"
        "v_mov_b32_e32 v6, 30720\n\t"
        "ds_read_b64 v[6:7], v6\n\t"
        "s_waitcnt lgkmcnt(0)\n\t"
        "v_readfirstlane_b32 s80, v6\n\t"
        "v_readfirstlane_b32 s81, v7\n\t"
        "s_cmp_eq_u32 s80, s40\n\t"
        "s_cselect_b32 s84, 1, 0\n\t"
        "s_cmp_eq_u32 s81, s40\n\t"
        "s_cselect_b32 s81, 2, 0\n\t"
        "s_or_b32 s84, s84, s81\n\t"
        "s_waitcnt vmcnt(7)\n\t"
        "s_cmp_eq_u32 s64, s36\n\t"
        "s_cbranch_scc1 .Lk11%=\n\t"
        "s_mov_b32 s97, 0\n\t"
        ".Lk01%=:\n\t"
        "v_readfirstlane_b32 s82, v48\n\t"
        "v_readfirstlane_b32 s83, v49\n\t"
        "s_xor_b32 s82, s82, %[tag]\n\t"
        "s_xor_b32 s83, s83, %[tag]\n\t"
        "s_or_b32 s82, s82, s83\n\t"
        "s_cbranch_scc0 .Lk11%=\n\t"
        "s_sleep 3\n\t"
        "buffer_load_dwordx2 v[48:49], off, %[wsd], s64 sc1\n\t"
        "s_add_u32 s97, s97, 1\n\t"
        "s_cmp_lt_u32 s97, 0x200000\n\t"
        "s_cbranch_scc0 .Lgu%=\n\t"
        "s_waitcnt vmcnt(0)\n\t"
        "s_branch .Lk01%=\n\t"
        ".Lk11%=:\n\t"
        "v_mov_b32_e32 v48, s76\n\t"
        "v_mov_b32_e32 v49, s84\n\t"
        "s_mov_b32 s85, s67\n\t"
        "s_and_b32 s80, s40, 3\n\t"
        "s_lshl_b32 s80, s80, 17\n\t"
        "s_add_u32 s57, s80, %[nA]\n\t"
        "s_add_u32 s58, s57, 0x1000\n\t"
        "s_add_u32 s59, s57, 0x4000\n\t"
        "s_add_u32 s60, s57, 0x5000\n\t"
        "s_and_b32 s80, s40, 3\n\t"
        "s_lshl_b32 s80, s80, 17\n\t"
        "s_add_u32 s63, s80, %[nC]\n\t"
        "s_add_u32 s66, s80, %[nS]\n\t"
        "s_add_u32 s81, s80, 0x20000\n\t"
        "s_and_b32 s81, s81, 0x60000\n\t"
        "s_add_u32 s67, s81, %[nW]\n\t"
        "s_cmp_eq_u32 %[w], 2\n\t"
        "s_cselect_b32 s81, s80, s81\n\t"
        "s_xor_b32 s81, s81, 0x40000\n\t"
        "s_add_u32 s68, s81, %[pob]\n\t"
        "s_lshl_b32 s72, s40, 17\n\t"
        "s_add_u32 s72, s72, %[kvo]\n\t"
        "v_lshlrev_b32_e32 v51, 6, v120\n\t"
        "s_add_u32 s80, s85, %[swo]\n\t"
        "s_cmp_eq_u32 %[w], 0\n\t"
        "s_cselect_b32 s73, s80, s36\n\t"
        "s_cselect_b32 s74, %[pra], s36\n\t"
        "s_add_u32 s80, s40, 1\n\t"
        "s_min_u32 s80, s80, 63\n\t"
        "s_lshl_b32 s80, s80, 18\n\t"
        "s_add_u32 s69, s80, %[kx]\n\t"
        "s_add_u32 s70, s69, 0x1000\n\t"
        "s_lshl_b32 s80, s40, 18\n\t"
        "s_add_u32 s71, s80, %[vx]\n\t"
        "s_add_u32 s80, s40, 2\n\t"
        "s_lshl_b32 s81, s80, 9\n\t"
        "s_add_u32 s81, s81, %[kf]\n\t"
        "s_and_b32 s80, s80, 0x43\n\t"
        "s_cselect_b32 s64, s36, s81\n\t"
        "s_mov_b32 s80, s61\n\t"
        "s_mov_b32 s61, s62\n\t"
        "s_mov_b32 s62, s80\n\t"
        "s_waitcnt lgkmcnt(0)\n\t"
        "s_branch .Lloop%=\n\t"
        ".Lmv%=:\n\t"
        "s_mov_b32 s48, 1\n\t"
        "s_cmp_eq_u32 s54, %c[iTOL]\n\t"
        "s_cbranch_scc1 .Lm1%=\n\t"
        "s_max_i32 s80, s85, s56\n\t"
        "s_add_u32 s43, s41, s80\n\t"
        "s_add_u32 s43, s43, %c[iPEN0]\n\t"
        "s_mov_b32 s54, %c[iTOL]\n\t"
        "s_add_u32 s46, s46, %c[iX0C]\n\t"
        "s_add_u32 s95, s47, %c[iX0A]\n\t"
        "v_and_b32_e32 v0, 3, v84\n\t"
        "v_and_b32_e32 v1, 3, v86\n\t"
        "v_lshlrev_b32_e64 v0, v0, 1\n\t"
        "v_lshlrev_b32_e64 v1, v1, 1\n\t"
        "v_or_b32_e32 v0, v0, v1\n\t"
        "s_mov_b32 s80, 0\n\t"
        "v_and_b32_e32 v1, 1, v0\n\t"
        "v_cmp_ne_u32_e32 vcc, 0, v1\n\t"
        "s_nop 1\n\t"
        "s_cmp_lg_u64 vcc, 0\n\t"
        "s_cselect_b32 s81, 2, 0\n\t"
        "s_or_b32 s80, s80, s81\n\t"
        "v_and_b32_e32 v1, 2, v0\n\t"
        "v_cmp_ne_u32_e32 vcc, 0, v1\n\t"
        "s_nop 1\n\t"
        "s_cmp_lg_u64 vcc, 0\n\t"
        "s_cselect_b32 s81, 4, 0\n\t"
        "s_or_b32 s80, s80, s81\n\t"
        "v_and_b32_e32 v1, 4, v0\n\t"
        "v_cmp_ne_u32_e32 vcc, 0, v1\n\t"
        "s_nop 1\n\t"
        "s_cmp_lg_u64 vcc, 0\n\t"
        "s_cselect_b32 s81, 8, 0\n\t"
        "s_or_b32 s80, s80, s81\n\t"
        "v_and_b32_e32 v1, 8, v0\n\t"
        "v_cmp_ne_u32_e32 vcc, 0, v1\n\t"
        "s_nop 1\n\t"
        "s_cmp_lg_u64 vcc, 0\n\t"
        "s_cselect_b32 s81, 16, 0\n\t"
        "s_or_b32 s80, s80, s81\n\t"
        "s_lshr_b32 s80, 0x1adf1210, s80\n\t"
        "s_and_b32 s55, s80, 3\n\t"
        "s_branch .Lal%=\n\t"
        ".Lm1%=:\n\t"
        "s_add_u32 s80, s37, %c[iPEN2]\n\t"
        "s_min_i32 s80, s80, %c[iPENM]\n\t"
        "s_cmp_lg_u32 s37, 0\n\t"
        "s_cselect_b32 s37, s80, %c[iPEN]\n\t"
        "s_add_u32 s80, s56, s37\n\t"
        "s_max_i32 s80, s80, %c[iPMIN]\n\t"
        "s_add_u32 s43, s41, s80\n\t"
        "s_add_u32 s95, s93, %c[LA]\n\t"
        "s_add_u32 s46, s46, %c[iXR]\n\t"
        ".Lal%=:\n\t"
        "s_sub_u32 s80, s55, s43\n\t"
        "s_and_b32 s80, s80, %c[iPHM]\n\t"
        "s_add_u32 s43, s43, s80\n\t"
        "s_branch .Lsd%=\n\t"
        ".Lsf%=:\n\t"
        "s_mov_b32 exec_hi, -1\n\t"
        "s_sleep 2\n\t"
        "buffer_load_dwordx4 v[84:87], v120, %[srd], s66 offen sc1\n\t"
        "s_add_u32 s97, s97, 1\n\t"
        "s_cmp_lt_u32 s97, 0x100000\n\t"
        "s_cbranch_scc0 .Lgu%=\n\t"
        "s_waitcnt vmcnt(0)\n\t"
        "s_branch .Lsr%=\n\t"
        ".Lf1%=:\n\t"
        "ds_read_b128 v[92:95], v120 offset:0\n\t"
        "ds_read_b128 v[68:71], v120 offset:4096\n\t"
        "v_max_u32_e32 v54, v16, v18\n\t"
        "v_max3_u32 v54, v54, v17, v19\n\t"
        "v_max3_u32 v54, v54, v20, v22\n\t"
        "v_max3_u32 v54, v54, v21, v23\n\t"
        "v_max3_u32 v54, v54, v24, v26\n\t"
        "v_max3_u32 v54, v54, v25, v27\n\t"
        "v_max3_u32 v54, v54, v28, v30\n\t"
        "v_max3_u32 v54, v54, v29, v31\n\t"
        "v_max3_u32 v54, v54, v32, v34\n\t"
        "v_max3_u32 v54, v54, v33, v35\n\t"
        "v_max3_u32 v54, v54, v36, v38\n\t"
        "v_max3_u32 v54, v54, v37, v39\n\t"
        "v_max3_u32 v54, v54, v40, v42\n\t"
        "v_max3_u32 v54, v54, v41, v43\n\t"
        "v_max3_u32 v54, v54, v44, v46\n\t"
        "v_max3_u32 v54, v54, v45, v47\n\t"
        "v_max3_u32 v54, v54, v48, v50\n\t"
        "v_max3_u32 v54, v54, v49, v51\n\t"
        "v_max3_u32 v54, v54, v0, v2\n\t"
        "v_max3_u32 v54, v54, v1, v3\n\t"
        "v_max3_u32 v54, v54, v4, v6\n\t"
        "v_max3_u32 v54, v54, v5, v7\n\t"
        "v_max3_u32 v54, v54, v8, v10\n\t"
        "v_max3_u32 v54, v54, v9, v11\n\t"
        "s_waitcnt lgkmcnt(0)\n\t"
        "v_max3_u32 v54, v54, v92, v94\n\t"
        "v_max3_u32 v54, v54, v93, v95\n\t"
        "v_max3_u32 v54, v54, v68, v70\n\t"
        "v_max3_u32 v54, v54, v69, v71\n\t"
        "ds_read_b128 v[92:95], v120 offset:8192\n\t"
        "ds_read_b128 v[68:71], v120 offset:12288\n\t"
        "s_waitcnt lgkmcnt(0)\n\t"
        "v_max3_u32 v54, v54, v92, v94\n\t"
        "v_max3_u32 v54, v54, v93, v95\n\t"
        "v_max3_u32 v54, v54, v68, v70\n\t"
        "v_max3_u32 v54, v54, v69, v71\n\t"
        "v_cmp_eq_u32_e32 vcc, -1, v54\n\t"
        "s_nop 1\n\t"
        "v_mov_b32_e32 v55, v55\n\t"
        "s_nop 0\n\t"
        "s_cbranch_vccz .Lr1%=\n\t"
        "buffer_load_dwordx4 v[0:3], v120, %[srd], s57 offen sc1\n\t"
        "buffer_load_dwordx4 v[4:7], v120, %[srd], s58 offen sc1\n\t"
        "buffer_load_dwordx4 v[8:11], v120, %[srd], s59 offen sc1\n\t"
        "buffer_load_dwordx4 v[12:15], v120, %[srd], s60 offen sc1\n\t"
        "s_add_u32 s80, s40, 1\n\t"
        "v_mov_b32_e32 v54, 30720\n\t"
        "v_mov_b32_e32 v55, s80\n\t"
        "ds_write_b32 v54, v55\n\t"
        "ds_read_b128 v[16:19], v121 offset:0\n\t"
        "ds_read_b128 v[20:23], v121 offset:1024\n\t"
        "ds_read_b128 v[24:27], v121 offset:2048\n\t"
        "ds_read_b128 v[28:31], v121 offset:3072\n\t"
        "ds_read_b128 v[32:35], v121 offset:4096\n\t"
        "ds_read_b128 v[36:39], v121 offset:5120\n\t"
        "ds_read_b128 v[40:43], v121 offset:6144\n\t"
        "ds_read_b128 v[44:47], v121 offset:7168\n\t"
        "ds_read_b128 v[56:59], v121 offset:8192\n\t"
        "ds_read_b128 v[60:63], v121 offset:9216\n\t"
        "ds_read_b128 v[64:67], v121 offset:10240\n\t"
        "ds_read_b128 v[68:71], v121 offset:11264\n\t"
        "ds_read_b128 v[72:75], v121 offset:12288\n\t"
        "ds_read_b128 v[76:79], v121 offset:13312\n\t"
        "ds_read_b128 v[80:83], v121 offset:14336\n\t"
        "ds_read_b128 v[84:87], v121 offset:15360\n\t"
        "v_mov_b32_e32 v88, 0\n\t"
        "v_mov_b32_e32 v89, 0\n\t"
        "v_mov_b32_e32 v90, 0\n\t"
        "v_mov_b32_e32 v91, 0\n\t"
        "v_mov_b32_e32 v92, 0\n\t"
        "v_mov_b32_e32 v93, 0\n\t"
        "v_mov_b32_e32 v94, 0\n\t"
        "v_mov_b32_e32 v95, 0\n\t"
        "v_mov_b32_e32 v96, 0\n\t"
        "v_mov_b32_e32 v97, 0\n\t"
        "v_mov_b32_e32 v98, 0\n\t"
        "v_mov_b32_e32 v99, 0\n\t"
        "v_mov_b32_e32 v100, 0\n\t"
        "v_mov_b32_e32 v101, 0\n\t"
        "v_mov_b32_e32 v102, 0\n\t"
        "v_mov_b32_e32 v103, 0\n\t"
        "s_waitcnt lgkmcnt(0)\n\t"
        "s_barrier\n\t"
        ".Lfa%=:\n\t"
        "s_waitcnt vmcnt(0)\n\t"
        "v_max_u32_e32 v54, v0, v2\n\t"
        "v_max3_u32 v54, v54, v1, v3\n\t"
        "v_max3_u32 v54, v54, v4, v6\n\t"
        "v_max3_u32 v54, v54, v5, v7\n\t"
        "v_max3_u32 v54, v54, v8, v10\n\t"
        "v_max3_u32 v54, v54, v9, v11\n\t"
        "v_max3_u32 v54, v54, v12, v14\n\t"
        "v_max3_u32 v54, v54, v13, v15\n\t"
        "v_cmp_eq_u32_e32 vcc, -1, v54\n\t"
        "s_nop 1\n\t"
        "v_mov_b32_e32 v55, v55\n\t"
        "s_nop 0\n\t"
        "s_cbranch_vccz .Lfg%=\n\t"
        "s_sleep %c[ASLP]\n\t"
        "buffer_load_dwordx4 v[0:3], v120, %[srd], s57 offen sc1\n\t"
        "buffer_load_dwordx4 v[4:7], v120, %[srd], s58 offen sc1\n\t"
        "buffer_load_dwordx4 v[8:11], v120, %[srd], s59 offen sc1\n\t"
        "buffer_load_dwordx4 v[12:15], v120, %[srd], s60 offen sc1\n\t"
        "s_add_u32 s97, s97, 1\n\t"
        "s_cmp_lt_u32 s97, 0x100000\n\t"
        "s_cbranch_scc0 .Lgu%=\n\t"
        "s_branch .Lfa%=\n\t"
        ".Lfg%=:\n\t"
        "s_sleep %c[RSLP]\n\t"
        "s_branch .Lgo%=\n\t"
        ".Lfb%=:\n\t"
        "s_nop 7\n\t"
        "s_mov_b32 s97, 0\n\t"
        "s_add_u32 s80, s40, 1\n\t"
        "v_mov_b32_e32 v54, 30724\n\t"
        "v_mov_b32_e32 v55, s80\n\t"
        "ds_write_b32 v54, v55\n\t"
        ".Lfp%=:\n\t"
        "s_sleep %c[ASLP]\n\t"
        "buffer_load_dword v88, v127, %[srd], s61 offen offset:0 sc1\n\t"
        "buffer_load_dword v89, v127, %[srd], s61 offen offset:256 sc1\n\t"
        "s_add_u32 s97, s97, 1\n\t"
        "s_cmp_lt_u32 s97, 0x100000\n\t"
        "s_cbranch_scc0 .Lgu%=\n\t"
        "s_waitcnt vmcnt(0)\n\t"
        "v_max_u32_e32 v54, v88, v89\n\t"
        "v_cmp_eq_u32_e32 vcc, -1, v54\n\t"
        "s_nop 1\n\t"
        "v_mov_b32_e32 v55, v55\n\t"
        "s_nop 0\n\t"
        "s_cbranch_vccnz .Lfp%=\n\t"
        "v_mfma_f32_16x16x32_bf16 v[40:43], v[112:115], v[36:39], 0\n\t"
        "v_mfma_f32_16x16x32_bf16 v[44:47], v[116:119], v[36:39], 0\n\t"
        "s_nop 1\n\t"
        "v_mov_b32_e32 v55, v55\n\t"
        "s_nop 1\n\t"
        "v_mov_b32_e32 v55, v55\n\t"
        "s_nop 0\n\t"
        "v_pk_mul_f32 v[40:41], v[40:41], v[10:11] op_sel_hi:[1,0]\n\t"
        "v_pk_mul_f32 v[42:43], v[42:43], v[10:11] op_sel_hi:[1,0]\n\t"
        "v_pk_mul_f32 v[44:45], v[44:45], v[10:11] op_sel_hi:[1,0]\n\t"
        "v_pk_mul_f32 v[46:47], v[46:47], v[10:11] op_sel_hi:[1,0]\n\t"
        "v_mfma_f32_16x16x16_bf16 v[40:43], v[88:89], v[90:91], v[40:43]\n\t"
        "s_nop 0\n\t"
        "v_mfma_f32_16x16x16_bf16 v[44:47], v[88:89], v[94:95], v[44:47]\n\t"
        "s_nop 7\n\t"
        "s_branch .Lrb%=\n\t"
        ".Lta%=:\n\t"
        "s_waitcnt lgkmcnt(0)\n\t"
        "s_branch .Lxa%=\n\t"
        ".Lgu%=:\n\t"
        "s_mov_b64 exec, -1\n\t"
        "v_mov_b32_e32 v54, %c[ferr]\n\t"
        "v_mov_b32_e32 v55, 1\n\t"
        "buffer_store_dword v55, v54, %[wsd], 0 offen sc1\n\t"
        "s_endpgm\n\t"
        ".Lend%=:\n\t"
        "s_nop 0\n\t"
        "v_permlane16_swap_b32_e32 v50, v51\n\t"
        "v_mov_b32_e32 v0, 64\n\t"
        "buffer_store_dwordx2 v[50:51], v34, %[wsd], s72 offen sc1\n\t"
        "s_cmp_lg_u32 %[w], 0\n\t"
        "s_waitcnt vmcnt(0)\n\t"
        "s_barrier\n\t"
        "s_cbranch_scc1 .Le2%=\n\t"
        "s_mov_b64 exec, 1\n\t"
        "buffer_store_dword v0, off, %[wsd], %[pra] sc1\n\t"
        "s_mov_b64 exec, -1\n\t"
        ".Le2%=:\n\t"
        "s_endpgm"
        :
        : [srd] "s"(rhd), [wsd] "s"(wsd), [w] "s"(w), [tag] "s"(tag), [nA] "s"(nA), [nS] "s"(nS), [nW] "s"(nW), [nC] "s"(nC), [b0] "s"(b0), [b1] "s"(b1), [kx] "s"(kx), [vx] "s"(vx), [kf] "s"(kf), [kvo] "s"(kvo), [pob] "s"(pob),
          [swo] "s"(swo), [pra] "s"(pr_a), [ferr] "i"(F_ERR), [iD1] "i"(D1), [iD2] "i"(D2), [LC] "i"(LC0), [LA] "i"(LA0), [LC24] "i"(LC0 + 24), [LA24] "i"(LA0 + 24), [E1] "i"(2 * (D1 - LC0)), [E2] "i"(2 * (D2 - LA0)),
          [iPEN] "i"(PEN), [iPEN2] "i"(PEN2), [iPENM] "i"(PEN + 2 * PEN2), [iPEN0] "i"(PEN0), [iTOL] "i"(TOL), [iCT0] "i"(CT0), [iX0C] "i"(X0C), [iX0A] "i"(X0A), [iPHM] "i"(PH_M),
          [iFT] "i"(-3), [iFN] "i"(FINE_N), [AUP] "i"(A_UP), [ASLP] "i"(A_SLP), [LDEC] "i"(L_DEC), [UPSH] "i"(UP_SH), [TRUP] "i"(TR_UP), [TRDN] "i"(TR_DN), [HSLP] "i"(H_SLP), [iPMIN] "i"(PMIN), [VSLP] "i"(V_SLP), [RSLP] "i"(R_SLP), [iVD] "i"(V_DEL), [iXR] "i"(XR),
          "{v[120:127]}"(pc), "{v[52:53]}"(pd), "{a[0:31]}"(wa[0]), "{a[32:63]}"(wa[1]), "{a[64:95]}"(wa[2]), "{a[96:127]}"(wa[3]), "{a[128:159]}"(wa[4]), "{a[160:191]}"(wa[5]), "{a[192:223]}"(wa[6]), "{a[224:255]}"(wa[7]), "{v[128:159]}"(wg[0]), "{v[160:191]}"(wg[1]), "{v[192:223]}"(wg[2]), "{v[224:255]}"(wg[3])
        : "memory", "vcc", "scc", "s36", "s37", "s38", "s39", "s40", "s41", "s42", "s43", "s44", "s45", "s46", "s47", "s48", "s49", "s50", "s51", "s52", "s53", "s54", "s55", "s56", "s57", "s58", "s59", "s60", "s61", "s62", "s63", "s64", "s65", "s66", "s67", "s68", "s69", "s70", "s71", "s72", "s73", "s74", "s75", "s76", "s77", "s78", "s79", "s80", "s81", "s82", "s83", "s84", "s85", "s86", "s87", "s88", "s89", "s90", "s91", "s92", "s93", "s94", "s95", "s96", "s97", "s98", "s99");
    } else {
        const int cb = i32;
        unsigned m0v = *(const unsigned*)(p.mem0 + (4 * G + ((l >> 2) & 1) + ((l >> 3) & 2)) * 1024 + cb * 32 + (l >> 5) * 16 + (l & 8) + (l & 3) * 2);
        {
            const v4u sv = mk_srd(p.Wv, 1024 * 4096 * 2), so = mk_srd(p.Wo, 1024 * 4096 * 2);
            const unsigned key = KEYM & ((cb & 1) * 2 + G), m0b = w * 32768, f = (g & 1) | ((c >> 1) << 1);
            const unsigned t0 = m0b + (16 * (g >> 1) + 4 * (g & 1) + (c & 1) + 8 * (c >> 1)) * 128 + (f << 5) + (n16 & 3) * 8, t1 = m0b + (16 * g + 4 * (c >> 1) + (c & 1)) * 64 + ((c >> 1) << 5) + (n16 & 3) * 8;
            v16u pa;
            pa[0] = (64 * w + (l >> 3)) * 8192 + vi * 128 + (((l & 7) ^ ((l >> 5) << 1)) << 4); pa[1] = pa[0] ^ 64u;
            pa[2] = (1024 * w + (l >> 2)) * 2048 + cb * 64 + (((l & 3) ^ ((g & 1) << 1)) << 4);
#pragma unroll
            for (int q = 0; q < 4; ++q) pa[3 + q] = t0 ^ (((w + q) & 3) << 5);
            pa[7] = t1; pa[8] = t1 ^ 32u; pa[9] = cal_a; pa[10] = tag; pa[11] = near; pa[13] = 0u; pa[14] = 0u; pa[15] = 0u;
            const unsigned tu = cb >> 2; pa[12] = (unsigned)((w * 1024 + cb * 32) * 2048 + l * 1024) + (((3u - (so.x >> 7)) & 7u) << 7);
    asm volatile(
        "s_mov_b32 s82, 3\n\t"
        ".Ld0%=:\n\t"
        "s_xor_b32 s80, s82, %[key]\n\t"
        "s_mul_i32 s81, s80, 131072\n\t"
        "s_lshl_b32 s80, s80, 11\n\t"
        "s_add_u32 s80, s80, %[m0b]\n\t"
        "s_add_u32 m0, s80, 0\n\t"
        "s_add_u32 s81, s81, 0\n\t"
        "buffer_load_dwordx4 v112, %[sv], s81 offen lds\n\t"
        "s_add_u32 m0, m0, 0x400\n\t"
        "s_add_u32 s81, s81, 65536\n\t"
        "buffer_load_dwordx4 v113, %[sv], s81 offen lds\n\t"
        "s_sub_u32 s82, s82, 1\n\t"
        "s_cbranch_scc0 .Ld0%=\n\t"
        "s_mov_b32 s82, 3\n\t"
        ".Ld1%=:\n\t"
        "s_xor_b32 s80, s82, %[key]\n\t"
        "s_mul_i32 s81, s80, 131072\n\t"
        "s_lshl_b32 s80, s80, 11\n\t"
        "s_add_u32 s80, s80, %[m0b]\n\t"
        "s_add_u32 m0, s80, 8192\n\t"
        "s_add_u32 s81, s81, 2097152\n\t"
        "buffer_load_dwordx4 v112, %[sv], s81 offen lds\n\t"
        "s_add_u32 m0, m0, 0x400\n\t"
        "s_add_u32 s81, s81, 65536\n\t"
        "buffer_load_dwordx4 v113, %[sv], s81 offen lds\n\t"
        "s_sub_u32 s82, s82, 1\n\t"
        "s_cbranch_scc0 .Ld1%=\n\t"
        "s_cmp_eq_u32 %[tu], 0\n\t"
        "s_cselect_b32 s81, 0, 0x7fff0000\n\t"
        "buffer_load_dword v125, v124, %[so], s81 offen\n\t"
        "s_mov_b32 s82, 3\n\t"
        ".Ld2%=:\n\t"
        "s_xor_b32 s80, s82, %[key]\n\t"
        "s_mul_i32 s81, s80, 131072\n\t"
        "s_lshl_b32 s80, s80, 11\n\t"
        "s_add_u32 s80, s80, %[m0b]\n\t"
        "s_add_u32 m0, s80, 16384\n\t"
        "s_add_u32 s81, s81, 4194304\n\t"
        "buffer_load_dwordx4 v112, %[sv], s81 offen lds\n\t"
        "s_add_u32 m0, m0, 0x400\n\t"
        "s_add_u32 s81, s81, 65536\n\t"
        "buffer_load_dwordx4 v113, %[sv], s81 offen lds\n\t"
        "s_sub_u32 s82, s82, 1\n\t"
        "s_cbranch_scc0 .Ld2%=\n\t"
        "s_cmp_eq_u32 %[tu], 1\n\t"
        "s_cselect_b32 s81, 0, 0x7fff0000\n\t"
        "buffer_load_dword v125, v124, %[so], s81 offen\n\t"
        "s_mov_b32 s82, 3\n\t"
        ".Ld3%=:\n\t"
        "s_xor_b32 s80, s82, %[key]\n\t"
        "s_mul_i32 s81, s80, 131072\n\t"
        "s_lshl_b32 s80, s80, 11\n\t"
        "s_add_u32 s80, s80, %[m0b]\n\t"
        "s_add_u32 m0, s80, 24576\n\t"
        "s_add_u32 s81, s81, 6291456\n\t"
        "buffer_load_dwordx4 v112, %[sv], s81 offen lds\n\t"
        "s_add_u32 m0, m0, 0x400\n\t"
        "s_add_u32 s81, s81, 65536\n\t"
        "buffer_load_dwordx4 v113, %[sv], s81 offen lds\n\t"
        "s_sub_u32 s82, s82, 1\n\t"
        "s_cbranch_scc0 .Ld3%=\n\t"
        "s_waitcnt vmcnt(34)\n\t"
        "buffer_store_dwordx2 v[122:123], v121, %[wsd], 0 offen sc1\n\t"
        "s_waitcnt vmcnt(27)\n\t"
        "ds_read_b64_tr_b16 v[128:129], v115 offset:0\n\t"
        "ds_read_b64_tr_b16 v[130:131], v115 offset:256\n\t"
        "ds_read_b64_tr_b16 v[132:133], v115 offset:4096\n\t"
        "ds_read_b64_tr_b16 v[134:135], v115 offset:4352\n\t"
        "ds_read_b64_tr_b16 v[136:137], v116 offset:0\n\t"
        "ds_read_b64_tr_b16 v[138:139], v116 offset:256\n\t"
        "ds_read_b64_tr_b16 v[140:141], v116 offset:4096\n\t"
        "ds_read_b64_tr_b16 v[142:143], v116 offset:4352\n\t"
        "ds_read_b64_tr_b16 v[144:145], v117 offset:0\n\t"
        "ds_read_b64_tr_b16 v[146:147], v117 offset:256\n\t"
        "ds_read_b64_tr_b16 v[148:149], v117 offset:4096\n\t"
        "ds_read_b64_tr_b16 v[150:151], v117 offset:4352\n\t"
        "ds_read_b64_tr_b16 v[152:153], v118 offset:0\n\t"
        "ds_read_b64_tr_b16 v[154:155], v118 offset:256\n\t"
        "ds_read_b64_tr_b16 v[156:157], v118 offset:4096\n\t"
        "ds_read_b64_tr_b16 v[158:159], v118 offset:4352\n\t"
        "s_waitcnt lgkmcnt(0)\n\t"
        "s_cmp_eq_u32 %[tu], 2\n\t"
        "s_cselect_b32 s81, 0, 0x7fff0000\n\t"
        "buffer_load_dword v125, v124, %[so], s81 offen\n\t"
        "s_mov_b32 s82, 3\n\t"
        ".Ld4%=:\n\t"
        "s_xor_b32 s80, s82, %[key]\n\t"
        "s_mul_i32 s81, s80, 65536\n\t"
        "s_lshl_b32 s80, s80, 11\n\t"
        "s_add_u32 s80, s80, %[m0b]\n\t"
        "s_add_u32 m0, s80, 0\n\t"
        "s_add_u32 s81, s81, 0\n\t"
        "buffer_load_dwordx4 v114, %[so], s81 offen lds\n\t"
        "s_add_u32 m0, m0, 0x400\n\t"
        "s_add_u32 s81, s81, 32768\n\t"
        "buffer_load_dwordx4 v114, %[so], s81 offen lds\n\t"
        "s_sub_u32 s82, s82, 1\n\t"
        "s_cbranch_scc0 .Ld4%=\n\t"
        "s_waitcnt vmcnt(28)\n\t"
        "ds_read_b64_tr_b16 v[160:161], v115 offset:8192\n\t"
        "ds_read_b64_tr_b16 v[162:163], v115 offset:8448\n\t"
        "ds_read_b64_tr_b16 v[164:165], v115 offset:12288\n\t"
        "ds_read_b64_tr_b16 v[166:167], v115 offset:12544\n\t"
        "ds_read_b64_tr_b16 v[168:169], v116 offset:8192\n\t"
        "ds_read_b64_tr_b16 v[170:171], v116 offset:8448\n\t"
        "ds_read_b64_tr_b16 v[172:173], v116 offset:12288\n\t"
        "ds_read_b64_tr_b16 v[174:175], v116 offset:12544\n\t"
        "ds_read_b64_tr_b16 v[176:177], v117 offset:8192\n\t"
        "ds_read_b64_tr_b16 v[178:179], v117 offset:8448\n\t"
        "ds_read_b64_tr_b16 v[180:181], v117 offset:12288\n\t"
        "ds_read_b64_tr_b16 v[182:183], v117 offset:12544\n\t"
        "ds_read_b64_tr_b16 v[184:185], v118 offset:8192\n\t"
        "ds_read_b64_tr_b16 v[186:187], v118 offset:8448\n\t"
        "ds_read_b64_tr_b16 v[188:189], v118 offset:12288\n\t"
        "ds_read_b64_tr_b16 v[190:191], v118 offset:12544\n\t"
        "s_waitcnt lgkmcnt(0)\n\t"
        "s_cmp_eq_u32 %[tu], 3\n\t"
        "s_cselect_b32 s81, 0, 0x7fff0000\n\t"
        "buffer_load_dword v125, v124, %[so], s81 offen\n\t"
        "s_mov_b32 s82, 3\n\t"
        ".Ld5%=:\n\t"
        "s_xor_b32 s80, s82, %[key]\n\t"
        "s_mul_i32 s81, s80, 65536\n\t"
        "s_lshl_b32 s80, s80, 11\n\t"
        "s_add_u32 s80, s80, %[m0b]\n\t"
        "s_add_u32 m0, s80, 8192\n\t"
        "s_add_u32 s81, s81, 262144\n\t"
        "buffer_load_dwordx4 v114, %[so], s81 offen lds\n\t"
        "s_add_u32 m0, m0, 0x400\n\t"
        "s_add_u32 s81, s81, 32768\n\t"
        "buffer_load_dwordx4 v114, %[so], s81 offen lds\n\t"
        "s_sub_u32 s82, s82, 1\n\t"
        "s_cbranch_scc0 .Ld5%=\n\t"
        "s_waitcnt vmcnt(28)\n\t"
        "ds_read_b64_tr_b16 v[192:193], v115 offset:16384\n\t"
        "ds_read_b64_tr_b16 v[194:195], v115 offset:16640\n\t"
        "ds_read_b64_tr_b16 v[196:197], v115 offset:20480\n\t"
        "ds_read_b64_tr_b16 v[198:199], v115 offset:20736\n\t"
        "ds_read_b64_tr_b16 v[200:201], v116 offset:16384\n\t"
        "ds_read_b64_tr_b16 v[202:203], v116 offset:16640\n\t"
        "ds_read_b64_tr_b16 v[204:205], v116 offset:20480\n\t"
        "ds_read_b64_tr_b16 v[206:207], v116 offset:20736\n\t"
        "ds_read_b64_tr_b16 v[208:209], v117 offset:16384\n\t"
        "ds_read_b64_tr_b16 v[210:211], v117 offset:16640\n\t"
        "ds_read_b64_tr_b16 v[212:213], v117 offset:20480\n\t"
        "ds_read_b64_tr_b16 v[214:215], v117 offset:20736\n\t"
        "ds_read_b64_tr_b16 v[216:217], v118 offset:16384\n\t"
        "ds_read_b64_tr_b16 v[218:219], v118 offset:16640\n\t"
        "ds_read_b64_tr_b16 v[220:221], v118 offset:20480\n\t"
        "ds_read_b64_tr_b16 v[222:223], v118 offset:20736\n\t"
        "s_waitcnt lgkmcnt(0)\n\t"
        "s_cmp_eq_u32 %[tu], 4\n\t"
        "s_cselect_b32 s81, 0, 0x7fff0000\n\t"
        "buffer_load_dword v125, v124, %[so], s81 offen\n\t"
        "s_mov_b32 s82, 3\n\t"
        ".Ld6%=:\n\t"
        "s_xor_b32 s80, s82, %[key]\n\t"
        "s_mul_i32 s81, s80, 65536\n\t"
        "s_lshl_b32 s80, s80, 11\n\t"
        "s_add_u32 s80, s80, %[m0b]\n\t"
        "s_add_u32 m0, s80, 16384\n\t"
        "s_add_u32 s81, s81, 524288\n\t"
        "buffer_load_dwordx4 v114, %[so], s81 offen lds\n\t"
        "s_add_u32 m0, m0, 0x400\n\t"
        "s_add_u32 s81, s81, 32768\n\t"
        "buffer_load_dwordx4 v114, %[so], s81 offen lds\n\t"
        "s_sub_u32 s82, s82, 1\n\t"
        "s_cbranch_scc0 .Ld6%=\n\t"
        "s_waitcnt vmcnt(28)\n\t"
        "ds_read_b64_tr_b16 v[224:225], v115 offset:24576\n\t"
        "ds_read_b64_tr_b16 v[226:227], v115 offset:24832\n\t"
        "ds_read_b64_tr_b16 v[228:229], v115 offset:28672\n\t"
        "ds_read_b64_tr_b16 v[230:231], v115 offset:28928\n\t"
        "ds_read_b64_tr_b16 v[232:233], v116 offset:24576\n\t"
        "ds_read_b64_tr_b16 v[234:235], v116 offset:24832\n\t"
        "ds_read_b64_tr_b16 v[236:237], v116 offset:28672\n\t"
        "ds_read_b64_tr_b16 v[238:239], v116 offset:28928\n\t"
        "ds_read_b64_tr_b16 v[240:241], v117 offset:24576\n\t"
        "ds_read_b64_tr_b16 v[242:243], v117 offset:24832\n\t"
        "ds_read_b64_tr_b16 v[244:245], v117 offset:28672\n\t"
        "ds_read_b64_tr_b16 v[246:247], v117 offset:28928\n\t"
        "ds_read_b64_tr_b16 v[248:249], v118 offset:24576\n\t"
        "ds_read_b64_tr_b16 v[250:251], v118 offset:24832\n\t"
        "ds_read_b64_tr_b16 v[252:253], v118 offset:28672\n\t"
        "ds_read_b64_tr_b16 v[254:255], v118 offset:28928\n\t"
        "s_waitcnt lgkmcnt(0)\n\t"
        "s_cmp_eq_u32 %[tu], 5\n\t"
        "s_cselect_b32 s81, 0, 0x7fff0000\n\t"
        "buffer_load_dword v125, v124, %[so], s81 offen\n\t"
        "s_mov_b32 s82, 3\n\t"
        ".Ld7%=:\n\t"
        "s_xor_b32 s80, s82, %[key]\n\t"
        "s_mul_i32 s81, s80, 65536\n\t"
        "s_lshl_b32 s80, s80, 11\n\t"
        "s_add_u32 s80, s80, %[m0b]\n\t"
        "s_add_u32 m0, s80, 24576\n\t"
        "s_add_u32 s81, s81, 786432\n\t"
        "buffer_load_dwordx4 v114, %[so], s81 offen lds\n\t"
        "s_add_u32 m0, m0, 0x400\n\t"
        "s_add_u32 s81, s81, 32768\n\t"
        "buffer_load_dwordx4 v114, %[so], s81 offen lds\n\t"
        "s_sub_u32 s82, s82, 1\n\t"
        "s_cbranch_scc0 .Ld7%=\n\t"
        "s_waitcnt vmcnt(27)\n\t"
        "ds_read_b64_tr_b16 a[0:1], v119 offset:0\n\t"
        "ds_read_b64_tr_b16 a[2:3], v119 offset:128\n\t"
        "ds_read_b64_tr_b16 a[4:5], v119 offset:4096\n\t"
        "ds_read_b64_tr_b16 a[6:7], v119 offset:4224\n\t"
        "ds_read_b64_tr_b16 a[8:9], v120 offset:0\n\t"
        "ds_read_b64_tr_b16 a[10:11], v120 offset:128\n\t"
        "ds_read_b64_tr_b16 a[12:13], v120 offset:4096\n\t"
        "ds_read_b64_tr_b16 a[14:15], v120 offset:4224\n\t"
        "ds_read_b64_tr_b16 a[16:17], v119 offset:512\n\t"
        "ds_read_b64_tr_b16 a[18:19], v119 offset:640\n\t"
        "ds_read_b64_tr_b16 a[20:21], v119 offset:4608\n\t"
        "ds_read_b64_tr_b16 a[22:23], v119 offset:4736\n\t"
        "ds_read_b64_tr_b16 a[24:25], v120 offset:512\n\t"
        "ds_read_b64_tr_b16 a[26:27], v120 offset:640\n\t"
        "ds_read_b64_tr_b16 a[28:29], v120 offset:4608\n\t"
        "ds_read_b64_tr_b16 a[30:31], v120 offset:4736\n\t"
        "s_waitcnt lgkmcnt(0)\n\t"
        "s_cmp_eq_u32 %[tu], 6\n\t"
        "s_cselect_b32 s81, 0, 0x7fff0000\n\t"
        "buffer_load_dword v125, v124, %[so], s81 offen\n\t"
        "s_mov_b32 s82, 3\n\t"
        ".Ld8%=:\n\t"
        "s_xor_b32 s80, s82, %[key]\n\t"
        "s_mul_i32 s81, s80, 65536\n\t"
        "s_lshl_b32 s80, s80, 11\n\t"
        "s_add_u32 s80, s80, %[m0b]\n\t"
        "s_add_u32 m0, s80, 0\n\t"
        "s_add_u32 s81, s81, 1048576\n\t"
        "buffer_load_dwordx4 v114, %[so], s81 offen lds\n\t"
        "s_add_u32 m0, m0, 0x400\n\t"
        "s_add_u32 s81, s81, 32768\n\t"
        "buffer_load_dwordx4 v114, %[so], s81 offen lds\n\t"
        "s_sub_u32 s82, s82, 1\n\t"
        "s_cbranch_scc0 .Ld8%=\n\t"
        "s_waitcnt vmcnt(27)\n\t"
        "ds_read_b64_tr_b16 a[32:33], v119 offset:8192\n\t"
        "ds_read_b64_tr_b16 a[34:35], v119 offset:8320\n\t"
        "ds_read_b64_tr_b16 a[36:37], v119 offset:12288\n\t"
        "ds_read_b64_tr_b16 a[38:39], v119 offset:12416\n\t"
        "ds_read_b64_tr_b16 a[40:41], v120 offset:8192\n\t"
        "ds_read_b64_tr_b16 a[42:43], v120 offset:8320\n\t"
        "ds_read_b64_tr_b16 a[44:45], v120 offset:12288\n\t"
        "ds_read_b64_tr_b16 a[46:47], v120 offset:12416\n\t"
        "ds_read_b64_tr_b16 a[48:49], v119 offset:8704\n\t"
        "ds_read_b64_tr_b16 a[50:51], v119 offset:8832\n\t"
        "ds_read_b64_tr_b16 a[52:53], v119 offset:12800\n\t"
        "ds_read_b64_tr_b16 a[54:55], v119 offset:12928\n\t"
        "ds_read_b64_tr_b16 a[56:57], v120 offset:8704\n\t"
        "ds_read_b64_tr_b16 a[58:59], v120 offset:8832\n\t"
        "ds_read_b64_tr_b16 a[60:61], v120 offset:12800\n\t"
        "ds_read_b64_tr_b16 a[62:63], v120 offset:12928\n\t"
        "s_waitcnt lgkmcnt(0)\n\t"
        "s_cmp_eq_u32 %[tu], 7\n\t"
        "s_cselect_b32 s81, 0, 0x7fff0000\n\t"
        "buffer_load_dword v125, v124, %[so], s81 offen\n\t"
        "s_mov_b32 s82, 3\n\t"
        ".Ld9%=:\n\t"
        "s_xor_b32 s80, s82, %[key]\n\t"
        "s_mul_i32 s81, s80, 65536\n\t"
        "s_lshl_b32 s80, s80, 11\n\t"
        "s_add_u32 s80, s80, %[m0b]\n\t"
        "s_add_u32 m0, s80, 8192\n\t"
        "s_add_u32 s81, s81, 1310720\n\t"
        "buffer_load_dwordx4 v114, %[so], s81 offen lds\n\t"
        "s_add_u32 m0, m0, 0x400\n\t"
        "s_add_u32 s81, s81, 32768\n\t"
        "buffer_load_dwordx4 v114, %[so], s81 offen lds\n\t"
        "s_sub_u32 s82, s82, 1\n\t"
        "s_cbranch_scc0 .Ld9%=\n\t"
        "s_waitcnt vmcnt(27)\n\t"
        "ds_read_b64_tr_b16 a[64:65], v119 offset:16384\n\t"
        "ds_read_b64_tr_b16 a[66:67], v119 offset:16512\n\t"
        "ds_read_b64_tr_b16 a[68:69], v119 offset:20480\n\t"
        "ds_read_b64_tr_b16 a[70:71], v119 offset:20608\n\t"
        "ds_read_b64_tr_b16 a[72:73], v120 offset:16384\n\t"
        "ds_read_b64_tr_b16 a[74:75], v120 offset:16512\n\t"
        "ds_read_b64_tr_b16 a[76:77], v120 offset:20480\n\t"
        "ds_read_b64_tr_b16 a[78:79], v120 offset:20608\n\t"
        "ds_read_b64_tr_b16 a[80:81], v119 offset:16896\n\t"
        "ds_read_b64_tr_b16 a[82:83], v119 offset:17024\n\t"
        "ds_read_b64_tr_b16 a[84:85], v119 offset:20992\n\t"
        "ds_read_b64_tr_b16 a[86:87], v119 offset:21120\n\t"
        "ds_read_b64_tr_b16 a[88:89], v120 offset:16896\n\t"
        "ds_read_b64_tr_b16 a[90:91], v120 offset:17024\n\t"
        "ds_read_b64_tr_b16 a[92:93], v120 offset:20992\n\t"
        "ds_read_b64_tr_b16 a[94:95], v120 offset:21120\n\t"
        "s_waitcnt lgkmcnt(0)\n\t"
        "s_mov_b32 s82, 3\n\t"
        ".Ld10%=:\n\t"
        "s_xor_b32 s80, s82, %[key]\n\t"
        "s_mul_i32 s81, s80, 65536\n\t"
        "s_lshl_b32 s80, s80, 11\n\t"
        "s_add_u32 s80, s80, %[m0b]\n\t"
        "s_add_u32 m0, s80, 16384\n\t"
        "s_add_u32 s81, s81, 1572864\n\t"
        "buffer_load_dwordx4 v114, %[so], s81 offen lds\n\t"
        "s_add_u32 m0, m0, 0x400\n\t"
        "s_add_u32 s81, s81, 32768\n\t"
        "buffer_load_dwordx4 v114, %[so], s81 offen lds\n\t"
        "s_sub_u32 s82, s82, 1\n\t"
        "s_cbranch_scc0 .Ld10%=\n\t"
        "s_waitcnt vmcnt(26)\n\t"
        "ds_read_b64_tr_b16 a[96:97], v119 offset:24576\n\t"
        "ds_read_b64_tr_b16 a[98:99], v119 offset:24704\n\t"
        "ds_read_b64_tr_b16 a[100:101], v119 offset:28672\n\t"
        "ds_read_b64_tr_b16 a[102:103], v119 offset:28800\n\t"
        "ds_read_b64_tr_b16 a[104:105], v120 offset:24576\n\t"
        "ds_read_b64_tr_b16 a[106:107], v120 offset:24704\n\t"
        "ds_read_b64_tr_b16 a[108:109], v120 offset:28672\n\t"
        "ds_read_b64_tr_b16 a[110:111], v120 offset:28800\n\t"
        "ds_read_b64_tr_b16 a[112:113], v119 offset:25088\n\t"
        "ds_read_b64_tr_b16 a[114:115], v119 offset:25216\n\t"
        "ds_read_b64_tr_b16 a[116:117], v119 offset:29184\n\t"
        "ds_read_b64_tr_b16 a[118:119], v119 offset:29312\n\t"
        "ds_read_b64_tr_b16 a[120:121], v120 offset:25088\n\t"
        "ds_read_b64_tr_b16 a[122:123], v120 offset:25216\n\t"
        "ds_read_b64_tr_b16 a[124:125], v120 offset:29184\n\t"
        "ds_read_b64_tr_b16 a[126:127], v120 offset:29312\n\t"
        "s_waitcnt lgkmcnt(0)\n\t"
        "s_mov_b32 s82, 3\n\t"
        ".Ld11%=:\n\t"
        "s_xor_b32 s80, s82, %[key]\n\t"
        "s_mul_i32 s81, s80, 65536\n\t"
        "s_lshl_b32 s80, s80, 11\n\t"
        "s_add_u32 s80, s80, %[m0b]\n\t"
        "s_add_u32 m0, s80, 24576\n\t"
        "s_add_u32 s81, s81, 1835008\n\t"
        "buffer_load_dwordx4 v114, %[so], s81 offen lds\n\t"
        "s_add_u32 m0, m0, 0x400\n\t"
        "s_add_u32 s81, s81, 32768\n\t"
        "buffer_load_dwordx4 v114, %[so], s81 offen lds\n\t"
        "s_sub_u32 s82, s82, 1\n\t"
        "s_cbranch_scc0 .Ld11%=\n\t"
        "s_waitcnt vmcnt(25)\n\t"
        "ds_read_b64_tr_b16 a[128:129], v119 offset:0\n\t"
        "ds_read_b64_tr_b16 a[130:131], v119 offset:128\n\t"
        "ds_read_b64_tr_b16 a[132:133], v119 offset:4096\n\t"
        "ds_read_b64_tr_b16 a[134:135], v119 offset:4224\n\t"
        "ds_read_b64_tr_b16 a[136:137], v120 offset:0\n\t"
        "ds_read_b64_tr_b16 a[138:139], v120 offset:128\n\t"
        "ds_read_b64_tr_b16 a[140:141], v120 offset:4096\n\t"
        "ds_read_b64_tr_b16 a[142:143], v120 offset:4224\n\t"
        "ds_read_b64_tr_b16 a[144:145], v119 offset:512\n\t"
        "ds_read_b64_tr_b16 a[146:147], v119 offset:640\n\t"
        "ds_read_b64_tr_b16 a[148:149], v119 offset:4608\n\t"
        "ds_read_b64_tr_b16 a[150:151], v119 offset:4736\n\t"
        "ds_read_b64_tr_b16 a[152:153], v120 offset:512\n\t"
        "ds_read_b64_tr_b16 a[154:155], v120 offset:640\n\t"
        "ds_read_b64_tr_b16 a[156:157], v120 offset:4608\n\t"
        "ds_read_b64_tr_b16 a[158:159], v120 offset:4736\n\t"
        "s_waitcnt lgkmcnt(0)\n\t"
        "s_waitcnt vmcnt(16)\n\t"
        "ds_read_b64_tr_b16 a[160:161], v119 offset:8192\n\t"
        "ds_read_b64_tr_b16 a[162:163], v119 offset:8320\n\t"
        "ds_read_b64_tr_b16 a[164:165], v119 offset:12288\n\t"
        "ds_read_b64_tr_b16 a[166:167], v119 offset:12416\n\t"
        "ds_read_b64_tr_b16 a[168:169], v120 offset:8192\n\t"
        "ds_read_b64_tr_b16 a[170:171], v120 offset:8320\n\t"
        "ds_read_b64_tr_b16 a[172:173], v120 offset:12288\n\t"
        "ds_read_b64_tr_b16 a[174:175], v120 offset:12416\n\t"
        "ds_read_b64_tr_b16 a[176:177], v119 offset:8704\n\t"
        "ds_read_b64_tr_b16 a[178:179], v119 offset:8832\n\t"
        "ds_read_b64_tr_b16 a[180:181], v119 offset:12800\n\t"
        "ds_read_b64_tr_b16 a[182:183], v119 offset:12928\n\t"
        "ds_read_b64_tr_b16 a[184:185], v120 offset:8704\n\t"
        "ds_read_b64_tr_b16 a[186:187], v120 offset:8832\n\t"
        "ds_read_b64_tr_b16 a[188:189], v120 offset:12800\n\t"
        "ds_read_b64_tr_b16 a[190:191], v120 offset:12928\n\t"
        "s_waitcnt lgkmcnt(0)\n\t"
        "s_waitcnt vmcnt(8)\n\t"
        "ds_read_b64_tr_b16 a[192:193], v119 offset:16384\n\t"
        "ds_read_b64_tr_b16 a[194:195], v119 offset:16512\n\t"
        "ds_read_b64_tr_b16 a[196:197], v119 offset:20480\n\t"
        "ds_read_b64_tr_b16 a[198:199], v119 offset:20608\n\t"
        "ds_read_b64_tr_b16 a[200:201], v120 offset:16384\n\t"
        "ds_read_b64_tr_b16 a[202:203], v120 offset:16512\n\t"
        "ds_read_b64_tr_b16 a[204:205], v120 offset:20480\n\t"
        "ds_read_b64_tr_b16 a[206:207], v120 offset:20608\n\t"
        "ds_read_b64_tr_b16 a[208:209], v119 offset:16896\n\t"
        "ds_read_b64_tr_b16 a[210:211], v119 offset:17024\n\t"
        "ds_read_b64_tr_b16 a[212:213], v119 offset:20992\n\t"
        "ds_read_b64_tr_b16 a[214:215], v119 offset:21120\n\t"
        "ds_read_b64_tr_b16 a[216:217], v120 offset:16896\n\t"
        "ds_read_b64_tr_b16 a[218:219], v120 offset:17024\n\t"
        "ds_read_b64_tr_b16 a[220:221], v120 offset:20992\n\t"
        "ds_read_b64_tr_b16 a[222:223], v120 offset:21120\n\t"
        "s_waitcnt lgkmcnt(0)\n\t"
        "s_waitcnt vmcnt(0)\n\t"
        "ds_read_b64_tr_b16 a[224:225], v119 offset:24576\n\t"
        "ds_read_b64_tr_b16 a[226:227], v119 offset:24704\n\t"
        "ds_read_b64_tr_b16 a[228:229], v119 offset:28672\n\t"
        "ds_read_b64_tr_b16 a[230:231], v119 offset:28800\n\t"
        "ds_read_b64_tr_b16 a[232:233], v120 offset:24576\n\t"
        "ds_read_b64_tr_b16 a[234:235], v120 offset:24704\n\t"
        "ds_read_b64_tr_b16 a[236:237], v120 offset:28672\n\t"
        "ds_read_b64_tr_b16 a[238:239], v120 offset:28800\n\t"
        "ds_read_b64_tr_b16 a[240:241], v119 offset:25088\n\t"
        "ds_read_b64_tr_b16 a[242:243], v119 offset:25216\n\t"
        "ds_read_b64_tr_b16 a[244:245], v119 offset:29184\n\t"
        "ds_read_b64_tr_b16 a[246:247], v119 offset:29312\n\t"
        "ds_read_b64_tr_b16 a[248:249], v120 offset:25088\n\t"
        "ds_read_b64_tr_b16 a[250:251], v120 offset:25216\n\t"
        "ds_read_b64_tr_b16 a[252:253], v120 offset:29184\n\t"
        "ds_read_b64_tr_b16 a[254:255], v120 offset:29312\n\t"
        "s_waitcnt lgkmcnt(0)"
        : "={a[0:31]}"(wa[0]), "={a[32:63]}"(wa[1]), "={a[64:95]}"(wa[2]), "={a[96:127]}"(wa[3]), "={a[128:159]}"(wa[4]), "={a[160:191]}"(wa[5]), "={a[192:223]}"(wa[6]), "={a[224:255]}"(wa[7]), "={v[128:159]}"(wg[0]), "={v[160:191]}"(wg[1]), "={v[192:223]}"(wg[2]), "={v[224:255]}"(wg[3]), "+{v[112:127]}"(pa)
        : [sv] "s"(sv), [so] "s"(so), [key] "s"(key), [tu] "s"(tu), [wsd] "s"(wsd), [m0b] "s"(m0b)
        : "memory", "scc", "m0", "s80", "s81", "s82");
        }
        asm volatile("" : "+v"(cpf), "+v"(m0v));
        __syncthreads();
        tid = otid(); l = tid & 63; n16 = l & 15; g = l >> 4;
        unsigned nm; if (!votes(nm)) return false;
        const unsigned nW = (nm & 1u) << 13, nA = nW + x * 32768, nS = nW + 6 * 16384 + x * 1024 - w * 1024, nC = 32 * 16384 + ((nm & 2u) << 12);
        const unsigned b0 = (64 + (h >> 2)) * 16384 + ((nm & 4u) << 11) + (h & 3) * 2048, b1 = b0 + 8 * 16384;
        const unsigned pob = w == 0 ? nA + (i32 >> 4) * 16384 + (i32 & 15) * 512 : w == 1 ? nC + (G * 4 + (cb >> 3)) * 16384 + (cb & 7) * 1024 - 1024 : (w == 2 && i32 == 0) ? nS : OOB;
        const unsigned aw = w < 3 ? (unsigned)(w * 32768 + (cb >> 4) * 16384 + ((cb >> 1) & 7) * 1024 + ((cb & 1) * 2 + (l >> 5)) * 256 + G * 128 + (l & 31) * 4) : OOB;
        v16u pc = {(unsigned)tid * 16, (n16 & 2) ? 0xEEEEu : 0x4444u, (unsigned)((vi & 1) * 1024 + w * 256 + n16 * 16 + g * 4), (unsigned)((G * 4 + w) * 16384 + l * 16), aw,
                   tid < 3 ? (unsigned)(6 * 16384 + l * 1024 + (32 + vi) * 8) : OOB, tid == 192 ? pr_a : OOB, (unsigned)(8192 + w * 512 + (g & 1) * 256 + (g >> 1) * 64 + n16 * 4),
                   (unsigned)((((l >> 2) & 1) + ((l >> 3) & 2)) * 128 + (l >> 5) * 64 + (l & 8) * 4 + (l & 3) * 8), (unsigned)(2048 + (64 * (vi & 1) + 16 * w + n16) * 16 + g * 4),
                   (unsigned)(w * 2048 + l * 8), (unsigned)(((w + 1) & 3) * 2048 + 512 + l * 8), (unsigned)(((w + 2) & 3) * 2048 + 1024 + l * 8), (unsigned)(((w + 3) & 3) * 2048 + 1536 + l * 8), 0u, g ? OOB : (unsigned)((vi & 1) * 1024 + w * 256 + n16 * 16)};
        m0v = min(m0v, 0xFFFFFFFEu);
        st4(rh, aw + nW, m0v);
        if (tid < 2) ((lu32*)(size_t)L_FL)[tid] = ~0u;
        __syncthreads();
    asm volatile(
        "rec_v_code:\n\t"
        "s_mov_b32 s36, 0xf0000000\n\t"
        "s_mov_b32 s38, 0xff00ff00\n\t"
        "s_mov_b32 s39, 0xff00ff00\n\t"
        "s_mov_b32 s40, 0\n\t"
        "s_mov_b32 s37, 0\n\t"
        "s_brev_b32 s54, 1\n\t"
        "s_mov_b32 s46, %c[iD1]\n\t"
        "s_mov_b32 s45, %c[iD1]\n\t"
        "s_mov_b32 s47, %c[iD2]\n\t"
        "s_mov_b32 s48, 1\n\t"
        "s_mov_b32 s50, %c[LC]\n\t"
        "s_mov_b32 s51, %c[LA]\n\t"
        "s_mov_b32 s52, %c[E1]\n\t"
        "s_mov_b32 s53, %c[E2]\n\t"
        "s_mov_b32 s43, 0\n\t"
        "s_mov_b32 s61, %[b0]\n\t"
        "s_mov_b32 s62, %[b1]\n\t"
        "s_mov_b32 s75, 31744\n\t"
        "s_and_b32 s80, s40, 3\n\t"
        "s_lshl_b32 s80, s80, 17\n\t"
        "s_add_u32 s57, s80, %[nA]\n\t"
        "s_add_u32 s58, s57, 0x1000\n\t"
        "s_add_u32 s59, s57, 0x4000\n\t"
        "s_add_u32 s60, s57, 0x5000\n\t"
        "v_mov_b32_e32 v92, -1\n\t"
        "v_mov_b32_e32 v93, -1\n\t"
        "v_mov_b32_e32 v94, -1\n\t"
        "v_mov_b32_e32 v95, -1\n\t"
        "v_mov_b32_e32 v48, 0\n\t"
        "v_mov_b32_e32 v49, 0\n\t"
        "v_mov_b32_e32 v50, 0\n\t"
        "v_mov_b32_e32 v51, 0\n\t"
        "v_mov_b32_e32 v52, 0\n\t"
        "v_mov_b32_e32 v53, 0\n\t"
        "v_mov_b32_e32 v54, 0\n\t"
        "v_mov_b32_e32 v55, 0\n\t"
        "v_mov_b32_e32 v56, 0\n\t"
        "v_mov_b32_e32 v57, 0\n\t"
        "v_mov_b32_e32 v58, 0\n\t"
        "v_mov_b32_e32 v59, 0\n\t"
        "v_mov_b32_e32 v60, 0\n\t"
        "v_mov_b32_e32 v61, 0\n\t"
        "v_mov_b32_e32 v62, 0\n\t"
        "v_mov_b32_e32 v63, 0\n\t"
        "s_memrealtime s[76:77]\n\t"
        "s_mov_b32 s67, %[nW]\n\t"
        "v_mov_b32_e32 v86, 64\n\t"
        "s_mov_b32 s61, %[b1]\n\t"
        "s_mov_b32 s62, %[b0]\n\t"
        "s_sub_u32 s75, s75, 16\n\t"
        "s_waitcnt lgkmcnt(0)\n\t"
        "s_mov_b32 s41, s76\n\t"
        "s_mov_b32 s69, s76\n\t"
        ".p2align 6\n\t"
        ".Lloop%=:\n\t"
        "s_mov_b32 s97, 0\n\t"
        "s_movk_i32 s96, 0x1000\n\t"
        ".Lwa%=:\n\t"
        "s_memrealtime s[98:99]\n\t"
        "s_addk_i32 s96, -1\n\t"
        "s_cmp_lt_i32 s96, 0\n\t"
        "s_cbranch_scc1 .Lta%=\n\t"
        "s_waitcnt lgkmcnt(0)\n\t"
        "s_sub_i32 s98, s98, s69\n\t"
        "s_cmp_lt_i32 s98, %c[iFT]\n\t"
        "s_cbranch_scc1 .Lwa%=\n\t"
        "s_cmp_ge_i32 s98, 0\n\t"
        "s_cbranch_scc1 .Lxa%=\n\t"
        ".Lqa%=:\n\t"
        "s_nop %c[iFN]\n\t"
        "s_addk_i32 s98, 1\n\t"
        "s_cmp_lt_i32 s98, 0\n\t"
        "s_cbranch_scc1 .Lqa%=\n\t"
        ".Lxa%=:\n\t"
        "buffer_load_dwordx4 v[0:3], v112, %[srd], s57 offen sc1\n\t"
        "s_nop 7\n\t"
        "buffer_load_dwordx4 v[4:7], v112, %[srd], s58 offen sc1\n\t"
        "s_nop 7\n\t"
        "buffer_load_dwordx4 v[8:11], v112, %[srd], s59 offen sc1\n\t"
        "s_nop 7\n\t"
        "buffer_load_dwordx4 v[12:15], v112, %[srd], s60 offen sc1\n\t"
        "v_mov_b32_e32 v80, 30720\n\t"
        "ds_read_b64 v[80:81], v80\n\t"
        "s_waitcnt lgkmcnt(0)\n\t"
        "v_readfirstlane_b32 s80, v80\n\t"
        "v_readfirstlane_b32 s81, v81\n\t"
        "s_cmp_eq_u32 s80, s40\n\t"
        "s_cselect_b32 s84, 1, 0\n\t"
        "s_cmp_eq_u32 s81, s40\n\t"
        "s_cselect_b32 s81, 4, 0\n\t"
        "s_or_b32 s84, s84, s81\n\t"
        "v_mov_b32_e32 v106, s76\n\t"
        "v_mov_b32_e32 v107, s84\n\t"
        "buffer_store_dwordx2 v[106:107], v117, %[srd], s67 offen sc1\n\t"
        "s_and_b32 s80, s40, 3\n\t"
        "s_lshl_b32 s80, s80, 17\n\t"
        "s_add_u32 s63, s80, %[nC]\n\t"
        "s_add_u32 s66, s80, %[nS]\n\t"
        "s_add_u32 s81, s80, 0x20000\n\t"
        "s_and_b32 s81, s81, 0x60000\n\t"
        "s_add_u32 s67, s81, %[nW]\n\t"
        "s_cmp_eq_u32 %[w], 2\n\t"
        "s_cselect_b32 s81, s80, s81\n\t"
        "s_xor_b32 s81, s81, 0x40000\n\t"
        "s_add_u32 s68, s81, %[pob]\n\t"
        "s_lshl_b32 s72, s40, 17\n\t"
        "s_add_u32 s72, s72, %[kvo]\n\t"
        "s_mov_b32 s80, s61\n\t"
        "s_mov_b32 s61, s62\n\t"
        "s_mov_b32 s62, s80\n\t"
        ".Lgo%=:\n\t"
        "s_waitcnt vmcnt(4)\n\t"
        "v_smfmac_f32_16x16x64_bf16 v[52:55], v[0:3], v[136:143], v113\n\t"
        "v_smfmac_f32_16x16x64_bf16 v[56:59], v[0:3], v[144:151], v113\n\t"
        "v_smfmac_f32_16x16x64_bf16 v[60:63], v[0:3], v[152:159], v113\n\t"
        "v_smfmac_f32_16x16x64_bf16 v[48:51], v[0:3], v[128:135], v113\n\t"
        "s_waitcnt vmcnt(3)\n\t"
        "v_smfmac_f32_16x16x64_bf16 v[52:55], v[4:7], v[168:175], v113\n\t"
        "v_smfmac_f32_16x16x64_bf16 v[56:59], v[4:7], v[176:183], v113\n\t"
        "v_smfmac_f32_16x16x64_bf16 v[60:63], v[4:7], v[184:191], v113\n\t"
        "v_smfmac_f32_16x16x64_bf16 v[48:51], v[4:7], v[160:167], v113\n\t"
        "s_waitcnt vmcnt(2)\n\t"
        "v_smfmac_f32_16x16x64_bf16 v[52:55], v[8:11], v[200:207], v113\n\t"
        "v_smfmac_f32_16x16x64_bf16 v[56:59], v[8:11], v[208:215], v113\n\t"
        "v_smfmac_f32_16x16x64_bf16 v[60:63], v[8:11], v[216:223], v113\n\t"
        "v_smfmac_f32_16x16x64_bf16 v[48:51], v[8:11], v[192:199], v113\n\t"
        "s_waitcnt vmcnt(1)\n\t"
        "v_smfmac_f32_16x16x64_bf16 v[52:55], v[12:15], v[232:239], v113\n\t"
        "v_smfmac_f32_16x16x64_bf16 v[56:59], v[12:15], v[240:247], v113\n\t"
        "v_smfmac_f32_16x16x64_bf16 v[60:63], v[12:15], v[248:255], v113\n\t"
        "v_smfmac_f32_16x16x64_bf16 v[48:51], v[12:15], v[224:231], v113\n\t"
        "s_nop 1\n\t"
        "v_mov_b32_e32 v104, v104\n\t"
        "s_nop 1\n\t"
        "v_add_f32_e32 v98, v52, v54\n\t"
        "v_add_f32_e32 v99, v53, v55\n\t"
        "ds_write_b64 v123, v[98:99]\n\t"
        "v_add_f32_e32 v100, v56, v58\n\t"
        "v_add_f32_e32 v101, v57, v59\n\t"
        "ds_write_b64 v124, v[100:101]\n\t"
        "v_add_f32_e32 v102, v60, v62\n\t"
        "v_add_f32_e32 v103, v61, v63\n\t"
        "ds_write_b64 v125, v[102:103]\n\t"
        "v_pk_add_f32 v[96:97], v[48:49], v[50:51]\n\t"
        "v_cmp_u_f32_e32 vcc, v96, v97\n\t"
        "s_nop 2\n\t"
        "v_mov_b32_e32 v104, v104\n\t"
        "s_cbranch_vccnz .Lfa%=\n\t"
        ".Lra%=:\n\t"
        "v_mov_b32_e32 v104, v104\n\t"
        "s_nop 2\n\t"
        "v_mov_b32_e32 v104, v104\n\t"
        "s_nop 2\n\t"
        "v_mov_b32_e32 v104, v104\n\t"
        "s_nop 2\n\t"
        "v_mov_b32_e32 v104, v104\n\t"
        "s_waitcnt lgkmcnt(0)\n\t"
        "s_barrier\n\t"
        "v_mov_b32_e32 v104, v104\n\t"
        "ds_read_b64 v[98:99], v122 offset:512\n\t"
        "ds_read_b64 v[100:101], v122 offset:1024\n\t"
        "ds_read_b64 v[102:103], v122 offset:1536\n\t"
        "v_mov_b32_e32 v104, v104\n\t"
        "s_nop 2\n\t"
        "v_mov_b32_e32 v104, v104\n\t"
        "s_nop 2\n\t"
        "v_mov_b32_e32 v104, v104\n\t"
        "s_waitcnt lgkmcnt(2)\n\t"
        "v_pk_add_f32 v[96:97], v[96:97], v[98:99]\n\t"
        "s_waitcnt lgkmcnt(0)\n\t"
        "v_pk_add_f32 v[100:101], v[100:101], v[102:103]\n\t"
        "v_pk_add_f32 v[96:97], v[96:97], v[100:101]\n\t"
        "v_cvt_pk_bf16_f32 v80, v96, v97\n\t"
        "v_min_u32_e32 v80, -2, v80\n\t"
        "buffer_store_dword v80, v114, %[srd], s61 offen sc1\n\t"
        "s_cmp_eq_u32 s40, 63\n\t"
        "s_cbranch_scc1 .Lend%=\n\t"
        "v_mov_b32_e32 v64, 0\n\t"
        "v_mov_b32_e32 v65, 0\n\t"
        "v_mov_b32_e32 v66, 0\n\t"
        "v_mov_b32_e32 v67, 0\n\t"
        "v_mov_b32_e32 v68, 0\n\t"
        "v_mov_b32_e32 v69, 0\n\t"
        "v_mov_b32_e32 v70, 0\n\t"
        "v_mov_b32_e32 v71, 0\n\t"
        "v_mov_b32_e32 v72, 0\n\t"
        "v_mov_b32_e32 v73, 0\n\t"
        "v_mov_b32_e32 v74, 0\n\t"
        "v_mov_b32_e32 v75, 0\n\t"
        "v_mov_b32_e32 v76, 0\n\t"
        "v_mov_b32_e32 v77, 0\n\t"
        "v_mov_b32_e32 v78, 0\n\t"
        "v_mov_b32_e32 v79, 0\n\t"
        "s_add_u32 s64, s63, 0x1000\n\t"
        "s_sleep %c[VSLP]\n\t"
        "buffer_load_dwordx4 v[88:91], v112, %[srd], s66 offen sc1\n\t"
        "s_cmp_eq_u32 %[w], 0\n\t"
        "s_cselect_b32 exec_hi, 0, -1\n\t"
        "buffer_store_dwordx4 v[92:95], v112, %[srd], s68 offen sc1\n\t"
        "s_mov_b32 exec_hi, -1\n\t"
        "s_waitcnt lgkmcnt(0)\n\t"
        "v_mov_b32_e32 v48, 0\n\t"
        "v_mov_b32_e32 v49, 0\n\t"
        "v_mov_b32_e32 v50, 0\n\t"
        "v_mov_b32_e32 v51, 0\n\t"
        "v_mov_b32_e32 v52, 0\n\t"
        "v_mov_b32_e32 v53, 0\n\t"
        "v_mov_b32_e32 v54, 0\n\t"
        "v_mov_b32_e32 v55, 0\n\t"
        "v_mov_b32_e32 v56, 0\n\t"
        "v_mov_b32_e32 v57, 0\n\t"
        "v_mov_b32_e32 v58, 0\n\t"
        "v_mov_b32_e32 v59, 0\n\t"
        "v_mov_b32_e32 v60, 0\n\t"
        "v_mov_b32_e32 v61, 0\n\t"
        "v_mov_b32_e32 v62, 0\n\t"
        "v_mov_b32_e32 v63, 0\n\t"
        "s_add_u32 s81, s40, 1\n\t"
        "s_and_b32 s80, s81, 3\n\t"
        "s_lshl_b32 s80, s80, 17\n\t"
        "s_add_u32 s57, s80, %[nA]\n\t"
        "s_add_u32 s58, s57, 0x1000\n\t"
        "s_add_u32 s59, s57, 0x4000\n\t"
        "s_add_u32 s60, s57, 0x5000\n\t"
        "s_waitcnt vmcnt(1)\n\t"
        "s_mov_b32 s97, 0\n\t"
        ".Lsr%=:\n\t"
        "s_mov_b32 exec_hi, 0xffff\n\t"
        "v_subrev_u32_e32 v4, s41, v88\n\t"
        "v_subrev_u32_e32 v5, s41, v90\n\t"
        "v_max_i32_e32 v4, v4, v5\n\t"
        "v_max_u32_e32 v5, v89, v91\n\t"
        "v_cmp_eq_u32_e32 vcc, -1, v5\n\t"
        "v_max_i32_dpp v4, v4, v4 quad_perm:[1,0,3,2] row_mask:0xf bank_mask:0xf\n\t"
        "v_or_b32_e32 v6, v89, v91\n\t"
        "s_nop 0\n\t"
        "v_max_i32_dpp v4, v4, v4 quad_perm:[2,3,0,1] row_mask:0xf bank_mask:0xf\n\t"
        "s_nop 1\n\t"
        "v_max_i32_dpp v4, v4, v4 row_ror:4 row_mask:0xf bank_mask:0xf\n\t"
        "s_nop 1\n\t"
        "v_max_i32_dpp v4, v4, v4 row_ror:8 row_mask:0xf bank_mask:0xf\n\t"
        "s_cbranch_vccnz .Lsf%=\n\t"
        "v_readlane_b32 s85, v4, 0\n\t"
        "v_readlane_b32 s81, v4, 16\n\t"
        "v_readlane_b32 s56, v4, 32\n\t"
        "v_and_b32_e32 v5, 1, v6\n\t"
        "v_cmp_ne_u32_e64 s[86:87], 0, v5\n\t"
        "v_and_b32_e32 v5, 2, v6\n\t"
        "v_cmp_ne_u32_e64 s[88:89], 0, v5\n\t"
        "v_and_b32_e32 v5, 4, v6\n\t"
        "v_cmp_ne_u32_e64 s[90:91], 0, v5\n\t"
        "s_max_i32 s56, s56, s81\n\t"
        "s_add_u32 s92, s85, s41\n\t"
        "s_sub_u32 s92, s92, s43\n\t"
        "s_add_i32 s93, s56, s47\n\t"
        "s_mov_b32 s44, s46\n\t"
        "s_mov_b32 s46, s45\n\t"
        "s_mov_b32 s94, s48\n\t"
        "s_mov_b32 s43, s41\n\t"
        "s_cmp_gt_i32 s56, s54\n\t"
        "s_cbranch_scc1 .Lmv%=\n\t"
        "s_mov_b32 s37, 0\n\t"
        "s_mov_b32 s48, 0\n\t"
        "s_mov_b32 s95, 0\n\t"
        ".Lsd%=:\n\t"
        "s_mov_b32 exec_hi, -1\n\t"
        "s_add_u32 s42, s43, s46\n\t"
        "s_mov_b32 s97, 0\n\t"
        "s_movk_i32 s96, 0x1000\n\t"
        ".Lwc%=:\n\t"
        "s_memrealtime s[98:99]\n\t"
        "s_addk_i32 s96, -1\n\t"
        "s_cmp_lt_i32 s96, 0\n\t"
        "s_cbranch_scc1 .Ltc%=\n\t"
        "s_waitcnt lgkmcnt(0)\n\t"
        "s_sub_i32 s98, s98, s42\n\t"
        "s_cmp_lt_i32 s98, %c[iFT]\n\t"
        "s_cbranch_scc1 .Lwc%=\n\t"
        "s_cmp_ge_i32 s98, 0\n\t"
        "s_cbranch_scc1 .Lxc%=\n\t"
        ".Lqc%=:\n\t"
        "s_nop %c[iFN]\n\t"
        "s_addk_i32 s98, 1\n\t"
        "s_cmp_lt_i32 s98, 0\n\t"
        "s_cbranch_scc1 .Lqc%=\n\t"
        ".Lxc%=:\n\t"
        "buffer_load_dwordx4 v[16:19], v115, %[srd], s63 offen offset:0 sc1\n\t"
        "s_cmp_ge_u32 s40, %c[iCT0]\n\t"
        "s_cselect_b32 s85, 1, 0\n\t"
        "s_cmp_lg_u64 s[86:87], 0\n\t"
        "s_cselect_b32 s86, s85, 0\n\t"
        "s_cmp_lg_u64 s[88:89], 0\n\t"
        "s_cselect_b32 s87, s85, 0\n\t"
        "s_cmp_lg_u64 s[90:91], 0\n\t"
        "s_cselect_b32 s88, s85, 0\n\t"
        "buffer_load_dwordx4 v[20:23], v115, %[srd], s63 offen offset:1024 sc1\n\t"
        "s_or_b32 s80, s94, s86\n\t"
        "s_or_b32 s80, s80, s87\n\t"
        "s_andn2_b32 s90, s85, s80\n\t"
        "s_cmp_le_i32 s92, s44\n\t"
        "s_cselect_b32 s80, s85, 0\n\t"
        "s_andn2_b32 s91, s80, s88\n\t"
        "s_and_b32 s80, s40, %c[LDEC]\n\t"
        "s_cselect_b32 s89, 0, 1\n\t"
        "buffer_load_dwordx4 v[24:27], v115, %[srd], s63 offen offset:2048 sc1\n\t"
        "s_mov_b32 s80, s88\n\t"
        "s_add_i32 s81, s92, s50\n\t"
        "s_sub_i32 s81, s81, 4\n\t"
        "s_cmp_le_i32 s81, s44\n\t"
        "s_cselect_b32 s80, s80, 0\n\t"
        "s_cmp_lt_i32 s50, %c[LC24]\n\t"
        "s_cselect_b32 s80, s80, 0\n\t"
        "s_lshl2_add_u32 s50, s80, s50\n\t"
        "buffer_load_dwordx4 v[28:31], v115, %[srd], s63 offen offset:3072 sc1\n\t"
        "s_sub_i32 s50, s50, s89\n\t"
        "s_max_i32 s50, s50, %c[LC]\n\t"
        "s_lshl_b32 s80, s92, 1\n\t"
        "s_sub_i32 s81, s80, s52\n\t"
        "s_ashr_i32 s82, s81, %c[UPSH]\n\t"
        "s_max_i32 s82, s82, %c[TRUP]\n\t"
        "s_add_i32 s82, s82, s52\n\t"
        "s_min_i32 s82, s82, s80\n\t"
        "buffer_load_dwordx4 v[32:35], v115, %[srd], s64 offen offset:0 sc1\n\t"
        "s_ashr_i32 s83, s81, 1\n\t"
        "s_min_i32 s83, s83, -%c[TRDN]\n\t"
        "s_add_i32 s83, s83, s52\n\t"
        "s_max_i32 s83, s83, s80\n\t"
        "s_cmp_gt_i32 s81, 0\n\t"
        "s_cselect_b32 s82, s82, s83\n\t"
        "s_cmp_lg_u32 s90, 0\n\t"
        "s_cselect_b32 s52, s82, s52\n\t"
        "buffer_load_dwordx4 v[36:39], v115, %[srd], s64 offen offset:1024 sc1\n\t"
        "s_add_i32 s80, s52, 1\n\t"
        "s_ashr_i32 s80, s80, 1\n\t"
        "s_add_i32 s45, s80, s50\n\t"
        "s_andn2_b32 s80, s86, s94\n\t"
        "s_cmp_lt_i32 s51, %c[LA24]\n\t"
        "s_cselect_b32 s80, s80, 0\n\t"
        "s_lshl2_add_u32 s51, s80, s51\n\t"
        "s_sub_i32 s51, s51, s89\n\t"
        "buffer_load_dwordx4 v[40:43], v115, %[srd], s64 offen offset:2048 sc1\n\t"
        "s_max_i32 s51, s51, %c[LA]\n\t"
        "s_lshl_b32 s80, s93, 1\n\t"
        "s_sub_i32 s81, s80, s53\n\t"
        "s_ashr_i32 s82, s81, %c[UPSH]\n\t"
        "s_max_i32 s82, s82, %c[TRUP]\n\t"
        "s_add_i32 s82, s82, s53\n\t"
        "s_min_i32 s82, s82, s80\n\t"
        "s_ashr_i32 s83, s81, 1\n\t"
        "buffer_load_dwordx4 v[44:47], v115, %[srd], s64 offen offset:3072 sc1\n\t"
        "s_min_i32 s83, s83, -%c[TRDN]\n\t"
        "s_add_i32 s83, s83, s53\n\t"
        "s_max_i32 s83, s83, s80\n\t"
        "s_cmp_gt_i32 s81, 0\n\t"
        "s_cselect_b32 s82, s82, s83\n\t"
        "s_cmp_lg_u32 s91, 0\n\t"
        "s_cselect_b32 s53, s82, s53\n\t"
        "s_add_i32 s80, s53, 1\n\t"
        "s_ashr_i32 s80, s80, 1\n\t"
        "s_add_i32 s47, s80, s51\n\t"
        "s_add_u32 s81, s47, %c[AUP]\n\t"
        "s_min_i32 s80, s95, s81\n\t"
        "s_max_i32 s47, s47, s80\n\t"
        "s_add_u32 s41, s42, s47\n\t"
        ".Lrc%=:\n\t"
        "s_waitcnt vmcnt(7)\n\t"
        "v_smfmac_f32_16x16x64_bf16 v[64:67], v[16:19], a[0:7], v113\n\t"
        "v_smfmac_f32_16x16x64_bf16 v[68:71], v[16:19], a[8:15], v113\n\t"
        "v_smfmac_f32_16x16x64_bf16 v[72:75], v[16:19], a[16:23], v113\n\t"
        "v_smfmac_f32_16x16x64_bf16 v[76:79], v[16:19], a[24:31], v113\n\t"
        "s_waitcnt vmcnt(6)\n\t"
        "v_smfmac_f32_16x16x64_bf16 v[64:67], v[20:23], a[32:39], v113\n\t"
        "v_smfmac_f32_16x16x64_bf16 v[68:71], v[20:23], a[40:47], v113\n\t"
        "v_smfmac_f32_16x16x64_bf16 v[72:75], v[20:23], a[48:55], v113\n\t"
        "v_smfmac_f32_16x16x64_bf16 v[76:79], v[20:23], a[56:63], v113\n\t"
        "s_waitcnt vmcnt(5)\n\t"
        "v_smfmac_f32_16x16x64_bf16 v[64:67], v[24:27], a[64:71], v113\n\t"
        "v_smfmac_f32_16x16x64_bf16 v[68:71], v[24:27], a[72:79], v113\n\t"
        "v_smfmac_f32_16x16x64_bf16 v[72:75], v[24:27], a[80:87], v113\n\t"
        "v_smfmac_f32_16x16x64_bf16 v[76:79], v[24:27], a[88:95], v113\n\t"
        "s_waitcnt vmcnt(4)\n\t"
        "v_smfmac_f32_16x16x64_bf16 v[64:67], v[28:31], a[96:103], v113\n\t"
        "v_smfmac_f32_16x16x64_bf16 v[68:71], v[28:31], a[104:111], v113\n\t"
        "v_smfmac_f32_16x16x64_bf16 v[72:75], v[28:31], a[112:119], v113\n\t"
        "v_smfmac_f32_16x16x64_bf16 v[76:79], v[28:31], a[120:127], v113\n\t"
        "s_waitcnt vmcnt(3)\n\t"
        "v_smfmac_f32_16x16x64_bf16 v[64:67], v[32:35], a[128:135], v113\n\t"
        "v_smfmac_f32_16x16x64_bf16 v[68:71], v[32:35], a[136:143], v113\n\t"
        "v_smfmac_f32_16x16x64_bf16 v[72:75], v[32:35], a[144:151], v113\n\t"
        "v_smfmac_f32_16x16x64_bf16 v[76:79], v[32:35], a[152:159], v113\n\t"
        "s_waitcnt vmcnt(2)\n\t"
        "v_smfmac_f32_16x16x64_bf16 v[64:67], v[36:39], a[160:167], v113\n\t"
        "v_smfmac_f32_16x16x64_bf16 v[68:71], v[36:39], a[168:175], v113\n\t"
        "v_smfmac_f32_16x16x64_bf16 v[72:75], v[36:39], a[176:183], v113\n\t"
        "v_smfmac_f32_16x16x64_bf16 v[76:79], v[36:39], a[184:191], v113\n\t"
        "s_waitcnt vmcnt(1)\n\t"
        "v_smfmac_f32_16x16x64_bf16 v[64:67], v[40:43], a[192:199], v113\n\t"
        "v_smfmac_f32_16x16x64_bf16 v[68:71], v[40:43], a[200:207], v113\n\t"
        "v_smfmac_f32_16x16x64_bf16 v[72:75], v[40:43], a[208:215], v113\n\t"
        "v_smfmac_f32_16x16x64_bf16 v[76:79], v[40:43], a[216:223], v113\n\t"
        "s_waitcnt vmcnt(0)\n\t"
        "v_smfmac_f32_16x16x64_bf16 v[64:67], v[44:47], a[224:231], v113\n\t"
        "s_nop 2\n\t"
        "v_smfmac_f32_16x16x64_bf16 v[68:71], v[44:47], a[232:239], v113\n\t"
        "s_cmp_gt_u32 %[w], 2\n\t"
        "s_nop 1\n\t"
        "v_smfmac_f32_16x16x64_bf16 v[72:75], v[44:47], a[240:247], v113\n\t"
        "v_add_f32_e32 v80, v64, v66\n\t"
        "s_nop 0\n\t"
        "v_smfmac_f32_16x16x64_bf16 v[76:79], v[44:47], a[248:255], v113\n\t"
        "v_add_f32_e32 v81, v65, v67\n\t"
        "v_add_f32_e32 v82, v68, v70\n\t"
        "v_add_f32_e32 v83, v69, v71\n\t"
        "s_mov_b32 exec_lo, 0\n\t"
        "s_nop 0\n\t"
        "v_add_f32_e32 v80, v72, v74\n\t"
        "v_add_f32_e32 v81, v73, v75\n\t"
        "s_nop 0\n\t"
        "v_pk_add_f32 v[82:83], v[76:77], v[78:79]\n\t"
        "s_mov_b32 exec_lo, -1\n\t"
        "s_nop 0\n\t"
        "v_permlane32_swap_b32_e32 v80, v82\n\t"
        "v_add_f32_e32 v80, v80, v82\n\t"
        "ds_write_b32 v119, v80\n\t"
        "v_permlane32_swap_b32_e32 v81, v83\n\t"
        "v_add_f32_e32 v81, v81, v83\n\t"
        "ds_write_b32 v119, v81 offset:128\n\t"
        "v_cmp_u_f32_e32 vcc, v80, v81\n\t"
        "s_nop 2\n\t"
        "v_mov_b32_e32 v104, v104\n\t"
        "s_cbranch_vccnz .Lfc%=\n\t"
        ".Lrd%=:\n\t"
        "v_mov_b32_e32 v104, v104\n\t"
        "s_cbranch_scc1 .Lno%=\n\t"
        "v_mov_b32_e32 v104, v104\n\t"
        "s_nop 2\n\t"
        "v_mov_b32_e32 v104, v104\n\t"
        "s_waitcnt lgkmcnt(0)\n\t"
        "s_barrier\n\t"
        "v_mov_b32_e32 v104, v104\n\t"
        "ds_read_b64 v[0:1], v120 offset:8192\n\t"
        "ds_read_b64 v[2:3], v120 offset:8704\n\t"
        "ds_read_b64 v[4:5], v120 offset:9216\n\t"
        "ds_read_b64 v[6:7], v120 offset:9728\n\t"
        "v_mov_b32_e32 v104, v104\n\t"
        "s_nop 2\n\t"
        "v_mov_b32_e32 v104, v104\n\t"
        "s_nop 2\n\t"
        "v_mov_b32_e32 v104, v104\n\t"
        "s_waitcnt lgkmcnt(2)\n\t"
        "v_pk_add_f32 v[0:1], v[0:1], v[2:3]\n\t"
        "s_waitcnt lgkmcnt(0)\n\t"
        "v_pk_add_f32 v[4:5], v[4:5], v[6:7]\n\t"
        "v_pk_add_f32 v[0:1], v[0:1], v[4:5]\n\t"
        "v_cvt_pk_bf16_f32 v0, v0, v1\n\t"
        "v_min_u32_e32 v0, -2, v0\n\t"
        "buffer_store_dword v0, v116, %[srd], s67 offen sc1\n\t"
        ".Lnp%=:\n\t"
        "s_memrealtime s[76:77]\n\t"
        "s_add_u32 s40, s40, 1\n\t"
        "s_waitcnt lgkmcnt(0)\n\t"
        "s_add_u32 s81, s76, %c[iVD]\n\t"
        "s_sub_u32 s80, s76, s41\n\t"
        "s_cmp_gt_i32 s80, %c[iTOL]\n\t"
        "s_cselect_b32 s69, s81, s41\n\t"
        "s_branch .Lloop%=\n\t"
        ".Lmv%=:\n\t"
        "s_mov_b32 s48, 1\n\t"
        "s_cmp_eq_u32 s54, %c[iTOL]\n\t"
        "s_cbranch_scc1 .Lm1%=\n\t"
        "s_max_i32 s80, s85, s56\n\t"
        "s_add_u32 s43, s41, s80\n\t"
        "s_add_u32 s43, s43, %c[iPEN0]\n\t"
        "s_mov_b32 s54, %c[iTOL]\n\t"
        "s_add_u32 s46, s46, %c[iX0C]\n\t"
        "s_add_u32 s95, s47, %c[iX0A]\n\t"
        "v_and_b32_e32 v4, 3, v88\n\t"
        "v_and_b32_e32 v5, 3, v90\n\t"
        "v_lshlrev_b32_e64 v4, v4, 1\n\t"
        "v_lshlrev_b32_e64 v5, v5, 1\n\t"
        "v_or_b32_e32 v4, v4, v5\n\t"
        "s_mov_b32 s80, 0\n\t"
        "v_and_b32_e32 v5, 1, v4\n\t"
        "v_cmp_ne_u32_e32 vcc, 0, v5\n\t"
        "s_nop 1\n\t"
        "s_cmp_lg_u64 vcc, 0\n\t"
        "s_cselect_b32 s81, 2, 0\n\t"
        "s_or_b32 s80, s80, s81\n\t"
        "v_and_b32_e32 v5, 2, v4\n\t"
        "v_cmp_ne_u32_e32 vcc, 0, v5\n\t"
        "s_nop 1\n\t"
        "s_cmp_lg_u64 vcc, 0\n\t"
        "s_cselect_b32 s81, 4, 0\n\t"
        "s_or_b32 s80, s80, s81\n\t"
        "v_and_b32_e32 v5, 4, v4\n\t"
        "v_cmp_ne_u32_e32 vcc, 0, v5\n\t"
        "s_nop 1\n\t"
        "s_cmp_lg_u64 vcc, 0\n\t"
        "s_cselect_b32 s81, 8, 0\n\t"
        "s_or_b32 s80, s80, s81\n\t"
        "v_and_b32_e32 v5, 8, v4\n\t"
        "v_cmp_ne_u32_e32 vcc, 0, v5\n\t"
        "s_nop 1\n\t"
        "s_cmp_lg_u64 vcc, 0\n\t"
        "s_cselect_b32 s81, 16, 0\n\t"
        "s_or_b32 s80, s80, s81\n\t"
        "s_lshr_b32 s80, 0x1adf1210, s80\n\t"
        "s_and_b32 s55, s80, 3\n\t"
        "s_branch .Lal%=\n\t"
        ".Lm1%=:\n\t"
        "s_add_u32 s80, s37, %c[iPEN2]\n\t"
        "s_min_i32 s80, s80, %c[iPENM]\n\t"
        "s_cmp_lg_u32 s37, 0\n\t"
        "s_cselect_b32 s37, s80, %c[iPEN]\n\t"
        "s_add_u32 s80, s56, s37\n\t"
        "s_max_i32 s80, s80, %c[iPMIN]\n\t"
        "s_add_u32 s43, s41, s80\n\t"
        "s_add_u32 s95, s93, %c[LA]\n\t"
        "s_add_u32 s46, s46, %c[iXR]\n\t"
        ".Lal%=:\n\t"
        "s_sub_u32 s80, s55, s43\n\t"
        "s_and_b32 s80, s80, %c[iPHM]\n\t"
        "s_add_u32 s43, s43, s80\n\t"
        "s_branch .Lsd%=\n\t"
        ".Lsf%=:\n\t"
        "s_mov_b32 exec_hi, -1\n\t"
        "s_sleep 2\n\t"
        "buffer_load_dwordx4 v[88:91], v112, %[srd], s66 offen sc1\n\t"
        "s_add_u32 s97, s97, 1\n\t"
        "s_cmp_lt_u32 s97, 0x100000\n\t"
        "s_cbranch_scc0 .Lgu%=\n\t"
        "s_waitcnt vmcnt(0)\n\t"
        "s_branch .Lsr%=\n\t"
        ".Lfa%=:\n\t"
        "v_max_u32_e32 v84, v0, v2\n\t"
        "v_max3_u32 v84, v84, v1, v3\n\t"
        "v_max3_u32 v84, v84, v4, v6\n\t"
        "v_max3_u32 v84, v84, v5, v7\n\t"
        "v_max3_u32 v84, v84, v8, v10\n\t"
        "v_max3_u32 v84, v84, v9, v11\n\t"
        "v_max3_u32 v84, v84, v12, v14\n\t"
        "v_max3_u32 v84, v84, v13, v15\n\t"
        "v_cmp_eq_u32_e32 vcc, -1, v84\n\t"
        "s_nop 1\n\t"
        "v_mov_b32_e32 v104, v104\n\t"
        "s_nop 0\n\t"
        "s_cbranch_vccz .Lra%=\n\t"
        "s_add_u32 s80, s40, 1\n\t"
        "v_mov_b32_e32 v80, 30720\n\t"
        "v_mov_b32_e32 v81, s80\n\t"
        "ds_write_b32 v80, v81\n\t"
        ".Lfp%=:\n\t"
        "s_sleep %c[ASLP]\n\t"
        "buffer_load_dwordx4 v[0:3], v112, %[srd], s57 offen sc1\n\t"
        "buffer_load_dwordx4 v[4:7], v112, %[srd], s58 offen sc1\n\t"
        "buffer_load_dwordx4 v[8:11], v112, %[srd], s59 offen sc1\n\t"
        "buffer_load_dwordx4 v[12:15], v112, %[srd], s60 offen sc1\n\t"
        "s_add_u32 s97, s97, 1\n\t"
        "s_cmp_lt_u32 s97, 0x100000\n\t"
        "s_cbranch_scc0 .Lgu%=\n\t"
        "s_waitcnt vmcnt(0)\n\t"
        "v_max_u32_e32 v84, v0, v2\n\t"
        "v_max3_u32 v84, v84, v1, v3\n\t"
        "v_max3_u32 v84, v84, v4, v6\n\t"
        "v_max3_u32 v84, v84, v5, v7\n\t"
        "v_max3_u32 v84, v84, v8, v10\n\t"
        "v_max3_u32 v84, v84, v9, v11\n\t"
        "v_max3_u32 v84, v84, v12, v14\n\t"
        "v_max3_u32 v84, v84, v13, v15\n\t"
        "v_cmp_eq_u32_e32 vcc, -1, v84\n\t"
        "s_nop 1\n\t"
        "v_mov_b32_e32 v104, v104\n\t"
        "s_nop 0\n\t"
        "s_cbranch_vccnz .Lfp%=\n\t"
        "v_mov_b32_e32 v48, 0\n\t"
        "v_mov_b32_e32 v49, 0\n\t"
        "v_mov_b32_e32 v50, 0\n\t"
        "v_mov_b32_e32 v51, 0\n\t"
        "v_mov_b32_e32 v52, 0\n\t"
        "v_mov_b32_e32 v53, 0\n\t"
        "v_mov_b32_e32 v54, 0\n\t"
        "v_mov_b32_e32 v55, 0\n\t"
        "v_mov_b32_e32 v56, 0\n\t"
        "v_mov_b32_e32 v57, 0\n\t"
        "v_mov_b32_e32 v58, 0\n\t"
        "v_mov_b32_e32 v59, 0\n\t"
        "v_mov_b32_e32 v60, 0\n\t"
        "v_mov_b32_e32 v61, 0\n\t"
        "v_mov_b32_e32 v62, 0\n\t"
        "v_mov_b32_e32 v63, 0\n\t"
        "s_nop 1\n\t"
        "s_branch .Lgo%=\n\t"
        ".Lfc%=:\n\t"
        "v_max_u32_e32 v84, v16, v18\n\t"
        "v_max3_u32 v84, v84, v20, v22\n\t"
        "v_max3_u32 v84, v84, v24, v26\n\t"
        "v_max3_u32 v84, v84, v28, v30\n\t"
        "v_max3_u32 v84, v84, v32, v34\n\t"
        "v_max3_u32 v84, v84, v36, v38\n\t"
        "v_max3_u32 v84, v84, v40, v42\n\t"
        "v_max3_u32 v84, v84, v44, v46\n\t"
        "v_cmp_eq_u32_e32 vcc, -1, v84\n\t"
        "s_nop 1\n\t"
        "v_mov_b32_e32 v104, v104\n\t"
        "s_nop 0\n\t"
        "s_cbranch_vccz .Lrd%=\n\t"
        "s_add_u32 s80, s40, 1\n\t"
        "v_mov_b32_e32 v80, 30724\n\t"
        "v_mov_b32_e32 v81, s80\n\t"
        "ds_write_b32 v80, v81\n\t"
        ".Lfq%=:\n\t"
        "s_sleep %c[ASLP]\n\t"
        "buffer_load_dwordx4 v[16:19], v115, %[srd], s63 offen offset:0 sc1\n\t"
        "buffer_load_dwordx4 v[20:23], v115, %[srd], s63 offen offset:1024 sc1\n\t"
        "buffer_load_dwordx4 v[24:27], v115, %[srd], s63 offen offset:2048 sc1\n\t"
        "buffer_load_dwordx4 v[28:31], v115, %[srd], s63 offen offset:3072 sc1\n\t"
        "buffer_load_dwordx4 v[32:35], v115, %[srd], s64 offen offset:0 sc1\n\t"
        "buffer_load_dwordx4 v[36:39], v115, %[srd], s64 offen offset:1024 sc1\n\t"
        "buffer_load_dwordx4 v[40:43], v115, %[srd], s64 offen offset:2048 sc1\n\t"
        "buffer_load_dwordx4 v[44:47], v115, %[srd], s64 offen offset:3072 sc1\n\t"
        "s_add_u32 s97, s97, 1\n\t"
        "s_cmp_lt_u32 s97, 0x100000\n\t"
        "s_cbranch_scc0 .Lgu%=\n\t"
        "s_waitcnt vmcnt(0)\n\t"
        "v_max_u32_e32 v84, v16, v18\n\t"
        "v_max3_u32 v84, v84, v20, v22\n\t"
        "v_max3_u32 v84, v84, v24, v26\n\t"
        "v_max3_u32 v84, v84, v28, v30\n\t"
        "v_max3_u32 v84, v84, v32, v34\n\t"
        "v_max3_u32 v84, v84, v36, v38\n\t"
        "v_max3_u32 v84, v84, v40, v42\n\t"
        "v_max3_u32 v84, v84, v44, v46\n\t"
        "v_cmp_eq_u32_e32 vcc, -1, v84\n\t"
        "s_nop 1\n\t"
        "v_mov_b32_e32 v104, v104\n\t"
        "s_nop 0\n\t"
        "s_cbranch_vccnz .Lfq%=\n\t"
        "v_mov_b32_e32 v64, 0\n\t"
        "v_mov_b32_e32 v65, 0\n\t"
        "v_mov_b32_e32 v66, 0\n\t"
        "v_mov_b32_e32 v67, 0\n\t"
        "v_mov_b32_e32 v68, 0\n\t"
        "v_mov_b32_e32 v69, 0\n\t"
        "v_mov_b32_e32 v70, 0\n\t"
        "v_mov_b32_e32 v71, 0\n\t"
        "v_mov_b32_e32 v72, 0\n\t"
        "v_mov_b32_e32 v73, 0\n\t"
        "v_mov_b32_e32 v74, 0\n\t"
        "v_mov_b32_e32 v75, 0\n\t"
        "v_mov_b32_e32 v76, 0\n\t"
        "v_mov_b32_e32 v77, 0\n\t"
        "v_mov_b32_e32 v78, 0\n\t"
        "v_mov_b32_e32 v79, 0\n\t"
        "s_nop 1\n\t"
        "s_branch .Lrc%=\n\t"
        ".Lno%=:\n\t"
        "s_waitcnt lgkmcnt(0)\n\t"
        "s_barrier\n\t"
        "s_branch .Lnp%=\n\t"
        ".Lta%=:\n\t"
        "s_waitcnt lgkmcnt(0)\n\t"
        "s_branch .Lxa%=\n\t"
        ".Ltc%=:\n\t"
        "s_waitcnt lgkmcnt(0)\n\t"
        "s_branch .Lxc%=\n\t"
        ".Lgu%=:\n\t"
        "s_mov_b64 exec, -1\n\t"
        "v_mov_b32_e32 v80, %c[ferr]\n\t"
        "v_mov_b32_e32 v81, 2\n\t"
        "buffer_store_dword v81, v80, %[wsd], 0 offen sc1\n\t"
        "s_endpgm\n\t"
        ".Lend%=:\n\t"
        "buffer_store_dword v80, v114, %[wsd], s72 offen sc1\n\t"
        "s_waitcnt vmcnt(0)\n\t"
        "s_barrier\n\t"
        "buffer_store_dword v86, v118, %[wsd], 0 offen sc1\n\t"
        "s_endpgm"
        :
        : [srd] "s"(rhd), [wsd] "s"(wsd), [w] "s"(w), [nA] "s"(nA), [nS] "s"(nS), [nW] "s"(nW), [nC] "s"(nC), [b0] "s"(b0), [b1] "s"(b1), [kvo] "s"(kvo), [pob] "s"(pob),
          [ferr] "i"(F_ERR), [iD1] "i"(D1), [iD2] "i"(D2), [LC] "i"(LC0), [LA] "i"(LA0), [LC24] "i"(LC0 + 24), [LA24] "i"(LA0 + 24), [E1] "i"(2 * (D1 - LC0)), [E2] "i"(2 * (D2 - LA0)),
          [iPEN] "i"(PEN), [iPEN2] "i"(PEN2), [iPENM] "i"(PEN + 2 * PEN2), [iPEN0] "i"(PEN0), [iTOL] "i"(TOL), [iCT0] "i"(CT0), [iX0C] "i"(X0C), [iX0A] "i"(X0A), [iPHM] "i"(PH_M),
          [iFT] "i"(-3), [iFN] "i"(FINE_N), [AUP] "i"(A_UP), [ASLP] "i"(A_SLP), [LDEC] "i"(L_DEC), [UPSH] "i"(UP_SH), [TRUP] "i"(TR_UP), [TRDN] "i"(TR_DN), [HSLP] "i"(H_SLP), [iPMIN] "i"(PMIN), [VSLP] "i"(V_SLP), [RSLP] "i"(R_SLP), [iVD] "i"(V_DEL), [iXR] "i"(XR),
          "{v[112:127]}"(pc), "{a[0:31]}"(wa[0]), "{a[32:63]}"(wa[1]), "{a[64:95]}"(wa[2]), "{a[96:127]}"(wa[3]), "{a[128:159]}"(wa[4]), "{a[160:191]}"(wa[5]), "{a[192:223]}"(wa[6]), "{a[224:255]}"(wa[7]), "{v[128:159]}"(wg[0]), "{v[160:191]}"(wg[1]), "{v[192:223]}"(wg[2]), "{v[224:255]}"(wg[3])
        : "memory", "vcc", "scc", "s36", "s37", "s38", "s39", "s40", "s41", "s42", "s43", "s44", "s45", "s46", "s47", "s48", "s49", "s50", "s51", "s52", "s53", "s54", "s55", "s56", "s57", "s58", "s59", "s60", "s61", "s62", "s63", "s64", "s65", "s66", "s67", "s68", "s69", "s70", "s71", "s72", "s73", "s74", "s75", "s76", "s77", "s78", "s79", "s80", "s81", "s82", "s83", "s84", "s85", "s86", "s87", "s88", "s89", "s90", "s91", "s92", "s93", "s94", "s95", "s96", "s97", "s98", "s99");
    }
    return false;
}

using f32x16 = __attribute__((ext_vector_type(16))) float;
constexpr int CV_RP = 80;
constexpr int CV_U = 96 * CV_RP;
constexpr int CV_KP = 4416;
constexpr int CV_K0 = 8 * CV_U;
__device__ __forceinline__ float lo16(unsigned x) { return __uint_as_float(x << 16); }
__device__ __forceinline__ float hi16(unsigned x) { return __uint_as_float(x & 0xFFFF0000u); }
__device__ __forceinline__ void prep_block(const Params& p, rsrc_t r, int k4, const int wv, const unsigned tag) {
    const int tid = otid(), w = wv, l = tid & 63;
    const int cu = (k4 & 3) * 32 + w * 8 + (l >> 3), oc0 = (k4 >> 2) * 8 + (l & 7);
    const rsrc_t rk = __builtin_amdgcn_make_buffer_rsrc((void*)p.Ak, 0, S * D * 2, 0x00020000), rv = __builtin_amdgcn_make_buffer_rsrc((void*)p.Av, 0, S * D * 2, 0x00020000);
    v4u sk[4], kn[11], vn[8], gw[2];
    auto fetch = [&](int oc) {
#pragma unroll
        for (int j = 0; j < 11; ++j) kn[j] = __builtin_amdgcn_raw_buffer_load_b128(rk, (unsigned)((oc * 8 + j - 3) * 2048 + cu * 16), 0, 2);
#pragma unroll
        for (int i = 0; i < 8; ++i) vn[i] = __builtin_amdgcn_raw_buffer_load_b128(rv, (unsigned)((oc * 8 + i) * 2048 + cu * 16), 0, 2); };
    fetch(oc0);
#pragma unroll
    for (int q = 0; q < 4; ++q) sk[q] = *(const v4u*)(p.shift_k + cu * 32 + q * 8);
#pragma unroll
    for (int q = 0; q < 2; ++q) gw[q] = *(const v4u*)(p.gate_w + q * 512 + l * 8);
    st4(r, tid < 128 ? F_RDY + k4 * 512 + tid * 4 : (tid == 128 && k4 == 0) ? F_CONV : 0xF0000000u, 0u);
#pragma unroll 1
    for (int it = 0; it < 4; ++it) {
        const int oc = oc0 + 64 * it, tk = (it * 32 + k4) * 16 + w;
        v4u kr[11], vr[8], xg[4][2];
#pragma unroll
        for (int j = 0; j < 11; ++j) kr[j] = kn[j];
#pragma unroll
        for (int i = 0; i < 8; ++i) vr[i] = vn[i];
        __builtin_amdgcn_sched_barrier(0);
#pragma unroll
        for (int i = 0; i < 4; ++i)
#pragma unroll
            for (int q = 0; q < 2; ++q) xg[i][q] = *(const v4u*)(p.Aq + (size_t)(tk + 4 * i) * D + q * 512 + l * 8);
        fetch(it < 3 ? oc + 64 : 4096);
        __builtin_amdgcn_sched_barrier(0);
#pragma unroll
        for (int c8 = 0; c8 < 8; ++c8) {
            const unsigned s01 = sk[c8 >> 1][(c8 & 1) * 2], s23 = sk[c8 >> 1][(c8 & 1) * 2 + 1];
            const float sv[4] = {hi16(s23), lo16(s23), hi16(s01), lo16(s01)};
            float kk[11], u[8];
#pragma unroll
            for (int j = 0; j < 11; ++j) kk[j] = (c8 & 1) ? hi16(kr[j][c8 >> 1]) : lo16(kr[j][c8 >> 1]);
            f32x2 acc[4];
#pragma unroll
            for (int j = 0; j < 4; ++j)
#pragma unroll
                for (int i = 0; i < 4; ++i) { const f32x2 t = rbf2((f32x2){kk[i + j], kk[i + j + 4]} * sv[j]); acc[i] = j == 0 ? t : rbf2(acc[i] + t); }
#pragma unroll
            for (int i = 0; i < 8; ++i) u[i] = acc[i & 3][i >> 2] * ((c8 & 1) ? hi16(vr[i][c8 >> 1]) : lo16(vr[i][c8 >> 1]));
            st16(r, W_U + (cu * 8 + c8) * 4096 + oc * 16, (v4u){pack2(u[7], u[6]), pack2(u[5], u[4]), pack2(u[3], u[2]), pack2(u[1], u[0])});
        }
        float ga[4];
#pragma unroll
        for (int i = 0; i < 4; ++i) { float acc = 0.f;
#pragma unroll
            for (int q = 0; q < 2; ++q)
#pragma unroll
                for (int e = 0; e < 4; ++e) { const unsigned xa = xg[i][q][e], gb = gw[q][e]; acc += rbfh(lo16(xa) * lo16(gb)); acc += rbfh(hi16(xa) * hi16(gb)); }
            ga[i] = acc; }
        for (int o = 32; o > 0; o >>= 1)
#pragma unroll
            for (int i = 0; i < 4; ++i) ga[i] += __shfl_xor(ga[i], o);
        const float z = rbfh(l == 0 ? ga[0] : l == 1 ? ga[1] : l == 2 ? ga[2] : ga[3]);
        st4(r, l < 4 ? W_GATE + (tk + 4 * l) * 4 : 0xF0000000u, __float_as_uint(rbfh(1.f / (1.f + expf(-z)))));
    }
    DRAIN(); __syncthreads();
    if (tid == 0) st4(r, F_PREP + k4 * 4, tag);
}
__device__ __forceinline__ bool conv_block(const Params& p, rsrc_t r, int k4, unsigned char* lds, const int wv, const int j, const unsigned tag) {
    (void)j;
    const int tid = otid(), w = wv, l = tid & 63;
    const rsrc_t rs = __builtin_amdgcn_make_buffer_rsrc((void*)p.ssm_k, 0, S * D * 2, 0x00020000);
    v4u kraw[2][4], uv[8]; unsigned dn;
    auto fetch_k = [&](int d0) {
#pragma unroll
        for (int ci = 0; ci < 2; ++ci)
#pragma unroll
            for (int i = 0; i < 4; ++i) kraw[ci][i] = __builtin_amdgcn_raw_buffer_load_b128(rs, (unsigned)((d0 + 2 * w + ci) * 4096 + (i * 64 + l) * 16), 0, 2);
        dn = *(const unsigned*)(p.ssm_D + (d0 & 1023) + 2 * w); };
    auto fetch_u = [&](unsigned o) {
#pragma unroll
        for (int i = 0; i < 8; ++i) uv[i] = ld16(r, o + i * 4096 + tid * 16); };
    fetch_k(k4 * 32);
    if (!wait_prep(r, l, tag)) return false;
    fetch_u(W_U + k4 * 32 * 4096);
#pragma unroll 1
    for (int it = 0; it < 4; ++it) {
    const int bb = k4 * 4 + it; const unsigned dd = dn;
    { const v4u z = {0u, 0u, 0u, 0u};
#pragma unroll
      for (int i = 0; i < 5; ++i) { const int q = tid + 256 * i; *(v4u*)(lds + (q / 160) * CV_U + (q % 160) * 16) = z; } }
#pragma unroll
    for (int i = 0; i < 8; ++i) *(v4u*)(lds + i * CV_U + (32 + (tid >> 2)) * CV_RP + (tid & 3) * 16) = uv[i];
    unsigned char* kc = lds + CV_K0 + w * 4 * CV_KP;
    auto copies = [&](int ci) {
        if (l < 32) *(unsigned*)(kc + l * 4) = 0u;
        if (l < 48) *(unsigned*)(kc + 128 + 4096 + l * 4) = 0u;
#pragma unroll
        for (int i = 0; i < 4; ++i) *(v4u*)(kc + 128 + (i * 64 + l) * 16) = kraw[ci][i];
        __builtin_amdgcn_wave_barrier();
#pragma unroll
        for (int it = 0; it < 9; ++it) { const int i = min(l + 64 * it, CV_KP / 8 - 2);
            const v2u e01 = *(const v2u*)(kc + i * 8), e23 = *(const v2u*)(kc + i * 8 + 8);
            const unsigned s0 = (e01.x >> 16) | (e01.y << 16), s1 = (e01.y >> 16) | (e23.x << 16), s2 = (e23.x >> 16) | (e23.y << 16);
            *(v2u*)(kc + CV_KP + i * 8) = (v2u){s0, s1};
            *(v2u*)(kc + 2 * CV_KP + i * 8) = (v2u){e01.y, e23.x};
            *(v2u*)(kc + 3 * CV_KP + i * 8) = (v2u){s1, s2}; }
        __builtin_amdgcn_wave_barrier(); };
    copies(0);
    __syncthreads();
    const int b = l & 31, kg = l >> 5, cc = (b + 1) & 3;
    float ys[32];
#pragma unroll
    for (int ci = 0; ci < 2; ++ci) {
        const int c8 = 2 * w + ci;
        if (ci == 1) { copies(1); __builtin_amdgcn_sched_barrier(0); fetch_k(it < 3 ? bb * 8 + 8 : 1024); fetch_u(it < 3 ? W_U + (bb * 8 + 8) * 4096 : 0xF0000000u); __builtin_amdgcn_sched_barrier(0); }
        f32x16 acc0, acc1, ad0, ad1;
#pragma unroll
        for (int i = 0; i < 16; ++i) { acc0[i] = 0.f; acc1[i] = 0.f; ad0[i] = 0.f; ad1[i] = 0.f; }
        typedef const __attribute__((address_space(3))) v2u* l2p;
        unsigned kb = (unsigned)(size_t)(kc + cc * CV_KP + (b - 8 * kg - 7 + 64 - cc) * 2) - 32, kb8 = kb + 8;
        asm volatile("" : "+v"(kb), "+v"(kb8));
        const unsigned char* ub = lds + c8 * CV_U + (32 + b) * CV_RP + kg * 16;
        v2u ka[4][2], kz[4][2]; bf16x8 fa[4], fz[4];
        auto ld_k = [&](int set, int un, int hi) { const int dl = un >> 1, kh = un & 1; const unsigned o = (unsigned)((32 * dl - 16 * kh + 16) * 2 + hi * 2048);
            if (hi) { kz[set][0] = *(l2p)(size_t)(kb + o); kz[set][1] = *(l2p)(size_t)(kb8 + o); } else { ka[set][0] = *(l2p)(size_t)(kb + o); ka[set][1] = *(l2p)(size_t)(kb8 + o); } };
        auto ld_f = [&](int set, int un) { const int dl = un >> 1, kh = un & 1;
            fa[set] = *(const bf16x8*)(ub + (32 - dl) * CV_RP + kh * 32); fz[set] = *(const bf16x8*)(ub - dl * CV_RP + kh * 32); };
#pragma unroll
        for (int q = 0; q < 2; ++q) { ld_k(q, q, 0); ld_f(q, q); ld_k(q, q, 1); }
        {
            const unsigned dv = ci ? dd >> 16 : dd & 0xFFFFu;
#pragma unroll
            for (int kh = 0; kh < 2; ++kh) { const int e = 7 - (b - 16 * kh - 8 * kg); const unsigned x = dv << ((e & 1) * 16); const int q = e >> 1;
                const v4u di = {q == 0 ? x : 0u, q == 1 ? x : 0u, q == 2 ? x : 0u, q == 3 ? x : 0u};
                asm volatile("s_nop 1\n\tv_mfma_f32_32x32x16_bf16 %0, %2, %4, %0\n\tv_mfma_f32_32x32x16_bf16 %1, %3, %4, %1" : "+v"(ad1), "+v"(ad0) : "v"(fa[kh]), "v"(fz[kh]), "v"(di)); }
        }
#pragma unroll 1
        for (int u0 = 0; u0 < 64; u0 += 4) {
#pragma unroll
            for (int q = 0; q < 4; ++q) {
                const int un = u0 + q + 2, ns = (q + 2) & 3;
                const v4u kav = {ka[q][0].x, ka[q][0].y, ka[q][1].x, ka[q][1].y}, kzv = {kz[q][0].x, kz[q][0].y, kz[q][1].x, kz[q][1].y};
                __builtin_amdgcn_sched_barrier(0);
                asm volatile("s_nop 1\n\tv_mfma_f32_32x32x16_bf16 %0, %1, %2, %0" : "+v"(acc1) : "v"(fa[q]), "v"(kav));
                ld_k(ns, un, 0);
                __builtin_amdgcn_sched_barrier(0);
                asm volatile("v_mfma_f32_32x32x16_bf16 %0, %1, %2, %0" : "+v"(acc0) : "v"(fz[q]), "v"(kav));
                ld_f(ns, un);
                __builtin_amdgcn_sched_barrier(0);
                asm volatile("v_mfma_f32_32x32x16_bf16 %0, %1, %2, %0" : "+v"(acc1) : "v"(fz[q]), "v"(kzv));
                ld_k(ns, un, 1);
            }
        }
        asm volatile("s_nop 7\n\ts_nop 7" : "+v"(acc0), "+v"(acc1), "+v"(ad0), "+v"(ad1));
#pragma unroll
        for (int i = 0; i < 32; ++i) { const float y = rbfh(i < 16 ? acc0[i] : acc1[i - 16]) + rbfh(i < 16 ? ad0[i] : ad1[i - 16]);
            ys[i] = ci == 0 ? y : __uint_as_float(pack2(ys[i], y)); }
    }
    __syncthreads();
#pragma unroll
    for (int i = 0; i < 32; ++i) { const int row = (i >> 4) * 32 + (i & 3) + 8 * ((i & 15) >> 2) + 4 * kg; *(float*)(lds + (row * 32 + b) * 16 + w * 4) = ys[i]; }
    __syncthreads();
#pragma unroll
    for (int i = 0; i < 8; ++i) { const int t = tid + 256 * i; st16(r, W_A + bb * 32768 + t * 16, *(const v4u*)(lds + t * 16)); }
    __syncthreads();
    }
    DRAIN(); __syncthreads();
    if (tid == 0) __hip_atomic_fetch_add((unsigned*)(p.ws + F_CONV), 1u, __ATOMIC_RELAXED, __HIP_MEMORY_SCOPE_AGENT);
    return true;
}

__device__ __forceinline__ void dma16(unsigned ldsdst, unsigned voff, const v4u& srd, unsigned soff) {
    asm volatile("s_mov_b32 m0, %0\n\ts_nop 0\n\tbuffer_load_dwordx4 %1, %2, %3 offen lds" :: "s"(ldsdst), "v"(voff), "s"(srd), "s"(soff) : "m0", "memory"); }
__device__ __forceinline__ void dma16n(unsigned ldsdst, unsigned voff, const v4u& srd, unsigned soff) {
    asm volatile("s_mov_b32 m0, %0\n\ts_nop 0\n\tbuffer_load_dwordx4 %1, %2, %3 offen nt lds" :: "s"(ldsdst), "v"(voff), "s"(srd), "s"(soff) : "m0", "memory"); }
__device__ __forceinline__ v4u pack_acc(const f32x4& a, const f32x4& b) {
    float x0, x1, x2, x3, y0, y1, y2, y3;
    asm volatile("v_accvgpr_read_b32 %0, %8\n\tv_accvgpr_read_b32 %1, %9\n\tv_accvgpr_read_b32 %2, %10\n\tv_accvgpr_read_b32 %3, %11\n\t"
                 "v_accvgpr_read_b32 %4, %12\n\tv_accvgpr_read_b32 %5, %13\n\tv_accvgpr_read_b32 %6, %14\n\tv_accvgpr_read_b32 %7, %15"
                 : "=v"(x0), "=v"(x1), "=v"(x2), "=v"(x3), "=v"(y0), "=v"(y1), "=v"(y2), "=v"(y3)
                 : "a"(a[0]), "a"(a[1]), "a"(a[2]), "a"(a[3]), "a"(b[0]), "a"(b[1]), "a"(b[2]), "a"(b[3]));
    return (v4u){pack2(x0, x1), pack2(x2, x3), pack2(y0, y1), pack2(y2, y3)}; }
#define SB() __builtin_amdgcn_sched_barrier(0)
#define WO_REGS "a128", "a129", "a130", "a131", "a132", "a133", "a134", "a135", "a136", "a137", "a138", "a139", "a140", "a141", "a142", "a143", "a144", "a145", "a146", "a147", "a148", "a149", "a150", "a151", "a152", "a153", "a154", "a155", "a156", "a157", "a158", "a159", "a160", "a161", "a162", "a163", "a164", "a165", "a166", "a167", "a168", "a169", "a170", "a171", "a172", "a173", "a174", "a175", "a176", "a177", "a178", "a179", "a180", "a181", "a182", "a183", "a184", "a185", "a186", "a187", "a188", "a189", "a190", "a191", "a192", "a193", "a194", "a195", "a196", "a197", "a198", "a199", "a200", "a201", "a202", "a203", "a204", "a205", "a206", "a207", "a208", "a209", "a210", "a211", "a212", "a213", "a214", "a215", "a216", "a217", "a218", "a219", "a220", "a221", "a222", "a223", "a224", "a225", "a226", "a227", "a228", "a229", "a230", "a231", "a232", "a233", "a234", "a235", "a236", "a237", "a238", "a239", "a240", "a241", "a242", "a243", "a244", "a245", "a246", "a247", "a248", "a249", "a250", "a251", "a252", "a253", "a254", "a255", "v192", "v193", "v194", "v195", "v196", "v197", "v198", "v199", "v200", "v201", "v202", "v203", "v204", "v205", "v206", "v207", "v208", "v209", "v210", "v211", "v212", "v213", "v214", "v215", "v216", "v217", "v218", "v219", "v220", "v221", "v222", "v223", "v224", "v225", "v226", "v227", "v228", "v229", "v230", "v231", "v232", "v233", "v234", "v235", "v236", "v237", "v238", "v239", "v240", "v241", "v242", "v243", "v244", "v245", "v246", "v247", "v248", "v249", "v250", "v251", "v252", "v253", "v254", "v255"
struct NoHook { __device__ void operator()(int) const {} };
template <int MODE, int MI, int NJ, int NS, int HX = 0, class HOOK = NoHook, int MN = MI>
__device__ __forceinline__ void gemm(rsrc_t r, const bf16_t* __restrict__ A, const bf16_t* __restrict__ B, int m0, int n0, unsigned char* lds, unsigned coff, const int wv,
                                     const int rot, const void* tk = nullptr,
                                     const bool have = false, const bf16_t* Bn = nullptr, int m0n = 0, int n0n = 0, HOOK&& hook = NoHook()) {
    constexpr int AB = MN * 4096, BB = NJ * 2048, B0 = (NS / 2) * AB, NB = NJ / 2, NM = MI * NJ, NR = MI + NJ, BP = NJ * 64;
    static_assert(NS % 2 == 0 && 32 % NS == 0 && B0 <= 65536 && NS * BB <= 65536 && (NJ == 4 || NJ == 8), "ring");
    const int tid = otid();
    const int w = wv, l = tid & 63, n16 = l & 15, g = l >> 4;
    const int wm = w >> 1, wn = w & 1;
    const unsigned lb = __builtin_amdgcn_readfirstlane((unsigned)(size_t)lds);
    const v4u sa = mk_srd(A, 2048 * 1024 * 2); v4u sb = mk_srd(B, 1024 * 4096 * 2);
    f32x4 acc[MI][NJ];
#pragma unroll
    for (int i = 0; i < MI; ++i)
#pragma unroll
        for (int j = 0; j < NJ; ++j) acc[i][j] = (f32x4){0.f, 0.f, 0.f, 0.f};
    unsigned va = (unsigned)((m0 + 8 * MI * w + (l >> 3)) * 2048 + (((l & 7) ^ (l >> 3)) << 4));
    unsigned vb[2];
    if (NJ == 8) { vb[0] = (unsigned)((8 * w + (l >> 5)) * 8192 + n0 * 2) + (unsigned)(((l & 31) << 4) ^ ((l >> 5) << 5) ^ (wn << 7)); vb[1] = vb[0] ^ 64u; }
    else { vb[0] = (unsigned)((8 * w + g) * 8192 + n0 * 2) + (unsigned)((n16 << 4) ^ (g << 5) ^ (wn << 7)); vb[1] = vb[0]; }
    auto rk = [&](int kt) { return (kt & ~31) | ((kt + rot) & 31); };
    unsigned la = lb + w * MI * 1024;
    auto st_a = [&](int buf, int kt, int q) { dma16(la + buf * AB + q * 1024, va, sa, (unsigned)(rk(kt) * 64 + q * 16384)); };
    auto st_b = [&](int buf, int kt, int x) { if ((B_NT >> (NJ == 4 ? 0 : MI == 4 ? 1 : 2)) & 1) dma16n(lb + B0 + buf * BB + (w * NB + x) * 1024, vb[x & 1], sb, (unsigned)((rk(kt) * 32 + x * (8 / NB)) * 8192));
        else dma16(lb + B0 + buf * BB + (w * NB + x) * 1024, vb[x & 1], sb, (unsigned)((rk(kt) * 32 + x * (8 / NB)) * 8192)); };
    const unsigned f = (unsigned)((n16 >> 2) | ((g & 1) << 2));
    constexpr int NQ = MODE == 1 ? NJ : NJ / 2;
    unsigned ra[2], rb[NQ];
    ra[0] = lb + (16 * MI * wm + n16) * 128 + ((g ^ (n16 & 7)) << 4); ra[1] = ra[0] ^ 64u;
    {   const unsigned row = lb + B0 + (8 * g + (n16 >> 2)) * BP + (NJ == 8 ? 256 * wn : 0), g0 = NJ == 8 ? 0u : 4u * wn;
#pragma unroll
        for (int j = 0; j < NQ; ++j) {
            if (MODE == 1) rb[j] = row + (((g0 + j) ^ f) << 5) + 8 * (n16 & 3);
            else rb[j] = row + (((g0 + 2 * j + ((n16 & 3) >> 1)) ^ f) << 5) + (n16 & 1) * 16;
            asm volatile("" : "+v"(rb[j])); } }
    bf16x8 af[2][MI], bfr[2][NJ];
    auto rd = [&](int set, int buf, int x) {
        if (x < NJ) { const unsigned a = rb[MODE == 1 ? x : (x >> 1)] + (MODE == 1 ? 0 : (x & 1) * 8) + buf * BB;
            const v2u lo = trb(a), hi = trb(a + 4 * BP); v4u v = {lo.x, lo.y, hi.x, hi.y}; bfr[set][x] = as_frag(v); }
        else af[set][x - NJ] = ldsa(ra[buf & 1] + (buf >> 1) * AB + (x - NJ) * 2048); };
    auto mf = [&](int set, int i, int j) {
        if (HX) asm volatile("v_mfma_f32_16x16x32_bf16 %0, %1, %2, %0" : "+a"(acc[i][j]) : "v"(MODE == 1 ? af[set][i] : bfr[set][j]), "v"(MODE == 1 ? bfr[set][j] : af[set][i]) : WO_REGS);
        else if (MODE == 1) asm volatile("v_mfma_f32_16x16x32_bf16 %0, %1, %2, %0" : "+a"(acc[i][j]) : "v"(af[set][i]), "v"(bfr[set][j]));
        else asm volatile("v_mfma_f32_16x16x32_bf16 %0, %1, %2, %0" : "+a"(acc[i][j]) : "v"(bfr[set][j]), "v"(af[set][i])); };
    if (!have) {
#pragma unroll
        for (int t = 0; t < NS; ++t) {
            if (t & 1) {
#pragma unroll
                for (int q = 0; q < MI; ++q) st_a(t >> 1, t - 1, q); }
#pragma unroll
            for (int q = 0; q < NB; ++q) st_b(t, t, q);
            if (HX) hook(t); }
    }
#pragma unroll
    for (int i = 0; i < MI; ++i)
#pragma unroll
        for (int j = 0; j < NJ; ++j) asm volatile("" : "+a"(acc[i][j]));
    asm volatile("s_nop 1" ::: "memory");
    if (!have) {
        asm volatile("s_waitcnt vmcnt(%c0)\n\ts_barrier" :: "i"((NS - 1) * (NB + HX) + (NS / 2 - 1) * MI) : "memory");
    }
#pragma unroll
    for (int x = 0; x < NR; ++x) rd(0, 0, x);
    constexpr int HEAD = NM / 8;
    const v4u st = mk_srd(tk, tk ? 32768 : 0); unsigned tdum = 0u;
#pragma unroll 1
    for (int k0 = 0; k0 < 32; k0 += NS) {
        int kb = k0 + NS;
        if (kb == 32) {
            kb = Bn ? 0 : 0x10100;
            if (Bn) { va += (unsigned)((m0n - m0 + 8 * (MN - MI) * w) * 2048); la = lb + w * MN * 1024; vb[0] += (unsigned)((n0n - n0) * 2); vb[1] += (unsigned)((n0n - n0) * 2); sb = mk_srd(Bn, 1024 * 4096 * 2); } }
        if (NS == 4) asm volatile("buffer_load_dword %0, %1, %2, %3 offen" : "+v"(tdum) : "v"(l < 32 ? (unsigned)l * 128u : 0xF0000000u), "s"(st), "s"((unsigned)k0 * 1024u) : "memory");
#pragma unroll
        for (int u = 0; u < NS; ++u) {
            SB();
#pragma unroll
            for (int q = 0; q < HEAD; ++q) mf(u & 1, q / NJ, q % NJ);
            SB();
            asm volatile("s_waitcnt vmcnt(%c0) lgkmcnt(0)\n\ts_barrier" :: "i"((NS - 2) * (NB + HX) + (NS / 2 - 1 - (u & 1)) * MI) : "memory");
            SB();
            constexpr int span = NM - HEAD;
#pragma unroll
            for (int q = HEAD; q < NM; ++q) {
                mf(u & 1, q / NJ, q % NJ);
                const int x = q - HEAD, nd = NB + ((u & 1) ? MI : 0);
#pragma unroll
                for (int y = 0; y < NR; ++y) if ((y * (span - HEAD)) / NR == x) rd((u & 1) ^ 1, (u + 1) % NS, y);
#pragma unroll
                for (int y = 0; y < nd; ++y) if ((y * span + span / 2) / nd == x) { if ((u & 1) && y < MI) st_a(u >> 1, kb + u - 1, y); else st_b(u, kb + u, y - ((u & 1) ? MI : 0)); }
                SB(); }
            if (MN > MI && (u & 1) && k0 == 32 - NS) {
#pragma unroll
                for (int y = MI; y < MN; ++y) st_a(u >> 1, kb + u - 1, y); }
            if (HX) hook(NS + k0 + u);
        }
    }
    asm volatile("s_nop 7\n\ts_nop 3" : "+v"(tdum) :: "memory");
    SB();
    const int col = n0 + 16 * NJ * wn, head = col >> 7, sub = (col & 127) >> 4, seg0 = (m0 >> 5) + (MI / 2) * wm;
#pragma unroll
    for (int sg = 0; sg < MI / 2; ++sg) {
        const unsigned base = coff + (unsigned)(((seg0 + sg) * 32 + head) * 8192 + l * 16);
#pragma unroll
        for (int q = 0; q < NJ; ++q) {
            if (HX) asm volatile("" ::: WO_REGS);
            if (MODE == 0) { const int tt = q / (NJ / 2), kk = q % (NJ / 2); st16(r, base + (tt * 4 + (sub >> 1) + kk) * 1024, pack_acc(acc[2 * sg + tt][2 * kk], acc[2 * sg + tt][2 * kk + 1])); }
            else st16(r, base + (sub + q) * 1024, pack_acc(acc[2 * sg][q], acc[2 * sg + 1][q])); }
    }
}

__device__ __forceinline__ unsigned min4(unsigned v) { return min(min(__builtin_amdgcn_readlane(v, 0), __builtin_amdgcn_readlane(v, 1)), min(__builtin_amdgcn_readlane(v, 2), __builtin_amdgcn_readlane(v, 3))); }
__device__ __forceinline__ void attn_block(const Params& p, rsrc_t r, unsigned char* lds, const int wv, const int h, const int j, const unsigned tag) {
    (void)j;
    const int l = otid() & 63, n16 = l & 15, g = l >> 4;
    { v4u q[4]; if (!wait_cal(r, l, tag, q)) return; }
    v4u qv[2][4], kv[2][4], va[8];
    int nev = 0; (void)nev;
    auto fetch = [&](int seg) -> bool {
        for (unsigned spin = 0;; ++spin) { MEMBAR();
            const unsigned fv = ld4(r, F_KVQ + ((seg >> 2) * 32 + h) * 64 + min(l, 2) * 4);
            if (__all(fv == tag)) break;
            if (spin > (1u << 20)) { st4(r, F_ERR, 64u); return false; }
            __builtin_amdgcn_s_sleep(16); }
        MEMBAR();
        const unsigned o = (unsigned)((seg * 32 + h) * 8192 + l * 16);
#pragma unroll
        for (int i = 0; i < 8; ++i) qv[i >> 2][i & 3] = ld16(r, W_QX + o + i * 1024);
#pragma unroll
        for (int i = 0; i < 8; ++i) kv[i >> 2][i & 3] = ld16(r, W_KX + o + i * 1024);
#pragma unroll
        for (int i = 0; i < 8; ++i) va[i] = ld16(r, W_VF + o + i * 1024);
        return true; };
    if (!fetch(wv)) return;
    unsigned prog = 0u, tseen = rtnow() - 100000u;
#pragma unroll 1
    for (int seg = wv; seg < NSEG; seg += 4) {
        for (unsigned spin = 0; prog <= (unsigned)seg; ++spin) {
            const unsigned now = rtnow();
            if (spin > (1u << 22)) { st4(r, F_ERR, 32u); return; }
            if (prog + (seg >= EARLY_FROM ? 2u : 1u) + (now - tseen) / STEP_MIN <= (unsigned)seg) { __builtin_amdgcn_s_sleep(16); continue; }
            MEMBAR();
            const unsigned pg = min4(ld4(r, F_PROG + h * 128 + min(l, 2) * 8));
            if (pg != prog) { prog = pg; tseen = now; }
            if (prog <= (unsigned)seg) __builtin_amdgcn_s_sleep(P_SLP);
        }
        MEMBAR();
        const unsigned km = HO_KVM + (seg * 32 + h) * 4096;
        v4u kmv[4]; v2u pm[8];
        const v4u k0 = ld16(r, km + l * 16), k1 = ld16(r, km + 1024 + l * 16);
#pragma unroll
        for (int t = 0; t < 8; ++t) pm[t] = ld8(r, km + 2048 + t * 256 + n16 * 16 + (g & 1) * 8);
        *(v4u*)(lds + wv * 2048 + l * 16) = k0; *(v4u*)(lds + wv * 2048 + 1024 + l * 16) = k1;
        __builtin_amdgcn_wave_barrier();
#pragma unroll
        for (int ks = 0; ks < 4; ++ks) { const unsigned a = (unsigned)(size_t)lds + wv * 2048 + (32 * ks + 8 * g + (n16 >> 2)) * 16 + (n16 & 1) * 8; const v2u x0 = trb(a), x1 = trb(a + 64); kmv[ks] = (v4u){x0.x, x0.y, x1.x, x1.y}; }
#pragma unroll
        for (int qt = 0; qt < 2; ++qt) {
            f32x4 sc[3];
            asm volatile("s_nop 1\n\t"
                "v_mfma_f32_16x16x32_bf16 %0, %3, %15, 0\n\tv_mfma_f32_16x16x32_bf16 %1, %7, %15, 0\n\tv_mfma_f32_16x16x32_bf16 %2, %11, %15, 0\n\t"
                "v_mfma_f32_16x16x32_bf16 %0, %4, %16, %0\n\tv_mfma_f32_16x16x32_bf16 %1, %8, %16, %1\n\tv_mfma_f32_16x16x32_bf16 %2, %12, %16, %2\n\t"
                "v_mfma_f32_16x16x32_bf16 %0, %5, %17, %0\n\tv_mfma_f32_16x16x32_bf16 %1, %9, %17, %1\n\tv_mfma_f32_16x16x32_bf16 %2, %13, %17, %2\n\t"
                "v_mfma_f32_16x16x32_bf16 %0, %6, %18, %0\n\tv_mfma_f32_16x16x32_bf16 %1, %10, %18, %1\n\tv_mfma_f32_16x16x32_bf16 %2, %14, %18, %2\n\t"
                "s_nop 7"
                : "=&v"(sc[0]), "=&v"(sc[1]), "=&v"(sc[2])
                : "v"(kv[0][0]), "v"(kv[0][1]), "v"(kv[0][2]), "v"(kv[0][3]), "v"(kv[1][0]), "v"(kv[1][1]), "v"(kv[1][2]), "v"(kv[1][3]),
                  "v"(kmv[0]), "v"(kmv[1]), "v"(kmv[2]), "v"(kmv[3]), "v"(qv[qt][0]), "v"(qv[qt][1]), "v"(qv[qt][2]), "v"(qv[qt][3]));
            const int qi = 16 * qt + n16; float mx = -INFINITY;
#pragma unroll
            for (int t = 0; t < 3; ++t)
#pragma unroll
                for (int rr = 0; rr < 4; ++rr) { float v = rbfh(rbfh(sc[t][rr]) * 0.08838834764831845f);
                    if (t < 2) v = (16 * t + 4 * g + rr <= qi) ? v : -1000000000.0f; else if (g >= 2) v = -INFINITY;
                    sc[t][rr] = v; mx = fmaxf(mx, v); }
            mx = xor16_max(mx); mx = xor32_max(mx); float sum = 0.f;
#pragma unroll
            for (int t = 0; t < 3; ++t)
#pragma unroll
                for (int rr = 0; rr < 4; ++rr) { const float e = __expf(sc[t][rr] - mx); sc[t][rr] = e; sum += e; }
            sum = xor16_add(sum); sum = xor32_add(sum); const float inv = 1.0f / sum;
            const v4u p0 = {pack2(sc[0][0] * inv, sc[0][1] * inv), pack2(sc[0][2] * inv, sc[0][3] * inv), pack2(sc[1][0] * inv, sc[1][1] * inv), pack2(sc[1][2] * inv, sc[1][3] * inv)};
            const v2u p1 = {pack2(sc[2][0] * inv, sc[2][1] * inv), pack2(sc[2][2] * inv, sc[2][3] * inv)};
            f32x4 oa[8];
            asm volatile("s_nop 1\n\t"
                "v_mfma_f32_16x16x32_bf16 %0, %8, %24, 0\n\tv_mfma_f32_16x16x32_bf16 %1, %9, %24, 0\n\tv_mfma_f32_16x16x32_bf16 %2, %10, %24, 0\n\tv_mfma_f32_16x16x32_bf16 %3, %11, %24, 0\n\t"
                "v_mfma_f32_16x16x32_bf16 %4, %12, %24, 0\n\tv_mfma_f32_16x16x32_bf16 %5, %13, %24, 0\n\tv_mfma_f32_16x16x32_bf16 %6, %14, %24, 0\n\tv_mfma_f32_16x16x32_bf16 %7, %15, %24, 0\n\t"
                "v_mfma_f32_16x16x16_bf16 %0, %16, %25, %0\n\tv_mfma_f32_16x16x16_bf16 %1, %17, %25, %1\n\tv_mfma_f32_16x16x16_bf16 %2, %18, %25, %2\n\tv_mfma_f32_16x16x16_bf16 %3, %19, %25, %3\n\t"
                "v_mfma_f32_16x16x16_bf16 %4, %20, %25, %4\n\tv_mfma_f32_16x16x16_bf16 %5, %21, %25, %5\n\tv_mfma_f32_16x16x16_bf16 %6, %22, %25, %6\n\tv_mfma_f32_16x16x16_bf16 %7, %23, %25, %7\n\t"
                "s_nop 7"
                : "=&v"(oa[0]), "=&v"(oa[1]), "=&v"(oa[2]), "=&v"(oa[3]), "=&v"(oa[4]), "=&v"(oa[5]), "=&v"(oa[6]), "=&v"(oa[7])
                : "v"(va[0]), "v"(va[1]), "v"(va[2]), "v"(va[3]), "v"(va[4]), "v"(va[5]), "v"(va[6]), "v"(va[7]),
                  "v"(pm[0]), "v"(pm[1]), "v"(pm[2]), "v"(pm[3]), "v"(pm[4]), "v"(pm[5]), "v"(pm[6]), "v"(pm[7]), "v"(p0), "v"(p1));
#pragma unroll
            for (int u = 0; u < 4; ++u) {
                const f32x4 o0 = oa[2 * u], o1 = oa[2 * u + 1];
                const v4u pk = {pack2(o0[0], o0[1]), pack2(o0[2], o0[3]), pack2(o1[0], o1[1]), pack2(o1[2], o1[3])};
                st16(r, W_OX + (((seg * 2 + qt) * 32 + h) * 4 + u) * 1024 + l * 16, pk);
            }
        }
        DRAIN();
        { const int tt = 2 * seg + (l >> 5);
            __hip_atomic_fetch_add((unsigned*)(p.ws + F_RDY + (((tt & 3) * 32 + (l & 31)) * 32 + (tt >> 2)) * 4), 1u, __ATOMIC_RELAXED, __HIP_MEMORY_SCOPE_AGENT); }
        if (seg + 4 < NSEG && !fetch(seg + 4)) return;
    }
}

constexpr int OP_P = 80;
__device__ __forceinline__ void oproj_block(const Params& p, rsrc_t r, unsigned char* lds, const int wv, const int nt, const int rho, const int j, const unsigned tag
    ) {
    (void)j;
    const int w = wv, l = otid() & 63, n16 = l & 15, g = l >> 4;
    const unsigned ldsbase = (unsigned)(size_t)lds;
    bf16x8 wof[32][2]; v32u wo[6];
    asm volatile("" : "={a[128:159]}"(wo[0]), "={a[160:191]}"(wo[1]), "={a[192:223]}"(wo[2]), "={a[224:255]}"(wo[3]), "={v[192:223]}"(wo[4]), "={v[224:255]}"(wo[5]));
#pragma unroll
    for (int kq = 0; kq < 32; ++kq) {
        const int ks = (kq + 16) & 31;
        const unsigned st = ldsbase + w * 8192 + (ks & 1) * 4096, sa = st + (l >> 2) * OP_P + (l & 3) * 16;
        switch (ks) {
            case 0: asm volatile("ds_write_b128 %0, a[128:131]\n\tds_write_b128 %0, a[132:135] offset:%c2" :: "v"(sa), "{a[128:159]}"(wo[0]), "i"(16 * OP_P) : "memory"); break;
            case 1: asm volatile("ds_write_b128 %0, a[136:139]\n\tds_write_b128 %0, a[140:143] offset:%c2" :: "v"(sa), "{a[128:159]}"(wo[0]), "i"(16 * OP_P) : "memory"); break;
            case 2: asm volatile("ds_write_b128 %0, a[144:147]\n\tds_write_b128 %0, a[148:151] offset:%c2" :: "v"(sa), "{a[128:159]}"(wo[0]), "i"(16 * OP_P) : "memory"); break;
            case 3: asm volatile("ds_write_b128 %0, a[152:155]\n\tds_write_b128 %0, a[156:159] offset:%c2" :: "v"(sa), "{a[128:159]}"(wo[0]), "i"(16 * OP_P) : "memory"); break;
            case 4: asm volatile("ds_write_b128 %0, a[160:163]\n\tds_write_b128 %0, a[164:167] offset:%c2" :: "v"(sa), "{a[160:191]}"(wo[1]), "i"(16 * OP_P) : "memory"); break;
            case 5: asm volatile("ds_write_b128 %0, a[168:171]\n\tds_write_b128 %0, a[172:175] offset:%c2" :: "v"(sa), "{a[160:191]}"(wo[1]), "i"(16 * OP_P) : "memory"); break;
            case 6: asm volatile("ds_write_b128 %0, a[176:179]\n\tds_write_b128 %0, a[180:183] offset:%c2" :: "v"(sa), "{a[160:191]}"(wo[1]), "i"(16 * OP_P) : "memory"); break;
            case 7: asm volatile("ds_write_b128 %0, a[184:187]\n\tds_write_b128 %0, a[188:191] offset:%c2" :: "v"(sa), "{a[160:191]}"(wo[1]), "i"(16 * OP_P) : "memory"); break;
            case 8: asm volatile("ds_write_b128 %0, a[192:195]\n\tds_write_b128 %0, a[196:199] offset:%c2" :: "v"(sa), "{a[192:223]}"(wo[2]), "i"(16 * OP_P) : "memory"); break;
            case 9: asm volatile("ds_write_b128 %0, a[200:203]\n\tds_write_b128 %0, a[204:207] offset:%c2" :: "v"(sa), "{a[192:223]}"(wo[2]), "i"(16 * OP_P) : "memory"); break;
            case 10: asm volatile("ds_write_b128 %0, a[208:211]\n\tds_write_b128 %0, a[212:215] offset:%c2" :: "v"(sa), "{a[192:223]}"(wo[2]), "i"(16 * OP_P) : "memory"); break;
            case 11: asm volatile("ds_write_b128 %0, a[216:219]\n\tds_write_b128 %0, a[220:223] offset:%c2" :: "v"(sa), "{a[192:223]}"(wo[2]), "i"(16 * OP_P) : "memory"); break;
            case 12: asm volatile("ds_write_b128 %0, a[224:227]\n\tds_write_b128 %0, a[228:231] offset:%c2" :: "v"(sa), "{a[224:255]}"(wo[3]), "i"(16 * OP_P) : "memory"); break;
            case 13: asm volatile("ds_write_b128 %0, a[232:235]\n\tds_write_b128 %0, a[236:239] offset:%c2" :: "v"(sa), "{a[224:255]}"(wo[3]), "i"(16 * OP_P) : "memory"); break;
            case 14: asm volatile("ds_write_b128 %0, a[240:243]\n\tds_write_b128 %0, a[244:247] offset:%c2" :: "v"(sa), "{a[224:255]}"(wo[3]), "i"(16 * OP_P) : "memory"); break;
            case 15: asm volatile("ds_write_b128 %0, a[248:251]\n\tds_write_b128 %0, a[252:255] offset:%c2" :: "v"(sa), "{a[224:255]}"(wo[3]), "i"(16 * OP_P) : "memory"); break;
            case 16: asm volatile("ds_write_b128 %0, v[192:195]\n\tds_write_b128 %0, v[196:199] offset:%c2" :: "v"(sa), "{v[192:223]}"(wo[4]), "i"(16 * OP_P) : "memory"); break;
            case 17: asm volatile("ds_write_b128 %0, v[200:203]\n\tds_write_b128 %0, v[204:207] offset:%c2" :: "v"(sa), "{v[192:223]}"(wo[4]), "i"(16 * OP_P) : "memory"); break;
            case 18: asm volatile("ds_write_b128 %0, v[208:211]\n\tds_write_b128 %0, v[212:215] offset:%c2" :: "v"(sa), "{v[192:223]}"(wo[4]), "i"(16 * OP_P) : "memory"); break;
            case 19: asm volatile("ds_write_b128 %0, v[216:219]\n\tds_write_b128 %0, v[220:223] offset:%c2" :: "v"(sa), "{v[192:223]}"(wo[4]), "i"(16 * OP_P) : "memory"); break;
            case 20: asm volatile("ds_write_b128 %0, v[224:227]\n\tds_write_b128 %0, v[228:231] offset:%c2" :: "v"(sa), "{v[224:255]}"(wo[5]), "i"(16 * OP_P) : "memory"); break;
            case 21: asm volatile("ds_write_b128 %0, v[232:235]\n\tds_write_b128 %0, v[236:239] offset:%c2" :: "v"(sa), "{v[224:255]}"(wo[5]), "i"(16 * OP_P) : "memory"); break;
            case 22: asm volatile("ds_write_b128 %0, v[240:243]\n\tds_write_b128 %0, v[244:247] offset:%c2" :: "v"(sa), "{v[224:255]}"(wo[5]), "i"(16 * OP_P) : "memory"); break;
            case 23: asm volatile("ds_write_b128 %0, v[248:251]\n\tds_write_b128 %0, v[252:255] offset:%c2" :: "v"(sa), "{v[224:255]}"(wo[5]), "i"(16 * OP_P) : "memory"); break;
        }
#pragma unroll
        for (int jn = 0; jn < 2; ++jn) {
            const int pt = ks < 24 ? OP_P : 64;
            const unsigned a0 = (ks < 24 ? st : WO_L + w * 16384 + (ks - 24) * 2048) + (4 * g + (n16 >> 2)) * pt + (16 * jn + 4 * (n16 & 3)) * 2;
            const v2u lo = trb(a0), hi = trb(a0 + 16 * pt);
            v4u v = {lo.x, lo.y, hi.x, hi.y}; asm volatile("" : "=a"(wof[ks][jn]) : "0"(as_frag(v))); }
    }
    __syncthreads();
    const v4u rd = mk_srd(p.ws, W_TOTAL), ro = mk_srd(p.out, S * D * 2), rx = mk_srd(p.Aq, S * D * 2);
    const unsigned OOB = 0xF0000000u;
    const int NT = 32, jn = w & 1;
    auto tile = [&](int i) -> unsigned { return (unsigned)(4 * i + rho); };
    const unsigned r_off = F_RDY + (rho * 32 + nt) * 128 + (l & 31) * 4;
    const unsigned a_b = W_OX + 8 * w * 4096 + l * 16;
    const int n0 = nt * 32 + 16 * jn + 4 * g;
    const unsigned c_b = W_A + (n0 >> 3) * 32768 + n16 * 16 + (g & 1) * 8, g_b = W_GATE + n16 * 4, o_b = (unsigned)((n16 * 1024 + n0) * 2);
    auto mix = [&](const v2u& av, const v2u& xv, unsigned gv, const f32x4& bs) -> v2u {
        const float gg = __uint_as_float(gv), og = rbfh(1.f - gg); float e[4];
#pragma unroll
        for (int k = 0; k < 4; ++k) { const unsigned aw = k < 2 ? av.x : av.y, xw = k < 2 ? xv.x : xv.y; e[k] = rbfh(gg * rbfh((k & 1) ? hi16(aw) * hi16(xw) : lo16(aw) * lo16(xw))) + rbfh(og * rbfh(bs[k])); }
        return (v2u){pack2(e[0], e[1]), pack2(e[2], e[3])}; };
    const unsigned s_w = ldsbase + w * 2048 + l * 16, s_r = ldsbase + jn * 1024 + l * 16;
    unsigned long long known = 0ull; int nev = 0; (void)nev;
    bool have = false, pend = false; v4u af[32]; v2u pa = {0u, 0u}, px = {0u, 0u}; unsigned pg = 0u, cf2 = 0u;
    auto finish = [&](int i, bool on) {
        v2u o = {0u, 0u};
        if (on) {
            const __attribute__((address_space(3))) f32x4* q = (const __attribute__((address_space(3))) f32x4*)(size_t)(s_r + (unsigned)(i & 1) * 8192u);
            const f32x4 bs = (q[0] + q[128]) + (q[256] + q[384]);
            o = mix(pa, px, pg, bs);
        }
        asm volatile("buffer_store_dwordx2 %0, %1, %2, 0 offen" :: "v"(o), "v"(on ? o_b + tile(i) * 32768u : OOB), "s"(ro) : "memory"); };
    if (!wait_prep(r, l, tag) || !wait_word(r, F_CONV, 32u, 256u)) ENDPGM();
#pragma unroll 1
    for (int i = 0; i < NT; ++i) {
        if (!have) {
            for (unsigned spin = 0; !((known >> i) & 1ull); ++spin) { MEMBAR();
                known = __ballot(ld4(r, r_off) == 32u);
                if ((known >> i) & 1ull) break;
                if (spin > (1u << 21)) { st4(r, F_ERR, 128u); ENDPGM(); }
                __builtin_amdgcn_s_sleep(O_SLP); }
            const unsigned a0 = a_b + tile(i) * 131072u;
#pragma unroll
            for (int q = 0; q < 8; ++q)
                asm volatile("buffer_load_dwordx4 %0, %4, %5, %6 offen sc1\n\tbuffer_load_dwordx4 %1, %4, %5, %6 offen offset:1024 sc1\n\t"
                             "buffer_load_dwordx4 %2, %4, %5, %6 offen offset:2048 sc1\n\tbuffer_load_dwordx4 %3, %4, %5, %6 offen offset:3072 sc1"
                             : "=&v"(af[4 * q]), "=&v"(af[4 * q + 1]), "=&v"(af[4 * q + 2]), "=&v"(af[4 * q + 3]) : "v"(a0), "s"(rd), "s"(q * 4096) : "memory");
            asm volatile("buffer_store_dword %0, %0, %1, 0 offen\n\tbuffer_store_dword %0, %0, %1, 0 offen" :: "v"(OOB), "s"(ro) : "memory");
        }
        const bool fin = (w >> 1) == (i & 1);
        const bool more = i + 1 < NT && ((known >> (i + 1)) & 1ull);
        const unsigned an = more ? a_b + tile(i + 1) * 131072u : OOB;
        unsigned cfl, fg; v2u fa, fx;
        asm volatile("buffer_load_dwordx2 %0, %1, %2, 0 offen" : "=&v"(fx) : "v"(fin ? o_b + tile(i) * 32768u : OOB), "s"(rx) : "memory");
        asm volatile("buffer_load_dword %0, %3, %6, 0 offen sc1\n\tbuffer_load_dwordx2 %1, %4, %6, 0 offen sc1\n\tbuffer_load_dword %2, %5, %6, 0 offen sc1"
                     : "=&v"(cfl), "=&v"(fa), "=&v"(fg) : "v"(((known >> (i + 2)) & 1ull) ? OOB : r_off), "v"(fin ? c_b + tile(i) * 256u : OOB), "v"(fin ? g_b + tile(i) * 64u : OOB), "s"(rd) : "memory");
        f32x4 acc0 = {0.f, 0.f, 0.f, 0.f}, acc1 = {0.f, 0.f, 0.f, 0.f};
        asm volatile("s_nop 1" : "+v"(acc0), "+v"(acc1));
#pragma unroll
        for (int q = 0; q < 8; ++q) {
            if (q == 2) finish(i - 1, pend);
            if (q == 6) asm volatile("buffer_load_dword %0, %1, %2, 0 offen sc1" : "+v"(cf2) : "v"(more ? OOB : r_off), "s"(rd) : "memory");
            asm volatile(
                "s_waitcnt vmcnt(37)\n\tv_mfma_f32_16x16x32_bf16 %4, %6, %0, %4\n\tv_mfma_f32_16x16x32_bf16 %5, %7, %0, %5\n\tbuffer_load_dwordx4 %0, %14, %15, %16 offen sc1\n\t"
                "s_waitcnt vmcnt(37)\n\tv_mfma_f32_16x16x32_bf16 %4, %8, %1, %4\n\tv_mfma_f32_16x16x32_bf16 %5, %9, %1, %5\n\tbuffer_load_dwordx4 %1, %14, %15, %16 offen offset:1024 sc1\n\t"
                "s_waitcnt vmcnt(37)\n\tv_mfma_f32_16x16x32_bf16 %4, %10, %2, %4\n\tv_mfma_f32_16x16x32_bf16 %5, %11, %2, %5\n\tbuffer_load_dwordx4 %2, %14, %15, %16 offen offset:2048 sc1\n\t"
                "s_waitcnt vmcnt(37)\n\tv_mfma_f32_16x16x32_bf16 %4, %12, %3, %4\n\tv_mfma_f32_16x16x32_bf16 %5, %13, %3, %5\n\tbuffer_load_dwordx4 %3, %14, %15, %16 offen offset:3072 sc1"
                : "+v"(af[4 * q]), "+v"(af[4 * q + 1]), "+v"(af[4 * q + 2]), "+v"(af[4 * q + 3]), "+v"(acc0), "+v"(acc1)
                : "a"(wof[4 * q][0]), "a"(wof[4 * q][1]), "a"(wof[4 * q + 1][0]), "a"(wof[4 * q + 1][1]), "a"(wof[4 * q + 2][0]), "a"(wof[4 * q + 2][1]), "a"(wof[4 * q + 3][0]), "a"(wof[4 * q + 3][1]),
                  "v"(an), "s"(rd), "s"(q * 4096) : "memory"); }
        const unsigned po = (unsigned)(i & 1) * 8192u;
        asm volatile("s_nop 7\n\tds_write_b128 %6, %4\n\tds_write_b128 %6, %5 offset:1024\n\ts_waitcnt vmcnt(34) lgkmcnt(0)\n\ts_barrier"
                     : "+v"(cfl), "+v"(fa), "+v"(fg), "+v"(fx) : "v"(acc0), "v"(acc1), "v"(s_w + po) : "memory");
        known |= __ballot(cfl == 32u);
        pa = fa; px = fx; pg = fg; pend = fin && more;
        if (!more) finish(i, fin);
        have = more;
        if (!more) {
            asm volatile("s_waitcnt vmcnt(1)" : "+v"(cf2), "+v"(af[0]), "+v"(af[1]), "+v"(af[2]), "+v"(af[3]), "+v"(af[4]), "+v"(af[5]), "+v"(af[6]), "+v"(af[7]), "+v"(af[8]), "+v"(af[9]), "+v"(af[10]), "+v"(af[11]),
                         "+v"(af[12]), "+v"(af[13]), "+v"(af[14]), "+v"(af[15]) :: "memory");
            known |= __ballot(cf2 == 32u);
        }
    }
    if (nt == 0 && rho == 0 && w == 0) st4(r, l == 0 ? F_EP : OOB, tag);
}

__device__ __forceinline__ void bulk_block(const Params& p, rsrc_t r, unsigned char* lds, const int wv, const int j, const unsigned tag
    ) {
    const int x = (j & 7) >> 1, i32 = (j >> 3) * 2 + (j & 1);
    auto flags = [&](int mt, int nmt, int h0, int nh, int mat) {
        DRAIN(); __syncthreads();
        const int tid = otid(); if (tid < nmt * nh) st4(r, F_KVQ + ((mt + tid / nh) * 32 + h0 + tid % nh) * 64 + mat * 4, tag); };
#pragma unroll 1
    for (int t = 0; t < 2; ++t) {
        const int mt = 2 * t + (i32 & 1), mat = (i32 >> 1) & 1, hd = 8 * x + (i32 >> 2), rot = ((i32 >> 1) & 7) * 2;
        const bf16_t* Bm = mat ? p.Wv : p.Wk;
        if (mat) gemm<1, 4, 4, 8>(r, p.Aq, Bm, mt * 128, hd * 128, lds, W_VF, wv, rot, nullptr, t == 1, t == 0 ? Bm : nullptr, (mt + 2) * 128, hd * 128);
        else gemm<0, 4, 4, 8>(r, p.Aq, Bm, mt * 128, hd * 128, lds, W_KX, wv, rot, nullptr, t == 1, t == 0 ? Bm : nullptr, (mt + 2) * 128, hd * 128);
        flags(mt, 1, hd, 1, mat);
    }
    const int hp = 4 * x + (i32 >> 3), q8 = i32 & 7, rot = ROT2 * (i32 >> 2);
    {
        const int m = q8 < 4 ? 4 + (q8 & 1) : q8 - 4, mat = q8 < 4 ? q8 >> 1 : 2;
        const bf16_t* Bn = mat == 2 ? p.Wq : mat == 1 ? p.Wv : p.Wk;
        const int mt = 4 + (q8 & 3), mv = q8 >> 2;
        const bf16_t* tk = (wv == 1 && j < 32) ? p.Aq + (64 + j) * 16384 : nullptr;
        if (mv) gemm<1, 4, 8, 4, 0, NoHook, 8>(r, p.Aq, p.Wv, mt * 128, hp * 256, lds, W_VF, wv, rot, tk, false, Bn, m * 256, hp * 256);
        else gemm<0, 4, 8, 4, 0, NoHook, 8>(r, p.Aq, p.Wk, mt * 128, hp * 256, lds, W_KX, wv, rot, tk, false, Bn, m * 256, hp * 256);
        flags(mt, 1, 2 * hp, 2, mv);
        const bf16_t* tl = (wv == 1 && j < 32) ? p.Aq + (96 + j) * 16384 : nullptr;
        if (mat == 1) gemm<1, 8, 8, 4>(r, p.Aq, Bn, m * 256, hp * 256, lds, W_VF, wv, rot, tl, true);
        else gemm<0, 8, 8, 4>(r, p.Aq, Bn, m * 256, hp * 256, lds, mat == 2 ? W_QX : W_KX, wv, rot, tl, true);
        flags(2 * m, 2, 2 * hp, 2, mat);
    }
    {
        unsigned wi = 0u;
        const int l = otid() & 63; const v4u sw = mk_srd(p.Wo, 4096 * 1024 * 2); const unsigned wa = (unsigned)((wv * 1024 + (l >> 2)) * 2048 + i32 * 64 + (l & 3) * 16);
        unsigned long long tab;
        asm volatile("s_getpc_b64 %0\n\ts_branch .Lwo%=\n\t"
                     "buffer_load_dwordx4 a[128:131], v191, s[92:95], s91 offen\n\ts_setpc_b64 s[96:97]\n\t" "buffer_load_dwordx4 a[132:135], v191, s[92:95], s91 offen\n\ts_setpc_b64 s[96:97]\n\t"
                     "buffer_load_dwordx4 a[136:139], v191, s[92:95], s91 offen\n\ts_setpc_b64 s[96:97]\n\t" "buffer_load_dwordx4 a[140:143], v191, s[92:95], s91 offen\n\ts_setpc_b64 s[96:97]\n\t"
                     "buffer_load_dwordx4 a[144:147], v191, s[92:95], s91 offen\n\ts_setpc_b64 s[96:97]\n\t" "buffer_load_dwordx4 a[148:151], v191, s[92:95], s91 offen\n\ts_setpc_b64 s[96:97]\n\t"
                     "buffer_load_dwordx4 a[152:155], v191, s[92:95], s91 offen\n\ts_setpc_b64 s[96:97]\n\t" "buffer_load_dwordx4 a[156:159], v191, s[92:95], s91 offen\n\ts_setpc_b64 s[96:97]\n\t"
                     "buffer_load_dwordx4 a[160:163], v191, s[92:95], s91 offen\n\ts_setpc_b64 s[96:97]\n\t" "buffer_load_dwordx4 a[164:167], v191, s[92:95], s91 offen\n\ts_setpc_b64 s[96:97]\n\t"
                     "buffer_load_dwordx4 a[168:171], v191, s[92:95], s91 offen\n\ts_setpc_b64 s[96:97]\n\t" "buffer_load_dwordx4 a[172:175], v191, s[92:95], s91 offen\n\ts_setpc_b64 s[96:97]\n\t"
                     "buffer_load_dwordx4 a[176:179], v191, s[92:95], s91 offen\n\ts_setpc_b64 s[96:97]\n\t" "buffer_load_dwordx4 a[180:183], v191, s[92:95], s91 offen\n\ts_setpc_b64 s[96:97]\n\t"
                     "buffer_load_dwordx4 a[184:187], v191, s[92:95], s91 offen\n\ts_setpc_b64 s[96:97]\n\t" "buffer_load_dwordx4 a[188:191], v191, s[92:95], s91 offen\n\ts_setpc_b64 s[96:97]\n\t"
                     "buffer_load_dwordx4 a[192:195], v191, s[92:95], s91 offen\n\ts_setpc_b64 s[96:97]\n\t" "buffer_load_dwordx4 a[196:199], v191, s[92:95], s91 offen\n\ts_setpc_b64 s[96:97]\n\t"
                     "buffer_load_dwordx4 a[200:203], v191, s[92:95], s91 offen\n\ts_setpc_b64 s[96:97]\n\t" "buffer_load_dwordx4 a[204:207], v191, s[92:95], s91 offen\n\ts_setpc_b64 s[96:97]\n\t"
                     "buffer_load_dwordx4 a[208:211], v191, s[92:95], s91 offen\n\ts_setpc_b64 s[96:97]\n\t" "buffer_load_dwordx4 a[212:215], v191, s[92:95], s91 offen\n\ts_setpc_b64 s[96:97]\n\t"
                     "buffer_load_dwordx4 a[216:219], v191, s[92:95], s91 offen\n\ts_setpc_b64 s[96:97]\n\t" "buffer_load_dwordx4 a[220:223], v191, s[92:95], s91 offen\n\ts_setpc_b64 s[96:97]\n\t"
                     "buffer_load_dwordx4 a[224:227], v191, s[92:95], s91 offen\n\ts_setpc_b64 s[96:97]\n\t" "buffer_load_dwordx4 a[228:231], v191, s[92:95], s91 offen\n\ts_setpc_b64 s[96:97]\n\t"
                     "buffer_load_dwordx4 a[232:235], v191, s[92:95], s91 offen\n\ts_setpc_b64 s[96:97]\n\t" "buffer_load_dwordx4 a[236:239], v191, s[92:95], s91 offen\n\ts_setpc_b64 s[96:97]\n\t"
                     "buffer_load_dwordx4 a[240:243], v191, s[92:95], s91 offen\n\ts_setpc_b64 s[96:97]\n\t" "buffer_load_dwordx4 a[244:247], v191, s[92:95], s91 offen\n\ts_setpc_b64 s[96:97]\n\t"
                     "buffer_load_dwordx4 a[248:251], v191, s[92:95], s91 offen\n\ts_setpc_b64 s[96:97]\n\t" "buffer_load_dwordx4 a[252:255], v191, s[92:95], s91 offen\n\ts_setpc_b64 s[96:97]\n\t"
                     "buffer_load_dwordx4 v[192:195], v191, s[92:95], s91 offen\n\ts_setpc_b64 s[96:97]\n\t" "buffer_load_dwordx4 v[196:199], v191, s[92:95], s91 offen\n\ts_setpc_b64 s[96:97]\n\t"
                     "buffer_load_dwordx4 v[200:203], v191, s[92:95], s91 offen\n\ts_setpc_b64 s[96:97]\n\t" "buffer_load_dwordx4 v[204:207], v191, s[92:95], s91 offen\n\ts_setpc_b64 s[96:97]\n\t"
                     "buffer_load_dwordx4 v[208:211], v191, s[92:95], s91 offen\n\ts_setpc_b64 s[96:97]\n\t" "buffer_load_dwordx4 v[212:215], v191, s[92:95], s91 offen\n\ts_setpc_b64 s[96:97]\n\t"
                     "buffer_load_dwordx4 v[216:219], v191, s[92:95], s91 offen\n\ts_setpc_b64 s[96:97]\n\t" "buffer_load_dwordx4 v[220:223], v191, s[92:95], s91 offen\n\ts_setpc_b64 s[96:97]\n\t"
                     "buffer_load_dwordx4 v[224:227], v191, s[92:95], s91 offen\n\ts_setpc_b64 s[96:97]\n\t" "buffer_load_dwordx4 v[228:231], v191, s[92:95], s91 offen\n\ts_setpc_b64 s[96:97]\n\t"
                     "buffer_load_dwordx4 v[232:235], v191, s[92:95], s91 offen\n\ts_setpc_b64 s[96:97]\n\t" "buffer_load_dwordx4 v[236:239], v191, s[92:95], s91 offen\n\ts_setpc_b64 s[96:97]\n\t"
                     "buffer_load_dwordx4 v[240:243], v191, s[92:95], s91 offen\n\ts_setpc_b64 s[96:97]\n\t" "buffer_load_dwordx4 v[244:247], v191, s[92:95], s91 offen\n\ts_setpc_b64 s[96:97]\n\t"
                     "buffer_load_dwordx4 v[248:251], v191, s[92:95], s91 offen\n\ts_setpc_b64 s[96:97]\n\t" "buffer_load_dwordx4 v[252:255], v191, s[92:95], s91 offen\n\ts_setpc_b64 s[96:97]\n\t"
                     "buffer_load_dwordx4 v191, s[92:95], s91 offen lds\n\ts_setpc_b64 s[96:97]\n.Lwo%=:" : "=s"(tab));
        auto hook = [&](int) {
            const unsigned q = min(wi, 63u);
            asm volatile("s_mov_b32 m0, %1\n\ts_swappc_b64 s[96:97], %0"
                         :: "s"(tab + (min(q, 48u) * 12u + 4u)), "s"(WO_L + wv * 16384 + (q - 48u) * 1024u), "{v191}"(wa), "{s[92:95]}"(sw), "{s91}"(q * 32768u) : "memory", "m0", "s96", "s97", WO_REGS);
            ++wi; };
#pragma unroll 1
        for (int t = 0; t < 2; ++t) {
            const int mt = t ? 8 + q8 : 12 + (q8 & 3), mv = t ? 2 : q8 >> 2;
            if (mv == 1) gemm<1, 4, 8, 4, 1>(r, p.Aq, p.Wv, mt * 128, hp * 256, lds, W_VF, wv, rot, nullptr, false, p.Wq, (8 + q8) * 128, hp * 256, hook);
            else gemm<0, 4, 8, 4, 1>(r, p.Aq, t ? p.Wq : p.Wk, mt * 128, hp * 256, lds, t ? W_QX : W_KX, wv, rot, nullptr, t == 1, t ? nullptr : p.Wq, (8 + q8) * 128, hp * 256, hook);
            flags(mt, 1, 2 * hp, 2, mv);
        }
        oproj_block(p, r, lds, wv, i32, x, j, tag);
    }
}

__device__ __forceinline__ void help_block(const Params& p, rsrc_t r, unsigned char* lds, const int wv, const int k, const unsigned tag) {
    const int j = -32 + k; (void)j;
    if (HELP_D) wait_long(rtnow() + HELP_D);
    prep_block(p, r, k, wv, tag);
    if (!conv_block(p, r, k, lds, wv, j, tag)) return;
    attn_block(p, r, lds, wv, k, j, tag);
}

__global__ void __launch_bounds__(256, 1) k_main(Params p) {
    extern __shared__ __attribute__((aligned(16))) unsigned char lds[];
    rsrc_t r = __builtin_amdgcn_make_buffer_rsrc(p.ws, 0, W_TOTAL, 0x00020000);
    const int wv = __builtin_amdgcn_readfirstlane(threadIdx.x >> 6);
    int role = blockIdx.x;
    unsigned xcc; asm volatile("s_getreg_b32 %0, hwreg(HW_REG_XCC_ID)" : "=s"(xcc)); xcc &= 7u;
    unsigned near = 0u, old = 0u, slot = (xcc >> 2) * 128u + (blockIdx.x >> 4) * 8u + (xcc & 3u) * 2u + ((blockIdx.x >> 3) & 1u);
    const unsigned tag = __builtin_amdgcn_readfirstlane(ld4(r, (threadIdx.x & 63) == 1 ? F_CLM + slot * 64 + 4 : F_EP)) * 2654435761u + 0x9E3779B9u;
    if (threadIdx.x == 0) old = __hip_atomic_exchange((unsigned*)(p.ws + F_CLM + slot * 64), tag, __ATOMIC_RELAXED, __HIP_MEMORY_SCOPE_AGENT);
    near = 126u;
    if (xcc < 4u && ((CALM >> (slot >> 5)) & 1u)) {
        const unsigned gr = (slot >> 5) & 3u;
        const rsrc_t rh = __builtin_amdgcn_make_buffer_rsrc(p.ws + hx_base(p.ws), 0, U_END * 16384, 0x00020000);
        const unsigned ca = (threadIdx.x & 63) != 0 ? 0xF0000000u : (slot & 127u) * 16384u + (unsigned)wv * 2048u + CAL_O;
        const v4u z = {~0u, ~0u, ~0u, ~0u}; unsigned best0 = ~0u, best1 = ~0u;
        for (int i = 0; i < CAL_N; ++i) {
            const unsigned t0 = (unsigned)__builtin_amdgcn_s_memtime(); __builtin_amdgcn_s_waitcnt(0);
            st16(rh, ca, z); DRAIN();
            const unsigned t1 = (unsigned)__builtin_amdgcn_s_memtime(); __builtin_amdgcn_s_waitcnt(0);
            st16(rh, ca + 8192, z); DRAIN();
            const unsigned t2 = (unsigned)__builtin_amdgcn_s_memtime(); __builtin_amdgcn_s_waitcnt(0);
            best0 = min(best0, t1 - t0); best1 = min(best1, t2 - t1); }
        near = gr * 2u + (best1 < best0 ? 1u : 0u);
    }
    if (threadIdx.x == 0) {
        for (int i = 0; i < 256 && old == tag; ++i) { slot = (slot + 1u) & 255u; old = __hip_atomic_exchange((unsigned*)(p.ws + F_CLM + slot * 64), tag, __ATOMIC_RELAXED, __HIP_MEMORY_SCOPE_AGENT); }
        *(volatile __attribute__((address_space(3))) int*)(size_t)(unsigned)(size_t)lds = (int)slot;
    }
    __syncthreads();
    role = __builtin_amdgcn_readfirstlane(*(volatile __attribute__((address_space(3))) int*)(size_t)(unsigned)(size_t)lds) & 255;
    __syncthreads();
    if (PFW && role >= 128) {
        const unsigned id = (unsigned)(role - 128) * 4u + (unsigned)wv, ln = threadIdx.x & 63u;
        const v4u s0 = mk_srd(p.Wq, 8u << 20), s1 = mk_srd(p.Wo, 8u << 20); unsigned d0;
        asm volatile("buffer_load_dword %0, %1, %2, 0 offen\n\tbuffer_load_dword %0, %1, %2, %4 offen" : "=&v"(d0) : "v"(id * 8192u + ln * 128u), "s"(s0), "s"(s1), "s"(4u << 20) : "memory");
        if (PFW & 2) asm volatile("buffer_load_dword %0, %1, %3, 0 offen\n\tbuffer_load_dword %0, %1, %3, %4 offen" : "+v"(d0) : "v"(id * 8192u + ln * 128u), "s"(s0), "s"(s1), "s"(4u << 20) : "memory");
        if (PFW & 4) { const v4u s2 = mk_srd(p.Wk, 8u << 20), s3 = mk_srd(p.Wv, 8u << 20);
            asm volatile("buffer_load_dword %0, %1, %2, 0 offen\n\tbuffer_load_dword %0, %1, %2, %4 offen\n\tbuffer_load_dword %0, %1, %3, 0 offen\n\tbuffer_load_dword %0, %1, %3, %4 offen" : "+v"(d0) : "v"(id * 8192u + ln * 128u), "s"(s2), "s"(s3), "s"(4u << 20) : "memory"); }
        asm volatile("s_waitcnt vmcnt(0)" : "+v"(d0) :: "memory");
    }
    if (role >= 128) bulk_block(p, r, lds, wv, role - 128, tag
        ); else if (rec_block(p, r, lds, wv, role, near, tag)) help_block(p, r, lds, wv, (role >> 3) * 2 + (role & 1), tag);
}

extern "C" void kernel_launch(void* const* d_in, const int* in_sizes, int n_in, void* d_out, int out_size, void* d_ws, size_t ws_size, hipStream_t stream) {
    Params p;
    p.Aq = (const bf16_t*)d_in[0]; p.Ak = (const bf16_t*)d_in[1]; p.Av = (const bf16_t*)d_in[2];
    p.shift_k = (const bf16_t*)d_in[3]; p.ssm_k = (const bf16_t*)d_in[4]; p.ssm_D = (const bf16_t*)d_in[5];
    p.mem0 = (const bf16_t*)d_in[7]; p.Wq = (const bf16_t*)d_in[8]; p.Wk = (const bf16_t*)d_in[9];
    p.Wv = (const bf16_t*)d_in[10]; p.Wo = (const bf16_t*)d_in[11]; p.gate_w = (const bf16_t*)d_in[13];
    p.out = (bf16_t*)d_out; p.ws = (unsigned char*)d_ws;
    (void)hipFuncSetAttribute((const void*)k_main, hipFuncAttributeMaxDynamicSharedMemorySize, L_TOTAL);
    k_main<<<256, 256, L_TOTAL, stream>>>(p);
}
```

```cpp
#include <hip/hip_runtime.h>
#include <stdint.h>
#include <type_traits>
typedef unsigned short bf16_t;
using bf16x8 = __attribute__((ext_vector_type(8))) short;
using f32x4 = __attribute__((ext_vector_type(4))) float;
typedef unsigned v4u __attribute__((ext_vector_type(4)));
typedef unsigned v2u __attribute__((ext_vector_type(2)));
typedef __amdgpu_buffer_rsrc_t rsrc_t;

typedef __bf16 hbf2 __attribute__((ext_vector_type(2)));
typedef float f32x2 __attribute__((ext_vector_type(2)));
__device__ __forceinline__ unsigned pack2(float a, float b) { f32x2 v = {a, b}; return __builtin_bit_cast(unsigned, __builtin_convertvector(v, hbf2)); }
__device__ __forceinline__ float rbfh(float f) { return __uint_as_float(pack2(0.f, f)); }
__device__ __forceinline__ f32x2 rbf2(f32x2 f) { return (f32x2){rbfh(f.x), rbfh(f.y)}; }
__device__ __forceinline__ unsigned nosent(unsigned x) {
    typedef unsigned short us2 __attribute__((ext_vector_type(2)));
    const us2 a = __builtin_bit_cast(us2, x), c = {(unsigned short)0xFFFE, (unsigned short)0xFFFE}; return __builtin_bit_cast(unsigned, __builtin_elementwise_min(a, c)); }

constexpr int S = 2048, D = 1024, H = 32, DH = 128, M = 8, SEG = 32, NSEG = 64;

constexpr unsigned W_FLG = 0;
constexpr unsigned F_KVQ = W_FLG;
constexpr unsigned F_EP = W_FLG + 32768;
constexpr unsigned F_ERR = W_FLG + 32768 + 256;
constexpr unsigned F_PREP = W_FLG + 32768 + 512;
constexpr unsigned F_CAL = W_FLG + 73728;
constexpr unsigned F_CLM = W_FLG + 77824;
constexpr unsigned F_CONV = W_FLG + 33792;
constexpr unsigned F_PROG = W_FLG + 36864;
constexpr unsigned F_ATT = W_FLG + 40960;
constexpr unsigned F_RDY = W_FLG + 49152;
constexpr unsigned FLG_BYTES = 94208;
constexpr unsigned HO_KVM = W_FLG + FLG_BYTES;
constexpr unsigned W_A = HO_KVM + 64 * 32 * 4096;
constexpr unsigned W_GATE = W_A + S * D * 2;
constexpr unsigned W_QX = W_GATE + 8192;
constexpr unsigned W_KX = W_QX + S * 4096 * 2;
constexpr unsigned W_VF = W_KX + S * 4096 * 2;
constexpr unsigned W_OX = W_VF + 64 * 32 * 8192;
constexpr unsigned DBG_BYTES = 0;
constexpr unsigned W_DBG = W_OX + S * 4096 * 2;
constexpr unsigned W_U = W_DBG + DBG_BYTES;
constexpr unsigned W_TOTAL = W_U + S * D * 2;

__device__ __forceinline__ v4u ld16(rsrc_t r, unsigned off) { return __builtin_amdgcn_raw_buffer_load_b128(r, off, 0, 16); }
__device__ __forceinline__ v2u ld8(rsrc_t r, unsigned off) { return __builtin_amdgcn_raw_buffer_load_b64(r, off, 0, 16); }
__device__ __forceinline__ unsigned ld4(rsrc_t r, unsigned off) { return __builtin_amdgcn_raw_buffer_load_b32(r, off, 0, 16); }
__device__ __forceinline__ void st16(rsrc_t r, unsigned off, v4u v) { __builtin_amdgcn_raw_buffer_store_b128(v, r, off, 0, 16); }
__device__ __forceinline__ void st8(rsrc_t r, unsigned off, v2u v) { __builtin_amdgcn_raw_buffer_store_b64(v, r, off, 0, 16); }
__device__ __forceinline__ void st4(rsrc_t r, unsigned off, unsigned v) { __builtin_amdgcn_raw_buffer_store_b32(v, r, off, 0, 16); }
__device__ __forceinline__ bf16x8 as_frag(v4u v) { return __builtin_bit_cast(bf16x8, v); }
#define D1 134
#define D2 90
#define D3 64
#define LB0 16
#define LC0 7
#define LA0 8
#define TR_UP 4
#define TR_DN 2
#define L_DEC 3
#define STAG 1
#define UP_SH 2
#define EARLY_FROM 58
#define OUT_WARM 0
#define AROT 0
#define P_SLP 2
#define O_SLP 2
#define W_NT 0
#define TOL 2
#define A_SLP 0
#define PEN2 20
#define A_UP 24
#define CT0 2
#define CPF 1
#define PH_M 3
#define SKEW 0
#define SKEW1 0
#define XT 1
#define TRK 3
#define WO_NT 0
#define WO_TRK 1
#define WIN 3
#define ROT0 3
#define ROT1 2
#define ROT2 2
#define B_NT 0
#define CALM 15
#define R4M 1
#define HX_SH 0u
#define FINE_T 1
#define FINE_N 0
#define FINE_A 0
#define O_ND 12
#define PEN0 32
#define X0C 20
#define X0A 16
#define X1A 8
#define PEN 24
#define MEMBAR() asm volatile("" ::: "memory")
#define ENDPGM() asm volatile("s_endpgm" ::: "memory")
#define DRAIN() asm volatile("s_waitcnt vmcnt(0)" ::: "memory")

__device__ __forceinline__ float xor16_max(float x) { auto r = __builtin_amdgcn_permlane16_swap(__float_as_uint(x), __float_as_uint(x), false, false); return fmaxf(__uint_as_float(r[0]), __uint_as_float(r[1])); }
__device__ __forceinline__ float xor32_max(float x) { auto r = __builtin_amdgcn_permlane32_swap(__float_as_uint(x), __float_as_uint(x), false, false); return fmaxf(__uint_as_float(r[0]), __uint_as_float(r[1])); }
__device__ __forceinline__ float xor16_add(float x) { auto r = __builtin_amdgcn_permlane16_swap(__float_as_uint(x), __float_as_uint(x), false, false); return __uint_as_float(r[0]) + __uint_as_float(r[1]); }
__device__ __forceinline__ float xor32_add(float x) { auto r = __builtin_amdgcn_permlane32_swap(__float_as_uint(x), __float_as_uint(x), false, false); return __uint_as_float(r[0]) + __uint_as_float(r[1]); }

typedef short v4i16_t __attribute__((ext_vector_type(4)));
__device__ __forceinline__ v2u trb(unsigned lds_addr) { return __builtin_bit_cast(v2u, __builtin_amdgcn_ds_read_tr16_b64_v4i16((__attribute__((address_space(3))) v4i16_t*)(size_t)lds_addr)); }
__device__ __forceinline__ int otid_(int wv) { int t; asm volatile("v_mbcnt_lo_u32_b32 %0, -1, 0\n\tv_mbcnt_hi_u32_b32 %0, -1, %0" : "=v"(t)); return wv * 64 + t; }
#define otid() otid_(wv)
__device__ __forceinline__ bool wait_word(rsrc_t r, unsigned off, unsigned want, unsigned code) {
    for (unsigned spin = 0;; ++spin) { MEMBAR();
        const unsigned v = __builtin_amdgcn_readfirstlane(ld4(r, off));
        if (v >= want) return true;
        if (spin > (1u << 20)) { st4(r, F_ERR, code); return false; }
        __builtin_amdgcn_s_sleep(8); }
}

__device__ __forceinline__ bool wait_prep(rsrc_t r, int l, unsigned tag) {
    for (unsigned spin = 0;; ++spin) { MEMBAR();
        const unsigned v = ld4(r, F_PREP + (l & 31) * 4);
        if (__all(v == tag)) return true;
        if (spin > (1u << 20)) { st4(r, F_ERR, 512u); return false; }
        __builtin_amdgcn_s_sleep(8); }
}
__device__ __forceinline__ unsigned rtnow() { return (unsigned)__builtin_amdgcn_s_memrealtime(); }
__device__ __forceinline__ void wait_long(unsigned target) { for (int i = 0; i < 4096; ++i) { if ((int)(rtnow() - target) >= 0) break; __builtin_amdgcn_s_sleep(8); } }
constexpr int L_TAB = 20480;
constexpr int L_QK = 24576;
__device__ __forceinline__ unsigned img(int f) { return (unsigned)(L_QK + f * 16 + (f >> 4) * 128); }
constexpr int L_FL = 30720, L_Z = L_FL + 128, L_LOG = 31744, L_W = 32768, L_V = 131072;
constexpr int L_TOTAL = 163840;
constexpr unsigned U_END = 96;
#define PMIN 76
#define V_SLP 2
#define R_SLP 1
constexpr unsigned WO_L = 98304;
#define HELP_D 1000
#define V_DEL 16
#define XR 12
#define PFW 0
#define KEYM 3
#define DIAG 0
#define NEARX 0
#define CAL_O 0u
#define H_SLP 0
#define CAL_N 4
#define STEP_MIN 180u

struct Params {
    const bf16_t *Aq, *Ak, *Av, *shift_k, *ssm_k, *ssm_D, *mem0, *Wq, *Wk, *Wv, *Wo, *gate_w;
    bf16_t* out; unsigned char* ws;
};
__host__ __device__ __forceinline__ unsigned hx_base(const void* ws) { const size_t a = (size_t)ws; return (unsigned)((((a + W_TOTAL + 0x7FFFFFu) >> 23) << 23) - a) + HX_SH; }

typedef short v4s __attribute__((ext_vector_type(4)));
typedef unsigned v32u __attribute__((ext_vector_type(32)));
typedef unsigned v16u __attribute__((ext_vector_type(16)));
typedef unsigned v8u __attribute__((ext_vector_type(8)));
typedef unsigned v12u __attribute__((ext_vector_type(12)));
__device__ __forceinline__ v4u mk_srd(const void* p, unsigned bytes) { const size_t a = (size_t)p; v4u d;
    d.x = __builtin_amdgcn_readfirstlane((unsigned)a); d.y = __builtin_amdgcn_readfirstlane((unsigned)(a >> 32) & 0xFFFFu); d.z = __builtin_amdgcn_readfirstlane(bytes); d.w = 0x00020000u; return d; }
__device__ __forceinline__ bf16x8 ldsa(unsigned addr) { return *(const __attribute__((address_space(3))) bf16x8*)(size_t)addr; }
__device__ __forceinline__ void rec_block(const Params& p, const int wv, const int b, const unsigned tag) {
    const int w = wv, tid = otid(), l = tid & 63, n16 = l & 15, g = l >> 4, c = n16 >> 2;
    const int x = (b & 7) >> 1, i32 = (b >> 3) * 2 + (b & 1);
    const unsigned OOB = 0xF0000000u;
    const v4u rhd = mk_srd(p.ws + hx_base(p.ws), U_END * 16384), wsd = mk_srd(p.ws, W_TOTAL);
    const int G = x - 1, vi = G * 32 + i32, h = x == 0 ? i32 : vi >> 1;
    const unsigned pr_a = F_PROG + h * 128 + (x == 0 ? 0 : 8 + (vi & 1) * 8), cal = F_CAL - w * 1024, kvo = HO_KVM + h * 4096 + (x ? 2048 : 0);
    if (CPF) {
        unsigned lo, hi, l2; asm volatile("s_getpc_b64 s[98:99]\n.Lpc%=:\n\ts_add_u32 %2, s98, rec_v_code-.Lpc%=\n\ts_add_u32 %0, s98, rec_s_code-.Lpc%=\n\ts_addc_u32 %1, s99, 0" : "=s"(lo), "=s"(hi), "=s"(l2) :: "s98", "s99", "scc");
        const size_t a = (((size_t)hi << 32) | lo) + (size_t)(long long)((x ? (int)(l2 - lo) : 0) + ((i32 * 4 + w) % 10) * 1024 - 4096 + l * 16);
        asm volatile("s_mov_b32 m0, %1\n\ts_nop 0\n\tglobal_load_lds_dwordx4 %0, off" :: "v"(a & ~(size_t)15), "s"(x ? L_V + 16384 : 0) : "memory", "m0");
    }
    typedef __attribute__((address_space(3))) volatile unsigned lu32;
    unsigned nW = 0u, nA = x * 32768, nS = 6 * 16384 + x * 1024 - w * 1024, nC = 32 * 16384, b0 = (64 + (h >> 2)) * 16384 + (h & 3) * 2048 - (x ? 0 : w * 256 + L_QK), b1 = b0 + 8 * 16384;
    unsigned pob = w == 0 ? nA + (i32 >> 4) * 16384 + (i32 & 15) * 512 : (w == 1 && x) ? nC + (G * 4 + (i32 >> 3)) * 16384 + (i32 & 7) * 1024 - 1024 : (w == 2 && i32 == 0) ? nS : OOB;
    if (x == 0) {
        const v4u sq = mk_srd(p.Wq, 1024 * 4096 * 2), sk = mk_srd(p.Wk, 1024 * 4096 * 2);
        const unsigned m0b = L_W + w * 1024, f = (c & 1) | ((g & 1) << 1) | ((c >> 1) << 2);
        const unsigned t0 = L_W + (16 * (g >> 1) + 4 * (g & 1) + (c & 1) + 8 * (c >> 1)) * 256 + (((2 * w) ^ f) << 5) + (n16 & 3) * 8;
        const unsigned dv = (4 * w + g) * 8192 + h * 256 + ((n16 ^ (((g & 1) | (w << 1)) << 1)) << 4);
        v8u pa;
#pragma unroll
        for (int q = 0; q < 8; ++q) pa[q] = (t0 + ((w + (q >> 1)) & 3) * 32768) ^ ((q & 1) << 5);
        const unsigned kx = W_KX + h * 8192, vx = W_VF + h * 8192 + w * 1024, kf = F_KVQ + h * 64, swo = 6 * 16384 + h * 8;
        const unsigned aq = img(8 * g + (n16 >> 2)) + (n16 & 1) * 8;
        const v8u pc = {(unsigned)tid * 16, (unsigned)(L_W + w * 32768 + l * 16), (n16 & 2) ? 0xEEEEu : 0x4444u, aq,
                  (unsigned)(((w + 1) & 3) * 1024 + l * 16), (unsigned)(((w + 2) & 3) * 1024 + l * 16), (unsigned)(((w + 3) & 3) * 1024 + l * 16), img(32 * w + n16) + g * 4};
        const v2u pd = {(n16 & 2) ? (unsigned)L_Z : aq + w * 768, (n16 & 2) ? aq + w * 768 : (unsigned)L_Z};
        *(__attribute__((address_space(3))) v4u*)(size_t)(L_TAB + tid * 16) = (v4u){(unsigned)(L_QK + 3072 + g * 384 + n16 * 8),
                  n16 < 8 ? (unsigned)(((n16 >> 2) * 4 + (h >> 3)) * 16384 + (h & 7) * 1024 + w * 256 + (g >> 1) * 128 + (n16 & 2) * 32 + (g & 1) * 32 + (n16 & 1) * 16) : OOB,
                  (unsigned)((32 * w + 16 * (g & 1) + n16) * 16 + (g >> 1) * 8), (unsigned)(L_QK + 3072 + w * 768 + ((n16 & 1) + 2 * (n16 >> 2)) * 32 + g * 8 + (n16 & 2) * 2)};
        if (tid < 64) ((lu32*)(size_t)L_FL)[tid] = 0u;
    asm volatile(
        "s_add_u32 s80, %[m0b], 0\n\t"
        "s_mov_b32 s81, 0\n\t"
        "s_mov_b32 s82, 4\n\t"
        ".Ld0%=:\n\t"
        "s_mov_b32 m0, s80\n\t"
        "s_add_u32 s80, s80, 0x8000\n\t"
        "buffer_load_dwordx4 v96, %[sq], s81 offen lds\n\t"
        "s_add_u32 m0, m0, 0x1000\n\t"
        "s_add_u32 s81, s81, 0x20000\n\t"
        "buffer_load_dwordx4 v96, %[sq], s81 offen lds\n\t"
        "s_add_u32 m0, m0, 0x1000\n\t"
        "s_add_u32 s81, s81, 0x20000\n\t"
        "buffer_load_dwordx4 v96, %[sq], s81 offen lds\n\t"
        "s_add_u32 m0, m0, 0x1000\n\t"
        "s_add_u32 s81, s81, 0x20000\n\t"
        "buffer_load_dwordx4 v96, %[sq], s81 offen lds\n\t"
        "s_add_u32 m0, m0, 0x1000\n\t"
        "s_add_u32 s81, s81, 0x20000\n\t"
        "s_sub_u32 s82, s82, 1\n\t"
        "s_cmp_lg_u32 s82, 0\n\t"
        "s_cbranch_scc1 .Ld0%=\n\t"
        "s_add_u32 s80, %[m0b], 16384\n\t"
        "s_mov_b32 s81, 0\n\t"
        "s_mov_b32 s82, 4\n\t"
        ".Ld1%=:\n\t"
        "s_mov_b32 m0, s80\n\t"
        "s_add_u32 s80, s80, 0x8000\n\t"
        "buffer_load_dwordx4 v96, %[sk], s81 offen lds\n\t"
        "s_add_u32 m0, m0, 0x1000\n\t"
        "s_add_u32 s81, s81, 0x20000\n\t"
        "buffer_load_dwordx4 v96, %[sk], s81 offen lds\n\t"
        "s_add_u32 m0, m0, 0x1000\n\t"
        "s_add_u32 s81, s81, 0x20000\n\t"
        "buffer_load_dwordx4 v96, %[sk], s81 offen lds\n\t"
        "s_add_u32 m0, m0, 0x1000\n\t"
        "s_add_u32 s81, s81, 0x20000\n\t"
        "buffer_load_dwordx4 v96, %[sk], s81 offen lds\n\t"
        "s_add_u32 m0, m0, 0x1000\n\t"
        "s_add_u32 s81, s81, 0x20000\n\t"
        "s_sub_u32 s82, s82, 1\n\t"
        "s_cmp_lg_u32 s82, 0\n\t"
        "s_cbranch_scc1 .Ld1%=\n\t"
        "s_waitcnt vmcnt(16)\n\t"
        "s_barrier\n\t"
        "ds_read_b64_tr_b16 v[0:1], v98 offset:0\n\t"
        "ds_read_b64_tr_b16 v[2:3], v98 offset:512\n\t"
        "ds_read_b64_tr_b16 v[4:5], v98 offset:8192\n\t"
        "ds_read_b64_tr_b16 v[6:7], v98 offset:8704\n\t"
        "ds_read_b64_tr_b16 v[8:9], v99 offset:0\n\t"
        "ds_read_b64_tr_b16 v[10:11], v99 offset:512\n\t"
        "ds_read_b64_tr_b16 v[12:13], v99 offset:8192\n\t"
        "ds_read_b64_tr_b16 v[14:15], v99 offset:8704\n\t"
        "ds_read_b64_tr_b16 a[16:17], v100 offset:0\n\t"
        "ds_read_b64_tr_b16 a[18:19], v100 offset:512\n\t"
        "ds_read_b64_tr_b16 a[20:21], v100 offset:8192\n\t"
        "ds_read_b64_tr_b16 a[22:23], v100 offset:8704\n\t"
        "ds_read_b64_tr_b16 a[24:25], v101 offset:0\n\t"
        "ds_read_b64_tr_b16 a[26:27], v101 offset:512\n\t"
        "ds_read_b64_tr_b16 a[28:29], v101 offset:8192\n\t"
        "ds_read_b64_tr_b16 a[30:31], v101 offset:8704\n\t"
        "ds_read_b64_tr_b16 a[32:33], v102 offset:0\n\t"
        "ds_read_b64_tr_b16 a[34:35], v102 offset:512\n\t"
        "ds_read_b64_tr_b16 a[36:37], v102 offset:8192\n\t"
        "ds_read_b64_tr_b16 a[38:39], v102 offset:8704\n\t"
        "ds_read_b64_tr_b16 a[40:41], v103 offset:0\n\t"
        "ds_read_b64_tr_b16 a[42:43], v103 offset:512\n\t"
        "ds_read_b64_tr_b16 a[44:45], v103 offset:8192\n\t"
        "ds_read_b64_tr_b16 a[46:47], v103 offset:8704\n\t"
        "ds_read_b64_tr_b16 a[48:49], v104 offset:0\n\t"
        "ds_read_b64_tr_b16 a[50:51], v104 offset:512\n\t"
        "ds_read_b64_tr_b16 a[52:53], v104 offset:8192\n\t"
        "ds_read_b64_tr_b16 a[54:55], v104 offset:8704\n\t"
        "ds_read_b64_tr_b16 a[56:57], v105 offset:0\n\t"
        "ds_read_b64_tr_b16 a[58:59], v105 offset:512\n\t"
        "ds_read_b64_tr_b16 a[60:61], v105 offset:8192\n\t"
        "ds_read_b64_tr_b16 a[62:63], v105 offset:8704\n\t"
        "s_waitcnt lgkmcnt(0)\n\t"
        "s_barrier\n\t"
        "s_add_u32 s80, %[m0b], 0\n\t"
        "s_mov_b32 s81, 2097152\n\t"
        "s_mov_b32 s82, 4\n\t"
        ".Ld2%=:\n\t"
        "s_mov_b32 m0, s80\n\t"
        "s_add_u32 s80, s80, 0x8000\n\t"
        "buffer_load_dwordx4 v96, %[sq], s81 offen lds\n\t"
        "s_add_u32 m0, m0, 0x1000\n\t"
        "s_add_u32 s81, s81, 0x20000\n\t"
        "buffer_load_dwordx4 v96, %[sq], s81 offen lds\n\t"
        "s_add_u32 m0, m0, 0x1000\n\t"
        "s_add_u32 s81, s81, 0x20000\n\t"
        "buffer_load_dwordx4 v96, %[sq], s81 offen lds\n\t"
        "s_add_u32 m0, m0, 0x1000\n\t"
        "s_add_u32 s81, s81, 0x20000\n\t"
        "buffer_load_dwordx4 v96, %[sq], s81 offen lds\n\t"
        "s_add_u32 m0, m0, 0x1000\n\t"
        "s_add_u32 s81, s81, 0x20000\n\t"
        "s_sub_u32 s82, s82, 1\n\t"
        "s_cmp_lg_u32 s82, 0\n\t"
        "s_cbranch_scc1 .Ld2%=\n\t"
        "s_waitcnt vmcnt(16)\n\t"
        "s_barrier\n\t"
        "ds_read_b64_tr_b16 v[16:17], v98 offset:16384\n\t"
        "ds_read_b64_tr_b16 v[18:19], v98 offset:16896\n\t"
        "ds_read_b64_tr_b16 v[20:21], v98 offset:24576\n\t"
        "ds_read_b64_tr_b16 v[22:23], v98 offset:25088\n\t"
        "ds_read_b64_tr_b16 v[24:25], v99 offset:16384\n\t"
        "ds_read_b64_tr_b16 v[26:27], v99 offset:16896\n\t"
        "ds_read_b64_tr_b16 v[28:29], v99 offset:24576\n\t"
        "ds_read_b64_tr_b16 v[30:31], v99 offset:25088\n\t"
        "ds_read_b64_tr_b16 a[80:81], v100 offset:16384\n\t"
        "ds_read_b64_tr_b16 a[82:83], v100 offset:16896\n\t"
        "ds_read_b64_tr_b16 a[84:85], v100 offset:24576\n\t"
        "ds_read_b64_tr_b16 a[86:87], v100 offset:25088\n\t"
        "ds_read_b64_tr_b16 a[88:89], v101 offset:16384\n\t"
        "ds_read_b64_tr_b16 a[90:91], v101 offset:16896\n\t"
        "ds_read_b64_tr_b16 a[92:93], v101 offset:24576\n\t"
        "ds_read_b64_tr_b16 a[94:95], v101 offset:25088\n\t"
        "ds_read_b64_tr_b16 a[96:97], v102 offset:16384\n\t"
        "ds_read_b64_tr_b16 a[98:99], v102 offset:16896\n\t"
        "ds_read_b64_tr_b16 a[100:101], v102 offset:24576\n\t"
        "ds_read_b64_tr_b16 a[102:103], v102 offset:25088\n\t"
        "ds_read_b64_tr_b16 a[104:105], v103 offset:16384\n\t"
        "ds_read_b64_tr_b16 a[106:107], v103 offset:16896\n\t"
        "ds_read_b64_tr_b16 a[108:109], v103 offset:24576\n\t"
        "ds_read_b64_tr_b16 a[110:111], v103 offset:25088\n\t"
        "ds_read_b64_tr_b16 a[112:113], v104 offset:16384\n\t"
        "ds_read_b64_tr_b16 a[114:115], v104 offset:16896\n\t"
        "ds_read_b64_tr_b16 a[116:117], v104 offset:24576\n\t"
        "ds_read_b64_tr_b16 a[118:119], v104 offset:25088\n\t"
        "ds_read_b64_tr_b16 a[120:121], v105 offset:16384\n\t"
        "ds_read_b64_tr_b16 a[122:123], v105 offset:16896\n\t"
        "ds_read_b64_tr_b16 a[124:125], v105 offset:24576\n\t"
        "ds_read_b64_tr_b16 a[126:127], v105 offset:25088\n\t"
        "s_waitcnt lgkmcnt(0)\n\t"
        "s_barrier\n\t"
        "s_add_u32 s80, %[m0b], 16384\n\t"
        "s_mov_b32 s81, 2097152\n\t"
        "s_mov_b32 s82, 4\n\t"
        ".Ld3%=:\n\t"
        "s_mov_b32 m0, s80\n\t"
        "s_add_u32 s80, s80, 0x8000\n\t"
        "buffer_load_dwordx4 v96, %[sk], s81 offen lds\n\t"
        "s_add_u32 m0, m0, 0x1000\n\t"
        "s_add_u32 s81, s81, 0x20000\n\t"
        "buffer_load_dwordx4 v96, %[sk], s81 offen lds\n\t"
        "s_add_u32 m0, m0, 0x1000\n\t"
        "s_add_u32 s81, s81, 0x20000\n\t"
        "buffer_load_dwordx4 v96, %[sk], s81 offen lds\n\t"
        "s_add_u32 m0, m0, 0x1000\n\t"
        "s_add_u32 s81, s81, 0x20000\n\t"
        "buffer_load_dwordx4 v96, %[sk], s81 offen lds\n\t"
        "s_add_u32 m0, m0, 0x1000\n\t"
        "s_add_u32 s81, s81, 0x20000\n\t"
        "s_sub_u32 s82, s82, 1\n\t"
        "s_cmp_lg_u32 s82, 0\n\t"
        "s_cbranch_scc1 .Ld3%=\n\t"
        "s_waitcnt vmcnt(16)\n\t"
        "s_barrier\n\t"
        "ds_read_b64_tr_b16 v[32:33], v98 offset:0\n\t"
        "ds_read_b64_tr_b16 v[34:35], v98 offset:512\n\t"
        "ds_read_b64_tr_b16 v[36:37], v98 offset:8192\n\t"
        "ds_read_b64_tr_b16 v[38:39], v98 offset:8704\n\t"
        "ds_read_b64_tr_b16 v[40:41], v99 offset:0\n\t"
        "ds_read_b64_tr_b16 v[42:43], v99 offset:512\n\t"
        "ds_read_b64_tr_b16 v[44:45], v99 offset:8192\n\t"
        "ds_read_b64_tr_b16 v[46:47], v99 offset:8704\n\t"
        "ds_read_b64_tr_b16 a[144:145], v100 offset:0\n\t"
        "ds_read_b64_tr_b16 a[146:147], v100 offset:512\n\t"
        "ds_read_b64_tr_b16 a[148:149], v100 offset:8192\n\t"
        "ds_read_b64_tr_b16 a[150:151], v100 offset:8704\n\t"
        "ds_read_b64_tr_b16 a[152:153], v101 offset:0\n\t"
        "ds_read_b64_tr_b16 a[154:155], v101 offset:512\n\t"
        "ds_read_b64_tr_b16 a[156:157], v101 offset:8192\n\t"
        "ds_read_b64_tr_b16 a[158:159], v101 offset:8704\n\t"
        "ds_read_b64_tr_b16 a[160:161], v102 offset:0\n\t"
        "ds_read_b64_tr_b16 a[162:163], v102 offset:512\n\t"
        "ds_read_b64_tr_b16 a[164:165], v102 offset:8192\n\t"
        "ds_read_b64_tr_b16 a[166:167], v102 offset:8704\n\t"
        "ds_read_b64_tr_b16 a[168:169], v103 offset:0\n\t"
        "ds_read_b64_tr_b16 a[170:171], v103 offset:512\n\t"
        "ds_read_b64_tr_b16 a[172:173], v103 offset:8192\n\t"
        "ds_read_b64_tr_b16 a[174:175], v103 offset:8704\n\t"
        "ds_read_b64_tr_b16 a[176:177], v104 offset:0\n\t"
        "ds_read_b64_tr_b16 a[178:179], v104 offset:512\n\t"
        "ds_read_b64_tr_b16 a[180:181], v104 offset:8192\n\t"
        "ds_read_b64_tr_b16 a[182:183], v104 offset:8704\n\t"
        "ds_read_b64_tr_b16 a[184:185], v105 offset:0\n\t"
        "ds_read_b64_tr_b16 a[186:187], v105 offset:512\n\t"
        "ds_read_b64_tr_b16 a[188:189], v105 offset:8192\n\t"
        "ds_read_b64_tr_b16 a[190:191], v105 offset:8704\n\t"
        "s_waitcnt lgkmcnt(0)\n\t"
        "s_barrier\n\t"
        "s_add_u32 s80, %[m0b], 0\n\t"
        "s_mov_b32 s81, 4194304\n\t"
        "s_mov_b32 s82, 4\n\t"
        ".Ld4%=:\n\t"
        "s_mov_b32 m0, s80\n\t"
        "s_add_u32 s80, s80, 0x8000\n\t"
        "buffer_load_dwordx4 v96, %[sq], s81 offen lds\n\t"
        "s_add_u32 m0, m0, 0x1000\n\t"
        "s_add_u32 s81, s81, 0x20000\n\t"
        "buffer_load_dwordx4 v96, %[sq], s81 offen lds\n\t"
        "s_add_u32 m0, m0, 0x1000\n\t"
        "s_add_u32 s81, s81, 0x20000\n\t"
        "buffer_load_dwordx4 v96, %[sq], s81 offen lds\n\t"
        "s_add_u32 m0, m0, 0x1000\n\t"
        "s_add_u32 s81, s81, 0x20000\n\t"
        "buffer_load_dwordx4 v96, %[sq], s81 offen lds\n\t"
        "s_add_u32 m0, m0, 0x1000\n\t"
        "s_add_u32 s81, s81, 0x20000\n\t"
        "s_sub_u32 s82, s82, 1\n\t"
        "s_cmp_lg_u32 s82, 0\n\t"
        "s_cbranch_scc1 .Ld4%=\n\t"
        "s_waitcnt vmcnt(16)\n\t"
        "s_barrier\n\t"
        "ds_read_b64_tr_b16 v[48:49], v98 offset:16384\n\t"
        "ds_read_b64_tr_b16 v[50:51], v98 offset:16896\n\t"
        "ds_read_b64_tr_b16 v[52:53], v98 offset:24576\n\t"
        "ds_read_b64_tr_b16 v[54:55], v98 offset:25088\n\t"
        "ds_read_b64_tr_b16 v[56:57], v99 offset:16384\n\t"
        "ds_read_b64_tr_b16 v[58:59], v99 offset:16896\n\t"
        "ds_read_b64_tr_b16 v[60:61], v99 offset:24576\n\t"
        "ds_read_b64_tr_b16 v[62:63], v99 offset:25088\n\t"
        "ds_read_b64_tr_b16 a[208:209], v100 offset:16384\n\t"
        "ds_read_b64_tr_b16 a[210:211], v100 offset:16896\n\t"
        "ds_read_b64_tr_b16 a[212:213], v100 offset:24576\n\t"
        "ds_read_b64_tr_b16 a[214:215], v100 offset:25088\n\t"
        "ds_read_b64_tr_b16 a[216:217], v101 offset:16384\n\t"
        "ds_read_b64_tr_b16 a[218:219], v101 offset:16896\n\t"
        "ds_read_b64_tr_b16 a[220:221], v101 offset:24576\n\t"
        "ds_read_b64_tr_b16 a[222:223], v101 offset:25088\n\t"
        "ds_read_b64_tr_b16 a[224:225], v102 offset:16384\n\t"
        "ds_read_b64_tr_b16 a[226:227], v102 offset:16896\n\t"
        "ds_read_b64_tr_b16 a[228:229], v102 offset:24576\n\t"
        "ds_read_b64_tr_b16 a[230:231], v102 offset:25088\n\t"
        "ds_read_b64_tr_b16 a[232:233], v103 offset:16384\n\t"
        "ds_read_b64_tr_b16 a[234:235], v103 offset:16896\n\t"
        "ds_read_b64_tr_b16 a[236:237], v103 offset:24576\n\t"
        "ds_read_b64_tr_b16 a[238:239], v103 offset:25088\n\t"
        "ds_read_b64_tr_b16 a[240:241], v104 offset:16384\n\t"
        "ds_read_b64_tr_b16 a[242:243], v104 offset:16896\n\t"
        "ds_read_b64_tr_b16 a[244:245], v104 offset:24576\n\t"
        "ds_read_b64_tr_b16 a[246:247], v104 offset:25088\n\t"
        "ds_read_b64_tr_b16 a[248:249], v105 offset:16384\n\t"
        "ds_read_b64_tr_b16 a[250:251], v105 offset:16896\n\t"
        "ds_read_b64_tr_b16 a[252:253], v105 offset:24576\n\t"
        "ds_read_b64_tr_b16 a[254:255], v105 offset:25088\n\t"
        "s_waitcnt lgkmcnt(0)\n\t"
        "s_barrier\n\t"
        "s_add_u32 s80, %[m0b], 16384\n\t"
        "s_mov_b32 s81, 4194304\n\t"
        "s_mov_b32 s82, 4\n\t"
        ".Ld5%=:\n\t"
        "s_mov_b32 m0, s80\n\t"
        "s_add_u32 s80, s80, 0x8000\n\t"
        "buffer_load_dwordx4 v96, %[sk], s81 offen lds\n\t"
        "s_add_u32 m0, m0, 0x1000\n\t"
        "s_add_u32 s81, s81, 0x20000\n\t"
        "buffer_load_dwordx4 v96, %[sk], s81 offen lds\n\t"
        "s_add_u32 m0, m0, 0x1000\n\t"
        "s_add_u32 s81, s81, 0x20000\n\t"
        "buffer_load_dwordx4 v96, %[sk], s81 offen lds\n\t"
        "s_add_u32 m0, m0, 0x1000\n\t"
        "s_add_u32 s81, s81, 0x20000\n\t"
        "buffer_load_dwordx4 v96, %[sk], s81 offen lds\n\t"
        "s_add_u32 m0, m0, 0x1000\n\t"
        "s_add_u32 s81, s81, 0x20000\n\t"
        "s_sub_u32 s82, s82, 1\n\t"
        "s_cmp_lg_u32 s82, 0\n\t"
        "s_cbranch_scc1 .Ld5%=\n\t"
        "s_waitcnt vmcnt(16)\n\t"
        "s_barrier\n\t"
        "ds_read_b64_tr_b16 v[128:129], v98 offset:0\n\t"
        "ds_read_b64_tr_b16 v[130:131], v98 offset:512\n\t"
        "ds_read_b64_tr_b16 v[132:133], v98 offset:8192\n\t"
        "ds_read_b64_tr_b16 v[134:135], v98 offset:8704\n\t"
        "ds_read_b64_tr_b16 v[136:137], v99 offset:0\n\t"
        "ds_read_b64_tr_b16 v[138:139], v99 offset:512\n\t"
        "ds_read_b64_tr_b16 v[140:141], v99 offset:8192\n\t"
        "ds_read_b64_tr_b16 v[142:143], v99 offset:8704\n\t"
        "ds_read_b64_tr_b16 v[144:145], v100 offset:0\n\t"
        "ds_read_b64_tr_b16 v[146:147], v100 offset:512\n\t"
        "ds_read_b64_tr_b16 v[148:149], v100 offset:8192\n\t"
        "ds_read_b64_tr_b16 v[150:151], v100 offset:8704\n\t"
        "ds_read_b64_tr_b16 v[152:153], v101 offset:0\n\t"
        "ds_read_b64_tr_b16 v[154:155], v101 offset:512\n\t"
        "ds_read_b64_tr_b16 v[156:157], v101 offset:8192\n\t"
        "ds_read_b64_tr_b16 v[158:159], v101 offset:8704\n\t"
        "ds_read_b64_tr_b16 v[160:161], v102 offset:0\n\t"
        "ds_read_b64_tr_b16 v[162:163], v102 offset:512\n\t"
        "ds_read_b64_tr_b16 v[164:165], v102 offset:8192\n\t"
        "ds_read_b64_tr_b16 v[166:167], v102 offset:8704\n\t"
        "ds_read_b64_tr_b16 v[168:169], v103 offset:0\n\t"
        "ds_read_b64_tr_b16 v[170:171], v103 offset:512\n\t"
        "ds_read_b64_tr_b16 v[172:173], v103 offset:8192\n\t"
        "ds_read_b64_tr_b16 v[174:175], v103 offset:8704\n\t"
        "ds_read_b64_tr_b16 v[176:177], v104 offset:0\n\t"
        "ds_read_b64_tr_b16 v[178:179], v104 offset:512\n\t"
        "ds_read_b64_tr_b16 v[180:181], v104 offset:8192\n\t"
        "ds_read_b64_tr_b16 v[182:183], v104 offset:8704\n\t"
        "ds_read_b64_tr_b16 v[184:185], v105 offset:0\n\t"
        "ds_read_b64_tr_b16 v[186:187], v105 offset:512\n\t"
        "ds_read_b64_tr_b16 v[188:189], v105 offset:8192\n\t"
        "ds_read_b64_tr_b16 v[190:191], v105 offset:8704\n\t"
        "s_waitcnt lgkmcnt(0)\n\t"
        "s_barrier\n\t"
        "s_add_u32 s80, %[m0b], 0\n\t"
        "s_mov_b32 s81, 6291456\n\t"
        "s_mov_b32 s82, 4\n\t"
        ".Ld6%=:\n\t"
        "s_mov_b32 m0, s80\n\t"
        "s_add_u32 s80, s80, 0x8000\n\t"
        "buffer_load_dwordx4 v96, %[sq], s81 offen lds\n\t"
        "s_add_u32 m0, m0, 0x1000\n\t"
        "s_add_u32 s81, s81, 0x20000\n\t"
        "buffer_load_dwordx4 v96, %[sq], s81 offen lds\n\t"
        "s_add_u32 m0, m0, 0x1000\n\t"
        "s_add_u32 s81, s81, 0x20000\n\t"
        "buffer_load_dwordx4 v96, %[sq], s81 offen lds\n\t"
        "s_add_u32 m0, m0, 0x1000\n\t"
        "s_add_u32 s81, s81, 0x20000\n\t"
        "buffer_load_dwordx4 v96, %[sq], s81 offen lds\n\t"
        "s_add_u32 m0, m0, 0x1000\n\t"
        "s_add_u32 s81, s81, 0x20000\n\t"
        "s_sub_u32 s82, s82, 1\n\t"
        "s_cmp_lg_u32 s82, 0\n\t"
        "s_cbranch_scc1 .Ld6%=\n\t"
        "s_waitcnt vmcnt(16)\n\t"
        "s_barrier\n\t"
        "ds_read_b64_tr_b16 v[192:193], v98 offset:16384\n\t"
        "ds_read_b64_tr_b16 v[194:195], v98 offset:16896\n\t"
        "ds_read_b64_tr_b16 v[196:197], v98 offset:24576\n\t"
        "ds_read_b64_tr_b16 v[198:199], v98 offset:25088\n\t"
        "ds_read_b64_tr_b16 v[200:201], v99 offset:16384\n\t"
        "ds_read_b64_tr_b16 v[202:203], v99 offset:16896\n\t"
        "ds_read_b64_tr_b16 v[204:205], v99 offset:24576\n\t"
        "ds_read_b64_tr_b16 v[206:207], v99 offset:25088\n\t"
        "ds_read_b64_tr_b16 v[208:209], v100 offset:16384\n\t"
        "ds_read_b64_tr_b16 v[210:211], v100 offset:16896\n\t"
        "ds_read_b64_tr_b16 v[212:213], v100 offset:24576\n\t"
        "ds_read_b64_tr_b16 v[214:215], v100 offset:25088\n\t"
        "ds_read_b64_tr_b16 v[216:217], v101 offset:16384\n\t"
        "ds_read_b64_tr_b16 v[218:219], v101 offset:16896\n\t"
        "ds_read_b64_tr_b16 v[220:221], v101 offset:24576\n\t"
        "ds_read_b64_tr_b16 v[222:223], v101 offset:25088\n\t"
        "ds_read_b64_tr_b16 v[224:225], v102 offset:16384\n\t"
        "ds_read_b64_tr_b16 v[226:227], v102 offset:16896\n\t"
        "ds_read_b64_tr_b16 v[228:229], v102 offset:24576\n\t"
        "ds_read_b64_tr_b16 v[230:231], v102 offset:25088\n\t"
        "ds_read_b64_tr_b16 v[232:233], v103 offset:16384\n\t"
        "ds_read_b64_tr_b16 v[234:235], v103 offset:16896\n\t"
        "ds_read_b64_tr_b16 v[236:237], v103 offset:24576\n\t"
        "ds_read_b64_tr_b16 v[238:239], v103 offset:25088\n\t"
        "ds_read_b64_tr_b16 v[240:241], v104 offset:16384\n\t"
        "ds_read_b64_tr_b16 v[242:243], v104 offset:16896\n\t"
        "ds_read_b64_tr_b16 v[244:245], v104 offset:24576\n\t"
        "ds_read_b64_tr_b16 v[246:247], v104 offset:25088\n\t"
        "ds_read_b64_tr_b16 v[248:249], v105 offset:16384\n\t"
        "ds_read_b64_tr_b16 v[250:251], v105 offset:16896\n\t"
        "ds_read_b64_tr_b16 v[252:253], v105 offset:24576\n\t"
        "ds_read_b64_tr_b16 v[254:255], v105 offset:25088\n\t"
        "s_waitcnt lgkmcnt(0)\n\t"
        "s_barrier\n\t"
        "s_add_u32 s80, %[m0b], 16384\n\t"
        "s_mov_b32 s81, 6291456\n\t"
        "s_mov_b32 s82, 4\n\t"
        ".Ld7%=:\n\t"
        "s_mov_b32 m0, s80\n\t"
        "s_add_u32 s80, s80, 0x8000\n\t"
        "buffer_load_dwordx4 v96, %[sk], s81 offen lds\n\t"
        "s_add_u32 m0, m0, 0x1000\n\t"
        "s_add_u32 s81, s81, 0x20000\n\t"
        "buffer_load_dwordx4 v96, %[sk], s81 offen lds\n\t"
        "s_add_u32 m0, m0, 0x1000\n\t"
        "s_add_u32 s81, s81, 0x20000\n\t"
        "buffer_load_dwordx4 v96, %[sk], s81 offen lds\n\t"
        "s_add_u32 m0, m0, 0x1000\n\t"
        "s_add_u32 s81, s81, 0x20000\n\t"
        "buffer_load_dwordx4 v96, %[sk], s81 offen lds\n\t"
        "s_add_u32 m0, m0, 0x1000\n\t"
        "s_add_u32 s81, s81, 0x20000\n\t"
        "s_sub_u32 s82, s82, 1\n\t"
        "s_cmp_lg_u32 s82, 0\n\t"
        "s_cbranch_scc1 .Ld7%=\n\t"
        "s_waitcnt vmcnt(16)\n\t"
        "s_barrier\n\t"
        "ds_read_b64_tr_b16 a[0:1], v98 offset:0\n\t"
        "ds_read_b64_tr_b16 a[2:3], v98 offset:512\n\t"
        "ds_read_b64_tr_b16 a[4:5], v98 offset:8192\n\t"
        "ds_read_b64_tr_b16 a[6:7], v98 offset:8704\n\t"
        "ds_read_b64_tr_b16 a[8:9], v99 offset:0\n\t"
        "ds_read_b64_tr_b16 a[10:11], v99 offset:512\n\t"
        "ds_read_b64_tr_b16 a[12:13], v99 offset:8192\n\t"
        "ds_read_b64_tr_b16 a[14:15], v99 offset:8704\n\t"
        "ds_read_b64_tr_b16 v[64:65], v100 offset:0\n\t"
        "ds_read_b64_tr_b16 v[66:67], v100 offset:512\n\t"
        "ds_read_b64_tr_b16 v[68:69], v100 offset:8192\n\t"
        "ds_read_b64_tr_b16 v[70:71], v100 offset:8704\n\t"
        "ds_read_b64_tr_b16 v[72:73], v101 offset:0\n\t"
        "ds_read_b64_tr_b16 v[74:75], v101 offset:512\n\t"
        "ds_read_b64_tr_b16 v[76:77], v101 offset:8192\n\t"
        "ds_read_b64_tr_b16 v[78:79], v101 offset:8704\n\t"
        "ds_read_b64_tr_b16 v[80:81], v102 offset:0\n\t"
        "ds_read_b64_tr_b16 v[82:83], v102 offset:512\n\t"
        "ds_read_b64_tr_b16 v[84:85], v102 offset:8192\n\t"
        "ds_read_b64_tr_b16 v[86:87], v102 offset:8704\n\t"
        "ds_read_b64_tr_b16 v[88:89], v103 offset:0\n\t"
        "ds_read_b64_tr_b16 v[90:91], v103 offset:512\n\t"
        "ds_read_b64_tr_b16 v[92:93], v103 offset:8192\n\t"
        "ds_read_b64_tr_b16 v[94:95], v103 offset:8704\n\t"
        "ds_read_b64_tr_b16 a[128:129], v104 offset:0\n\t"
        "ds_read_b64_tr_b16 a[130:131], v104 offset:512\n\t"
        "ds_read_b64_tr_b16 a[132:133], v104 offset:8192\n\t"
        "ds_read_b64_tr_b16 a[134:135], v104 offset:8704\n\t"
        "ds_read_b64_tr_b16 a[136:137], v105 offset:0\n\t"
        "ds_read_b64_tr_b16 a[138:139], v105 offset:512\n\t"
        "ds_read_b64_tr_b16 a[140:141], v105 offset:8192\n\t"
        "ds_read_b64_tr_b16 a[142:143], v105 offset:8704\n\t"
        "s_waitcnt lgkmcnt(0)\n\t"
        "s_barrier\n\t"
        "ds_write_b128 v121, v[0:3] offset:0\n\t"
        "ds_write_b128 v121, v[4:7] offset:1024\n\t"
        "ds_write_b128 v121, v[8:11] offset:2048\n\t"
        "ds_write_b128 v121, v[12:15] offset:3072\n\t"
        "ds_write_b128 v121, v[16:19] offset:4096\n\t"
        "ds_write_b128 v121, v[20:23] offset:5120\n\t"
        "ds_write_b128 v121, v[24:27] offset:6144\n\t"
        "ds_write_b128 v121, v[28:31] offset:7168\n\t"
        "ds_write_b128 v121, v[32:35] offset:8192\n\t"
        "ds_write_b128 v121, v[36:39] offset:9216\n\t"
        "ds_write_b128 v121, v[40:43] offset:10240\n\t"
        "ds_write_b128 v121, v[44:47] offset:11264\n\t"
        "ds_write_b128 v121, v[48:51] offset:12288\n\t"
        "ds_write_b128 v121, v[52:55] offset:13312\n\t"
        "ds_write_b128 v121, v[56:59] offset:14336\n\t"
        "ds_write_b128 v121, v[60:63] offset:15360\n\t"
        "s_waitcnt lgkmcnt(0)\n\t"
        "s_mov_b32 s64, %[kf]\n\t"
        "s_mov_b32 s36, 0xf0000000\n\t"
        "buffer_load_dwordx4 v[116:119], v120, %[wsd], %[cal] offen sc1\n\t"
        "buffer_load_dwordx2 v[114:115], off, %[wsd], s64 sc1\n\t"
        "s_waitcnt vmcnt(2)\n\t"
        "s_barrier\n\t"
        "ds_read_b64_tr_b16 a[64:65], v98 offset:16384\n\t"
        "ds_read_b64_tr_b16 a[66:67], v98 offset:16896\n\t"
        "ds_read_b64_tr_b16 a[68:69], v98 offset:24576\n\t"
        "ds_read_b64_tr_b16 a[70:71], v98 offset:25088\n\t"
        "ds_read_b64_tr_b16 a[72:73], v99 offset:16384\n\t"
        "ds_read_b64_tr_b16 a[74:75], v99 offset:16896\n\t"
        "ds_read_b64_tr_b16 a[76:77], v99 offset:24576\n\t"
        "ds_read_b64_tr_b16 a[78:79], v99 offset:25088\n\t"
        "ds_read_b64_tr_b16 v[0:1], v100 offset:16384\n\t"
        "ds_read_b64_tr_b16 v[2:3], v100 offset:16896\n\t"
        "ds_read_b64_tr_b16 v[4:5], v100 offset:24576\n\t"
        "ds_read_b64_tr_b16 v[6:7], v100 offset:25088\n\t"
        "ds_read_b64_tr_b16 v[8:9], v101 offset:16384\n\t"
        "ds_read_b64_tr_b16 v[10:11], v101 offset:16896\n\t"
        "ds_read_b64_tr_b16 v[12:13], v101 offset:24576\n\t"
        "ds_read_b64_tr_b16 v[14:15], v101 offset:25088\n\t"
        "ds_read_b64_tr_b16 v[16:17], v102 offset:16384\n\t"
        "ds_read_b64_tr_b16 v[18:19], v102 offset:16896\n\t"
        "ds_read_b64_tr_b16 v[20:21], v102 offset:24576\n\t"
        "ds_read_b64_tr_b16 v[22:23], v102 offset:25088\n\t"
        "ds_read_b64_tr_b16 v[24:25], v103 offset:16384\n\t"
        "ds_read_b64_tr_b16 v[26:27], v103 offset:16896\n\t"
        "ds_read_b64_tr_b16 v[28:29], v103 offset:24576\n\t"
        "ds_read_b64_tr_b16 v[30:31], v103 offset:25088\n\t"
        "ds_read_b64_tr_b16 a[192:193], v104 offset:16384\n\t"
        "ds_read_b64_tr_b16 a[194:195], v104 offset:16896\n\t"
        "ds_read_b64_tr_b16 a[196:197], v104 offset:24576\n\t"
        "ds_read_b64_tr_b16 a[198:199], v104 offset:25088\n\t"
        "ds_read_b64_tr_b16 a[200:201], v105 offset:16384\n\t"
        "ds_read_b64_tr_b16 a[202:203], v105 offset:16896\n\t"
        "ds_read_b64_tr_b16 a[204:205], v105 offset:24576\n\t"
        "ds_read_b64_tr_b16 a[206:207], v105 offset:25088\n\t"
        "s_waitcnt vmcnt(0)\n\t"
        "s_mov_b32 s97, 0\n\t"
        ".Lg0%=:\n\t"
        "v_cmp_ne_u32_e32 vcc, %[tag], v116\n\t"
        "v_cmp_ne_u32_e64 s[82:83], %[tag], v118\n\t"
        "s_nop 1\n\t"
        "s_or_b64 vcc, vcc, s[82:83]\n\t"
        "s_cbranch_scc0 .Lg1%=\n\t"
        "s_sleep 4\n\t"
        "buffer_load_dwordx4 v[116:119], v120, %[wsd], %[cal] offen sc1\n\t"
        "s_add_u32 s97, s97, 1\n\t"
        "s_cmp_lt_u32 s97, 0x100000\n\t"
        "s_cbranch_scc0 .Lgu%=\n\t"
        "s_waitcnt vmcnt(0)\n\t"
        "s_branch .Lg0%=\n\t"
        ".Lg1%=:\n\t"
        "v_cmp_ne_u32_e32 vcc, 0, v117\n\t"
        "v_cmp_ne_u32_e64 s[82:83], 0, v119\n\t"
        "s_mov_b32 s84, 0x55555555\n\t"
        "s_mov_b32 s85, s84\n\t"
        "s_and_b64 s[82:83], s[82:83], s[84:85]\n\t"
        "s_bcnt1_i32_b64 s87, s[82:83]\n\t"
        "s_and_b64 s[82:83], vcc, s[84:85]\n\t"
        "s_bcnt1_i32_b64 s86, s[82:83]\n\t"
        "s_andn2_b64 s[82:83], vcc, s[84:85]\n\t"
        "s_bcnt1_i32_b64 s88, s[82:83]\n\t"
        "s_add_u32 s80, s86, s87\n\t"
        "s_add_u32 s80, s80, s88\n\t"
        "s_cmp_ge_u32 s80, 48\n\t"
        "s_cselect_b32 s80, 8, 24\n\t"
        "s_cmp_gt_u32 s86, s80\n\t"
        "s_cselect_b32 s86, %c[NX1], %c[NX0]\n\t"
        "s_cmp_gt_u32 s87, s80\n\t"
        "s_cselect_b32 s87, %c[NX1], %c[NX0]\n\t"
        "s_cmp_gt_u32 s88, s80\n\t"
        "s_cselect_b32 s88, %c[NX1], %c[NX0]\n\t"
        "s_add_u32 %[nA], %[nA], s86\n\t"
        "s_add_u32 %[nS], %[nS], s86\n\t"
        "s_add_u32 %[nW], %[nW], s86\n\t"
        "s_add_u32 %[nC], %[nC], s87\n\t"
        "s_add_u32 %[b0], %[b0], s88\n\t"
        "s_add_u32 %[b1], %[b1], s88\n\t"
        "s_cmp_eq_u32 %[w], 1\n\t"
        "s_cselect_b32 s80, s87, s86\n\t"
        "s_add_u32 %[pob], %[pob], s80\n\t"
        "s_mov_b32 s36, 0xf0000000\n\t"
        "s_mov_b32 s38, 0xff00ff00\n\t"
        "s_mov_b32 s39, 0xff00ff00\n\t"
        "s_mov_b32 s40, 0\n\t"
        "s_mov_b32 s37, 0\n\t"
        "s_brev_b32 s54, 1\n\t"
        "s_mov_b32 s46, %c[iD1]\n\t"
        "s_mov_b32 s45, %c[iD1]\n\t"
        "s_mov_b32 s47, %c[iD2]\n\t"
        "s_mov_b32 s48, 1\n\t"
        "s_mov_b32 s50, %c[LC]\n\t"
        "s_mov_b32 s51, %c[LA]\n\t"
        "s_mov_b32 s52, %c[E1]\n\t"
        "s_mov_b32 s53, %c[E2]\n\t"
        "s_mov_b32 s43, 0\n\t"
        "s_mov_b32 s61, %[b0]\n\t"
        "s_mov_b32 s62, %[b1]\n\t"
        "s_mov_b32 s75, 31744\n\t"
        "s_cmp_eq_u32 s64, s36\n\t"
        "s_cbranch_scc1 .Lk10%=\n\t"
        "s_mov_b32 s97, 0\n\t"
        ".Lk00%=:\n\t"
        "v_readfirstlane_b32 s82, v114\n\t"
        "v_readfirstlane_b32 s83, v115\n\t"
        "s_xor_b32 s82, s82, %[tag]\n\t"
        "s_xor_b32 s83, s83, %[tag]\n\t"
        "s_or_b32 s82, s82, s83\n\t"
        "s_cbranch_scc0 .Lk10%=\n\t"
        "s_sleep 3\n\t"
        "buffer_load_dwordx2 v[114:115], off, %[wsd], s64 sc1\n\t"
        "s_add_u32 s97, s97, 1\n\t"
        "s_cmp_lt_u32 s97, 0x200000\n\t"
        "s_cbranch_scc0 .Lgu%=\n\t"
        "s_waitcnt vmcnt(0)\n\t"
        "s_branch .Lk00%=\n\t"
        ".Lk10%=:\n\t"
        "s_add_u32 s80, s40, 0\n\t"
        "s_min_u32 s80, s80, 63\n\t"
        "s_lshl_b32 s80, s80, 18\n\t"
        "s_add_u32 s69, s80, %[kx]\n\t"
        "s_add_u32 s70, s69, 0x1000\n\t"
        "s_lshl_b32 s80, s40, 18\n\t"
        "s_add_u32 s71, s80, %[vx]\n\t"
        "s_add_u32 s80, s40, 1\n\t"
        "s_lshl_b32 s81, s80, 9\n\t"
        "s_add_u32 s81, s81, %[kf]\n\t"
        "s_and_b32 s80, s80, 0x43\n\t"
        "s_cselect_b32 s64, s36, s81\n\t"
        "buffer_load_dwordx4 v[104:107], v120, %[wsd], s69 offen sc1\n\t"
        "buffer_load_dwordx4 v[108:111], v120, %[wsd], s70 offen sc1\n\t"
        "s_waitcnt lgkmcnt(0)\n\t"
        "s_barrier\n\t"
        "ds_write_b128 v121, v[64:67] offset:16384\n\t"
        "ds_write_b128 v121, v[68:71] offset:17408\n\t"
        "ds_write_b128 v121, v[72:75] offset:18432\n\t"
        "ds_write_b128 v121, v[76:79] offset:19456\n\t"
        "ds_write_b128 v121, v[80:83] offset:20480\n\t"
        "ds_write_b128 v121, v[84:87] offset:21504\n\t"
        "ds_write_b128 v121, v[88:91] offset:22528\n\t"
        "ds_write_b128 v121, v[92:95] offset:23552\n\t"
        "ds_write_b128 v121, v[0:3] offset:24576\n\t"
        "ds_write_b128 v121, v[4:7] offset:25600\n\t"
        "ds_write_b128 v121, v[8:11] offset:26624\n\t"
        "ds_write_b128 v121, v[12:15] offset:27648\n\t"
        "ds_write_b128 v121, v[16:19] offset:28672\n\t"
        "ds_write_b128 v121, v[20:23] offset:29696\n\t"
        "ds_write_b128 v121, v[24:27] offset:30720\n\t"
        "ds_write_b128 v121, v[28:31] offset:31744\n\t"
        "s_waitcnt lgkmcnt(0)\n\t"
        "rec_s_code:\n\t"
        "ds_read_b128 v[16:19], v121 offset:0\n\t"
        "ds_read_b128 v[20:23], v121 offset:1024\n\t"
        "ds_read_b128 v[24:27], v121 offset:2048\n\t"
        "ds_read_b128 v[28:31], v121 offset:3072\n\t"
        "ds_read_b128 v[32:35], v121 offset:4096\n\t"
        "ds_read_b128 v[36:39], v121 offset:5120\n\t"
        "ds_read_b128 v[40:43], v121 offset:6144\n\t"
        "ds_read_b128 v[44:47], v121 offset:7168\n\t"
        "ds_read_b128 v[56:59], v121 offset:8192\n\t"
        "ds_read_b128 v[60:63], v121 offset:9216\n\t"
        "ds_read_b128 v[64:67], v121 offset:10240\n\t"
        "ds_read_b128 v[68:71], v121 offset:11264\n\t"
        "ds_read_b128 v[72:75], v121 offset:12288\n\t"
        "ds_read_b128 v[76:79], v121 offset:13312\n\t"
        "ds_read_b128 v[80:83], v121 offset:14336\n\t"
        "ds_read_b128 v[84:87], v121 offset:15360\n\t"
        "s_and_b32 s80, s40, 3\n\t"
        "s_lshl_b32 s80, s80, 17\n\t"
        "s_add_u32 s57, s80, %[nA]\n\t"
        "s_add_u32 s58, s57, 0x1000\n\t"
        "s_add_u32 s59, s57, 0x4000\n\t"
        "s_add_u32 s60, s57, 0x5000\n\t"
        "s_and_b32 s80, s40, 3\n\t"
        "s_lshl_b32 s80, s80, 17\n\t"
        "s_add_u32 s63, s80, %[nC]\n\t"
        "s_add_u32 s66, s80, %[nS]\n\t"
        "s_add_u32 s81, s80, 0x20000\n\t"
        "s_and_b32 s81, s81, 0x60000\n\t"
        "s_add_u32 s67, s81, %[nW]\n\t"
        "s_cmp_eq_u32 %[w], 2\n\t"
        "s_cselect_b32 s81, s80, s81\n\t"
        "s_xor_b32 s81, s81, 0x40000\n\t"
        "s_add_u32 s68, s81, %[pob]\n\t"
        "s_lshl_b32 s72, s40, 17\n\t"
        "s_add_u32 s72, s72, %[kvo]\n\t"
        "s_add_u32 s80, s40, 1\n\t"
        "s_min_u32 s80, s80, 63\n\t"
        "s_lshl_b32 s80, s80, 18\n\t"
        "s_add_u32 s69, s80, %[kx]\n\t"
        "s_add_u32 s70, s69, 0x1000\n\t"
        "s_lshl_b32 s80, s40, 18\n\t"
        "s_add_u32 s71, s80, %[vx]\n\t"
        "s_add_u32 s80, s40, 2\n\t"
        "s_lshl_b32 s81, s80, 9\n\t"
        "s_add_u32 s81, s81, %[kf]\n\t"
        "s_and_b32 s80, s80, 0x43\n\t"
        "s_cselect_b32 s64, s36, s81\n\t"
        "v_mov_b32_e32 v88, 0\n\t"
        "v_mov_b32_e32 v89, 0\n\t"
        "v_mov_b32_e32 v90, 0\n\t"
        "v_mov_b32_e32 v91, 0\n\t"
        "v_mov_b32_e32 v92, 0\n\t"
        "v_mov_b32_e32 v93, 0\n\t"
        "v_mov_b32_e32 v94, 0\n\t"
        "v_mov_b32_e32 v95, 0\n\t"
        "v_mov_b32_e32 v96, 0\n\t"
        "v_mov_b32_e32 v97, 0\n\t"
        "v_mov_b32_e32 v98, 0\n\t"
        "v_mov_b32_e32 v99, 0\n\t"
        "v_mov_b32_e32 v100, 0\n\t"
        "v_mov_b32_e32 v101, 0\n\t"
        "v_mov_b32_e32 v102, 0\n\t"
        "v_mov_b32_e32 v103, 0\n\t"
        "v_mov_b32_e32 v52, v112\n\t"
        "v_mov_b32_e32 v53, v113\n\t"
        "v_mov_b32_e32 v49, 0\n\t"
        "v_lshlrev_b32_e32 v51, 6, v120\n\t"
        "s_add_u32 s80, %[nW], %[swo]\n\t"
        "s_cmp_eq_u32 %[w], 0\n\t"
        "s_cselect_b32 s73, s80, s36\n\t"
        "s_cselect_b32 s74, %[pra], s36\n\t"
        "s_waitcnt lgkmcnt(0)\n\t"
        "s_barrier\n\t"
        "s_memrealtime s[98:99]\n\t"
        "s_waitcnt lgkmcnt(0)\n\t"
        "s_mov_b32 s41, s98\n\t"
        "v_mov_b32_e32 v48, s98\n\t"
        ".p2align 6\n\t"
        ".Lloop%=:\n\t"
        "s_mov_b32 s97, 0\n\t"
        "s_movk_i32 s96, 0x1000\n\t"
        ".Lwa%=:\n\t"
        "s_memrealtime s[98:99]\n\t"
        "s_addk_i32 s96, -1\n\t"
        "s_cmp_lt_i32 s96, 0\n\t"
        "s_cbranch_scc1 .Lta%=\n\t"
        "s_waitcnt lgkmcnt(0)\n\t"
        "s_sub_i32 s98, s98, s41\n\t"
        "s_cmp_lt_i32 s98, %c[iFT]\n\t"
        "s_cbranch_scc1 .Lwa%=\n\t"
        "s_cmp_ge_i32 s98, 0\n\t"
        "s_cbranch_scc1 .Lxa%=\n\t"
        ".Lqa%=:\n\t"
        "s_nop %c[iFN]\n\t"
        "s_addk_i32 s98, 1\n\t"
        "s_cmp_lt_i32 s98, 0\n\t"
        "s_cbranch_scc1 .Lqa%=\n\t"
        ".Lxa%=:\n\t"
        "buffer_load_dwordx4 v[0:3], v120, %[srd], s57 offen sc1\n\t"
        "s_nop 7\n\t"
        "buffer_load_dwordx4 v[4:7], v120, %[srd], s58 offen sc1\n\t"
        "s_nop 7\n\t"
        "buffer_load_dwordx4 v[8:11], v120, %[srd], s59 offen sc1\n\t"
        "s_nop 7\n\t"
        "buffer_load_dwordx4 v[12:15], v120, %[srd], s60 offen sc1\n\t"
        "s_nop 15\n\t"
        "s_nop 15\n\t"
        "s_nop 3\n\t"
        "s_mov_b64 exec, 7\n\t"
        "buffer_store_dwordx2 v[48:49], v51, %[srd], s73 offen sc1\n\t"
        "s_mov_b64 exec, -1\n\t"
        "buffer_load_dwordx4 v[112:115], v120, %[wsd], s71 offen offset:0 sc1\n\t"
        "buffer_load_dwordx4 v[116:119], v120, %[wsd], s71 offen offset:1024 sc1\n\t"
        ".Lgo%=:\n\t"
        "s_waitcnt vmcnt(6)\n\t"
        "v_smfmac_f32_16x16x64_bf16 v[88:91], v[0:3], v[16:23], v122\n\t"
        "ds_write_b128 v120, v[0:3] offset:0\n\t"
        "v_smfmac_f32_16x16x64_bf16 v[92:95], v[0:3], v[24:31], v122\n\t"
        "v_smfmac_f32_16x16x64_bf16 v[96:99], v[0:3], v[32:39], v122\n\t"
        "v_smfmac_f32_16x16x64_bf16 v[100:103], v[0:3], v[40:47], v122\n\t"
        "s_waitcnt vmcnt(5)\n\t"
        "v_smfmac_f32_16x16x64_bf16 v[88:91], v[4:7], v[56:63], v122\n\t"
        "ds_write_b128 v120, v[4:7] offset:4096\n\t"
        "v_smfmac_f32_16x16x64_bf16 v[92:95], v[4:7], v[64:71], v122\n\t"
        "v_smfmac_f32_16x16x64_bf16 v[96:99], v[4:7], v[72:79], v122\n\t"
        "v_smfmac_f32_16x16x64_bf16 v[100:103], v[4:7], v[80:87], v122\n\t"
        "s_waitcnt vmcnt(4)\n\t"
        "v_smfmac_f32_16x16x64_bf16 v[88:91], v[8:11], v[128:135], v122\n\t"
        "ds_write_b128 v120, v[8:11] offset:8192\n\t"
        "v_smfmac_f32_16x16x64_bf16 v[92:95], v[8:11], v[136:143], v122\n\t"
        "s_waitcnt lgkmcnt(1)\n\t"
        "s_barrier\n\t"
        "v_smfmac_f32_16x16x64_bf16 v[96:99], v[8:11], v[192:199], v122\n\t"
        "ds_read_b128 v[16:19], v124 offset:0\n\t"
        "v_smfmac_f32_16x16x64_bf16 v[100:103], v[8:11], v[200:207], v122\n\t"
        "ds_read_b128 v[20:23], v125 offset:0\n\t"
        "s_waitcnt vmcnt(3)\n\t"
        "v_smfmac_f32_16x16x64_bf16 v[88:91], v[12:15], a[0:7], v122\n\t"
        "ds_write_b128 v120, v[12:15] offset:12288\n\t"
        "v_smfmac_f32_16x16x64_bf16 v[92:95], v[12:15], a[8:15], v122\n\t"
        "v_smfmac_f32_16x16x64_bf16 v[96:99], v[12:15], a[64:71], v122\n\t"
        "ds_read_b128 v[24:27], v126 offset:0\n\t"
        "v_smfmac_f32_16x16x64_bf16 v[100:103], v[12:15], a[72:79], v122\n\t"
        "ds_read_b128 v[28:31], v124 offset:4096\n\t"
        "s_waitcnt lgkmcnt(4)\n\t"
        "v_smfmac_f32_16x16x64_bf16 v[88:91], v[16:19], a[16:23], v122\n\t"
        "ds_read_b128 v[32:35], v125 offset:4096\n\t"
        "v_smfmac_f32_16x16x64_bf16 v[92:95], v[16:19], a[24:31], v122\n\t"
        "ds_read_b128 v[36:39], v126 offset:4096\n\t"
        "s_waitcnt lgkmcnt(5)\n\t"
        "v_smfmac_f32_16x16x64_bf16 v[88:91], v[20:23], a[32:39], v122\n\t"
        "ds_read_b128 v[72:75], v121 offset:16384\n\t"
        "v_smfmac_f32_16x16x64_bf16 v[92:95], v[20:23], a[40:47], v122\n\t"
        "s_waitcnt lgkmcnt(5)\n\t"
        "s_barrier\n\t"
        "ds_read_b128 v[76:79], v121 offset:17408\n\t"
        "s_waitcnt lgkmcnt(5)\n\t"
        "v_smfmac_f32_16x16x64_bf16 v[88:91], v[24:27], a[48:55], v122\n\t"
        "ds_read_b128 v[0:3], v124 offset:12288\n\t"
        "v_smfmac_f32_16x16x64_bf16 v[92:95], v[24:27], a[56:63], v122\n\t"
        "ds_read_b128 v[80:83], v121 offset:18432\n\t"
        "s_waitcnt lgkmcnt(6)\n\t"
        "v_smfmac_f32_16x16x64_bf16 v[88:91], v[28:31], a[144:151], v122\n\t"
        "ds_read_b128 v[84:87], v121 offset:19456\n\t"
        "v_smfmac_f32_16x16x64_bf16 v[92:95], v[28:31], a[152:159], v122\n\t"
        "ds_read_b128 v[40:43], v124 offset:8192\n\t"
        "s_waitcnt lgkmcnt(7)\n\t"
        "v_smfmac_f32_16x16x64_bf16 v[88:91], v[32:35], a[160:167], v122\n\t"
        "ds_read_b128 v[44:47], v125 offset:8192\n\t"
        "v_mov_b32_e32 v12, 0x3f803f80\n\t"
        "v_smfmac_f32_16x16x64_bf16 v[92:95], v[32:35], a[168:175], v122\n\t"
        "ds_read_b128 v[56:59], v121 offset:20480\n\t"
        "v_mov_b32_e32 v13, 0x3f803f80\n\t"
        "s_waitcnt lgkmcnt(8)\n\t"
        "v_smfmac_f32_16x16x64_bf16 v[88:91], v[36:39], a[176:183], v122\n\t"
        "ds_read_b128 v[60:63], v121 offset:21504\n\t"
        "v_mov_b32_e32 v14, 0x3f803f80\n\t"
        "v_smfmac_f32_16x16x64_bf16 v[92:95], v[36:39], a[184:191], v122\n\t"
        "ds_read_b128 v[4:7], v125 offset:12288\n\t"
        "v_mov_b32_e32 v15, 0x3f803f80\n\t"
        "s_waitcnt lgkmcnt(8)\n\t"
        "s_waitcnt lgkmcnt(7)\n\t"
        "v_smfmac_f32_16x16x64_bf16 v[88:91], v[0:3], v[72:79], v122\n\t"
        "ds_read_b128 v[64:67], v121 offset:22528\n\t"
        "s_waitcnt lgkmcnt(6)\n\t"
        "v_smfmac_f32_16x16x64_bf16 v[92:95], v[0:3], v[80:87], v122\n\t"
        "ds_read_b128 v[68:71], v121 offset:23552\n\t"
        "s_waitcnt lgkmcnt(6)\n\t"
        "v_smfmac_f32_16x16x64_bf16 v[88:91], v[40:43], v[144:151], v122\n\t"
        "ds_read_b128 v[48:51], v126 offset:8192\n\t"
        "v_smfmac_f32_16x16x64_bf16 v[92:95], v[40:43], v[152:159], v122\n\t"
        "ds_read_b128 v[8:11], v126 offset:12288\n\t"
        "s_waitcnt lgkmcnt(7)\n\t"
        "v_smfmac_f32_16x16x64_bf16 v[88:91], v[44:47], v[160:167], v122\n\t"
        "ds_read_b128 v[72:75], v121 offset:24576\n\t"
        "v_smfmac_f32_16x16x64_bf16 v[92:95], v[44:47], v[168:175], v122\n\t"
        "ds_read_b128 v[76:79], v121 offset:25600\n\t"
        "s_waitcnt lgkmcnt(7)\n\t"
        "s_waitcnt lgkmcnt(6)\n\t"
        "v_smfmac_f32_16x16x64_bf16 v[88:91], v[4:7], v[56:63], v122\n\t"
        "ds_read_b128 v[80:83], v121 offset:26624\n\t"
        "s_waitcnt lgkmcnt(5)\n\t"
        "v_smfmac_f32_16x16x64_bf16 v[92:95], v[4:7], v[64:71], v122\n\t"
        "ds_read_b128 v[84:87], v121 offset:27648\n\t"
        "s_waitcnt lgkmcnt(5)\n\t"
        "v_smfmac_f32_16x16x64_bf16 v[88:91], v[48:51], v[176:183], v122\n\t"
        "v_mov_b32_e32 v64, 0\n\t"
        "v_smfmac_f32_16x16x64_bf16 v[92:95], v[48:51], v[184:191], v122\n\t"
        "v_mov_b32_e32 v65, 0\n\t"
        "s_waitcnt lgkmcnt(4)\n\t"
        "v_smfmac_f32_16x16x64_bf16 v[88:91], v[8:11], a[128:135], v122\n\t"
        "v_mov_b32_e32 v66, 0\n\t"
        "v_smfmac_f32_16x16x64_bf16 v[92:95], v[8:11], a[136:143], v122\n\t"
        "v_mov_b32_e32 v67, 0\n\t"
        "s_waitcnt lgkmcnt(2)\n\t"
        "v_smfmac_f32_16x16x64_bf16 v[96:99], v[0:3], v[72:79], v122\n\t"
        "ds_read_b128 v[72:75], v121 offset:28672\n\t"
        "s_waitcnt lgkmcnt(1)\n\t"
        "v_smfmac_f32_16x16x64_bf16 v[100:103], v[0:3], v[80:87], v122\n\t"
        "ds_read_b128 v[76:79], v121 offset:29696\n\t"
        "v_smfmac_f32_16x16x64_bf16 v[96:99], v[16:19], a[80:87], v122\n\t"
        "v_add_f32_e32 v88, v88, v90\n\t"
        "v_add_f32_e32 v89, v89, v91\n\t"
        "v_smfmac_f32_16x16x64_bf16 v[100:103], v[16:19], a[88:95], v122\n\t"
        "v_cvt_pk_bf16_f32 v90, v88, v89\n\t"
        "v_add_f32_e32 v92, v92, v94\n\t"
        "v_smfmac_f32_16x16x64_bf16 v[96:99], v[20:23], a[96:103], v122\n\t"
        "ds_write_b32 v127, v90\n\t"
        "v_add_f32_e32 v93, v93, v95\n\t"
        "v_cvt_pk_bf16_f32 v91, v92, v93\n\t"
        "v_smfmac_f32_16x16x64_bf16 v[100:103], v[20:23], a[104:111], v122\n\t"
        "ds_write_b32 v127, v91 offset:384\n\t"
        "v_cmp_u_f32_e32 vcc, v88, v89\n\t"
        "v_smfmac_f32_16x16x64_bf16 v[96:99], v[24:27], a[112:119], v122\n\t"
        "ds_read_b64_tr_b16 v[56:57], v52 offset:0\n\t"
        "ds_read_b64_tr_b16 v[58:59], v52 offset:64\n\t"
        "v_smfmac_f32_16x16x64_bf16 v[100:103], v[24:27], a[120:127], v122\n\t"
        "ds_read_b64_tr_b16 v[60:61], v53 offset:0\n\t"
        "ds_read_b64_tr_b16 v[62:63], v53 offset:64\n\t"
        "v_smfmac_f32_16x16x64_bf16 v[96:99], v[28:31], a[208:215], v122\n\t"
        "s_cbranch_vccnz .Lf1%=\n\t"
        ".Lr1%=:\n\t"
        "ds_read_b128 v[80:83], v121 offset:30720\n\t"
        "v_smfmac_f32_16x16x64_bf16 v[100:103], v[28:31], a[216:223], v122\n\t"
        "ds_read_b128 v[84:87], v121 offset:31744\n\t"
        "v_mov_b32_e32 v91, 0\n\t"
        "v_smfmac_f32_16x16x64_bf16 v[96:99], v[32:35], a[224:231], v122\n\t"
        "v_mov_b32_e32 v94, 0\n\t"
        "v_smfmac_f32_16x16x64_bf16 v[100:103], v[32:35], a[232:239], v122\n\t"
        "ds_read_b128 v[32:35], v120 offset:20480\n\t"
        "v_smfmac_f32_16x16x64_bf16 v[96:99], v[36:39], a[240:247], v122\n\t"
        "s_waitcnt lgkmcnt(3)\n\t"
        "s_barrier\n\t"
        "v_mfma_f32_16x16x32_bf16 v[68:71], v[104:107], v[56:59], 0\n\t"
        "ds_read_b64_tr_b16 v[16:17], v123 offset:0\n\t"
        "v_mfma_f32_16x16x32_bf16 v[68:71], v[108:111], v[60:63], v[68:71]\n\t"
        "ds_read_b64_tr_b16 v[18:19], v123 offset:64\n\t"
        "v_smfmac_f32_16x16x64_bf16 v[100:103], v[36:39], a[248:255], v122\n\t"
        "ds_read_b64_tr_b16 v[20:21], v123 offset:768\n\t"
        "v_smfmac_f32_16x16x64_bf16 v[96:99], v[8:11], a[192:199], v122\n\t"
        "ds_read_b64_tr_b16 v[22:23], v123 offset:832\n\t"
        "v_smfmac_f32_16x16x64_bf16 v[100:103], v[8:11], a[200:207], v122\n\t"
        "s_nop 1\n\t"
        "ds_write_b128 v120, v[68:71] offset:16384\n\t"
        "v_smfmac_f32_16x16x64_bf16 v[96:99], v[4:7], v[72:79], v122\n\t"
        "buffer_load_dword v88, v127, %[srd], s61 offen offset:0 sc1\n\t"
        "s_waitcnt lgkmcnt(6)\n\t"
        "v_smfmac_f32_16x16x64_bf16 v[100:103], v[4:7], v[80:87], v122\n\t"
        "buffer_load_dword v89, v127, %[srd], s61 offen offset:256 sc1\n\t"
        "v_smfmac_f32_16x16x64_bf16 v[96:99], v[40:43], v[208:215], v122\n\t"
        "ds_read_b64_tr_b16 v[24:25], v123 offset:1536\n\t"
        "v_smfmac_f32_16x16x64_bf16 v[100:103], v[40:43], v[216:223], v122\n\t"
        "ds_read_b64_tr_b16 v[26:27], v123 offset:1600\n\t"
        "v_smfmac_f32_16x16x64_bf16 v[96:99], v[44:47], v[224:231], v122\n\t"
        "s_waitcnt lgkmcnt(2)\n\t"
        "s_barrier\n\t"
        "ds_read_b128 v[36:39], v124 offset:16384\n\t"
        "v_smfmac_f32_16x16x64_bf16 v[100:103], v[44:47], v[232:239], v122\n\t"
        "ds_read_b128 v[56:59], v125 offset:16384\n\t"
        "v_smfmac_f32_16x16x64_bf16 v[96:99], v[48:51], v[240:247], v122\n\t"
        "ds_read_b128 v[60:63], v126 offset:16384\n\t"
        "v_smfmac_f32_16x16x64_bf16 v[100:103], v[48:51], v[248:255], v122\n\t"
        "ds_read_b64_tr_b16 v[28:29], v123 offset:2304\n\t"
        "ds_read_b64_tr_b16 v[30:31], v123 offset:2368\n\t"
        "buffer_load_dwordx4 v[84:87], v120, %[srd], s66 offen sc1\n\t"
        "s_waitcnt lgkmcnt(4)\n\t"
        "s_cmp_eq_u32 s40, 63\n\t"
        "v_add_f32_e32 v68, v68, v36\n\t"
        "v_add_f32_e32 v69, v69, v37\n\t"
        "v_add_f32_e32 v70, v70, v38\n\t"
        "v_add_f32_e32 v71, v71, v39\n\t"
        "v_add_f32_e32 v96, v96, v98\n\t"
        "v_add_f32_e32 v97, v97, v99\n\t"
        "s_waitcnt lgkmcnt(2)\n\t"
        "v_cvt_pk_bf16_f32 v50, v96, v97\n\t"
        "v_pk_add_f32 v[100:101], v[100:101], v[102:103]\n\t"
        "ds_write_b32 v35, v50\n\t"
        "v_cvt_pk_bf16_f32 v51, v100, v101\n\t"
        "ds_write_b32 v35, v51 offset:384\n\t"
        "v_pk_add_f32 v[56:57], v[56:57], v[60:61]\n\t"
        "s_cbranch_scc1 .Lend%=\n\t"
        "v_pk_add_f32 v[58:59], v[58:59], v[62:63]\n\t"
        "v_pk_add_f32 v[68:69], v[68:69], v[56:57]\n\t"
        "v_pk_add_f32 v[70:71], v[70:71], v[58:59]\n\t"
        "v_max3_f32 v2, v68, v69, v70\n\t"
        "v_max_f32_e32 v2, v2, v71\n\t"
        "s_nop 1\n\t"
        "v_max_f32_dpp v3, v2, v2 row_ror:8 row_mask:0xf bank_mask:0xf\n\t"
        "v_max_f32_dpp v1, v2, v2 row_ror:8 row_mask:0xf bank_mask:0xf\n\t"
        "s_nop 1\n\t"
        "v_permlane16_swap_b32_e32 v3, v1\n\t"
        "v_max_f32_e32 v2, v3, v1\n\t"
        "v_max_f32_e32 v3, v3, v1\n\t"
        "s_waitcnt lgkmcnt(0)\n\t"
        "s_barrier\n\t"
        "ds_read_b64_tr_b16 v[72:73], v32 offset:0\n\t"
        "v_permlane32_swap_b32_e32 v2, v3\n\t"
        "ds_read_b64_tr_b16 v[74:75], v32 offset:128\n\t"
        "v_max_f32_e32 v0, v2, v3\n\t"
        "ds_read_b64_tr_b16 v[76:77], v32 offset:1536\n\t"
        "v_mul_f32_e32 v2, 0xbe0293ee, v0\n\t"
        "ds_read_b64_tr_b16 v[78:79], v32 offset:1664\n\t"
        "v_fmamk_f32 v68, v68, 0x3e0293ee, v2\n\t"
        "v_fmamk_f32 v69, v69, 0x3e0293ee, v2\n\t"
        "v_fmamk_f32 v70, v70, 0x3e0293ee, v2\n\t"
        "v_fmamk_f32 v71, v71, 0x3e0293ee, v2\n\t"
        "v_exp_f32_e32 v68, v68\n\t"
        "v_exp_f32_e32 v69, v69\n\t"
        "v_exp_f32_e32 v70, v70\n\t"
        "s_waitcnt lgkmcnt(2)\n\t"
        "v_smfmac_f32_16x16x64_bf16 v[64:67], v[72:75], v[16:23], v122\n\t"
        "s_waitcnt lgkmcnt(0)\n\t"
        "v_smfmac_f32_16x16x64_bf16 v[64:67], v[76:79], v[24:31], v122\n\t"
        "s_waitcnt vmcnt(3)\n\t"
        "v_cvt_pk_bf16_f32 v36, v68, v69\n\t"
        "v_exp_f32_e32 v71, v71\n\t"
        "s_nop 0\n\t"
        "v_mov_b32_dpp v38, v36 row_ror:8 row_mask:0xf bank_mask:0xf\n\t"
        "v_cvt_pk_bf16_f32 v37, v70, v71\n\t"
        "s_nop 1\n\t"
        "v_mov_b32_dpp v39, v37 row_ror:8 row_mask:0xf bank_mask:0xf\n\t"
        "v_pk_add_f32 v[64:65], v[64:65], v[66:67]\n\t"
        "v_max3_f32 v3, v0, v64, v65\n\t"
        "v_max3_f32 v1, v0, v64, v65\n\t"
        "v_mfma_f32_16x16x32_bf16 v[40:43], v[112:115], v[36:39], 0\n\t"
        "s_nop 0\n\t"
        "v_permlane16_swap_b32_e32 v3, v1\n\t"
        "v_max_f32_e32 v2, v3, v1\n\t"
        "v_max_f32_e32 v3, v3, v1\n\t"
        "v_mfma_f32_16x16x32_bf16 v[44:47], v[116:119], v[36:39], 0\n\t"
        "s_nop 0\n\t"
        "v_permlane32_swap_b32_e32 v2, v3\n\t"
        "v_max_f32_e32 v2, v2, v3\n\t"
        "v_mfma_f32_16x16x32_bf16 v[96:99], v[12:15], v[36:39], 0\n\t"
        "v_mul_f32_e32 v2, 0xbe0293ee, v2\n\t"
        "v_fmamk_f32 v10, v0, 0x3e0293ee, v2\n\t"
        "v_fmamk_f32 v64, v64, 0x3e0293ee, v2\n\t"
        "v_exp_f32_e32 v10, v10\n\t"
        "v_fmamk_f32 v65, v65, 0x3e0293ee, v2\n\t"
        "v_exp_f32_e32 v64, v64\n\t"
        "s_nop 1\n\t"
        "v_mul_f32_e32 v96, v96, v10\n\t"
        "v_exp_f32_e32 v65, v65\n\t"
        "v_pk_mul_f32 v[40:41], v[40:41], v[10:11] op_sel_hi:[1,0]\n\t"
        "v_cvt_pk_bf16_f32 v90, v64, v65\n\t"
        "v_pk_mul_f32 v[42:43], v[42:43], v[10:11] op_sel_hi:[1,0]\n\t"
        "v_cvt_pk_bf16_f32 v95, v64, v65\n\t"
        "v_pk_mul_f32 v[44:45], v[44:45], v[10:11] op_sel_hi:[1,0]\n\t"
        "v_mfma_f32_16x16x16_bf16 v[96:99], v[12:13], v[90:91], v[96:99]\n\t"
        "s_waitcnt vmcnt(1)\n\t"
        "v_mul_f32_e32 v46, v46, v10\n\t"
        "v_mul_f32_e32 v47, v47, v10\n\t"
        "v_mfma_f32_16x16x16_bf16 v[40:43], v[88:89], v[90:91], v[40:43]\n\t"
        "v_max_u32_e32 v54, v88, v89\n\t"
        "v_cmp_eq_u32_e32 vcc, -1, v54\n\t"
        "v_mfma_f32_16x16x16_bf16 v[44:47], v[88:89], v[94:95], v[44:47]\n\t"
        "s_nop 0\n\t"
        "v_rcp_f32_e32 v2, v96\n\t"
        "s_nop 0\n\t"
        "s_cbranch_vccnz .Lfb%=\n\t"
        ".Lrb%=:\n\t"
        "s_nop 0\n\t"
        "v_pk_mul_f32 v[40:41], v[40:41], v[2:3] op_sel_hi:[1,0]\n\t"
        "v_pk_mul_f32 v[42:43], v[42:43], v[2:3] op_sel_hi:[1,0]\n\t"
        "v_cvt_pk_bf16_f32 v100, v40, v41\n\t"
        "v_pk_mul_f32 v[44:45], v[44:45], v[2:3] op_sel_hi:[1,0]\n\t"
        "v_pk_mul_f32 v[46:47], v[46:47], v[2:3] op_sel_hi:[1,0]\n\t"
        "v_cvt_pk_bf16_f32 v101, v42, v43\n\t"
        "v_cvt_pk_bf16_f32 v102, v44, v45\n\t"
        "v_min_u32_e32 v100, -2, v100\n\t"
        "v_cvt_pk_bf16_f32 v103, v46, v47\n\t"
        "v_min_u32_e32 v102, -2, v102\n\t"
        "buffer_store_dwordx4 v[100:103], v33, %[srd], s63 offen sc1\n\t"
        "s_memrealtime s[76:77]\n\t"
        "v_mov_b32_e32 v8, -1\n\t"
        "v_mov_b32_e32 v9, -1\n\t"
        "v_mov_b32_e32 v10, -1\n\t"
        "v_mov_b32_e32 v11, -1\n\t"
        "v_permlane16_swap_b32_e32 v50, v51\n\t"
        "v_permlane16_swap_b32_e32 v88, v89\n\t"
        "v_mov_b32_e32 v14, s40\n\t"
        "s_waitcnt lgkmcnt(0)\n\t"
        "s_waitcnt vmcnt(1)\n\t"
        "s_mov_b32 s97, 0\n\t"
        ".Lsr%=:\n\t"
        "s_mov_b32 exec_hi, 0xffff\n\t"
        "v_subrev_u32_e32 v0, s41, v84\n\t"
        "v_subrev_u32_e32 v1, s41, v86\n\t"
        "v_max_i32_e32 v0, v0, v1\n\t"
        "v_max_u32_e32 v1, v85, v87\n\t"
        "v_cmp_eq_u32_e32 vcc, -1, v1\n\t"
        "v_max_i32_dpp v0, v0, v0 quad_perm:[1,0,3,2] row_mask:0xf bank_mask:0xf\n\t"
        "v_or_b32_e32 v2, v85, v87\n\t"
        "s_nop 0\n\t"
        "v_max_i32_dpp v0, v0, v0 quad_perm:[2,3,0,1] row_mask:0xf bank_mask:0xf\n\t"
        "s_nop 1\n\t"
        "v_max_i32_dpp v0, v0, v0 row_ror:4 row_mask:0xf bank_mask:0xf\n\t"
        "s_nop 1\n\t"
        "v_max_i32_dpp v0, v0, v0 row_ror:8 row_mask:0xf bank_mask:0xf\n\t"
        "s_cbranch_vccnz .Lsf%=\n\t"
        "v_readlane_b32 s85, v0, 0\n\t"
        "v_readlane_b32 s81, v0, 16\n\t"
        "v_readlane_b32 s56, v0, 32\n\t"
        "v_and_b32_e32 v1, 1, v2\n\t"
        "v_cmp_ne_u32_e64 s[86:87], 0, v1\n\t"
        "v_and_b32_e32 v1, 2, v2\n\t"
        "v_cmp_ne_u32_e64 s[88:89], 0, v1\n\t"
        "v_and_b32_e32 v1, 4, v2\n\t"
        "v_cmp_ne_u32_e64 s[90:91], 0, v1\n\t"
        "s_max_i32 s56, s56, s81\n\t"
        "s_add_u32 s92, s85, s41\n\t"
        "s_sub_u32 s92, s92, s43\n\t"
        "s_add_i32 s93, s56, s47\n\t"
        "s_mov_b32 s44, s46\n\t"
        "s_mov_b32 s46, s45\n\t"
        "s_mov_b32 s94, s48\n\t"
        "s_mov_b32 s43, s41\n\t"
        "s_cmp_gt_i32 s56, s54\n\t"
        "s_cbranch_scc1 .Lmv%=\n\t"
        "s_mov_b32 s37, 0\n\t"
        "s_mov_b32 s48, 0\n\t"
        "s_mov_b32 s95, 0\n\t"
        ".Lsd%=:\n\t"
        "s_mov_b32 exec_hi, -1\n\t"
        "s_add_u32 s42, s43, s46\n\t"
        "s_sleep %c[HSLP]\n\t"
        "buffer_load_dwordx2 v[48:49], off, %[wsd], s64 sc1\n\t"
        "buffer_load_dwordx4 v[104:107], v120, %[wsd], s69 offen sc1\n\t"
        "buffer_load_dwordx4 v[108:111], v120, %[wsd], s70 offen sc1\n\t"
        "buffer_store_dwordx2 v[50:51], v34, %[wsd], s72 offen sc1\n\t"
        "buffer_store_dwordx2 v[88:89], v34, %[wsd], s72 offen offset:2048 sc1\n\t"
        "s_lshl_b32 s80, %[w], 8\n\t"
        "s_add_u32 s80, s80, s61\n\t"
        "s_add_u32 s80, s80, 24576\n\t"
        "buffer_store_dwordx2 v[8:9], v34, %[srd], s80 offen sc1\n\t"
        "s_cmp_eq_u32 %[w], 0\n\t"
        "s_cselect_b32 exec_hi, 0, -1\n\t"
        "buffer_store_dwordx4 v[8:11], v120, %[srd], s68 offen sc1\n\t"
        "s_mov_b32 exec_hi, -1\n\t"
        "s_mov_b64 exec, 1\n\t"
        "buffer_store_dword v14, off, %[wsd], s74 sc1\n\t"
        "s_mov_b64 exec, -1\n\t"
        "s_cmp_ge_u32 s40, %c[iCT0]\n\t"
        "s_cselect_b32 s85, 1, 0\n\t"
        "s_cmp_lg_u64 s[86:87], 0\n\t"
        "s_cselect_b32 s86, s85, 0\n\t"
        "s_cmp_lg_u64 s[88:89], 0\n\t"
        "s_cselect_b32 s87, s85, 0\n\t"
        "s_cmp_lg_u64 s[90:91], 0\n\t"
        "s_cselect_b32 s88, s85, 0\n\t"
        "s_or_b32 s80, s94, s86\n\t"
        "s_or_b32 s80, s80, s87\n\t"
        "s_andn2_b32 s90, s85, s80\n\t"
        "s_cmp_le_i32 s92, s44\n\t"
        "s_cselect_b32 s80, s85, 0\n\t"
        "s_andn2_b32 s91, s80, s88\n\t"
        "s_and_b32 s80, s40, %c[LDEC]\n\t"
        "s_cselect_b32 s89, 0, 1\n\t"
        "s_mov_b32 s80, s88\n\t"
        "s_add_i32 s81, s92, s50\n\t"
        "s_sub_i32 s81, s81, 4\n\t"
        "s_cmp_le_i32 s81, s44\n\t"
        "s_cselect_b32 s80, s80, 0\n\t"
        "s_cmp_lt_i32 s50, %c[LC24]\n\t"
        "s_cselect_b32 s80, s80, 0\n\t"
        "s_lshl2_add_u32 s50, s80, s50\n\t"
        "s_sub_i32 s50, s50, s89\n\t"
        "s_max_i32 s50, s50, %c[LC]\n\t"
        "s_lshl_b32 s80, s92, 1\n\t"
        "s_sub_i32 s81, s80, s52\n\t"
        "s_ashr_i32 s82, s81, %c[UPSH]\n\t"
        "s_max_i32 s82, s82, %c[TRUP]\n\t"
        "s_add_i32 s82, s82, s52\n\t"
        "s_min_i32 s82, s82, s80\n\t"
        "s_ashr_i32 s83, s81, 1\n\t"
        "s_min_i32 s83, s83, -%c[TRDN]\n\t"
        "s_add_i32 s83, s83, s52\n\t"
        "s_max_i32 s83, s83, s80\n\t"
        "s_cmp_gt_i32 s81, 0\n\t"
        "s_cselect_b32 s82, s82, s83\n\t"
        "s_cmp_lg_u32 s90, 0\n\t"
        "s_cselect_b32 s52, s82, s52\n\t"
        "s_add_i32 s80, s52, 1\n\t"
        "s_ashr_i32 s80, s80, 1\n\t"
        "s_add_i32 s45, s80, s50\n\t"
        "s_andn2_b32 s80, s86, s94\n\t"
        "s_cmp_lt_i32 s51, %c[LA24]\n\t"
        "s_cselect_b32 s80, s80, 0\n\t"
        "s_lshl2_add_u32 s51, s80, s51\n\t"
        "s_sub_i32 s51, s51, s89\n\t"
        "s_max_i32 s51, s51, %c[LA]\n\t"
        "s_lshl_b32 s80, s93, 1\n\t"
        "s_sub_i32 s81, s80, s53\n\t"
        "s_ashr_i32 s82, s81, %c[UPSH]\n\t"
        "s_max_i32 s82, s82, %c[TRUP]\n\t"
        "s_add_i32 s82, s82, s53\n\t"
        "s_min_i32 s82, s82, s80\n\t"
        "s_ashr_i32 s83, s81, 1\n\t"
        "s_min_i32 s83, s83, -%c[TRDN]\n\t"
        "s_add_i32 s83, s83, s53\n\t"
        "s_max_i32 s83, s83, s80\n\t"
        "s_cmp_gt_i32 s81, 0\n\t"
        "s_cselect_b32 s82, s82, s83\n\t"
        "s_cmp_lg_u32 s91, 0\n\t"
        "s_cselect_b32 s53, s82, s53\n\t"
        "s_add_i32 s80, s53, 1\n\t"
        "s_ashr_i32 s80, s80, 1\n\t"
        "s_add_i32 s47, s80, s51\n\t"
        "s_add_u32 s81, s47, %c[AUP]\n\t"
        "s_min_i32 s80, s95, s81\n\t"
        "s_max_i32 s47, s47, s80\n\t"
        "s_add_u32 s41, s42, s47\n\t"
        "s_add_u32 s40, s40, 1\n\t"
        "v_mov_b32_e32 v88, 0\n\t"
        "v_mov_b32_e32 v89, 0\n\t"
        "v_mov_b32_e32 v90, 0\n\t"
        "v_mov_b32_e32 v91, 0\n\t"
        "v_mov_b32_e32 v92, 0\n\t"
        "v_mov_b32_e32 v93, 0\n\t"
        "v_mov_b32_e32 v94, 0\n\t"
        "v_mov_b32_e32 v95, 0\n\t"
        "v_mov_b32_e32 v96, 0\n\t"
        "v_mov_b32_e32 v97, 0\n\t"
        "v_mov_b32_e32 v98, 0\n\t"
        "v_mov_b32_e32 v99, 0\n\t"
        "v_mov_b32_e32 v100, 0\n\t"
        "v_mov_b32_e32 v101, 0\n\t"
        "v_mov_b32_e32 v102, 0\n\t"
        "v_mov_b32_e32 v103, 0\n\t"
        "ds_read_b128 v[16:19], v121 offset:0\n\t"
        "ds_read_b128 v[20:23], v121 offset:1024\n\t"
        "ds_read_b128 v[24:27], v121 offset:2048\n\t"
        "ds_read_b128 v[28:31], v121 offset:3072\n\t"
        "ds_read_b128 v[32:35], v121 offset:4096\n\t"
        "ds_read_b128 v[36:39], v121 offset:5120\n\t"
        "ds_read_b128 v[40:43], v121 offset:6144\n\t"
        "ds_read_b128 v[44:47], v121 offset:7168\n\t"
        "ds_read_b128 v[56:59], v121 offset:8192\n\t"
        "ds_read_b128 v[60:63], v121 offset:9216\n\t"
        "ds_read_b128 v[64:67], v121 offset:10240\n\t"
        "ds_read_b128 v[68:71], v121 offset:11264\n\t"
        "ds_read_b128 v[72:75], v121 offset:12288\n\t"
        "ds_read_b128 v[76:79], v121 offset:13312\n\t"
        "ds_read_b128 v[80:83], v121 offset:14336\n\t"
        "ds_read_b128 v[84:87], v121 offset:15360\n\t"
        "v_mov_b32_e32 v6, 30720\n\t"
        "ds_read_b64 v[6:7], v6\n\t"
        "s_waitcnt lgkmcnt(0)\n\t"
        "v_readfirstlane_b32 s80, v6\n\t"
        "v_readfirstlane_b32 s81, v7\n\t"
        "s_cmp_eq_u32 s80, s40\n\t"
        "s_cselect_b32 s84, 1, 0\n\t"
        "s_cmp_eq_u32 s81, s40\n\t"
        "s_cselect_b32 s81, 2, 0\n\t"
        "s_or_b32 s84, s84, s81\n\t"
        "s_waitcnt vmcnt(7)\n\t"
        "s_cmp_eq_u32 s64, s36\n\t"
        "s_cbranch_scc1 .Lk11%=\n\t"
        "s_mov_b32 s97, 0\n\t"
        ".Lk01%=:\n\t"
        "v_readfirstlane_b32 s82, v48\n\t"
        "v_readfirstlane_b32 s83, v49\n\t"
        "s_xor_b32 s82, s82, %[tag]\n\t"
        "s_xor_b32 s83, s83, %[tag]\n\t"
        "s_or_b32 s82, s82, s83\n\t"
        "s_cbranch_scc0 .Lk11%=\n\t"
        "s_sleep 3\n\t"
        "buffer_load_dwordx2 v[48:49], off, %[wsd], s64 sc1\n\t"
        "s_add_u32 s97, s97, 1\n\t"
        "s_cmp_lt_u32 s97, 0x200000\n\t"
        "s_cbranch_scc0 .Lgu%=\n\t"
        "s_waitcnt vmcnt(0)\n\t"
        "s_branch .Lk01%=\n\t"
        ".Lk11%=:\n\t"
        "v_mov_b32_e32 v48, s76\n\t"
        "v_mov_b32_e32 v49, s84\n\t"
        "s_mov_b32 s85, s67\n\t"
        "s_and_b32 s80, s40, 3\n\t"
        "s_lshl_b32 s80, s80, 17\n\t"
        "s_add_u32 s57, s80, %[nA]\n\t"
        "s_add_u32 s58, s57, 0x1000\n\t"
        "s_add_u32 s59, s57, 0x4000\n\t"
        "s_add_u32 s60, s57, 0x5000\n\t"
        "s_and_b32 s80, s40, 3\n\t"
        "s_lshl_b32 s80, s80, 17\n\t"
        "s_add_u32 s63, s80, %[nC]\n\t"
        "s_add_u32 s66, s80, %[nS]\n\t"
        "s_add_u32 s81, s80, 0x20000\n\t"
        "s_and_b32 s81, s81, 0x60000\n\t"
        "s_add_u32 s67, s81, %[nW]\n\t"
        "s_cmp_eq_u32 %[w], 2\n\t"
        "s_cselect_b32 s81, s80, s81\n\t"
        "s_xor_b32 s81, s81, 0x40000\n\t"
        "s_add_u32 s68, s81, %[pob]\n\t"
        "s_lshl_b32 s72, s40, 17\n\t"
        "s_add_u32 s72, s72, %[kvo]\n\t"
        "v_lshlrev_b32_e32 v51, 6, v120\n\t"
        "s_add_u32 s80, s85, %[swo]\n\t"
        "s_cmp_eq_u32 %[w], 0\n\t"
        "s_cselect_b32 s73, s80, s36\n\t"
        "s_cselect_b32 s74, %[pra], s36\n\t"
        "s_add_u32 s80, s40, 1\n\t"
        "s_min_u32 s80, s80, 63\n\t"
        "s_lshl_b32 s80, s80, 18\n\t"
        "s_add_u32 s69, s80, %[kx]\n\t"
        "s_add_u32 s70, s69, 0x1000\n\t"
        "s_lshl_b32 s80, s40, 18\n\t"
        "s_add_u32 s71, s80, %[vx]\n\t"
        "s_add_u32 s80, s40, 2\n\t"
        "s_lshl_b32 s81, s80, 9\n\t"
        "s_add_u32 s81, s81, %[kf]\n\t"
        "s_and_b32 s80, s80, 0x43\n\t"
        "s_cselect_b32 s64, s36, s81\n\t"
        "s_mov_b32 s80, s61\n\t"
        "s_mov_b32 s61, s62\n\t"
        "s_mov_b32 s62, s80\n\t"
        "s_waitcnt lgkmcnt(0)\n\t"
        "s_branch .Lloop%=\n\t"
        ".Lmv%=:\n\t"
        "s_mov_b32 s48, 1\n\t"
        "s_cmp_eq_u32 s54, %c[iTOL]\n\t"
        "s_cbranch_scc1 .Lm1%=\n\t"
        "s_max_i32 s80, s85, s56\n\t"
        "s_add_u32 s43, s41, s80\n\t"
        "s_add_u32 s43, s43, %c[iPEN0]\n\t"
        "s_mov_b32 s54, %c[iTOL]\n\t"
        "s_add_u32 s46, s46, %c[iX0C]\n\t"
        "s_add_u32 s95, s47, %c[iX0A]\n\t"
        "v_and_b32_e32 v0, 3, v84\n\t"
        "v_and_b32_e32 v1, 3, v86\n\t"
        "v_lshlrev_b32_e64 v0, v0, 1\n\t"
        "v_lshlrev_b32_e64 v1, v1, 1\n\t"
        "v_or_b32_e32 v0, v0, v1\n\t"
        "s_mov_b32 s80, 0\n\t"
        "v_and_b32_e32 v1, 1, v0\n\t"
        "v_cmp_ne_u32_e32 vcc, 0, v1\n\t"
        "s_nop 1\n\t"
        "s_cmp_lg_u64 vcc, 0\n\t"
        "s_cselect_b32 s81, 2, 0\n\t"
        "s_or_b32 s80, s80, s81\n\t"
        "v_and_b32_e32 v1, 2, v0\n\t"
        "v_cmp_ne_u32_e32 vcc, 0, v1\n\t"
        "s_nop 1\n\t"
        "s_cmp_lg_u64 vcc, 0\n\t"
        "s_cselect_b32 s81, 4, 0\n\t"
        "s_or_b32 s80, s80, s81\n\t"
        "v_and_b32_e32 v1, 4, v0\n\t"
        "v_cmp_ne_u32_e32 vcc, 0, v1\n\t"
        "s_nop 1\n\t"
        "s_cmp_lg_u64 vcc, 0\n\t"
        "s_cselect_b32 s81, 8, 0\n\t"
        "s_or_b32 s80, s80, s81\n\t"
        "v_and_b32_e32 v1, 8, v0\n\t"
        "v_cmp_ne_u32_e32 vcc, 0, v1\n\t"
        "s_nop 1\n\t"
        "s_cmp_lg_u64 vcc, 0\n\t"
        "s_cselect_b32 s81, 16, 0\n\t"
        "s_or_b32 s80, s80, s81\n\t"
        "s_lshr_b32 s80, 0x1adf1210, s80\n\t"
        "s_and_b32 s55, s80, 3\n\t"
        "s_branch .Lal%=\n\t"
        ".Lm1%=:\n\t"
        "s_add_u32 s80, s37, %c[iPEN2]\n\t"
        "s_min_i32 s80, s80, %c[iPENM]\n\t"
        "s_cmp_lg_u32 s37, 0\n\t"
        "s_cselect_b32 s37, s80, %c[iPEN]\n\t"
        "s_add_u32 s80, s56, s37\n\t"
        "s_max_i32 s80, s80, %c[iPMIN]\n\t"
        "s_add_u32 s43, s41, s80\n\t"
        "s_add_u32 s95, s93, %c[LA]\n\t"
        "s_add_u32 s46, s46, %c[iXR]\n\t"
        ".Lal%=:\n\t"
        "s_sub_u32 s80, s55, s43\n\t"
        "s_and_b32 s80, s80, %c[iPHM]\n\t"
        "s_add_u32 s43, s43, s80\n\t"
        "s_branch .Lsd%=\n\t"
        ".Lsf%=:\n\t"
        "s_mov_b32 exec_hi, -1\n\t"
        "s_sleep 2\n\t"
        "buffer_load_dwordx4 v[84:87], v120, %[srd], s66 offen sc1\n\t"
        "s_add_u32 s97, s97, 1\n\t"
        "s_cmp_lt_u32 s97, 0x100000\n\t"
        "s_cbranch_scc0 .Lgu%=\n\t"
        "s_waitcnt vmcnt(0)\n\t"
        "s_branch .Lsr%=\n\t"
        ".Lf1%=:\n\t"
        "ds_read_b128 v[92:95], v120 offset:0\n\t"
        "ds_read_b128 v[68:71], v120 offset:4096\n\t"
        "v_max_u32_e32 v54, v16, v18\n\t"
        "v_max3_u32 v54, v54, v17, v19\n\t"
        "v_max3_u32 v54, v54, v20, v22\n\t"
        "v_max3_u32 v54, v54, v21, v23\n\t"
        "v_max3_u32 v54, v54, v24, v26\n\t"
        "v_max3_u32 v54, v54, v25, v27\n\t"
        "v_max3_u32 v54, v54, v28, v30\n\t"
        "v_max3_u32 v54, v54, v29, v31\n\t"
        "v_max3_u32 v54, v54, v32, v34\n\t"
        "v_max3_u32 v54, v54, v33, v35\n\t"
        "v_max3_u32 v54, v54, v36, v38\n\t"
        "v_max3_u32 v54, v54, v37, v39\n\t"
        "v_max3_u32 v54, v54, v40, v42\n\t"
        "v_max3_u32 v54, v54, v41, v43\n\t"
        "v_max3_u32 v54, v54, v44, v46\n\t"
        "v_max3_u32 v54, v54, v45, v47\n\t"
        "v_max3_u32 v54, v54, v48, v50\n\t"
        "v_max3_u32 v54, v54, v49, v51\n\t"
        "v_max3_u32 v54, v54, v0, v2\n\t"
        "v_max3_u32 v54, v54, v1, v3\n\t"
        "v_max3_u32 v54, v54, v4, v6\n\t"
        "v_max3_u32 v54, v54, v5, v7\n\t"
        "v_max3_u32 v54, v54, v8, v10\n\t"
        "v_max3_u32 v54, v54, v9, v11\n\t"
        "s_waitcnt lgkmcnt(0)\n\t"
        "v_max3_u32 v54, v54, v92, v94\n\t"
        "v_max3_u32 v54, v54, v93, v95\n\t"
        "v_max3_u32 v54, v54, v68, v70\n\t"
        "v_max3_u32 v54, v54, v69, v71\n\t"
        "ds_read_b128 v[92:95], v120 offset:8192\n\t"
        "ds_read_b128 v[68:71], v120 offset:12288\n\t"
        "s_waitcnt lgkmcnt(0)\n\t"
        "v_max3_u32 v54, v54, v92, v94\n\t"
        "v_max3_u32 v54, v54, v93, v95\n\t"
        "v_max3_u32 v54, v54, v68, v70\n\t"
        "v_max3_u32 v54, v54, v69, v71\n\t"
        "v_cmp_eq_u32_e32 vcc, -1, v54\n\t"
        "s_nop 1\n\t"
        "v_mov_b32_e32 v55, v55\n\t"
        "s_nop 0\n\t"
        "s_cbranch_vccz .Lr1%=\n\t"
        "buffer_load_dwordx4 v[0:3], v120, %[srd], s57 offen sc1\n\t"
        "buffer_load_dwordx4 v[4:7], v120, %[srd], s58 offen sc1\n\t"
        "buffer_load_dwordx4 v[8:11], v120, %[srd], s59 offen sc1\n\t"
        "buffer_load_dwordx4 v[12:15], v120, %[srd], s60 offen sc1\n\t"
        "s_add_u32 s80, s40, 1\n\t"
        "v_mov_b32_e32 v54, 30720\n\t"
        "v_mov_b32_e32 v55, s80\n\t"
        "ds_write_b32 v54, v55\n\t"
        "ds_read_b128 v[16:19], v121 offset:0\n\t"
        "ds_read_b128 v[20:23], v121 offset:1024\n\t"
        "ds_read_b128 v[24:27], v121 offset:2048\n\t"
        "ds_read_b128 v[28:31], v121 offset:3072\n\t"
        "ds_read_b128 v[32:35], v121 offset:4096\n\t"
        "ds_read_b128 v[36:39], v121 offset:5120\n\t"
        "ds_read_b128 v[40:43], v121 offset:6144\n\t"
        "ds_read_b128 v[44:47], v121 offset:7168\n\t"
        "ds_read_b128 v[56:59], v121 offset:8192\n\t"
        "ds_read_b128 v[60:63], v121 offset:9216\n\t"
        "ds_read_b128 v[64:67], v121 offset:10240\n\t"
        "ds_read_b128 v[68:71], v121 offset:11264\n\t"
        "ds_read_b128 v[72:75], v121 offset:12288\n\t"
        "ds_read_b128 v[76:79], v121 offset:13312\n\t"
        "ds_read_b128 v[80:83], v121 offset:14336\n\t"
        "ds_read_b128 v[84:87], v121 offset:15360\n\t"
        "v_mov_b32_e32 v88, 0\n\t"
        "v_mov_b32_e32 v89, 0\n\t"
        "v_mov_b32_e32 v90, 0\n\t"
        "v_mov_b32_e32 v91, 0\n\t"
        "v_mov_b32_e32 v92, 0\n\t"
        "v_mov_b32_e32 v93, 0\n\t"
        "v_mov_b32_e32 v94, 0\n\t"
        "v_mov_b32_e32 v95, 0\n\t"
        "v_mov_b32_e32 v96, 0\n\t"
        "v_mov_b32_e32 v97, 0\n\t"
        "v_mov_b32_e32 v98, 0\n\t"
        "v_mov_b32_e32 v99, 0\n\t"
        "v_mov_b32_e32 v100, 0\n\t"
        "v_mov_b32_e32 v101, 0\n\t"
        "v_mov_b32_e32 v102, 0\n\t"
        "v_mov_b32_e32 v103, 0\n\t"
        "s_waitcnt lgkmcnt(0)\n\t"
        "s_barrier\n\t"
        ".Lfa%=:\n\t"
        "s_waitcnt vmcnt(0)\n\t"
        "v_max_u32_e32 v54, v0, v2\n\t"
        "v_max3_u32 v54, v54, v1, v3\n\t"
        "v_max3_u32 v54, v54, v4, v6\n\t"
        "v_max3_u32 v54, v54, v5, v7\n\t"
        "v_max3_u32 v54, v54, v8, v10\n\t"
        "v_max3_u32 v54, v54, v9, v11\n\t"
        "v_max3_u32 v54, v54, v12, v14\n\t"
        "v_max3_u32 v54, v54, v13, v15\n\t"
        "v_cmp_eq_u32_e32 vcc, -1, v54\n\t"
        "s_nop 1\n\t"
        "v_mov_b32_e32 v55, v55\n\t"
        "s_nop 0\n\t"
        "s_cbranch_vccz .Lfg%=\n\t"
        "s_sleep %c[ASLP]\n\t"
        "buffer_load_dwordx4 v[0:3], v120, %[srd], s57 offen sc1\n\t"
        "buffer_load_dwordx4 v[4:7], v120, %[srd], s58 offen sc1\n\t"
        "buffer_load_dwordx4 v[8:11], v120, %[srd], s59 offen sc1\n\t"
        "buffer_load_dwordx4 v[12:15], v120, %[srd], s60 offen sc1\n\t"
        "s_add_u32 s97, s97, 1\n\t"
        "s_cmp_lt_u32 s97, 0x100000\n\t"
        "s_cbranch_scc0 .Lgu%=\n\t"
        "s_branch .Lfa%=\n\t"
        ".Lfg%=:\n\t"
        "s_sleep %c[RSLP]\n\t"
        "s_branch .Lgo%=\n\t"
        ".Lfb%=:\n\t"
        "s_nop 7\n\t"
        "s_mov_b32 s97, 0\n\t"
        "s_add_u32 s80, s40, 1\n\t"
        "v_mov_b32_e32 v54, 30724\n\t"
        "v_mov_b32_e32 v55, s80\n\t"
        "ds_write_b32 v54, v55\n\t"
        ".Lfp%=:\n\t"
        "s_sleep %c[ASLP]\n\t"
        "buffer_load_dword v88, v127, %[srd], s61 offen offset:0 sc1\n\t"
        "buffer_load_dword v89, v127, %[srd], s61 offen offset:256 sc1\n\t"
        "s_add_u32 s97, s97, 1\n\t"
        "s_cmp_lt_u32 s97, 0x100000\n\t"
        "s_cbranch_scc0 .Lgu%=\n\t"
        "s_waitcnt vmcnt(0)\n\t"
        "v_max_u32_e32 v54, v88, v89\n\t"
        "v_cmp_eq_u32_e32 vcc, -1, v54\n\t"
        "s_nop 1\n\t"
        "v_mov_b32_e32 v55, v55\n\t"
        "s_nop 0\n\t"
        "s_cbranch_vccnz .Lfp%=\n\t"
        "v_mfma_f32_16x16x32_bf16 v[40:43], v[112:115], v[36:39], 0\n\t"
        "v_mfma_f32_16x16x32_bf16 v[44:47], v[116:119], v[36:39], 0\n\t"
        "s_nop 1\n\t"
        "v_mov_b32_e32 v55, v55\n\t"
        "s_nop 1\n\t"
        "v_mov_b32_e32 v55, v55\n\t"
        "s_nop 0\n\t"
        "v_pk_mul_f32 v[40:41], v[40:41], v[10:11] op_sel_hi:[1,0]\n\t"
        "v_pk_mul_f32 v[42:43], v[42:43], v[10:11] op_sel_hi:[1,0]\n\t"
        "v_pk_mul_f32 v[44:45], v[44:45], v[10:11] op_sel_hi:[1,0]\n\t"
        "v_pk_mul_f32 v[46:47], v[46:47], v[10:11] op_sel_hi:[1,0]\n\t"
        "v_mfma_f32_16x16x16_bf16 v[40:43], v[88:89], v[90:91], v[40:43]\n\t"
        "s_nop 0\n\t"
        "v_mfma_f32_16x16x16_bf16 v[44:47], v[88:89], v[94:95], v[44:47]\n\t"
        "s_nop 7\n\t"
        "s_branch .Lrb%=\n\t"
        ".Lta%=:\n\t"
        "s_waitcnt lgkmcnt(0)\n\t"
        "s_branch .Lxa%=\n\t"
        ".Lgu%=:\n\t"
        "s_mov_b64 exec, -1\n\t"
        "v_mov_b32_e32 v54, %c[ferr]\n\t"
        "v_mov_b32_e32 v55, 1\n\t"
        "buffer_store_dword v55, v54, %[wsd], 0 offen sc1\n\t"
        "s_endpgm\n\t"
        ".Lend%=:\n\t"
        "s_nop 0\n\t"
        "v_permlane16_swap_b32_e32 v50, v51\n\t"
        "v_mov_b32_e32 v0, 64\n\t"
        "buffer_store_dwordx2 v[50:51], v34, %[wsd], s72 offen sc1\n\t"
        "s_cmp_lg_u32 %[w], 0\n\t"
        "s_waitcnt vmcnt(0)\n\t"
        "s_barrier\n\t"
        "s_cbranch_scc1 .Le2%=\n\t"
        "s_mov_b64 exec, 1\n\t"
        "buffer_store_dword v0, off, %[wsd], %[pra] sc1\n\t"
        "s_mov_b64 exec, -1\n\t"
        ".Le2%=:\n\t"
        "s_endpgm"
        : [nA] "+s"(nA), [nS] "+s"(nS), [nW] "+s"(nW), [nC] "+s"(nC), [b0] "+s"(b0), [b1] "+s"(b1), [pob] "+s"(pob)
        : [srd] "s"(rhd), [wsd] "s"(wsd), [w] "s"(w), [tag] "s"(tag), [kx] "s"(kx), [vx] "s"(vx), [kf] "s"(kf), [kvo] "s"(kvo), [cal] "s"(cal), [sq] "s"(sq), [sk] "s"(sk), [m0b] "s"(m0b),
          [swo] "s"(swo), [pra] "s"(pr_a), [ferr] "i"(F_ERR), [iD1] "i"(D1), [iD2] "i"(D2), [LC] "i"(LC0), [LA] "i"(LA0), [LC24] "i"(LC0 + 24), [LA24] "i"(LA0 + 24), [E1] "i"(2 * (D1 - LC0)), [E2] "i"(2 * (D2 - LA0)),
          [iPEN] "i"(PEN), [iPEN2] "i"(PEN2), [iPENM] "i"(PEN + 2 * PEN2), [iPEN0] "i"(PEN0), [iTOL] "i"(TOL), [iCT0] "i"(CT0), [iX0C] "i"(X0C), [iX0A] "i"(X0A), [iPHM] "i"(PH_M),
          [iFT] "i"(-3), [iFN] "i"(FINE_N), [AUP] "i"(A_UP), [ASLP] "i"(A_SLP), [LDEC] "i"(L_DEC), [UPSH] "i"(UP_SH), [TRUP] "i"(TR_UP), [TRDN] "i"(TR_DN), [HSLP] "i"(H_SLP), [iPMIN] "i"(PMIN), [VSLP] "i"(V_SLP), [RSLP] "i"(R_SLP), [iVD] "i"(V_DEL), [iXR] "i"(XR), [NX0] "i"(NEARX), [NX1] "i"(8192 - NEARX),
          "{v[120:127]}"(pc), "{v[112:113]}"(pd), "{v96}"(dv), "{v[98:105]}"(pa)
        : "memory", "vcc", "scc", "m0", "s36", "s37", "s38", "s39", "s40", "s41", "s42", "s43", "s44", "s45", "s46", "s47", "s48", "s49", "s50", "s51", "s52", "s53", "s54", "s55", "s56", "s57", "s58", "s59", "s60", "s61", "s62", "s63", "s64", "s65", "s66", "s67", "s68", "s69", "s70", "s71", "s72", "s73", "s74", "s75", "s76", "s77", "s78", "s79", "s80", "s81", "s82", "s83", "s84", "s85", "s86", "s87", "s88", "s89", "s90", "s91", "s92", "s93", "s94", "s95", "s96", "s97", "s98", "s99");
    } else {
        const int cb = i32;
        const v4u sv = mk_srd(p.Wv, 1024 * 4096 * 2), so = mk_srd(p.Wo, 1024 * 4096 * 2);
        const unsigned key = KEYM & ((cb & 1) * 2 + G), tu = cb >> 2, m0b = w * 32768, f = (g & 1) | ((c >> 1) << 1);
        const unsigned t0 = m0b + (16 * (g >> 1) + 4 * (g & 1) + (c & 1) + 8 * (c >> 1)) * 128 + (f << 5) + (n16 & 3) * 8, t1 = m0b + (16 * g + 4 * (c >> 1) + (c & 1)) * 64 + ((c >> 1) << 5) + (n16 & 3) * 8;
        v8u pa;
        pa[0] = (64 * w + (l >> 3)) * 8192 + vi * 128 + (((l & 7) ^ ((l >> 5) << 1)) << 4); pa[1] = pa[0] ^ 64u;
        pa[2] = (1024 * w + (l >> 2)) * 2048 + cb * 64 + (((l & 3) ^ ((g & 1) << 1)) << 4);
#pragma unroll
        for (int q = 0; q < 4; ++q) pa[3 + q] = t0 ^ (((w + q) & 3) << 5);
        pa[7] = t1;
        const v2u pb = {t1 ^ 32u, (unsigned)((w * 1024 + cb * 32) * 2048 + l * 1024) + (((3u - (so.x >> 7)) & 7u) << 7)};
        const v16u pc = {(unsigned)tid * 16, (n16 & 2) ? 0xEEEEu : 0x4444u, (unsigned)((vi & 1) * 1024 + w * 256 + n16 * 16 + g * 4), (unsigned)((G * 4 + w) * 16384 + l * 16),
                   w < 3 ? (unsigned)(w * 32768 + (cb >> 4) * 16384 + ((cb >> 1) & 7) * 1024 + ((cb & 1) * 2 + (l >> 5)) * 256 + G * 128 + (l & 31) * 4) : OOB,
                   tid < 3 ? (unsigned)(6 * 16384 + l * 1024 + (32 + vi) * 8) : OOB, tid == 192 ? pr_a : OOB, (unsigned)(L_V + 8192 + w * 512 + (g & 1) * 256 + (g >> 1) * 64 + n16 * 4),
                   (unsigned)(L_V + (((l >> 2) & 1) + ((l >> 3) & 2)) * 128 + (l >> 5) * 64 + (l & 8) * 4 + (l & 3) * 8), 0u,
                   (unsigned)(L_V + w * 2048 + l * 8), (unsigned)(L_V + ((w + 1) & 3) * 2048 + 512 + l * 8), (unsigned)(L_V + ((w + 2) & 3) * 2048 + 1024 + l * 8), (unsigned)(L_V + ((w + 3) & 3) * 2048 + 1536 + l * 8), 0u, 0u};
        if (tid < 2) ((lu32*)(size_t)(L_V + L_FL))[tid] = ~0u;
    asm volatile(
        "s_mov_b32 s82, 3\n\t"
        ".Ld0%=:\n\t"
        "s_xor_b32 s80, s82, %[key]\n\t"
        "s_mul_i32 s81, s80, 131072\n\t"
        "s_lshl_b32 s80, s80, 11\n\t"
        "s_add_u32 s80, s80, %[m0b]\n\t"
        "s_add_u32 m0, s80, 0\n\t"
        "s_add_u32 s81, s81, 0\n\t"
        "buffer_load_dwordx4 v96, %[sv], s81 offen lds\n\t"
        "s_add_u32 m0, m0, 0x400\n\t"
        "s_add_u32 s81, s81, 65536\n\t"
        "buffer_load_dwordx4 v97, %[sv], s81 offen lds\n\t"
        "s_sub_u32 s82, s82, 1\n\t"
        "s_cbranch_scc0 .Ld0%=\n\t"
        "s_mov_b32 s82, 3\n\t"
        ".Ld1%=:\n\t"
        "s_xor_b32 s80, s82, %[key]\n\t"
        "s_mul_i32 s81, s80, 131072\n\t"
        "s_lshl_b32 s80, s80, 11\n\t"
        "s_add_u32 s80, s80, %[m0b]\n\t"
        "s_add_u32 m0, s80, 8192\n\t"
        "s_add_u32 s81, s81, 2097152\n\t"
        "buffer_load_dwordx4 v96, %[sv], s81 offen lds\n\t"
        "s_add_u32 m0, m0, 0x400\n\t"
        "s_add_u32 s81, s81, 65536\n\t"
        "buffer_load_dwordx4 v97, %[sv], s81 offen lds\n\t"
        "s_sub_u32 s82, s82, 1\n\t"
        "s_cbranch_scc0 .Ld1%=\n\t"
        "s_cmp_eq_u32 %[tu], 0\n\t"
        "s_cselect_b32 s81, 0, 0x7fff0000\n\t"
        "buffer_load_dword v110, v105, %[so], s81 offen\n\t"
        "s_mov_b32 s82, 3\n\t"
        ".Ld2%=:\n\t"
        "s_xor_b32 s80, s82, %[key]\n\t"
        "s_mul_i32 s81, s80, 131072\n\t"
        "s_lshl_b32 s80, s80, 11\n\t"
        "s_add_u32 s80, s80, %[m0b]\n\t"
        "s_add_u32 m0, s80, 16384\n\t"
        "s_add_u32 s81, s81, 4194304\n\t"
        "buffer_load_dwordx4 v96, %[sv], s81 offen lds\n\t"
        "s_add_u32 m0, m0, 0x400\n\t"
        "s_add_u32 s81, s81, 65536\n\t"
        "buffer_load_dwordx4 v97, %[sv], s81 offen lds\n\t"
        "s_sub_u32 s82, s82, 1\n\t"
        "s_cbranch_scc0 .Ld2%=\n\t"
        "s_cmp_eq_u32 %[tu], 1\n\t"
        "s_cselect_b32 s81, 0, 0x7fff0000\n\t"
        "buffer_load_dword v110, v105, %[so], s81 offen\n\t"
        "s_mov_b32 s82, 3\n\t"
        ".Ld3%=:\n\t"
        "s_xor_b32 s80, s82, %[key]\n\t"
        "s_mul_i32 s81, s80, 131072\n\t"
        "s_lshl_b32 s80, s80, 11\n\t"
        "s_add_u32 s80, s80, %[m0b]\n\t"
        "s_add_u32 m0, s80, 24576\n\t"
        "s_add_u32 s81, s81, 6291456\n\t"
        "buffer_load_dwordx4 v96, %[sv], s81 offen lds\n\t"
        "s_add_u32 m0, m0, 0x400\n\t"
        "s_add_u32 s81, s81, 65536\n\t"
        "buffer_load_dwordx4 v97, %[sv], s81 offen lds\n\t"
        "s_sub_u32 s82, s82, 1\n\t"
        "s_cbranch_scc0 .Ld3%=\n\t"
        "s_waitcnt vmcnt(26)\n\t"
        "ds_read_b64_tr_b16 v[128:129], v99 offset:0\n\t"
        "ds_read_b64_tr_b16 v[130:131], v99 offset:256\n\t"
        "ds_read_b64_tr_b16 v[132:133], v99 offset:4096\n\t"
        "ds_read_b64_tr_b16 v[134:135], v99 offset:4352\n\t"
        "ds_read_b64_tr_b16 v[136:137], v100 offset:0\n\t"
        "ds_read_b64_tr_b16 v[138:139], v100 offset:256\n\t"
        "ds_read_b64_tr_b16 v[140:141], v100 offset:4096\n\t"
        "ds_read_b64_tr_b16 v[142:143], v100 offset:4352\n\t"
        "ds_read_b64_tr_b16 v[144:145], v101 offset:0\n\t"
        "ds_read_b64_tr_b16 v[146:147], v101 offset:256\n\t"
        "ds_read_b64_tr_b16 v[148:149], v101 offset:4096\n\t"
        "ds_read_b64_tr_b16 v[150:151], v101 offset:4352\n\t"
        "ds_read_b64_tr_b16 v[152:153], v102 offset:0\n\t"
        "ds_read_b64_tr_b16 v[154:155], v102 offset:256\n\t"
        "ds_read_b64_tr_b16 v[156:157], v102 offset:4096\n\t"
        "ds_read_b64_tr_b16 v[158:159], v102 offset:4352\n\t"
        "s_waitcnt lgkmcnt(0)\n\t"
        "s_cmp_eq_u32 %[tu], 2\n\t"
        "s_cselect_b32 s81, 0, 0x7fff0000\n\t"
        "buffer_load_dword v110, v105, %[so], s81 offen\n\t"
        "s_mov_b32 s82, 3\n\t"
        ".Ld4%=:\n\t"
        "s_xor_b32 s80, s82, %[key]\n\t"
        "s_mul_i32 s81, s80, 65536\n\t"
        "s_lshl_b32 s80, s80, 11\n\t"
        "s_add_u32 s80, s80, %[m0b]\n\t"
        "s_add_u32 m0, s80, 0\n\t"
        "s_add_u32 s81, s81, 0\n\t"
        "buffer_load_dwordx4 v98, %[so], s81 offen lds\n\t"
        "s_add_u32 m0, m0, 0x400\n\t"
        "s_add_u32 s81, s81, 32768\n\t"
        "buffer_load_dwordx4 v98, %[so], s81 offen lds\n\t"
        "s_sub_u32 s82, s82, 1\n\t"
        "s_cbranch_scc0 .Ld4%=\n\t"
        "s_waitcnt vmcnt(27)\n\t"
        "ds_read_b64_tr_b16 v[160:161], v99 offset:8192\n\t"
        "ds_read_b64_tr_b16 v[162:163], v99 offset:8448\n\t"
        "ds_read_b64_tr_b16 v[164:165], v99 offset:12288\n\t"
        "ds_read_b64_tr_b16 v[166:167], v99 offset:12544\n\t"
        "ds_read_b64_tr_b16 v[168:169], v100 offset:8192\n\t"
        "ds_read_b64_tr_b16 v[170:171], v100 offset:8448\n\t"
        "ds_read_b64_tr_b16 v[172:173], v100 offset:12288\n\t"
        "ds_read_b64_tr_b16 v[174:175], v100 offset:12544\n\t"
        "ds_read_b64_tr_b16 v[176:177], v101 offset:8192\n\t"
        "ds_read_b64_tr_b16 v[178:179], v101 offset:8448\n\t"
        "ds_read_b64_tr_b16 v[180:181], v101 offset:12288\n\t"
        "ds_read_b64_tr_b16 v[182:183], v101 offset:12544\n\t"
        "ds_read_b64_tr_b16 v[184:185], v102 offset:8192\n\t"
        "ds_read_b64_tr_b16 v[186:187], v102 offset:8448\n\t"
        "ds_read_b64_tr_b16 v[188:189], v102 offset:12288\n\t"
        "ds_read_b64_tr_b16 v[190:191], v102 offset:12544\n\t"
        "s_waitcnt lgkmcnt(0)\n\t"
        "s_cmp_eq_u32 %[tu], 3\n\t"
        "s_cselect_b32 s81, 0, 0x7fff0000\n\t"
        "buffer_load_dword v110, v105, %[so], s81 offen\n\t"
        "s_mov_b32 s82, 3\n\t"
        ".Ld5%=:\n\t"
        "s_xor_b32 s80, s82, %[key]\n\t"
        "s_mul_i32 s81, s80, 65536\n\t"
        "s_lshl_b32 s80, s80, 11\n\t"
        "s_add_u32 s80, s80, %[m0b]\n\t"
        "s_add_u32 m0, s80, 8192\n\t"
        "s_add_u32 s81, s81, 262144\n\t"
        "buffer_load_dwordx4 v98, %[so], s81 offen lds\n\t"
        "s_add_u32 m0, m0, 0x400\n\t"
        "s_add_u32 s81, s81, 32768\n\t"
        "buffer_load_dwordx4 v98, %[so], s81 offen lds\n\t"
        "s_sub_u32 s82, s82, 1\n\t"
        "s_cbranch_scc0 .Ld5%=\n\t"
        "s_waitcnt vmcnt(27)\n\t"
        "ds_read_b64_tr_b16 v[192:193], v99 offset:16384\n\t"
        "ds_read_b64_tr_b16 v[194:195], v99 offset:16640\n\t"
        "ds_read_b64_tr_b16 v[196:197], v99 offset:20480\n\t"
        "ds_read_b64_tr_b16 v[198:199], v99 offset:20736\n\t"
        "ds_read_b64_tr_b16 v[200:201], v100 offset:16384\n\t"
        "ds_read_b64_tr_b16 v[202:203], v100 offset:16640\n\t"
        "ds_read_b64_tr_b16 v[204:205], v100 offset:20480\n\t"
        "ds_read_b64_tr_b16 v[206:207], v100 offset:20736\n\t"
        "ds_read_b64_tr_b16 v[208:209], v101 offset:16384\n\t"
        "ds_read_b64_tr_b16 v[210:211], v101 offset:16640\n\t"
        "ds_read_b64_tr_b16 v[212:213], v101 offset:20480\n\t"
        "ds_read_b64_tr_b16 v[214:215], v101 offset:20736\n\t"
        "ds_read_b64_tr_b16 v[216:217], v102 offset:16384\n\t"
        "ds_read_b64_tr_b16 v[218:219], v102 offset:16640\n\t"
        "ds_read_b64_tr_b16 v[220:221], v102 offset:20480\n\t"
        "ds_read_b64_tr_b16 v[222:223], v102 offset:20736\n\t"
        "s_waitcnt lgkmcnt(0)\n\t"
        "s_cmp_eq_u32 %[tu], 4\n\t"
        "s_cselect_b32 s81, 0, 0x7fff0000\n\t"
        "buffer_load_dword v110, v105, %[so], s81 offen\n\t"
        "s_mov_b32 s82, 3\n\t"
        ".Ld6%=:\n\t"
        "s_xor_b32 s80, s82, %[key]\n\t"
        "s_mul_i32 s81, s80, 65536\n\t"
        "s_lshl_b32 s80, s80, 11\n\t"
        "s_add_u32 s80, s80, %[m0b]\n\t"
        "s_add_u32 m0, s80, 16384\n\t"
        "s_add_u32 s81, s81, 524288\n\t"
        "buffer_load_dwordx4 v98, %[so], s81 offen lds\n\t"
        "s_add_u32 m0, m0, 0x400\n\t"
        "s_add_u32 s81, s81, 32768\n\t"
        "buffer_load_dwordx4 v98, %[so], s81 offen lds\n\t"
        "s_sub_u32 s82, s82, 1\n\t"
        "s_cbranch_scc0 .Ld6%=\n\t"
        "s_waitcnt vmcnt(27)\n\t"
        "ds_read_b64_tr_b16 v[224:225], v99 offset:24576\n\t"
        "ds_read_b64_tr_b16 v[226:227], v99 offset:24832\n\t"
        "ds_read_b64_tr_b16 v[228:229], v99 offset:28672\n\t"
        "ds_read_b64_tr_b16 v[230:231], v99 offset:28928\n\t"
        "ds_read_b64_tr_b16 v[232:233], v100 offset:24576\n\t"
        "ds_read_b64_tr_b16 v[234:235], v100 offset:24832\n\t"
        "ds_read_b64_tr_b16 v[236:237], v100 offset:28672\n\t"
        "ds_read_b64_tr_b16 v[238:239], v100 offset:28928\n\t"
        "ds_read_b64_tr_b16 v[240:241], v101 offset:24576\n\t"
        "ds_read_b64_tr_b16 v[242:243], v101 offset:24832\n\t"
        "ds_read_b64_tr_b16 v[244:245], v101 offset:28672\n\t"
        "ds_read_b64_tr_b16 v[246:247], v101 offset:28928\n\t"
        "ds_read_b64_tr_b16 v[248:249], v102 offset:24576\n\t"
        "ds_read_b64_tr_b16 v[250:251], v102 offset:24832\n\t"
        "ds_read_b64_tr_b16 v[252:253], v102 offset:28672\n\t"
        "ds_read_b64_tr_b16 v[254:255], v102 offset:28928\n\t"
        "s_waitcnt lgkmcnt(0)\n\t"
        "s_cmp_eq_u32 %[tu], 5\n\t"
        "s_cselect_b32 s81, 0, 0x7fff0000\n\t"
        "buffer_load_dword v110, v105, %[so], s81 offen\n\t"
        "s_mov_b32 s82, 3\n\t"
        ".Ld7%=:\n\t"
        "s_xor_b32 s80, s82, %[key]\n\t"
        "s_mul_i32 s81, s80, 65536\n\t"
        "s_lshl_b32 s80, s80, 11\n\t"
        "s_add_u32 s80, s80, %[m0b]\n\t"
        "s_add_u32 m0, s80, 24576\n\t"
        "s_add_u32 s81, s81, 786432\n\t"
        "buffer_load_dwordx4 v98, %[so], s81 offen lds\n\t"
        "s_add_u32 m0, m0, 0x400\n\t"
        "s_add_u32 s81, s81, 32768\n\t"
        "buffer_load_dwordx4 v98, %[so], s81 offen lds\n\t"
        "s_sub_u32 s82, s82, 1\n\t"
        "s_cbranch_scc0 .Ld7%=\n\t"
        "s_waitcnt vmcnt(27)\n\t"
        "ds_read_b64_tr_b16 a[0:1], v103 offset:0\n\t"
        "ds_read_b64_tr_b16 a[2:3], v103 offset:128\n\t"
        "ds_read_b64_tr_b16 a[4:5], v103 offset:4096\n\t"
        "ds_read_b64_tr_b16 a[6:7], v103 offset:4224\n\t"
        "ds_read_b64_tr_b16 a[8:9], v104 offset:0\n\t"
        "ds_read_b64_tr_b16 a[10:11], v104 offset:128\n\t"
        "ds_read_b64_tr_b16 a[12:13], v104 offset:4096\n\t"
        "ds_read_b64_tr_b16 a[14:15], v104 offset:4224\n\t"
        "ds_read_b64_tr_b16 a[16:17], v103 offset:512\n\t"
        "ds_read_b64_tr_b16 a[18:19], v103 offset:640\n\t"
        "ds_read_b64_tr_b16 a[20:21], v103 offset:4608\n\t"
        "ds_read_b64_tr_b16 a[22:23], v103 offset:4736\n\t"
        "ds_read_b64_tr_b16 a[24:25], v104 offset:512\n\t"
        "ds_read_b64_tr_b16 a[26:27], v104 offset:640\n\t"
        "ds_read_b64_tr_b16 a[28:29], v104 offset:4608\n\t"
        "ds_read_b64_tr_b16 a[30:31], v104 offset:4736\n\t"
        "s_waitcnt lgkmcnt(0)\n\t"
        "s_cmp_eq_u32 %[tu], 6\n\t"
        "s_cselect_b32 s81, 0, 0x7fff0000\n\t"
        "buffer_load_dword v110, v105, %[so], s81 offen\n\t"
        "s_mov_b32 s82, 3\n\t"
        ".Ld8%=:\n\t"
        "s_xor_b32 s80, s82, %[key]\n\t"
        "s_mul_i32 s81, s80, 65536\n\t"
        "s_lshl_b32 s80, s80, 11\n\t"
        "s_add_u32 s80, s80, %[m0b]\n\t"
        "s_add_u32 m0, s80, 0\n\t"
        "s_add_u32 s81, s81, 1048576\n\t"
        "buffer_load_dwordx4 v98, %[so], s81 offen lds\n\t"
        "s_add_u32 m0, m0, 0x400\n\t"
        "s_add_u32 s81, s81, 32768\n\t"
        "buffer_load_dwordx4 v98, %[so], s81 offen lds\n\t"
        "s_sub_u32 s82, s82, 1\n\t"
        "s_cbranch_scc0 .Ld8%=\n\t"
        "s_waitcnt vmcnt(27)\n\t"
        "ds_read_b64_tr_b16 a[32:33], v103 offset:8192\n\t"
        "ds_read_b64_tr_b16 a[34:35], v103 offset:8320\n\t"
        "ds_read_b64_tr_b16 a[36:37], v103 offset:12288\n\t"
        "ds_read_b64_tr_b16 a[38:39], v103 offset:12416\n\t"
        "ds_read_b64_tr_b16 a[40:41], v104 offset:8192\n\t"
        "ds_read_b64_tr_b16 a[42:43], v104 offset:8320\n\t"
        "ds_read_b64_tr_b16 a[44:45], v104 offset:12288\n\t"
        "ds_read_b64_tr_b16 a[46:47], v104 offset:12416\n\t"
        "ds_read_b64_tr_b16 a[48:49], v103 offset:8704\n\t"
        "ds_read_b64_tr_b16 a[50:51], v103 offset:8832\n\t"
        "ds_read_b64_tr_b16 a[52:53], v103 offset:12800\n\t"
        "ds_read_b64_tr_b16 a[54:55], v103 offset:12928\n\t"
        "ds_read_b64_tr_b16 a[56:57], v104 offset:8704\n\t"
        "ds_read_b64_tr_b16 a[58:59], v104 offset:8832\n\t"
        "ds_read_b64_tr_b16 a[60:61], v104 offset:12800\n\t"
        "ds_read_b64_tr_b16 a[62:63], v104 offset:12928\n\t"
        "s_waitcnt lgkmcnt(0)\n\t"
        "s_cmp_eq_u32 %[tu], 7\n\t"
        "s_cselect_b32 s81, 0, 0x7fff0000\n\t"
        "buffer_load_dword v110, v105, %[so], s81 offen\n\t"
        "s_mov_b32 s82, 3\n\t"
        ".Ld9%=:\n\t"
        "s_xor_b32 s80, s82, %[key]\n\t"
        "s_mul_i32 s81, s80, 65536\n\t"
        "s_lshl_b32 s80, s80, 11\n\t"
        "s_add_u32 s80, s80, %[m0b]\n\t"
        "s_add_u32 m0, s80, 8192\n\t"
        "s_add_u32 s81, s81, 1310720\n\t"
        "buffer_load_dwordx4 v98, %[so], s81 offen lds\n\t"
        "s_add_u32 m0, m0, 0x400\n\t"
        "s_add_u32 s81, s81, 32768\n\t"
        "buffer_load_dwordx4 v98, %[so], s81 offen lds\n\t"
        "s_sub_u32 s82, s82, 1\n\t"
        "s_cbranch_scc0 .Ld9%=\n\t"
        "s_waitcnt vmcnt(27)\n\t"
        "ds_read_b64_tr_b16 a[64:65], v103 offset:16384\n\t"
        "ds_read_b64_tr_b16 a[66:67], v103 offset:16512\n\t"
        "ds_read_b64_tr_b16 a[68:69], v103 offset:20480\n\t"
        "ds_read_b64_tr_b16 a[70:71], v103 offset:20608\n\t"
        "ds_read_b64_tr_b16 a[72:73], v104 offset:16384\n\t"
        "ds_read_b64_tr_b16 a[74:75], v104 offset:16512\n\t"
        "ds_read_b64_tr_b16 a[76:77], v104 offset:20480\n\t"
        "ds_read_b64_tr_b16 a[78:79], v104 offset:20608\n\t"
        "ds_read_b64_tr_b16 a[80:81], v103 offset:16896\n\t"
        "ds_read_b64_tr_b16 a[82:83], v103 offset:17024\n\t"
        "ds_read_b64_tr_b16 a[84:85], v103 offset:20992\n\t"
        "ds_read_b64_tr_b16 a[86:87], v103 offset:21120\n\t"
        "ds_read_b64_tr_b16 a[88:89], v104 offset:16896\n\t"
        "ds_read_b64_tr_b16 a[90:91], v104 offset:17024\n\t"
        "ds_read_b64_tr_b16 a[92:93], v104 offset:20992\n\t"
        "ds_read_b64_tr_b16 a[94:95], v104 offset:21120\n\t"
        "buffer_load_dwordx4 v[0:3], v112, %[wsd], %[cal] offen sc1\n\t"
        "s_waitcnt lgkmcnt(0)\n\t"
        "s_mov_b32 s82, 3\n\t"
        ".Ld10%=:\n\t"
        "s_xor_b32 s80, s82, %[key]\n\t"
        "s_mul_i32 s81, s80, 65536\n\t"
        "s_lshl_b32 s80, s80, 11\n\t"
        "s_add_u32 s80, s80, %[m0b]\n\t"
        "s_add_u32 m0, s80, 16384\n\t"
        "s_add_u32 s81, s81, 1572864\n\t"
        "buffer_load_dwordx4 v98, %[so], s81 offen lds\n\t"
        "s_add_u32 m0, m0, 0x400\n\t"
        "s_add_u32 s81, s81, 32768\n\t"
        "buffer_load_dwordx4 v98, %[so], s81 offen lds\n\t"
        "s_sub_u32 s82, s82, 1\n\t"
        "s_cbranch_scc0 .Ld10%=\n\t"
        "s_waitcnt vmcnt(27)\n\t"
        "ds_read_b64_tr_b16 a[96:97], v103 offset:24576\n\t"
        "ds_read_b64_tr_b16 a[98:99], v103 offset:24704\n\t"
        "ds_read_b64_tr_b16 a[100:101], v103 offset:28672\n\t"
        "ds_read_b64_tr_b16 a[102:103], v103 offset:28800\n\t"
        "ds_read_b64_tr_b16 a[104:105], v104 offset:24576\n\t"
        "ds_read_b64_tr_b16 a[106:107], v104 offset:24704\n\t"
        "ds_read_b64_tr_b16 a[108:109], v104 offset:28672\n\t"
        "ds_read_b64_tr_b16 a[110:111], v104 offset:28800\n\t"
        "ds_read_b64_tr_b16 a[112:113], v103 offset:25088\n\t"
        "ds_read_b64_tr_b16 a[114:115], v103 offset:25216\n\t"
        "ds_read_b64_tr_b16 a[116:117], v103 offset:29184\n\t"
        "ds_read_b64_tr_b16 a[118:119], v103 offset:29312\n\t"
        "ds_read_b64_tr_b16 a[120:121], v104 offset:25088\n\t"
        "ds_read_b64_tr_b16 a[122:123], v104 offset:25216\n\t"
        "ds_read_b64_tr_b16 a[124:125], v104 offset:29184\n\t"
        "ds_read_b64_tr_b16 a[126:127], v104 offset:29312\n\t"
        "s_waitcnt lgkmcnt(0)\n\t"
        "s_mov_b32 s82, 3\n\t"
        ".Ld11%=:\n\t"
        "s_xor_b32 s80, s82, %[key]\n\t"
        "s_mul_i32 s81, s80, 65536\n\t"
        "s_lshl_b32 s80, s80, 11\n\t"
        "s_add_u32 s80, s80, %[m0b]\n\t"
        "s_add_u32 m0, s80, 24576\n\t"
        "s_add_u32 s81, s81, 1835008\n\t"
        "buffer_load_dwordx4 v98, %[so], s81 offen lds\n\t"
        "s_add_u32 m0, m0, 0x400\n\t"
        "s_add_u32 s81, s81, 32768\n\t"
        "buffer_load_dwordx4 v98, %[so], s81 offen lds\n\t"
        "s_sub_u32 s82, s82, 1\n\t"
        "s_cbranch_scc0 .Ld11%=\n\t"
        "s_waitcnt vmcnt(26)\n\t"
        "ds_read_b64_tr_b16 a[128:129], v103 offset:0\n\t"
        "ds_read_b64_tr_b16 a[130:131], v103 offset:128\n\t"
        "ds_read_b64_tr_b16 a[132:133], v103 offset:4096\n\t"
        "ds_read_b64_tr_b16 a[134:135], v103 offset:4224\n\t"
        "ds_read_b64_tr_b16 a[136:137], v104 offset:0\n\t"
        "ds_read_b64_tr_b16 a[138:139], v104 offset:128\n\t"
        "ds_read_b64_tr_b16 a[140:141], v104 offset:4096\n\t"
        "ds_read_b64_tr_b16 a[142:143], v104 offset:4224\n\t"
        "ds_read_b64_tr_b16 a[144:145], v103 offset:512\n\t"
        "ds_read_b64_tr_b16 a[146:147], v103 offset:640\n\t"
        "ds_read_b64_tr_b16 a[148:149], v103 offset:4608\n\t"
        "ds_read_b64_tr_b16 a[150:151], v103 offset:4736\n\t"
        "ds_read_b64_tr_b16 a[152:153], v104 offset:512\n\t"
        "ds_read_b64_tr_b16 a[154:155], v104 offset:640\n\t"
        "ds_read_b64_tr_b16 a[156:157], v104 offset:4608\n\t"
        "ds_read_b64_tr_b16 a[158:159], v104 offset:4736\n\t"
        "s_waitcnt lgkmcnt(0)\n\t"
        "s_waitcnt vmcnt(17)\n\t"
        "ds_read_b64_tr_b16 a[160:161], v103 offset:8192\n\t"
        "ds_read_b64_tr_b16 a[162:163], v103 offset:8320\n\t"
        "ds_read_b64_tr_b16 a[164:165], v103 offset:12288\n\t"
        "ds_read_b64_tr_b16 a[166:167], v103 offset:12416\n\t"
        "ds_read_b64_tr_b16 a[168:169], v104 offset:8192\n\t"
        "ds_read_b64_tr_b16 a[170:171], v104 offset:8320\n\t"
        "ds_read_b64_tr_b16 a[172:173], v104 offset:12288\n\t"
        "ds_read_b64_tr_b16 a[174:175], v104 offset:12416\n\t"
        "ds_read_b64_tr_b16 a[176:177], v103 offset:8704\n\t"
        "ds_read_b64_tr_b16 a[178:179], v103 offset:8832\n\t"
        "ds_read_b64_tr_b16 a[180:181], v103 offset:12800\n\t"
        "ds_read_b64_tr_b16 a[182:183], v103 offset:12928\n\t"
        "ds_read_b64_tr_b16 a[184:185], v104 offset:8704\n\t"
        "ds_read_b64_tr_b16 a[186:187], v104 offset:8832\n\t"
        "ds_read_b64_tr_b16 a[188:189], v104 offset:12800\n\t"
        "ds_read_b64_tr_b16 a[190:191], v104 offset:12928\n\t"
        "s_waitcnt lgkmcnt(0)\n\t"
        "s_waitcnt vmcnt(8)\n\t"
        "ds_read_b64_tr_b16 a[192:193], v103 offset:16384\n\t"
        "ds_read_b64_tr_b16 a[194:195], v103 offset:16512\n\t"
        "ds_read_b64_tr_b16 a[196:197], v103 offset:20480\n\t"
        "ds_read_b64_tr_b16 a[198:199], v103 offset:20608\n\t"
        "ds_read_b64_tr_b16 a[200:201], v104 offset:16384\n\t"
        "ds_read_b64_tr_b16 a[202:203], v104 offset:16512\n\t"
        "ds_read_b64_tr_b16 a[204:205], v104 offset:20480\n\t"
        "ds_read_b64_tr_b16 a[206:207], v104 offset:20608\n\t"
        "ds_read_b64_tr_b16 a[208:209], v103 offset:16896\n\t"
        "ds_read_b64_tr_b16 a[210:211], v103 offset:17024\n\t"
        "ds_read_b64_tr_b16 a[212:213], v103 offset:20992\n\t"
        "ds_read_b64_tr_b16 a[214:215], v103 offset:21120\n\t"
        "ds_read_b64_tr_b16 a[216:217], v104 offset:16896\n\t"
        "ds_read_b64_tr_b16 a[218:219], v104 offset:17024\n\t"
        "ds_read_b64_tr_b16 a[220:221], v104 offset:20992\n\t"
        "ds_read_b64_tr_b16 a[222:223], v104 offset:21120\n\t"
        "s_mov_b32 s97, 0\n\t"
        ".Lg0%=:\n\t"
        "v_cmp_ne_u32_e32 vcc, %[tag], v0\n\t"
        "v_cmp_ne_u32_e64 s[82:83], %[tag], v2\n\t"
        "s_nop 1\n\t"
        "s_or_b64 vcc, vcc, s[82:83]\n\t"
        "s_cbranch_scc0 .Lg1%=\n\t"
        "s_sleep 4\n\t"
        "buffer_load_dwordx4 v[0:3], v112, %[wsd], %[cal] offen sc1\n\t"
        "s_add_u32 s97, s97, 1\n\t"
        "s_cmp_lt_u32 s97, 0x100000\n\t"
        "s_cbranch_scc0 .Lgu%=\n\t"
        "s_waitcnt vmcnt(0)\n\t"
        "s_branch .Lg0%=\n\t"
        ".Lg1%=:\n\t"
        "v_cmp_ne_u32_e32 vcc, 0, v1\n\t"
        "v_cmp_ne_u32_e64 s[82:83], 0, v3\n\t"
        "s_mov_b32 s84, 0x55555555\n\t"
        "s_mov_b32 s85, s84\n\t"
        "s_and_b64 s[82:83], s[82:83], s[84:85]\n\t"
        "s_bcnt1_i32_b64 s87, s[82:83]\n\t"
        "s_and_b64 s[82:83], vcc, s[84:85]\n\t"
        "s_bcnt1_i32_b64 s86, s[82:83]\n\t"
        "s_andn2_b64 s[82:83], vcc, s[84:85]\n\t"
        "s_bcnt1_i32_b64 s88, s[82:83]\n\t"
        "s_add_u32 s80, s86, s87\n\t"
        "s_add_u32 s80, s80, s88\n\t"
        "s_cmp_ge_u32 s80, 48\n\t"
        "s_cselect_b32 s80, 8, 24\n\t"
        "s_cmp_gt_u32 s86, s80\n\t"
        "s_cselect_b32 s86, %c[NX1], %c[NX0]\n\t"
        "s_cmp_gt_u32 s87, s80\n\t"
        "s_cselect_b32 s87, %c[NX1], %c[NX0]\n\t"
        "s_cmp_gt_u32 s88, s80\n\t"
        "s_cselect_b32 s88, %c[NX1], %c[NX0]\n\t"
        "s_add_u32 %[nA], %[nA], s86\n\t"
        "s_add_u32 %[nS], %[nS], s86\n\t"
        "s_add_u32 %[nW], %[nW], s86\n\t"
        "s_add_u32 %[nC], %[nC], s87\n\t"
        "s_add_u32 %[b0], %[b0], s88\n\t"
        "s_add_u32 %[b1], %[b1], s88\n\t"
        "s_cmp_eq_u32 %[w], 1\n\t"
        "s_cselect_b32 s80, s87, s86\n\t"
        "s_add_u32 %[pob], %[pob], s80\n\t"
        "s_mov_b32 s36, 0xf0000000\n\t"
        "s_mov_b32 s38, 0xff00ff00\n\t"
        "s_mov_b32 s39, 0xff00ff00\n\t"
        "s_mov_b32 s40, 0\n\t"
        "s_mov_b32 s37, 0\n\t"
        "s_brev_b32 s54, 1\n\t"
        "s_mov_b32 s46, %c[iD1]\n\t"
        "s_mov_b32 s45, %c[iD1]\n\t"
        "s_mov_b32 s47, %c[iD2]\n\t"
        "s_mov_b32 s48, 1\n\t"
        "s_mov_b32 s50, %c[LC]\n\t"
        "s_mov_b32 s51, %c[LA]\n\t"
        "s_mov_b32 s52, %c[E1]\n\t"
        "s_mov_b32 s53, %c[E2]\n\t"
        "s_mov_b32 s43, 0\n\t"
        "s_mov_b32 s61, %[b0]\n\t"
        "s_mov_b32 s62, %[b1]\n\t"
        "s_mov_b32 s75, 162816\n\t"
        "s_waitcnt lgkmcnt(0)\n\t"
        "s_waitcnt vmcnt(0)\n\t"
        "ds_read_b64_tr_b16 a[224:225], v103 offset:24576\n\t"
        "ds_read_b64_tr_b16 a[226:227], v103 offset:24704\n\t"
        "ds_read_b64_tr_b16 a[228:229], v103 offset:28672\n\t"
        "ds_read_b64_tr_b16 a[230:231], v103 offset:28800\n\t"
        "ds_read_b64_tr_b16 a[232:233], v104 offset:24576\n\t"
        "ds_read_b64_tr_b16 a[234:235], v104 offset:24704\n\t"
        "ds_read_b64_tr_b16 a[236:237], v104 offset:28672\n\t"
        "ds_read_b64_tr_b16 a[238:239], v104 offset:28800\n\t"
        "ds_read_b64_tr_b16 a[240:241], v103 offset:25088\n\t"
        "ds_read_b64_tr_b16 a[242:243], v103 offset:25216\n\t"
        "ds_read_b64_tr_b16 a[244:245], v103 offset:29184\n\t"
        "ds_read_b64_tr_b16 a[246:247], v103 offset:29312\n\t"
        "ds_read_b64_tr_b16 a[248:249], v104 offset:25088\n\t"
        "ds_read_b64_tr_b16 a[250:251], v104 offset:25216\n\t"
        "ds_read_b64_tr_b16 a[252:253], v104 offset:29184\n\t"
        "ds_read_b64_tr_b16 a[254:255], v104 offset:29312\n\t"
        "s_waitcnt lgkmcnt(0)\n\t"
        "rec_v_code:\n\t"
        "s_and_b32 s80, s40, 3\n\t"
        "s_lshl_b32 s80, s80, 17\n\t"
        "s_add_u32 s57, s80, %[nA]\n\t"
        "s_add_u32 s58, s57, 0x1000\n\t"
        "s_add_u32 s59, s57, 0x4000\n\t"
        "s_add_u32 s60, s57, 0x5000\n\t"
        "v_mov_b32_e32 v92, -1\n\t"
        "v_mov_b32_e32 v93, -1\n\t"
        "v_mov_b32_e32 v94, -1\n\t"
        "v_mov_b32_e32 v95, -1\n\t"
        "v_mov_b32_e32 v48, 0\n\t"
        "v_mov_b32_e32 v49, 0\n\t"
        "v_mov_b32_e32 v50, 0\n\t"
        "v_mov_b32_e32 v51, 0\n\t"
        "v_mov_b32_e32 v52, 0\n\t"
        "v_mov_b32_e32 v53, 0\n\t"
        "v_mov_b32_e32 v54, 0\n\t"
        "v_mov_b32_e32 v55, 0\n\t"
        "v_mov_b32_e32 v56, 0\n\t"
        "v_mov_b32_e32 v57, 0\n\t"
        "v_mov_b32_e32 v58, 0\n\t"
        "v_mov_b32_e32 v59, 0\n\t"
        "v_mov_b32_e32 v60, 0\n\t"
        "v_mov_b32_e32 v61, 0\n\t"
        "v_mov_b32_e32 v62, 0\n\t"
        "v_mov_b32_e32 v63, 0\n\t"
        "s_barrier\n\t"
        "s_memrealtime s[76:77]\n\t"
        "s_mov_b32 s67, %[nW]\n\t"
        "v_mov_b32_e32 v86, 64\n\t"
        "s_mov_b32 s61, %[b1]\n\t"
        "s_mov_b32 s62, %[b0]\n\t"
        "s_sub_u32 s75, s75, 16\n\t"
        "s_waitcnt lgkmcnt(0)\n\t"
        "s_mov_b32 s41, s76\n\t"
        "s_mov_b32 s69, s76\n\t"
        ".p2align 6\n\t"
        ".Lloop%=:\n\t"
        "s_mov_b32 s97, 0\n\t"
        "s_movk_i32 s96, 0x1000\n\t"
        ".Lwa%=:\n\t"
        "s_memrealtime s[98:99]\n\t"
        "s_addk_i32 s96, -1\n\t"
        "s_cmp_lt_i32 s96, 0\n\t"
        "s_cbranch_scc1 .Lta%=\n\t"
        "s_waitcnt lgkmcnt(0)\n\t"
        "s_sub_i32 s98, s98, s69\n\t"
        "s_cmp_lt_i32 s98, %c[iFT]\n\t"
        "s_cbranch_scc1 .Lwa%=\n\t"
        "s_cmp_ge_i32 s98, 0\n\t"
        "s_cbranch_scc1 .Lxa%=\n\t"
        ".Lqa%=:\n\t"
        "s_nop %c[iFN]\n\t"
        "s_addk_i32 s98, 1\n\t"
        "s_cmp_lt_i32 s98, 0\n\t"
        "s_cbranch_scc1 .Lqa%=\n\t"
        ".Lxa%=:\n\t"
        "buffer_load_dwordx4 v[0:3], v112, %[srd], s57 offen sc1\n\t"
        "s_nop 7\n\t"
        "buffer_load_dwordx4 v[4:7], v112, %[srd], s58 offen sc1\n\t"
        "s_nop 7\n\t"
        "buffer_load_dwordx4 v[8:11], v112, %[srd], s59 offen sc1\n\t"
        "s_nop 7\n\t"
        "buffer_load_dwordx4 v[12:15], v112, %[srd], s60 offen sc1\n\t"
        "v_mov_b32_e32 v80, 161792\n\t"
        "ds_read_b64 v[80:81], v80\n\t"
        "s_waitcnt lgkmcnt(0)\n\t"
        "v_readfirstlane_b32 s80, v80\n\t"
        "v_readfirstlane_b32 s81, v81\n\t"
        "s_cmp_eq_u32 s80, s40\n\t"
        "s_cselect_b32 s84, 1, 0\n\t"
        "s_cmp_eq_u32 s81, s40\n\t"
        "s_cselect_b32 s81, 4, 0\n\t"
        "s_or_b32 s84, s84, s81\n\t"
        "v_mov_b32_e32 v106, s76\n\t"
        "v_mov_b32_e32 v107, s84\n\t"
        "buffer_store_dwordx2 v[106:107], v117, %[srd], s67 offen sc1\n\t"
        "s_and_b32 s80, s40, 3\n\t"
        "s_lshl_b32 s80, s80, 17\n\t"
        "s_add_u32 s63, s80, %[nC]\n\t"
        "s_add_u32 s66, s80, %[nS]\n\t"
        "s_add_u32 s81, s80, 0x20000\n\t"
        "s_and_b32 s81, s81, 0x60000\n\t"
        "s_add_u32 s67, s81, %[nW]\n\t"
        "s_cmp_eq_u32 %[w], 2\n\t"
        "s_cselect_b32 s81, s80, s81\n\t"
        "s_xor_b32 s81, s81, 0x40000\n\t"
        "s_add_u32 s68, s81, %[pob]\n\t"
        "s_lshl_b32 s72, s40, 17\n\t"
        "s_add_u32 s72, s72, %[kvo]\n\t"
        "s_mov_b32 s80, s61\n\t"
        "s_mov_b32 s61, s62\n\t"
        "s_mov_b32 s62, s80\n\t"
        ".Lgo%=:\n\t"
        "s_waitcnt vmcnt(4)\n\t"
        "v_smfmac_f32_16x16x64_bf16 v[52:55], v[0:3], v[136:143], v113\n\t"
        "v_smfmac_f32_16x16x64_bf16 v[56:59], v[0:3], v[144:151], v113\n\t"
        "v_smfmac_f32_16x16x64_bf16 v[60:63], v[0:3], v[152:159], v113\n\t"
        "v_smfmac_f32_16x16x64_bf16 v[48:51], v[0:3], v[128:135], v113\n\t"
        "s_waitcnt vmcnt(3)\n\t"
        "v_smfmac_f32_16x16x64_bf16 v[52:55], v[4:7], v[168:175], v113\n\t"
        "v_smfmac_f32_16x16x64_bf16 v[56:59], v[4:7], v[176:183], v113\n\t"
        "v_smfmac_f32_16x16x64_bf16 v[60:63], v[4:7], v[184:191], v113\n\t"
        "v_smfmac_f32_16x16x64_bf16 v[48:51], v[4:7], v[160:167], v113\n\t"
        "s_waitcnt vmcnt(2)\n\t"
        "v_smfmac_f32_16x16x64_bf16 v[52:55], v[8:11], v[200:207], v113\n\t"
        "v_smfmac_f32_16x16x64_bf16 v[56:59], v[8:11], v[208:215], v113\n\t"
        "v_smfmac_f32_16x16x64_bf16 v[60:63], v[8:11], v[216:223], v113\n\t"
        "v_smfmac_f32_16x16x64_bf16 v[48:51], v[8:11], v[192:199], v113\n\t"
        "s_waitcnt vmcnt(1)\n\t"
        "v_smfmac_f32_16x16x64_bf16 v[52:55], v[12:15], v[232:239], v113\n\t"
        "v_smfmac_f32_16x16x64_bf16 v[56:59], v[12:15], v[240:247], v113\n\t"
        "v_smfmac_f32_16x16x64_bf16 v[60:63], v[12:15], v[248:255], v113\n\t"
        "v_smfmac_f32_16x16x64_bf16 v[48:51], v[12:15], v[224:231], v113\n\t"
        "s_nop 1\n\t"
        "v_mov_b32_e32 v104, v104\n\t"
        "s_nop 1\n\t"
        "v_add_f32_e32 v98, v52, v54\n\t"
        "v_add_f32_e32 v99, v53, v55\n\t"
        "ds_write_b64 v123, v[98:99]\n\t"
        "v_add_f32_e32 v100, v56, v58\n\t"
        "v_add_f32_e32 v101, v57, v59\n\t"
        "ds_write_b64 v124, v[100:101]\n\t"
        "v_add_f32_e32 v102, v60, v62\n\t"
        "v_add_f32_e32 v103, v61, v63\n\t"
        "ds_write_b64 v125, v[102:103]\n\t"
        "v_pk_add_f32 v[96:97], v[48:49], v[50:51]\n\t"
        "v_cmp_u_f32_e32 vcc, v96, v97\n\t"
        "s_nop 2\n\t"
        "v_mov_b32_e32 v104, v104\n\t"
        "s_cbranch_vccnz .Lfa%=\n\t"
        ".Lra%=:\n\t"
        "v_mov_b32_e32 v104, v104\n\t"
        "s_nop 2\n\t"
        "v_mov_b32_e32 v104, v104\n\t"
        "s_nop 2\n\t"
        "v_mov_b32_e32 v104, v104\n\t"
        "s_nop 2\n\t"
        "v_mov_b32_e32 v104, v104\n\t"
        "s_waitcnt lgkmcnt(0)\n\t"
        "s_barrier\n\t"
        "v_mov_b32_e32 v104, v104\n\t"
        "ds_read_b64 v[98:99], v122 offset:512\n\t"
        "ds_read_b64 v[100:101], v122 offset:1024\n\t"
        "ds_read_b64 v[102:103], v122 offset:1536\n\t"
        "v_mov_b32_e32 v104, v104\n\t"
        "s_nop 2\n\t"
        "v_mov_b32_e32 v104, v104\n\t"
        "s_nop 2\n\t"
        "v_mov_b32_e32 v104, v104\n\t"
        "s_waitcnt lgkmcnt(2)\n\t"
        "v_pk_add_f32 v[96:97], v[96:97], v[98:99]\n\t"
        "s_waitcnt lgkmcnt(0)\n\t"
        "v_pk_add_f32 v[100:101], v[100:101], v[102:103]\n\t"
        "v_pk_add_f32 v[96:97], v[96:97], v[100:101]\n\t"
        "v_cvt_pk_bf16_f32 v80, v96, v97\n\t"
        "v_min_u32_e32 v80, -2, v80\n\t"
        "buffer_store_dword v80, v114, %[srd], s61 offen sc1\n\t"
        "s_cmp_eq_u32 s40, 63\n\t"
        "s_cbranch_scc1 .Lend%=\n\t"
        "v_mov_b32_e32 v64, 0\n\t"
        "v_mov_b32_e32 v65, 0\n\t"
        "v_mov_b32_e32 v66, 0\n\t"
        "v_mov_b32_e32 v67, 0\n\t"
        "v_mov_b32_e32 v68, 0\n\t"
        "v_mov_b32_e32 v69, 0\n\t"
        "v_mov_b32_e32 v70, 0\n\t"
        "v_mov_b32_e32 v71, 0\n\t"
        "v_mov_b32_e32 v72, 0\n\t"
        "v_mov_b32_e32 v73, 0\n\t"
        "v_mov_b32_e32 v74, 0\n\t"
        "v_mov_b32_e32 v75, 0\n\t"
        "v_mov_b32_e32 v76, 0\n\t"
        "v_mov_b32_e32 v77, 0\n\t"
        "v_mov_b32_e32 v78, 0\n\t"
        "v_mov_b32_e32 v79, 0\n\t"
        "s_add_u32 s64, s63, 0x1000\n\t"
        "s_sleep %c[VSLP]\n\t"
        "buffer_load_dwordx4 v[88:91], v112, %[srd], s66 offen sc1\n\t"
        "s_cmp_eq_u32 %[w], 0\n\t"
        "s_cselect_b32 exec_hi, 0, -1\n\t"
        "buffer_store_dwordx4 v[92:95], v112, %[srd], s68 offen sc1\n\t"
        "s_mov_b32 exec_hi, -1\n\t"
        "s_waitcnt lgkmcnt(0)\n\t"
        "v_mov_b32_e32 v48, 0\n\t"
        "v_mov_b32_e32 v49, 0\n\t"
        "v_mov_b32_e32 v50, 0\n\t"
        "v_mov_b32_e32 v51, 0\n\t"
        "v_mov_b32_e32 v52, 0\n\t"
        "v_mov_b32_e32 v53, 0\n\t"
        "v_mov_b32_e32 v54, 0\n\t"
        "v_mov_b32_e32 v55, 0\n\t"
        "v_mov_b32_e32 v56, 0\n\t"
        "v_mov_b32_e32 v57, 0\n\t"
        "v_mov_b32_e32 v58, 0\n\t"
        "v_mov_b32_e32 v59, 0\n\t"
        "v_mov_b32_e32 v60, 0\n\t"
        "v_mov_b32_e32 v61, 0\n\t"
        "v_mov_b32_e32 v62, 0\n\t"
        "v_mov_b32_e32 v63, 0\n\t"
        "s_add_u32 s81, s40, 1\n\t"
        "s_and_b32 s80, s81, 3\n\t"
        "s_lshl_b32 s80, s80, 17\n\t"
        "s_add_u32 s57, s80, %[nA]\n\t"
        "s_add_u32 s58, s57, 0x1000\n\t"
        "s_add_u32 s59, s57, 0x4000\n\t"
        "s_add_u32 s60, s57, 0x5000\n\t"
        "s_waitcnt vmcnt(1)\n\t"
        "s_mov_b32 s97, 0\n\t"
        ".Lsr%=:\n\t"
        "s_mov_b32 exec_hi, 0xffff\n\t"
        "v_subrev_u32_e32 v4, s41, v88\n\t"
        "v_subrev_u32_e32 v5, s41, v90\n\t"
        "v_max_i32_e32 v4, v4, v5\n\t"
        "v_max_u32_e32 v5, v89, v91\n\t"
        "v_cmp_eq_u32_e32 vcc, -1, v5\n\t"
        "v_max_i32_dpp v4, v4, v4 quad_perm:[1,0,3,2] row_mask:0xf bank_mask:0xf\n\t"
        "v_or_b32_e32 v6, v89, v91\n\t"
        "s_nop 0\n\t"
        "v_max_i32_dpp v4, v4, v4 quad_perm:[2,3,0,1] row_mask:0xf bank_mask:0xf\n\t"
        "s_nop 1\n\t"
        "v_max_i32_dpp v4, v4, v4 row_ror:4 row_mask:0xf bank_mask:0xf\n\t"
        "s_nop 1\n\t"
        "v_max_i32_dpp v4, v4, v4 row_ror:8 row_mask:0xf bank_mask:0xf\n\t"
        "s_cbranch_vccnz .Lsf%=\n\t"
        "v_readlane_b32 s85, v4, 0\n\t"
        "v_readlane_b32 s81, v4, 16\n\t"
        "v_readlane_b32 s56, v4, 32\n\t"
        "v_and_b32_e32 v5, 1, v6\n\t"
        "v_cmp_ne_u32_e64 s[86:87], 0, v5\n\t"
        "v_and_b32_e32 v5, 2, v6\n\t"
        "v_cmp_ne_u32_e64 s[88:89], 0, v5\n\t"
        "v_and_b32_e32 v5, 4, v6\n\t"
        "v_cmp_ne_u32_e64 s[90:91], 0, v5\n\t"
        "s_max_i32 s56, s56, s81\n\t"
        "s_add_u32 s92, s85, s41\n\t"
        "s_sub_u32 s92, s92, s43\n\t"
        "s_add_i32 s93, s56, s47\n\t"
        "s_mov_b32 s44, s46\n\t"
        "s_mov_b32 s46, s45\n\t"
        "s_mov_b32 s94, s48\n\t"
        "s_mov_b32 s43, s41\n\t"
        "s_cmp_gt_i32 s56, s54\n\t"
        "s_cbranch_scc1 .Lmv%=\n\t"
        "s_mov_b32 s37, 0\n\t"
        "s_mov_b32 s48, 0\n\t"
        "s_mov_b32 s95, 0\n\t"
        ".Lsd%=:\n\t"
        "s_mov_b32 exec_hi, -1\n\t"
        "s_add_u32 s42, s43, s46\n\t"
        "s_mov_b32 s97, 0\n\t"
        "s_movk_i32 s96, 0x1000\n\t"
        ".Lwc%=:\n\t"
        "s_memrealtime s[98:99]\n\t"
        "s_addk_i32 s96, -1\n\t"
        "s_cmp_lt_i32 s96, 0\n\t"
        "s_cbranch_scc1 .Ltc%=\n\t"
        "s_waitcnt lgkmcnt(0)\n\t"
        "s_sub_i32 s98, s98, s42\n\t"
        "s_cmp_lt_i32 s98, %c[iFT]\n\t"
        "s_cbranch_scc1 .Lwc%=\n\t"
        "s_cmp_ge_i32 s98, 0\n\t"
        "s_cbranch_scc1 .Lxc%=\n\t"
        ".Lqc%=:\n\t"
        "s_nop %c[iFN]\n\t"
        "s_addk_i32 s98, 1\n\t"
        "s_cmp_lt_i32 s98, 0\n\t"
        "s_cbranch_scc1 .Lqc%=\n\t"
        ".Lxc%=:\n\t"
        "buffer_load_dwordx4 v[16:19], v115, %[srd], s63 offen offset:0 sc1\n\t"
        "s_cmp_ge_u32 s40, %c[iCT0]\n\t"
        "s_cselect_b32 s85, 1, 0\n\t"
        "s_cmp_lg_u64 s[86:87], 0\n\t"
        "s_cselect_b32 s86, s85, 0\n\t"
        "s_cmp_lg_u64 s[88:89], 0\n\t"
        "s_cselect_b32 s87, s85, 0\n\t"
        "s_cmp_lg_u64 s[90:91], 0\n\t"
        "s_cselect_b32 s88, s85, 0\n\t"
        "buffer_load_dwordx4 v[20:23], v115, %[srd], s63 offen offset:1024 sc1\n\t"
        "s_or_b32 s80, s94, s86\n\t"
        "s_or_b32 s80, s80, s87\n\t"
        "s_andn2_b32 s90, s85, s80\n\t"
        "s_cmp_le_i32 s92, s44\n\t"
        "s_cselect_b32 s80, s85, 0\n\t"
        "s_andn2_b32 s91, s80, s88\n\t"
        "s_and_b32 s80, s40, %c[LDEC]\n\t"
        "s_cselect_b32 s89, 0, 1\n\t"
        "buffer_load_dwordx4 v[24:27], v115, %[srd], s63 offen offset:2048 sc1\n\t"
        "s_mov_b32 s80, s88\n\t"
        "s_add_i32 s81, s92, s50\n\t"
        "s_sub_i32 s81, s81, 4\n\t"
        "s_cmp_le_i32 s81, s44\n\t"
        "s_cselect_b32 s80, s80, 0\n\t"
        "s_cmp_lt_i32 s50, %c[LC24]\n\t"
        "s_cselect_b32 s80, s80, 0\n\t"
        "s_lshl2_add_u32 s50, s80, s50\n\t"
        "buffer_load_dwordx4 v[28:31], v115, %[srd], s63 offen offset:3072 sc1\n\t"
        "s_sub_i32 s50, s50, s89\n\t"
        "s_max_i32 s50, s50, %c[LC]\n\t"
        "s_lshl_b32 s80, s92, 1\n\t"
        "s_sub_i32 s81, s80, s52\n\t"
        "s_ashr_i32 s82, s81, %c[UPSH]\n\t"
        "s_max_i32 s82, s82, %c[TRUP]\n\t"
        "s_add_i32 s82, s82, s52\n\t"
        "s_min_i32 s82, s82, s80\n\t"
        "buffer_load_dwordx4 v[32:35], v115, %[srd], s64 offen offset:0 sc1\n\t"
        "s_ashr_i32 s83, s81, 1\n\t"
        "s_min_i32 s83, s83, -%c[TRDN]\n\t"
        "s_add_i32 s83, s83, s52\n\t"
        "s_max_i32 s83, s83, s80\n\t"
        "s_cmp_gt_i32 s81, 0\n\t"
        "s_cselect_b32 s82, s82, s83\n\t"
        "s_cmp_lg_u32 s90, 0\n\t"
        "s_cselect_b32 s52, s82, s52\n\t"
        "buffer_load_dwordx4 v[36:39], v115, %[srd], s64 offen offset:1024 sc1\n\t"
        "s_add_i32 s80, s52, 1\n\t"
        "s_ashr_i32 s80, s80, 1\n\t"
        "s_add_i32 s45, s80, s50\n\t"
        "s_andn2_b32 s80, s86, s94\n\t"
        "s_cmp_lt_i32 s51, %c[LA24]\n\t"
        "s_cselect_b32 s80, s80, 0\n\t"
        "s_lshl2_add_u32 s51, s80, s51\n\t"
        "s_sub_i32 s51, s51, s89\n\t"
        "buffer_load_dwordx4 v[40:43], v115, %[srd], s64 offen offset:2048 sc1\n\t"
        "s_max_i32 s51, s51, %c[LA]\n\t"
        "s_lshl_b32 s80, s93, 1\n\t"
        "s_sub_i32 s81, s80, s53\n\t"
        "s_ashr_i32 s82, s81, %c[UPSH]\n\t"
        "s_max_i32 s82, s82, %c[TRUP]\n\t"
        "s_add_i32 s82, s82, s53\n\t"
        "s_min_i32 s82, s82, s80\n\t"
        "s_ashr_i32 s83, s81, 1\n\t"
        "buffer_load_dwordx4 v[44:47], v115, %[srd], s64 offen offset:3072 sc1\n\t"
        "s_min_i32 s83, s83, -%c[TRDN]\n\t"
        "s_add_i32 s83, s83, s53\n\t"
        "s_max_i32 s83, s83, s80\n\t"
        "s_cmp_gt_i32 s81, 0\n\t"
        "s_cselect_b32 s82, s82, s83\n\t"
        "s_cmp_lg_u32 s91, 0\n\t"
        "s_cselect_b32 s53, s82, s53\n\t"
        "s_add_i32 s80, s53, 1\n\t"
        "s_ashr_i32 s80, s80, 1\n\t"
        "s_add_i32 s47, s80, s51\n\t"
        "s_add_u32 s81, s47, %c[AUP]\n\t"
        "s_min_i32 s80, s95, s81\n\t"
        "s_max_i32 s47, s47, s80\n\t"
        "s_add_u32 s41, s42, s47\n\t"
        ".Lrc%=:\n\t"
        "s_waitcnt vmcnt(7)\n\t"
        "v_smfmac_f32_16x16x64_bf16 v[64:67], v[16:19], a[0:7], v113\n\t"
        "v_smfmac_f32_16x16x64_bf16 v[68:71], v[16:19], a[8:15], v113\n\t"
        "v_smfmac_f32_16x16x64_bf16 v[72:75], v[16:19], a[16:23], v113\n\t"
        "v_smfmac_f32_16x16x64_bf16 v[76:79], v[16:19], a[24:31], v113\n\t"
        "s_waitcnt vmcnt(6)\n\t"
        "v_smfmac_f32_16x16x64_bf16 v[64:67], v[20:23], a[32:39], v113\n\t"
        "v_smfmac_f32_16x16x64_bf16 v[68:71], v[20:23], a[40:47], v113\n\t"
        "v_smfmac_f32_16x16x64_bf16 v[72:75], v[20:23], a[48:55], v113\n\t"
        "v_smfmac_f32_16x16x64_bf16 v[76:79], v[20:23], a[56:63], v113\n\t"
        "s_waitcnt vmcnt(5)\n\t"
        "v_smfmac_f32_16x16x64_bf16 v[64:67], v[24:27], a[64:71], v113\n\t"
        "v_smfmac_f32_16x16x64_bf16 v[68:71], v[24:27], a[72:79], v113\n\t"
        "v_smfmac_f32_16x16x64_bf16 v[72:75], v[24:27], a[80:87], v113\n\t"
        "v_smfmac_f32_16x16x64_bf16 v[76:79], v[24:27], a[88:95], v113\n\t"
        "s_waitcnt vmcnt(4)\n\t"
        "v_smfmac_f32_16x16x64_bf16 v[64:67], v[28:31], a[96:103], v113\n\t"
        "v_smfmac_f32_16x16x64_bf16 v[68:71], v[28:31], a[104:111], v113\n\t"
        "v_smfmac_f32_16x16x64_bf16 v[72:75], v[28:31], a[112:119], v113\n\t"
        "v_smfmac_f32_16x16x64_bf16 v[76:79], v[28:31], a[120:127], v113\n\t"
        "s_waitcnt vmcnt(3)\n\t"
        "v_smfmac_f32_16x16x64_bf16 v[64:67], v[32:35], a[128:135], v113\n\t"
        "v_smfmac_f32_16x16x64_bf16 v[68:71], v[32:35], a[136:143], v113\n\t"
        "v_smfmac_f32_16x16x64_bf16 v[72:75], v[32:35], a[144:151], v113\n\t"
        "v_smfmac_f32_16x16x64_bf16 v[76:79], v[32:35], a[152:159], v113\n\t"
        "s_waitcnt vmcnt(2)\n\t"
        "v_smfmac_f32_16x16x64_bf16 v[64:67], v[36:39], a[160:167], v113\n\t"
        "v_smfmac_f32_16x16x64_bf16 v[68:71], v[36:39], a[168:175], v113\n\t"
        "v_smfmac_f32_16x16x64_bf16 v[72:75], v[36:39], a[176:183], v113\n\t"
        "v_smfmac_f32_16x16x64_bf16 v[76:79], v[36:39], a[184:191], v113\n\t"
        "s_waitcnt vmcnt(1)\n\t"
        "v_smfmac_f32_16x16x64_bf16 v[64:67], v[40:43], a[192:199], v113\n\t"
        "v_smfmac_f32_16x16x64_bf16 v[68:71], v[40:43], a[200:207], v113\n\t"
        "v_smfmac_f32_16x16x64_bf16 v[72:75], v[40:43], a[208:215], v113\n\t"
        "v_smfmac_f32_16x16x64_bf16 v[76:79], v[40:43], a[216:223], v113\n\t"
        "s_waitcnt vmcnt(0)\n\t"
        "v_smfmac_f32_16x16x64_bf16 v[64:67], v[44:47], a[224:231], v113\n\t"
        "s_nop 2\n\t"
        "v_smfmac_f32_16x16x64_bf16 v[68:71], v[44:47], a[232:239], v113\n\t"
        "s_cmp_gt_u32 %[w], 2\n\t"
        "s_nop 1\n\t"
        "v_smfmac_f32_16x16x64_bf16 v[72:75], v[44:47], a[240:247], v113\n\t"
        "v_add_f32_e32 v80, v64, v66\n\t"
        "s_nop 0\n\t"
        "v_smfmac_f32_16x16x64_bf16 v[76:79], v[44:47], a[248:255], v113\n\t"
        "v_add_f32_e32 v81, v65, v67\n\t"
        "v_add_f32_e32 v82, v68, v70\n\t"
        "v_add_f32_e32 v83, v69, v71\n\t"
        "s_mov_b32 exec_lo, 0\n\t"
        "s_nop 0\n\t"
        "v_add_f32_e32 v80, v72, v74\n\t"
        "v_add_f32_e32 v81, v73, v75\n\t"
        "s_nop 0\n\t"
        "v_pk_add_f32 v[82:83], v[76:77], v[78:79]\n\t"
        "s_mov_b32 exec_lo, -1\n\t"
        "s_nop 0\n\t"
        "v_permlane32_swap_b32_e32 v80, v82\n\t"
        "v_add_f32_e32 v80, v80, v82\n\t"
        "ds_write_b32 v119, v80\n\t"
        "v_permlane32_swap_b32_e32 v81, v83\n\t"
        "v_add_f32_e32 v81, v81, v83\n\t"
        "ds_write_b32 v119, v81 offset:128\n\t"
        "v_cmp_u_f32_e32 vcc, v80, v81\n\t"
        "s_nop 2\n\t"
        "v_mov_b32_e32 v104, v104\n\t"
        "s_cbranch_vccnz .Lfc%=\n\t"
        ".Lrd%=:\n\t"
        "v_mov_b32_e32 v104, v104\n\t"
        "s_cbranch_scc1 .Lno%=\n\t"
        "v_mov_b32_e32 v104, v104\n\t"
        "s_nop 2\n\t"
        "v_mov_b32_e32 v104, v104\n\t"
        "s_waitcnt lgkmcnt(0)\n\t"
        "s_barrier\n\t"
        "v_mov_b32_e32 v104, v104\n\t"
        "ds_read_b64 v[0:1], v120 offset:8192\n\t"
        "ds_read_b64 v[2:3], v120 offset:8704\n\t"
        "ds_read_b64 v[4:5], v120 offset:9216\n\t"
        "ds_read_b64 v[6:7], v120 offset:9728\n\t"
        "v_mov_b32_e32 v104, v104\n\t"
        "s_nop 2\n\t"
        "v_mov_b32_e32 v104, v104\n\t"
        "s_nop 2\n\t"
        "v_mov_b32_e32 v104, v104\n\t"
        "s_waitcnt lgkmcnt(2)\n\t"
        "v_pk_add_f32 v[0:1], v[0:1], v[2:3]\n\t"
        "s_waitcnt lgkmcnt(0)\n\t"
        "v_pk_add_f32 v[4:5], v[4:5], v[6:7]\n\t"
        "v_pk_add_f32 v[0:1], v[0:1], v[4:5]\n\t"
        "v_cvt_pk_bf16_f32 v0, v0, v1\n\t"
        "v_min_u32_e32 v0, -2, v0\n\t"
        "buffer_store_dword v0, v116, %[srd], s67 offen sc1\n\t"
        ".Lnp%=:\n\t"
        "s_memrealtime s[76:77]\n\t"
        "s_add_u32 s40, s40, 1\n\t"
        "s_waitcnt lgkmcnt(0)\n\t"
        "s_add_u32 s81, s76, %c[iVD]\n\t"
        "s_sub_u32 s80, s76, s41\n\t"
        "s_cmp_gt_i32 s80, %c[iTOL]\n\t"
        "s_cselect_b32 s69, s81, s41\n\t"
        "s_branch .Lloop%=\n\t"
        ".Lmv%=:\n\t"
        "s_mov_b32 s48, 1\n\t"
        "s_cmp_eq_u32 s54, %c[iTOL]\n\t"
        "s_cbranch_scc1 .Lm1%=\n\t"
        "s_max_i32 s80, s85, s56\n\t"
        "s_add_u32 s43, s41, s80\n\t"
        "s_add_u32 s43, s43, %c[iPEN0]\n\t"
        "s_mov_b32 s54, %c[iTOL]\n\t"
        "s_add_u32 s46, s46, %c[iX0C]\n\t"
        "s_add_u32 s95, s47, %c[iX0A]\n\t"
        "v_and_b32_e32 v4, 3, v88\n\t"
        "v_and_b32_e32 v5, 3, v90\n\t"
        "v_lshlrev_b32_e64 v4, v4, 1\n\t"
        "v_lshlrev_b32_e64 v5, v5, 1\n\t"
        "v_or_b32_e32 v4, v4, v5\n\t"
        "s_mov_b32 s80, 0\n\t"
        "v_and_b32_e32 v5, 1, v4\n\t"
        "v_cmp_ne_u32_e32 vcc, 0, v5\n\t"
        "s_nop 1\n\t"
        "s_cmp_lg_u64 vcc, 0\n\t"
        "s_cselect_b32 s81, 2, 0\n\t"
        "s_or_b32 s80, s80, s81\n\t"
        "v_and_b32_e32 v5, 2, v4\n\t"
        "v_cmp_ne_u32_e32 vcc, 0, v5\n\t"
        "s_nop 1\n\t"
        "s_cmp_lg_u64 vcc, 0\n\t"
        "s_cselect_b32 s81, 4, 0\n\t"
        "s_or_b32 s80, s80, s81\n\t"
        "v_and_b32_e32 v5, 4, v4\n\t"
        "v_cmp_ne_u32_e32 vcc, 0, v5\n\t"
        "s_nop 1\n\t"
        "s_cmp_lg_u64 vcc, 0\n\t"
        "s_cselect_b32 s81, 8, 0\n\t"
        "s_or_b32 s80, s80, s81\n\t"
        "v_and_b32_e32 v5, 8, v4\n\t"
        "v_cmp_ne_u32_e32 vcc, 0, v5\n\t"
        "s_nop 1\n\t"
        "s_cmp_lg_u64 vcc, 0\n\t"
        "s_cselect_b32 s81, 16, 0\n\t"
        "s_or_b32 s80, s80, s81\n\t"
        "s_lshr_b32 s80, 0x1adf1210, s80\n\t"
        "s_and_b32 s55, s80, 3\n\t"
        "s_branch .Lal%=\n\t"
        ".Lm1%=:\n\t"
        "s_add_u32 s80, s37, %c[iPEN2]\n\t"
        "s_min_i32 s80, s80, %c[iPENM]\n\t"
        "s_cmp_lg_u32 s37, 0\n\t"
        "s_cselect_b32 s37, s80, %c[iPEN]\n\t"
        "s_add_u32 s80, s56, s37\n\t"
        "s_max_i32 s80, s80, %c[iPMIN]\n\t"
        "s_add_u32 s43, s41, s80\n\t"
        "s_add_u32 s95, s93, %c[LA]\n\t"
        "s_add_u32 s46, s46, %c[iXR]\n\t"
        ".Lal%=:\n\t"
        "s_sub_u32 s80, s55, s43\n\t"
        "s_and_b32 s80, s80, %c[iPHM]\n\t"
        "s_add_u32 s43, s43, s80\n\t"
        "s_branch .Lsd%=\n\t"
        ".Lsf%=:\n\t"
        "s_mov_b32 exec_hi, -1\n\t"
        "s_sleep 2\n\t"
        "buffer_load_dwordx4 v[88:91], v112, %[srd], s66 offen sc1\n\t"
        "s_add_u32 s97, s97, 1\n\t"
        "s_cmp_lt_u32 s97, 0x100000\n\t"
        "s_cbranch_scc0 .Lgu%=\n\t"
        "s_waitcnt vmcnt(0)\n\t"
        "s_branch .Lsr%=\n\t"
        ".Lfa%=:\n\t"
        "v_max_u32_e32 v84, v0, v2\n\t"
        "v_max3_u32 v84, v84, v1, v3\n\t"
        "v_max3_u32 v84, v84, v4, v6\n\t"
        "v_max3_u32 v84, v84, v5, v7\n\t"
        "v_max3_u32 v84, v84, v8, v10\n\t"
        "v_max3_u32 v84, v84, v9, v11\n\t"
        "v_max3_u32 v84, v84, v12, v14\n\t"
        "v_max3_u32 v84, v84, v13, v15\n\t"
        "v_cmp_eq_u32_e32 vcc, -1, v84\n\t"
        "s_nop 1\n\t"
        "v_mov_b32_e32 v104, v104\n\t"
        "s_nop 0\n\t"
        "s_cbranch_vccz .Lra%=\n\t"
        "s_add_u32 s80, s40, 1\n\t"
        "v_mov_b32_e32 v80, 161792\n\t"
        "v_mov_b32_e32 v81, s80\n\t"
        "ds_write_b32 v80, v81\n\t"
        ".Lfp%=:\n\t"
        "s_sleep %c[ASLP]\n\t"
        "buffer_load_dwordx4 v[0:3], v112, %[srd], s57 offen sc1\n\t"
        "buffer_load_dwordx4 v[4:7], v112, %[srd], s58 offen sc1\n\t"
        "buffer_load_dwordx4 v[8:11], v112, %[srd], s59 offen sc1\n\t"
        "buffer_load_dwordx4 v[12:15], v112, %[srd], s60 offen sc1\n\t"
        "s_add_u32 s97, s97, 1\n\t"
        "s_cmp_lt_u32 s97, 0x100000\n\t"
        "s_cbranch_scc0 .Lgu%=\n\t"
        "s_waitcnt vmcnt(0)\n\t"
        "v_max_u32_e32 v84, v0, v2\n\t"
        "v_max3_u32 v84, v84, v1, v3\n\t"
        "v_max3_u32 v84, v84, v4, v6\n\t"
        "v_max3_u32 v84, v84, v5, v7\n\t"
        "v_max3_u32 v84, v84, v8, v10\n\t"
        "v_max3_u32 v84, v84, v9, v11\n\t"
        "v_max3_u32 v84, v84, v12, v14\n\t"
        "v_max3_u32 v84, v84, v13, v15\n\t"
        "v_cmp_eq_u32_e32 vcc, -1, v84\n\t"
        "s_nop 1\n\t"
        "v_mov_b32_e32 v104, v104\n\t"
        "s_nop 0\n\t"
        "s_cbranch_vccnz .Lfp%=\n\t"
        "v_mov_b32_e32 v48, 0\n\t"
        "v_mov_b32_e32 v49, 0\n\t"
        "v_mov_b32_e32 v50, 0\n\t"
        "v_mov_b32_e32 v51, 0\n\t"
        "v_mov_b32_e32 v52, 0\n\t"
        "v_mov_b32_e32 v53, 0\n\t"
        "v_mov_b32_e32 v54, 0\n\t"
        "v_mov_b32_e32 v55, 0\n\t"
        "v_mov_b32_e32 v56, 0\n\t"
        "v_mov_b32_e32 v57, 0\n\t"
        "v_mov_b32_e32 v58, 0\n\t"
        "v_mov_b32_e32 v59, 0\n\t"
        "v_mov_b32_e32 v60, 0\n\t"
        "v_mov_b32_e32 v61, 0\n\t"
        "v_mov_b32_e32 v62, 0\n\t"
        "v_mov_b32_e32 v63, 0\n\t"
        "s_nop 1\n\t"
        "s_branch .Lgo%=\n\t"
        ".Lfc%=:\n\t"
        "v_max_u32_e32 v84, v16, v18\n\t"
        "v_max3_u32 v84, v84, v20, v22\n\t"
        "v_max3_u32 v84, v84, v24, v26\n\t"
        "v_max3_u32 v84, v84, v28, v30\n\t"
        "v_max3_u32 v84, v84, v32, v34\n\t"
        "v_max3_u32 v84, v84, v36, v38\n\t"
        "v_max3_u32 v84, v84, v40, v42\n\t"
        "v_max3_u32 v84, v84, v44, v46\n\t"
        "v_cmp_eq_u32_e32 vcc, -1, v84\n\t"
        "s_nop 1\n\t"
        "v_mov_b32_e32 v104, v104\n\t"
        "s_nop 0\n\t"
        "s_cbranch_vccz .Lrd%=\n\t"
        "s_add_u32 s80, s40, 1\n\t"
        "v_mov_b32_e32 v80, 161796\n\t"
        "v_mov_b32_e32 v81, s80\n\t"
        "ds_write_b32 v80, v81\n\t"
        ".Lfq%=:\n\t"
        "s_sleep %c[ASLP]\n\t"
        "buffer_load_dwordx4 v[16:19], v115, %[srd], s63 offen offset:0 sc1\n\t"
        "buffer_load_dwordx4 v[20:23], v115, %[srd], s63 offen offset:1024 sc1\n\t"
        "buffer_load_dwordx4 v[24:27], v115, %[srd], s63 offen offset:2048 sc1\n\t"
        "buffer_load_dwordx4 v[28:31], v115, %[srd], s63 offen offset:3072 sc1\n\t"
        "buffer_load_dwordx4 v[32:35], v115, %[srd], s64 offen offset:0 sc1\n\t"
        "buffer_load_dwordx4 v[36:39], v115, %[srd], s64 offen offset:1024 sc1\n\t"
        "buffer_load_dwordx4 v[40:43], v115, %[srd], s64 offen offset:2048 sc1\n\t"
        "buffer_load_dwordx4 v[44:47], v115, %[srd], s64 offen offset:3072 sc1\n\t"
        "s_add_u32 s97, s97, 1\n\t"
        "s_cmp_lt_u32 s97, 0x100000\n\t"
        "s_cbranch_scc0 .Lgu%=\n\t"
        "s_waitcnt vmcnt(0)\n\t"
        "v_max_u32_e32 v84, v16, v18\n\t"
        "v_max3_u32 v84, v84, v20, v22\n\t"
        "v_max3_u32 v84, v84, v24, v26\n\t"
        "v_max3_u32 v84, v84, v28, v30\n\t"
        "v_max3_u32 v84, v84, v32, v34\n\t"
        "v_max3_u32 v84, v84, v36, v38\n\t"
        "v_max3_u32 v84, v84, v40, v42\n\t"
        "v_max3_u32 v84, v84, v44, v46\n\t"
        "v_cmp_eq_u32_e32 vcc, -1, v84\n\t"
        "s_nop 1\n\t"
        "v_mov_b32_e32 v104, v104\n\t"
        "s_nop 0\n\t"
        "s_cbranch_vccnz .Lfq%=\n\t"
        "v_mov_b32_e32 v64, 0\n\t"
        "v_mov_b32_e32 v65, 0\n\t"
        "v_mov_b32_e32 v66, 0\n\t"
        "v_mov_b32_e32 v67, 0\n\t"
        "v_mov_b32_e32 v68, 0\n\t"
        "v_mov_b32_e32 v69, 0\n\t"
        "v_mov_b32_e32 v70, 0\n\t"
        "v_mov_b32_e32 v71, 0\n\t"
        "v_mov_b32_e32 v72, 0\n\t"
        "v_mov_b32_e32 v73, 0\n\t"
        "v_mov_b32_e32 v74, 0\n\t"
        "v_mov_b32_e32 v75, 0\n\t"
        "v_mov_b32_e32 v76, 0\n\t"
        "v_mov_b32_e32 v77, 0\n\t"
        "v_mov_b32_e32 v78, 0\n\t"
        "v_mov_b32_e32 v79, 0\n\t"
        "s_nop 1\n\t"
        "s_branch .Lrc%=\n\t"
        ".Lno%=:\n\t"
        "s_waitcnt lgkmcnt(0)\n\t"
        "s_barrier\n\t"
        "s_branch .Lnp%=\n\t"
        ".Lta%=:\n\t"
        "s_waitcnt lgkmcnt(0)\n\t"
        "s_branch .Lxa%=\n\t"
        ".Ltc%=:\n\t"
        "s_waitcnt lgkmcnt(0)\n\t"
        "s_branch .Lxc%=\n\t"
        ".Lgu%=:\n\t"
        "s_mov_b64 exec, -1\n\t"
        "v_mov_b32_e32 v80, %c[ferr]\n\t"
        "v_mov_b32_e32 v81, 2\n\t"
        "buffer_store_dword v81, v80, %[wsd], 0 offen sc1\n\t"
        "s_endpgm\n\t"
        ".Lend%=:\n\t"
        "buffer_store_dword v80, v114, %[wsd], s72 offen sc1\n\t"
        "s_waitcnt vmcnt(0)\n\t"
        "s_barrier\n\t"
        "buffer_store_dword v86, v118, %[wsd], 0 offen sc1\n\t"
        "s_endpgm"
        : [nA] "+s"(nA), [nS] "+s"(nS), [nW] "+s"(nW), [nC] "+s"(nC), [b0] "+s"(b0), [b1] "+s"(b1), [pob] "+s"(pob)
        : [srd] "s"(rhd), [wsd] "s"(wsd), [w] "s"(w), [tag] "s"(tag), [kvo] "s"(kvo), [cal] "s"(cal), [sv] "s"(sv), [so] "s"(so), [m0b] "s"(m0b), [key] "s"(key), [tu] "s"(tu),
          [ferr] "i"(F_ERR), [iD1] "i"(D1), [iD2] "i"(D2), [LC] "i"(LC0), [LA] "i"(LA0), [LC24] "i"(LC0 + 24), [LA24] "i"(LA0 + 24), [E1] "i"(2 * (D1 - LC0)), [E2] "i"(2 * (D2 - LA0)),
          [iPEN] "i"(PEN), [iPEN2] "i"(PEN2), [iPENM] "i"(PEN + 2 * PEN2), [iPEN0] "i"(PEN0), [iTOL] "i"(TOL), [iCT0] "i"(CT0), [iX0C] "i"(X0C), [iX0A] "i"(X0A), [iPHM] "i"(PH_M),
          [iFT] "i"(-3), [iFN] "i"(FINE_N), [AUP] "i"(A_UP), [ASLP] "i"(A_SLP), [LDEC] "i"(L_DEC), [UPSH] "i"(UP_SH), [TRUP] "i"(TR_UP), [TRDN] "i"(TR_DN), [HSLP] "i"(H_SLP), [iPMIN] "i"(PMIN), [VSLP] "i"(V_SLP), [RSLP] "i"(R_SLP), [iVD] "i"(V_DEL), [iXR] "i"(XR), [NX0] "i"(NEARX), [NX1] "i"(8192 - NEARX),
          "{v[112:127]}"(pc), "{v[96:103]}"(pa), "{v[104:105]}"(pb)
        : "memory", "vcc", "scc", "m0", "s36", "s37", "s38", "s39", "s40", "s41", "s42", "s43", "s44", "s45", "s46", "s47", "s48", "s49", "s50", "s51", "s52", "s53", "s54", "s55", "s56", "s57", "s58", "s59", "s60", "s61", "s62", "s63", "s64", "s65", "s66", "s67", "s68", "s69", "s70", "s71", "s72", "s73", "s74", "s75", "s76", "s77", "s78", "s79", "s80", "s81", "s82", "s83", "s84", "s85", "s86", "s87", "s88", "s89", "s90", "s91", "s92", "s93", "s94", "s95", "s96", "s97", "s98", "s99");
    }
}

using f32x16 = __attribute__((ext_vector_type(16))) float;
constexpr int CV_RP = 80;
constexpr int CV_U = 96 * CV_RP;
constexpr int CV_KP = 4416;
constexpr int CV_K0 = 8 * CV_U;
__device__ __forceinline__ float lo16(unsigned x) { return __uint_as_float(x << 16); }
__device__ __forceinline__ float hi16(unsigned x) { return __uint_as_float(x & 0xFFFF0000u); }
__device__ __forceinline__ void prep_block(const Params& p, rsrc_t r, int k4, const int wv, const unsigned tag) {
    const int tid = otid(), w = wv, l = tid & 63;
    const int cu = (k4 & 3) * 32 + w * 8 + (l >> 3), oc0 = (k4 >> 2) * 8 + (l & 7);
    const rsrc_t rk = __builtin_amdgcn_make_buffer_rsrc((void*)p.Ak, 0, S * D * 2, 0x00020000), rv = __builtin_amdgcn_make_buffer_rsrc((void*)p.Av, 0, S * D * 2, 0x00020000);
    v4u sk[4], kn[11], vn[8], gw[2];
    auto fetch = [&](int oc) {
#pragma unroll
        for (int j = 0; j < 11; ++j) kn[j] = __builtin_amdgcn_raw_buffer_load_b128(rk, (unsigned)((oc * 8 + j - 3) * 2048 + cu * 16), 0, 2);
#pragma unroll
        for (int i = 0; i < 8; ++i) vn[i] = __builtin_amdgcn_raw_buffer_load_b128(rv, (unsigned)((oc * 8 + i) * 2048 + cu * 16), 0, 2); };
    fetch(oc0);
#pragma unroll
    for (int q = 0; q < 4; ++q) sk[q] = *(const v4u*)(p.shift_k + cu * 32 + q * 8);
#pragma unroll
    for (int q = 0; q < 2; ++q) gw[q] = *(const v4u*)(p.gate_w + q * 512 + l * 8);
    st4(r, tid < 128 ? F_RDY + k4 * 512 + tid * 4 : (tid == 128 && k4 == 0) ? F_CONV : 0xF0000000u, 0u);
#pragma unroll 1
    for (int it = 0; it < 4; ++it) {
        const int oc = oc0 + 64 * it, tk = (it * 32 + k4) * 16 + w;
        v4u kr[11], vr[8], xg[4][2];
#pragma unroll
        for (int j = 0; j < 11; ++j) kr[j] = kn[j];
#pragma unroll
        for (int i = 0; i < 8; ++i) vr[i] = vn[i];
        __builtin_amdgcn_sched_barrier(0);
#pragma unroll
        for (int i = 0; i < 4; ++i)
#pragma unroll
            for (int q = 0; q < 2; ++q) xg[i][q] = *(const v4u*)(p.Aq + (size_t)(tk + 4 * i) * D + q * 512 + l * 8);
        fetch(it < 3 ? oc + 64 : 4096);
        __builtin_amdgcn_sched_barrier(0);
#pragma unroll
        for (int c8 = 0; c8 < 8; ++c8) {
            const unsigned s01 = sk[c8 >> 1][(c8 & 1) * 2], s23 = sk[c8 >> 1][(c8 & 1) * 2 + 1];
            const float sv[4] = {hi16(s23), lo16(s23), hi16(s01), lo16(s01)};
            float kk[11], u[8];
#pragma unroll
            for (int j = 0; j < 11; ++j) kk[j] = (c8 & 1) ? hi16(kr[j][c8 >> 1]) : lo16(kr[j][c8 >> 1]);
            f32x2 acc[4];
#pragma unroll
            for (int j = 0; j < 4; ++j)
#pragma unroll
                for (int i = 0; i < 4; ++i) { const f32x2 t = rbf2((f32x2){kk[i + j], kk[i + j + 4]} * sv[j]); acc[i] = j == 0 ? t : rbf2(acc[i] + t); }
#pragma unroll
            for (int i = 0; i < 8; ++i) u[i] = acc[i & 3][i >> 2] * ((c8 & 1) ? hi16(vr[i][c8 >> 1]) : lo16(vr[i][c8 >> 1]));
            st16(r, W_U + (cu * 8 + c8) * 4096 + oc * 16, (v4u){pack2(u[7], u[6]), pack2(u[5], u[4]), pack2(u[3], u[2]), pack2(u[1], u[0])});
        }
        float ga[4];
#pragma unroll
        for (int i = 0; i < 4; ++i) { float acc = 0.f;
#pragma unroll
            for (int q = 0; q < 2; ++q)
#pragma unroll
                for (int e = 0; e < 4; ++e) { const unsigned xa = xg[i][q][e], gb = gw[q][e]; acc += rbfh(lo16(xa) * lo16(gb)); acc += rbfh(hi16(xa) * hi16(gb)); }
            ga[i] = acc; }
        for (int o = 32; o > 0; o >>= 1)
#pragma unroll
            for (int i = 0; i < 4; ++i) ga[i] += __shfl_xor(ga[i], o);
        const float z = rbfh(l == 0 ? ga[0] : l == 1 ? ga[1] : l == 2 ? ga[2] : ga[3]);
        st4(r, l < 4 ? W_GATE + (tk + 4 * l) * 4 : 0xF0000000u, __float_as_uint(rbfh(1.f / (1.f + expf(-z)))));
    }
    DRAIN(); __syncthreads();
    if (tid == 0) st4(r, F_PREP + k4 * 4, tag);
}
__device__ __forceinline__ bool conv_block(const Params& p, rsrc_t r, int k4, unsigned char* lds, const int wv, const int j, const unsigned tag) {
    (void)j;
    const int tid = otid(), w = wv, l = tid & 63;
    const rsrc_t rs = __builtin_amdgcn_make_buffer_rsrc((void*)p.ssm_k, 0, S * D * 2, 0x00020000);
    v4u kraw[2][4], uv[8]; unsigned dn;
    auto fetch_k = [&](int d0) {
#pragma unroll
        for (int ci = 0; ci < 2; ++ci)
#pragma unroll
            for (int i = 0; i < 4; ++i) kraw[ci][i] = __builtin_amdgcn_raw_buffer_load_b128(rs, (unsigned)((d0 + 2 * w + ci) * 4096 + (i * 64 + l) * 16), 0, 2);
        dn = *(const unsigned*)(p.ssm_D + (d0 & 1023) + 2 * w); };
    auto fetch_u = [&](unsigned o) {
#pragma unroll
        for (int i = 0; i < 8; ++i) uv[i] = ld16(r, o + i * 4096 + tid * 16); };
    fetch_k(k4 * 32);
    if (!wait_prep(r, l, tag)) return false;
    fetch_u(W_U + k4 * 32 * 4096);
#pragma unroll 1
    for (int it = 0; it < 4; ++it) {
    const int bb = k4 * 4 + it; const unsigned dd = dn;
    { const v4u z = {0u, 0u, 0u, 0u};
#pragma unroll
      for (int i = 0; i < 5; ++i) { const int q = tid + 256 * i; *(v4u*)(lds + (q / 160) * CV_U + (q % 160) * 16) = z; } }
#pragma unroll
    for (int i = 0; i < 8; ++i) *(v4u*)(lds + i * CV_U + (32 + (tid >> 2)) * CV_RP + (tid & 3) * 16) = uv[i];
    unsigned char* kc = lds + CV_K0 + w * 4 * CV_KP;
    auto copies = [&](int ci) {
        if (l < 32) *(unsigned*)(kc + l * 4) = 0u;
        if (l < 48) *(unsigned*)(kc + 128 + 4096 + l * 4) = 0u;
#pragma unroll
        for (int i = 0; i < 4; ++i) *(v4u*)(kc + 128 + (i * 64 + l) * 16) = kraw[ci][i];
        __builtin_amdgcn_wave_barrier();
#pragma unroll
        for (int it = 0; it < 9; ++it) { const int i = min(l + 64 * it, CV_KP / 8 - 2);
            const v2u e01 = *(const v2u*)(kc + i * 8), e23 = *(const v2u*)(kc + i * 8 + 8);
            const unsigned s0 = (e01.x >> 16) | (e01.y << 16), s1 = (e01.y >> 16) | (e23.x << 16), s2 = (e23.x >> 16) | (e23.y << 16);
            *(v2u*)(kc + CV_KP + i * 8) = (v2u){s0, s1};
            *(v2u*)(kc + 2 * CV_KP + i * 8) = (v2u){e01.y, e23.x};
            *(v2u*)(kc + 3 * CV_KP + i * 8) = (v2u){s1, s2}; }
        __builtin_amdgcn_wave_barrier(); };
    copies(0);
    __syncthreads();
    const int b = l & 31, kg = l >> 5, cc = (b + 1) & 3;
    float ys[32];
#pragma unroll
    for (int ci = 0; ci < 2; ++ci) {
        const int c8 = 2 * w + ci;
        if (ci == 1) { copies(1); __builtin_amdgcn_sched_barrier(0); fetch_k(it < 3 ? bb * 8 + 8 : 1024); fetch_u(it < 3 ? W_U + (bb * 8 + 8) * 4096 : 0xF0000000u); __builtin_amdgcn_sched_barrier(0); }
        f32x16 acc0, acc1, ad0, ad1;
#pragma unroll
        for (int i = 0; i < 16; ++i) { acc0[i] = 0.f; acc1[i] = 0.f; ad0[i] = 0.f; ad1[i] = 0.f; }
        typedef const __attribute__((address_space(3))) v2u* l2p;
        unsigned kb = (unsigned)(size_t)(kc + cc * CV_KP + (b - 8 * kg - 7 + 64 - cc) * 2) - 32, kb8 = kb + 8;
        asm volatile("" : "+v"(kb), "+v"(kb8));
        const unsigned char* ub = lds + c8 * CV_U + (32 + b) * CV_RP + kg * 16;
        v2u ka[4][2], kz[4][2]; bf16x8 fa[4], fz[4];
        auto ld_k = [&](int set, int un, int hi) { const int dl = un >> 1, kh = un & 1; const unsigned o = (unsigned)((32 * dl - 16 * kh + 16) * 2 + hi * 2048);
            if (hi) { kz[set][0] = *(l2p)(size_t)(kb + o); kz[set][1] = *(l2p)(size_t)(kb8 + o); } else { ka[set][0] = *(l2p)(size_t)(kb + o); ka[set][1] = *(l2p)(size_t)(kb8 + o); } };
        auto ld_f = [&](int set, int un) { const int dl = un >> 1, kh = un & 1;
            fa[set] = *(const bf16x8*)(ub + (32 - dl) * CV_RP + kh * 32); fz[set] = *(const bf16x8*)(ub - dl * CV_RP + kh * 32); };
#pragma unroll
        for (int q = 0; q < 2; ++q) { ld_k(q, q, 0); ld_f(q, q); ld_k(q, q, 1); }
        {
            const unsigned dv = ci ? dd >> 16 : dd & 0xFFFFu;
#pragma unroll
            for (int kh = 0; kh < 2; ++kh) { const int e = 7 - (b - 16 * kh - 8 * kg); const unsigned x = dv << ((e & 1) * 16); const int q = e >> 1;
                const v4u di = {q == 0 ? x : 0u, q == 1 ? x : 0u, q == 2 ? x : 0u, q == 3 ? x : 0u};
                asm volatile("s_nop 1\n\tv_mfma_f32_32x32x16_bf16 %0, %2, %4, %0\n\tv_mfma_f32_32x32x16_bf16 %1, %3, %4, %1" : "+v"(ad1), "+v"(ad0) : "v"(fa[kh]), "v"(fz[kh]), "v"(di)); }
        }
#pragma unroll 1
        for (int u0 = 0; u0 < 64; u0 += 4) {
#pragma unroll
            for (int q = 0; q < 4; ++q) {
                const int un = u0 + q + 2, ns = (q + 2) & 3;
                const v4u kav = {ka[q][0].x, ka[q][0].y, ka[q][1].x, ka[q][1].y}, kzv = {kz[q][0].x, kz[q][0].y, kz[q][1].x, kz[q][1].y};
                __builtin_amdgcn_sched_barrier(0);
                asm volatile("s_nop 1\n\tv_mfma_f32_32x32x16_bf16 %0, %1, %2, %0" : "+v"(acc1) : "v"(fa[q]), "v"(kav));
                ld_k(ns, un, 0);
                __builtin_amdgcn_sched_barrier(0);
                asm volatile("v_mfma_f32_32x32x16_bf16 %0, %1, %2, %0" : "+v"(acc0) : "v"(fz[q]), "v"(kav));
                ld_f(ns, un);
                __builtin_amdgcn_sched_barrier(0);
                asm volatile("v_mfma_f32_32x32x16_bf16 %0, %1, %2, %0" : "+v"(acc1) : "v"(fz[q]), "v"(kzv));
                ld_k(ns, un, 1);
            }
        }
        asm volatile("s_nop 7\n\ts_nop 7" : "+v"(acc0), "+v"(acc1), "+v"(ad0), "+v"(ad1));
#pragma unroll
        for (int i = 0; i < 32; ++i) { const float y = rbfh(i < 16 ? acc0[i] : acc1[i - 16]) + rbfh(i < 16 ? ad0[i] : ad1[i - 16]);
            ys[i] = ci == 0 ? y : __uint_as_float(pack2(ys[i], y)); }
    }
    __syncthreads();
#pragma unroll
    for (int i = 0; i < 32; ++i) { const int row = (i >> 4) * 32 + (i & 3) + 8 * ((i & 15) >> 2) + 4 * kg; *(float*)(lds + (row * 32 + b) * 16 + w * 4) = ys[i]; }
    __syncthreads();
#pragma unroll
    for (int i = 0; i < 8; ++i) { const int t = tid + 256 * i; st16(r, W_A + bb * 32768 + t * 16, *(const v4u*)(lds + t * 16)); }
    __syncthreads();
    }
    DRAIN(); __syncthreads();
    if (tid == 0) __hip_atomic_fetch_add((unsigned*)(p.ws + F_CONV), 1u, __ATOMIC_RELAXED, __HIP_MEMORY_SCOPE_AGENT);
    return true;
}

__device__ __forceinline__ void dma16(unsigned ldsdst, unsigned voff, const v4u& srd, unsigned soff) {
    asm volatile("s_mov_b32 m0, %0\n\ts_nop 0\n\tbuffer_load_dwordx4 %1, %2, %3 offen lds" :: "s"(ldsdst), "v"(voff), "s"(srd), "s"(soff) : "m0", "memory"); }
__device__ __forceinline__ void dma16n(unsigned ldsdst, unsigned voff, const v4u& srd, unsigned soff) {
    asm volatile("s_mov_b32 m0, %0\n\ts_nop 0\n\tbuffer_load_dwordx4 %1, %2, %3 offen nt lds" :: "s"(ldsdst), "v"(voff), "s"(srd), "s"(soff) : "m0", "memory"); }
__device__ __forceinline__ v4u pack_acc(const f32x4& a, const f32x4& b) {
    float x0, x1, x2, x3, y0, y1, y2, y3;
    asm volatile("v_accvgpr_read_b32 %0, %8\n\tv_accvgpr_read_b32 %1, %9\n\tv_accvgpr_read_b32 %2, %10\n\tv_accvgpr_read_b32 %3, %11\n\t"
                 "v_accvgpr_read_b32 %4, %12\n\tv_accvgpr_read_b32 %5, %13\n\tv_accvgpr_read_b32 %6, %14\n\tv_accvgpr_read_b32 %7, %15"
                 : "=v"(x0), "=v"(x1), "=v"(x2), "=v"(x3), "=v"(y0), "=v"(y1), "=v"(y2), "=v"(y3)
                 : "a"(a[0]), "a"(a[1]), "a"(a[2]), "a"(a[3]), "a"(b[0]), "a"(b[1]), "a"(b[2]), "a"(b[3]));
    return (v4u){pack2(x0, x1), pack2(x2, x3), pack2(y0, y1), pack2(y2, y3)}; }
#define SB() __builtin_amdgcn_sched_barrier(0)
#define WO_REGS "a128", "a129", "a130", "a131", "a132", "a133", "a134", "a135", "a136", "a137", "a138", "a139", "a140", "a141", "a142", "a143", "a144", "a145", "a146", "a147", "a148", "a149", "a150", "a151", "a152", "a153", "a154", "a155", "a156", "a157", "a158", "a159", "a160", "a161", "a162", "a163", "a164", "a165", "a166", "a167", "a168", "a169", "a170", "a171", "a172", "a173", "a174", "a175", "a176", "a177", "a178", "a179", "a180", "a181", "a182", "a183", "a184", "a185", "a186", "a187", "a188", "a189", "a190", "a191", "a192", "a193", "a194", "a195", "a196", "a197", "a198", "a199", "a200", "a201", "a202", "a203", "a204", "a205", "a206", "a207", "a208", "a209", "a210", "a211", "a212", "a213", "a214", "a215", "a216", "a217", "a218", "a219", "a220", "a221", "a222", "a223", "a224", "a225", "a226", "a227", "a228", "a229", "a230", "a231", "a232", "a233", "a234", "a235", "a236", "a237", "a238", "a239", "a240", "a241", "a242", "a243", "a244", "a245", "a246", "a247", "a248", "a249", "a250", "a251", "a252", "a253", "a254", "a255", "v192", "v193", "v194", "v195", "v196", "v197", "v198", "v199", "v200", "v201", "v202", "v203", "v204", "v205", "v206", "v207", "v208", "v209", "v210", "v211", "v212", "v213", "v214", "v215", "v216", "v217", "v218", "v219", "v220", "v221", "v222", "v223", "v224", "v225", "v226", "v227", "v228", "v229", "v230", "v231", "v232", "v233", "v234", "v235", "v236", "v237", "v238", "v239", "v240", "v241", "v242", "v243", "v244", "v245", "v246", "v247", "v248", "v249", "v250", "v251", "v252", "v253", "v254", "v255"
struct NoHook { __device__ void operator()(int) const {} };
template <int MODE, int MI, int NJ, int NS, int HX = 0, class HOOK = NoHook, int MN = MI>
__device__ __forceinline__ void gemm(rsrc_t r, const bf16_t* __restrict__ A, const bf16_t* __restrict__ B, int m0, int n0, unsigned char* lds, unsigned coff, const int wv,
                                     const int rot, const void* tk = nullptr,
                                     const bool have = false, const bf16_t* Bn = nullptr, int m0n = 0, int n0n = 0, HOOK&& hook = NoHook()) {
    constexpr int AB = MN * 4096, BB = NJ * 2048, B0 = (NS / 2) * AB, NB = NJ / 2, NM = MI * NJ, NR = MI + NJ, BP = NJ * 64;
    static_assert(NS % 2 == 0 && 32 % NS == 0 && B0 <= 65536 && NS * BB <= 65536 && (NJ == 4 || NJ == 8), "ring");
    const int tid = otid();
    const int w = wv, l = tid & 63, n16 = l & 15, g = l >> 4;
    const int wm = w >> 1, wn = w & 1;
    const unsigned lb = __builtin_amdgcn_readfirstlane((unsigned)(size_t)lds);
    const v4u sa = mk_srd(A, 2048 * 1024 * 2); v4u sb = mk_srd(B, 1024 * 4096 * 2);
    f32x4 acc[MI][NJ];
#pragma unroll
    for (int i = 0; i < MI; ++i)
#pragma unroll
        for (int j = 0; j < NJ; ++j) acc[i][j] = (f32x4){0.f, 0.f, 0.f, 0.f};
    unsigned va = (unsigned)((m0 + 8 * MI * w + (l >> 3)) * 2048 + (((l & 7) ^ (l >> 3)) << 4));
    unsigned vb[2];
    if (NJ == 8) { vb[0] = (unsigned)((8 * w + (l >> 5)) * 8192 + n0 * 2) + (unsigned)(((l & 31) << 4) ^ ((l >> 5) << 5) ^ (wn << 7)); vb[1] = vb[0] ^ 64u; }
    else { vb[0] = (unsigned)((8 * w + g) * 8192 + n0 * 2) + (unsigned)((n16 << 4) ^ (g << 5) ^ (wn << 7)); vb[1] = vb[0]; }
    auto rk = [&](int kt) { return (kt & ~31) | ((kt + rot) & 31); };
    unsigned la = lb + w * MI * 1024;
    auto st_a = [&](int buf, int kt, int q) { dma16(la + buf * AB + q * 1024, va, sa, (unsigned)(rk(kt) * 64 + q * 16384)); };
    auto st_b = [&](int buf, int kt, int x) { if ((B_NT >> (NJ == 4 ? 0 : MI == 4 ? 1 : 2)) & 1) dma16n(lb + B0 + buf * BB + (w * NB + x) * 1024, vb[x & 1], sb, (unsigned)((rk(kt) * 32 + x * (8 / NB)) * 8192));
        else dma16(lb + B0 + buf * BB + (w * NB + x) * 1024, vb[x & 1], sb, (unsigned)((rk(kt) * 32 + x * (8 / NB)) * 8192)); };
    const unsigned f = (unsigned)((n16 >> 2) | ((g & 1) << 2));
    constexpr int NQ = MODE == 1 ? NJ : NJ / 2;
    unsigned ra[2], rb[NQ];
    ra[0] = lb + (16 * MI * wm + n16) * 128 + ((g ^ (n16 & 7)) << 4); ra[1] = ra[0] ^ 64u;
    {   const unsigned row = lb + B0 + (8 * g + (n16 >> 2)) * BP + (NJ == 8 ? 256 * wn : 0), g0 = NJ == 8 ? 0u : 4u * wn;
#pragma unroll
        for (int j = 0; j < NQ; ++j) {
            if (MODE == 1) rb[j] = row + (((g0 + j) ^ f) << 5) + 8 * (n16 & 3);
            else rb[j] = row + (((g0 + 2 * j + ((n16 & 3) >> 1)) ^ f) << 5) + (n16 & 1) * 16;
            asm volatile("" : "+v"(rb[j])); } }
    bf16x8 af[2][MI], bfr[2][NJ];
    auto rd = [&](int set, int buf, int x) {
        if (x < NJ) { const unsigned a = rb[MODE == 1 ? x : (x >> 1)] + (MODE == 1 ? 0 : (x & 1) * 8) + buf * BB;
            const v2u lo = trb(a), hi = trb(a + 4 * BP); v4u v = {lo.x, lo.y, hi.x, hi.y}; bfr[set][x] = as_frag(v); }
        else af[set][x - NJ] = ldsa(ra[buf & 1] + (buf >> 1) * AB + (x - NJ) * 2048); };
    auto mf = [&](int set, int i, int j) {
        if (HX) asm volatile("v_mfma_f32_16x16x32_bf16 %0, %1, %2, %0" : "+a"(acc[i][j]) : "v"(MODE == 1 ? af[set][i] : bfr[set][j]), "v"(MODE == 1 ? bfr[set][j] : af[set][i]) : WO_REGS);
        else if (MODE == 1) asm volatile("v_mfma_f32_16x16x32_bf16 %0, %1, %2, %0" : "+a"(acc[i][j]) : "v"(af[set][i]), "v"(bfr[set][j]));
        else asm volatile("v_mfma_f32_16x16x32_bf16 %0, %1, %2, %0" : "+a"(acc[i][j]) : "v"(bfr[set][j]), "v"(af[set][i])); };
    if (!have) {
#pragma unroll
        for (int t = 0; t < NS; ++t) {
            if (t & 1) {
#pragma unroll
                for (int q = 0; q < MI; ++q) st_a(t >> 1, t - 1, q); }
#pragma unroll
            for (int q = 0; q < NB; ++q) st_b(t, t, q);
            if (HX) hook(t); }
    }
#pragma unroll
    for (int i = 0; i < MI; ++i)
#pragma unroll
        for (int j = 0; j < NJ; ++j) asm volatile("" : "+a"(acc[i][j]));
    asm volatile("s_nop 1" ::: "memory");
    if (!have) {
        asm volatile("s_waitcnt vmcnt(%c0)\n\ts_barrier" :: "i"((NS - 1) * (NB + HX) + (NS / 2 - 1) * MI) : "memory");
    }
#pragma unroll
    for (int x = 0; x < NR; ++x) rd(0, 0, x);
    constexpr int HEAD = NM / 8;
    const v4u st = mk_srd(tk, tk ? 32768 : 0); unsigned tdum = 0u;
#pragma unroll 1
    for (int k0 = 0; k0 < 32; k0 += NS) {
        int kb = k0 + NS;
        if (kb == 32) {
            kb = Bn ? 0 : 0x10100;
            if (Bn) { va += (unsigned)((m0n - m0 + 8 * (MN - MI) * w) * 2048); la = lb + w * MN * 1024; vb[0] += (unsigned)((n0n - n0) * 2); vb[1] += (unsigned)((n0n - n0) * 2); sb = mk_srd(Bn, 1024 * 4096 * 2); } }
        if (NS == 4) asm volatile("buffer_load_dword %0, %1, %2, %3 offen" : "+v"(tdum) : "v"(l < 32 ? (unsigned)l * 128u : 0xF0000000u), "s"(st), "s"((unsigned)k0 * 1024u) : "memory");
#pragma unroll
        for (int u = 0; u < NS; ++u) {
            SB();
#pragma unroll
            for (int q = 0; q < HEAD; ++q) mf(u & 1, q / NJ, q % NJ);
            SB();
            asm volatile("s_waitcnt vmcnt(%c0) lgkmcnt(0)\n\ts_barrier" :: "i"((NS - 2) * (NB + HX) + (NS / 2 - 1 - (u & 1)) * MI) : "memory");
            SB();
            constexpr int span = NM - HEAD;
#pragma unroll
            for (int q = HEAD; q < NM; ++q) {
                mf(u & 1, q / NJ, q % NJ);
                const int x = q - HEAD, nd = NB + ((u & 1) ? MI : 0);
#pragma unroll
                for (int y = 0; y < NR; ++y) if ((y * (span - HEAD)) / NR == x) rd((u & 1) ^ 1, (u + 1) % NS, y);
#pragma unroll
                for (int y = 0; y < nd; ++y) if ((y * span + span / 2) / nd == x) { if ((u & 1) && y < MI) st_a(u >> 1, kb + u - 1, y); else st_b(u, kb + u, y - ((u & 1) ? MI : 0)); }
                SB(); }
            if (MN > MI && (u & 1) && k0 == 32 - NS) {
#pragma unroll
                for (int y = MI; y < MN; ++y) st_a(u >> 1, kb + u - 1, y); }
            if (HX) hook(NS + k0 + u);
        }
    }
    asm volatile("s_nop 7\n\ts_nop 3" : "+v"(tdum) :: "memory");
    SB();
    const int col = n0 + 16 * NJ * wn, head = col >> 7, sub = (col & 127) >> 4, seg0 = (m0 >> 5) + (MI / 2) * wm;
#pragma unroll
    for (int sg = 0; sg < MI / 2; ++sg) {
        const unsigned base = coff + (unsigned)(((seg0 + sg) * 32 + head) * 8192 + l * 16);
#pragma unroll
        for (int q = 0; q < NJ; ++q) {
            if (HX) asm volatile("" ::: WO_REGS);
            if (MODE == 0) { const int tt = q / (NJ / 2), kk = q % (NJ / 2); st16(r, base + (tt * 4 + (sub >> 1) + kk) * 1024, pack_acc(acc[2 * sg + tt][2 * kk], acc[2 * sg + tt][2 * kk + 1])); }
            else st16(r, base + (sub + q) * 1024, pack_acc(acc[2 * sg][q], acc[2 * sg + 1][q])); }
    }
}

__device__ __forceinline__ unsigned min4(unsigned v) { return min(min(__builtin_amdgcn_readlane(v, 0), __builtin_amdgcn_readlane(v, 1)), min(__builtin_amdgcn_readlane(v, 2), __builtin_amdgcn_readlane(v, 3))); }
__device__ __forceinline__ void attn_block(const Params& p, rsrc_t r, unsigned char* lds, const int wv, const int h, const int j, const unsigned tag) {
    (void)j;
    const int l = otid() & 63, n16 = l & 15, g = l >> 4;
    v4u qv[2][4], kv[2][4], va[8];
    int nev = 0; (void)nev;
    auto fetch = [&](int seg) -> bool {
        for (unsigned spin = 0;; ++spin) { MEMBAR();
            const unsigned fv = ld4(r, F_KVQ + ((seg >> 2) * 32 + h) * 64 + min(l, 2) * 4);
            if (__all(fv == tag)) break;
            if (spin > (1u << 20)) { st4(r, F_ERR, 64u); return false; }
            __builtin_amdgcn_s_sleep(16); }
        MEMBAR();
        const unsigned o = (unsigned)((seg * 32 + h) * 8192 + l * 16);
#pragma unroll
        for (int i = 0; i < 8; ++i) qv[i >> 2][i & 3] = ld16(r, W_QX + o + i * 1024);
#pragma unroll
        for (int i = 0; i < 8; ++i) kv[i >> 2][i & 3] = ld16(r, W_KX + o + i * 1024);
#pragma unroll
        for (int i = 0; i < 8; ++i) va[i] = ld16(r, W_VF + o + i * 1024);
        return true; };
    if (!fetch(wv)) return;
    unsigned prog = 0u, tseen = rtnow() - 100000u;
#pragma unroll 1
    for (int seg = wv; seg < NSEG; seg += 4) {
        for (unsigned spin = 0; prog <= (unsigned)seg; ++spin) {
            const unsigned now = rtnow();
            if (spin > (1u << 22)) { st4(r, F_ERR, 32u); return; }
            if (prog + (seg >= EARLY_FROM ? 2u : 1u) + (now - tseen) / STEP_MIN <= (unsigned)seg) { __builtin_amdgcn_s_sleep(16); continue; }
            MEMBAR();
            const unsigned pg = min4(ld4(r, F_PROG + h * 128 + min(l, 2) * 8));
            if (pg != prog) { prog = pg; tseen = now; }
            if (prog <= (unsigned)seg) __builtin_amdgcn_s_sleep(P_SLP);
        }
        MEMBAR();
        const unsigned km = HO_KVM + (seg * 32 + h) * 4096;
        v4u kmv[4]; v2u pm[8];
        const v4u k0 = ld16(r, km + l * 16), k1 = ld16(r, km + 1024 + l * 16);
#pragma unroll
        for (int t = 0; t < 8; ++t) pm[t] = ld8(r, km + 2048 + t * 256 + n16 * 16 + (g & 1) * 8);
        *(v4u*)(lds + wv * 2048 + l * 16) = k0; *(v4u*)(lds + wv * 2048 + 1024 + l * 16) = k1;
        __builtin_amdgcn_wave_barrier();
#pragma unroll
        for (int ks = 0; ks < 4; ++ks) { const unsigned a = (unsigned)(size_t)lds + wv * 2048 + (32 * ks + 8 * g + (n16 >> 2)) * 16 + (n16 & 1) * 8; const v2u x0 = trb(a), x1 = trb(a + 64); kmv[ks] = (v4u){x0.x, x0.y, x1.x, x1.y}; }
#pragma unroll
        for (int qt = 0; qt < 2; ++qt) {
            f32x4 sc[3];
            asm volatile("s_nop 1\n\t"
                "v_mfma_f32_16x16x32_bf16 %0, %3, %15, 0\n\tv_mfma_f32_16x16x32_bf16 %1, %7, %15, 0\n\tv_mfma_f32_16x16x32_bf16 %2, %11, %15, 0\n\t"
                "v_mfma_f32_16x16x32_bf16 %0, %4, %16, %0\n\tv_mfma_f32_16x16x32_bf16 %1, %8, %16, %1\n\tv_mfma_f32_16x16x32_bf16 %2, %12, %16, %2\n\t"
                "v_mfma_f32_16x16x32_bf16 %0, %5, %17, %0\n\tv_mfma_f32_16x16x32_bf16 %1, %9, %17, %1\n\tv_mfma_f32_16x16x32_bf16 %2, %13, %17, %2\n\t"
                "v_mfma_f32_16x16x32_bf16 %0, %6, %18, %0\n\tv_mfma_f32_16x16x32_bf16 %1, %10, %18, %1\n\tv_mfma_f32_16x16x32_bf16 %2, %14, %18, %2\n\t"
                "s_nop 7"
                : "=&v"(sc[0]), "=&v"(sc[1]), "=&v"(sc[2])
                : "v"(kv[0][0]), "v"(kv[0][1]), "v"(kv[0][2]), "v"(kv[0][3]), "v"(kv[1][0]), "v"(kv[1][1]), "v"(kv[1][2]), "v"(kv[1][3]),
                  "v"(kmv[0]), "v"(kmv[1]), "v"(kmv[2]), "v"(kmv[3]), "v"(qv[qt][0]), "v"(qv[qt][1]), "v"(qv[qt][2]), "v"(qv[qt][3]));
            const int qi = 16 * qt + n16; float mx = -INFINITY;
#pragma unroll
            for (int t = 0; t < 3; ++t)
#pragma unroll
                for (int rr = 0; rr < 4; ++rr) { float v = rbfh(rbfh(sc[t][rr]) * 0.08838834764831845f);
                    if (t < 2) v = (16 * t + 4 * g + rr <= qi) ? v : -1000000000.0f; else if (g >= 2) v = -INFINITY;
                    sc[t][rr] = v; mx = fmaxf(mx, v); }
            mx = xor16_max(mx); mx = xor32_max(mx); float sum = 0.f;
#pragma unroll
            for (int t = 0; t < 3; ++t)
#pragma unroll
                for (int rr = 0; rr < 4; ++rr) { const float e = __expf(sc[t][rr] - mx); sc[t][rr] = e; sum += e; }
            sum = xor16_add(sum); sum = xor32_add(sum); const float inv = 1.0f / sum;
            const v4u p0 = {pack2(sc[0][0] * inv, sc[0][1] * inv), pack2(sc[0][2] * inv, sc[0][3] * inv), pack2(sc[1][0] * inv, sc[1][1] * inv), pack2(sc[1][2] * inv, sc[1][3] * inv)};
            const v2u p1 = {pack2(sc[2][0] * inv, sc[2][1] * inv), pack2(sc[2][2] * inv, sc[2][3] * inv)};
            f32x4 oa[8];
            asm volatile("s_nop 1\n\t"
                "v_mfma_f32_16x16x32_bf16 %0, %8, %24, 0\n\tv_mfma_f32_16x16x32_bf16 %1, %9, %24, 0\n\tv_mfma_f32_16x16x32_bf16 %2, %10, %24, 0\n\tv_mfma_f32_16x16x32_bf16 %3, %11, %24, 0\n\t"
                "v_mfma_f32_16x16x32_bf16 %4, %12, %24, 0\n\tv_mfma_f32_16x16x32_bf16 %5, %13, %24, 0\n\tv_mfma_f32_16x16x32_bf16 %6, %14, %24, 0\n\tv_mfma_f32_16x16x32_bf16 %7, %15, %24, 0\n\t"
                "v_mfma_f32_16x16x16_bf16 %0, %16, %25, %0\n\tv_mfma_f32_16x16x16_bf16 %1, %17, %25, %1\n\tv_mfma_f32_16x16x16_bf16 %2, %18, %25, %2\n\tv_mfma_f32_16x16x16_bf16 %3, %19, %25, %3\n\t"
                "v_mfma_f32_16x16x16_bf16 %4, %20, %25, %4\n\tv_mfma_f32_16x16x16_bf16 %5, %21, %25, %5\n\tv_mfma_f32_16x16x16_bf16 %6, %22, %25, %6\n\tv_mfma_f32_16x16x16_bf16 %7, %23, %25, %7\n\t"
                "s_nop 7"
                : "=&v"(oa[0]), "=&v"(oa[1]), "=&v"(oa[2]), "=&v"(oa[3]), "=&v"(oa[4]), "=&v"(oa[5]), "=&v"(oa[6]), "=&v"(oa[7])
                : "v"(va[0]), "v"(va[1]), "v"(va[2]), "v"(va[3]), "v"(va[4]), "v"(va[5]), "v"(va[6]), "v"(va[7]),
                  "v"(pm[0]), "v"(pm[1]), "v"(pm[2]), "v"(pm[3]), "v"(pm[4]), "v"(pm[5]), "v"(pm[6]), "v"(pm[7]), "v"(p0), "v"(p1));
#pragma unroll
            for (int u = 0; u < 4; ++u) {
                const f32x4 o0 = oa[2 * u], o1 = oa[2 * u + 1];
                const v4u pk = {pack2(o0[0], o0[1]), pack2(o0[2], o0[3]), pack2(o1[0], o1[1]), pack2(o1[2], o1[3])};
                st16(r, W_OX + (((seg * 2 + qt) * 32 + h) * 4 + u) * 1024 + l * 16, pk);
            }
        }
        DRAIN();
        { const int tt = 2 * seg + (l >> 5);
            __hip_atomic_fetch_add((unsigned*)(p.ws + F_RDY + (((tt & 3) * 32 + (l & 31)) * 32 + (tt >> 2)) * 4), 1u, __ATOMIC_RELAXED, __HIP_MEMORY_SCOPE_AGENT); }
        if (seg + 4 < NSEG && !fetch(seg + 4)) return;
    }
}

constexpr int OP_P = 80;
__device__ __forceinline__ void oproj_block(const Params& p, rsrc_t r, unsigned char* lds, const int wv, const int nt, const int rho, const int j, const unsigned tag, const unsigned g_t0) {
    unsigned g_t1 = 0u, g_t2 = 0u; (void)g_t0; (void)g_t1; (void)g_t2;
    (void)j;
    const int w = wv, l = otid() & 63, n16 = l & 15, g = l >> 4;
    const unsigned ldsbase = (unsigned)(size_t)lds;
    bf16x8 wof[32][2]; v32u wo[6];
    asm volatile("" : "={a[128:159]}"(wo[0]), "={a[160:191]}"(wo[1]), "={a[192:223]}"(wo[2]), "={a[224:255]}"(wo[3]), "={v[192:223]}"(wo[4]), "={v[224:255]}"(wo[5]));
#pragma unroll
    for (int kq = 0; kq < 32; ++kq) {
        const int ks = (kq + 16) & 31;
        const unsigned st = ldsbase + w * 8192 + (ks & 1) * 4096, sa = st + (l >> 2) * OP_P + (l & 3) * 16;
        switch (ks) {
            case 0: asm volatile("ds_write_b128 %0, a[128:131]\n\tds_write_b128 %0, a[132:135] offset:%c2" :: "v"(sa), "{a[128:159]}"(wo[0]), "i"(16 * OP_P) : "memory"); break;
            case 1: asm volatile("ds_write_b128 %0, a[136:139]\n\tds_write_b128 %0, a[140:143] offset:%c2" :: "v"(sa), "{a[128:159]}"(wo[0]), "i"(16 * OP_P) : "memory"); break;
            case 2: asm volatile("ds_write_b128 %0, a[144:147]\n\tds_write_b128 %0, a[148:151] offset:%c2" :: "v"(sa), "{a[128:159]}"(wo[0]), "i"(16 * OP_P) : "memory"); break;
            case 3: asm volatile("ds_write_b128 %0, a[152:155]\n\tds_write_b128 %0, a[156:159] offset:%c2" :: "v"(sa), "{a[128:159]}"(wo[0]), "i"(16 * OP_P) : "memory"); break;
            case 4: asm volatile("ds_write_b128 %0, a[160:163]\n\tds_write_b128 %0, a[164:167] offset:%c2" :: "v"(sa), "{a[160:191]}"(wo[1]), "i"(16 * OP_P) : "memory"); break;
            case 5: asm volatile("ds_write_b128 %0, a[168:171]\n\tds_write_b128 %0, a[172:175] offset:%c2" :: "v"(sa), "{a[160:191]}"(wo[1]), "i"(16 * OP_P) : "memory"); break;
            case 6: asm volatile("ds_write_b128 %0, a[176:179]\n\tds_write_b128 %0, a[180:183] offset:%c2" :: "v"(sa), "{a[160:191]}"(wo[1]), "i"(16 * OP_P) : "memory"); break;
            case 7: asm volatile("ds_write_b128 %0, a[184:187]\n\tds_write_b128 %0, a[188:191] offset:%c2" :: "v"(sa), "{a[160:191]}"(wo[1]), "i"(16 * OP_P) : "memory"); break;
            case 8: asm volatile("ds_write_b128 %0, a[192:195]\n\tds_write_b128 %0, a[196:199] offset:%c2" :: "v"(sa), "{a[192:223]}"(wo[2]), "i"(16 * OP_P) : "memory"); break;
            case 9: asm volatile("ds_write_b128 %0, a[200:203]\n\tds_write_b128 %0, a[204:207] offset:%c2" :: "v"(sa), "{a[192:223]}"(wo[2]), "i"(16 * OP_P) : "memory"); break;
            case 10: asm volatile("ds_write_b128 %0, a[208:211]\n\tds_write_b128 %0, a[212:215] offset:%c2" :: "v"(sa), "{a[192:223]}"(wo[2]), "i"(16 * OP_P) : "memory"); break;
            case 11: asm volatile("ds_write_b128 %0, a[216:219]\n\tds_write_b128 %0, a[220:223] offset:%c2" :: "v"(sa), "{a[192:223]}"(wo[2]), "i"(16 * OP_P) : "memory"); break;
            case 12: asm volatile("ds_write_b128 %0, a[224:227]\n\tds_write_b128 %0, a[228:231] offset:%c2" :: "v"(sa), "{a[224:255]}"(wo[3]), "i"(16 * OP_P) : "memory"); break;
            case 13: asm volatile("ds_write_b128 %0, a[232:235]\n\tds_write_b128 %0, a[236:239] offset:%c2" :: "v"(sa), "{a[224:255]}"(wo[3]), "i"(16 * OP_P) : "memory"); break;
            case 14: asm volatile("ds_write_b128 %0, a[240:243]\n\tds_write_b128 %0, a[244:247] offset:%c2" :: "v"(sa), "{a[224:255]}"(wo[3]), "i"(16 * OP_P) : "memory"); break;
            case 15: asm volatile("ds_write_b128 %0, a[248:251]\n\tds_write_b128 %0, a[252:255] offset:%c2" :: "v"(sa), "{a[224:255]}"(wo[3]), "i"(16 * OP_P) : "memory"); break;
            case 16: asm volatile("ds_write_b128 %0, v[192:195]\n\tds_write_b128 %0, v[196:199] offset:%c2" :: "v"(sa), "{v[192:223]}"(wo[4]), "i"(16 * OP_P) : "memory"); break;
            case 17: asm volatile("ds_write_b128 %0, v[200:203]\n\tds_write_b128 %0, v[204:207] offset:%c2" :: "v"(sa), "{v[192:223]}"(wo[4]), "i"(16 * OP_P) : "memory"); break;
            case 18: asm volatile("ds_write_b128 %0, v[208:211]\n\tds_write_b128 %0, v[212:215] offset:%c2" :: "v"(sa), "{v[192:223]}"(wo[4]), "i"(16 * OP_P) : "memory"); break;
            case 19: asm volatile("ds_write_b128 %0, v[216:219]\n\tds_write_b128 %0, v[220:223] offset:%c2" :: "v"(sa), "{v[192:223]}"(wo[4]), "i"(16 * OP_P) : "memory"); break;
            case 20: asm volatile("ds_write_b128 %0, v[224:227]\n\tds_write_b128 %0, v[228:231] offset:%c2" :: "v"(sa), "{v[224:255]}"(wo[5]), "i"(16 * OP_P) : "memory"); break;
            case 21: asm volatile("ds_write_b128 %0, v[232:235]\n\tds_write_b128 %0, v[236:239] offset:%c2" :: "v"(sa), "{v[224:255]}"(wo[5]), "i"(16 * OP_P) : "memory"); break;
            case 22: asm volatile("ds_write_b128 %0, v[240:243]\n\tds_write_b128 %0, v[244:247] offset:%c2" :: "v"(sa), "{v[224:255]}"(wo[5]), "i"(16 * OP_P) : "memory"); break;
            case 23: asm volatile("ds_write_b128 %0, v[248:251]\n\tds_write_b128 %0, v[252:255] offset:%c2" :: "v"(sa), "{v[224:255]}"(wo[5]), "i"(16 * OP_P) : "memory"); break;
        }
#pragma unroll
        for (int jn = 0; jn < 2; ++jn) {
            const int pt = ks < 24 ? OP_P : 64;
            const unsigned a0 = (ks < 24 ? st : WO_L + w * 16384 + (ks - 24) * 2048) + (4 * g + (n16 >> 2)) * pt + (16 * jn + 4 * (n16 & 3)) * 2;
            const v2u lo = trb(a0), hi = trb(a0 + 16 * pt);
            v4u v = {lo.x, lo.y, hi.x, hi.y}; asm volatile("" : "=a"(wof[ks][jn]) : "0"(as_frag(v))); }
    }
    __syncthreads();
    const v4u rd = mk_srd(p.ws, W_TOTAL), ro = mk_srd(p.out, S * D * 2), rx = mk_srd(p.Aq, S * D * 2);
    const unsigned OOB = 0xF0000000u;
    const int NT = 32, jn = w & 1;
    auto tile = [&](int i) -> unsigned { return (unsigned)(4 * i + rho); };
    const unsigned r_off = F_RDY + (rho * 32 + nt) * 128 + (l & 31) * 4;
    const unsigned a_b = W_OX + 8 * w * 4096 + l * 16;
    const int n0 = nt * 32 + 16 * jn + 4 * g;
    const unsigned c_b = W_A + (n0 >> 3) * 32768 + n16 * 16 + (g & 1) * 8, g_b = W_GATE + n16 * 4, o_b = (unsigned)((n16 * 1024 + n0) * 2);
    auto mix = [&](const v2u& av, const v2u& xv, unsigned gv, const f32x4& bs) -> v2u {
        const float gg = __uint_as_float(gv), og = rbfh(1.f - gg); float e[4];
#pragma unroll
        for (int k = 0; k < 4; ++k) { const unsigned aw = k < 2 ? av.x : av.y, xw = k < 2 ? xv.x : xv.y; e[k] = rbfh(gg * rbfh((k & 1) ? hi16(aw) * hi16(xw) : lo16(aw) * lo16(xw))) + rbfh(og * rbfh(bs[k])); }
        return (v2u){pack2(e[0], e[1]), pack2(e[2], e[3])}; };
    const unsigned s_w = ldsbase + w * 2048 + l * 16, s_r = ldsbase + jn * 1024 + l * 16;
    unsigned long long known = 0ull; int nev = 0; (void)nev;
    bool have = false, pend = false; v4u af[32]; v2u pa = {0u, 0u}, px = {0u, 0u}; unsigned pg = 0u, cf2 = 0u;
    auto finish = [&](int i, bool on) {
        v2u o = {0u, 0u};
        if (on) {
            const __attribute__((address_space(3))) f32x4* q = (const __attribute__((address_space(3))) f32x4*)(size_t)(s_r + (unsigned)(i & 1) * 8192u);
            const f32x4 bs = (q[0] + q[128]) + (q[256] + q[384]);
            o = mix(pa, px, pg, bs);
        }
        asm volatile("buffer_store_dwordx2 %0, %1, %2, 0 offen" :: "v"(o), "v"(on ? o_b + tile(i) * 32768u : OOB), "s"(ro) : "memory"); };
    if (!wait_prep(r, l, tag) || !wait_word(r, F_CONV, 32u, 256u)) ENDPGM();
#pragma unroll 1
    for (int i = 0; i < NT; ++i) {
        if (!have) {
            for (unsigned spin = 0; !((known >> i) & 1ull); ++spin) { MEMBAR();
                known = __ballot(ld4(r, r_off) == 32u);
                if ((known >> i) & 1ull) break;
                if (spin > (1u << 21)) { st4(r, F_ERR, 128u); ENDPGM(); }
                __builtin_amdgcn_s_sleep(O_SLP); }
            const unsigned a0 = a_b + tile(i) * 131072u;
#pragma unroll
            for (int q = 0; q < 8; ++q)
                asm volatile("buffer_load_dwordx4 %0, %4, %5, %6 offen sc1\n\tbuffer_load_dwordx4 %1, %4, %5, %6 offen offset:1024 sc1\n\t"
                             "buffer_load_dwordx4 %2, %4, %5, %6 offen offset:2048 sc1\n\tbuffer_load_dwordx4 %3, %4, %5, %6 offen offset:3072 sc1"
                             : "=&v"(af[4 * q]), "=&v"(af[4 * q + 1]), "=&v"(af[4 * q + 2]), "=&v"(af[4 * q + 3]) : "v"(a0), "s"(rd), "s"(q * 4096) : "memory");
            asm volatile("buffer_store_dword %0, %0, %1, 0 offen\n\tbuffer_store_dword %0, %0, %1, 0 offen" :: "v"(OOB), "s"(ro) : "memory");
        }
        const bool fin = (w >> 1) == (i & 1);
        const bool more = i + 1 < NT && ((known >> (i + 1)) & 1ull);
        const unsigned an = more ? a_b + tile(i + 1) * 131072u : OOB;
        unsigned cfl, fg; v2u fa, fx;
        asm volatile("buffer_load_dwordx2 %0, %1, %2, 0 offen" : "=&v"(fx) : "v"(fin ? o_b + tile(i) * 32768u : OOB), "s"(rx) : "memory");
        asm volatile("buffer_load_dword %0, %3, %6, 0 offen sc1\n\tbuffer_load_dwordx2 %1, %4, %6, 0 offen sc1\n\tbuffer_load_dword %2, %5, %6, 0 offen sc1"
                     : "=&v"(cfl), "=&v"(fa), "=&v"(fg) : "v"(((known >> (i + 2)) & 1ull) ? OOB : r_off), "v"(fin ? c_b + tile(i) * 256u : OOB), "v"(fin ? g_b + tile(i) * 64u : OOB), "s"(rd) : "memory");
        f32x4 acc0 = {0.f, 0.f, 0.f, 0.f}, acc1 = {0.f, 0.f, 0.f, 0.f};
        asm volatile("s_nop 1" : "+v"(acc0), "+v"(acc1));
#pragma unroll
        for (int q = 0; q < 8; ++q) {
            if (q == 2) finish(i - 1, pend);
            if (q == 6) asm volatile("buffer_load_dword %0, %1, %2, 0 offen sc1" : "+v"(cf2) : "v"(more ? OOB : r_off), "s"(rd) : "memory");
            asm volatile(
                "s_waitcnt vmcnt(37)\n\tv_mfma_f32_16x16x32_bf16 %4, %6, %0, %4\n\tv_mfma_f32_16x16x32_bf16 %5, %7, %0, %5\n\tbuffer_load_dwordx4 %0, %14, %15, %16 offen sc1\n\t"
                "s_waitcnt vmcnt(37)\n\tv_mfma_f32_16x16x32_bf16 %4, %8, %1, %4\n\tv_mfma_f32_16x16x32_bf16 %5, %9, %1, %5\n\tbuffer_load_dwordx4 %1, %14, %15, %16 offen offset:1024 sc1\n\t"
                "s_waitcnt vmcnt(37)\n\tv_mfma_f32_16x16x32_bf16 %4, %10, %2, %4\n\tv_mfma_f32_16x16x32_bf16 %5, %11, %2, %5\n\tbuffer_load_dwordx4 %2, %14, %15, %16 offen offset:2048 sc1\n\t"
                "s_waitcnt vmcnt(37)\n\tv_mfma_f32_16x16x32_bf16 %4, %12, %3, %4\n\tv_mfma_f32_16x16x32_bf16 %5, %13, %3, %5\n\tbuffer_load_dwordx4 %3, %14, %15, %16 offen offset:3072 sc1"
                : "+v"(af[4 * q]), "+v"(af[4 * q + 1]), "+v"(af[4 * q + 2]), "+v"(af[4 * q + 3]), "+v"(acc0), "+v"(acc1)
                : "a"(wof[4 * q][0]), "a"(wof[4 * q][1]), "a"(wof[4 * q + 1][0]), "a"(wof[4 * q + 1][1]), "a"(wof[4 * q + 2][0]), "a"(wof[4 * q + 2][1]), "a"(wof[4 * q + 3][0]), "a"(wof[4 * q + 3][1]),
                  "v"(an), "s"(rd), "s"(q * 4096) : "memory"); }
        const unsigned po = (unsigned)(i & 1) * 8192u;
        asm volatile("s_nop 7\n\tds_write_b128 %6, %4\n\tds_write_b128 %6, %5 offset:1024\n\ts_waitcnt vmcnt(34) lgkmcnt(0)\n\ts_barrier"
                     : "+v"(cfl), "+v"(fa), "+v"(fg), "+v"(fx) : "v"(acc0), "v"(acc1), "v"(s_w + po) : "memory");
        known |= __ballot(cfl == 32u);
        pa = fa; px = fx; pg = fg; pend = fin && more;
        if (!more) finish(i, fin);
        have = more;
        if (!more) {
            asm volatile("s_waitcnt vmcnt(1)" : "+v"(cf2), "+v"(af[0]), "+v"(af[1]), "+v"(af[2]), "+v"(af[3]), "+v"(af[4]), "+v"(af[5]), "+v"(af[6]), "+v"(af[7]), "+v"(af[8]), "+v"(af[9]), "+v"(af[10]), "+v"(af[11]),
                         "+v"(af[12]), "+v"(af[13]), "+v"(af[14]), "+v"(af[15]) :: "memory");
            known |= __ballot(cf2 == 32u);
        }
    }
    if (nt == 0 && rho == 0 && w == 0) st4(r, l == 0 ? F_EP : OOB, tag);
}

__device__ __forceinline__ void bulk_block(const Params& p, rsrc_t r, unsigned char* lds, const int wv, const int j, const unsigned tag, const unsigned g_t0) {
    const int x = (j & 7) >> 1, i32 = (j >> 3) * 2 + (j & 1);
    auto flags = [&](int mt, int nmt, int h0, int nh, int mat) {
        DRAIN(); __syncthreads();
        const int tid = otid(); if (tid < nmt * nh) st4(r, F_KVQ + ((mt + tid / nh) * 32 + h0 + tid % nh) * 64 + mat * 4, tag); };
#pragma unroll 1
    for (int t = 0; t < 2; ++t) {
        const int mt = 2 * t + (i32 & 1), mat = (i32 >> 1) & 1, hd = 8 * x + (i32 >> 2), rot = ((i32 >> 1) & 7) * 2;
        const bf16_t* Bm = mat ? p.Wv : p.Wk;
        if (mat) gemm<1, 4, 4, 8>(r, p.Aq, Bm, mt * 128, hd * 128, lds, W_VF, wv, rot, nullptr, t == 1, t == 0 ? Bm : nullptr, (mt + 2) * 128, hd * 128);
        else gemm<0, 4, 4, 8>(r, p.Aq, Bm, mt * 128, hd * 128, lds, W_KX, wv, rot, nullptr, t == 1, t == 0 ? Bm : nullptr, (mt + 2) * 128, hd * 128);
        flags(mt, 1, hd, 1, mat);
    }
    const int hp = 4 * x + (i32 >> 3), q8 = i32 & 7, rot = ROT2 * (i32 >> 2);
    {
        const int m = q8 < 4 ? 4 + (q8 & 1) : q8 - 4, mat = q8 < 4 ? q8 >> 1 : 2;
        const bf16_t* Bn = mat == 2 ? p.Wq : mat == 1 ? p.Wv : p.Wk;
        const int mt = 4 + (q8 & 3), mv = q8 >> 2;
        const bf16_t* tk = (wv == 1 && j < 32) ? p.Aq + (64 + j) * 16384 : nullptr;
        if (mv) gemm<1, 4, 8, 4, 0, NoHook, 8>(r, p.Aq, p.Wv, mt * 128, hp * 256, lds, W_VF, wv, rot, tk, false, Bn, m * 256, hp * 256);
        else gemm<0, 4, 8, 4, 0, NoHook, 8>(r, p.Aq, p.Wk, mt * 128, hp * 256, lds, W_KX, wv, rot, tk, false, Bn, m * 256, hp * 256);
        flags(mt, 1, 2 * hp, 2, mv);
        const bf16_t* tl = (wv == 1 && j < 32) ? p.Aq + (96 + j) * 16384 : nullptr;
        if (mat == 1) gemm<1, 8, 8, 4>(r, p.Aq, Bn, m * 256, hp * 256, lds, W_VF, wv, rot, tl, true);
        else gemm<0, 8, 8, 4>(r, p.Aq, Bn, m * 256, hp * 256, lds, mat == 2 ? W_QX : W_KX, wv, rot, tl, true);
        flags(2 * m, 2, 2 * hp, 2, mat);
    }
    {
        unsigned wi = 0u;
        const int l = otid() & 63; const v4u sw = mk_srd(p.Wo, 4096 * 1024 * 2); const unsigned wa = (unsigned)((wv * 1024 + (l >> 2)) * 2048 + i32 * 64 + (l & 3) * 16);
        unsigned long long tab;
        asm volatile("s_getpc_b64 %0\n\ts_branch .Lwo%=\n\t"
                     "buffer_load_dwordx4 a[128:131], v191, s[92:95], s91 offen\n\ts_setpc_b64 s[96:97]\n\t" "buffer_load_dwordx4 a[132:135], v191, s[92:95], s91 offen\n\ts_setpc_b64 s[96:97]\n\t"
                     "buffer_load_dwordx4 a[136:139], v191, s[92:95], s91 offen\n\ts_setpc_b64 s[96:97]\n\t" "buffer_load_dwordx4 a[140:143], v191, s[92:95], s91 offen\n\ts_setpc_b64 s[96:97]\n\t"
                     "buffer_load_dwordx4 a[144:147], v191, s[92:95], s91 offen\n\ts_setpc_b64 s[96:97]\n\t" "buffer_load_dwordx4 a[148:151], v191, s[92:95], s91 offen\n\ts_setpc_b64 s[96:97]\n\t"
                     "buffer_load_dwordx4 a[152:155], v191, s[92:95], s91 offen\n\ts_setpc_b64 s[96:97]\n\t" "buffer_load_dwordx4 a[156:159], v191, s[92:95], s91 offen\n\ts_setpc_b64 s[96:97]\n\t"
                     "buffer_load_dwordx4 a[160:163], v191, s[92:95], s91 offen\n\ts_setpc_b64 s[96:97]\n\t" "buffer_load_dwordx4 a[164:167], v191, s[92:95], s91 offen\n\ts_setpc_b64 s[96:97]\n\t"
                     "buffer_load_dwordx4 a[168:171], v191, s[92:95], s91 offen\n\ts_setpc_b64 s[96:97]\n\t" "buffer_load_dwordx4 a[172:175], v191, s[92:95], s91 offen\n\ts_setpc_b64 s[96:97]\n\t"
                     "buffer_load_dwordx4 a[176:179], v191, s[92:95], s91 offen\n\ts_setpc_b64 s[96:97]\n\t" "buffer_load_dwordx4 a[180:183], v191, s[92:95], s91 offen\n\ts_setpc_b64 s[96:97]\n\t"
                     "buffer_load_dwordx4 a[184:187], v191, s[92:95], s91 offen\n\ts_setpc_b64 s[96:97]\n\t" "buffer_load_dwordx4 a[188:191], v191, s[92:95], s91 offen\n\ts_setpc_b64 s[96:97]\n\t"
                     "buffer_load_dwordx4 a[192:195], v191, s[92:95], s91 offen\n\ts_setpc_b64 s[96:97]\n\t" "buffer_load_dwordx4 a[196:199], v191, s[92:95], s91 offen\n\ts_setpc_b64 s[96:97]\n\t"
                     "buffer_load_dwordx4 a[200:203], v191, s[92:95], s91 offen\n\ts_setpc_b64 s[96:97]\n\t" "buffer_load_dwordx4 a[204:207], v191, s[92:95], s91 offen\n\ts_setpc_b64 s[96:97]\n\t"
                     "buffer_load_dwordx4 a[208:211], v191, s[92:95], s91 offen\n\ts_setpc_b64 s[96:97]\n\t" "buffer_load_dwordx4 a[212:215], v191, s[92:95], s91 offen\n\ts_setpc_b64 s[96:97]\n\t"
                     "buffer_load_dwordx4 a[216:219], v191, s[92:95], s91 offen\n\ts_setpc_b64 s[96:97]\n\t" "buffer_load_dwordx4 a[220:223], v191, s[92:95], s91 offen\n\ts_setpc_b64 s[96:97]\n\t"
                     "buffer_load_dwordx4 a[224:227], v191, s[92:95], s91 offen\n\ts_setpc_b64 s[96:97]\n\t" "buffer_load_dwordx4 a[228:231], v191, s[92:95], s91 offen\n\ts_setpc_b64 s[96:97]\n\t"
                     "buffer_load_dwordx4 a[232:235], v191, s[92:95], s91 offen\n\ts_setpc_b64 s[96:97]\n\t" "buffer_load_dwordx4 a[236:239], v191, s[92:95], s91 offen\n\ts_setpc_b64 s[96:97]\n\t"
                     "buffer_load_dwordx4 a[240:243], v191, s[92:95], s91 offen\n\ts_setpc_b64 s[96:97]\n\t" "buffer_load_dwordx4 a[244:247], v191, s[92:95], s91 offen\n\ts_setpc_b64 s[96:97]\n\t"
                     "buffer_load_dwordx4 a[248:251], v191, s[92:95], s91 offen\n\ts_setpc_b64 s[96:97]\n\t" "buffer_load_dwordx4 a[252:255], v191, s[92:95], s91 offen\n\ts_setpc_b64 s[96:97]\n\t"
                     "buffer_load_dwordx4 v[192:195], v191, s[92:95], s91 offen\n\ts_setpc_b64 s[96:97]\n\t" "buffer_load_dwordx4 v[196:199], v191, s[92:95], s91 offen\n\ts_setpc_b64 s[96:97]\n\t"
                     "buffer_load_dwordx4 v[200:203], v191, s[92:95], s91 offen\n\ts_setpc_b64 s[96:97]\n\t" "buffer_load_dwordx4 v[204:207], v191, s[92:95], s91 offen\n\ts_setpc_b64 s[96:97]\n\t"
                     "buffer_load_dwordx4 v[208:211], v191, s[92:95], s91 offen\n\ts_setpc_b64 s[96:97]\n\t" "buffer_load_dwordx4 v[212:215], v191, s[92:95], s91 offen\n\ts_setpc_b64 s[96:97]\n\t"
                     "buffer_load_dwordx4 v[216:219], v191, s[92:95], s91 offen\n\ts_setpc_b64 s[96:97]\n\t" "buffer_load_dwordx4 v[220:223], v191, s[92:95], s91 offen\n\ts_setpc_b64 s[96:97]\n\t"
                     "buffer_load_dwordx4 v[224:227], v191, s[92:95], s91 offen\n\ts_setpc_b64 s[96:97]\n\t" "buffer_load_dwordx4 v[228:231], v191, s[92:95], s91 offen\n\ts_setpc_b64 s[96:97]\n\t"
                     "buffer_load_dwordx4 v[232:235], v191, s[92:95], s91 offen\n\ts_setpc_b64 s[96:97]\n\t" "buffer_load_dwordx4 v[236:239], v191, s[92:95], s91 offen\n\ts_setpc_b64 s[96:97]\n\t"
                     "buffer_load_dwordx4 v[240:243], v191, s[92:95], s91 offen\n\ts_setpc_b64 s[96:97]\n\t" "buffer_load_dwordx4 v[244:247], v191, s[92:95], s91 offen\n\ts_setpc_b64 s[96:97]\n\t"
                     "buffer_load_dwordx4 v[248:251], v191, s[92:95], s91 offen\n\ts_setpc_b64 s[96:97]\n\t" "buffer_load_dwordx4 v[252:255], v191, s[92:95], s91 offen\n\ts_setpc_b64 s[96:97]\n\t"
                     "buffer_load_dwordx4 v191, s[92:95], s91 offen lds\n\ts_setpc_b64 s[96:97]\n.Lwo%=:" : "=s"(tab));
        auto hook = [&](int) {
            const unsigned q = min(wi, 63u);
            asm volatile("s_mov_b32 m0, %1\n\ts_swappc_b64 s[96:97], %0"
                         :: "s"(tab + (min(q, 48u) * 12u + 4u)), "s"(WO_L + wv * 16384 + (q - 48u) * 1024u), "{v191}"(wa), "{s[92:95]}"(sw), "{s91}"(q * 32768u) : "memory", "m0", "s96", "s97", WO_REGS);
            ++wi; };
#pragma unroll 1
        for (int t = 0; t < 2; ++t) {
            const int mt = t ? 8 + q8 : 12 + (q8 & 3), mv = t ? 2 : q8 >> 2;
            if (mv == 1) gemm<1, 4, 8, 4, 1>(r, p.Aq, p.Wv, mt * 128, hp * 256, lds, W_VF, wv, rot, nullptr, false, p.Wq, (8 + q8) * 128, hp * 256, hook);
            else gemm<0, 4, 8, 4, 1>(r, p.Aq, t ? p.Wq : p.Wk, mt * 128, hp * 256, lds, t ? W_QX : W_KX, wv, rot, nullptr, t == 1, t ? nullptr : p.Wq, (8 + q8) * 128, hp * 256, hook);
            flags(mt, 1, 2 * hp, 2, mv);
        }
        oproj_block(p, r, lds, wv, i32, x, j, tag, g_t0);
    }
}

__device__ __forceinline__ void help_block(const Params& p, rsrc_t r, unsigned char* lds, const int wv, const int k, const unsigned tag) {
    const int j = -32 + k; (void)j;
    {
        const unsigned th = rtnow() + HELP_D, OOB = 0xF0000000u;
        const int l = otid() & 63, n16 = l & 15;
        const rsrc_t rh = __builtin_amdgcn_make_buffer_rsrc(p.ws + hx_base(p.ws), 0, U_END * 16384, 0x00020000);
        const unsigned un = (unsigned)(wv * 32 + k) * 16384u, ca = l == 0 ? un + CAL_O : OOB;
        v4u z[8]; unsigned best0 = ~0u, best1 = ~0u;
#pragma unroll
        for (int i = 0; i < 8; ++i) z[i] = (v4u){~0u, ~0u, ~0u, ~0u};
        for (int i = 0; i < CAL_N; ++i) {
            const unsigned t0 = (unsigned)__builtin_amdgcn_s_memtime(); __builtin_amdgcn_s_waitcnt(0);
            st16(rh, ca, z[0]); DRAIN();
            const unsigned t1 = (unsigned)__builtin_amdgcn_s_memtime(); __builtin_amdgcn_s_waitcnt(0);
            st16(rh, ca + 8192, z[0]); DRAIN();
            const unsigned t2 = (unsigned)__builtin_amdgcn_s_memtime(); __builtin_amdgcn_s_waitcnt(0);
            best0 = min(best0, t1 - t0); best1 = min(best1, t2 - t1); }
        if (wv == 0 && k < 6) {
#pragma unroll
            for (int i = 0; i < 8; ++i) z[i] = __builtin_elementwise_min(*(const v4u*)(p.mem0 + ((n16 & 1) + 2 * (n16 >> 2)) * 1024 + ((k & 1) * 8 + i) * 64 + (l >> 4) * 16 + (n16 & 2) * 4), (v4u){~1u, ~1u, ~1u, ~1u}); }
#pragma unroll
        for (int i = 0; i < 16; ++i) st16(rh, un + i * 1024 + l * 16, z[i & 7]);
        st4(r, (wv == 3 && l < 3) ? F_PROG + k * 128 + l * 8 : OOB, l ? 63u : 0u);
        DRAIN();
        st8(r, l == 0 ? F_CAL + k * 32 + wv * 8 : OOB, (v2u){tag, best1 < best0 ? 1u : 0u});
        wait_long(th);
    }
    prep_block(p, r, k, wv, tag);
    if (!conv_block(p, r, k, lds, wv, j, tag)) return;
    attn_block(p, r, lds, wv, k, j, tag);
}

__global__ void __launch_bounds__(256, 1) k_main(Params p) {
    extern __shared__ __attribute__((aligned(16))) unsigned char lds[];
    const unsigned g_t0 = DIAG ? rtnow() : 0u;
    rsrc_t r = __builtin_amdgcn_make_buffer_rsrc(p.ws, 0, W_TOTAL, 0x00020000);
    const int wv = __builtin_amdgcn_readfirstlane(threadIdx.x >> 6);
    int role = blockIdx.x;
    unsigned xcc; asm volatile("s_getreg_b32 %0, hwreg(HW_REG_XCC_ID)" : "=s"(xcc)); xcc &= 7u;
    unsigned old = 0u, slot = (xcc >> 2) * 128u + (blockIdx.x >> 4) * 8u + (xcc & 3u) * 2u + ((blockIdx.x >> 3) & 1u);
    const unsigned tag = __builtin_amdgcn_readfirstlane(ld4(r, (threadIdx.x & 63) == 1 ? F_CLM + slot * 64 + 4 : F_EP)) * 2654435761u + 0x9E3779B9u;
    if (threadIdx.x == 0) old = __hip_atomic_exchange((unsigned*)(p.ws + F_CLM + slot * 64), tag, __ATOMIC_RELAXED, __HIP_MEMORY_SCOPE_AGENT);
    if (threadIdx.x == 0) {
        for (int i = 0; i < 256 && old == tag; ++i) { slot = (slot + 1u) & 255u; old = __hip_atomic_exchange((unsigned*)(p.ws + F_CLM + slot * 64), tag, __ATOMIC_RELAXED, __HIP_MEMORY_SCOPE_AGENT); }
        *(volatile __attribute__((address_space(3))) int*)(size_t)(unsigned)(size_t)lds = (int)slot;
    }
    __syncthreads();
    role = __builtin_amdgcn_readfirstlane(*(volatile __attribute__((address_space(3))) int*)(size_t)(unsigned)(size_t)lds) & 255;
    __syncthreads();
    if (role >= 128) bulk_block(p, r, lds, wv, role - 128, tag, g_t0);
    else if ((role & 6) == 6) help_block(p, r, lds, wv, (role >> 3) * 2 + (role & 1), tag);
    else rec_block(p, wv, role, tag);
}

extern "C" void kernel_launch(void* const* d_in, const int* in_sizes, int n_in, void* d_out, int out_size, void* d_ws, size_t ws_size, hipStream_t stream) {
    Params p;
    p.Aq = (const bf16_t*)d_in[0]; p.Ak = (const bf16_t*)d_in[1]; p.Av = (const bf16_t*)d_in[2];
    p.shift_k = (const bf16_t*)d_in[3]; p.ssm_k = (const bf16_t*)d_in[4]; p.ssm_D = (const bf16_t*)d_in[5];
    p.mem0 = (const bf16_t*)d_in[7]; p.Wq = (const bf16_t*)d_in[8]; p.Wk = (const bf16_t*)d_in[9];
    p.Wv = (const bf16_t*)d_in[10]; p.Wo = (const bf16_t*)d_in[11]; p.gate_w = (const bf16_t*)d_in[13];
    p.out = (bf16_t*)d_out; p.ws = (unsigned char*)d_ws;
    (void)hipFuncSetAttribute((const void*)k_main, hipFuncAttributeMaxDynamicSharedMemorySize, L_TOTAL);
    k_main<<<256, 256, L_TOTAL, stream>>>(p);
}
```

```cpp
#include <hip/hip_runtime.h>
#include <stdint.h>
#include <type_traits>
typedef unsigned short bf16_t;
using bf16x8 = __attribute__((ext_vector_type(8))) short;
using f32x4 = __attribute__((ext_vector_type(4))) float;
typedef unsigned v4u __attribute__((ext_vector_type(4)));
typedef unsigned v2u __attribute__((ext_vector_type(2)));
typedef __amdgpu_buffer_rsrc_t rsrc_t;

typedef __bf16 hbf2 __attribute__((ext_vector_type(2)));
typedef float f32x2 __attribute__((ext_vector_type(2)));
__device__ __forceinline__ unsigned pack2(float a, float b) { f32x2 v = {a, b}; return __builtin_bit_cast(unsigned, __builtin_convertvector(v, hbf2)); }
__device__ __forceinline__ float rbfh(float f) { return __uint_as_float(pack2(0.f, f)); }
__device__ __forceinline__ f32x2 rbf2(f32x2 f) { return (f32x2){rbfh(f.x), rbfh(f.y)}; }
__device__ __forceinline__ unsigned nosent(unsigned x) {
    typedef unsigned short us2 __attribute__((ext_vector_type(2)));
    const us2 a = __builtin_bit_cast(us2, x), c = {(unsigned short)0xFFFE, (unsigned short)0xFFFE}; return __builtin_bit_cast(unsigned, __builtin_elementwise_min(a, c)); }

constexpr int S = 2048, D = 1024, H = 32, DH = 128, M = 8, SEG = 32, NSEG = 64;

constexpr unsigned W_FLG = 0;
constexpr unsigned F_KVQ = W_FLG;
constexpr unsigned F_EP = W_FLG + 32768;
constexpr unsigned F_ERR = W_FLG + 32768 + 256;
constexpr unsigned F_PREP = W_FLG + 32768 + 512;
constexpr unsigned F_CAL = W_FLG + 73728;
constexpr unsigned F_CLM = W_FLG + 77824;
constexpr unsigned F_CONV = W_FLG + 33792;
constexpr unsigned F_PROG = W_FLG + 36864;
constexpr unsigned F_ATT = W_FLG + 40960;
constexpr unsigned F_RDY = W_FLG + 49152;
constexpr unsigned FLG_BYTES = 94208;
constexpr unsigned HO_KVM = W_FLG + FLG_BYTES;
constexpr unsigned W_A = HO_KVM + 64 * 32 * 4096;
constexpr unsigned W_GATE = W_A + S * D * 2;
constexpr unsigned W_QX = W_GATE + 8192;
constexpr unsigned W_KX = W_QX + S * 4096 * 2;
constexpr unsigned W_VF = W_KX + S * 4096 * 2;
constexpr unsigned W_OX = W_VF + 64 * 32 * 8192;
constexpr unsigned DBG_BYTES = 0;
constexpr unsigned W_DBG = W_OX + S * 4096 * 2;
constexpr unsigned W_U = W_DBG + DBG_BYTES;
constexpr unsigned W_TOTAL = W_U + S * D * 2;

__device__ __forceinline__ v4u ld16(rsrc_t r, unsigned off) { return __builtin_amdgcn_raw_buffer_load_b128(r, off, 0, 16); }
__device__ __forceinline__ v2u ld8(rsrc_t r, unsigned off) { return __builtin_amdgcn_raw_buffer_load_b64(r, off, 0, 16); }
__device__ __forceinline__ unsigned ld4(rsrc_t r, unsigned off) { return __builtin_amdgcn_raw_buffer_load_b32(r, off, 0, 16); }
__device__ __forceinline__ void st16(rsrc_t r, unsigned off, v4u v) { __builtin_amdgcn_raw_buffer_store_b128(v, r, off, 0, 16); }
__device__ __forceinline__ void st8(rsrc_t r, unsigned off, v2u v) { __builtin_amdgcn_raw_buffer_store_b64(v, r, off, 0, 16); }
__device__ __forceinline__ void st4(rsrc_t r, unsigned off, unsigned v) { __builtin_amdgcn_raw_buffer_store_b32(v, r, off, 0, 16); }
__device__ __forceinline__ bf16x8 as_frag(v4u v) { return __builtin_bit_cast(bf16x8, v); }
#define D1 134
#define D2 90
#define D3 64
#define LB0 16
#define LC0 7
#define LA0 8
#define TR_UP 4
#define TR_DN 2
#define L_DEC 3
#define STAG 1
#define UP_SH 2
#define EARLY_FROM 58
#define OUT_WARM 0
#define AROT 0
#define P_SLP 2
#define O_SLP 2
#define W_NT 0
#define TOL 2
#define A_SLP 0
#define PEN2 20
#define A_UP 24
#define CT0 2
#define CPF 1
#define PH_M 3
#define SKEW 0
#define SKEW1 0
#define XT 1
#define TRK 3
#define WO_NT 0
#define WO_TRK 1
#define WIN 3
#define ROT0 3
#define ROT1 2
#define ROT2 2
#define B_NT 0
#define CALM 15
#define R4M 1
#define HX_SH 0u
#define FINE_T 1
#define FINE_N 0
#define FINE_A 0
#define O_ND 12
#define PEN0 32
#define X0C 20
#define X0A 16
#define X1A 8
#define PEN 24
#define MEMBAR() asm volatile("" ::: "memory")
#define ENDPGM() asm volatile("s_endpgm" ::: "memory")
#define DRAIN() asm volatile("s_waitcnt vmcnt(0)" ::: "memory")

__device__ __forceinline__ float xor16_max(float x) { auto r = __builtin_amdgcn_permlane16_swap(__float_as_uint(x), __float_as_uint(x), false, false); return fmaxf(__uint_as_float(r[0]), __uint_as_float(r[1])); }
__device__ __forceinline__ float xor32_max(float x) { auto r = __builtin_amdgcn_permlane32_swap(__float_as_uint(x), __float_as_uint(x), false, false); return fmaxf(__uint_as_float(r[0]), __uint_as_float(r[1])); }
__device__ __forceinline__ float xor16_add(float x) { auto r = __builtin_amdgcn_permlane16_swap(__float_as_uint(x), __float_as_uint(x), false, false); return __uint_as_float(r[0]) + __uint_as_float(r[1]); }
__device__ __forceinline__ float xor32_add(float x) { auto r = __builtin_amdgcn_permlane32_swap(__float_as_uint(x), __float_as_uint(x), false, false); return __uint_as_float(r[0]) + __uint_as_float(r[1]); }

typedef short v4i16_t __attribute__((ext_vector_type(4)));
__device__ __forceinline__ v2u trb(unsigned lds_addr) { return __builtin_bit_cast(v2u, __builtin_amdgcn_ds_read_tr16_b64_v4i16((__attribute__((address_space(3))) v4i16_t*)(size_t)lds_addr)); }
__device__ __forceinline__ int otid_(int wv) { int t; asm volatile("v_mbcnt_lo_u32_b32 %0, -1, 0\n\tv_mbcnt_hi_u32_b32 %0, -1, %0" : "=v"(t)); return wv * 64 + t; }
#define otid() otid_(wv)
__device__ __forceinline__ bool wait_word(rsrc_t r, unsigned off, unsigned want, unsigned code) {
    for (unsigned spin = 0;; ++spin) { MEMBAR();
        const unsigned v = __builtin_amdgcn_readfirstlane(ld4(r, off));
        if (v >= want) return true;
        if (spin > (1u << 20)) { st4(r, F_ERR, code); return false; }
        __builtin_amdgcn_s_sleep(8); }
}

__device__ __forceinline__ bool wait_prep(rsrc_t r, int l, unsigned tag) {
    for (unsigned spin = 0;; ++spin) { MEMBAR();
        const unsigned v = ld4(r, F_PREP + (l & 31) * 4);
        if (__all(v == tag)) return true;
        if (spin > (1u << 20)) { st4(r, F_ERR, 512u); return false; }
        __builtin_amdgcn_s_sleep(8); }
}
__device__ __forceinline__ unsigned rtnow() { return (unsigned)__builtin_amdgcn_s_memrealtime(); }
__device__ __forceinline__ void wait_long(unsigned target) { for (int i = 0; i < 4096; ++i) { if ((int)(rtnow() - target) >= 0) break; __builtin_amdgcn_s_sleep(8); } }
constexpr int L_TAB = 20480;
constexpr int L_QK = 24576;
__device__ __forceinline__ unsigned img(int f) { return (unsigned)(L_QK + f * 16 + (f >> 4) * 128); }
constexpr int L_FL = 30720, L_Z = L_FL + 128, L_LOG = 31744, L_W = 32768, L_V = 131072;
constexpr int L_TOTAL = 163840;
constexpr unsigned U_END = 96;
#define PMIN 76
#define V_SLP 2
#define R_SLP 1
constexpr unsigned WO_L = 98304;
#define HELP_D 500
#define V_DEL 16
#define XR 12
#define PFW 0
#define KEYM 3
#define DIAG 0
#define NEARX 0
#define MIXH 1
#define SKEWH 20
#define SKEWP 15
#define NEARY 0
#define CS1 0x8000u
#define CS2 0x10000u
#define CAL_O 0u
#define H_SLP 0
#define CAL_N 3
#define STEP_MIN 180u

struct Params {
    const bf16_t *Aq, *Ak, *Av, *shift_k, *ssm_k, *ssm_D, *mem0, *Wq, *Wk, *Wv, *Wo, *gate_w;
    bf16_t* out; unsigned char* ws;
};
__host__ __device__ __forceinline__ unsigned hx_base(const void* ws) { const size_t a = (size_t)ws; return (unsigned)((((a + W_TOTAL + 0x7FFFFFu) >> 23) << 23) - a) + HX_SH; }

typedef short v4s __attribute__((ext_vector_type(4)));
typedef unsigned v32u __attribute__((ext_vector_type(32)));
typedef unsigned v16u __attribute__((ext_vector_type(16)));
typedef unsigned v8u __attribute__((ext_vector_type(8)));
typedef unsigned v12u __attribute__((ext_vector_type(12)));
__device__ __forceinline__ v4u mk_srd(const void* p, unsigned bytes) { const size_t a = (size_t)p; v4u d;
    d.x = __builtin_amdgcn_readfirstlane((unsigned)a); d.y = __builtin_amdgcn_readfirstlane((unsigned)(a >> 32) & 0xFFFFu); d.z = __builtin_amdgcn_readfirstlane(bytes); d.w = 0x00020000u; return d; }
__device__ __forceinline__ bf16x8 ldsa(unsigned addr) { return *(const __attribute__((address_space(3))) bf16x8*)(size_t)addr; }
__device__ __forceinline__ void rec_block(const Params& p, const int wv, const int b, const unsigned tag) {
    const int w = wv, tid = otid(), l = tid & 63, n16 = l & 15, g = l >> 4, c = n16 >> 2;
    const int x = (b & 7) >> 1, i32 = (b >> 3) * 2 + (b & 1);
    const unsigned OOB = 0xF0000000u;
    const v4u rhd = mk_srd(p.ws + hx_base(p.ws), U_END * 16384), wsd = mk_srd(p.ws, W_TOTAL);
    const int G = x - 1, vi = G * 32 + i32, h = x == 0 ? i32 : vi >> 1;
    const unsigned pr_a = F_PROG + h * 128 + (x == 0 ? 0 : 8 + (vi & 1) * 8), cal = F_CAL - w * 1024, kvo = HO_KVM + h * 4096 + (x ? 2048 : 0);
    if (CPF) {
        unsigned lo, hi, l2; asm volatile("s_getpc_b64 s[98:99]\n.Lpc%=:\n\ts_add_u32 %2, s98, rec_v_code-.Lpc%=\n\ts_add_u32 %0, s98, rec_s_code-.Lpc%=\n\ts_addc_u32 %1, s99, 0" : "=s"(lo), "=s"(hi), "=s"(l2) :: "s98", "s99", "scc");
        const size_t a = (((size_t)hi << 32) | lo) + (size_t)(long long)((x ? (int)(l2 - lo) : 0) + ((i32 * 4 + w) % 10) * 1024 - 4096 + l * 16);
        asm volatile("s_mov_b32 m0, %1\n\ts_nop 0\n\tglobal_load_lds_dwordx4 %0, off" :: "v"(a & ~(size_t)15), "s"(x ? L_V + 16384 : 0) : "memory", "m0");
    }
    typedef __attribute__((address_space(3))) volatile unsigned lu32;
    auto cpy = [](int k) -> unsigned { return k == 1 ? 0u : k * 32768u + 16384u; };
    auto cun = [](int u) -> unsigned { return ((u & 1) ? CS1 : 0u) ^ ((u & 2) ? CS2 : 0u); };
    unsigned nA = cpy(x), nW = cpy(w), nT = 64 * 16384 + (x ? 0 : h * 8), nS = 64 * 16384 + x * 1024, nC = 32 * 16384 + (x == 2 ? 16384 : 0), b0 = 65 * 16384 + (h >> 2) * 32768 + (h & 3) * 2048;
    unsigned pob = w == 0 ? nA + (i32 >> 4) * 32768 + (i32 & 15) * 512 : (w == 1 && x) ? (nC ^ cun(i32 >> 3)) + (i32 & 7) * 1024 : (w == 2 && i32 == 0) ? nS : OOB;
    if (x == 0) {
        const v4u sq = mk_srd(p.Wq, 1024 * 4096 * 2), sk = mk_srd(p.Wk, 1024 * 4096 * 2);
        const unsigned m0b = L_W + w * 1024, f = (c & 1) | ((g & 1) << 1) | ((c >> 1) << 2);
        const unsigned t0 = L_W + (16 * (g >> 1) + 4 * (g & 1) + (c & 1) + 8 * (c >> 1)) * 256 + (((2 * w) ^ f) << 5) + (n16 & 3) * 8;
        const unsigned dv = (4 * w + g) * 8192 + h * 256 + ((n16 ^ (((g & 1) | (w << 1)) << 1)) << 4);
        v8u pa;
#pragma unroll
        for (int q = 0; q < 8; ++q) pa[q] = (t0 + ((w + (q >> 1)) & 3) * 32768) ^ ((q & 1) << 5);
        const unsigned kx = W_KX + h * 8192, vx = W_VF + h * 8192 + w * 1024, kf = F_KVQ + h * 64;
        const unsigned aq = img(8 * g + (n16 >> 2)) + (n16 & 1) * 8;
        const v8u pc = {(unsigned)tid * 16, (unsigned)(L_W + w * 32768 + l * 16), (n16 & 2) ? 0xEEEEu : 0x4444u, aq,
                  (unsigned)(((w + 1) & 3) * 1024 + l * 16), (unsigned)(((w + 2) & 3) * 1024 + l * 16), (unsigned)(((w + 3) & 3) * 1024 + l * 16), img(32 * w + n16) + g * 4};
        const v2u pd = {(n16 & 2) ? (unsigned)L_Z : aq + w * 768, (n16 & 2) ? aq + w * 768 : (unsigned)L_Z};
        *(__attribute__((address_space(3))) v4u*)(size_t)(L_TAB + tid * 16) = (v4u){(unsigned)(L_QK + 3072 + g * 384 + n16 * 8),
                  n16 < 8 ? (unsigned)((((n16 >> 2) * 16384) ^ cun((h >> 1) & 3)) + ((h & 1) * 4 + (h >> 3)) * 1024 + w * 256 + (g >> 1) * 128 + (n16 & 2) * 32 + (g & 1) * 32 + (n16 & 1) * 16) : OOB,
                  (unsigned)((32 * w + 16 * (g & 1) + n16) * 16 + (g >> 1) * 8), (unsigned)(L_QK + 3072 + w * 768 + ((n16 & 1) + 2 * (n16 >> 2)) * 32 + g * 8 + (n16 & 2) * 2)};
        if (tid < 64) ((lu32*)(size_t)L_FL)[tid] = 0u;
    asm volatile(
        "s_add_u32 s80, %[m0b], 0\n\t"
        "s_mov_b32 s81, 0\n\t"
        "s_mov_b32 s82, 4\n\t"
        ".Ld0%=:\n\t"
        "s_mov_b32 m0, s80\n\t"
        "s_add_u32 s80, s80, 0x8000\n\t"
        "buffer_load_dwordx4 v96, %[sq], s81 offen lds\n\t"
        "s_add_u32 m0, m0, 0x1000\n\t"
        "s_add_u32 s81, s81, 0x20000\n\t"
        "buffer_load_dwordx4 v96, %[sq], s81 offen lds\n\t"
        "s_add_u32 m0, m0, 0x1000\n\t"
        "s_add_u32 s81, s81, 0x20000\n\t"
        "buffer_load_dwordx4 v96, %[sq], s81 offen lds\n\t"
        "s_add_u32 m0, m0, 0x1000\n\t"
        "s_add_u32 s81, s81, 0x20000\n\t"
        "buffer_load_dwordx4 v96, %[sq], s81 offen lds\n\t"
        "s_add_u32 m0, m0, 0x1000\n\t"
        "s_add_u32 s81, s81, 0x20000\n\t"
        "s_sub_u32 s82, s82, 1\n\t"
        "s_cmp_lg_u32 s82, 0\n\t"
        "s_cbranch_scc1 .Ld0%=\n\t"
        "s_add_u32 s80, %[m0b], 16384\n\t"
        "s_mov_b32 s81, 0\n\t"
        "s_mov_b32 s82, 4\n\t"
        ".Ld1%=:\n\t"
        "s_mov_b32 m0, s80\n\t"
        "s_add_u32 s80, s80, 0x8000\n\t"
        "buffer_load_dwordx4 v96, %[sk], s81 offen lds\n\t"
        "s_add_u32 m0, m0, 0x1000\n\t"
        "s_add_u32 s81, s81, 0x20000\n\t"
        "buffer_load_dwordx4 v96, %[sk], s81 offen lds\n\t"
        "s_add_u32 m0, m0, 0x1000\n\t"
        "s_add_u32 s81, s81, 0x20000\n\t"
        "buffer_load_dwordx4 v96, %[sk], s81 offen lds\n\t"
        "s_add_u32 m0, m0, 0x1000\n\t"
        "s_add_u32 s81, s81, 0x20000\n\t"
        "buffer_load_dwordx4 v96, %[sk], s81 offen lds\n\t"
        "s_add_u32 m0, m0, 0x1000\n\t"
        "s_add_u32 s81, s81, 0x20000\n\t"
        "s_sub_u32 s82, s82, 1\n\t"
        "s_cmp_lg_u32 s82, 0\n\t"
        "s_cbranch_scc1 .Ld1%=\n\t"
        "s_waitcnt vmcnt(16)\n\t"
        "s_barrier\n\t"
        "ds_read_b64_tr_b16 v[0:1], v98 offset:0\n\t"
        "ds_read_b64_tr_b16 v[2:3], v98 offset:512\n\t"
        "ds_read_b64_tr_b16 v[4:5], v98 offset:8192\n\t"
        "ds_read_b64_tr_b16 v[6:7], v98 offset:8704\n\t"
        "ds_read_b64_tr_b16 v[8:9], v99 offset:0\n\t"
        "ds_read_b64_tr_b16 v[10:11], v99 offset:512\n\t"
        "ds_read_b64_tr_b16 v[12:13], v99 offset:8192\n\t"
        "ds_read_b64_tr_b16 v[14:15], v99 offset:8704\n\t"
        "ds_read_b64_tr_b16 a[16:17], v100 offset:0\n\t"
        "ds_read_b64_tr_b16 a[18:19], v100 offset:512\n\t"
        "ds_read_b64_tr_b16 a[20:21], v100 offset:8192\n\t"
        "ds_read_b64_tr_b16 a[22:23], v100 offset:8704\n\t"
        "ds_read_b64_tr_b16 a[24:25], v101 offset:0\n\t"
        "ds_read_b64_tr_b16 a[26:27], v101 offset:512\n\t"
        "ds_read_b64_tr_b16 a[28:29], v101 offset:8192\n\t"
        "ds_read_b64_tr_b16 a[30:31], v101 offset:8704\n\t"
        "ds_read_b64_tr_b16 a[32:33], v102 offset:0\n\t"
        "ds_read_b64_tr_b16 a[34:35], v102 offset:512\n\t"
        "ds_read_b64_tr_b16 a[36:37], v102 offset:8192\n\t"
        "ds_read_b64_tr_b16 a[38:39], v102 offset:8704\n\t"
        "ds_read_b64_tr_b16 a[40:41], v103 offset:0\n\t"
        "ds_read_b64_tr_b16 a[42:43], v103 offset:512\n\t"
        "ds_read_b64_tr_b16 a[44:45], v103 offset:8192\n\t"
        "ds_read_b64_tr_b16 a[46:47], v103 offset:8704\n\t"
        "ds_read_b64_tr_b16 a[48:49], v104 offset:0\n\t"
        "ds_read_b64_tr_b16 a[50:51], v104 offset:512\n\t"
        "ds_read_b64_tr_b16 a[52:53], v104 offset:8192\n\t"
        "ds_read_b64_tr_b16 a[54:55], v104 offset:8704\n\t"
        "ds_read_b64_tr_b16 a[56:57], v105 offset:0\n\t"
        "ds_read_b64_tr_b16 a[58:59], v105 offset:512\n\t"
        "ds_read_b64_tr_b16 a[60:61], v105 offset:8192\n\t"
        "ds_read_b64_tr_b16 a[62:63], v105 offset:8704\n\t"
        "s_waitcnt lgkmcnt(0)\n\t"
        "s_barrier\n\t"
        "s_add_u32 s80, %[m0b], 0\n\t"
        "s_mov_b32 s81, 2097152\n\t"
        "s_mov_b32 s82, 4\n\t"
        ".Ld2%=:\n\t"
        "s_mov_b32 m0, s80\n\t"
        "s_add_u32 s80, s80, 0x8000\n\t"
        "buffer_load_dwordx4 v96, %[sq], s81 offen lds\n\t"
        "s_add_u32 m0, m0, 0x1000\n\t"
        "s_add_u32 s81, s81, 0x20000\n\t"
        "buffer_load_dwordx4 v96, %[sq], s81 offen lds\n\t"
        "s_add_u32 m0, m0, 0x1000\n\t"
        "s_add_u32 s81, s81, 0x20000\n\t"
        "buffer_load_dwordx4 v96, %[sq], s81 offen lds\n\t"
        "s_add_u32 m0, m0, 0x1000\n\t"
        "s_add_u32 s81, s81, 0x20000\n\t"
        "buffer_load_dwordx4 v96, %[sq], s81 offen lds\n\t"
        "s_add_u32 m0, m0, 0x1000\n\t"
        "s_add_u32 s81, s81, 0x20000\n\t"
        "s_sub_u32 s82, s82, 1\n\t"
        "s_cmp_lg_u32 s82, 0\n\t"
        "s_cbranch_scc1 .Ld2%=\n\t"
        "s_waitcnt vmcnt(16)\n\t"
        "s_barrier\n\t"
        "ds_read_b64_tr_b16 v[16:17], v98 offset:16384\n\t"
        "ds_read_b64_tr_b16 v[18:19], v98 offset:16896\n\t"
        "ds_read_b64_tr_b16 v[20:21], v98 offset:24576\n\t"
        "ds_read_b64_tr_b16 v[22:23], v98 offset:25088\n\t"
        "ds_read_b64_tr_b16 v[24:25], v99 offset:16384\n\t"
        "ds_read_b64_tr_b16 v[26:27], v99 offset:16896\n\t"
        "ds_read_b64_tr_b16 v[28:29], v99 offset:24576\n\t"
        "ds_read_b64_tr_b16 v[30:31], v99 offset:25088\n\t"
        "ds_read_b64_tr_b16 a[80:81], v100 offset:16384\n\t"
        "ds_read_b64_tr_b16 a[82:83], v100 offset:16896\n\t"
        "ds_read_b64_tr_b16 a[84:85], v100 offset:24576\n\t"
        "ds_read_b64_tr_b16 a[86:87], v100 offset:25088\n\t"
        "ds_read_b64_tr_b16 a[88:89], v101 offset:16384\n\t"
        "ds_read_b64_tr_b16 a[90:91], v101 offset:16896\n\t"
        "ds_read_b64_tr_b16 a[92:93], v101 offset:24576\n\t"
        "ds_read_b64_tr_b16 a[94:95], v101 offset:25088\n\t"
        "ds_read_b64_tr_b16 a[96:97], v102 offset:16384\n\t"
        "ds_read_b64_tr_b16 a[98:99], v102 offset:16896\n\t"
        "ds_read_b64_tr_b16 a[100:101], v102 offset:24576\n\t"
        "ds_read_b64_tr_b16 a[102:103], v102 offset:25088\n\t"
        "ds_read_b64_tr_b16 a[104:105], v103 offset:16384\n\t"
        "ds_read_b64_tr_b16 a[106:107], v103 offset:16896\n\t"
        "ds_read_b64_tr_b16 a[108:109], v103 offset:24576\n\t"
        "ds_read_b64_tr_b16 a[110:111], v103 offset:25088\n\t"
        "ds_read_b64_tr_b16 a[112:113], v104 offset:16384\n\t"
        "ds_read_b64_tr_b16 a[114:115], v104 offset:16896\n\t"
        "ds_read_b64_tr_b16 a[116:117], v104 offset:24576\n\t"
        "ds_read_b64_tr_b16 a[118:119], v104 offset:25088\n\t"
        "ds_read_b64_tr_b16 a[120:121], v105 offset:16384\n\t"
        "ds_read_b64_tr_b16 a[122:123], v105 offset:16896\n\t"
        "ds_read_b64_tr_b16 a[124:125], v105 offset:24576\n\t"
        "ds_read_b64_tr_b16 a[126:127], v105 offset:25088\n\t"
        "s_waitcnt lgkmcnt(0)\n\t"
        "s_barrier\n\t"
        "s_add_u32 s80, %[m0b], 16384\n\t"
        "s_mov_b32 s81, 2097152\n\t"
        "s_mov_b32 s82, 4\n\t"
        ".Ld3%=:\n\t"
        "s_mov_b32 m0, s80\n\t"
        "s_add_u32 s80, s80, 0x8000\n\t"
        "buffer_load_dwordx4 v96, %[sk], s81 offen lds\n\t"
        "s_add_u32 m0, m0, 0x1000\n\t"
        "s_add_u32 s81, s81, 0x20000\n\t"
        "buffer_load_dwordx4 v96, %[sk], s81 offen lds\n\t"
        "s_add_u32 m0, m0, 0x1000\n\t"
        "s_add_u32 s81, s81, 0x20000\n\t"
        "buffer_load_dwordx4 v96, %[sk], s81 offen lds\n\t"
        "s_add_u32 m0, m0, 0x1000\n\t"
        "s_add_u32 s81, s81, 0x20000\n\t"
        "buffer_load_dwordx4 v96, %[sk], s81 offen lds\n\t"
        "s_add_u32 m0, m0, 0x1000\n\t"
        "s_add_u32 s81, s81, 0x20000\n\t"
        "s_sub_u32 s82, s82, 1\n\t"
        "s_cmp_lg_u32 s82, 0\n\t"
        "s_cbranch_scc1 .Ld3%=\n\t"
        "s_waitcnt vmcnt(16)\n\t"
        "s_barrier\n\t"
        "ds_read_b64_tr_b16 v[32:33], v98 offset:0\n\t"
        "ds_read_b64_tr_b16 v[34:35], v98 offset:512\n\t"
        "ds_read_b64_tr_b16 v[36:37], v98 offset:8192\n\t"
        "ds_read_b64_tr_b16 v[38:39], v98 offset:8704\n\t"
        "ds_read_b64_tr_b16 v[40:41], v99 offset:0\n\t"
        "ds_read_b64_tr_b16 v[42:43], v99 offset:512\n\t"
        "ds_read_b64_tr_b16 v[44:45], v99 offset:8192\n\t"
        "ds_read_b64_tr_b16 v[46:47], v99 offset:8704\n\t"
        "ds_read_b64_tr_b16 a[144:145], v100 offset:0\n\t"
        "ds_read_b64_tr_b16 a[146:147], v100 offset:512\n\t"
        "ds_read_b64_tr_b16 a[148:149], v100 offset:8192\n\t"
        "ds_read_b64_tr_b16 a[150:151], v100 offset:8704\n\t"
        "ds_read_b64_tr_b16 a[152:153], v101 offset:0\n\t"
        "ds_read_b64_tr_b16 a[154:155], v101 offset:512\n\t"
        "ds_read_b64_tr_b16 a[156:157], v101 offset:8192\n\t"
        "ds_read_b64_tr_b16 a[158:159], v101 offset:8704\n\t"
        "ds_read_b64_tr_b16 a[160:161], v102 offset:0\n\t"
        "ds_read_b64_tr_b16 a[162:163], v102 offset:512\n\t"
        "ds_read_b64_tr_b16 a[164:165], v102 offset:8192\n\t"
        "ds_read_b64_tr_b16 a[166:167], v102 offset:8704\n\t"
        "ds_read_b64_tr_b16 a[168:169], v103 offset:0\n\t"
        "ds_read_b64_tr_b16 a[170:171], v103 offset:512\n\t"
        "ds_read_b64_tr_b16 a[172:173], v103 offset:8192\n\t"
        "ds_read_b64_tr_b16 a[174:175], v103 offset:8704\n\t"
        "ds_read_b64_tr_b16 a[176:177], v104 offset:0\n\t"
        "ds_read_b64_tr_b16 a[178:179], v104 offset:512\n\t"
        "ds_read_b64_tr_b16 a[180:181], v104 offset:8192\n\t"
        "ds_read_b64_tr_b16 a[182:183], v104 offset:8704\n\t"
        "ds_read_b64_tr_b16 a[184:185], v105 offset:0\n\t"
        "ds_read_b64_tr_b16 a[186:187], v105 offset:512\n\t"
        "ds_read_b64_tr_b16 a[188:189], v105 offset:8192\n\t"
        "ds_read_b64_tr_b16 a[190:191], v105 offset:8704\n\t"
        "s_waitcnt lgkmcnt(0)\n\t"
        "s_barrier\n\t"
        "s_add_u32 s80, %[m0b], 0\n\t"
        "s_mov_b32 s81, 4194304\n\t"
        "s_mov_b32 s82, 4\n\t"
        ".Ld4%=:\n\t"
        "s_mov_b32 m0, s80\n\t"
        "s_add_u32 s80, s80, 0x8000\n\t"
        "buffer_load_dwordx4 v96, %[sq], s81 offen lds\n\t"
        "s_add_u32 m0, m0, 0x1000\n\t"
        "s_add_u32 s81, s81, 0x20000\n\t"
        "buffer_load_dwordx4 v96, %[sq], s81 offen lds\n\t"
        "s_add_u32 m0, m0, 0x1000\n\t"
        "s_add_u32 s81, s81, 0x20000\n\t"
        "buffer_load_dwordx4 v96, %[sq], s81 offen lds\n\t"
        "s_add_u32 m0, m0, 0x1000\n\t"
        "s_add_u32 s81, s81, 0x20000\n\t"
        "buffer_load_dwordx4 v96, %[sq], s81 offen lds\n\t"
        "s_add_u32 m0, m0, 0x1000\n\t"
        "s_add_u32 s81, s81, 0x20000\n\t"
        "s_sub_u32 s82, s82, 1\n\t"
        "s_cmp_lg_u32 s82, 0\n\t"
        "s_cbranch_scc1 .Ld4%=\n\t"
        "s_waitcnt vmcnt(16)\n\t"
        "s_barrier\n\t"
        "ds_read_b64_tr_b16 v[48:49], v98 offset:16384\n\t"
        "ds_read_b64_tr_b16 v[50:51], v98 offset:16896\n\t"
        "ds_read_b64_tr_b16 v[52:53], v98 offset:24576\n\t"
        "ds_read_b64_tr_b16 v[54:55], v98 offset:25088\n\t"
        "ds_read_b64_tr_b16 v[56:57], v99 offset:16384\n\t"
        "ds_read_b64_tr_b16 v[58:59], v99 offset:16896\n\t"
        "ds_read_b64_tr_b16 v[60:61], v99 offset:24576\n\t"
        "ds_read_b64_tr_b16 v[62:63], v99 offset:25088\n\t"
        "ds_read_b64_tr_b16 a[208:209], v100 offset:16384\n\t"
        "ds_read_b64_tr_b16 a[210:211], v100 offset:16896\n\t"
        "ds_read_b64_tr_b16 a[212:213], v100 offset:24576\n\t"
        "ds_read_b64_tr_b16 a[214:215], v100 offset:25088\n\t"
        "ds_read_b64_tr_b16 a[216:217], v101 offset:16384\n\t"
        "ds_read_b64_tr_b16 a[218:219], v101 offset:16896\n\t"
        "ds_read_b64_tr_b16 a[220:221], v101 offset:24576\n\t"
        "ds_read_b64_tr_b16 a[222:223], v101 offset:25088\n\t"
        "ds_read_b64_tr_b16 a[224:225], v102 offset:16384\n\t"
        "ds_read_b64_tr_b16 a[226:227], v102 offset:16896\n\t"
        "ds_read_b64_tr_b16 a[228:229], v102 offset:24576\n\t"
        "ds_read_b64_tr_b16 a[230:231], v102 offset:25088\n\t"
        "ds_read_b64_tr_b16 a[232:233], v103 offset:16384\n\t"
        "ds_read_b64_tr_b16 a[234:235], v103 offset:16896\n\t"
        "ds_read_b64_tr_b16 a[236:237], v103 offset:24576\n\t"
        "ds_read_b64_tr_b16 a[238:239], v103 offset:25088\n\t"
        "ds_read_b64_tr_b16 a[240:241], v104 offset:16384\n\t"
        "ds_read_b64_tr_b16 a[242:243], v104 offset:16896\n\t"
        "ds_read_b64_tr_b16 a[244:245], v104 offset:24576\n\t"
        "ds_read_b64_tr_b16 a[246:247], v104 offset:25088\n\t"
        "ds_read_b64_tr_b16 a[248:249], v105 offset:16384\n\t"
        "ds_read_b64_tr_b16 a[250:251], v105 offset:16896\n\t"
        "ds_read_b64_tr_b16 a[252:253], v105 offset:24576\n\t"
        "ds_read_b64_tr_b16 a[254:255], v105 offset:25088\n\t"
        "s_waitcnt lgkmcnt(0)\n\t"
        "s_barrier\n\t"
        "s_add_u32 s80, %[m0b], 16384\n\t"
        "s_mov_b32 s81, 4194304\n\t"
        "s_mov_b32 s82, 4\n\t"
        ".Ld5%=:\n\t"
        "s_mov_b32 m0, s80\n\t"
        "s_add_u32 s80, s80, 0x8000\n\t"
        "buffer_load_dwordx4 v96, %[sk], s81 offen lds\n\t"
        "s_add_u32 m0, m0, 0x1000\n\t"
        "s_add_u32 s81, s81, 0x20000\n\t"
        "buffer_load_dwordx4 v96, %[sk], s81 offen lds\n\t"
        "s_add_u32 m0, m0, 0x1000\n\t"
        "s_add_u32 s81, s81, 0x20000\n\t"
        "buffer_load_dwordx4 v96, %[sk], s81 offen lds\n\t"
        "s_add_u32 m0, m0, 0x1000\n\t"
        "s_add_u32 s81, s81, 0x20000\n\t"
        "buffer_load_dwordx4 v96, %[sk], s81 offen lds\n\t"
        "s_add_u32 m0, m0, 0x1000\n\t"
        "s_add_u32 s81, s81, 0x20000\n\t"
        "s_sub_u32 s82, s82, 1\n\t"
        "s_cmp_lg_u32 s82, 0\n\t"
        "s_cbranch_scc1 .Ld5%=\n\t"
        "s_waitcnt vmcnt(16)\n\t"
        "s_barrier\n\t"
        "ds_read_b64_tr_b16 v[128:129], v98 offset:0\n\t"
        "ds_read_b64_tr_b16 v[130:131], v98 offset:512\n\t"
        "ds_read_b64_tr_b16 v[132:133], v98 offset:8192\n\t"
        "ds_read_b64_tr_b16 v[134:135], v98 offset:8704\n\t"
        "ds_read_b64_tr_b16 v[136:137], v99 offset:0\n\t"
        "ds_read_b64_tr_b16 v[138:139], v99 offset:512\n\t"
        "ds_read_b64_tr_b16 v[140:141], v99 offset:8192\n\t"
        "ds_read_b64_tr_b16 v[142:143], v99 offset:8704\n\t"
        "ds_read_b64_tr_b16 v[144:145], v100 offset:0\n\t"
        "ds_read_b64_tr_b16 v[146:147], v100 offset:512\n\t"
        "ds_read_b64_tr_b16 v[148:149], v100 offset:8192\n\t"
        "ds_read_b64_tr_b16 v[150:151], v100 offset:8704\n\t"
        "ds_read_b64_tr_b16 v[152:153], v101 offset:0\n\t"
        "ds_read_b64_tr_b16 v[154:155], v101 offset:512\n\t"
        "ds_read_b64_tr_b16 v[156:157], v101 offset:8192\n\t"
        "ds_read_b64_tr_b16 v[158:159], v101 offset:8704\n\t"
        "ds_read_b64_tr_b16 v[160:161], v102 offset:0\n\t"
        "ds_read_b64_tr_b16 v[162:163], v102 offset:512\n\t"
        "ds_read_b64_tr_b16 v[164:165], v102 offset:8192\n\t"
        "ds_read_b64_tr_b16 v[166:167], v102 offset:8704\n\t"
        "ds_read_b64_tr_b16 v[168:169], v103 offset:0\n\t"
        "ds_read_b64_tr_b16 v[170:171], v103 offset:512\n\t"
        "ds_read_b64_tr_b16 v[172:173], v103 offset:8192\n\t"
        "ds_read_b64_tr_b16 v[174:175], v103 offset:8704\n\t"
        "ds_read_b64_tr_b16 v[176:177], v104 offset:0\n\t"
        "ds_read_b64_tr_b16 v[178:179], v104 offset:512\n\t"
        "ds_read_b64_tr_b16 v[180:181], v104 offset:8192\n\t"
        "ds_read_b64_tr_b16 v[182:183], v104 offset:8704\n\t"
        "ds_read_b64_tr_b16 v[184:185], v105 offset:0\n\t"
        "ds_read_b64_tr_b16 v[186:187], v105 offset:512\n\t"
        "ds_read_b64_tr_b16 v[188:189], v105 offset:8192\n\t"
        "ds_read_b64_tr_b16 v[190:191], v105 offset:8704\n\t"
        "s_waitcnt lgkmcnt(0)\n\t"
        "s_barrier\n\t"
        "s_add_u32 s80, %[m0b], 0\n\t"
        "s_mov_b32 s81, 6291456\n\t"
        "s_mov_b32 s82, 4\n\t"
        ".Ld6%=:\n\t"
        "s_mov_b32 m0, s80\n\t"
        "s_add_u32 s80, s80, 0x8000\n\t"
        "buffer_load_dwordx4 v96, %[sq], s81 offen lds\n\t"
        "s_add_u32 m0, m0, 0x1000\n\t"
        "s_add_u32 s81, s81, 0x20000\n\t"
        "buffer_load_dwordx4 v96, %[sq], s81 offen lds\n\t"
        "s_add_u32 m0, m0, 0x1000\n\t"
        "s_add_u32 s81, s81, 0x20000\n\t"
        "buffer_load_dwordx4 v96, %[sq], s81 offen lds\n\t"
        "s_add_u32 m0, m0, 0x1000\n\t"
        "s_add_u32 s81, s81, 0x20000\n\t"
        "buffer_load_dwordx4 v96, %[sq], s81 offen lds\n\t"
        "s_add_u32 m0, m0, 0x1000\n\t"
        "s_add_u32 s81, s81, 0x20000\n\t"
        "s_sub_u32 s82, s82, 1\n\t"
        "s_cmp_lg_u32 s82, 0\n\t"
        "s_cbranch_scc1 .Ld6%=\n\t"
        "s_waitcnt vmcnt(16)\n\t"
        "s_barrier\n\t"
        "ds_read_b64_tr_b16 v[192:193], v98 offset:16384\n\t"
        "ds_read_b64_tr_b16 v[194:195], v98 offset:16896\n\t"
        "ds_read_b64_tr_b16 v[196:197], v98 offset:24576\n\t"
        "ds_read_b64_tr_b16 v[198:199], v98 offset:25088\n\t"
        "ds_read_b64_tr_b16 v[200:201], v99 offset:16384\n\t"
        "ds_read_b64_tr_b16 v[202:203], v99 offset:16896\n\t"
        "ds_read_b64_tr_b16 v[204:205], v99 offset:24576\n\t"
        "ds_read_b64_tr_b16 v[206:207], v99 offset:25088\n\t"
        "ds_read_b64_tr_b16 v[208:209], v100 offset:16384\n\t"
        "ds_read_b64_tr_b16 v[210:211], v100 offset:16896\n\t"
        "ds_read_b64_tr_b16 v[212:213], v100 offset:24576\n\t"
        "ds_read_b64_tr_b16 v[214:215], v100 offset:25088\n\t"
        "ds_read_b64_tr_b16 v[216:217], v101 offset:16384\n\t"
        "ds_read_b64_tr_b16 v[218:219], v101 offset:16896\n\t"
        "ds_read_b64_tr_b16 v[220:221], v101 offset:24576\n\t"
        "ds_read_b64_tr_b16 v[222:223], v101 offset:25088\n\t"
        "ds_read_b64_tr_b16 v[224:225], v102 offset:16384\n\t"
        "ds_read_b64_tr_b16 v[226:227], v102 offset:16896\n\t"
        "ds_read_b64_tr_b16 v[228:229], v102 offset:24576\n\t"
        "ds_read_b64_tr_b16 v[230:231], v102 offset:25088\n\t"
        "ds_read_b64_tr_b16 v[232:233], v103 offset:16384\n\t"
        "ds_read_b64_tr_b16 v[234:235], v103 offset:16896\n\t"
        "ds_read_b64_tr_b16 v[236:237], v103 offset:24576\n\t"
        "ds_read_b64_tr_b16 v[238:239], v103 offset:25088\n\t"
        "ds_read_b64_tr_b16 v[240:241], v104 offset:16384\n\t"
        "ds_read_b64_tr_b16 v[242:243], v104 offset:16896\n\t"
        "ds_read_b64_tr_b16 v[244:245], v104 offset:24576\n\t"
        "ds_read_b64_tr_b16 v[246:247], v104 offset:25088\n\t"
        "ds_read_b64_tr_b16 v[248:249], v105 offset:16384\n\t"
        "ds_read_b64_tr_b16 v[250:251], v105 offset:16896\n\t"
        "ds_read_b64_tr_b16 v[252:253], v105 offset:24576\n\t"
        "ds_read_b64_tr_b16 v[254:255], v105 offset:25088\n\t"
        "s_waitcnt lgkmcnt(0)\n\t"
        "s_barrier\n\t"
        "s_add_u32 s80, %[m0b], 16384\n\t"
        "s_mov_b32 s81, 6291456\n\t"
        "s_mov_b32 s82, 4\n\t"
        ".Ld7%=:\n\t"
        "s_mov_b32 m0, s80\n\t"
        "s_add_u32 s80, s80, 0x8000\n\t"
        "buffer_load_dwordx4 v96, %[sk], s81 offen lds\n\t"
        "s_add_u32 m0, m0, 0x1000\n\t"
        "s_add_u32 s81, s81, 0x20000\n\t"
        "buffer_load_dwordx4 v96, %[sk], s81 offen lds\n\t"
        "s_add_u32 m0, m0, 0x1000\n\t"
        "s_add_u32 s81, s81, 0x20000\n\t"
        "buffer_load_dwordx4 v96, %[sk], s81 offen lds\n\t"
        "s_add_u32 m0, m0, 0x1000\n\t"
        "s_add_u32 s81, s81, 0x20000\n\t"
        "buffer_load_dwordx4 v96, %[sk], s81 offen lds\n\t"
        "s_add_u32 m0, m0, 0x1000\n\t"
        "s_add_u32 s81, s81, 0x20000\n\t"
        "s_sub_u32 s82, s82, 1\n\t"
        "s_cmp_lg_u32 s82, 0\n\t"
        "s_cbranch_scc1 .Ld7%=\n\t"
        "s_waitcnt vmcnt(16)\n\t"
        "s_barrier\n\t"
        "ds_read_b64_tr_b16 a[0:1], v98 offset:0\n\t"
        "ds_read_b64_tr_b16 a[2:3], v98 offset:512\n\t"
        "ds_read_b64_tr_b16 a[4:5], v98 offset:8192\n\t"
        "ds_read_b64_tr_b16 a[6:7], v98 offset:8704\n\t"
        "ds_read_b64_tr_b16 a[8:9], v99 offset:0\n\t"
        "ds_read_b64_tr_b16 a[10:11], v99 offset:512\n\t"
        "ds_read_b64_tr_b16 a[12:13], v99 offset:8192\n\t"
        "ds_read_b64_tr_b16 a[14:15], v99 offset:8704\n\t"
        "ds_read_b64_tr_b16 v[64:65], v100 offset:0\n\t"
        "ds_read_b64_tr_b16 v[66:67], v100 offset:512\n\t"
        "ds_read_b64_tr_b16 v[68:69], v100 offset:8192\n\t"
        "ds_read_b64_tr_b16 v[70:71], v100 offset:8704\n\t"
        "ds_read_b64_tr_b16 v[72:73], v101 offset:0\n\t"
        "ds_read_b64_tr_b16 v[74:75], v101 offset:512\n\t"
        "ds_read_b64_tr_b16 v[76:77], v101 offset:8192\n\t"
        "ds_read_b64_tr_b16 v[78:79], v101 offset:8704\n\t"
        "ds_read_b64_tr_b16 v[80:81], v102 offset:0\n\t"
        "ds_read_b64_tr_b16 v[82:83], v102 offset:512\n\t"
        "ds_read_b64_tr_b16 v[84:85], v102 offset:8192\n\t"
        "ds_read_b64_tr_b16 v[86:87], v102 offset:8704\n\t"
        "ds_read_b64_tr_b16 v[88:89], v103 offset:0\n\t"
        "ds_read_b64_tr_b16 v[90:91], v103 offset:512\n\t"
        "ds_read_b64_tr_b16 v[92:93], v103 offset:8192\n\t"
        "ds_read_b64_tr_b16 v[94:95], v103 offset:8704\n\t"
        "ds_read_b64_tr_b16 a[128:129], v104 offset:0\n\t"
        "ds_read_b64_tr_b16 a[130:131], v104 offset:512\n\t"
        "ds_read_b64_tr_b16 a[132:133], v104 offset:8192\n\t"
        "ds_read_b64_tr_b16 a[134:135], v104 offset:8704\n\t"
        "ds_read_b64_tr_b16 a[136:137], v105 offset:0\n\t"
        "ds_read_b64_tr_b16 a[138:139], v105 offset:512\n\t"
        "ds_read_b64_tr_b16 a[140:141], v105 offset:8192\n\t"
        "ds_read_b64_tr_b16 a[142:143], v105 offset:8704\n\t"
        "s_waitcnt lgkmcnt(0)\n\t"
        "s_barrier\n\t"
        "ds_write_b128 v121, v[0:3] offset:0\n\t"
        "ds_write_b128 v121, v[4:7] offset:1024\n\t"
        "ds_write_b128 v121, v[8:11] offset:2048\n\t"
        "ds_write_b128 v121, v[12:15] offset:3072\n\t"
        "ds_write_b128 v121, v[16:19] offset:4096\n\t"
        "ds_write_b128 v121, v[20:23] offset:5120\n\t"
        "ds_write_b128 v121, v[24:27] offset:6144\n\t"
        "ds_write_b128 v121, v[28:31] offset:7168\n\t"
        "ds_write_b128 v121, v[32:35] offset:8192\n\t"
        "ds_write_b128 v121, v[36:39] offset:9216\n\t"
        "ds_write_b128 v121, v[40:43] offset:10240\n\t"
        "ds_write_b128 v121, v[44:47] offset:11264\n\t"
        "ds_write_b128 v121, v[48:51] offset:12288\n\t"
        "ds_write_b128 v121, v[52:55] offset:13312\n\t"
        "ds_write_b128 v121, v[56:59] offset:14336\n\t"
        "ds_write_b128 v121, v[60:63] offset:15360\n\t"
        "s_waitcnt lgkmcnt(0)\n\t"
        "s_mov_b32 s64, %[kf]\n\t"
        "s_mov_b32 s36, 0xf0000000\n\t"
        "buffer_load_dwordx4 v[116:119], v120, %[wsd], %[cal] offen sc1\n\t"
        "buffer_load_dwordx2 v[114:115], off, %[wsd], s64 sc1\n\t"
        "s_waitcnt vmcnt(2)\n\t"
        "s_barrier\n\t"
        "ds_read_b64_tr_b16 a[64:65], v98 offset:16384\n\t"
        "ds_read_b64_tr_b16 a[66:67], v98 offset:16896\n\t"
        "ds_read_b64_tr_b16 a[68:69], v98 offset:24576\n\t"
        "ds_read_b64_tr_b16 a[70:71], v98 offset:25088\n\t"
        "ds_read_b64_tr_b16 a[72:73], v99 offset:16384\n\t"
        "ds_read_b64_tr_b16 a[74:75], v99 offset:16896\n\t"
        "ds_read_b64_tr_b16 a[76:77], v99 offset:24576\n\t"
        "ds_read_b64_tr_b16 a[78:79], v99 offset:25088\n\t"
        "ds_read_b64_tr_b16 v[0:1], v100 offset:16384\n\t"
        "ds_read_b64_tr_b16 v[2:3], v100 offset:16896\n\t"
        "ds_read_b64_tr_b16 v[4:5], v100 offset:24576\n\t"
        "ds_read_b64_tr_b16 v[6:7], v100 offset:25088\n\t"
        "ds_read_b64_tr_b16 v[8:9], v101 offset:16384\n\t"
        "ds_read_b64_tr_b16 v[10:11], v101 offset:16896\n\t"
        "ds_read_b64_tr_b16 v[12:13], v101 offset:24576\n\t"
        "ds_read_b64_tr_b16 v[14:15], v101 offset:25088\n\t"
        "ds_read_b64_tr_b16 v[16:17], v102 offset:16384\n\t"
        "ds_read_b64_tr_b16 v[18:19], v102 offset:16896\n\t"
        "ds_read_b64_tr_b16 v[20:21], v102 offset:24576\n\t"
        "ds_read_b64_tr_b16 v[22:23], v102 offset:25088\n\t"
        "ds_read_b64_tr_b16 v[24:25], v103 offset:16384\n\t"
        "ds_read_b64_tr_b16 v[26:27], v103 offset:16896\n\t"
        "ds_read_b64_tr_b16 v[28:29], v103 offset:24576\n\t"
        "ds_read_b64_tr_b16 v[30:31], v103 offset:25088\n\t"
        "ds_read_b64_tr_b16 a[192:193], v104 offset:16384\n\t"
        "ds_read_b64_tr_b16 a[194:195], v104 offset:16896\n\t"
        "ds_read_b64_tr_b16 a[196:197], v104 offset:24576\n\t"
        "ds_read_b64_tr_b16 a[198:199], v104 offset:25088\n\t"
        "ds_read_b64_tr_b16 a[200:201], v105 offset:16384\n\t"
        "ds_read_b64_tr_b16 a[202:203], v105 offset:16896\n\t"
        "ds_read_b64_tr_b16 a[204:205], v105 offset:24576\n\t"
        "ds_read_b64_tr_b16 a[206:207], v105 offset:25088\n\t"
        "s_waitcnt vmcnt(0)\n\t"
        "s_mov_b32 s97, 0\n\t"
        ".Lg0%=:\n\t"
        "v_cmp_ne_u32_e32 vcc, %[tag], v116\n\t"
        "v_cmp_ne_u32_e64 s[82:83], %[tag], v118\n\t"
        "s_nop 1\n\t"
        "s_or_b64 vcc, vcc, s[82:83]\n\t"
        "s_cbranch_scc0 .Lg1%=\n\t"
        "s_sleep 4\n\t"
        "buffer_load_dwordx4 v[116:119], v120, %[wsd], %[cal] offen sc1\n\t"
        "s_add_u32 s97, s97, 1\n\t"
        "s_cmp_lt_u32 s97, 0x100000\n\t"
        "s_cbranch_scc0 .Lgu%=\n\t"
        "s_waitcnt vmcnt(0)\n\t"
        "s_branch .Lg0%=\n\t"
        ".Lg1%=:\n\t"
        "s_mov_b32 s84, 0x55555555\n\t"
        "s_mov_b32 s85, s84\n\t"
        "v_and_b32_e32 v116, 1, v117\n\t"
        "v_and_b32_e32 v118, 1, v119\n\t"
        "v_cmp_ne_u32_e32 vcc, 0, v116\n\t"
        "v_cmp_ne_u32_e64 s[82:83], 0, v118\n\t"
        "s_nop 0\n\t"
        "s_and_b64 s[82:83], s[82:83], s[84:85]\n\t"
        "s_bcnt1_i32_b64 s87, s[82:83]\n\t"
        "s_and_b64 s[82:83], vcc, s[84:85]\n\t"
        "s_bcnt1_i32_b64 s86, s[82:83]\n\t"
        "s_andn2_b64 s[82:83], vcc, s[84:85]\n\t"
        "s_bcnt1_i32_b64 s88, s[82:83]\n\t"
        "v_and_b32_e32 v116, 2, v117\n\t"
        "v_and_b32_e32 v118, 2, v119\n\t"
        "v_cmp_ne_u32_e32 vcc, 0, v116\n\t"
        "v_cmp_ne_u32_e64 s[82:83], 0, v118\n\t"
        "s_nop 0\n\t"
        "s_and_b64 s[82:83], s[82:83], s[84:85]\n\t"
        "s_bcnt1_i32_b64 s90, s[82:83]\n\t"
        "s_and_b64 s[82:83], vcc, s[84:85]\n\t"
        "s_bcnt1_i32_b64 s89, s[82:83]\n\t"
        "s_andn2_b64 s[82:83], vcc, s[84:85]\n\t"
        "s_bcnt1_i32_b64 s91, s[82:83]\n\t"
        "s_add_u32 s80, s86, s87\n\t"
        "s_add_u32 s80, s80, s88\n\t"
        "s_cmp_ge_u32 s80, 48\n\t"
        "s_cselect_b32 s80, 8, 24\n\t"
        "s_cmp_gt_u32 s86, s80\n\t"
        "s_cselect_b32 s86, %c[NX1], %c[NX0]\n\t"
        "s_cmp_gt_u32 s89, 16\n\t"
        "s_cselect_b32 s81, %c[NY1], %c[NY0]\n\t"
        "s_or_b32 s86, s86, s81\n\t"
        "s_cmp_gt_u32 s87, s80\n\t"
        "s_cselect_b32 s87, %c[NX1], %c[NX0]\n\t"
        "s_cmp_gt_u32 s90, 16\n\t"
        "s_cselect_b32 s81, %c[NY1], %c[NY0]\n\t"
        "s_or_b32 s87, s87, s81\n\t"
        "s_cmp_gt_u32 s88, s80\n\t"
        "s_cselect_b32 s88, %c[NX1], %c[NX0]\n\t"
        "s_cmp_gt_u32 s91, 16\n\t"
        "s_cselect_b32 s81, %c[NY1], %c[NY0]\n\t"
        "s_or_b32 s88, s88, s81\n\t"
        "s_xor_b32 %[nA], %[nA], s86\n\t"
        "s_xor_b32 %[nW], %[nW], s86\n\t"
        "s_xor_b32 %[nC], %[nC], s87\n\t"
        "s_xor_b32 %[nS], %[nS], s88\n\t"
        "s_xor_b32 %[nT], %[nT], s88\n\t"
        "s_xor_b32 %[b0], %[b0], s88\n\t"
        "s_cmp_eq_u32 %[w], 1\n\t"
        "s_cselect_b32 s80, s87, s86\n\t"
        "s_cmp_eq_u32 %[w], 2\n\t"
        "s_cselect_b32 s80, s88, s80\n\t"
        "s_xor_b32 %[pob], %[pob], s80\n\t"
        "s_lshl_b32 s80, %[w], 10\n\t"
        "s_sub_u32 %[nS], %[nS], s80\n\t"
        "s_sub_u32 %[pob], %[pob], s80\n\t"
        "ds_read_b32 v116, v120 offset:20484\n\t"
        "s_and_b32 s80, s87, 0x4000\n\t"
        "s_xor_b32 %[nC], %[nC], s80\n\t"
        "s_lshl_b32 s81, %[w], 8\n\t"
        "s_add_u32 s81, s81, 24576\n\t"
        "s_sub_u32 %[b0], %[b0], s81\n\t"
        "s_waitcnt lgkmcnt(0)\n\t"
        "v_xor_b32_e32 v116, s80, v116\n\t"
        "ds_write_b32 v120, v116 offset:20484\n\t"
        "s_mov_b32 s36, 0xf0000000\n\t"
        "s_mov_b32 s38, 0xff00ff00\n\t"
        "s_mov_b32 s39, 0xff00ff00\n\t"
        "s_mov_b32 s40, 0\n\t"
        "s_mov_b32 s37, 0\n\t"
        "s_brev_b32 s54, 1\n\t"
        "s_mov_b32 s46, %c[iD1]\n\t"
        "s_mov_b32 s45, %c[iD1]\n\t"
        "s_mov_b32 s47, %c[iD2]\n\t"
        "s_mov_b32 s48, 1\n\t"
        "s_mov_b32 s50, %c[LC]\n\t"
        "s_mov_b32 s51, %c[LA]\n\t"
        "s_mov_b32 s52, %c[E1]\n\t"
        "s_mov_b32 s53, %c[E2]\n\t"
        "s_mov_b32 s43, 0\n\t"
        "s_mov_b32 s61, %[b0]\n\t"
        "s_add_u32 s62, %[b0], 0x40000\n\t"
        "s_mov_b32 s75, 31744\n\t"
        "s_cmp_eq_u32 s64, s36\n\t"
        "s_cbranch_scc1 .Lk10%=\n\t"
        "s_mov_b32 s97, 0\n\t"
        ".Lk00%=:\n\t"
        "v_readfirstlane_b32 s82, v114\n\t"
        "v_readfirstlane_b32 s83, v115\n\t"
        "s_xor_b32 s82, s82, %[tag]\n\t"
        "s_xor_b32 s83, s83, %[tag]\n\t"
        "s_or_b32 s82, s82, s83\n\t"
        "s_cbranch_scc0 .Lk10%=\n\t"
        "s_sleep 3\n\t"
        "buffer_load_dwordx2 v[114:115], off, %[wsd], s64 sc1\n\t"
        "s_add_u32 s97, s97, 1\n\t"
        "s_cmp_lt_u32 s97, 0x200000\n\t"
        "s_cbranch_scc0 .Lgu%=\n\t"
        "s_waitcnt vmcnt(0)\n\t"
        "s_branch .Lk00%=\n\t"
        ".Lk10%=:\n\t"
        "s_add_u32 s80, s40, 0\n\t"
        "s_min_u32 s80, s80, 63\n\t"
        "s_lshl_b32 s80, s80, 18\n\t"
        "s_add_u32 s69, s80, %[kx]\n\t"
        "s_add_u32 s70, s69, 0x1000\n\t"
        "s_lshl_b32 s80, s40, 18\n\t"
        "s_add_u32 s71, s80, %[vx]\n\t"
        "s_add_u32 s80, s40, 1\n\t"
        "s_lshl_b32 s81, s80, 9\n\t"
        "s_add_u32 s81, s81, %[kf]\n\t"
        "s_and_b32 s80, s80, 0x43\n\t"
        "s_cselect_b32 s64, s36, s81\n\t"
        "buffer_load_dwordx4 v[104:107], v120, %[wsd], s69 offen sc1\n\t"
        "buffer_load_dwordx4 v[108:111], v120, %[wsd], s70 offen sc1\n\t"
        "s_waitcnt lgkmcnt(0)\n\t"
        "s_barrier\n\t"
        "ds_write_b128 v121, v[64:67] offset:16384\n\t"
        "ds_write_b128 v121, v[68:71] offset:17408\n\t"
        "ds_write_b128 v121, v[72:75] offset:18432\n\t"
        "ds_write_b128 v121, v[76:79] offset:19456\n\t"
        "ds_write_b128 v121, v[80:83] offset:20480\n\t"
        "ds_write_b128 v121, v[84:87] offset:21504\n\t"
        "ds_write_b128 v121, v[88:91] offset:22528\n\t"
        "ds_write_b128 v121, v[92:95] offset:23552\n\t"
        "ds_write_b128 v121, v[0:3] offset:24576\n\t"
        "ds_write_b128 v121, v[4:7] offset:25600\n\t"
        "ds_write_b128 v121, v[8:11] offset:26624\n\t"
        "ds_write_b128 v121, v[12:15] offset:27648\n\t"
        "ds_write_b128 v121, v[16:19] offset:28672\n\t"
        "ds_write_b128 v121, v[20:23] offset:29696\n\t"
        "ds_write_b128 v121, v[24:27] offset:30720\n\t"
        "ds_write_b128 v121, v[28:31] offset:31744\n\t"
        "s_waitcnt lgkmcnt(0)\n\t"
        "rec_s_code:\n\t"
        "ds_read_b128 v[16:19], v121 offset:0\n\t"
        "ds_read_b128 v[20:23], v121 offset:1024\n\t"
        "ds_read_b128 v[24:27], v121 offset:2048\n\t"
        "ds_read_b128 v[28:31], v121 offset:3072\n\t"
        "ds_read_b128 v[32:35], v121 offset:4096\n\t"
        "ds_read_b128 v[36:39], v121 offset:5120\n\t"
        "ds_read_b128 v[40:43], v121 offset:6144\n\t"
        "ds_read_b128 v[44:47], v121 offset:7168\n\t"
        "ds_read_b128 v[56:59], v121 offset:8192\n\t"
        "ds_read_b128 v[60:63], v121 offset:9216\n\t"
        "ds_read_b128 v[64:67], v121 offset:10240\n\t"
        "ds_read_b128 v[68:71], v121 offset:11264\n\t"
        "ds_read_b128 v[72:75], v121 offset:12288\n\t"
        "ds_read_b128 v[76:79], v121 offset:13312\n\t"
        "ds_read_b128 v[80:83], v121 offset:14336\n\t"
        "ds_read_b128 v[84:87], v121 offset:15360\n\t"
        "s_and_b32 s80, s40, 3\n\t"
        "s_lshl_b32 s80, s80, 17\n\t"
        "s_add_u32 s57, s80, %[nA]\n\t"
        "s_add_u32 s58, s57, 0x1000\n\t"
        "s_add_u32 s59, s57, 0x8000\n\t"
        "s_add_u32 s60, s57, 0x9000\n\t"
        "s_and_b32 s80, s40, 3\n\t"
        "s_lshl_b32 s80, s80, 17\n\t"
        "s_add_u32 s63, s80, %[nC]\n\t"
        "s_add_u32 s66, s80, %[nS]\n\t"
        "s_add_u32 s81, s80, 0x20000\n\t"
        "s_and_b32 s81, s81, 0x60000\n\t"
        "s_add_u32 s67, s81, %[nW]\n\t"
        "s_add_u32 s79, s81, %[nT]\n\t"
        "s_cmp_eq_u32 %[w], 2\n\t"
        "s_cselect_b32 s81, s80, s81\n\t"
        "s_xor_b32 s81, s81, 0x40000\n\t"
        "s_add_u32 s68, s81, %[pob]\n\t"
        "s_lshl_b32 s72, s40, 17\n\t"
        "s_add_u32 s72, s72, %[kvo]\n\t"
        "s_add_u32 s80, s40, 1\n\t"
        "s_min_u32 s80, s80, 63\n\t"
        "s_lshl_b32 s80, s80, 18\n\t"
        "s_add_u32 s69, s80, %[kx]\n\t"
        "s_add_u32 s70, s69, 0x1000\n\t"
        "s_lshl_b32 s80, s40, 18\n\t"
        "s_add_u32 s71, s80, %[vx]\n\t"
        "s_add_u32 s80, s40, 2\n\t"
        "s_lshl_b32 s81, s80, 9\n\t"
        "s_add_u32 s81, s81, %[kf]\n\t"
        "s_and_b32 s80, s80, 0x43\n\t"
        "s_cselect_b32 s64, s36, s81\n\t"
        "v_mov_b32_e32 v88, 0\n\t"
        "v_mov_b32_e32 v89, 0\n\t"
        "v_mov_b32_e32 v90, 0\n\t"
        "v_mov_b32_e32 v91, 0\n\t"
        "v_mov_b32_e32 v92, 0\n\t"
        "v_mov_b32_e32 v93, 0\n\t"
        "v_mov_b32_e32 v94, 0\n\t"
        "v_mov_b32_e32 v95, 0\n\t"
        "v_mov_b32_e32 v96, 0\n\t"
        "v_mov_b32_e32 v97, 0\n\t"
        "v_mov_b32_e32 v98, 0\n\t"
        "v_mov_b32_e32 v99, 0\n\t"
        "v_mov_b32_e32 v100, 0\n\t"
        "v_mov_b32_e32 v101, 0\n\t"
        "v_mov_b32_e32 v102, 0\n\t"
        "v_mov_b32_e32 v103, 0\n\t"
        "v_mov_b32_e32 v52, v112\n\t"
        "v_mov_b32_e32 v53, v113\n\t"
        "v_mov_b32_e32 v49, 0\n\t"
        "v_lshlrev_b32_e32 v51, 6, v120\n\t"
        "s_cmp_eq_u32 %[w], 0\n\t"
        "s_cselect_b32 s73, %[nT], s36\n\t"
        "s_cselect_b32 s74, %[pra], s36\n\t"
        "v_mov_b32_e32 v50, s40\n\t"
        "s_cmp_ge_u32 s40, 60\n\t"
        "s_cselect_b32 s65, s74, s36\n\t"
        "s_waitcnt lgkmcnt(0)\n\t"
        "s_barrier\n\t"
        "s_memrealtime s[98:99]\n\t"
        "s_waitcnt lgkmcnt(0)\n\t"
        "s_mov_b32 s41, s98\n\t"
        "v_mov_b32_e32 v48, s98\n\t"
        ".p2align 6\n\t"
        ".Lloop%=:\n\t"
        "s_mov_b32 s97, 0\n\t"
        "s_movk_i32 s96, 0x1000\n\t"
        ".Lwa%=:\n\t"
        "s_memrealtime s[98:99]\n\t"
        "s_addk_i32 s96, -1\n\t"
        "s_cmp_lt_i32 s96, 0\n\t"
        "s_cbranch_scc1 .Lta%=\n\t"
        "s_waitcnt lgkmcnt(0)\n\t"
        "s_sub_i32 s98, s98, s41\n\t"
        "s_cmp_lt_i32 s98, %c[iFT]\n\t"
        "s_cbranch_scc1 .Lwa%=\n\t"
        "s_cmp_ge_i32 s98, 0\n\t"
        "s_cbranch_scc1 .Lxa%=\n\t"
        ".Lqa%=:\n\t"
        "s_nop %c[iFN]\n\t"
        "s_addk_i32 s98, 1\n\t"
        "s_cmp_lt_i32 s98, 0\n\t"
        "s_cbranch_scc1 .Lqa%=\n\t"
        ".Lxa%=:\n\t"
        "buffer_load_dwordx4 v[0:3], v120, %[srd], s57 offen sc1\n\t"
        "s_nop 7\n\t"
        "buffer_load_dwordx4 v[4:7], v120, %[srd], s58 offen sc1\n\t"
        "s_nop 7\n\t"
        "buffer_load_dwordx4 v[8:11], v120, %[srd], s59 offen sc1\n\t"
        "s_nop 7\n\t"
        "buffer_load_dwordx4 v[12:15], v120, %[srd], s60 offen sc1\n\t"
        "s_nop 15\n\t"
        "s_nop 15\n\t"
        "s_nop 3\n\t"
        "s_mov_b64 exec, 7\n\t"
        "buffer_store_dwordx2 v[48:49], v51, %[srd], s73 offen sc1\n\t"
        "s_mov_b64 exec, -1\n\t"
        "buffer_load_dwordx4 v[112:115], v120, %[wsd], s71 offen offset:0 sc1\n\t"
        "buffer_load_dwordx4 v[116:119], v120, %[wsd], s71 offen offset:1024 sc1\n\t"
        ".Lgo%=:\n\t"
        "s_waitcnt vmcnt(6)\n\t"
        "v_smfmac_f32_16x16x64_bf16 v[88:91], v[0:3], v[16:23], v122\n\t"
        "ds_write_b128 v120, v[0:3] offset:0\n\t"
        "v_smfmac_f32_16x16x64_bf16 v[92:95], v[0:3], v[24:31], v122\n\t"
        "v_smfmac_f32_16x16x64_bf16 v[96:99], v[0:3], v[32:39], v122\n\t"
        "v_smfmac_f32_16x16x64_bf16 v[100:103], v[0:3], v[40:47], v122\n\t"
        "s_waitcnt vmcnt(5)\n\t"
        "v_smfmac_f32_16x16x64_bf16 v[88:91], v[4:7], v[56:63], v122\n\t"
        "ds_write_b128 v120, v[4:7] offset:4096\n\t"
        "v_smfmac_f32_16x16x64_bf16 v[92:95], v[4:7], v[64:71], v122\n\t"
        "v_smfmac_f32_16x16x64_bf16 v[96:99], v[4:7], v[72:79], v122\n\t"
        "v_smfmac_f32_16x16x64_bf16 v[100:103], v[4:7], v[80:87], v122\n\t"
        "s_waitcnt vmcnt(4)\n\t"
        "v_smfmac_f32_16x16x64_bf16 v[88:91], v[8:11], v[128:135], v122\n\t"
        "ds_write_b128 v120, v[8:11] offset:8192\n\t"
        "v_smfmac_f32_16x16x64_bf16 v[92:95], v[8:11], v[136:143], v122\n\t"
        "s_waitcnt lgkmcnt(1)\n\t"
        "s_barrier\n\t"
        "v_smfmac_f32_16x16x64_bf16 v[96:99], v[8:11], v[192:199], v122\n\t"
        "ds_read_b128 v[16:19], v124 offset:0\n\t"
        "v_smfmac_f32_16x16x64_bf16 v[100:103], v[8:11], v[200:207], v122\n\t"
        "ds_read_b128 v[20:23], v125 offset:0\n\t"
        "s_waitcnt vmcnt(3)\n\t"
        "v_smfmac_f32_16x16x64_bf16 v[88:91], v[12:15], a[0:7], v122\n\t"
        "ds_write_b128 v120, v[12:15] offset:12288\n\t"
        "v_smfmac_f32_16x16x64_bf16 v[92:95], v[12:15], a[8:15], v122\n\t"
        "buffer_store_dword v50, off, %[wsd], s65 sc1\n\t"
        "v_smfmac_f32_16x16x64_bf16 v[96:99], v[12:15], a[64:71], v122\n\t"
        "ds_read_b128 v[24:27], v126 offset:0\n\t"
        "v_smfmac_f32_16x16x64_bf16 v[100:103], v[12:15], a[72:79], v122\n\t"
        "ds_read_b128 v[28:31], v124 offset:4096\n\t"
        "s_waitcnt lgkmcnt(4)\n\t"
        "v_smfmac_f32_16x16x64_bf16 v[88:91], v[16:19], a[16:23], v122\n\t"
        "ds_read_b128 v[32:35], v125 offset:4096\n\t"
        "v_smfmac_f32_16x16x64_bf16 v[92:95], v[16:19], a[24:31], v122\n\t"
        "ds_read_b128 v[36:39], v126 offset:4096\n\t"
        "s_waitcnt lgkmcnt(5)\n\t"
        "v_smfmac_f32_16x16x64_bf16 v[88:91], v[20:23], a[32:39], v122\n\t"
        "ds_read_b128 v[72:75], v121 offset:16384\n\t"
        "v_smfmac_f32_16x16x64_bf16 v[92:95], v[20:23], a[40:47], v122\n\t"
        "s_waitcnt lgkmcnt(5)\n\t"
        "s_barrier\n\t"
        "ds_read_b128 v[76:79], v121 offset:17408\n\t"
        "s_waitcnt lgkmcnt(5)\n\t"
        "v_smfmac_f32_16x16x64_bf16 v[88:91], v[24:27], a[48:55], v122\n\t"
        "ds_read_b128 v[0:3], v124 offset:12288\n\t"
        "v_smfmac_f32_16x16x64_bf16 v[92:95], v[24:27], a[56:63], v122\n\t"
        "ds_read_b128 v[80:83], v121 offset:18432\n\t"
        "s_waitcnt lgkmcnt(6)\n\t"
        "v_smfmac_f32_16x16x64_bf16 v[88:91], v[28:31], a[144:151], v122\n\t"
        "ds_read_b128 v[84:87], v121 offset:19456\n\t"
        "v_smfmac_f32_16x16x64_bf16 v[92:95], v[28:31], a[152:159], v122\n\t"
        "ds_read_b128 v[40:43], v124 offset:8192\n\t"
        "s_waitcnt lgkmcnt(7)\n\t"
        "v_smfmac_f32_16x16x64_bf16 v[88:91], v[32:35], a[160:167], v122\n\t"
        "ds_read_b128 v[44:47], v125 offset:8192\n\t"
        "v_mov_b32_e32 v12, 0x3f803f80\n\t"
        "v_smfmac_f32_16x16x64_bf16 v[92:95], v[32:35], a[168:175], v122\n\t"
        "ds_read_b128 v[56:59], v121 offset:20480\n\t"
        "v_mov_b32_e32 v13, 0x3f803f80\n\t"
        "s_waitcnt lgkmcnt(8)\n\t"
        "v_smfmac_f32_16x16x64_bf16 v[88:91], v[36:39], a[176:183], v122\n\t"
        "ds_read_b128 v[60:63], v121 offset:21504\n\t"
        "v_mov_b32_e32 v14, 0x3f803f80\n\t"
        "v_smfmac_f32_16x16x64_bf16 v[92:95], v[36:39], a[184:191], v122\n\t"
        "ds_read_b128 v[4:7], v125 offset:12288\n\t"
        "v_mov_b32_e32 v15, 0x3f803f80\n\t"
        "s_waitcnt lgkmcnt(8)\n\t"
        "s_waitcnt lgkmcnt(7)\n\t"
        "v_smfmac_f32_16x16x64_bf16 v[88:91], v[0:3], v[72:79], v122\n\t"
        "ds_read_b128 v[64:67], v121 offset:22528\n\t"
        "s_waitcnt lgkmcnt(6)\n\t"
        "v_smfmac_f32_16x16x64_bf16 v[92:95], v[0:3], v[80:87], v122\n\t"
        "ds_read_b128 v[68:71], v121 offset:23552\n\t"
        "s_waitcnt lgkmcnt(6)\n\t"
        "v_smfmac_f32_16x16x64_bf16 v[88:91], v[40:43], v[144:151], v122\n\t"
        "ds_read_b128 v[48:51], v126 offset:8192\n\t"
        "v_smfmac_f32_16x16x64_bf16 v[92:95], v[40:43], v[152:159], v122\n\t"
        "ds_read_b128 v[8:11], v126 offset:12288\n\t"
        "s_waitcnt lgkmcnt(7)\n\t"
        "v_smfmac_f32_16x16x64_bf16 v[88:91], v[44:47], v[160:167], v122\n\t"
        "ds_read_b128 v[72:75], v121 offset:24576\n\t"
        "v_smfmac_f32_16x16x64_bf16 v[92:95], v[44:47], v[168:175], v122\n\t"
        "ds_read_b128 v[76:79], v121 offset:25600\n\t"
        "s_waitcnt lgkmcnt(7)\n\t"
        "s_waitcnt lgkmcnt(6)\n\t"
        "v_smfmac_f32_16x16x64_bf16 v[88:91], v[4:7], v[56:63], v122\n\t"
        "ds_read_b128 v[80:83], v121 offset:26624\n\t"
        "s_waitcnt lgkmcnt(5)\n\t"
        "v_smfmac_f32_16x16x64_bf16 v[92:95], v[4:7], v[64:71], v122\n\t"
        "ds_read_b128 v[84:87], v121 offset:27648\n\t"
        "s_waitcnt lgkmcnt(5)\n\t"
        "v_smfmac_f32_16x16x64_bf16 v[88:91], v[48:51], v[176:183], v122\n\t"
        "v_mov_b32_e32 v64, 0\n\t"
        "v_smfmac_f32_16x16x64_bf16 v[92:95], v[48:51], v[184:191], v122\n\t"
        "v_mov_b32_e32 v65, 0\n\t"
        "s_waitcnt lgkmcnt(4)\n\t"
        "v_smfmac_f32_16x16x64_bf16 v[88:91], v[8:11], a[128:135], v122\n\t"
        "v_mov_b32_e32 v66, 0\n\t"
        "v_smfmac_f32_16x16x64_bf16 v[92:95], v[8:11], a[136:143], v122\n\t"
        "v_mov_b32_e32 v67, 0\n\t"
        "s_waitcnt lgkmcnt(2)\n\t"
        "v_smfmac_f32_16x16x64_bf16 v[96:99], v[0:3], v[72:79], v122\n\t"
        "ds_read_b128 v[72:75], v121 offset:28672\n\t"
        "s_waitcnt lgkmcnt(1)\n\t"
        "v_smfmac_f32_16x16x64_bf16 v[100:103], v[0:3], v[80:87], v122\n\t"
        "ds_read_b128 v[76:79], v121 offset:29696\n\t"
        "v_smfmac_f32_16x16x64_bf16 v[96:99], v[16:19], a[80:87], v122\n\t"
        "v_add_f32_e32 v88, v88, v90\n\t"
        "v_add_f32_e32 v89, v89, v91\n\t"
        "v_smfmac_f32_16x16x64_bf16 v[100:103], v[16:19], a[88:95], v122\n\t"
        "v_cvt_pk_bf16_f32 v90, v88, v89\n\t"
        "v_add_f32_e32 v92, v92, v94\n\t"
        "v_smfmac_f32_16x16x64_bf16 v[96:99], v[20:23], a[96:103], v122\n\t"
        "ds_write_b32 v127, v90\n\t"
        "v_add_f32_e32 v93, v93, v95\n\t"
        "v_cvt_pk_bf16_f32 v91, v92, v93\n\t"
        "v_smfmac_f32_16x16x64_bf16 v[100:103], v[20:23], a[104:111], v122\n\t"
        "ds_write_b32 v127, v91 offset:384\n\t"
        "v_cmp_u_f32_e32 vcc, v88, v89\n\t"
        "v_smfmac_f32_16x16x64_bf16 v[96:99], v[24:27], a[112:119], v122\n\t"
        "ds_read_b64_tr_b16 v[56:57], v52 offset:0\n\t"
        "ds_read_b64_tr_b16 v[58:59], v52 offset:64\n\t"
        "v_smfmac_f32_16x16x64_bf16 v[100:103], v[24:27], a[120:127], v122\n\t"
        "ds_read_b64_tr_b16 v[60:61], v53 offset:0\n\t"
        "ds_read_b64_tr_b16 v[62:63], v53 offset:64\n\t"
        "v_smfmac_f32_16x16x64_bf16 v[96:99], v[28:31], a[208:215], v122\n\t"
        "s_cbranch_vccnz .Lf1%=\n\t"
        ".Lr1%=:\n\t"
        "ds_read_b128 v[80:83], v121 offset:30720\n\t"
        "v_smfmac_f32_16x16x64_bf16 v[100:103], v[28:31], a[216:223], v122\n\t"
        "ds_read_b128 v[84:87], v121 offset:31744\n\t"
        "v_mov_b32_e32 v91, 0\n\t"
        "v_smfmac_f32_16x16x64_bf16 v[96:99], v[32:35], a[224:231], v122\n\t"
        "v_mov_b32_e32 v94, 0\n\t"
        "v_smfmac_f32_16x16x64_bf16 v[100:103], v[32:35], a[232:239], v122\n\t"
        "ds_read_b128 v[32:35], v120 offset:20480\n\t"
        "v_smfmac_f32_16x16x64_bf16 v[96:99], v[36:39], a[240:247], v122\n\t"
        "s_waitcnt lgkmcnt(3)\n\t"
        "s_barrier\n\t"
        "v_mfma_f32_16x16x32_bf16 v[68:71], v[104:107], v[56:59], 0\n\t"
        "ds_read_b64_tr_b16 v[16:17], v123 offset:0\n\t"
        "v_mfma_f32_16x16x32_bf16 v[68:71], v[108:111], v[60:63], v[68:71]\n\t"
        "ds_read_b64_tr_b16 v[18:19], v123 offset:64\n\t"
        "v_smfmac_f32_16x16x64_bf16 v[100:103], v[36:39], a[248:255], v122\n\t"
        "ds_read_b64_tr_b16 v[20:21], v123 offset:768\n\t"
        "v_smfmac_f32_16x16x64_bf16 v[96:99], v[8:11], a[192:199], v122\n\t"
        "ds_read_b64_tr_b16 v[22:23], v123 offset:832\n\t"
        "v_smfmac_f32_16x16x64_bf16 v[100:103], v[8:11], a[200:207], v122\n\t"
        "s_nop 1\n\t"
        "ds_write_b128 v120, v[68:71] offset:16384\n\t"
        "v_smfmac_f32_16x16x64_bf16 v[96:99], v[4:7], v[72:79], v122\n\t"
        "buffer_load_dword v88, v127, %[srd], s61 offen offset:0 sc1\n\t"
        "s_waitcnt lgkmcnt(6)\n\t"
        "v_smfmac_f32_16x16x64_bf16 v[100:103], v[4:7], v[80:87], v122\n\t"
        "buffer_load_dword v89, v127, %[srd], s61 offen offset:256 sc1\n\t"
        "v_smfmac_f32_16x16x64_bf16 v[96:99], v[40:43], v[208:215], v122\n\t"
        "ds_read_b64_tr_b16 v[24:25], v123 offset:1536\n\t"
        "v_smfmac_f32_16x16x64_bf16 v[100:103], v[40:43], v[216:223], v122\n\t"
        "ds_read_b64_tr_b16 v[26:27], v123 offset:1600\n\t"
        "v_smfmac_f32_16x16x64_bf16 v[96:99], v[44:47], v[224:231], v122\n\t"
        "s_waitcnt lgkmcnt(2)\n\t"
        "s_barrier\n\t"
        "ds_read_b128 v[36:39], v124 offset:16384\n\t"
        "v_smfmac_f32_16x16x64_bf16 v[100:103], v[44:47], v[232:239], v122\n\t"
        "ds_read_b128 v[56:59], v125 offset:16384\n\t"
        "v_smfmac_f32_16x16x64_bf16 v[96:99], v[48:51], v[240:247], v122\n\t"
        "ds_read_b128 v[60:63], v126 offset:16384\n\t"
        "v_smfmac_f32_16x16x64_bf16 v[100:103], v[48:51], v[248:255], v122\n\t"
        "ds_read_b64_tr_b16 v[28:29], v123 offset:2304\n\t"
        "ds_read_b64_tr_b16 v[30:31], v123 offset:2368\n\t"
        "buffer_load_dwordx4 v[84:87], v120, %[srd], s66 offen sc1\n\t"
        "s_waitcnt lgkmcnt(4)\n\t"
        "s_cmp_eq_u32 s40, 63\n\t"
        "v_add_f32_e32 v68, v68, v36\n\t"
        "v_add_f32_e32 v69, v69, v37\n\t"
        "v_add_f32_e32 v70, v70, v38\n\t"
        "v_add_f32_e32 v71, v71, v39\n\t"
        "v_add_f32_e32 v96, v96, v98\n\t"
        "v_add_f32_e32 v97, v97, v99\n\t"
        "s_waitcnt lgkmcnt(2)\n\t"
        "v_cvt_pk_bf16_f32 v50, v96, v97\n\t"
        "v_pk_add_f32 v[100:101], v[100:101], v[102:103]\n\t"
        "ds_write_b32 v35, v50\n\t"
        "v_cvt_pk_bf16_f32 v51, v100, v101\n\t"
        "ds_write_b32 v35, v51 offset:384\n\t"
        "v_pk_add_f32 v[56:57], v[56:57], v[60:61]\n\t"
        "s_cbranch_scc1 .Lend%=\n\t"
        "v_pk_add_f32 v[58:59], v[58:59], v[62:63]\n\t"
        "v_pk_add_f32 v[68:69], v[68:69], v[56:57]\n\t"
        "v_pk_add_f32 v[70:71], v[70:71], v[58:59]\n\t"
        "v_max3_f32 v2, v68, v69, v70\n\t"
        "v_max_f32_e32 v2, v2, v71\n\t"
        "s_nop 1\n\t"
        "v_max_f32_dpp v3, v2, v2 row_ror:8 row_mask:0xf bank_mask:0xf\n\t"
        "v_max_f32_dpp v1, v2, v2 row_ror:8 row_mask:0xf bank_mask:0xf\n\t"
        "s_nop 1\n\t"
        "v_permlane16_swap_b32_e32 v3, v1\n\t"
        "v_max_f32_e32 v2, v3, v1\n\t"
        "v_max_f32_e32 v3, v3, v1\n\t"
        "s_waitcnt lgkmcnt(0)\n\t"
        "s_barrier\n\t"
        "ds_read_b64_tr_b16 v[72:73], v32 offset:0\n\t"
        "v_permlane32_swap_b32_e32 v2, v3\n\t"
        "ds_read_b64_tr_b16 v[74:75], v32 offset:128\n\t"
        "v_max_f32_e32 v0, v2, v3\n\t"
        "ds_read_b64_tr_b16 v[76:77], v32 offset:1536\n\t"
        "v_mul_f32_e32 v2, 0xbe0293ee, v0\n\t"
        "ds_read_b64_tr_b16 v[78:79], v32 offset:1664\n\t"
        "v_fmamk_f32 v68, v68, 0x3e0293ee, v2\n\t"
        "v_fmamk_f32 v69, v69, 0x3e0293ee, v2\n\t"
        "v_fmamk_f32 v70, v70, 0x3e0293ee, v2\n\t"
        "v_fmamk_f32 v71, v71, 0x3e0293ee, v2\n\t"
        "v_exp_f32_e32 v68, v68\n\t"
        "v_exp_f32_e32 v69, v69\n\t"
        "v_exp_f32_e32 v70, v70\n\t"
        "s_waitcnt lgkmcnt(2)\n\t"
        "v_smfmac_f32_16x16x64_bf16 v[64:67], v[72:75], v[16:23], v122\n\t"
        "s_waitcnt lgkmcnt(0)\n\t"
        "v_smfmac_f32_16x16x64_bf16 v[64:67], v[76:79], v[24:31], v122\n\t"
        "s_waitcnt vmcnt(4)\n\t"
        "v_cvt_pk_bf16_f32 v36, v68, v69\n\t"
        "v_exp_f32_e32 v71, v71\n\t"
        "s_nop 0\n\t"
        "v_mov_b32_dpp v38, v36 row_ror:8 row_mask:0xf bank_mask:0xf\n\t"
        "v_cvt_pk_bf16_f32 v37, v70, v71\n\t"
        "s_nop 1\n\t"
        "v_mov_b32_dpp v39, v37 row_ror:8 row_mask:0xf bank_mask:0xf\n\t"
        "v_pk_add_f32 v[64:65], v[64:65], v[66:67]\n\t"
        "v_max3_f32 v3, v0, v64, v65\n\t"
        "v_max3_f32 v1, v0, v64, v65\n\t"
        "v_mfma_f32_16x16x32_bf16 v[40:43], v[112:115], v[36:39], 0\n\t"
        "s_nop 0\n\t"
        "v_permlane16_swap_b32_e32 v3, v1\n\t"
        "v_max_f32_e32 v2, v3, v1\n\t"
        "v_max_f32_e32 v3, v3, v1\n\t"
        "v_mfma_f32_16x16x32_bf16 v[44:47], v[116:119], v[36:39], 0\n\t"
        "s_nop 0\n\t"
        "v_permlane32_swap_b32_e32 v2, v3\n\t"
        "v_max_f32_e32 v2, v2, v3\n\t"
        "v_mfma_f32_16x16x32_bf16 v[96:99], v[12:15], v[36:39], 0\n\t"
        "v_mul_f32_e32 v2, 0xbe0293ee, v2\n\t"
        "v_fmamk_f32 v10, v0, 0x3e0293ee, v2\n\t"
        "v_fmamk_f32 v64, v64, 0x3e0293ee, v2\n\t"
        "v_exp_f32_e32 v10, v10\n\t"
        "v_fmamk_f32 v65, v65, 0x3e0293ee, v2\n\t"
        "v_exp_f32_e32 v64, v64\n\t"
        "s_nop 1\n\t"
        "v_mul_f32_e32 v96, v96, v10\n\t"
        "v_exp_f32_e32 v65, v65\n\t"
        "v_pk_mul_f32 v[40:41], v[40:41], v[10:11] op_sel_hi:[1,0]\n\t"
        "v_cvt_pk_bf16_f32 v90, v64, v65\n\t"
        "v_pk_mul_f32 v[42:43], v[42:43], v[10:11] op_sel_hi:[1,0]\n\t"
        "v_cvt_pk_bf16_f32 v95, v64, v65\n\t"
        "v_pk_mul_f32 v[44:45], v[44:45], v[10:11] op_sel_hi:[1,0]\n\t"
        "v_mfma_f32_16x16x16_bf16 v[96:99], v[12:13], v[90:91], v[96:99]\n\t"
        "s_waitcnt vmcnt(1)\n\t"
        "v_mul_f32_e32 v46, v46, v10\n\t"
        "v_mul_f32_e32 v47, v47, v10\n\t"
        "v_mfma_f32_16x16x16_bf16 v[40:43], v[88:89], v[90:91], v[40:43]\n\t"
        "v_max_u32_e32 v54, v88, v89\n\t"
        "v_cmp_eq_u32_e32 vcc, -1, v54\n\t"
        "v_mfma_f32_16x16x16_bf16 v[44:47], v[88:89], v[94:95], v[44:47]\n\t"
        "s_nop 0\n\t"
        "v_rcp_f32_e32 v2, v96\n\t"
        "s_nop 0\n\t"
        "s_cbranch_vccnz .Lfb%=\n\t"
        ".Lrb%=:\n\t"
        "s_nop 0\n\t"
        "v_pk_mul_f32 v[40:41], v[40:41], v[2:3] op_sel_hi:[1,0]\n\t"
        "v_pk_mul_f32 v[42:43], v[42:43], v[2:3] op_sel_hi:[1,0]\n\t"
        "v_cvt_pk_bf16_f32 v100, v40, v41\n\t"
        "v_pk_mul_f32 v[44:45], v[44:45], v[2:3] op_sel_hi:[1,0]\n\t"
        "v_pk_mul_f32 v[46:47], v[46:47], v[2:3] op_sel_hi:[1,0]\n\t"
        "v_cvt_pk_bf16_f32 v101, v42, v43\n\t"
        "v_cvt_pk_bf16_f32 v102, v44, v45\n\t"
        "v_min_u32_e32 v100, -2, v100\n\t"
        "v_cvt_pk_bf16_f32 v103, v46, v47\n\t"
        "v_min_u32_e32 v102, -2, v102\n\t"
        "buffer_store_dwordx4 v[100:103], v33, %[srd], s63 offen sc1\n\t"
        "s_memrealtime s[76:77]\n\t"
        "v_mov_b32_e32 v8, -1\n\t"
        "v_mov_b32_e32 v9, -1\n\t"
        "v_mov_b32_e32 v10, -1\n\t"
        "v_mov_b32_e32 v11, -1\n\t"
        "v_permlane16_swap_b32_e32 v50, v51\n\t"
        "v_permlane16_swap_b32_e32 v88, v89\n\t"
        "v_mov_b32_e32 v14, s40\n\t"
        "s_waitcnt lgkmcnt(0)\n\t"
        "s_waitcnt vmcnt(1)\n\t"
        "s_mov_b32 s97, 0\n\t"
        ".Lsr%=:\n\t"
        "s_mov_b32 exec_hi, 0xffff\n\t"
        "v_subrev_u32_e32 v0, s41, v84\n\t"
        "v_subrev_u32_e32 v1, s41, v86\n\t"
        "v_max_i32_e32 v0, v0, v1\n\t"
        "v_max_u32_e32 v1, v85, v87\n\t"
        "v_cmp_eq_u32_e32 vcc, -1, v1\n\t"
        "v_max_i32_dpp v0, v0, v0 quad_perm:[1,0,3,2] row_mask:0xf bank_mask:0xf\n\t"
        "v_or_b32_e32 v2, v85, v87\n\t"
        "s_nop 0\n\t"
        "v_max_i32_dpp v0, v0, v0 quad_perm:[2,3,0,1] row_mask:0xf bank_mask:0xf\n\t"
        "s_nop 1\n\t"
        "v_max_i32_dpp v0, v0, v0 row_ror:4 row_mask:0xf bank_mask:0xf\n\t"
        "s_nop 1\n\t"
        "v_max_i32_dpp v0, v0, v0 row_ror:8 row_mask:0xf bank_mask:0xf\n\t"
        "s_cbranch_vccnz .Lsf%=\n\t"
        "v_readlane_b32 s85, v0, 0\n\t"
        "v_readlane_b32 s81, v0, 16\n\t"
        "v_readlane_b32 s56, v0, 32\n\t"
        "v_and_b32_e32 v1, 1, v2\n\t"
        "v_cmp_ne_u32_e64 s[86:87], 0, v1\n\t"
        "v_and_b32_e32 v1, 2, v2\n\t"
        "v_cmp_ne_u32_e64 s[88:89], 0, v1\n\t"
        "v_and_b32_e32 v1, 4, v2\n\t"
        "v_cmp_ne_u32_e64 s[90:91], 0, v1\n\t"
        "s_max_i32 s56, s56, s81\n\t"
        "s_add_u32 s92, s85, s41\n\t"
        "s_sub_u32 s92, s92, s43\n\t"
        "s_add_i32 s93, s56, s47\n\t"
        "s_mov_b32 s44, s46\n\t"
        "s_mov_b32 s46, s45\n\t"
        "s_mov_b32 s94, s48\n\t"
        "s_mov_b32 s43, s41\n\t"
        "s_cmp_gt_i32 s56, s54\n\t"
        "s_cbranch_scc1 .Lmv%=\n\t"
        "s_mov_b32 s37, 0\n\t"
        "s_mov_b32 s48, 0\n\t"
        "s_mov_b32 s95, 0\n\t"
        ".Lsd%=:\n\t"
        "s_mov_b32 exec_hi, -1\n\t"
        "s_add_u32 s42, s43, s46\n\t"
        "s_sleep %c[HSLP]\n\t"
        "buffer_load_dwordx2 v[48:49], off, %[wsd], s64 sc1\n\t"
        "buffer_load_dwordx4 v[104:107], v120, %[wsd], s69 offen sc1\n\t"
        "buffer_load_dwordx4 v[108:111], v120, %[wsd], s70 offen sc1\n\t"
        "buffer_store_dwordx2 v[50:51], v34, %[wsd], s72 offen sc1\n\t"
        "buffer_store_dwordx2 v[88:89], v34, %[wsd], s72 offen offset:2048 sc1\n\t"
        "s_lshl_b32 s80, %[w], 8\n\t"
        "s_add_u32 s80, s80, s61\n\t"
        "s_add_u32 s80, s80, 24576\n\t"
        "buffer_store_dwordx2 v[8:9], v34, %[srd], s80 offen sc1\n\t"
        "s_cmp_eq_u32 %[w], 0\n\t"
        "s_cselect_b32 exec_hi, 0, -1\n\t"
        "buffer_store_dwordx4 v[8:11], v120, %[srd], s68 offen sc1\n\t"
        "s_mov_b32 exec_hi, -1\n\t"
        "s_mov_b64 exec, 1\n\t"
        "buffer_store_dword v14, off, %[wsd], s74 sc1\n\t"
        "s_mov_b64 exec, -1\n\t"
        "s_cmp_ge_u32 s40, %c[iCT0]\n\t"
        "s_cselect_b32 s85, 1, 0\n\t"
        "s_cmp_lg_u64 s[86:87], 0\n\t"
        "s_cselect_b32 s86, s85, 0\n\t"
        "s_cmp_lg_u64 s[88:89], 0\n\t"
        "s_cselect_b32 s87, s85, 0\n\t"
        "s_cmp_lg_u64 s[90:91], 0\n\t"
        "s_cselect_b32 s88, s85, 0\n\t"
        "s_or_b32 s80, s94, s86\n\t"
        "s_or_b32 s80, s80, s87\n\t"
        "s_andn2_b32 s90, s85, s80\n\t"
        "s_cmp_le_i32 s92, s44\n\t"
        "s_cselect_b32 s80, s85, 0\n\t"
        "s_andn2_b32 s91, s80, s88\n\t"
        "s_and_b32 s80, s40, %c[LDEC]\n\t"
        "s_cselect_b32 s89, 0, 1\n\t"
        "s_mov_b32 s80, s88\n\t"
        "s_add_i32 s81, s92, s50\n\t"
        "s_sub_i32 s81, s81, 4\n\t"
        "s_cmp_le_i32 s81, s44\n\t"
        "s_cselect_b32 s80, s80, 0\n\t"
        "s_cmp_lt_i32 s50, %c[LC24]\n\t"
        "s_cselect_b32 s80, s80, 0\n\t"
        "s_lshl2_add_u32 s50, s80, s50\n\t"
        "s_sub_i32 s50, s50, s89\n\t"
        "s_max_i32 s50, s50, %c[LC]\n\t"
        "s_lshl_b32 s80, s92, 1\n\t"
        "s_sub_i32 s81, s80, s52\n\t"
        "s_ashr_i32 s82, s81, %c[UPSH]\n\t"
        "s_max_i32 s82, s82, %c[TRUP]\n\t"
        "s_add_i32 s82, s82, s52\n\t"
        "s_min_i32 s82, s82, s80\n\t"
        "s_ashr_i32 s83, s81, 1\n\t"
        "s_min_i32 s83, s83, -%c[TRDN]\n\t"
        "s_add_i32 s83, s83, s52\n\t"
        "s_max_i32 s83, s83, s80\n\t"
        "s_cmp_gt_i32 s81, 0\n\t"
        "s_cselect_b32 s82, s82, s83\n\t"
        "s_cmp_lg_u32 s90, 0\n\t"
        "s_cselect_b32 s52, s82, s52\n\t"
        "s_add_i32 s80, s52, 1\n\t"
        "s_ashr_i32 s80, s80, 1\n\t"
        "s_add_i32 s45, s80, s50\n\t"
        "s_andn2_b32 s80, s86, s94\n\t"
        "s_cmp_lt_i32 s51, %c[LA24]\n\t"
        "s_cselect_b32 s80, s80, 0\n\t"
        "s_lshl2_add_u32 s51, s80, s51\n\t"
        "s_sub_i32 s51, s51, s89\n\t"
        "s_max_i32 s51, s51, %c[LA]\n\t"
        "s_lshl_b32 s80, s93, 1\n\t"
        "s_sub_i32 s81, s80, s53\n\t"
        "s_ashr_i32 s82, s81, %c[UPSH]\n\t"
        "s_max_i32 s82, s82, %c[TRUP]\n\t"
        "s_add_i32 s82, s82, s53\n\t"
        "s_min_i32 s82, s82, s80\n\t"
        "s_ashr_i32 s83, s81, 1\n\t"
        "s_min_i32 s83, s83, -%c[TRDN]\n\t"
        "s_add_i32 s83, s83, s53\n\t"
        "s_max_i32 s83, s83, s80\n\t"
        "s_cmp_gt_i32 s81, 0\n\t"
        "s_cselect_b32 s82, s82, s83\n\t"
        "s_cmp_lg_u32 s91, 0\n\t"
        "s_cselect_b32 s53, s82, s53\n\t"
        "s_add_i32 s80, s53, 1\n\t"
        "s_ashr_i32 s80, s80, 1\n\t"
        "s_add_i32 s47, s80, s51\n\t"
        "s_add_u32 s81, s47, %c[AUP]\n\t"
        "s_min_i32 s80, s95, s81\n\t"
        "s_max_i32 s47, s47, s80\n\t"
        "s_add_u32 s41, s42, s47\n\t"
        "s_add_u32 s40, s40, 1\n\t"
        "v_mov_b32_e32 v88, 0\n\t"
        "v_mov_b32_e32 v89, 0\n\t"
        "v_mov_b32_e32 v90, 0\n\t"
        "v_mov_b32_e32 v91, 0\n\t"
        "v_mov_b32_e32 v92, 0\n\t"
        "v_mov_b32_e32 v93, 0\n\t"
        "v_mov_b32_e32 v94, 0\n\t"
        "v_mov_b32_e32 v95, 0\n\t"
        "v_mov_b32_e32 v96, 0\n\t"
        "v_mov_b32_e32 v97, 0\n\t"
        "v_mov_b32_e32 v98, 0\n\t"
        "v_mov_b32_e32 v99, 0\n\t"
        "v_mov_b32_e32 v100, 0\n\t"
        "v_mov_b32_e32 v101, 0\n\t"
        "v_mov_b32_e32 v102, 0\n\t"
        "v_mov_b32_e32 v103, 0\n\t"
        "ds_read_b128 v[16:19], v121 offset:0\n\t"
        "ds_read_b128 v[20:23], v121 offset:1024\n\t"
        "ds_read_b128 v[24:27], v121 offset:2048\n\t"
        "ds_read_b128 v[28:31], v121 offset:3072\n\t"
        "ds_read_b128 v[32:35], v121 offset:4096\n\t"
        "ds_read_b128 v[36:39], v121 offset:5120\n\t"
        "ds_read_b128 v[40:43], v121 offset:6144\n\t"
        "ds_read_b128 v[44:47], v121 offset:7168\n\t"
        "ds_read_b128 v[56:59], v121 offset:8192\n\t"
        "ds_read_b128 v[60:63], v121 offset:9216\n\t"
        "ds_read_b128 v[64:67], v121 offset:10240\n\t"
        "ds_read_b128 v[68:71], v121 offset:11264\n\t"
        "ds_read_b128 v[72:75], v121 offset:12288\n\t"
        "ds_read_b128 v[76:79], v121 offset:13312\n\t"
        "ds_read_b128 v[80:83], v121 offset:14336\n\t"
        "ds_read_b128 v[84:87], v121 offset:15360\n\t"
        "v_mov_b32_e32 v6, 30720\n\t"
        "ds_read_b64 v[6:7], v6\n\t"
        "s_waitcnt lgkmcnt(0)\n\t"
        "v_readfirstlane_b32 s80, v6\n\t"
        "v_readfirstlane_b32 s81, v7\n\t"
        "s_cmp_eq_u32 s80, s40\n\t"
        "s_cselect_b32 s84, 1, 0\n\t"
        "s_cmp_eq_u32 s81, s40\n\t"
        "s_cselect_b32 s81, 2, 0\n\t"
        "s_or_b32 s84, s84, s81\n\t"
        "s_waitcnt vmcnt(7)\n\t"
        "s_cmp_eq_u32 s64, s36\n\t"
        "s_cbranch_scc1 .Lk11%=\n\t"
        "s_mov_b32 s97, 0\n\t"
        ".Lk01%=:\n\t"
        "v_readfirstlane_b32 s82, v48\n\t"
        "v_readfirstlane_b32 s83, v49\n\t"
        "s_xor_b32 s82, s82, %[tag]\n\t"
        "s_xor_b32 s83, s83, %[tag]\n\t"
        "s_or_b32 s82, s82, s83\n\t"
        "s_cbranch_scc0 .Lk11%=\n\t"
        "s_sleep 3\n\t"
        "buffer_load_dwordx2 v[48:49], off, %[wsd], s64 sc1\n\t"
        "s_add_u32 s97, s97, 1\n\t"
        "s_cmp_lt_u32 s97, 0x200000\n\t"
        "s_cbranch_scc0 .Lgu%=\n\t"
        "s_waitcnt vmcnt(0)\n\t"
        "s_branch .Lk01%=\n\t"
        ".Lk11%=:\n\t"
        "v_mov_b32_e32 v48, s76\n\t"
        "v_mov_b32_e32 v49, s84\n\t"
        "s_mov_b32 s85, s79\n\t"
        "s_and_b32 s80, s40, 3\n\t"
        "s_lshl_b32 s80, s80, 17\n\t"
        "s_add_u32 s57, s80, %[nA]\n\t"
        "s_add_u32 s58, s57, 0x1000\n\t"
        "s_add_u32 s59, s57, 0x8000\n\t"
        "s_add_u32 s60, s57, 0x9000\n\t"
        "s_and_b32 s80, s40, 3\n\t"
        "s_lshl_b32 s80, s80, 17\n\t"
        "s_add_u32 s63, s80, %[nC]\n\t"
        "s_add_u32 s66, s80, %[nS]\n\t"
        "s_add_u32 s81, s80, 0x20000\n\t"
        "s_and_b32 s81, s81, 0x60000\n\t"
        "s_add_u32 s67, s81, %[nW]\n\t"
        "s_add_u32 s79, s81, %[nT]\n\t"
        "s_cmp_eq_u32 %[w], 2\n\t"
        "s_cselect_b32 s81, s80, s81\n\t"
        "s_xor_b32 s81, s81, 0x40000\n\t"
        "s_add_u32 s68, s81, %[pob]\n\t"
        "s_lshl_b32 s72, s40, 17\n\t"
        "s_add_u32 s72, s72, %[kvo]\n\t"
        "v_lshlrev_b32_e32 v51, 6, v120\n\t"
        "s_cmp_eq_u32 %[w], 0\n\t"
        "s_cselect_b32 s73, s85, s36\n\t"
        "s_cselect_b32 s74, %[pra], s36\n\t"
        "v_mov_b32_e32 v50, s40\n\t"
        "s_cmp_ge_u32 s40, 60\n\t"
        "s_cselect_b32 s65, s74, s36\n\t"
        "s_add_u32 s80, s40, 1\n\t"
        "s_min_u32 s80, s80, 63\n\t"
        "s_lshl_b32 s80, s80, 18\n\t"
        "s_add_u32 s69, s80, %[kx]\n\t"
        "s_add_u32 s70, s69, 0x1000\n\t"
        "s_lshl_b32 s80, s40, 18\n\t"
        "s_add_u32 s71, s80, %[vx]\n\t"
        "s_add_u32 s80, s40, 2\n\t"
        "s_lshl_b32 s81, s80, 9\n\t"
        "s_add_u32 s81, s81, %[kf]\n\t"
        "s_and_b32 s80, s80, 0x43\n\t"
        "s_cselect_b32 s64, s36, s81\n\t"
        "s_mov_b32 s80, s61\n\t"
        "s_mov_b32 s61, s62\n\t"
        "s_mov_b32 s62, s80\n\t"
        "s_waitcnt lgkmcnt(0)\n\t"
        "s_branch .Lloop%=\n\t"
        ".Lmv%=:\n\t"
        "s_mov_b32 s48, 1\n\t"
        "s_cmp_eq_u32 s54, %c[iTOL]\n\t"
        "s_cbranch_scc1 .Lm1%=\n\t"
        "s_max_i32 s80, s85, s56\n\t"
        "s_add_u32 s43, s41, s80\n\t"
        "s_add_u32 s43, s43, %c[iPEN0]\n\t"
        "s_mov_b32 s54, %c[iTOL]\n\t"
        "s_add_u32 s46, s46, %c[iX0C]\n\t"
        "s_add_u32 s95, s47, %c[iX0A]\n\t"
        "v_and_b32_e32 v0, 3, v84\n\t"
        "v_and_b32_e32 v1, 3, v86\n\t"
        "v_lshlrev_b32_e64 v0, v0, 1\n\t"
        "v_lshlrev_b32_e64 v1, v1, 1\n\t"
        "v_or_b32_e32 v0, v0, v1\n\t"
        "s_mov_b32 s80, 0\n\t"
        "v_and_b32_e32 v1, 1, v0\n\t"
        "v_cmp_ne_u32_e32 vcc, 0, v1\n\t"
        "s_nop 1\n\t"
        "s_cmp_lg_u64 vcc, 0\n\t"
        "s_cselect_b32 s81, 2, 0\n\t"
        "s_or_b32 s80, s80, s81\n\t"
        "v_and_b32_e32 v1, 2, v0\n\t"
        "v_cmp_ne_u32_e32 vcc, 0, v1\n\t"
        "s_nop 1\n\t"
        "s_cmp_lg_u64 vcc, 0\n\t"
        "s_cselect_b32 s81, 4, 0\n\t"
        "s_or_b32 s80, s80, s81\n\t"
        "v_and_b32_e32 v1, 4, v0\n\t"
        "v_cmp_ne_u32_e32 vcc, 0, v1\n\t"
        "s_nop 1\n\t"
        "s_cmp_lg_u64 vcc, 0\n\t"
        "s_cselect_b32 s81, 8, 0\n\t"
        "s_or_b32 s80, s80, s81\n\t"
        "v_and_b32_e32 v1, 8, v0\n\t"
        "v_cmp_ne_u32_e32 vcc, 0, v1\n\t"
        "s_nop 1\n\t"
        "s_cmp_lg_u64 vcc, 0\n\t"
        "s_cselect_b32 s81, 16, 0\n\t"
        "s_or_b32 s80, s80, s81\n\t"
        "s_lshr_b32 s80, 0x1adf1210, s80\n\t"
        "s_and_b32 s55, s80, 3\n\t"
        "s_branch .Lal%=\n\t"
        ".Lm1%=:\n\t"
        "s_add_u32 s80, s37, %c[iPEN2]\n\t"
        "s_min_i32 s80, s80, %c[iPENM]\n\t"
        "s_cmp_lg_u32 s37, 0\n\t"
        "s_cselect_b32 s37, s80, %c[iPEN]\n\t"
        "s_add_u32 s80, s56, s37\n\t"
        "s_max_i32 s80, s80, %c[iPMIN]\n\t"
        "s_add_u32 s43, s41, s80\n\t"
        "s_add_u32 s95, s93, %c[LA]\n\t"
        "s_add_u32 s46, s46, %c[iXR]\n\t"
        ".Lal%=:\n\t"
        "s_sub_u32 s80, s55, s43\n\t"
        "s_and_b32 s80, s80, %c[iPHM]\n\t"
        "s_add_u32 s43, s43, s80\n\t"
        "s_branch .Lsd%=\n\t"
        ".Lsf%=:\n\t"
        "s_mov_b32 exec_hi, -1\n\t"
        "s_sleep 2\n\t"
        "buffer_load_dwordx4 v[84:87], v120, %[srd], s66 offen sc1\n\t"
        "s_add_u32 s97, s97, 1\n\t"
        "s_cmp_lt_u32 s97, 0x100000\n\t"
        "s_cbranch_scc0 .Lgu%=\n\t"
        "s_waitcnt vmcnt(0)\n\t"
        "s_branch .Lsr%=\n\t"
        ".Lf1%=:\n\t"
        "ds_read_b128 v[92:95], v120 offset:0\n\t"
        "ds_read_b128 v[68:71], v120 offset:4096\n\t"
        "v_max_u32_e32 v54, v16, v18\n\t"
        "v_max3_u32 v54, v54, v17, v19\n\t"
        "v_max3_u32 v54, v54, v20, v22\n\t"
        "v_max3_u32 v54, v54, v21, v23\n\t"
        "v_max3_u32 v54, v54, v24, v26\n\t"
        "v_max3_u32 v54, v54, v25, v27\n\t"
        "v_max3_u32 v54, v54, v28, v30\n\t"
        "v_max3_u32 v54, v54, v29, v31\n\t"
        "v_max3_u32 v54, v54, v32, v34\n\t"
        "v_max3_u32 v54, v54, v33, v35\n\t"
        "v_max3_u32 v54, v54, v36, v38\n\t"
        "v_max3_u32 v54, v54, v37, v39\n\t"
        "v_max3_u32 v54, v54, v40, v42\n\t"
        "v_max3_u32 v54, v54, v41, v43\n\t"
        "v_max3_u32 v54, v54, v44, v46\n\t"
        "v_max3_u32 v54, v54, v45, v47\n\t"
        "v_max3_u32 v54, v54, v48, v50\n\t"
        "v_max3_u32 v54, v54, v49, v51\n\t"
        "v_max3_u32 v54, v54, v0, v2\n\t"
        "v_max3_u32 v54, v54, v1, v3\n\t"
        "v_max3_u32 v54, v54, v4, v6\n\t"
        "v_max3_u32 v54, v54, v5, v7\n\t"
        "v_max3_u32 v54, v54, v8, v10\n\t"
        "v_max3_u32 v54, v54, v9, v11\n\t"
        "s_waitcnt lgkmcnt(0)\n\t"
        "v_max3_u32 v54, v54, v92, v94\n\t"
        "v_max3_u32 v54, v54, v93, v95\n\t"
        "v_max3_u32 v54, v54, v68, v70\n\t"
        "v_max3_u32 v54, v54, v69, v71\n\t"
        "ds_read_b128 v[92:95], v120 offset:8192\n\t"
        "ds_read_b128 v[68:71], v120 offset:12288\n\t"
        "s_waitcnt lgkmcnt(0)\n\t"
        "v_max3_u32 v54, v54, v92, v94\n\t"
        "v_max3_u32 v54, v54, v93, v95\n\t"
        "v_max3_u32 v54, v54, v68, v70\n\t"
        "v_max3_u32 v54, v54, v69, v71\n\t"
        "v_cmp_eq_u32_e32 vcc, -1, v54\n\t"
        "s_nop 1\n\t"
        "v_mov_b32_e32 v55, v55\n\t"
        "s_nop 0\n\t"
        "s_cbranch_vccz .Lr1%=\n\t"
        "buffer_load_dwordx4 v[0:3], v120, %[srd], s57 offen sc1\n\t"
        "buffer_load_dwordx4 v[4:7], v120, %[srd], s58 offen sc1\n\t"
        "buffer_load_dwordx4 v[8:11], v120, %[srd], s59 offen sc1\n\t"
        "buffer_load_dwordx4 v[12:15], v120, %[srd], s60 offen sc1\n\t"
        "s_add_u32 s80, s40, 1\n\t"
        "v_mov_b32_e32 v54, 30720\n\t"
        "v_mov_b32_e32 v55, s80\n\t"
        "ds_write_b32 v54, v55\n\t"
        "ds_read_b128 v[16:19], v121 offset:0\n\t"
        "ds_read_b128 v[20:23], v121 offset:1024\n\t"
        "ds_read_b128 v[24:27], v121 offset:2048\n\t"
        "ds_read_b128 v[28:31], v121 offset:3072\n\t"
        "ds_read_b128 v[32:35], v121 offset:4096\n\t"
        "ds_read_b128 v[36:39], v121 offset:5120\n\t"
        "ds_read_b128 v[40:43], v121 offset:6144\n\t"
        "ds_read_b128 v[44:47], v121 offset:7168\n\t"
        "ds_read_b128 v[56:59], v121 offset:8192\n\t"
        "ds_read_b128 v[60:63], v121 offset:9216\n\t"
        "ds_read_b128 v[64:67], v121 offset:10240\n\t"
        "ds_read_b128 v[68:71], v121 offset:11264\n\t"
        "ds_read_b128 v[72:75], v121 offset:12288\n\t"
        "ds_read_b128 v[76:79], v121 offset:13312\n\t"
        "ds_read_b128 v[80:83], v121 offset:14336\n\t"
        "ds_read_b128 v[84:87], v121 offset:15360\n\t"
        "s_mov_b32 s65, s36\n\t"
        "v_mov_b32_e32 v88, 0\n\t"
        "v_mov_b32_e32 v89, 0\n\t"
        "v_mov_b32_e32 v90, 0\n\t"
        "v_mov_b32_e32 v91, 0\n\t"
        "v_mov_b32_e32 v92, 0\n\t"
        "v_mov_b32_e32 v93, 0\n\t"
        "v_mov_b32_e32 v94, 0\n\t"
        "v_mov_b32_e32 v95, 0\n\t"
        "v_mov_b32_e32 v96, 0\n\t"
        "v_mov_b32_e32 v97, 0\n\t"
        "v_mov_b32_e32 v98, 0\n\t"
        "v_mov_b32_e32 v99, 0\n\t"
        "v_mov_b32_e32 v100, 0\n\t"
        "v_mov_b32_e32 v101, 0\n\t"
        "v_mov_b32_e32 v102, 0\n\t"
        "v_mov_b32_e32 v103, 0\n\t"
        "s_waitcnt lgkmcnt(0)\n\t"
        "s_barrier\n\t"
        ".Lfa%=:\n\t"
        "s_waitcnt vmcnt(0)\n\t"
        "v_max_u32_e32 v54, v0, v2\n\t"
        "v_max3_u32 v54, v54, v1, v3\n\t"
        "v_max3_u32 v54, v54, v4, v6\n\t"
        "v_max3_u32 v54, v54, v5, v7\n\t"
        "v_max3_u32 v54, v54, v8, v10\n\t"
        "v_max3_u32 v54, v54, v9, v11\n\t"
        "v_max3_u32 v54, v54, v12, v14\n\t"
        "v_max3_u32 v54, v54, v13, v15\n\t"
        "v_cmp_eq_u32_e32 vcc, -1, v54\n\t"
        "s_nop 1\n\t"
        "v_mov_b32_e32 v55, v55\n\t"
        "s_nop 0\n\t"
        "s_cbranch_vccz .Lfg%=\n\t"
        "s_sleep %c[ASLP]\n\t"
        "buffer_load_dwordx4 v[0:3], v120, %[srd], s57 offen sc1\n\t"
        "buffer_load_dwordx4 v[4:7], v120, %[srd], s58 offen sc1\n\t"
        "buffer_load_dwordx4 v[8:11], v120, %[srd], s59 offen sc1\n\t"
        "buffer_load_dwordx4 v[12:15], v120, %[srd], s60 offen sc1\n\t"
        "s_add_u32 s97, s97, 1\n\t"
        "s_cmp_lt_u32 s97, 0x100000\n\t"
        "s_cbranch_scc0 .Lgu%=\n\t"
        "s_branch .Lfa%=\n\t"
        ".Lfg%=:\n\t"
        "s_sleep %c[RSLP]\n\t"
        "s_branch .Lgo%=\n\t"
        ".Lfb%=:\n\t"
        "s_nop 7\n\t"
        "s_mov_b32 s97, 0\n\t"
        "s_add_u32 s80, s40, 1\n\t"
        "v_mov_b32_e32 v54, 30724\n\t"
        "v_mov_b32_e32 v55, s80\n\t"
        "ds_write_b32 v54, v55\n\t"
        ".Lfp%=:\n\t"
        "s_sleep %c[ASLP]\n\t"
        "buffer_load_dword v88, v127, %[srd], s61 offen offset:0 sc1\n\t"
        "buffer_load_dword v89, v127, %[srd], s61 offen offset:256 sc1\n\t"
        "s_add_u32 s97, s97, 1\n\t"
        "s_cmp_lt_u32 s97, 0x100000\n\t"
        "s_cbranch_scc0 .Lgu%=\n\t"
        "s_waitcnt vmcnt(0)\n\t"
        "v_max_u32_e32 v54, v88, v89\n\t"
        "v_cmp_eq_u32_e32 vcc, -1, v54\n\t"
        "s_nop 1\n\t"
        "v_mov_b32_e32 v55, v55\n\t"
        "s_nop 0\n\t"
        "s_cbranch_vccnz .Lfp%=\n\t"
        "v_mfma_f32_16x16x32_bf16 v[40:43], v[112:115], v[36:39], 0\n\t"
        "v_mfma_f32_16x16x32_bf16 v[44:47], v[116:119], v[36:39], 0\n\t"
        "s_nop 1\n\t"
        "v_mov_b32_e32 v55, v55\n\t"
        "s_nop 1\n\t"
        "v_mov_b32_e32 v55, v55\n\t"
        "s_nop 0\n\t"
        "v_pk_mul_f32 v[40:41], v[40:41], v[10:11] op_sel_hi:[1,0]\n\t"
        "v_pk_mul_f32 v[42:43], v[42:43], v[10:11] op_sel_hi:[1,0]\n\t"
        "v_pk_mul_f32 v[44:45], v[44:45], v[10:11] op_sel_hi:[1,0]\n\t"
        "v_pk_mul_f32 v[46:47], v[46:47], v[10:11] op_sel_hi:[1,0]\n\t"
        "v_mfma_f32_16x16x16_bf16 v[40:43], v[88:89], v[90:91], v[40:43]\n\t"
        "s_nop 0\n\t"
        "v_mfma_f32_16x16x16_bf16 v[44:47], v[88:89], v[94:95], v[44:47]\n\t"
        "s_nop 7\n\t"
        "s_branch .Lrb%=\n\t"
        ".Lta%=:\n\t"
        "s_waitcnt lgkmcnt(0)\n\t"
        "s_branch .Lxa%=\n\t"
        ".Lgu%=:\n\t"
        "s_mov_b64 exec, -1\n\t"
        "v_mov_b32_e32 v54, %c[ferr]\n\t"
        "v_mov_b32_e32 v55, 1\n\t"
        "buffer_store_dword v55, v54, %[wsd], 0 offen sc1\n\t"
        "s_endpgm\n\t"
        ".Lend%=:\n\t"
        "s_nop 0\n\t"
        "v_permlane16_swap_b32_e32 v50, v51\n\t"
        "v_mov_b32_e32 v0, 64\n\t"
        "buffer_store_dwordx2 v[50:51], v34, %[wsd], s72 offen sc1\n\t"
        "s_cmp_lg_u32 %[w], 0\n\t"
        "s_waitcnt vmcnt(0)\n\t"
        "s_barrier\n\t"
        "s_cbranch_scc1 .Le2%=\n\t"
        "s_mov_b64 exec, 1\n\t"
        "buffer_store_dword v0, off, %[wsd], %[pra] sc1\n\t"
        "s_mov_b64 exec, -1\n\t"
        ".Le2%=:\n\t"
        "s_endpgm"
        : [nA] "+s"(nA), [nS] "+s"(nS), [nT] "+s"(nT), [nW] "+s"(nW), [nC] "+s"(nC), [b0] "+s"(b0), [pob] "+s"(pob)
        : [srd] "s"(rhd), [wsd] "s"(wsd), [w] "s"(w), [tag] "s"(tag), [kx] "s"(kx), [vx] "s"(vx), [kf] "s"(kf), [kvo] "s"(kvo), [cal] "s"(cal), [sq] "s"(sq), [sk] "s"(sk), [m0b] "s"(m0b),
          [pra] "s"(pr_a), [ferr] "i"(F_ERR), [iD1] "i"(D1), [iD2] "i"(D2), [LC] "i"(LC0), [LA] "i"(LA0), [LC24] "i"(LC0 + 24), [LA24] "i"(LA0 + 24), [E1] "i"(2 * (D1 - LC0)), [E2] "i"(2 * (D2 - LA0)),
          [iPEN] "i"(PEN), [iPEN2] "i"(PEN2), [iPENM] "i"(PEN + 2 * PEN2), [iPEN0] "i"(PEN0), [iTOL] "i"(TOL), [iCT0] "i"(CT0), [iX0C] "i"(X0C), [iX0A] "i"(X0A), [iPHM] "i"(PH_M),
          [iFT] "i"(-3), [iFN] "i"(FINE_N), [AUP] "i"(A_UP), [ASLP] "i"(A_SLP), [LDEC] "i"(L_DEC), [UPSH] "i"(UP_SH), [TRUP] "i"(TR_UP), [TRDN] "i"(TR_DN), [HSLP] "i"(H_SLP), [iPMIN] "i"(PMIN), [VSLP] "i"(V_SLP), [RSLP] "i"(R_SLP), [iVD] "i"(V_DEL), [iXR] "i"(XR), [NX0] "i"(NEARX), [NX1] "i"(8192 - NEARX), [iCS1] "i"(CS1), [iCS2] "i"(CS2), [NY0] "i"(NEARY), [NY1] "i"(16384 - NEARY),
          "{v[120:127]}"(pc), "{v[112:113]}"(pd), "{v96}"(dv), "{v[98:105]}"(pa)
        : "memory", "vcc", "scc", "m0", "s36", "s37", "s38", "s39", "s40", "s41", "s42", "s43", "s44", "s45", "s46", "s47", "s48", "s49", "s50", "s51", "s52", "s53", "s54", "s55", "s56", "s57", "s58", "s59", "s60", "s61", "s62", "s63", "s64", "s65", "s66", "s67", "s68", "s69", "s70", "s71", "s72", "s73", "s74", "s75", "s76", "s77", "s78", "s79", "s80", "s81", "s82", "s83", "s84", "s85", "s86", "s87", "s88", "s89", "s90", "s91", "s92", "s93", "s94", "s95", "s96", "s97", "s98", "s99");
    } else {
        const int cb = i32;
        const v4u sv = mk_srd(p.Wv, 1024 * 4096 * 2), so = mk_srd(p.Wo, 1024 * 4096 * 2);
        const unsigned key = KEYM & ((cb & 1) * 2 + G), tu = cb >> 2, m0b = w * 32768, f = (g & 1) | ((c >> 1) << 1);
        const unsigned t0 = m0b + (16 * (g >> 1) + 4 * (g & 1) + (c & 1) + 8 * (c >> 1)) * 128 + (f << 5) + (n16 & 3) * 8, t1 = m0b + (16 * g + 4 * (c >> 1) + (c & 1)) * 64 + ((c >> 1) << 5) + (n16 & 3) * 8;
        v8u pa;
        pa[0] = (64 * w + (l >> 3)) * 8192 + vi * 128 + (((l & 7) ^ ((l >> 5) << 1)) << 4); pa[1] = pa[0] ^ 64u;
        pa[2] = (1024 * w + (l >> 2)) * 2048 + cb * 64 + (((l & 3) ^ ((g & 1) << 1)) << 4);
#pragma unroll
        for (int q = 0; q < 4; ++q) pa[3 + q] = t0 ^ (((w + q) & 3) << 5);
        pa[7] = t1;
        const v2u pb = {t1 ^ 32u, (unsigned)((w * 1024 + cb * 32) * 2048 + l * 1024) + (((3u - (so.x >> 7)) & 7u) << 7)};
        const v16u pc = {(unsigned)tid * 16, (n16 & 2) ? 0xEEEEu : 0x4444u, (unsigned)((vi & 1) * 1024 + w * 256 + n16 * 16 + g * 4), (unsigned)(4096 + tid * 16),
                   w < 3 ? (unsigned)((cb >> 4) * 32768 + ((cb >> 1) & 7) * 1024 + ((cb & 1) * 2 + (l >> 5)) * 256 + G * 128 + (l & 31) * 4) : OOB,
                   tid < 3 ? (unsigned)(l * 1024 + (32 + vi) * 8) : OOB, tid == 192 ? pr_a : OOB, (unsigned)(L_V + 8192 + w * 512 + (g & 1) * 256 + (g >> 1) * 64 + n16 * 4),
                   (unsigned)(L_V + (((l >> 2) & 1) + ((l >> 3) & 2)) * 128 + (l >> 5) * 64 + (l & 8) * 4 + (l & 3) * 8), 0u,
                   (unsigned)(L_V + w * 2048 + l * 8), (unsigned)(L_V + ((w + 1) & 3) * 2048 + 512 + l * 8), (unsigned)(L_V + ((w + 2) & 3) * 2048 + 1024 + l * 8), (unsigned)(L_V + ((w + 3) & 3) * 2048 + 1536 + l * 8), 0u, 0u};
        if (tid < 2) ((lu32*)(size_t)(L_V + L_FL))[tid] = ~0u;
    asm volatile(
        "s_mov_b32 s82, 3\n\t"
        ".Ld0%=:\n\t"
        "s_xor_b32 s80, s82, %[key]\n\t"
        "s_mul_i32 s81, s80, 131072\n\t"
        "s_lshl_b32 s80, s80, 11\n\t"
        "s_add_u32 s80, s80, %[m0b]\n\t"
        "s_add_u32 m0, s80, 0\n\t"
        "s_add_u32 s81, s81, 0\n\t"
        "buffer_load_dwordx4 v96, %[sv], s81 offen lds\n\t"
        "s_add_u32 m0, m0, 0x400\n\t"
        "s_add_u32 s81, s81, 65536\n\t"
        "buffer_load_dwordx4 v97, %[sv], s81 offen lds\n\t"
        "s_sub_u32 s82, s82, 1\n\t"
        "s_cbranch_scc0 .Ld0%=\n\t"
        "s_mov_b32 s82, 3\n\t"
        ".Ld1%=:\n\t"
        "s_xor_b32 s80, s82, %[key]\n\t"
        "s_mul_i32 s81, s80, 131072\n\t"
        "s_lshl_b32 s80, s80, 11\n\t"
        "s_add_u32 s80, s80, %[m0b]\n\t"
        "s_add_u32 m0, s80, 8192\n\t"
        "s_add_u32 s81, s81, 2097152\n\t"
        "buffer_load_dwordx4 v96, %[sv], s81 offen lds\n\t"
        "s_add_u32 m0, m0, 0x400\n\t"
        "s_add_u32 s81, s81, 65536\n\t"
        "buffer_load_dwordx4 v97, %[sv], s81 offen lds\n\t"
        "s_sub_u32 s82, s82, 1\n\t"
        "s_cbranch_scc0 .Ld1%=\n\t"
        "s_cmp_eq_u32 %[tu], 0\n\t"
        "s_cselect_b32 s81, 0, 0x7fff0000\n\t"
        "buffer_load_dword v110, v105, %[so], s81 offen\n\t"
        "s_mov_b32 s82, 3\n\t"
        ".Ld2%=:\n\t"
        "s_xor_b32 s80, s82, %[key]\n\t"
        "s_mul_i32 s81, s80, 131072\n\t"
        "s_lshl_b32 s80, s80, 11\n\t"
        "s_add_u32 s80, s80, %[m0b]\n\t"
        "s_add_u32 m0, s80, 16384\n\t"
        "s_add_u32 s81, s81, 4194304\n\t"
        "buffer_load_dwordx4 v96, %[sv], s81 offen lds\n\t"
        "s_add_u32 m0, m0, 0x400\n\t"
        "s_add_u32 s81, s81, 65536\n\t"
        "buffer_load_dwordx4 v97, %[sv], s81 offen lds\n\t"
        "s_sub_u32 s82, s82, 1\n\t"
        "s_cbranch_scc0 .Ld2%=\n\t"
        "s_cmp_eq_u32 %[tu], 1\n\t"
        "s_cselect_b32 s81, 0, 0x7fff0000\n\t"
        "buffer_load_dword v110, v105, %[so], s81 offen\n\t"
        "s_mov_b32 s82, 3\n\t"
        ".Ld3%=:\n\t"
        "s_xor_b32 s80, s82, %[key]\n\t"
        "s_mul_i32 s81, s80, 131072\n\t"
        "s_lshl_b32 s80, s80, 11\n\t"
        "s_add_u32 s80, s80, %[m0b]\n\t"
        "s_add_u32 m0, s80, 24576\n\t"
        "s_add_u32 s81, s81, 6291456\n\t"
        "buffer_load_dwordx4 v96, %[sv], s81 offen lds\n\t"
        "s_add_u32 m0, m0, 0x400\n\t"
        "s_add_u32 s81, s81, 65536\n\t"
        "buffer_load_dwordx4 v97, %[sv], s81 offen lds\n\t"
        "s_sub_u32 s82, s82, 1\n\t"
        "s_cbranch_scc0 .Ld3%=\n\t"
        "s_waitcnt vmcnt(26)\n\t"
        "ds_read_b64_tr_b16 v[128:129], v99 offset:0\n\t"
        "ds_read_b64_tr_b16 v[130:131], v99 offset:256\n\t"
        "ds_read_b64_tr_b16 v[132:133], v99 offset:4096\n\t"
        "ds_read_b64_tr_b16 v[134:135], v99 offset:4352\n\t"
        "ds_read_b64_tr_b16 v[136:137], v100 offset:0\n\t"
        "ds_read_b64_tr_b16 v[138:139], v100 offset:256\n\t"
        "ds_read_b64_tr_b16 v[140:141], v100 offset:4096\n\t"
        "ds_read_b64_tr_b16 v[142:143], v100 offset:4352\n\t"
        "ds_read_b64_tr_b16 v[144:145], v101 offset:0\n\t"
        "ds_read_b64_tr_b16 v[146:147], v101 offset:256\n\t"
        "ds_read_b64_tr_b16 v[148:149], v101 offset:4096\n\t"
        "ds_read_b64_tr_b16 v[150:151], v101 offset:4352\n\t"
        "ds_read_b64_tr_b16 v[152:153], v102 offset:0\n\t"
        "ds_read_b64_tr_b16 v[154:155], v102 offset:256\n\t"
        "ds_read_b64_tr_b16 v[156:157], v102 offset:4096\n\t"
        "ds_read_b64_tr_b16 v[158:159], v102 offset:4352\n\t"
        "s_waitcnt lgkmcnt(0)\n\t"
        "s_cmp_eq_u32 %[tu], 2\n\t"
        "s_cselect_b32 s81, 0, 0x7fff0000\n\t"
        "buffer_load_dword v110, v105, %[so], s81 offen\n\t"
        "s_mov_b32 s82, 3\n\t"
        ".Ld4%=:\n\t"
        "s_xor_b32 s80, s82, %[key]\n\t"
        "s_mul_i32 s81, s80, 65536\n\t"
        "s_lshl_b32 s80, s80, 11\n\t"
        "s_add_u32 s80, s80, %[m0b]\n\t"
        "s_add_u32 m0, s80, 0\n\t"
        "s_add_u32 s81, s81, 0\n\t"
        "buffer_load_dwordx4 v98, %[so], s81 offen lds\n\t"
        "s_add_u32 m0, m0, 0x400\n\t"
        "s_add_u32 s81, s81, 32768\n\t"
        "buffer_load_dwordx4 v98, %[so], s81 offen lds\n\t"
        "s_sub_u32 s82, s82, 1\n\t"
        "s_cbranch_scc0 .Ld4%=\n\t"
        "s_waitcnt vmcnt(27)\n\t"
        "ds_read_b64_tr_b16 v[160:161], v99 offset:8192\n\t"
        "ds_read_b64_tr_b16 v[162:163], v99 offset:8448\n\t"
        "ds_read_b64_tr_b16 v[164:165], v99 offset:12288\n\t"
        "ds_read_b64_tr_b16 v[166:167], v99 offset:12544\n\t"
        "ds_read_b64_tr_b16 v[168:169], v100 offset:8192\n\t"
        "ds_read_b64_tr_b16 v[170:171], v100 offset:8448\n\t"
        "ds_read_b64_tr_b16 v[172:173], v100 offset:12288\n\t"
        "ds_read_b64_tr_b16 v[174:175], v100 offset:12544\n\t"
        "ds_read_b64_tr_b16 v[176:177], v101 offset:8192\n\t"
        "ds_read_b64_tr_b16 v[178:179], v101 offset:8448\n\t"
        "ds_read_b64_tr_b16 v[180:181], v101 offset:12288\n\t"
        "ds_read_b64_tr_b16 v[182:183], v101 offset:12544\n\t"
        "ds_read_b64_tr_b16 v[184:185], v102 offset:8192\n\t"
        "ds_read_b64_tr_b16 v[186:187], v102 offset:8448\n\t"
        "ds_read_b64_tr_b16 v[188:189], v102 offset:12288\n\t"
        "ds_read_b64_tr_b16 v[190:191], v102 offset:12544\n\t"
        "s_waitcnt lgkmcnt(0)\n\t"
        "s_cmp_eq_u32 %[tu], 3\n\t"
        "s_cselect_b32 s81, 0, 0x7fff0000\n\t"
        "buffer_load_dword v110, v105, %[so], s81 offen\n\t"
        "s_mov_b32 s82, 3\n\t"
        ".Ld5%=:\n\t"
        "s_xor_b32 s80, s82, %[key]\n\t"
        "s_mul_i32 s81, s80, 65536\n\t"
        "s_lshl_b32 s80, s80, 11\n\t"
        "s_add_u32 s80, s80, %[m0b]\n\t"
        "s_add_u32 m0, s80, 8192\n\t"
        "s_add_u32 s81, s81, 262144\n\t"
        "buffer_load_dwordx4 v98, %[so], s81 offen lds\n\t"
        "s_add_u32 m0, m0, 0x400\n\t"
        "s_add_u32 s81, s81, 32768\n\t"
        "buffer_load_dwordx4 v98, %[so], s81 offen lds\n\t"
        "s_sub_u32 s82, s82, 1\n\t"
        "s_cbranch_scc0 .Ld5%=\n\t"
        "s_waitcnt vmcnt(27)\n\t"
        "ds_read_b64_tr_b16 v[192:193], v99 offset:16384\n\t"
        "ds_read_b64_tr_b16 v[194:195], v99 offset:16640\n\t"
        "ds_read_b64_tr_b16 v[196:197], v99 offset:20480\n\t"
        "ds_read_b64_tr_b16 v[198:199], v99 offset:20736\n\t"
        "ds_read_b64_tr_b16 v[200:201], v100 offset:16384\n\t"
        "ds_read_b64_tr_b16 v[202:203], v100 offset:16640\n\t"
        "ds_read_b64_tr_b16 v[204:205], v100 offset:20480\n\t"
        "ds_read_b64_tr_b16 v[206:207], v100 offset:20736\n\t"
        "ds_read_b64_tr_b16 v[208:209], v101 offset:16384\n\t"
        "ds_read_b64_tr_b16 v[210:211], v101 offset:16640\n\t"
        "ds_read_b64_tr_b16 v[212:213], v101 offset:20480\n\t"
        "ds_read_b64_tr_b16 v[214:215], v101 offset:20736\n\t"
        "ds_read_b64_tr_b16 v[216:217], v102 offset:16384\n\t"
        "ds_read_b64_tr_b16 v[218:219], v102 offset:16640\n\t"
        "ds_read_b64_tr_b16 v[220:221], v102 offset:20480\n\t"
        "ds_read_b64_tr_b16 v[222:223], v102 offset:20736\n\t"
        "s_waitcnt lgkmcnt(0)\n\t"
        "s_cmp_eq_u32 %[tu], 4\n\t"
        "s_cselect_b32 s81, 0, 0x7fff0000\n\t"
        "buffer_load_dword v110, v105, %[so], s81 offen\n\t"
        "s_mov_b32 s82, 3\n\t"
        ".Ld6%=:\n\t"
        "s_xor_b32 s80, s82, %[key]\n\t"
        "s_mul_i32 s81, s80, 65536\n\t"
        "s_lshl_b32 s80, s80, 11\n\t"
        "s_add_u32 s80, s80, %[m0b]\n\t"
        "s_add_u32 m0, s80, 16384\n\t"
        "s_add_u32 s81, s81, 524288\n\t"
        "buffer_load_dwordx4 v98, %[so], s81 offen lds\n\t"
        "s_add_u32 m0, m0, 0x400\n\t"
        "s_add_u32 s81, s81, 32768\n\t"
        "buffer_load_dwordx4 v98, %[so], s81 offen lds\n\t"
        "s_sub_u32 s82, s82, 1\n\t"
        "s_cbranch_scc0 .Ld6%=\n\t"
        "s_waitcnt vmcnt(27)\n\t"
        "ds_read_b64_tr_b16 v[224:225], v99 offset:24576\n\t"
        "ds_read_b64_tr_b16 v[226:227], v99 offset:24832\n\t"
        "ds_read_b64_tr_b16 v[228:229], v99 offset:28672\n\t"
        "ds_read_b64_tr_b16 v[230:231], v99 offset:28928\n\t"
        "ds_read_b64_tr_b16 v[232:233], v100 offset:24576\n\t"
        "ds_read_b64_tr_b16 v[234:235], v100 offset:24832\n\t"
        "ds_read_b64_tr_b16 v[236:237], v100 offset:28672\n\t"
        "ds_read_b64_tr_b16 v[238:239], v100 offset:28928\n\t"
        "ds_read_b64_tr_b16 v[240:241], v101 offset:24576\n\t"
        "ds_read_b64_tr_b16 v[242:243], v101 offset:24832\n\t"
        "ds_read_b64_tr_b16 v[244:245], v101 offset:28672\n\t"
        "ds_read_b64_tr_b16 v[246:247], v101 offset:28928\n\t"
        "ds_read_b64_tr_b16 v[248:249], v102 offset:24576\n\t"
        "ds_read_b64_tr_b16 v[250:251], v102 offset:24832\n\t"
        "ds_read_b64_tr_b16 v[252:253], v102 offset:28672\n\t"
        "ds_read_b64_tr_b16 v[254:255], v102 offset:28928\n\t"
        "s_waitcnt lgkmcnt(0)\n\t"
        "s_cmp_eq_u32 %[tu], 5\n\t"
        "s_cselect_b32 s81, 0, 0x7fff0000\n\t"
        "buffer_load_dword v110, v105, %[so], s81 offen\n\t"
        "s_mov_b32 s82, 3\n\t"
        ".Ld7%=:\n\t"
        "s_xor_b32 s80, s82, %[key]\n\t"
        "s_mul_i32 s81, s80, 65536\n\t"
        "s_lshl_b32 s80, s80, 11\n\t"
        "s_add_u32 s80, s80, %[m0b]\n\t"
        "s_add_u32 m0, s80, 24576\n\t"
        "s_add_u32 s81, s81, 786432\n\t"
        "buffer_load_dwordx4 v98, %[so], s81 offen lds\n\t"
        "s_add_u32 m0, m0, 0x400\n\t"
        "s_add_u32 s81, s81, 32768\n\t"
        "buffer_load_dwordx4 v98, %[so], s81 offen lds\n\t"
        "s_sub_u32 s82, s82, 1\n\t"
        "s_cbranch_scc0 .Ld7%=\n\t"
        "s_waitcnt vmcnt(27)\n\t"
        "ds_read_b64_tr_b16 a[0:1], v103 offset:0\n\t"
        "ds_read_b64_tr_b16 a[2:3], v103 offset:128\n\t"
        "ds_read_b64_tr_b16 a[4:5], v103 offset:4096\n\t"
        "ds_read_b64_tr_b16 a[6:7], v103 offset:4224\n\t"
        "ds_read_b64_tr_b16 a[8:9], v104 offset:0\n\t"
        "ds_read_b64_tr_b16 a[10:11], v104 offset:128\n\t"
        "ds_read_b64_tr_b16 a[12:13], v104 offset:4096\n\t"
        "ds_read_b64_tr_b16 a[14:15], v104 offset:4224\n\t"
        "ds_read_b64_tr_b16 a[16:17], v103 offset:512\n\t"
        "ds_read_b64_tr_b16 a[18:19], v103 offset:640\n\t"
        "ds_read_b64_tr_b16 a[20:21], v103 offset:4608\n\t"
        "ds_read_b64_tr_b16 a[22:23], v103 offset:4736\n\t"
        "ds_read_b64_tr_b16 a[24:25], v104 offset:512\n\t"
        "ds_read_b64_tr_b16 a[26:27], v104 offset:640\n\t"
        "ds_read_b64_tr_b16 a[28:29], v104 offset:4608\n\t"
        "ds_read_b64_tr_b16 a[30:31], v104 offset:4736\n\t"
        "s_waitcnt lgkmcnt(0)\n\t"
        "s_cmp_eq_u32 %[tu], 6\n\t"
        "s_cselect_b32 s81, 0, 0x7fff0000\n\t"
        "buffer_load_dword v110, v105, %[so], s81 offen\n\t"
        "s_mov_b32 s82, 3\n\t"
        ".Ld8%=:\n\t"
        "s_xor_b32 s80, s82, %[key]\n\t"
        "s_mul_i32 s81, s80, 65536\n\t"
        "s_lshl_b32 s80, s80, 11\n\t"
        "s_add_u32 s80, s80, %[m0b]\n\t"
        "s_add_u32 m0, s80, 0\n\t"
        "s_add_u32 s81, s81, 1048576\n\t"
        "buffer_load_dwordx4 v98, %[so], s81 offen lds\n\t"
        "s_add_u32 m0, m0, 0x400\n\t"
        "s_add_u32 s81, s81, 32768\n\t"
        "buffer_load_dwordx4 v98, %[so], s81 offen lds\n\t"
        "s_sub_u32 s82, s82, 1\n\t"
        "s_cbranch_scc0 .Ld8%=\n\t"
        "s_waitcnt vmcnt(27)\n\t"
        "ds_read_b64_tr_b16 a[32:33], v103 offset:8192\n\t"
        "ds_read_b64_tr_b16 a[34:35], v103 offset:8320\n\t"
        "ds_read_b64_tr_b16 a[36:37], v103 offset:12288\n\t"
        "ds_read_b64_tr_b16 a[38:39], v103 offset:12416\n\t"
        "ds_read_b64_tr_b16 a[40:41], v104 offset:8192\n\t"
        "ds_read_b64_tr_b16 a[42:43], v104 offset:8320\n\t"
        "ds_read_b64_tr_b16 a[44:45], v104 offset:12288\n\t"
        "ds_read_b64_tr_b16 a[46:47], v104 offset:12416\n\t"
        "ds_read_b64_tr_b16 a[48:49], v103 offset:8704\n\t"
        "ds_read_b64_tr_b16 a[50:51], v103 offset:8832\n\t"
        "ds_read_b64_tr_b16 a[52:53], v103 offset:12800\n\t"
        "ds_read_b64_tr_b16 a[54:55], v103 offset:12928\n\t"
        "ds_read_b64_tr_b16 a[56:57], v104 offset:8704\n\t"
        "ds_read_b64_tr_b16 a[58:59], v104 offset:8832\n\t"
        "ds_read_b64_tr_b16 a[60:61], v104 offset:12800\n\t"
        "ds_read_b64_tr_b16 a[62:63], v104 offset:12928\n\t"
        "s_waitcnt lgkmcnt(0)\n\t"
        "s_cmp_eq_u32 %[tu], 7\n\t"
        "s_cselect_b32 s81, 0, 0x7fff0000\n\t"
        "buffer_load_dword v110, v105, %[so], s81 offen\n\t"
        "s_mov_b32 s82, 3\n\t"
        ".Ld9%=:\n\t"
        "s_xor_b32 s80, s82, %[key]\n\t"
        "s_mul_i32 s81, s80, 65536\n\t"
        "s_lshl_b32 s80, s80, 11\n\t"
        "s_add_u32 s80, s80, %[m0b]\n\t"
        "s_add_u32 m0, s80, 8192\n\t"
        "s_add_u32 s81, s81, 1310720\n\t"
        "buffer_load_dwordx4 v98, %[so], s81 offen lds\n\t"
        "s_add_u32 m0, m0, 0x400\n\t"
        "s_add_u32 s81, s81, 32768\n\t"
        "buffer_load_dwordx4 v98, %[so], s81 offen lds\n\t"
        "s_sub_u32 s82, s82, 1\n\t"
        "s_cbranch_scc0 .Ld9%=\n\t"
        "s_waitcnt vmcnt(27)\n\t"
        "ds_read_b64_tr_b16 a[64:65], v103 offset:16384\n\t"
        "ds_read_b64_tr_b16 a[66:67], v103 offset:16512\n\t"
        "ds_read_b64_tr_b16 a[68:69], v103 offset:20480\n\t"
        "ds_read_b64_tr_b16 a[70:71], v103 offset:20608\n\t"
        "ds_read_b64_tr_b16 a[72:73], v104 offset:16384\n\t"
        "ds_read_b64_tr_b16 a[74:75], v104 offset:16512\n\t"
        "ds_read_b64_tr_b16 a[76:77], v104 offset:20480\n\t"
        "ds_read_b64_tr_b16 a[78:79], v104 offset:20608\n\t"
        "ds_read_b64_tr_b16 a[80:81], v103 offset:16896\n\t"
        "ds_read_b64_tr_b16 a[82:83], v103 offset:17024\n\t"
        "ds_read_b64_tr_b16 a[84:85], v103 offset:20992\n\t"
        "ds_read_b64_tr_b16 a[86:87], v103 offset:21120\n\t"
        "ds_read_b64_tr_b16 a[88:89], v104 offset:16896\n\t"
        "ds_read_b64_tr_b16 a[90:91], v104 offset:17024\n\t"
        "ds_read_b64_tr_b16 a[92:93], v104 offset:20992\n\t"
        "ds_read_b64_tr_b16 a[94:95], v104 offset:21120\n\t"
        "buffer_load_dwordx4 v[0:3], v112, %[wsd], %[cal] offen sc1\n\t"
        "s_waitcnt lgkmcnt(0)\n\t"
        "s_mov_b32 s82, 3\n\t"
        ".Ld10%=:\n\t"
        "s_xor_b32 s80, s82, %[key]\n\t"
        "s_mul_i32 s81, s80, 65536\n\t"
        "s_lshl_b32 s80, s80, 11\n\t"
        "s_add_u32 s80, s80, %[m0b]\n\t"
        "s_add_u32 m0, s80, 16384\n\t"
        "s_add_u32 s81, s81, 1572864\n\t"
        "buffer_load_dwordx4 v98, %[so], s81 offen lds\n\t"
        "s_add_u32 m0, m0, 0x400\n\t"
        "s_add_u32 s81, s81, 32768\n\t"
        "buffer_load_dwordx4 v98, %[so], s81 offen lds\n\t"
        "s_sub_u32 s82, s82, 1\n\t"
        "s_cbranch_scc0 .Ld10%=\n\t"
        "s_waitcnt vmcnt(27)\n\t"
        "ds_read_b64_tr_b16 a[96:97], v103 offset:24576\n\t"
        "ds_read_b64_tr_b16 a[98:99], v103 offset:24704\n\t"
        "ds_read_b64_tr_b16 a[100:101], v103 offset:28672\n\t"
        "ds_read_b64_tr_b16 a[102:103], v103 offset:28800\n\t"
        "ds_read_b64_tr_b16 a[104:105], v104 offset:24576\n\t"
        "ds_read_b64_tr_b16 a[106:107], v104 offset:24704\n\t"
        "ds_read_b64_tr_b16 a[108:109], v104 offset:28672\n\t"
        "ds_read_b64_tr_b16 a[110:111], v104 offset:28800\n\t"
        "ds_read_b64_tr_b16 a[112:113], v103 offset:25088\n\t"
        "ds_read_b64_tr_b16 a[114:115], v103 offset:25216\n\t"
        "ds_read_b64_tr_b16 a[116:117], v103 offset:29184\n\t"
        "ds_read_b64_tr_b16 a[118:119], v103 offset:29312\n\t"
        "ds_read_b64_tr_b16 a[120:121], v104 offset:25088\n\t"
        "ds_read_b64_tr_b16 a[122:123], v104 offset:25216\n\t"
        "ds_read_b64_tr_b16 a[124:125], v104 offset:29184\n\t"
        "ds_read_b64_tr_b16 a[126:127], v104 offset:29312\n\t"
        "s_waitcnt lgkmcnt(0)\n\t"
        "s_mov_b32 s82, 3\n\t"
        ".Ld11%=:\n\t"
        "s_xor_b32 s80, s82, %[key]\n\t"
        "s_mul_i32 s81, s80, 65536\n\t"
        "s_lshl_b32 s80, s80, 11\n\t"
        "s_add_u32 s80, s80, %[m0b]\n\t"
        "s_add_u32 m0, s80, 24576\n\t"
        "s_add_u32 s81, s81, 1835008\n\t"
        "buffer_load_dwordx4 v98, %[so], s81 offen lds\n\t"
        "s_add_u32 m0, m0, 0x400\n\t"
        "s_add_u32 s81, s81, 32768\n\t"
        "buffer_load_dwordx4 v98, %[so], s81 offen lds\n\t"
        "s_sub_u32 s82, s82, 1\n\t"
        "s_cbranch_scc0 .Ld11%=\n\t"
        "s_waitcnt vmcnt(26)\n\t"
        "ds_read_b64_tr_b16 a[128:129], v103 offset:0\n\t"
        "ds_read_b64_tr_b16 a[130:131], v103 offset:128\n\t"
        "ds_read_b64_tr_b16 a[132:133], v103 offset:4096\n\t"
        "ds_read_b64_tr_b16 a[134:135], v103 offset:4224\n\t"
        "ds_read_b64_tr_b16 a[136:137], v104 offset:0\n\t"
        "ds_read_b64_tr_b16 a[138:139], v104 offset:128\n\t"
        "ds_read_b64_tr_b16 a[140:141], v104 offset:4096\n\t"
        "ds_read_b64_tr_b16 a[142:143], v104 offset:4224\n\t"
        "ds_read_b64_tr_b16 a[144:145], v103 offset:512\n\t"
        "ds_read_b64_tr_b16 a[146:147], v103 offset:640\n\t"
        "ds_read_b64_tr_b16 a[148:149], v103 offset:4608\n\t"
        "ds_read_b64_tr_b16 a[150:151], v103 offset:4736\n\t"
        "ds_read_b64_tr_b16 a[152:153], v104 offset:512\n\t"
        "ds_read_b64_tr_b16 a[154:155], v104 offset:640\n\t"
        "ds_read_b64_tr_b16 a[156:157], v104 offset:4608\n\t"
        "ds_read_b64_tr_b16 a[158:159], v104 offset:4736\n\t"
        "s_waitcnt lgkmcnt(0)\n\t"
        "s_waitcnt vmcnt(17)\n\t"
        "ds_read_b64_tr_b16 a[160:161], v103 offset:8192\n\t"
        "ds_read_b64_tr_b16 a[162:163], v103 offset:8320\n\t"
        "ds_read_b64_tr_b16 a[164:165], v103 offset:12288\n\t"
        "ds_read_b64_tr_b16 a[166:167], v103 offset:12416\n\t"
        "ds_read_b64_tr_b16 a[168:169], v104 offset:8192\n\t"
        "ds_read_b64_tr_b16 a[170:171], v104 offset:8320\n\t"
        "ds_read_b64_tr_b16 a[172:173], v104 offset:12288\n\t"
        "ds_read_b64_tr_b16 a[174:175], v104 offset:12416\n\t"
        "ds_read_b64_tr_b16 a[176:177], v103 offset:8704\n\t"
        "ds_read_b64_tr_b16 a[178:179], v103 offset:8832\n\t"
        "ds_read_b64_tr_b16 a[180:181], v103 offset:12800\n\t"
        "ds_read_b64_tr_b16 a[182:183], v103 offset:12928\n\t"
        "ds_read_b64_tr_b16 a[184:185], v104 offset:8704\n\t"
        "ds_read_b64_tr_b16 a[186:187], v104 offset:8832\n\t"
        "ds_read_b64_tr_b16 a[188:189], v104 offset:12800\n\t"
        "ds_read_b64_tr_b16 a[190:191], v104 offset:12928\n\t"
        "s_waitcnt lgkmcnt(0)\n\t"
        "s_waitcnt vmcnt(8)\n\t"
        "ds_read_b64_tr_b16 a[192:193], v103 offset:16384\n\t"
        "ds_read_b64_tr_b16 a[194:195], v103 offset:16512\n\t"
        "ds_read_b64_tr_b16 a[196:197], v103 offset:20480\n\t"
        "ds_read_b64_tr_b16 a[198:199], v103 offset:20608\n\t"
        "ds_read_b64_tr_b16 a[200:201], v104 offset:16384\n\t"
        "ds_read_b64_tr_b16 a[202:203], v104 offset:16512\n\t"
        "ds_read_b64_tr_b16 a[204:205], v104 offset:20480\n\t"
        "ds_read_b64_tr_b16 a[206:207], v104 offset:20608\n\t"
        "ds_read_b64_tr_b16 a[208:209], v103 offset:16896\n\t"
        "ds_read_b64_tr_b16 a[210:211], v103 offset:17024\n\t"
        "ds_read_b64_tr_b16 a[212:213], v103 offset:20992\n\t"
        "ds_read_b64_tr_b16 a[214:215], v103 offset:21120\n\t"
        "ds_read_b64_tr_b16 a[216:217], v104 offset:16896\n\t"
        "ds_read_b64_tr_b16 a[218:219], v104 offset:17024\n\t"
        "ds_read_b64_tr_b16 a[220:221], v104 offset:20992\n\t"
        "ds_read_b64_tr_b16 a[222:223], v104 offset:21120\n\t"
        "s_mov_b32 s97, 0\n\t"
        ".Lg0%=:\n\t"
        "v_cmp_ne_u32_e32 vcc, %[tag], v0\n\t"
        "v_cmp_ne_u32_e64 s[82:83], %[tag], v2\n\t"
        "s_nop 1\n\t"
        "s_or_b64 vcc, vcc, s[82:83]\n\t"
        "s_cbranch_scc0 .Lg1%=\n\t"
        "s_sleep 4\n\t"
        "buffer_load_dwordx4 v[0:3], v112, %[wsd], %[cal] offen sc1\n\t"
        "s_add_u32 s97, s97, 1\n\t"
        "s_cmp_lt_u32 s97, 0x100000\n\t"
        "s_cbranch_scc0 .Lgu%=\n\t"
        "s_waitcnt vmcnt(0)\n\t"
        "s_branch .Lg0%=\n\t"
        ".Lg1%=:\n\t"
        "s_mov_b32 s84, 0x55555555\n\t"
        "s_mov_b32 s85, s84\n\t"
        "v_and_b32_e32 v0, 1, v1\n\t"
        "v_and_b32_e32 v2, 1, v3\n\t"
        "v_cmp_ne_u32_e32 vcc, 0, v0\n\t"
        "v_cmp_ne_u32_e64 s[82:83], 0, v2\n\t"
        "s_nop 0\n\t"
        "s_and_b64 s[82:83], s[82:83], s[84:85]\n\t"
        "s_bcnt1_i32_b64 s87, s[82:83]\n\t"
        "s_and_b64 s[82:83], vcc, s[84:85]\n\t"
        "s_bcnt1_i32_b64 s86, s[82:83]\n\t"
        "s_andn2_b64 s[82:83], vcc, s[84:85]\n\t"
        "s_bcnt1_i32_b64 s88, s[82:83]\n\t"
        "v_and_b32_e32 v0, 2, v1\n\t"
        "v_and_b32_e32 v2, 2, v3\n\t"
        "v_cmp_ne_u32_e32 vcc, 0, v0\n\t"
        "v_cmp_ne_u32_e64 s[82:83], 0, v2\n\t"
        "s_nop 0\n\t"
        "s_and_b64 s[82:83], s[82:83], s[84:85]\n\t"
        "s_bcnt1_i32_b64 s90, s[82:83]\n\t"
        "s_and_b64 s[82:83], vcc, s[84:85]\n\t"
        "s_bcnt1_i32_b64 s89, s[82:83]\n\t"
        "s_andn2_b64 s[82:83], vcc, s[84:85]\n\t"
        "s_bcnt1_i32_b64 s91, s[82:83]\n\t"
        "s_add_u32 s80, s86, s87\n\t"
        "s_add_u32 s80, s80, s88\n\t"
        "s_cmp_ge_u32 s80, 48\n\t"
        "s_cselect_b32 s80, 8, 24\n\t"
        "s_cmp_gt_u32 s86, s80\n\t"
        "s_cselect_b32 s86, %c[NX1], %c[NX0]\n\t"
        "s_cmp_gt_u32 s89, 16\n\t"
        "s_cselect_b32 s81, %c[NY1], %c[NY0]\n\t"
        "s_or_b32 s86, s86, s81\n\t"
        "s_cmp_gt_u32 s87, s80\n\t"
        "s_cselect_b32 s87, %c[NX1], %c[NX0]\n\t"
        "s_cmp_gt_u32 s90, 16\n\t"
        "s_cselect_b32 s81, %c[NY1], %c[NY0]\n\t"
        "s_or_b32 s87, s87, s81\n\t"
        "s_cmp_gt_u32 s88, s80\n\t"
        "s_cselect_b32 s88, %c[NX1], %c[NX0]\n\t"
        "s_cmp_gt_u32 s91, 16\n\t"
        "s_cselect_b32 s81, %c[NY1], %c[NY0]\n\t"
        "s_or_b32 s88, s88, s81\n\t"
        "s_xor_b32 %[nA], %[nA], s86\n\t"
        "s_xor_b32 %[nW], %[nW], s86\n\t"
        "s_xor_b32 %[nC], %[nC], s87\n\t"
        "s_xor_b32 %[nS], %[nS], s88\n\t"
        "s_xor_b32 %[nT], %[nT], s88\n\t"
        "s_xor_b32 %[b0], %[b0], s88\n\t"
        "s_cmp_eq_u32 %[w], 1\n\t"
        "s_cselect_b32 s80, s87, s86\n\t"
        "s_cmp_eq_u32 %[w], 2\n\t"
        "s_cselect_b32 s80, s88, s80\n\t"
        "s_xor_b32 %[pob], %[pob], s80\n\t"
        "s_lshl_b32 s80, %[w], 10\n\t"
        "s_sub_u32 %[nS], %[nS], s80\n\t"
        "s_sub_u32 %[pob], %[pob], s80\n\t"
        "s_mov_b32 s36, 0xf0000000\n\t"
        "s_mov_b32 s38, 0xff00ff00\n\t"
        "s_mov_b32 s39, 0xff00ff00\n\t"
        "s_mov_b32 s40, 0\n\t"
        "s_mov_b32 s37, 0\n\t"
        "s_brev_b32 s54, 1\n\t"
        "s_mov_b32 s46, %c[iD1]\n\t"
        "s_mov_b32 s45, %c[iD1]\n\t"
        "s_mov_b32 s47, %c[iD2]\n\t"
        "s_mov_b32 s48, 1\n\t"
        "s_mov_b32 s50, %c[LC]\n\t"
        "s_mov_b32 s51, %c[LA]\n\t"
        "s_mov_b32 s52, %c[E1]\n\t"
        "s_mov_b32 s53, %c[E2]\n\t"
        "s_mov_b32 s43, 0\n\t"
        "s_mov_b32 s61, %[b0]\n\t"
        "s_add_u32 s62, %[b0], 0x40000\n\t"
        "s_mov_b32 s75, 162816\n\t"
        "s_waitcnt lgkmcnt(0)\n\t"
        "s_waitcnt vmcnt(0)\n\t"
        "ds_read_b64_tr_b16 a[224:225], v103 offset:24576\n\t"
        "ds_read_b64_tr_b16 a[226:227], v103 offset:24704\n\t"
        "ds_read_b64_tr_b16 a[228:229], v103 offset:28672\n\t"
        "ds_read_b64_tr_b16 a[230:231], v103 offset:28800\n\t"
        "ds_read_b64_tr_b16 a[232:233], v104 offset:24576\n\t"
        "ds_read_b64_tr_b16 a[234:235], v104 offset:24704\n\t"
        "ds_read_b64_tr_b16 a[236:237], v104 offset:28672\n\t"
        "ds_read_b64_tr_b16 a[238:239], v104 offset:28800\n\t"
        "ds_read_b64_tr_b16 a[240:241], v103 offset:25088\n\t"
        "ds_read_b64_tr_b16 a[242:243], v103 offset:25216\n\t"
        "ds_read_b64_tr_b16 a[244:245], v103 offset:29184\n\t"
        "ds_read_b64_tr_b16 a[246:247], v103 offset:29312\n\t"
        "ds_read_b64_tr_b16 a[248:249], v104 offset:25088\n\t"
        "ds_read_b64_tr_b16 a[250:251], v104 offset:25216\n\t"
        "ds_read_b64_tr_b16 a[252:253], v104 offset:29184\n\t"
        "ds_read_b64_tr_b16 a[254:255], v104 offset:29312\n\t"
        "s_waitcnt lgkmcnt(0)\n\t"
        "rec_v_code:\n\t"
        "s_and_b32 s80, s40, 3\n\t"
        "s_lshl_b32 s80, s80, 17\n\t"
        "s_add_u32 s57, s80, %[nA]\n\t"
        "s_add_u32 s58, s57, 0x1000\n\t"
        "s_add_u32 s59, s57, 0x8000\n\t"
        "s_add_u32 s60, s57, 0x9000\n\t"
        "v_mov_b32_e32 v92, -1\n\t"
        "v_mov_b32_e32 v93, -1\n\t"
        "v_mov_b32_e32 v94, -1\n\t"
        "v_mov_b32_e32 v95, -1\n\t"
        "v_mov_b32_e32 v48, 0\n\t"
        "v_mov_b32_e32 v49, 0\n\t"
        "v_mov_b32_e32 v50, 0\n\t"
        "v_mov_b32_e32 v51, 0\n\t"
        "v_mov_b32_e32 v52, 0\n\t"
        "v_mov_b32_e32 v53, 0\n\t"
        "v_mov_b32_e32 v54, 0\n\t"
        "v_mov_b32_e32 v55, 0\n\t"
        "v_mov_b32_e32 v56, 0\n\t"
        "v_mov_b32_e32 v57, 0\n\t"
        "v_mov_b32_e32 v58, 0\n\t"
        "v_mov_b32_e32 v59, 0\n\t"
        "v_mov_b32_e32 v60, 0\n\t"
        "v_mov_b32_e32 v61, 0\n\t"
        "v_mov_b32_e32 v62, 0\n\t"
        "v_mov_b32_e32 v63, 0\n\t"
        "s_barrier\n\t"
        "s_memrealtime s[76:77]\n\t"
        "s_mov_b32 s79, %[nT]\n\t"
        "v_mov_b32_e32 v86, 64\n\t"
        "s_add_u32 s61, %[b0], 0x40000\n\t"
        "s_mov_b32 s62, %[b0]\n\t"
        "s_sub_u32 s75, s75, 16\n\t"
        "s_waitcnt lgkmcnt(0)\n\t"
        "s_mov_b32 s41, s76\n\t"
        "s_mov_b32 s69, s76\n\t"
        ".p2align 6\n\t"
        ".Lloop%=:\n\t"
        "s_mov_b32 s97, 0\n\t"
        "s_movk_i32 s96, 0x1000\n\t"
        ".Lwa%=:\n\t"
        "s_memrealtime s[98:99]\n\t"
        "s_addk_i32 s96, -1\n\t"
        "s_cmp_lt_i32 s96, 0\n\t"
        "s_cbranch_scc1 .Lta%=\n\t"
        "s_waitcnt lgkmcnt(0)\n\t"
        "s_sub_i32 s98, s98, s69\n\t"
        "s_cmp_lt_i32 s98, %c[iFT]\n\t"
        "s_cbranch_scc1 .Lwa%=\n\t"
        "s_cmp_ge_i32 s98, 0\n\t"
        "s_cbranch_scc1 .Lxa%=\n\t"
        ".Lqa%=:\n\t"
        "s_nop %c[iFN]\n\t"
        "s_addk_i32 s98, 1\n\t"
        "s_cmp_lt_i32 s98, 0\n\t"
        "s_cbranch_scc1 .Lqa%=\n\t"
        ".Lxa%=:\n\t"
        "buffer_load_dwordx4 v[0:3], v112, %[srd], s57 offen sc1\n\t"
        "s_nop 7\n\t"
        "buffer_load_dwordx4 v[4:7], v112, %[srd], s58 offen sc1\n\t"
        "s_nop 7\n\t"
        "buffer_load_dwordx4 v[8:11], v112, %[srd], s59 offen sc1\n\t"
        "s_nop 7\n\t"
        "buffer_load_dwordx4 v[12:15], v112, %[srd], s60 offen sc1\n\t"
        "v_mov_b32_e32 v80, 161792\n\t"
        "ds_read_b64 v[80:81], v80\n\t"
        "s_waitcnt lgkmcnt(0)\n\t"
        "v_readfirstlane_b32 s80, v80\n\t"
        "v_readfirstlane_b32 s81, v81\n\t"
        "s_cmp_eq_u32 s80, s40\n\t"
        "s_cselect_b32 s84, 1, 0\n\t"
        "s_cmp_eq_u32 s81, s40\n\t"
        "s_cselect_b32 s81, 4, 0\n\t"
        "s_or_b32 s84, s84, s81\n\t"
        "v_mov_b32_e32 v106, s76\n\t"
        "v_mov_b32_e32 v107, s84\n\t"
        "buffer_store_dwordx2 v[106:107], v117, %[srd], s79 offen sc1\n\t"
        "s_and_b32 s80, s40, 3\n\t"
        "s_lshl_b32 s80, s80, 17\n\t"
        "s_add_u32 s63, s80, %[nC]\n\t"
        "s_add_u32 s66, s80, %[nS]\n\t"
        "s_add_u32 s81, s80, 0x20000\n\t"
        "s_and_b32 s81, s81, 0x60000\n\t"
        "s_add_u32 s67, s81, %[nW]\n\t"
        "s_add_u32 s79, s81, %[nT]\n\t"
        "s_cmp_eq_u32 %[w], 2\n\t"
        "s_cselect_b32 s81, s80, s81\n\t"
        "s_xor_b32 s81, s81, 0x40000\n\t"
        "s_add_u32 s68, s81, %[pob]\n\t"
        "s_lshl_b32 s72, s40, 17\n\t"
        "s_add_u32 s72, s72, %[kvo]\n\t"
        "s_mov_b32 s80, s61\n\t"
        "s_mov_b32 s61, s62\n\t"
        "s_mov_b32 s62, s80\n\t"
        ".Lgo%=:\n\t"
        "s_waitcnt vmcnt(4)\n\t"
        "v_smfmac_f32_16x16x64_bf16 v[52:55], v[0:3], v[136:143], v113\n\t"
        "v_smfmac_f32_16x16x64_bf16 v[56:59], v[0:3], v[144:151], v113\n\t"
        "v_smfmac_f32_16x16x64_bf16 v[60:63], v[0:3], v[152:159], v113\n\t"
        "v_smfmac_f32_16x16x64_bf16 v[48:51], v[0:3], v[128:135], v113\n\t"
        "s_waitcnt vmcnt(3)\n\t"
        "v_smfmac_f32_16x16x64_bf16 v[52:55], v[4:7], v[168:175], v113\n\t"
        "v_smfmac_f32_16x16x64_bf16 v[56:59], v[4:7], v[176:183], v113\n\t"
        "v_smfmac_f32_16x16x64_bf16 v[60:63], v[4:7], v[184:191], v113\n\t"
        "v_smfmac_f32_16x16x64_bf16 v[48:51], v[4:7], v[160:167], v113\n\t"
        "s_waitcnt vmcnt(2)\n\t"
        "v_smfmac_f32_16x16x64_bf16 v[52:55], v[8:11], v[200:207], v113\n\t"
        "v_smfmac_f32_16x16x64_bf16 v[56:59], v[8:11], v[208:215], v113\n\t"
        "v_smfmac_f32_16x16x64_bf16 v[60:63], v[8:11], v[216:223], v113\n\t"
        "v_smfmac_f32_16x16x64_bf16 v[48:51], v[8:11], v[192:199], v113\n\t"
        "s_waitcnt vmcnt(1)\n\t"
        "v_smfmac_f32_16x16x64_bf16 v[52:55], v[12:15], v[232:239], v113\n\t"
        "v_smfmac_f32_16x16x64_bf16 v[56:59], v[12:15], v[240:247], v113\n\t"
        "v_smfmac_f32_16x16x64_bf16 v[60:63], v[12:15], v[248:255], v113\n\t"
        "v_smfmac_f32_16x16x64_bf16 v[48:51], v[12:15], v[224:231], v113\n\t"
        "s_nop 1\n\t"
        "v_mov_b32_e32 v104, v104\n\t"
        "s_nop 1\n\t"
        "v_add_f32_e32 v98, v52, v54\n\t"
        "v_add_f32_e32 v99, v53, v55\n\t"
        "ds_write_b64 v123, v[98:99]\n\t"
        "v_add_f32_e32 v100, v56, v58\n\t"
        "v_add_f32_e32 v101, v57, v59\n\t"
        "ds_write_b64 v124, v[100:101]\n\t"
        "v_add_f32_e32 v102, v60, v62\n\t"
        "v_add_f32_e32 v103, v61, v63\n\t"
        "ds_write_b64 v125, v[102:103]\n\t"
        "v_pk_add_f32 v[96:97], v[48:49], v[50:51]\n\t"
        "v_cmp_u_f32_e32 vcc, v96, v97\n\t"
        "s_nop 2\n\t"
        "v_mov_b32_e32 v104, v104\n\t"
        "s_cbranch_vccnz .Lfa%=\n\t"
        ".Lra%=:\n\t"
        "v_mov_b32_e32 v104, v104\n\t"
        "s_nop 2\n\t"
        "v_mov_b32_e32 v104, v104\n\t"
        "s_nop 2\n\t"
        "v_mov_b32_e32 v104, v104\n\t"
        "s_nop 2\n\t"
        "v_mov_b32_e32 v104, v104\n\t"
        "s_waitcnt lgkmcnt(0)\n\t"
        "s_barrier\n\t"
        "v_mov_b32_e32 v104, v104\n\t"
        "ds_read_b64 v[98:99], v122 offset:512\n\t"
        "ds_read_b64 v[100:101], v122 offset:1024\n\t"
        "ds_read_b64 v[102:103], v122 offset:1536\n\t"
        "v_mov_b32_e32 v104, v104\n\t"
        "s_nop 2\n\t"
        "v_mov_b32_e32 v104, v104\n\t"
        "s_nop 2\n\t"
        "v_mov_b32_e32 v104, v104\n\t"
        "s_waitcnt lgkmcnt(2)\n\t"
        "v_pk_add_f32 v[96:97], v[96:97], v[98:99]\n\t"
        "s_waitcnt lgkmcnt(0)\n\t"
        "v_pk_add_f32 v[100:101], v[100:101], v[102:103]\n\t"
        "v_pk_add_f32 v[96:97], v[96:97], v[100:101]\n\t"
        "v_cvt_pk_bf16_f32 v80, v96, v97\n\t"
        "v_min_u32_e32 v80, -2, v80\n\t"
        "buffer_store_dword v80, v114, %[srd], s61 offen sc1\n\t"
        "s_cmp_eq_u32 s40, 63\n\t"
        "s_cbranch_scc1 .Lend%=\n\t"
        "v_mov_b32_e32 v64, 0\n\t"
        "v_mov_b32_e32 v65, 0\n\t"
        "v_mov_b32_e32 v66, 0\n\t"
        "v_mov_b32_e32 v67, 0\n\t"
        "v_mov_b32_e32 v68, 0\n\t"
        "v_mov_b32_e32 v69, 0\n\t"
        "v_mov_b32_e32 v70, 0\n\t"
        "v_mov_b32_e32 v71, 0\n\t"
        "v_mov_b32_e32 v72, 0\n\t"
        "v_mov_b32_e32 v73, 0\n\t"
        "v_mov_b32_e32 v74, 0\n\t"
        "v_mov_b32_e32 v75, 0\n\t"
        "v_mov_b32_e32 v76, 0\n\t"
        "v_mov_b32_e32 v77, 0\n\t"
        "v_mov_b32_e32 v78, 0\n\t"
        "v_mov_b32_e32 v79, 0\n\t"
        "s_xor_b32 s64, s63, %c[iCS1]\n\t"
        "s_xor_b32 s65, s63, %c[iCS2]\n\t"
        "s_xor_b32 s73, s64, %c[iCS2]\n\t"
        "s_sleep %c[VSLP]\n\t"
        "buffer_load_dwordx4 v[88:91], v112, %[srd], s66 offen sc1\n\t"
        "s_cmp_eq_u32 %[w], 0\n\t"
        "s_cselect_b32 exec_hi, 0, -1\n\t"
        "buffer_store_dwordx4 v[92:95], v112, %[srd], s68 offen sc1\n\t"
        "s_mov_b32 exec_hi, -1\n\t"
        "s_waitcnt lgkmcnt(0)\n\t"
        "v_mov_b32_e32 v48, 0\n\t"
        "v_mov_b32_e32 v49, 0\n\t"
        "v_mov_b32_e32 v50, 0\n\t"
        "v_mov_b32_e32 v51, 0\n\t"
        "v_mov_b32_e32 v52, 0\n\t"
        "v_mov_b32_e32 v53, 0\n\t"
        "v_mov_b32_e32 v54, 0\n\t"
        "v_mov_b32_e32 v55, 0\n\t"
        "v_mov_b32_e32 v56, 0\n\t"
        "v_mov_b32_e32 v57, 0\n\t"
        "v_mov_b32_e32 v58, 0\n\t"
        "v_mov_b32_e32 v59, 0\n\t"
        "v_mov_b32_e32 v60, 0\n\t"
        "v_mov_b32_e32 v61, 0\n\t"
        "v_mov_b32_e32 v62, 0\n\t"
        "v_mov_b32_e32 v63, 0\n\t"
        "s_add_u32 s81, s40, 1\n\t"
        "s_and_b32 s80, s81, 3\n\t"
        "s_lshl_b32 s80, s80, 17\n\t"
        "s_add_u32 s57, s80, %[nA]\n\t"
        "s_add_u32 s58, s57, 0x1000\n\t"
        "s_add_u32 s59, s57, 0x8000\n\t"
        "s_add_u32 s60, s57, 0x9000\n\t"
        "s_waitcnt vmcnt(1)\n\t"
        "s_mov_b32 s97, 0\n\t"
        ".Lsr%=:\n\t"
        "s_mov_b32 exec_hi, 0xffff\n\t"
        "v_subrev_u32_e32 v4, s41, v88\n\t"
        "v_subrev_u32_e32 v5, s41, v90\n\t"
        "v_max_i32_e32 v4, v4, v5\n\t"
        "v_max_u32_e32 v5, v89, v91\n\t"
        "v_cmp_eq_u32_e32 vcc, -1, v5\n\t"
        "v_max_i32_dpp v4, v4, v4 quad_perm:[1,0,3,2] row_mask:0xf bank_mask:0xf\n\t"
        "v_or_b32_e32 v6, v89, v91\n\t"
        "s_nop 0\n\t"
        "v_max_i32_dpp v4, v4, v4 quad_perm:[2,3,0,1] row_mask:0xf bank_mask:0xf\n\t"
        "s_nop 1\n\t"
        "v_max_i32_dpp v4, v4, v4 row_ror:4 row_mask:0xf bank_mask:0xf\n\t"
        "s_nop 1\n\t"
        "v_max_i32_dpp v4, v4, v4 row_ror:8 row_mask:0xf bank_mask:0xf\n\t"
        "s_cbranch_vccnz .Lsf%=\n\t"
        "v_readlane_b32 s85, v4, 0\n\t"
        "v_readlane_b32 s81, v4, 16\n\t"
        "v_readlane_b32 s56, v4, 32\n\t"
        "v_and_b32_e32 v5, 1, v6\n\t"
        "v_cmp_ne_u32_e64 s[86:87], 0, v5\n\t"
        "v_and_b32_e32 v5, 2, v6\n\t"
        "v_cmp_ne_u32_e64 s[88:89], 0, v5\n\t"
        "v_and_b32_e32 v5, 4, v6\n\t"
        "v_cmp_ne_u32_e64 s[90:91], 0, v5\n\t"
        "s_max_i32 s56, s56, s81\n\t"
        "s_add_u32 s92, s85, s41\n\t"
        "s_sub_u32 s92, s92, s43\n\t"
        "s_add_i32 s93, s56, s47\n\t"
        "s_mov_b32 s44, s46\n\t"
        "s_mov_b32 s46, s45\n\t"
        "s_mov_b32 s94, s48\n\t"
        "s_mov_b32 s43, s41\n\t"
        "s_cmp_gt_i32 s56, s54\n\t"
        "s_cbranch_scc1 .Lmv%=\n\t"
        "s_mov_b32 s37, 0\n\t"
        "s_mov_b32 s48, 0\n\t"
        "s_mov_b32 s95, 0\n\t"
        ".Lsd%=:\n\t"
        "s_mov_b32 exec_hi, -1\n\t"
        "s_add_u32 s42, s43, s46\n\t"
        "s_mov_b32 s97, 0\n\t"
        "s_movk_i32 s96, 0x1000\n\t"
        ".Lwc%=:\n\t"
        "s_memrealtime s[98:99]\n\t"
        "s_addk_i32 s96, -1\n\t"
        "s_cmp_lt_i32 s96, 0\n\t"
        "s_cbranch_scc1 .Ltc%=\n\t"
        "s_waitcnt lgkmcnt(0)\n\t"
        "s_sub_i32 s98, s98, s42\n\t"
        "s_cmp_lt_i32 s98, %c[iFT]\n\t"
        "s_cbranch_scc1 .Lwc%=\n\t"
        "s_cmp_ge_i32 s98, 0\n\t"
        "s_cbranch_scc1 .Lxc%=\n\t"
        ".Lqc%=:\n\t"
        "s_nop %c[iFN]\n\t"
        "s_addk_i32 s98, 1\n\t"
        "s_cmp_lt_i32 s98, 0\n\t"
        "s_cbranch_scc1 .Lqc%=\n\t"
        ".Lxc%=:\n\t"
        "buffer_load_dwordx4 v[16:19], v112, %[srd], s63 offen sc1\n\t"
        "s_cmp_ge_u32 s40, %c[iCT0]\n\t"
        "s_cselect_b32 s85, 1, 0\n\t"
        "s_cmp_lg_u64 s[86:87], 0\n\t"
        "s_cselect_b32 s86, s85, 0\n\t"
        "s_cmp_lg_u64 s[88:89], 0\n\t"
        "s_cselect_b32 s87, s85, 0\n\t"
        "s_cmp_lg_u64 s[90:91], 0\n\t"
        "s_cselect_b32 s88, s85, 0\n\t"
        "buffer_load_dwordx4 v[20:23], v115, %[srd], s63 offen sc1\n\t"
        "s_or_b32 s80, s94, s86\n\t"
        "s_or_b32 s80, s80, s87\n\t"
        "s_andn2_b32 s90, s85, s80\n\t"
        "s_cmp_le_i32 s92, s44\n\t"
        "s_cselect_b32 s80, s85, 0\n\t"
        "s_andn2_b32 s91, s80, s88\n\t"
        "s_and_b32 s80, s40, %c[LDEC]\n\t"
        "s_cselect_b32 s89, 0, 1\n\t"
        "buffer_load_dwordx4 v[24:27], v112, %[srd], s64 offen sc1\n\t"
        "s_mov_b32 s80, s88\n\t"
        "s_add_i32 s81, s92, s50\n\t"
        "s_sub_i32 s81, s81, 4\n\t"
        "s_cmp_le_i32 s81, s44\n\t"
        "s_cselect_b32 s80, s80, 0\n\t"
        "s_cmp_lt_i32 s50, %c[LC24]\n\t"
        "s_cselect_b32 s80, s80, 0\n\t"
        "s_lshl2_add_u32 s50, s80, s50\n\t"
        "buffer_load_dwordx4 v[28:31], v115, %[srd], s64 offen sc1\n\t"
        "s_sub_i32 s50, s50, s89\n\t"
        "s_max_i32 s50, s50, %c[LC]\n\t"
        "s_lshl_b32 s80, s92, 1\n\t"
        "s_sub_i32 s81, s80, s52\n\t"
        "s_ashr_i32 s82, s81, %c[UPSH]\n\t"
        "s_max_i32 s82, s82, %c[TRUP]\n\t"
        "s_add_i32 s82, s82, s52\n\t"
        "s_min_i32 s82, s82, s80\n\t"
        "buffer_load_dwordx4 v[32:35], v112, %[srd], s65 offen sc1\n\t"
        "s_ashr_i32 s83, s81, 1\n\t"
        "s_min_i32 s83, s83, -%c[TRDN]\n\t"
        "s_add_i32 s83, s83, s52\n\t"
        "s_max_i32 s83, s83, s80\n\t"
        "s_cmp_gt_i32 s81, 0\n\t"
        "s_cselect_b32 s82, s82, s83\n\t"
        "s_cmp_lg_u32 s90, 0\n\t"
        "s_cselect_b32 s52, s82, s52\n\t"
        "buffer_load_dwordx4 v[36:39], v115, %[srd], s65 offen sc1\n\t"
        "s_add_i32 s80, s52, 1\n\t"
        "s_ashr_i32 s80, s80, 1\n\t"
        "s_add_i32 s45, s80, s50\n\t"
        "s_andn2_b32 s80, s86, s94\n\t"
        "s_cmp_lt_i32 s51, %c[LA24]\n\t"
        "s_cselect_b32 s80, s80, 0\n\t"
        "s_lshl2_add_u32 s51, s80, s51\n\t"
        "s_sub_i32 s51, s51, s89\n\t"
        "buffer_load_dwordx4 v[40:43], v112, %[srd], s73 offen sc1\n\t"
        "s_max_i32 s51, s51, %c[LA]\n\t"
        "s_lshl_b32 s80, s93, 1\n\t"
        "s_sub_i32 s81, s80, s53\n\t"
        "s_ashr_i32 s82, s81, %c[UPSH]\n\t"
        "s_max_i32 s82, s82, %c[TRUP]\n\t"
        "s_add_i32 s82, s82, s53\n\t"
        "s_min_i32 s82, s82, s80\n\t"
        "s_ashr_i32 s83, s81, 1\n\t"
        "buffer_load_dwordx4 v[44:47], v115, %[srd], s73 offen sc1\n\t"
        "s_min_i32 s83, s83, -%c[TRDN]\n\t"
        "s_add_i32 s83, s83, s53\n\t"
        "s_max_i32 s83, s83, s80\n\t"
        "s_cmp_gt_i32 s81, 0\n\t"
        "s_cselect_b32 s82, s82, s83\n\t"
        "s_cmp_lg_u32 s91, 0\n\t"
        "s_cselect_b32 s53, s82, s53\n\t"
        "s_add_i32 s80, s53, 1\n\t"
        "s_ashr_i32 s80, s80, 1\n\t"
        "s_add_i32 s47, s80, s51\n\t"
        "s_add_u32 s81, s47, %c[AUP]\n\t"
        "s_min_i32 s80, s95, s81\n\t"
        "s_max_i32 s47, s47, s80\n\t"
        "s_add_u32 s41, s42, s47\n\t"
        ".Lrc%=:\n\t"
        "s_waitcnt vmcnt(7)\n\t"
        "v_smfmac_f32_16x16x64_bf16 v[64:67], v[16:19], a[0:7], v113\n\t"
        "v_smfmac_f32_16x16x64_bf16 v[68:71], v[16:19], a[8:15], v113\n\t"
        "v_smfmac_f32_16x16x64_bf16 v[72:75], v[16:19], a[16:23], v113\n\t"
        "v_smfmac_f32_16x16x64_bf16 v[76:79], v[16:19], a[24:31], v113\n\t"
        "s_waitcnt vmcnt(6)\n\t"
        "v_smfmac_f32_16x16x64_bf16 v[64:67], v[20:23], a[32:39], v113\n\t"
        "v_smfmac_f32_16x16x64_bf16 v[68:71], v[20:23], a[40:47], v113\n\t"
        "v_smfmac_f32_16x16x64_bf16 v[72:75], v[20:23], a[48:55], v113\n\t"
        "v_smfmac_f32_16x16x64_bf16 v[76:79], v[20:23], a[56:63], v113\n\t"
        "s_waitcnt vmcnt(5)\n\t"
        "v_smfmac_f32_16x16x64_bf16 v[64:67], v[24:27], a[64:71], v113\n\t"
        "v_smfmac_f32_16x16x64_bf16 v[68:71], v[24:27], a[72:79], v113\n\t"
        "v_smfmac_f32_16x16x64_bf16 v[72:75], v[24:27], a[80:87], v113\n\t"
        "v_smfmac_f32_16x16x64_bf16 v[76:79], v[24:27], a[88:95], v113\n\t"
        "s_waitcnt vmcnt(4)\n\t"
        "v_smfmac_f32_16x16x64_bf16 v[64:67], v[28:31], a[96:103], v113\n\t"
        "v_smfmac_f32_16x16x64_bf16 v[68:71], v[28:31], a[104:111], v113\n\t"
        "v_smfmac_f32_16x16x64_bf16 v[72:75], v[28:31], a[112:119], v113\n\t"
        "v_smfmac_f32_16x16x64_bf16 v[76:79], v[28:31], a[120:127], v113\n\t"
        "s_waitcnt vmcnt(3)\n\t"
        "v_smfmac_f32_16x16x64_bf16 v[64:67], v[32:35], a[128:135], v113\n\t"
        "v_smfmac_f32_16x16x64_bf16 v[68:71], v[32:35], a[136:143], v113\n\t"
        "v_smfmac_f32_16x16x64_bf16 v[72:75], v[32:35], a[144:151], v113\n\t"
        "v_smfmac_f32_16x16x64_bf16 v[76:79], v[32:35], a[152:159], v113\n\t"
        "s_waitcnt vmcnt(2)\n\t"
        "v_smfmac_f32_16x16x64_bf16 v[64:67], v[36:39], a[160:167], v113\n\t"
        "v_smfmac_f32_16x16x64_bf16 v[68:71], v[36:39], a[168:175], v113\n\t"
        "v_smfmac_f32_16x16x64_bf16 v[72:75], v[36:39], a[176:183], v113\n\t"
        "v_smfmac_f32_16x16x64_bf16 v[76:79], v[36:39], a[184:191], v113\n\t"
        "s_waitcnt vmcnt(1)\n\t"
        "v_smfmac_f32_16x16x64_bf16 v[64:67], v[40:43], a[192:199], v113\n\t"
        "v_smfmac_f32_16x16x64_bf16 v[68:71], v[40:43], a[200:207], v113\n\t"
        "v_smfmac_f32_16x16x64_bf16 v[72:75], v[40:43], a[208:215], v113\n\t"
        "v_smfmac_f32_16x16x64_bf16 v[76:79], v[40:43], a[216:223], v113\n\t"
        "s_waitcnt vmcnt(0)\n\t"
        "v_smfmac_f32_16x16x64_bf16 v[64:67], v[44:47], a[224:231], v113\n\t"
        "s_nop 2\n\t"
        "v_smfmac_f32_16x16x64_bf16 v[68:71], v[44:47], a[232:239], v113\n\t"
        "s_cmp_gt_u32 %[w], 2\n\t"
        "s_nop 1\n\t"
        "v_smfmac_f32_16x16x64_bf16 v[72:75], v[44:47], a[240:247], v113\n\t"
        "v_add_f32_e32 v80, v64, v66\n\t"
        "s_nop 0\n\t"
        "v_smfmac_f32_16x16x64_bf16 v[76:79], v[44:47], a[248:255], v113\n\t"
        "v_add_f32_e32 v81, v65, v67\n\t"
        "v_add_f32_e32 v82, v68, v70\n\t"
        "v_add_f32_e32 v83, v69, v71\n\t"
        "s_mov_b32 exec_lo, 0\n\t"
        "s_nop 0\n\t"
        "v_add_f32_e32 v80, v72, v74\n\t"
        "v_add_f32_e32 v81, v73, v75\n\t"
        "s_nop 0\n\t"
        "v_pk_add_f32 v[82:83], v[76:77], v[78:79]\n\t"
        "s_mov_b32 exec_lo, -1\n\t"
        "s_nop 0\n\t"
        "v_permlane32_swap_b32_e32 v80, v82\n\t"
        "v_add_f32_e32 v80, v80, v82\n\t"
        "ds_write_b32 v119, v80\n\t"
        "v_permlane32_swap_b32_e32 v81, v83\n\t"
        "v_add_f32_e32 v81, v81, v83\n\t"
        "ds_write_b32 v119, v81 offset:128\n\t"
        "v_cmp_u_f32_e32 vcc, v80, v81\n\t"
        "s_nop 2\n\t"
        "v_mov_b32_e32 v104, v104\n\t"
        "s_cbranch_vccnz .Lfc%=\n\t"
        ".Lrd%=:\n\t"
        "v_mov_b32_e32 v104, v104\n\t"
        "s_cbranch_scc1 .Lno%=\n\t"
        "v_mov_b32_e32 v104, v104\n\t"
        "s_nop 2\n\t"
        "v_mov_b32_e32 v104, v104\n\t"
        "s_waitcnt lgkmcnt(0)\n\t"
        "s_barrier\n\t"
        "v_mov_b32_e32 v104, v104\n\t"
        "ds_read_b64 v[0:1], v120 offset:8192\n\t"
        "ds_read_b64 v[2:3], v120 offset:8704\n\t"
        "ds_read_b64 v[4:5], v120 offset:9216\n\t"
        "ds_read_b64 v[6:7], v120 offset:9728\n\t"
        "v_mov_b32_e32 v104, v104\n\t"
        "s_nop 2\n\t"
        "v_mov_b32_e32 v104, v104\n\t"
        "s_nop 2\n\t"
        "v_mov_b32_e32 v104, v104\n\t"
        "s_waitcnt lgkmcnt(2)\n\t"
        "v_pk_add_f32 v[0:1], v[0:1], v[2:3]\n\t"
        "s_waitcnt lgkmcnt(0)\n\t"
        "v_pk_add_f32 v[4:5], v[4:5], v[6:7]\n\t"
        "v_pk_add_f32 v[0:1], v[0:1], v[4:5]\n\t"
        "v_cvt_pk_bf16_f32 v0, v0, v1\n\t"
        "v_min_u32_e32 v0, -2, v0\n\t"
        "buffer_store_dword v0, v116, %[srd], s67 offen sc1\n\t"
        ".Lnp%=:\n\t"
        "s_memrealtime s[76:77]\n\t"
        "s_add_u32 s40, s40, 1\n\t"
        "s_waitcnt lgkmcnt(0)\n\t"
        "s_add_u32 s81, s76, %c[iVD]\n\t"
        "s_sub_u32 s80, s76, s41\n\t"
        "s_cmp_gt_i32 s80, %c[iTOL]\n\t"
        "s_cselect_b32 s69, s81, s41\n\t"
        "s_branch .Lloop%=\n\t"
        ".Lmv%=:\n\t"
        "s_mov_b32 s48, 1\n\t"
        "s_cmp_eq_u32 s54, %c[iTOL]\n\t"
        "s_cbranch_scc1 .Lm1%=\n\t"
        "s_max_i32 s80, s85, s56\n\t"
        "s_add_u32 s43, s41, s80\n\t"
        "s_add_u32 s43, s43, %c[iPEN0]\n\t"
        "s_mov_b32 s54, %c[iTOL]\n\t"
        "s_add_u32 s46, s46, %c[iX0C]\n\t"
        "s_add_u32 s95, s47, %c[iX0A]\n\t"
        "v_and_b32_e32 v4, 3, v88\n\t"
        "v_and_b32_e32 v5, 3, v90\n\t"
        "v_lshlrev_b32_e64 v4, v4, 1\n\t"
        "v_lshlrev_b32_e64 v5, v5, 1\n\t"
        "v_or_b32_e32 v4, v4, v5\n\t"
        "s_mov_b32 s80, 0\n\t"
        "v_and_b32_e32 v5, 1, v4\n\t"
        "v_cmp_ne_u32_e32 vcc, 0, v5\n\t"
        "s_nop 1\n\t"
        "s_cmp_lg_u64 vcc, 0\n\t"
        "s_cselect_b32 s81, 2, 0\n\t"
        "s_or_b32 s80, s80, s81\n\t"
        "v_and_b32_e32 v5, 2, v4\n\t"
        "v_cmp_ne_u32_e32 vcc, 0, v5\n\t"
        "s_nop 1\n\t"
        "s_cmp_lg_u64 vcc, 0\n\t"
        "s_cselect_b32 s81, 4, 0\n\t"
        "s_or_b32 s80, s80, s81\n\t"
        "v_and_b32_e32 v5, 4, v4\n\t"
        "v_cmp_ne_u32_e32 vcc, 0, v5\n\t"
        "s_nop 1\n\t"
        "s_cmp_lg_u64 vcc, 0\n\t"
        "s_cselect_b32 s81, 8, 0\n\t"
        "s_or_b32 s80, s80, s81\n\t"
        "v_and_b32_e32 v5, 8, v4\n\t"
        "v_cmp_ne_u32_e32 vcc, 0, v5\n\t"
        "s_nop 1\n\t"
        "s_cmp_lg_u64 vcc, 0\n\t"
        "s_cselect_b32 s81, 16, 0\n\t"
        "s_or_b32 s80, s80, s81\n\t"
        "s_lshr_b32 s80, 0x1adf1210, s80\n\t"
        "s_and_b32 s55, s80, 3\n\t"
        "s_branch .Lal%=\n\t"
        ".Lm1%=:\n\t"
        "s_add_u32 s80, s37, %c[iPEN2]\n\t"
        "s_min_i32 s80, s80, %c[iPENM]\n\t"
        "s_cmp_lg_u32 s37, 0\n\t"
        "s_cselect_b32 s37, s80, %c[iPEN]\n\t"
        "s_add_u32 s80, s56, s37\n\t"
        "s_max_i32 s80, s80, %c[iPMIN]\n\t"
        "s_add_u32 s43, s41, s80\n\t"
        "s_add_u32 s95, s93, %c[LA]\n\t"
        "s_add_u32 s46, s46, %c[iXR]\n\t"
        ".Lal%=:\n\t"
        "s_sub_u32 s80, s55, s43\n\t"
        "s_and_b32 s80, s80, %c[iPHM]\n\t"
        "s_add_u32 s43, s43, s80\n\t"
        "s_branch .Lsd%=\n\t"
        ".Lsf%=:\n\t"
        "s_mov_b32 exec_hi, -1\n\t"
        "s_sleep 2\n\t"
        "buffer_load_dwordx4 v[88:91], v112, %[srd], s66 offen sc1\n\t"
        "s_add_u32 s97, s97, 1\n\t"
        "s_cmp_lt_u32 s97, 0x100000\n\t"
        "s_cbranch_scc0 .Lgu%=\n\t"
        "s_waitcnt vmcnt(0)\n\t"
        "s_branch .Lsr%=\n\t"
        ".Lfa%=:\n\t"
        "v_max_u32_e32 v84, v0, v2\n\t"
        "v_max3_u32 v84, v84, v1, v3\n\t"
        "v_max3_u32 v84, v84, v4, v6\n\t"
        "v_max3_u32 v84, v84, v5, v7\n\t"
        "v_max3_u32 v84, v84, v8, v10\n\t"
        "v_max3_u32 v84, v84, v9, v11\n\t"
        "v_max3_u32 v84, v84, v12, v14\n\t"
        "v_max3_u32 v84, v84, v13, v15\n\t"
        "v_cmp_eq_u32_e32 vcc, -1, v84\n\t"
        "s_nop 1\n\t"
        "v_mov_b32_e32 v104, v104\n\t"
        "s_nop 0\n\t"
        "s_cbranch_vccz .Lra%=\n\t"
        "s_add_u32 s80, s40, 1\n\t"
        "v_mov_b32_e32 v80, 161792\n\t"
        "v_mov_b32_e32 v81, s80\n\t"
        "ds_write_b32 v80, v81\n\t"
        ".Lfp%=:\n\t"
        "s_sleep %c[ASLP]\n\t"
        "buffer_load_dwordx4 v[0:3], v112, %[srd], s57 offen sc1\n\t"
        "buffer_load_dwordx4 v[4:7], v112, %[srd], s58 offen sc1\n\t"
        "buffer_load_dwordx4 v[8:11], v112, %[srd], s59 offen sc1\n\t"
        "buffer_load_dwordx4 v[12:15], v112, %[srd], s60 offen sc1\n\t"
        "s_add_u32 s97, s97, 1\n\t"
        "s_cmp_lt_u32 s97, 0x100000\n\t"
        "s_cbranch_scc0 .Lgu%=\n\t"
        "s_waitcnt vmcnt(0)\n\t"
        "v_max_u32_e32 v84, v0, v2\n\t"
        "v_max3_u32 v84, v84, v1, v3\n\t"
        "v_max3_u32 v84, v84, v4, v6\n\t"
        "v_max3_u32 v84, v84, v5, v7\n\t"
        "v_max3_u32 v84, v84, v8, v10\n\t"
        "v_max3_u32 v84, v84, v9, v11\n\t"
        "v_max3_u32 v84, v84, v12, v14\n\t"
        "v_max3_u32 v84, v84, v13, v15\n\t"
        "v_cmp_eq_u32_e32 vcc, -1, v84\n\t"
        "s_nop 1\n\t"
        "v_mov_b32_e32 v104, v104\n\t"
        "s_nop 0\n\t"
        "s_cbranch_vccnz .Lfp%=\n\t"
        "v_mov_b32_e32 v48, 0\n\t"
        "v_mov_b32_e32 v49, 0\n\t"
        "v_mov_b32_e32 v50, 0\n\t"
        "v_mov_b32_e32 v51, 0\n\t"
        "v_mov_b32_e32 v52, 0\n\t"
        "v_mov_b32_e32 v53, 0\n\t"
        "v_mov_b32_e32 v54, 0\n\t"
        "v_mov_b32_e32 v55, 0\n\t"
        "v_mov_b32_e32 v56, 0\n\t"
        "v_mov_b32_e32 v57, 0\n\t"
        "v_mov_b32_e32 v58, 0\n\t"
        "v_mov_b32_e32 v59, 0\n\t"
        "v_mov_b32_e32 v60, 0\n\t"
        "v_mov_b32_e32 v61, 0\n\t"
        "v_mov_b32_e32 v62, 0\n\t"
        "v_mov_b32_e32 v63, 0\n\t"
        "s_nop 1\n\t"
        "s_branch .Lgo%=\n\t"
        ".Lfc%=:\n\t"
        "v_max_u32_e32 v84, v16, v18\n\t"
        "v_max3_u32 v84, v84, v20, v22\n\t"
        "v_max3_u32 v84, v84, v24, v26\n\t"
        "v_max3_u32 v84, v84, v28, v30\n\t"
        "v_max3_u32 v84, v84, v32, v34\n\t"
        "v_max3_u32 v84, v84, v36, v38\n\t"
        "v_max3_u32 v84, v84, v40, v42\n\t"
        "v_max3_u32 v84, v84, v44, v46\n\t"
        "v_cmp_eq_u32_e32 vcc, -1, v84\n\t"
        "s_nop 1\n\t"
        "v_mov_b32_e32 v104, v104\n\t"
        "s_nop 0\n\t"
        "s_cbranch_vccz .Lrd%=\n\t"
        "s_add_u32 s80, s40, 1\n\t"
        "v_mov_b32_e32 v80, 161796\n\t"
        "v_mov_b32_e32 v81, s80\n\t"
        "ds_write_b32 v80, v81\n\t"
        ".Lfq%=:\n\t"
        "s_sleep %c[ASLP]\n\t"
        "buffer_load_dwordx4 v[16:19], v112, %[srd], s63 offen sc1\n\t"
        "buffer_load_dwordx4 v[20:23], v115, %[srd], s63 offen sc1\n\t"
        "buffer_load_dwordx4 v[24:27], v112, %[srd], s64 offen sc1\n\t"
        "buffer_load_dwordx4 v[28:31], v115, %[srd], s64 offen sc1\n\t"
        "buffer_load_dwordx4 v[32:35], v112, %[srd], s65 offen sc1\n\t"
        "buffer_load_dwordx4 v[36:39], v115, %[srd], s65 offen sc1\n\t"
        "buffer_load_dwordx4 v[40:43], v112, %[srd], s73 offen sc1\n\t"
        "buffer_load_dwordx4 v[44:47], v115, %[srd], s73 offen sc1\n\t"
        "s_add_u32 s97, s97, 1\n\t"
        "s_cmp_lt_u32 s97, 0x100000\n\t"
        "s_cbranch_scc0 .Lgu%=\n\t"
        "s_waitcnt vmcnt(0)\n\t"
        "v_max_u32_e32 v84, v16, v18\n\t"
        "v_max3_u32 v84, v84, v20, v22\n\t"
        "v_max3_u32 v84, v84, v24, v26\n\t"
        "v_max3_u32 v84, v84, v28, v30\n\t"
        "v_max3_u32 v84, v84, v32, v34\n\t"
        "v_max3_u32 v84, v84, v36, v38\n\t"
        "v_max3_u32 v84, v84, v40, v42\n\t"
        "v_max3_u32 v84, v84, v44, v46\n\t"
        "v_cmp_eq_u32_e32 vcc, -1, v84\n\t"
        "s_nop 1\n\t"
        "v_mov_b32_e32 v104, v104\n\t"
        "s_nop 0\n\t"
        "s_cbranch_vccnz .Lfq%=\n\t"
        "v_mov_b32_e32 v64, 0\n\t"
        "v_mov_b32_e32 v65, 0\n\t"
        "v_mov_b32_e32 v66, 0\n\t"
        "v_mov_b32_e32 v67, 0\n\t"
        "v_mov_b32_e32 v68, 0\n\t"
        "v_mov_b32_e32 v69, 0\n\t"
        "v_mov_b32_e32 v70, 0\n\t"
        "v_mov_b32_e32 v71, 0\n\t"
        "v_mov_b32_e32 v72, 0\n\t"
        "v_mov_b32_e32 v73, 0\n\t"
        "v_mov_b32_e32 v74, 0\n\t"
        "v_mov_b32_e32 v75, 0\n\t"
        "v_mov_b32_e32 v76, 0\n\t"
        "v_mov_b32_e32 v77, 0\n\t"
        "v_mov_b32_e32 v78, 0\n\t"
        "v_mov_b32_e32 v79, 0\n\t"
        "s_nop 1\n\t"
        "s_branch .Lrc%=\n\t"
        ".Lno%=:\n\t"
        "s_waitcnt lgkmcnt(0)\n\t"
        "s_barrier\n\t"
        "s_branch .Lnp%=\n\t"
        ".Lta%=:\n\t"
        "s_waitcnt lgkmcnt(0)\n\t"
        "s_branch .Lxa%=\n\t"
        ".Ltc%=:\n\t"
        "s_waitcnt lgkmcnt(0)\n\t"
        "s_branch .Lxc%=\n\t"
        ".Lgu%=:\n\t"
        "s_mov_b64 exec, -1\n\t"
        "v_mov_b32_e32 v80, %c[ferr]\n\t"
        "v_mov_b32_e32 v81, 2\n\t"
        "buffer_store_dword v81, v80, %[wsd], 0 offen sc1\n\t"
        "s_endpgm\n\t"
        ".Lend%=:\n\t"
        "buffer_store_dword v80, v114, %[wsd], s72 offen sc1\n\t"
        "s_waitcnt vmcnt(0)\n\t"
        "s_barrier\n\t"
        "buffer_store_dword v86, v118, %[wsd], 0 offen sc1\n\t"
        "s_endpgm"
        : [nA] "+s"(nA), [nS] "+s"(nS), [nT] "+s"(nT), [nW] "+s"(nW), [nC] "+s"(nC), [b0] "+s"(b0), [pob] "+s"(pob)
        : [srd] "s"(rhd), [wsd] "s"(wsd), [w] "s"(w), [tag] "s"(tag), [kvo] "s"(kvo), [cal] "s"(cal), [sv] "s"(sv), [so] "s"(so), [m0b] "s"(m0b), [key] "s"(key), [tu] "s"(tu),
          [ferr] "i"(F_ERR), [iD1] "i"(D1), [iD2] "i"(D2), [LC] "i"(LC0), [LA] "i"(LA0), [LC24] "i"(LC0 + 24), [LA24] "i"(LA0 + 24), [E1] "i"(2 * (D1 - LC0)), [E2] "i"(2 * (D2 - LA0)),
          [iPEN] "i"(PEN), [iPEN2] "i"(PEN2), [iPENM] "i"(PEN + 2 * PEN2), [iPEN0] "i"(PEN0), [iTOL] "i"(TOL), [iCT0] "i"(CT0), [iX0C] "i"(X0C), [iX0A] "i"(X0A), [iPHM] "i"(PH_M),
          [iFT] "i"(-3), [iFN] "i"(FINE_N), [AUP] "i"(A_UP), [ASLP] "i"(A_SLP), [LDEC] "i"(L_DEC), [UPSH] "i"(UP_SH), [TRUP] "i"(TR_UP), [TRDN] "i"(TR_DN), [HSLP] "i"(H_SLP), [iPMIN] "i"(PMIN), [VSLP] "i"(V_SLP), [RSLP] "i"(R_SLP), [iVD] "i"(V_DEL), [iXR] "i"(XR), [NX0] "i"(NEARX), [NX1] "i"(8192 - NEARX), [iCS1] "i"(CS1), [iCS2] "i"(CS2), [NY0] "i"(NEARY), [NY1] "i"(16384 - NEARY),
          "{v[112:127]}"(pc), "{v[96:103]}"(pa), "{v[104:105]}"(pb)
        : "memory", "vcc", "scc", "m0", "s36", "s37", "s38", "s39", "s40", "s41", "s42", "s43", "s44", "s45", "s46", "s47", "s48", "s49", "s50", "s51", "s52", "s53", "s54", "s55", "s56", "s57", "s58", "s59", "s60", "s61", "s62", "s63", "s64", "s65", "s66", "s67", "s68", "s69", "s70", "s71", "s72", "s73", "s74", "s75", "s76", "s77", "s78", "s79", "s80", "s81", "s82", "s83", "s84", "s85", "s86", "s87", "s88", "s89", "s90", "s91", "s92", "s93", "s94", "s95", "s96", "s97", "s98", "s99");
    }
}

using f32x16 = __attribute__((ext_vector_type(16))) float;
constexpr int CV_RP = 80;
constexpr int CV_U = 96 * CV_RP;
constexpr int CV_KP = 4416;
constexpr int CV_K0 = 8 * CV_U;
__device__ __forceinline__ float lo16(unsigned x) { return __uint_as_float(x << 16); }
__device__ __forceinline__ float hi16(unsigned x) { return __uint_as_float(x & 0xFFFF0000u); }
__device__ __forceinline__ void prep_block(const Params& p, rsrc_t r, int k4, const int wv, const unsigned tag) {
    const int tid = otid(), w = wv, l = tid & 63;
    const int cu = (k4 & 3) * 32 + w * 8 + (l >> 3), oc0 = (k4 >> 2) * 8 + (l & 7);
    const rsrc_t rk = __builtin_amdgcn_make_buffer_rsrc((void*)p.Ak, 0, S * D * 2, 0x00020000), rv = __builtin_amdgcn_make_buffer_rsrc((void*)p.Av, 0, S * D * 2, 0x00020000);
    v4u sk[4], kn[11], vn[8], gw[2];
    auto fetch = [&](int oc) {
#pragma unroll
        for (int j = 0; j < 11; ++j) kn[j] = __builtin_amdgcn_raw_buffer_load_b128(rk, (unsigned)((oc * 8 + j - 3) * 2048 + cu * 16), 0, 2);
#pragma unroll
        for (int i = 0; i < 8; ++i) vn[i] = __builtin_amdgcn_raw_buffer_load_b128(rv, (unsigned)((oc * 8 + i) * 2048 + cu * 16), 0, 2); };
    fetch(oc0);
#pragma unroll
    for (int q = 0; q < 4; ++q) sk[q] = *(const v4u*)(p.shift_k + cu * 32 + q * 8);
#pragma unroll
    for (int q = 0; q < 2; ++q) gw[q] = *(const v4u*)(p.gate_w + q * 512 + l * 8);
    st4(r, tid < 128 ? F_RDY + k4 * 512 + tid * 4 : tid < 132 ? F_ATT + (k4 * 4 + tid - 128) * 64 : (tid == 192 && k4 == 0) ? F_CONV : 0xF0000000u, 0u);
#pragma unroll 1
    for (int it = 0; it < 4; ++it) {
        const int oc = oc0 + 64 * it, tk = (it * 32 + k4) * 16 + w;
        v4u kr[11], vr[8], xg[4][2];
#pragma unroll
        for (int j = 0; j < 11; ++j) kr[j] = kn[j];
#pragma unroll
        for (int i = 0; i < 8; ++i) vr[i] = vn[i];
        __builtin_amdgcn_sched_barrier(0);
#pragma unroll
        for (int i = 0; i < 4; ++i)
#pragma unroll
            for (int q = 0; q < 2; ++q) xg[i][q] = *(const v4u*)(p.Aq + (size_t)(tk + 4 * i) * D + q * 512 + l * 8);
        fetch(it < 3 ? oc + 64 : 4096);
        __builtin_amdgcn_sched_barrier(0);
#pragma unroll
        for (int c8 = 0; c8 < 8; ++c8) {
            const unsigned s01 = sk[c8 >> 1][(c8 & 1) * 2], s23 = sk[c8 >> 1][(c8 & 1) * 2 + 1];
            const float sv[4] = {hi16(s23), lo16(s23), hi16(s01), lo16(s01)};
            float kk[11], u[8];
#pragma unroll
            for (int j = 0; j < 11; ++j) kk[j] = (c8 & 1) ? hi16(kr[j][c8 >> 1]) : lo16(kr[j][c8 >> 1]);
            f32x2 acc[4];
#pragma unroll
            for (int j = 0; j < 4; ++j)
#pragma unroll
                for (int i = 0; i < 4; ++i) { const f32x2 t = rbf2((f32x2){kk[i + j], kk[i + j + 4]} * sv[j]); acc[i] = j == 0 ? t : rbf2(acc[i] + t); }
#pragma unroll
            for (int i = 0; i < 8; ++i) u[i] = acc[i & 3][i >> 2] * ((c8 & 1) ? hi16(vr[i][c8 >> 1]) : lo16(vr[i][c8 >> 1]));
            st16(r, W_U + (cu * 8 + c8) * 4096 + oc * 16, (v4u){pack2(u[7], u[6]), pack2(u[5], u[4]), pack2(u[3], u[2]), pack2(u[1], u[0])});
        }
        float ga[4];
#pragma unroll
        for (int i = 0; i < 4; ++i) { float acc = 0.f;
#pragma unroll
            for (int q = 0; q < 2; ++q)
#pragma unroll
                for (int e = 0; e < 4; ++e) { const unsigned xa = xg[i][q][e], gb = gw[q][e]; acc += rbfh(lo16(xa) * lo16(gb)); acc += rbfh(hi16(xa) * hi16(gb)); }
            ga[i] = acc; }
        for (int o = 32; o > 0; o >>= 1)
#pragma unroll
            for (int i = 0; i < 4; ++i) ga[i] += __shfl_xor(ga[i], o);
        const float z = rbfh(l == 0 ? ga[0] : l == 1 ? ga[1] : l == 2 ? ga[2] : ga[3]);
        st4(r, l < 4 ? W_GATE + (tk + 4 * l) * 4 : 0xF0000000u, __float_as_uint(rbfh(1.f / (1.f + expf(-z)))));
    }
    DRAIN(); __syncthreads();
    if (tid == 0) st4(r, F_PREP + k4 * 4, tag);
}
__device__ __forceinline__ bool conv_block(const Params& p, rsrc_t r, int k4, unsigned char* lds, const int wv, const int j, const unsigned tag) {
    (void)j;
    const int tid = otid(), w = wv, l = tid & 63;
    const rsrc_t rs = __builtin_amdgcn_make_buffer_rsrc((void*)p.ssm_k, 0, S * D * 2, 0x00020000);
    v4u kraw[2][4], uv[8]; unsigned dn;
    auto fetch_k = [&](int d0) {
#pragma unroll
        for (int ci = 0; ci < 2; ++ci)
#pragma unroll
            for (int i = 0; i < 4; ++i) kraw[ci][i] = __builtin_amdgcn_raw_buffer_load_b128(rs, (unsigned)((d0 + 2 * w + ci) * 4096 + (i * 64 + l) * 16), 0, 2);
        dn = *(const unsigned*)(p.ssm_D + (d0 & 1023) + 2 * w); };
    auto fetch_u = [&](unsigned o) {
#pragma unroll
        for (int i = 0; i < 8; ++i) uv[i] = ld16(r, o + i * 4096 + tid * 16); };
    fetch_k(k4 * 32);
    if (!wait_prep(r, l, tag)) return false;
    if (SKEWH) wait_long(rtnow() + (unsigned)k4 * SKEWH);
    fetch_u(W_U + k4 * 32 * 4096);
#pragma unroll 1
    for (int it = 0; it < 4; ++it) {
    const int bb = k4 * 4 + it; const unsigned dd = dn;
    { const v4u z = {0u, 0u, 0u, 0u};
#pragma unroll
      for (int i = 0; i < 5; ++i) { const int q = tid + 256 * i; *(v4u*)(lds + (q / 160) * CV_U + (q % 160) * 16) = z; } }
#pragma unroll
    for (int i = 0; i < 8; ++i) *(v4u*)(lds + i * CV_U + (32 + (tid >> 2)) * CV_RP + (tid & 3) * 16) = uv[i];
    unsigned char* kc = lds + CV_K0 + w * 4 * CV_KP;
    auto copies = [&](int ci) {
        if (l < 32) *(unsigned*)(kc + l * 4) = 0u;
        if (l < 48) *(unsigned*)(kc + 128 + 4096 + l * 4) = 0u;
#pragma unroll
        for (int i = 0; i < 4; ++i) *(v4u*)(kc + 128 + (i * 64 + l) * 16) = kraw[ci][i];
        __builtin_amdgcn_wave_barrier();
#pragma unroll
        for (int it = 0; it < 9; ++it) { const int i = min(l + 64 * it, CV_KP / 8 - 2);
            const v2u e01 = *(const v2u*)(kc + i * 8), e23 = *(const v2u*)(kc + i * 8 + 8);
            const unsigned s0 = (e01.x >> 16) | (e01.y << 16), s1 = (e01.y >> 16) | (e23.x << 16), s2 = (e23.x >> 16) | (e23.y << 16);
            *(v2u*)(kc + CV_KP + i * 8) = (v2u){s0, s1};
            *(v2u*)(kc + 2 * CV_KP + i * 8) = (v2u){e01.y, e23.x};
            *(v2u*)(kc + 3 * CV_KP + i * 8) = (v2u){s1, s2}; }
        __builtin_amdgcn_wave_barrier(); };
    copies(0);
    __syncthreads();
    const int b = l & 31, kg = l >> 5, cc = (b + 1) & 3;
    float ys[32];
#pragma unroll
    for (int ci = 0; ci < 2; ++ci) {
        const int c8 = 2 * w + ci;
        if (ci == 1) { copies(1); __builtin_amdgcn_sched_barrier(0); fetch_k(it < 3 ? bb * 8 + 8 : 1024); fetch_u(it < 3 ? W_U + (bb * 8 + 8) * 4096 : 0xF0000000u); __builtin_amdgcn_sched_barrier(0); }
        f32x16 acc0, acc1, ad0, ad1;
#pragma unroll
        for (int i = 0; i < 16; ++i) { acc0[i] = 0.f; acc1[i] = 0.f; ad0[i] = 0.f; ad1[i] = 0.f; }
        typedef const __attribute__((address_space(3))) v2u* l2p;
        unsigned kb = (unsigned)(size_t)(kc + cc * CV_KP + (b - 8 * kg - 7 + 64 - cc) * 2) - 32, kb8 = kb + 8;
        asm volatile("" : "+v"(kb), "+v"(kb8));
        const unsigned char* ub = lds + c8 * CV_U + (32 + b) * CV_RP + kg * 16;
        v2u ka[4][2], kz[4][2]; bf16x8 fa[4], fz[4];
        auto ld_k = [&](int set, int un, int hi) { const int dl = un >> 1, kh = un & 1; const unsigned o = (unsigned)((32 * dl - 16 * kh + 16) * 2 + hi * 2048);
            if (hi) { kz[set][0] = *(l2p)(size_t)(kb + o); kz[set][1] = *(l2p)(size_t)(kb8 + o); } else { ka[set][0] = *(l2p)(size_t)(kb + o); ka[set][1] = *(l2p)(size_t)(kb8 + o); } };
        auto ld_f = [&](int set, int un) { const int dl = un >> 1, kh = un & 1;
            fa[set] = *(const bf16x8*)(ub + (32 - dl) * CV_RP + kh * 32); fz[set] = *(const bf16x8*)(ub - dl * CV_RP + kh * 32); };
#pragma unroll
        for (int q = 0; q < 2; ++q) { ld_k(q, q, 0); ld_f(q, q); ld_k(q, q, 1); }
        {
            const unsigned dv = ci ? dd >> 16 : dd & 0xFFFFu;
#pragma unroll
            for (int kh = 0; kh < 2; ++kh) { const int e = 7 - (b - 16 * kh - 8 * kg); const unsigned x = dv << ((e & 1) * 16); const int q = e >> 1;
                const v4u di = {q == 0 ? x : 0u, q == 1 ? x : 0u, q == 2 ? x : 0u, q == 3 ? x : 0u};
                asm volatile("s_nop 1\n\tv_mfma_f32_32x32x16_bf16 %0, %2, %4, %0\n\tv_mfma_f32_32x32x16_bf16 %1, %3, %4, %1" : "+v"(ad1), "+v"(ad0) : "v"(fa[kh]), "v"(fz[kh]), "v"(di)); }
        }
#pragma unroll 1
        for (int u0 = 0; u0 < 64; u0 += 4) {
#pragma unroll
            for (int q = 0; q < 4; ++q) {
                const int un = u0 + q + 2, ns = (q + 2) & 3;
                const v4u kav = {ka[q][0].x, ka[q][0].y, ka[q][1].x, ka[q][1].y}, kzv = {kz[q][0].x, kz[q][0].y, kz[q][1].x, kz[q][1].y};
                __builtin_amdgcn_sched_barrier(0);
                asm volatile("s_nop 1\n\tv_mfma_f32_32x32x16_bf16 %0, %1, %2, %0" : "+v"(acc1) : "v"(fa[q]), "v"(kav));
                ld_k(ns, un, 0);
                __builtin_amdgcn_sched_barrier(0);
                asm volatile("v_mfma_f32_32x32x16_bf16 %0, %1, %2, %0" : "+v"(acc0) : "v"(fz[q]), "v"(kav));
                ld_f(ns, un);
                __builtin_amdgcn_sched_barrier(0);
                asm volatile("v_mfma_f32_32x32x16_bf16 %0, %1, %2, %0" : "+v"(acc1) : "v"(fz[q]), "v"(kzv));
                ld_k(ns, un, 1);
            }
        }
        asm volatile("s_nop 7\n\ts_nop 7" : "+v"(acc0), "+v"(acc1), "+v"(ad0), "+v"(ad1));
#pragma unroll
        for (int i = 0; i < 32; ++i) { const float y = rbfh(i < 16 ? acc0[i] : acc1[i - 16]) + rbfh(i < 16 ? ad0[i] : ad1[i - 16]);
            ys[i] = ci == 0 ? y : __uint_as_float(pack2(ys[i], y)); }
    }
    __syncthreads();
#pragma unroll
    for (int i = 0; i < 32; ++i) { const int row = (i >> 4) * 32 + (i & 3) + 8 * ((i & 15) >> 2) + 4 * kg; *(float*)(lds + (row * 32 + b) * 16 + w * 4) = ys[i]; }
    __syncthreads();
#pragma unroll
    for (int i = 0; i < 8; ++i) { const int t = tid + 256 * i; st16(r, W_A + bb * 32768 + t * 16, *(const v4u*)(lds + t * 16)); }
    __syncthreads();
    }
    DRAIN(); __syncthreads();
    if (tid == 0) __hip_atomic_fetch_add((unsigned*)(p.ws + F_CONV), 1u, __ATOMIC_RELAXED, __HIP_MEMORY_SCOPE_AGENT);
    return true;
}

__device__ __forceinline__ void dma16(unsigned ldsdst, unsigned voff, const v4u& srd, unsigned soff) {
    asm volatile("s_mov_b32 m0, %0\n\ts_nop 0\n\tbuffer_load_dwordx4 %1, %2, %3 offen lds" :: "s"(ldsdst), "v"(voff), "s"(srd), "s"(soff) : "m0", "memory"); }
__device__ __forceinline__ void dma16n(unsigned ldsdst, unsigned voff, const v4u& srd, unsigned soff) {
    asm volatile("s_mov_b32 m0, %0\n\ts_nop 0\n\tbuffer_load_dwordx4 %1, %2, %3 offen nt lds" :: "s"(ldsdst), "v"(voff), "s"(srd), "s"(soff) : "m0", "memory"); }
__device__ __forceinline__ v4u pack_acc(const f32x4& a, const f32x4& b) {
    float x0, x1, x2, x3, y0, y1, y2, y3;
    asm volatile("v_accvgpr_read_b32 %0, %8\n\tv_accvgpr_read_b32 %1, %9\n\tv_accvgpr_read_b32 %2, %10\n\tv_accvgpr_read_b32 %3, %11\n\t"
                 "v_accvgpr_read_b32 %4, %12\n\tv_accvgpr_read_b32 %5, %13\n\tv_accvgpr_read_b32 %6, %14\n\tv_accvgpr_read_b32 %7, %15"
                 : "=v"(x0), "=v"(x1), "=v"(x2), "=v"(x3), "=v"(y0), "=v"(y1), "=v"(y2), "=v"(y3)
                 : "a"(a[0]), "a"(a[1]), "a"(a[2]), "a"(a[3]), "a"(b[0]), "a"(b[1]), "a"(b[2]), "a"(b[3]));
    return (v4u){pack2(x0, x1), pack2(x2, x3), pack2(y0, y1), pack2(y2, y3)}; }
#define SB() __builtin_amdgcn_sched_barrier(0)
#define WO_REGS "a128", "a129", "a130", "a131", "a132", "a133", "a134", "a135", "a136", "a137", "a138", "a139", "a140", "a141", "a142", "a143", "a144", "a145", "a146", "a147", "a148", "a149", "a150", "a151", "a152", "a153", "a154", "a155", "a156", "a157", "a158", "a159", "a160", "a161", "a162", "a163", "a164", "a165", "a166", "a167", "a168", "a169", "a170", "a171", "a172", "a173", "a174", "a175", "a176", "a177", "a178", "a179", "a180", "a181", "a182", "a183", "a184", "a185", "a186", "a187", "a188", "a189", "a190", "a191", "a192", "a193", "a194", "a195", "a196", "a197", "a198", "a199", "a200", "a201", "a202", "a203", "a204", "a205", "a206", "a207", "a208", "a209", "a210", "a211", "a212", "a213", "a214", "a215", "a216", "a217", "a218", "a219", "a220", "a221", "a222", "a223", "a224", "a225", "a226", "a227", "a228", "a229", "a230", "a231", "a232", "a233", "a234", "a235", "a236", "a237", "a238", "a239", "a240", "a241", "a242", "a243", "a244", "a245", "a246", "a247", "a248", "a249", "a250", "a251", "a252", "a253", "a254", "a255", "v192", "v193", "v194", "v195", "v196", "v197", "v198", "v199", "v200", "v201", "v202", "v203", "v204", "v205", "v206", "v207", "v208", "v209", "v210", "v211", "v212", "v213", "v214", "v215", "v216", "v217", "v218", "v219", "v220", "v221", "v222", "v223", "v224", "v225", "v226", "v227", "v228", "v229", "v230", "v231", "v232", "v233", "v234", "v235", "v236", "v237", "v238", "v239", "v240", "v241", "v242", "v243", "v244", "v245", "v246", "v247", "v248", "v249", "v250", "v251", "v252", "v253", "v254", "v255"
struct NoHook { __device__ void operator()(int) const {} };
template <int MODE, int MI, int NJ, int NS, int HX = 0, class HOOK = NoHook, int MN = MI>
__device__ __forceinline__ void gemm(rsrc_t r, const bf16_t* __restrict__ A, const bf16_t* __restrict__ B, int m0, int n0, unsigned char* lds, unsigned coff, const int wv,
                                     const int rot, const void* tk = nullptr,
                                     const bool have = false, const bf16_t* Bn = nullptr, int m0n = 0, int n0n = 0, HOOK&& hook = NoHook()) {
    constexpr int AB = MN * 4096, BB = NJ * 2048, B0 = (NS / 2) * AB, NB = NJ / 2, NM = MI * NJ, NR = MI + NJ, BP = NJ * 64;
    static_assert(NS % 2 == 0 && 32 % NS == 0 && B0 <= 65536 && NS * BB <= 65536 && (NJ == 4 || NJ == 8), "ring");
    const int tid = otid();
    const int w = wv, l = tid & 63, n16 = l & 15, g = l >> 4;
    const int wm = w >> 1, wn = w & 1;
    const unsigned lb = __builtin_amdgcn_readfirstlane((unsigned)(size_t)lds);
    const v4u sa = mk_srd(A, 2048 * 1024 * 2); v4u sb = mk_srd(B, 1024 * 4096 * 2);
    f32x4 acc[MI][NJ];
#pragma unroll
    for (int i = 0; i < MI; ++i)
#pragma unroll
        for (int j = 0; j < NJ; ++j) acc[i][j] = (f32x4){0.f, 0.f, 0.f, 0.f};
    unsigned va = (unsigned)((m0 + 8 * MI * w + (l >> 3)) * 2048 + (((l & 7) ^ (l >> 3)) << 4));
    unsigned vb[2];
    if (NJ == 8) { vb[0] = (unsigned)((8 * w + (l >> 5)) * 8192 + n0 * 2) + (unsigned)(((l & 31) << 4) ^ ((l >> 5) << 5) ^ (wn << 7)); vb[1] = vb[0] ^ 64u; }
    else { vb[0] = (unsigned)((8 * w + g) * 8192 + n0 * 2) + (unsigned)((n16 << 4) ^ (g << 5) ^ (wn << 7)); vb[1] = vb[0]; }
    auto rk = [&](int kt) { return (kt & ~31) | ((kt + rot) & 31); };
    unsigned la = lb + w * MI * 1024;
    auto st_a = [&](int buf, int kt, int q) { dma16(la + buf * AB + q * 1024, va, sa, (unsigned)(rk(kt) * 64 + q * 16384)); };
    auto st_b = [&](int buf, int kt, int x) { if ((B_NT >> (NJ == 4 ? 0 : MI == 4 ? 1 : 2)) & 1) dma16n(lb + B0 + buf * BB + (w * NB + x) * 1024, vb[x & 1], sb, (unsigned)((rk(kt) * 32 + x * (8 / NB)) * 8192));
        else dma16(lb + B0 + buf * BB + (w * NB + x) * 1024, vb[x & 1], sb, (unsigned)((rk(kt) * 32 + x * (8 / NB)) * 8192)); };
    const unsigned f = (unsigned)((n16 >> 2) | ((g & 1) << 2));
    constexpr int NQ = MODE == 1 ? NJ : NJ / 2;
    unsigned ra[2], rb[NQ];
    ra[0] = lb + (16 * MI * wm + n16) * 128 + ((g ^ (n16 & 7)) << 4); ra[1] = ra[0] ^ 64u;
    {   const unsigned row = lb + B0 + (8 * g + (n16 >> 2)) * BP + (NJ == 8 ? 256 * wn : 0), g0 = NJ == 8 ? 0u : 4u * wn;
#pragma unroll
        for (int j = 0; j < NQ; ++j) {
            if (MODE == 1) rb[j] = row + (((g0 + j) ^ f) << 5) + 8 * (n16 & 3);
            else rb[j] = row + (((g0 + 2 * j + ((n16 & 3) >> 1)) ^ f) << 5) + (n16 & 1) * 16;
            asm volatile("" : "+v"(rb[j])); } }
    bf16x8 af[2][MI], bfr[2][NJ];
    auto rd = [&](int set, int buf, int x) {
        if (x < NJ) { const unsigned a = rb[MODE == 1 ? x : (x >> 1)] + (MODE == 1 ? 0 : (x & 1) * 8) + buf * BB;
            const v2u lo = trb(a), hi = trb(a + 4 * BP); v4u v = {lo.x, lo.y, hi.x, hi.y}; bfr[set][x] = as_frag(v); }
        else af[set][x - NJ] = ldsa(ra[buf & 1] + (buf >> 1) * AB + (x - NJ) * 2048); };
    auto mf = [&](int set, int i, int j) {
        if (HX) asm volatile("v_mfma_f32_16x16x32_bf16 %0, %1, %2, %0" : "+a"(acc[i][j]) : "v"(MODE == 1 ? af[set][i] : bfr[set][j]), "v"(MODE == 1 ? bfr[set][j] : af[set][i]) : WO_REGS);
        else if (MODE == 1) asm volatile("v_mfma_f32_16x16x32_bf16 %0, %1, %2, %0" : "+a"(acc[i][j]) : "v"(af[set][i]), "v"(bfr[set][j]));
        else asm volatile("v_mfma_f32_16x16x32_bf16 %0, %1, %2, %0" : "+a"(acc[i][j]) : "v"(bfr[set][j]), "v"(af[set][i])); };
    if (!have) {
#pragma unroll
        for (int t = 0; t < NS; ++t) {
            if (t & 1) {
#pragma unroll
                for (int q = 0; q < MI; ++q) st_a(t >> 1, t - 1, q); }
#pragma unroll
            for (int q = 0; q < NB; ++q) st_b(t, t, q);
            if (HX) hook(t); }
    }
#pragma unroll
    for (int i = 0; i < MI; ++i)
#pragma unroll
        for (int j = 0; j < NJ; ++j) asm volatile("" : "+a"(acc[i][j]));
    asm volatile("s_nop 1" ::: "memory");
    if (!have) {
        asm volatile("s_waitcnt vmcnt(%c0)\n\ts_barrier" :: "i"((NS - 1) * (NB + HX) + (NS / 2 - 1) * MI) : "memory");
    }
#pragma unroll
    for (int x = 0; x < NR; ++x) rd(0, 0, x);
    constexpr int HEAD = NM / 8;
    const v4u st = mk_srd(tk, tk ? 32768 : 0); unsigned tdum = 0u;
#pragma unroll 1
    for (int k0 = 0; k0 < 32; k0 += NS) {
        int kb = k0 + NS;
        if (kb == 32) {
            kb = Bn ? 0 : 0x10100;
            if (Bn) { va += (unsigned)((m0n - m0 + 8 * (MN - MI) * w) * 2048); la = lb + w * MN * 1024; vb[0] += (unsigned)((n0n - n0) * 2); vb[1] += (unsigned)((n0n - n0) * 2); sb = mk_srd(Bn, 1024 * 4096 * 2); } }
        if (NS == 4) asm volatile("buffer_load_dword %0, %1, %2, %3 offen" : "+v"(tdum) : "v"(l < 32 ? (unsigned)l * 128u : 0xF0000000u), "s"(st), "s"((unsigned)k0 * 1024u) : "memory");
#pragma unroll
        for (int u = 0; u < NS; ++u) {
            SB();
#pragma unroll
            for (int q = 0; q < HEAD; ++q) mf(u & 1, q / NJ, q % NJ);
            SB();
            asm volatile("s_waitcnt vmcnt(%c0) lgkmcnt(0)\n\ts_barrier" :: "i"((NS - 2) * (NB + HX) + (NS / 2 - 1 - (u & 1)) * MI) : "memory");
            SB();
            constexpr int span = NM - HEAD;
#pragma unroll
            for (int q = HEAD; q < NM; ++q) {
                mf(u & 1, q / NJ, q % NJ);
                const int x = q - HEAD, nd = NB + ((u & 1) ? MI : 0);
#pragma unroll
                for (int y = 0; y < NR; ++y) if ((y * (span - HEAD)) / NR == x) rd((u & 1) ^ 1, (u + 1) % NS, y);
#pragma unroll
                for (int y = 0; y < nd; ++y) if ((y * span + span / 2) / nd == x) { if ((u & 1) && y < MI) st_a(u >> 1, kb + u - 1, y); else st_b(u, kb + u, y - ((u & 1) ? MI : 0)); }
                SB(); }
            if (MN > MI && (u & 1) && k0 == 32 - NS) {
#pragma unroll
                for (int y = MI; y < MN; ++y) st_a(u >> 1, kb + u - 1, y); }
            if (HX) hook(NS + k0 + u);
        }
    }
    asm volatile("s_nop 7\n\ts_nop 3" : "+v"(tdum) :: "memory");
    SB();
    const int col = n0 + 16 * NJ * wn, head = col >> 7, sub = (col & 127) >> 4, seg0 = (m0 >> 5) + (MI / 2) * wm;
#pragma unroll
    for (int sg = 0; sg < MI / 2; ++sg) {
        const unsigned base = coff + (unsigned)(((seg0 + sg) * 32 + head) * 8192 + l * 16);
#pragma unroll
        for (int q = 0; q < NJ; ++q) {
            if (HX) asm volatile("" ::: WO_REGS);
            if (MODE == 0) { const int tt = q / (NJ / 2), kk = q % (NJ / 2); st16(r, base + (tt * 4 + (sub >> 1) + kk) * 1024, pack_acc(acc[2 * sg + tt][2 * kk], acc[2 * sg + tt][2 * kk + 1])); }
            else st16(r, base + (sub + q) * 1024, pack_acc(acc[2 * sg][q], acc[2 * sg + 1][q])); }
    }
}

__device__ __forceinline__ unsigned min4(unsigned v) { return min(min(__builtin_amdgcn_readlane(v, 0), __builtin_amdgcn_readlane(v, 1)), min(__builtin_amdgcn_readlane(v, 2), __builtin_amdgcn_readlane(v, 3))); }
__device__ __forceinline__ void attn_block(const Params& p, rsrc_t r, unsigned char* lds, const int wv, const int h, const int j, const unsigned tag) {
    (void)j;
    const int l = otid() & 63, n16 = l & 15, g = l >> 4;
    const unsigned OOB = 0xF0000000u;
    v4u qv[2][4], kv[2][4], va[8];
    int nev = 0; (void)nev;
    auto ld_q = [&](unsigned o) {
#pragma unroll
        for (int i = 0; i < 8; ++i) qv[i >> 2][i & 3] = ld16(r, W_QX + o + i * 1024); };
    auto ld_k = [&](unsigned o) {
#pragma unroll
        for (int i = 0; i < 8; ++i) kv[i >> 2][i & 3] = ld16(r, W_KX + o + i * 1024); };
    auto ld_v = [&](unsigned o) {
#pragma unroll
        for (int i = 0; i < 8; ++i) va[i] = ld16(r, W_VF + o + i * 1024); };
    auto count = [&](int seg, int qm) -> unsigned { unsigned old = 0u;
        if ((l & 31) == 0 && ((qm >> (l >> 5)) & 1)) old = __hip_atomic_fetch_add((unsigned*)(p.ws + F_ATT + (2 * seg + (l >> 5)) * 64), 1u, __ATOMIC_RELAXED, __HIP_MEMORY_SCOPE_AGENT);
        return old; };
    auto tell = [&](int seg, unsigned old) { const int tt = 2 * seg + (l >> 5);
        const unsigned long long last = __ballot(old == 31u);
        st4(r, ((last >> (l & 32)) & 1ull) ? F_RDY + (((tt & 3) * 32 + (l & 31)) * 32 + (tt >> 2)) * 4 : OOB, 32u); };
    unsigned prog = 0u, tseen = rtnow() - 100000u;
    unsigned long long tiles = 0ull;
    bool have = false; int pend = -1;
    const int nit = wv < 2 ? 17 : 16;
    auto task = [&](int it) -> int { return it == nit - 1 ? 63 - (wv >> 1) : 4 * it + wv; };
#pragma unroll 1
    for (int it = 0; it < nit; ++it) {
        const int seg = task(it), qm = it == nit - 1 ? 1 << (wv & 1) : 3;
        if (!have) {
            for (unsigned spin = 0; ((tiles >> (seg & 60)) & 15ull) != 15ull; ++spin) { MEMBAR();
                if (spin) __builtin_amdgcn_s_sleep(16);
                tiles = __ballot(ld4(r, F_KVQ + ((l >> 2) * 32 + h) * 64 + min(l & 3, 2) * 4) == tag);
                if (spin > (1u << 20)) { st4(r, F_ERR, 64u); return; } }
            MEMBAR();
            const unsigned o = (unsigned)((seg * 32 + h) * 8192 + l * 16);
            ld_k(o); ld_q(o); ld_v(o);
        }
        const bool early = prog > (unsigned)seg;
        v4u k0, k1; v2u pm[8];
        auto ld_m = [&](unsigned km) {
            k0 = ld16(r, km + l * 16); k1 = ld16(r, km + 1024 + l * 16);
#pragma unroll
            for (int t = 0; t < 8; ++t) pm[t] = ld8(r, km + 2048 + t * 256 + n16 * 16 + (g & 1) * 8); };
        ld_m(early ? HO_KVM + (seg * 32 + h) * 4096 : OOB);
        f32x4 sc[2][3]; float mx[2];
#pragma unroll
        for (int qt = 0; qt < 2; ++qt)
            asm volatile("s_nop 1\n\t"
                "v_mfma_f32_16x16x32_bf16 %0, %2, %10, 0\n\tv_mfma_f32_16x16x32_bf16 %1, %6, %10, 0\n\tv_mfma_f32_16x16x32_bf16 %0, %3, %11, %0\n\tv_mfma_f32_16x16x32_bf16 %1, %7, %11, %1\n\t"
                "v_mfma_f32_16x16x32_bf16 %0, %4, %12, %0\n\tv_mfma_f32_16x16x32_bf16 %1, %8, %12, %1\n\tv_mfma_f32_16x16x32_bf16 %0, %5, %13, %0\n\tv_mfma_f32_16x16x32_bf16 %1, %9, %13, %1"
                : "=&v"(sc[qt][0]), "=&v"(sc[qt][1])
                : "v"(kv[0][0]), "v"(kv[0][1]), "v"(kv[0][2]), "v"(kv[0][3]), "v"(kv[1][0]), "v"(kv[1][1]), "v"(kv[1][2]), "v"(kv[1][3]), "v"(qv[qt][0]), "v"(qv[qt][1]), "v"(qv[qt][2]), "v"(qv[qt][3]));
        asm volatile("s_nop 7\n\ts_waitcnt vmcnt(0)" : "+v"(sc[0][0]), "+v"(sc[0][1]), "+v"(sc[1][0]), "+v"(sc[1][1]), "+v"(k0), "+v"(k1), "+v"(pm[0]), "+v"(pm[1]), "+v"(pm[2]), "+v"(pm[3]), "+v"(pm[4]), "+v"(pm[5]), "+v"(pm[6]), "+v"(pm[7]),
                     "+v"(va[0]), "+v"(va[1]), "+v"(va[2]), "+v"(va[3]), "+v"(va[4]), "+v"(va[5]), "+v"(va[6]), "+v"(va[7]) :: "memory");
        const unsigned po = count(pend, pend >= 0 ? 3 : 0);
        const int sn = task(it + 1);
        have = it + 1 < nit && ((tiles >> (sn & 60)) & 15ull) == 15ull;
        const unsigned on = have ? (unsigned)((sn * 32 + h) * 8192 + l * 16) : OOB;
        ld_k(on);
#pragma unroll
        for (int qt = 0; qt < 2; ++qt) { const int qi = 16 * qt + n16; float m = -INFINITY;
#pragma unroll
            for (int t = 0; t < 2; ++t)
#pragma unroll
                for (int rr = 0; rr < 4; ++rr) { const float v = (16 * t + 4 * g + rr <= qi) ? rbfh(rbfh(sc[qt][t][rr]) * 0.08838834764831845f) : -1000000000.0f; sc[qt][t][rr] = v; m = fmaxf(m, v); }
            mx[qt] = m; }
        tell(pend, po);
        if (!early) {
            for (unsigned spin = 0; prog <= (unsigned)seg; ++spin) {
                const unsigned now = rtnow();
                if (spin > (1u << 22)) { st4(r, F_ERR, 32u); return; }
                if (prog + (seg >= EARLY_FROM ? 2u : 1u) + (now - tseen) / STEP_MIN <= (unsigned)seg) { __builtin_amdgcn_s_sleep(16); continue; }
                MEMBAR();
                const unsigned pg = min4(ld4(r, F_PROG + h * 128 + min(l, 2) * 8));
                if (pg != prog) { prog = pg; tseen = now; }
                if (prog <= (unsigned)seg) __builtin_amdgcn_s_sleep(P_SLP);
            }
            MEMBAR();
            ld_m(HO_KVM + (seg * 32 + h) * 4096);
            asm volatile("" : "+v"(k0), "+v"(k1), "+v"(pm[0]), "+v"(pm[1]), "+v"(pm[2]), "+v"(pm[3]), "+v"(pm[4]), "+v"(pm[5]), "+v"(pm[6]), "+v"(pm[7]));
        }
        v4u kmv[4];
        *(v4u*)(lds + wv * 2048 + l * 16) = k0; *(v4u*)(lds + wv * 2048 + 1024 + l * 16) = k1;
        __builtin_amdgcn_wave_barrier();
#pragma unroll
        for (int ks = 0; ks < 4; ++ks) { const unsigned a = (unsigned)(size_t)lds + wv * 2048 + (32 * ks + 8 * g + (n16 >> 2)) * 16 + (n16 & 1) * 8; const v2u x0 = trb(a), x1 = trb(a + 64); kmv[ks] = (v4u){x0.x, x0.y, x1.x, x1.y}; }
        asm volatile("s_nop 1\n\t"
            "v_mfma_f32_16x16x32_bf16 %0, %2, %6, 0\n\tv_mfma_f32_16x16x32_bf16 %1, %2, %10, 0\n\tv_mfma_f32_16x16x32_bf16 %0, %3, %7, %0\n\tv_mfma_f32_16x16x32_bf16 %1, %3, %11, %1\n\t"
            "v_mfma_f32_16x16x32_bf16 %0, %4, %8, %0\n\tv_mfma_f32_16x16x32_bf16 %1, %4, %12, %1\n\tv_mfma_f32_16x16x32_bf16 %0, %5, %9, %0\n\tv_mfma_f32_16x16x32_bf16 %1, %5, %13, %1\n\t"
            "s_nop 2\n\tv_mov_b32 %14, %14\n\ts_nop 2\n\tv_mov_b32 %14, %14"
            : "=&v"(sc[0][2]), "=&v"(sc[1][2])
            : "v"(kmv[0]), "v"(kmv[1]), "v"(kmv[2]), "v"(kmv[3]), "v"(qv[0][0]), "v"(qv[0][1]), "v"(qv[0][2]), "v"(qv[0][3]), "v"(qv[1][0]), "v"(qv[1][1]), "v"(qv[1][2]), "v"(qv[1][3]), "v"(l));
        v4u pk[2][4];
#pragma unroll
        for (int qt = 0; qt < 2; ++qt) {
            if (!((qm >> qt) & 1)) continue;
            float m = mx[qt];
#pragma unroll
            for (int rr = 0; rr < 4; ++rr) { const float v = g >= 2 ? -INFINITY : rbfh(rbfh(sc[qt][2][rr]) * 0.08838834764831845f); sc[qt][2][rr] = v; m = fmaxf(m, v); }
            m = xor16_max(m); m = xor32_max(m); float sum = 0.f;
#pragma unroll
            for (int t = 0; t < 3; ++t)
#pragma unroll
                for (int rr = 0; rr < 4; ++rr) { const float e = __expf(sc[qt][t][rr] - m); sc[qt][t][rr] = e; sum += e; }
            sum = xor16_add(sum); sum = xor32_add(sum); const float inv = __builtin_amdgcn_rcpf(sum);
            const v4u p0 = {pack2(sc[qt][0][0] * inv, sc[qt][0][1] * inv), pack2(sc[qt][0][2] * inv, sc[qt][0][3] * inv), pack2(sc[qt][1][0] * inv, sc[qt][1][1] * inv), pack2(sc[qt][1][2] * inv, sc[qt][1][3] * inv)};
            const v2u p1 = {pack2(sc[qt][2][0] * inv, sc[qt][2][1] * inv), pack2(sc[qt][2][2] * inv, sc[qt][2][3] * inv)};
            f32x4 oa[8];
            asm volatile("s_nop 1\n\t"
                "v_mfma_f32_16x16x32_bf16 %0, %8, %24, 0\n\tv_mfma_f32_16x16x32_bf16 %1, %9, %24, 0\n\tv_mfma_f32_16x16x32_bf16 %2, %10, %24, 0\n\tv_mfma_f32_16x16x32_bf16 %3, %11, %24, 0\n\t"
                "v_mfma_f32_16x16x32_bf16 %4, %12, %24, 0\n\tv_mfma_f32_16x16x32_bf16 %5, %13, %24, 0\n\tv_mfma_f32_16x16x32_bf16 %6, %14, %24, 0\n\tv_mfma_f32_16x16x32_bf16 %7, %15, %24, 0\n\t"
                "v_mfma_f32_16x16x16_bf16 %0, %16, %25, %0\n\tv_mfma_f32_16x16x16_bf16 %1, %17, %25, %1\n\tv_mfma_f32_16x16x16_bf16 %2, %18, %25, %2\n\tv_mfma_f32_16x16x16_bf16 %3, %19, %25, %3\n\t"
                "v_mfma_f32_16x16x16_bf16 %4, %20, %25, %4\n\tv_mfma_f32_16x16x16_bf16 %5, %21, %25, %5\n\tv_mfma_f32_16x16x16_bf16 %6, %22, %25, %6\n\tv_mfma_f32_16x16x16_bf16 %7, %23, %25, %7\n\t"
                "s_nop 2\n\tv_mov_b32 %26, %26\n\ts_nop 2\n\tv_mov_b32 %26, %26"
                : "=&v"(oa[0]), "=&v"(oa[1]), "=&v"(oa[2]), "=&v"(oa[3]), "=&v"(oa[4]), "=&v"(oa[5]), "=&v"(oa[6]), "=&v"(oa[7])
                : "v"(va[0]), "v"(va[1]), "v"(va[2]), "v"(va[3]), "v"(va[4]), "v"(va[5]), "v"(va[6]), "v"(va[7]),
                  "v"(pm[0]), "v"(pm[1]), "v"(pm[2]), "v"(pm[3]), "v"(pm[4]), "v"(pm[5]), "v"(pm[6]), "v"(pm[7]), "v"(p0), "v"(p1), "v"(l));
#pragma unroll
            for (int u = 0; u < 4; ++u) {
                const f32x4 o0 = oa[2 * u], o1 = oa[2 * u + 1];
                pk[qt][u] = (v4u){pack2(o0[0], o0[1]), pack2(o0[2], o0[3]), pack2(o1[0], o1[1]), pack2(o1[2], o1[3])};
                st16(r, W_OX + ((((seg * 2 + qt) * 8 + (h & 7)) * 4 + u) * 4 + (h >> 3)) * 1024 + l * 16, pk[qt][u]);
            }
        }
        pend = (early && have) ? seg : -1;
        if (pend < 0) { DRAIN(); tell(seg, count(seg, qm)); }
        MEMBAR();
        ld_q(on); ld_v(on);
    }
}

constexpr int OP_P = 80;
__device__ __forceinline__ void oproj_block(const Params& p, rsrc_t r, unsigned char* lds, const int wv, const int nt, const int rho, const int j, const unsigned tag, const unsigned g_t0) {
    unsigned g_t1 = 0u, g_t2 = 0u; (void)g_t0; (void)g_t1; (void)g_t2;
    (void)j;
    const int w = wv, l = otid() & 63, n16 = l & 15, g = l >> 4;
    const unsigned ldsbase = (unsigned)(size_t)lds;
    bf16x8 wof[32][2]; v32u wo[6];
    asm volatile("" : "={a[128:159]}"(wo[0]), "={a[160:191]}"(wo[1]), "={a[192:223]}"(wo[2]), "={a[224:255]}"(wo[3]), "={v[192:223]}"(wo[4]), "={v[224:255]}"(wo[5]));
#pragma unroll
    for (int kq = 0; kq < 32; ++kq) {
        const int ks = (kq + 16) & 31;
        const unsigned st = ldsbase + w * 8192 + (ks & 1) * 4096, sa = st + (l >> 2) * OP_P + (l & 3) * 16;
        switch (ks) {
            case 0: asm volatile("ds_write_b128 %0, a[128:131]\n\tds_write_b128 %0, a[132:135] offset:%c2" :: "v"(sa), "{a[128:159]}"(wo[0]), "i"(16 * OP_P) : "memory"); break;
            case 1: asm volatile("ds_write_b128 %0, a[136:139]\n\tds_write_b128 %0, a[140:143] offset:%c2" :: "v"(sa), "{a[128:159]}"(wo[0]), "i"(16 * OP_P) : "memory"); break;
            case 2: asm volatile("ds_write_b128 %0, a[144:147]\n\tds_write_b128 %0, a[148:151] offset:%c2" :: "v"(sa), "{a[128:159]}"(wo[0]), "i"(16 * OP_P) : "memory"); break;
            case 3: asm volatile("ds_write_b128 %0, a[152:155]\n\tds_write_b128 %0, a[156:159] offset:%c2" :: "v"(sa), "{a[128:159]}"(wo[0]), "i"(16 * OP_P) : "memory"); break;
            case 4: asm volatile("ds_write_b128 %0, a[160:163]\n\tds_write_b128 %0, a[164:167] offset:%c2" :: "v"(sa), "{a[160:191]}"(wo[1]), "i"(16 * OP_P) : "memory"); break;
            case 5: asm volatile("ds_write_b128 %0, a[168:171]\n\tds_write_b128 %0, a[172:175] offset:%c2" :: "v"(sa), "{a[160:191]}"(wo[1]), "i"(16 * OP_P) : "memory"); break;
            case 6: asm volatile("ds_write_b128 %0, a[176:179]\n\tds_write_b128 %0, a[180:183] offset:%c2" :: "v"(sa), "{a[160:191]}"(wo[1]), "i"(16 * OP_P) : "memory"); break;
            case 7: asm volatile("ds_write_b128 %0, a[184:187]\n\tds_write_b128 %0, a[188:191] offset:%c2" :: "v"(sa), "{a[160:191]}"(wo[1]), "i"(16 * OP_P) : "memory"); break;
            case 8: asm volatile("ds_write_b128 %0, a[192:195]\n\tds_write_b128 %0, a[196:199] offset:%c2" :: "v"(sa), "{a[192:223]}"(wo[2]), "i"(16 * OP_P) : "memory"); break;
            case 9: asm volatile("ds_write_b128 %0, a[200:203]\n\tds_write_b128 %0, a[204:207] offset:%c2" :: "v"(sa), "{a[192:223]}"(wo[2]), "i"(16 * OP_P) : "memory"); break;
            case 10: asm volatile("ds_write_b128 %0, a[208:211]\n\tds_write_b128 %0, a[212:215] offset:%c2" :: "v"(sa), "{a[192:223]}"(wo[2]), "i"(16 * OP_P) : "memory"); break;
            case 11: asm volatile("ds_write_b128 %0, a[216:219]\n\tds_write_b128 %0, a[220:223] offset:%c2" :: "v"(sa), "{a[192:223]}"(wo[2]), "i"(16 * OP_P) : "memory"); break;
            case 12: asm volatile("ds_write_b128 %0, a[224:227]\n\tds_write_b128 %0, a[228:231] offset:%c2" :: "v"(sa), "{a[224:255]}"(wo[3]), "i"(16 * OP_P) : "memory"); break;
            case 13: asm volatile("ds_write_b128 %0, a[232:235]\n\tds_write_b128 %0, a[236:239] offset:%c2" :: "v"(sa), "{a[224:255]}"(wo[3]), "i"(16 * OP_P) : "memory"); break;
            case 14: asm volatile("ds_write_b128 %0, a[240:243]\n\tds_write_b128 %0, a[244:247] offset:%c2" :: "v"(sa), "{a[224:255]}"(wo[3]), "i"(16 * OP_P) : "memory"); break;
            case 15: asm volatile("ds_write_b128 %0, a[248:251]\n\tds_write_b128 %0, a[252:255] offset:%c2" :: "v"(sa), "{a[224:255]}"(wo[3]), "i"(16 * OP_P) : "memory"); break;
            case 16: asm volatile("ds_write_b128 %0, v[192:195]\n\tds_write_b128 %0, v[196:199] offset:%c2" :: "v"(sa), "{v[192:223]}"(wo[4]), "i"(16 * OP_P) : "memory"); break;
            case 17: asm volatile("ds_write_b128 %0, v[200:203]\n\tds_write_b128 %0, v[204:207] offset:%c2" :: "v"(sa), "{v[192:223]}"(wo[4]), "i"(16 * OP_P) : "memory"); break;
            case 18: asm volatile("ds_write_b128 %0, v[208:211]\n\tds_write_b128 %0, v[212:215] offset:%c2" :: "v"(sa), "{v[192:223]}"(wo[4]), "i"(16 * OP_P) : "memory"); break;
            case 19: asm volatile("ds_write_b128 %0, v[216:219]\n\tds_write_b128 %0, v[220:223] offset:%c2" :: "v"(sa), "{v[192:223]}"(wo[4]), "i"(16 * OP_P) : "memory"); break;
            case 20: asm volatile("ds_write_b128 %0, v[224:227]\n\tds_write_b128 %0, v[228:231] offset:%c2" :: "v"(sa), "{v[224:255]}"(wo[5]), "i"(16 * OP_P) : "memory"); break;
            case 21: asm volatile("ds_write_b128 %0, v[232:235]\n\tds_write_b128 %0, v[236:239] offset:%c2" :: "v"(sa), "{v[224:255]}"(wo[5]), "i"(16 * OP_P) : "memory"); break;
            case 22: asm volatile("ds_write_b128 %0, v[240:243]\n\tds_write_b128 %0, v[244:247] offset:%c2" :: "v"(sa), "{v[224:255]}"(wo[5]), "i"(16 * OP_P) : "memory"); break;
            case 23: asm volatile("ds_write_b128 %0, v[248:251]\n\tds_write_b128 %0, v[252:255] offset:%c2" :: "v"(sa), "{v[224:255]}"(wo[5]), "i"(16 * OP_P) : "memory"); break;
        }
#pragma unroll
        for (int jn = 0; jn < 2; ++jn) {
            const int pt = ks < 24 ? OP_P : 64;
            const unsigned a0 = (ks < 24 ? st : WO_L + w * 16384 + (ks - 24) * 2048) + (4 * g + (n16 >> 2)) * pt + (16 * jn + 4 * (n16 & 3)) * 2;
            const v2u lo = trb(a0), hi = trb(a0 + 16 * pt);
            v4u v = {lo.x, lo.y, hi.x, hi.y}; asm volatile("" : "=a"(wof[ks][jn]) : "0"(as_frag(v))); }
    }
    __syncthreads();
    const v4u rd = mk_srd(p.ws, W_TOTAL), ro = mk_srd(p.out, S * D * 2), rx = mk_srd(p.Aq, S * D * 2);
    const unsigned OOB = 0xF0000000u;
    const int NT = 32, jn = w & 1;
    auto tile = [&](int i) -> unsigned { return (unsigned)(4 * i + rho); };
    const unsigned r_off = F_RDY + (rho * 32 + nt) * 128 + (l & 31) * 4;
    const unsigned a_b = W_OX + w * 1024 + l * 16;
    const int n0 = nt * 32 + 16 * jn + 4 * g;
    const unsigned c_b = W_A + (n0 >> 3) * 32768 + n16 * 16 + (g & 1) * 8, g_b = W_GATE + n16 * 4, o_b = (unsigned)((n16 * 1024 + n0) * 2);
    auto mix = [&](const v2u& av, const v2u& xv, unsigned gv, const f32x4& bs) -> v2u {
        const float gg = __uint_as_float(gv), og = rbfh(1.f - gg); float e[4];
#pragma unroll
        for (int k = 0; k < 4; ++k) { const unsigned aw = k < 2 ? av.x : av.y, xw = k < 2 ? xv.x : xv.y; e[k] = rbfh(gg * rbfh((k & 1) ? hi16(aw) * hi16(xw) : lo16(aw) * lo16(xw))) + rbfh(og * rbfh(bs[k])); }
        return (v2u){pack2(e[0], e[1]), pack2(e[2], e[3])}; };
    const unsigned s_w = ldsbase + w * 2048 + l * 16, s_r = ldsbase + jn * 1024 + l * 16;
    unsigned long long known = 0ull; int nev = 0; (void)nev;
    bool have = false, pend = false; v4u af[32]; v2u pa = {0u, 0u}, px = {0u, 0u}; unsigned pg = 0u, cf2 = 0u;
    auto finish = [&](int i, bool on) {
        v2u o = {0u, 0u};
        if (on) {
            const __attribute__((address_space(3))) f32x4* q = (const __attribute__((address_space(3))) f32x4*)(size_t)(s_r + (unsigned)(i & 1) * 8192u);
            const f32x4 bs = (q[0] + q[128]) + (q[256] + q[384]);
            o = mix(pa, px, pg, bs);
        }
        asm volatile("buffer_store_dwordx2 %0, %1, %2, 0 offen" :: "v"(o), "v"(on ? o_b + tile(i) * 32768u : OOB), "s"(ro) : "memory"); };
    if (!wait_prep(r, l, tag) || !wait_word(r, F_CONV, 32u, 256u)) ENDPGM();
#pragma unroll 1
    for (int i = 0; i < NT; ++i) {
        if (!have) {
            for (unsigned spin = 0; !((known >> i) & 1ull); ++spin) { MEMBAR();
                known = __ballot(ld4(r, r_off) == 32u);
                if ((known >> i) & 1ull) break;
                if (spin > (1u << 21)) { st4(r, F_ERR, 128u); ENDPGM(); }
                __builtin_amdgcn_s_sleep(O_SLP); }
            const unsigned a0 = a_b + tile(i) * 131072u;
#pragma unroll
            for (int q = 0; q < 8; ++q)
                asm volatile("buffer_load_dwordx4 %0, %4, %5, %6 offen sc1\n\tbuffer_load_dwordx4 %1, %7, %5, %6 offen sc1\n\t"
                             "buffer_load_dwordx4 %2, %8, %5, %6 offen sc1\n\tbuffer_load_dwordx4 %3, %9, %5, %6 offen sc1"
                             : "=&v"(af[4 * q]), "=&v"(af[4 * q + 1]), "=&v"(af[4 * q + 2]), "=&v"(af[4 * q + 3]) : "v"(a0), "s"(rd), "s"(q * 16384), "v"(a0 + 4096u), "v"(a0 + 8192u), "v"(a0 + 12288u) : "memory");
            asm volatile("buffer_store_dword %0, %0, %1, 0 offen\n\tbuffer_store_dword %0, %0, %1, 0 offen" :: "v"(OOB), "s"(ro) : "memory");
        }
        const bool fin = (w >> 1) == (i & 1);
        const bool more = i + 1 < NT && ((known >> (i + 1)) & 1ull);
        const unsigned an = more ? a_b + tile(i + 1) * 131072u : OOB;
        unsigned cfl, fg; v2u fa, fx;
        asm volatile("buffer_load_dwordx2 %0, %1, %2, 0 offen" : "=&v"(fx) : "v"(fin ? o_b + tile(i) * 32768u : OOB), "s"(rx) : "memory");
        asm volatile("buffer_load_dword %0, %3, %6, 0 offen sc1\n\tbuffer_load_dwordx2 %1, %4, %6, 0 offen sc1\n\tbuffer_load_dword %2, %5, %6, 0 offen sc1"
                     : "=&v"(cfl), "=&v"(fa), "=&v"(fg) : "v"(((known >> (i + 2)) & 1ull) ? OOB : r_off), "v"(fin ? c_b + tile(i) * 256u : OOB), "v"(fin ? g_b + tile(i) * 64u : OOB), "s"(rd) : "memory");
        f32x4 acc0 = {0.f, 0.f, 0.f, 0.f}, acc1 = {0.f, 0.f, 0.f, 0.f};
        asm volatile("s_nop 1" : "+v"(acc0), "+v"(acc1));
#pragma unroll
        for (int q = 0; q < 8; ++q) {
            if (q == 2) finish(i - 1, pend);
            if (q == 6) asm volatile("buffer_load_dword %0, %1, %2, 0 offen sc1" : "+v"(cf2) : "v"(more ? OOB : r_off), "s"(rd) : "memory");
            asm volatile(
                "s_waitcnt vmcnt(37)\n\tv_mfma_f32_16x16x32_bf16 %4, %6, %0, %4\n\tv_mfma_f32_16x16x32_bf16 %5, %7, %0, %5\n\tbuffer_load_dwordx4 %0, %14, %15, %16 offen sc1\n\t"
                "s_waitcnt vmcnt(37)\n\tv_mfma_f32_16x16x32_bf16 %4, %8, %1, %4\n\tv_mfma_f32_16x16x32_bf16 %5, %9, %1, %5\n\tbuffer_load_dwordx4 %1, %17, %15, %16 offen sc1\n\t"
                "s_waitcnt vmcnt(37)\n\tv_mfma_f32_16x16x32_bf16 %4, %10, %2, %4\n\tv_mfma_f32_16x16x32_bf16 %5, %11, %2, %5\n\tbuffer_load_dwordx4 %2, %18, %15, %16 offen sc1\n\t"
                "s_waitcnt vmcnt(37)\n\tv_mfma_f32_16x16x32_bf16 %4, %12, %3, %4\n\tv_mfma_f32_16x16x32_bf16 %5, %13, %3, %5\n\tbuffer_load_dwordx4 %3, %19, %15, %16 offen sc1"
                : "+v"(af[4 * q]), "+v"(af[4 * q + 1]), "+v"(af[4 * q + 2]), "+v"(af[4 * q + 3]), "+v"(acc0), "+v"(acc1)
                : "a"(wof[4 * q][0]), "a"(wof[4 * q][1]), "a"(wof[4 * q + 1][0]), "a"(wof[4 * q + 1][1]), "a"(wof[4 * q + 2][0]), "a"(wof[4 * q + 2][1]), "a"(wof[4 * q + 3][0]), "a"(wof[4 * q + 3][1]),
                  "v"(an), "s"(rd), "s"(q * 16384), "v"(an + 4096u), "v"(an + 8192u), "v"(an + 12288u) : "memory"); }
        const unsigned po = (unsigned)(i & 1) * 8192u;
        asm volatile("s_nop 7\n\tds_write_b128 %6, %4\n\tds_write_b128 %6, %5 offset:1024\n\ts_waitcnt vmcnt(34) lgkmcnt(0)\n\ts_barrier"
                     : "+v"(cfl), "+v"(fa), "+v"(fg), "+v"(fx) : "v"(acc0), "v"(acc1), "v"(s_w + po) : "memory");
        known |= __ballot(cfl == 32u);
        pa = fa; px = fx; pg = fg; pend = fin && more;
        if (!more) finish(i, fin);
        have = more;
        if (!more) {
            asm volatile("s_waitcnt vmcnt(1)" : "+v"(cf2), "+v"(af[0]), "+v"(af[1]), "+v"(af[2]), "+v"(af[3]), "+v"(af[4]), "+v"(af[5]), "+v"(af[6]), "+v"(af[7]), "+v"(af[8]), "+v"(af[9]), "+v"(af[10]), "+v"(af[11]),
                         "+v"(af[12]), "+v"(af[13]), "+v"(af[14]), "+v"(af[15]) :: "memory");
            known |= __ballot(cf2 == 32u);
        }
    }
    if (nt == 0 && rho == 0 && w == 0) st4(r, l == 0 ? F_EP : OOB, tag);
}

__device__ __forceinline__ void bulk_block(const Params& p, rsrc_t r, unsigned char* lds, const int wv, const int j, const unsigned tag, const unsigned g_t0) {
    const int x = (j & 7) >> 1, i32 = (j >> 3) * 2 + (j & 1);
    auto flags = [&](int mt, int nmt, int h0, int nh, int mat) {
        DRAIN(); __syncthreads();
        const int tid = otid(); if (tid < nmt * nh) st4(r, F_KVQ + ((mt + tid / nh) * 32 + h0 + tid % nh) * 64 + mat * 4, tag); };
#pragma unroll 1
    for (int t = 0; t < 2; ++t) {
        const int mt = 2 * t + (i32 & 1), mat = (i32 >> 1) & 1, hd = 8 * x + (i32 >> 2), rot = ((i32 >> 1) & 7) * 2;
        const bf16_t* Bm = mat ? p.Wv : p.Wk;
        if (mat) gemm<1, 4, 4, 8>(r, p.Aq, Bm, mt * 128, hd * 128, lds, W_VF, wv, rot, nullptr, t == 1, t == 0 ? Bm : nullptr, (mt + 2) * 128, hd * 128);
        else gemm<0, 4, 4, 8>(r, p.Aq, Bm, mt * 128, hd * 128, lds, W_KX, wv, rot, nullptr, t == 1, t == 0 ? Bm : nullptr, (mt + 2) * 128, hd * 128);
        flags(mt, 1, hd, 1, mat);
    }
    const int hp = 4 * x + (i32 >> 3), q8 = i32 & 7, rot = ROT2 * (i32 >> 2);
    if (SKEW) wait_long(rtnow() + (unsigned)((q8 & 3) * 4 + x) * SKEW);
    {
        const int m = q8 < 4 ? 4 + (q8 & 1) : q8 - 4, mat = q8 < 4 ? q8 >> 1 : 2;
        const bf16_t* Bn = mat == 2 ? p.Wq : mat == 1 ? p.Wv : p.Wk;
        const int mt = 4 + (q8 & 3), mv = q8 >> 2;
        const bf16_t* tk = (wv == 1 && j < 32) ? p.Aq + (64 + j) * 16384 : nullptr;
        if (mv) gemm<1, 4, 8, 4, 0, NoHook, 8>(r, p.Aq, p.Wv, mt * 128, hp * 256, lds, W_VF, wv, rot, tk, false, Bn, m * 256, hp * 256);
        else gemm<0, 4, 8, 4, 0, NoHook, 8>(r, p.Aq, p.Wk, mt * 128, hp * 256, lds, W_KX, wv, rot, tk, false, Bn, m * 256, hp * 256);
        flags(mt, 1, 2 * hp, 2, mv);
        const bf16_t* tl = (wv == 1 && j < 32) ? p.Aq + (96 + j) * 16384 : nullptr;
        if (mat == 1) gemm<1, 8, 8, 4>(r, p.Aq, Bn, m * 256, hp * 256, lds, W_VF, wv, rot, tl, true);
        else gemm<0, 8, 8, 4>(r, p.Aq, Bn, m * 256, hp * 256, lds, mat == 2 ? W_QX : W_KX, wv, rot, tl, true);
        flags(2 * m, 2, 2 * hp, 2, mat);
    }
    {
        unsigned wi = 0u;
        const int l = otid() & 63; const v4u sw = mk_srd(p.Wo, 4096 * 1024 * 2); const unsigned wa = (unsigned)((wv * 1024 + (l >> 2)) * 2048 + i32 * 64 + (l & 3) * 16);
        unsigned long long tab;
        asm volatile("s_getpc_b64 %0\n\ts_branch .Lwo%=\n\t"
                     "buffer_load_dwordx4 a[128:131], v191, s[92:95], s91 offen\n\ts_setpc_b64 s[96:97]\n\t" "buffer_load_dwordx4 a[132:135], v191, s[92:95], s91 offen\n\ts_setpc_b64 s[96:97]\n\t"
                     "buffer_load_dwordx4 a[136:139], v191, s[92:95], s91 offen\n\ts_setpc_b64 s[96:97]\n\t" "buffer_load_dwordx4 a[140:143], v191, s[92:95], s91 offen\n\ts_setpc_b64 s[96:97]\n\t"
                     "buffer_load_dwordx4 a[144:147], v191, s[92:95], s91 offen\n\ts_setpc_b64 s[96:97]\n\t" "buffer_load_dwordx4 a[148:151], v191, s[92:95], s91 offen\n\ts_setpc_b64 s[96:97]\n\t"
                     "buffer_load_dwordx4 a[152:155], v191, s[92:95], s91 offen\n\ts_setpc_b64 s[96:97]\n\t" "buffer_load_dwordx4 a[156:159], v191, s[92:95], s91 offen\n\ts_setpc_b64 s[96:97]\n\t"
                     "buffer_load_dwordx4 a[160:163], v191, s[92:95], s91 offen\n\ts_setpc_b64 s[96:97]\n\t" "buffer_load_dwordx4 a[164:167], v191, s[92:95], s91 offen\n\ts_setpc_b64 s[96:97]\n\t"
                     "buffer_load_dwordx4 a[168:171], v191, s[92:95], s91 offen\n\ts_setpc_b64 s[96:97]\n\t" "buffer_load_dwordx4 a[172:175], v191, s[92:95], s91 offen\n\ts_setpc_b64 s[96:97]\n\t"
                     "buffer_load_dwordx4 a[176:179], v191, s[92:95], s91 offen\n\ts_setpc_b64 s[96:97]\n\t" "buffer_load_dwordx4 a[180:183], v191, s[92:95], s91 offen\n\ts_setpc_b64 s[96:97]\n\t"
                     "buffer_load_dwordx4 a[184:187], v191, s[92:95], s91 offen\n\ts_setpc_b64 s[96:97]\n\t" "buffer_load_dwordx4 a[188:191], v191, s[92:95], s91 offen\n\ts_setpc_b64 s[96:97]\n\t"
                     "buffer_load_dwordx4 a[192:195], v191, s[92:95], s91 offen\n\ts_setpc_b64 s[96:97]\n\t" "buffer_load_dwordx4 a[196:199], v191, s[92:95], s91 offen\n\ts_setpc_b64 s[96:97]\n\t"
                     "buffer_load_dwordx4 a[200:203], v191, s[92:95], s91 offen\n\ts_setpc_b64 s[96:97]\n\t" "buffer_load_dwordx4 a[204:207], v191, s[92:95], s91 offen\n\ts_setpc_b64 s[96:97]\n\t"
                     "buffer_load_dwordx4 a[208:211], v191, s[92:95], s91 offen\n\ts_setpc_b64 s[96:97]\n\t" "buffer_load_dwordx4 a[212:215], v191, s[92:95], s91 offen\n\ts_setpc_b64 s[96:97]\n\t"
                     "buffer_load_dwordx4 a[216:219], v191, s[92:95], s91 offen\n\ts_setpc_b64 s[96:97]\n\t" "buffer_load_dwordx4 a[220:223], v191, s[92:95], s91 offen\n\ts_setpc_b64 s[96:97]\n\t"
                     "buffer_load_dwordx4 a[224:227], v191, s[92:95], s91 offen\n\ts_setpc_b64 s[96:97]\n\t" "buffer_load_dwordx4 a[228:231], v191, s[92:95], s91 offen\n\ts_setpc_b64 s[96:97]\n\t"
                     "buffer_load_dwordx4 a[232:235], v191, s[92:95], s91 offen\n\ts_setpc_b64 s[96:97]\n\t" "buffer_load_dwordx4 a[236:239], v191, s[92:95], s91 offen\n\ts_setpc_b64 s[96:97]\n\t"
                     "buffer_load_dwordx4 a[240:243], v191, s[92:95], s91 offen\n\ts_setpc_b64 s[96:97]\n\t" "buffer_load_dwordx4 a[244:247], v191, s[92:95], s91 offen\n\ts_setpc_b64 s[96:97]\n\t"
                     "buffer_load_dwordx4 a[248:251], v191, s[92:95], s91 offen\n\ts_setpc_b64 s[96:97]\n\t" "buffer_load_dwordx4 a[252:255], v191, s[92:95], s91 offen\n\ts_setpc_b64 s[96:97]\n\t"
                     "buffer_load_dwordx4 v[192:195], v191, s[92:95], s91 offen\n\ts_setpc_b64 s[96:97]\n\t" "buffer_load_dwordx4 v[196:199], v191, s[92:95], s91 offen\n\ts_setpc_b64 s[96:97]\n\t"
                     "buffer_load_dwordx4 v[200:203], v191, s[92:95], s91 offen\n\ts_setpc_b64 s[96:97]\n\t" "buffer_load_dwordx4 v[204:207], v191, s[92:95], s91 offen\n\ts_setpc_b64 s[96:97]\n\t"
                     "buffer_load_dwordx4 v[208:211], v191, s[92:95], s91 offen\n\ts_setpc_b64 s[96:97]\n\t" "buffer_load_dwordx4 v[212:215], v191, s[92:95], s91 offen\n\ts_setpc_b64 s[96:97]\n\t"
                     "buffer_load_dwordx4 v[216:219], v191, s[92:95], s91 offen\n\ts_setpc_b64 s[96:97]\n\t" "buffer_load_dwordx4 v[220:223], v191, s[92:95], s91 offen\n\ts_setpc_b64 s[96:97]\n\t"
                     "buffer_load_dwordx4 v[224:227], v191, s[92:95], s91 offen\n\ts_setpc_b64 s[96:97]\n\t" "buffer_load_dwordx4 v[228:231], v191, s[92:95], s91 offen\n\ts_setpc_b64 s[96:97]\n\t"
                     "buffer_load_dwordx4 v[232:235], v191, s[92:95], s91 offen\n\ts_setpc_b64 s[96:97]\n\t" "buffer_load_dwordx4 v[236:239], v191, s[92:95], s91 offen\n\ts_setpc_b64 s[96:97]\n\t"
                     "buffer_load_dwordx4 v[240:243], v191, s[92:95], s91 offen\n\ts_setpc_b64 s[96:97]\n\t" "buffer_load_dwordx4 v[244:247], v191, s[92:95], s91 offen\n\ts_setpc_b64 s[96:97]\n\t"
                     "buffer_load_dwordx4 v[248:251], v191, s[92:95], s91 offen\n\ts_setpc_b64 s[96:97]\n\t" "buffer_load_dwordx4 v[252:255], v191, s[92:95], s91 offen\n\ts_setpc_b64 s[96:97]\n\t"
                     "buffer_load_dwordx4 v191, s[92:95], s91 offen lds\n\ts_setpc_b64 s[96:97]\n.Lwo%=:" : "=s"(tab));
        auto hook = [&](int) {
            const unsigned q = min(wi, 63u);
            asm volatile("s_mov_b32 m0, %1\n\ts_swappc_b64 s[96:97], %0"
                         :: "s"(tab + (min(q, 48u) * 12u + 4u)), "s"(WO_L + wv * 16384 + (q - 48u) * 1024u), "{v191}"(wa), "{s[92:95]}"(sw), "{s91}"(q * 32768u) : "memory", "m0", "s96", "s97", WO_REGS);
            ++wi; };
#pragma unroll 1
        for (int t = 0; t < 2; ++t) {
            const int mt = t ? 8 + q8 : 12 + (q8 & 3), mv = t ? 2 : q8 >> 2;
            if (mv == 1) gemm<1, 4, 8, 4, 1>(r, p.Aq, p.Wv, mt * 128, hp * 256, lds, W_VF, wv, rot, nullptr, false, p.Wq, (8 + q8) * 128, hp * 256, hook);
            else gemm<0, 4, 8, 4, 1>(r, p.Aq, t ? p.Wq : p.Wk, mt * 128, hp * 256, lds, t ? W_QX : W_KX, wv, rot, nullptr, t == 1, t ? nullptr : p.Wq, (8 + q8) * 128, hp * 256, hook);
            flags(mt, 1, 2 * hp, 2, mv);
        }
        oproj_block(p, r, lds, wv, i32, x, j, tag, g_t0);
    }
}

__device__ __forceinline__ void help_block(const Params& p, rsrc_t r, unsigned char* lds, const int wv, const int k, const unsigned tag, const unsigned far) {
    const int j = -32 + k; (void)j;
    {
        const unsigned th = rtnow() + HELP_D + (unsigned)k * SKEWP, OOB = 0xF0000000u;
        const int l = otid() & 63, n16 = l & 15;
        const rsrc_t rh = __builtin_amdgcn_make_buffer_rsrc(p.ws + hx_base(p.ws), 0, U_END * 16384, 0x00020000);
        const unsigned un = (unsigned)(wv * 32 + (k & 30)) * 16384u + (k & 1) * 4096u, ca = l == 0 ? un : OOB;
        v4u z[4]; unsigned best[4] = {~0u, ~0u, ~0u, ~0u};
#pragma unroll
        for (int i = 0; i < 4; ++i) z[i] = (v4u){~0u, ~0u, ~0u, ~0u};
        for (int i = 0; i < CAL_N; ++i)
#pragma unroll
            for (int q = 0; q < 4; ++q) {
                const unsigned t0 = (unsigned)__builtin_amdgcn_s_memtime(); __builtin_amdgcn_s_waitcnt(0);
                st16(rh, ca + q * 8192, z[0]); DRAIN();
                const unsigned t1 = (unsigned)__builtin_amdgcn_s_memtime(); __builtin_amdgcn_s_waitcnt(0);
                best[q] = min(best[q], t1 - t0); }
        const unsigned up = min(best[1], best[3]) < min(best[0], best[2]) ? 1u : 0u, vote = (up ^ far) | ((up ? best[2] < best[0] : best[3] < best[1]) ? 2u : 0u);
        if (wv == 0 && k < 8) {
#pragma unroll
            for (int i = 0; i < 4; ++i) z[i] = __builtin_elementwise_min(*(const v4u*)(p.mem0 + ((n16 & 1) + 2 * (n16 >> 2)) * 1024 + ((k & 3) * 4 + i) * 64 + (l >> 4) * 16 + (n16 & 2) * 4), (v4u){~1u, ~1u, ~1u, ~1u}); }
#pragma unroll
        for (int i = 0; i < 16; ++i) st16(rh, un + (i >> 2) * 8192 + (i & 3) * 1024 + l * 16, z[i & 3]);
        st4(r, (wv == 3 && l < 3) ? F_PROG + k * 128 + l * 8 : OOB, l ? 63u : 0u);
        DRAIN();
        st8(r, l == 0 ? F_CAL + k * 32 + wv * 8 : OOB, (v2u){tag, vote});
        wait_long(th);
    }
    prep_block(p, r, k, wv, tag);
    if (!conv_block(p, r, k, lds, wv, j, tag)) return;
    attn_block(p, r, lds, wv, k, j, tag);
}

__global__ void __launch_bounds__(256, 1) k_main(Params p) {
    extern __shared__ __attribute__((aligned(16))) unsigned char lds[];
    const unsigned g_t0 = DIAG ? rtnow() : 0u;
    rsrc_t r = __builtin_amdgcn_make_buffer_rsrc(p.ws, 0, W_TOTAL, 0x00020000);
    const int wv = __builtin_amdgcn_readfirstlane(threadIdx.x >> 6);
    int role = blockIdx.x;
    unsigned xcc; asm volatile("s_getreg_b32 %0, hwreg(HW_REG_XCC_ID)" : "=s"(xcc)); xcc &= 7u;
    unsigned old = 0u, xr = xcc, rk = blockIdx.x >> 3;
    if (MIXH) { if (xcc == 3u) { xr = 4u + (rk >> 3); rk = 24u + (rk & 7u); } else if (xcc > 3u && rk >= 24u) { xr = 3u; rk = (xcc - 4u) * 8u + (rk & 7u); } }
    unsigned slot = (xr >> 2) * 128u + (rk >> 1) * 8u + (xr & 3u) * 2u + (rk & 1u);
    const unsigned tag = __builtin_amdgcn_readfirstlane(ld4(r, (threadIdx.x & 63) == 1 ? F_CLM + slot * 64 + 4 : F_EP)) * 2654435761u + 0x9E3779B9u;
    if (threadIdx.x == 0) old = __hip_atomic_exchange((unsigned*)(p.ws + F_CLM + slot * 64), tag, __ATOMIC_RELAXED, __HIP_MEMORY_SCOPE_AGENT);
    if (threadIdx.x == 0) {
        for (int i = 0; i < 256 && old == tag; ++i) { slot = (slot + 1u) & 255u; old = __hip_atomic_exchange((unsigned*)(p.ws + F_CLM + slot * 64), tag, __ATOMIC_RELAXED, __HIP_MEMORY_SCOPE_AGENT); }
        *(volatile __attribute__((address_space(3))) int*)(size_t)(unsigned)(size_t)lds = (int)slot;
    }
    __syncthreads();
    role = __builtin_amdgcn_readfirstlane(*(volatile __attribute__((address_space(3))) int*)(size_t)(unsigned)(size_t)lds) & 255;
    __syncthreads();
    if (role >= 128) bulk_block(p, r, lds, wv, role - 128, tag, g_t0);
    else if ((role & 6) == 6) help_block(p, r, lds, wv, (role >> 3) * 2 + (role & 1), tag, xcc >> 2);
    else rec_block(p, wv, role, tag);
}

extern "C" void kernel_launch(void* const* d_in, const int* in_sizes, int n_in, void* d_out, int out_size, void* d_ws, size_t ws_size, hipStream_t stream) {
    Params p;
    p.Aq = (const bf16_t*)d_in[0]; p.Ak = (const bf16_t*)d_in[1]; p.Av = (const bf16_t*)d_in[2];
    p.shift_k = (const bf16_t*)d_in[3]; p.ssm_k = (const bf16_t*)d_in[4]; p.ssm_D = (const bf16_t*)d_in[5];
    p.mem0 = (const bf16_t*)d_in[7]; p.Wq = (const bf16_t*)d_in[8]; p.Wk = (const bf16_t*)d_in[9];
    p.Wv = (const bf16_t*)d_in[10]; p.Wo = (const bf16_t*)d_in[11]; p.gate_w = (const bf16_t*)d_in[13];
    p.out = (bf16_t*)d_out; p.ws = (unsigned char*)d_ws;
    (void)hipFuncSetAttribute((const void*)k_main, hipFuncAttributeMaxDynamicSharedMemorySize, L_TOTAL);
    k_main<<<256, 256, L_TOTAL, stream>>>(p);
}
```

```cpp
#include <hip/hip_runtime.h>
#include <stdint.h>
#include <type_traits>
typedef unsigned short bf16_t;
using bf16x8 = __attribute__((ext_vector_type(8))) short;
using f32x4 = __attribute__((ext_vector_type(4))) float;
typedef unsigned v4u __attribute__((ext_vector_type(4)));
typedef unsigned v2u __attribute__((ext_vector_type(2)));
typedef __amdgpu_buffer_rsrc_t rsrc_t;

typedef __bf16 hbf2 __attribute__((ext_vector_type(2)));
typedef float f32x2 __attribute__((ext_vector_type(2)));
__device__ __forceinline__ unsigned pack2(float a, float b) { f32x2 v = {a, b}; return __builtin_bit_cast(unsigned, __builtin_convertvector(v, hbf2)); }
__device__ __forceinline__ float rbfh(float f) { return __uint_as_float(pack2(0.f, f)); }
__device__ __forceinline__ f32x2 rbf2(f32x2 f) { return (f32x2){rbfh(f.x), rbfh(f.y)}; }
__device__ __forceinline__ unsigned nosent(unsigned x) {
    typedef unsigned short us2 __attribute__((ext_vector_type(2)));
    const us2 a = __builtin_bit_cast(us2, x), c = {(unsigned short)0xFFFE, (unsigned short)0xFFFE}; return __builtin_bit_cast(unsigned, __builtin_elementwise_min(a, c)); }

constexpr int S = 2048, D = 1024, H = 32, DH = 128, M = 8, SEG = 32, NSEG = 64;

constexpr unsigned W_FLG = 0;
constexpr unsigned F_KVQ = W_FLG;
constexpr unsigned F_EP = W_FLG + 32768;
constexpr unsigned F_ERR = W_FLG + 32768 + 256;
constexpr unsigned F_PREP = W_FLG + 32768 + 512;
constexpr unsigned F_CAL = W_FLG + 73728;
constexpr unsigned F_CLM = W_FLG + 77824;
constexpr unsigned F_CONV = W_FLG + 33792;
constexpr unsigned F_PROG = W_FLG + 36864;
constexpr unsigned F_ATT = W_FLG + 40960;
constexpr unsigned F_RDY = W_FLG + 49152;
constexpr unsigned FLG_BYTES = 94208;
constexpr unsigned HO_KVM = W_FLG + FLG_BYTES;
constexpr unsigned W_A = HO_KVM + 64 * 32 * 4096;
constexpr unsigned W_GATE = W_A + S * D * 2;
constexpr unsigned W_QX = W_GATE + 8192;
constexpr unsigned W_KX = W_QX + S * 4096 * 2;
constexpr unsigned W_VF = W_KX + S * 4096 * 2;
constexpr unsigned W_OX = W_VF + 64 * 32 * 8192;
constexpr unsigned DBG_BYTES = 0;
constexpr unsigned W_DBG = W_OX + S * 4096 * 2;
constexpr unsigned W_U = W_DBG + DBG_BYTES;
constexpr unsigned W_XC = W_U + S * D * 2;
constexpr unsigned W_TOTAL = W_XC + 128 * 131072;

__device__ __forceinline__ v4u ld16(rsrc_t r, unsigned off) { return __builtin_amdgcn_raw_buffer_load_b128(r, off, 0, 16); }
__device__ __forceinline__ v2u ld8(rsrc_t r, unsigned off) { return __builtin_amdgcn_raw_buffer_load_b64(r, off, 0, 16); }
__device__ __forceinline__ unsigned ld4(rsrc_t r, unsigned off) { return __builtin_amdgcn_raw_buffer_load_b32(r, off, 0, 16); }
__device__ __forceinline__ void st16(rsrc_t r, unsigned off, v4u v) { __builtin_amdgcn_raw_buffer_store_b128(v, r, off, 0, 16); }
__device__ __forceinline__ void st8(rsrc_t r, unsigned off, v2u v) { __builtin_amdgcn_raw_buffer_store_b64(v, r, off, 0, 16); }
__device__ __forceinline__ void st4(rsrc_t r, unsigned off, unsigned v) { __builtin_amdgcn_raw_buffer_store_b32(v, r, off, 0, 16); }
__device__ __forceinline__ bf16x8 as_frag(v4u v) { return __builtin_bit_cast(bf16x8, v); }
#define D1 134
#define D2 90
#define D3 64
#define LB0 16
#define LC0 7
#define LA0 8
#define TR_UP 4
#define TR_DN 2
#define L_DEC 3
#define STAG 1
#define UP_SH 2
#define EARLY_FROM 58
#define OUT_WARM 0
#define AROT 0
#define P_SLP 2
#define O_SLP 2
#define W_NT 0
#define TOL 2
#define A_SLP 0
#define PEN2 20
#define A_UP 24
#define CT0 2
#define CPF 1
#define PH_M 3
#define SKEW 20
#define SKEW1 0
#define XT 1
#define TRK 3
#define WO_NT 0
#define WO_TRK 1
#define WIN 3
#define ROT0 3
#define ROT1 2
#define ROT2 2
#define B_NT 0
#define CALM 15
#define R4M 1
#define HX_SH 0u
#define FINE_T 1
#define FINE_N 0
#define FINE_A 0
#define O_ND 12
#define PEN0 32
#define X0C 20
#define X0A 16
#define X1A 8
#define PEN 24
#define MEMBAR() asm volatile("" ::: "memory")
#define ENDPGM() asm volatile("s_endpgm" ::: "memory")
#define DRAIN() asm volatile("s_waitcnt vmcnt(0)" ::: "memory")

__device__ __forceinline__ float xor16_max(float x) { auto r = __builtin_amdgcn_permlane16_swap(__float_as_uint(x), __float_as_uint(x), false, false); return fmaxf(__uint_as_float(r[0]), __uint_as_float(r[1])); }
__device__ __forceinline__ float xor32_max(float x) { auto r = __builtin_amdgcn_permlane32_swap(__float_as_uint(x), __float_as_uint(x), false, false); return fmaxf(__uint_as_float(r[0]), __uint_as_float(r[1])); }
__device__ __forceinline__ float xor16_add(float x) { auto r = __builtin_amdgcn_permlane16_swap(__float_as_uint(x), __float_as_uint(x), false, false); return __uint_as_float(r[0]) + __uint_as_float(r[1]); }
__device__ __forceinline__ float xor32_add(float x) { auto r = __builtin_amdgcn_permlane32_swap(__float_as_uint(x), __float_as_uint(x), false, false); return __uint_as_float(r[0]) + __uint_as_float(r[1]); }

typedef short v4i16_t __attribute__((ext_vector_type(4)));
__device__ __forceinline__ v2u trb(unsigned lds_addr) { return __builtin_bit_cast(v2u, __builtin_amdgcn_ds_read_tr16_b64_v4i16((__attribute__((address_space(3))) v4i16_t*)(size_t)lds_addr)); }
__device__ __forceinline__ int otid_(int wv) { int t; asm volatile("v_mbcnt_lo_u32_b32 %0, -1, 0\n\tv_mbcnt_hi_u32_b32 %0, -1, %0" : "=v"(t)); return wv * 64 + t; }
#define otid() otid_(wv)
__device__ __forceinline__ bool wait_word(rsrc_t r, unsigned off, unsigned want, unsigned code) {
    for (unsigned spin = 0;; ++spin) { MEMBAR();
        const unsigned v = __builtin_amdgcn_readfirstlane(ld4(r, off));
        if (v >= want) return true;
        if (spin > (1u << 20)) { st4(r, F_ERR, code); return false; }
        __builtin_amdgcn_s_sleep(8); }
}

__device__ __forceinline__ bool wait_prep(rsrc_t r, int l, unsigned tag) {
    for (unsigned spin = 0;; ++spin) { MEMBAR();
        const unsigned v = ld4(r, F_PREP + (l & 31) * 4);
        if (__all(v == tag)) return true;
        if (spin > (1u << 20)) { st4(r, F_ERR, 512u); return false; }
        __builtin_amdgcn_s_sleep(8); }
}
__device__ __forceinline__ unsigned rtnow() { return (unsigned)__builtin_amdgcn_s_memrealtime(); }
__device__ __forceinline__ void wait_long(unsigned target) { for (int i = 0; i < 4096; ++i) { if ((int)(rtnow() - target) >= 0) break; __builtin_amdgcn_s_sleep(8); } }
constexpr int L_TAB = 20480;
constexpr int L_QK = 24576;
__device__ __forceinline__ unsigned img(int f) { return (unsigned)(L_QK + f * 16 + (f >> 4) * 128); }
constexpr int L_FL = 30720, L_Z = L_FL + 128, L_LOG = 31744, L_W = 32768, L_V = 131072;
constexpr int L_TOTAL = 163840;
constexpr unsigned U_END = 96;
#define PMIN 76
#define V_SLP 2
#define R_SLP 1
constexpr unsigned WO_L = 98304;
#define HELP_D 200
#define V_DEL 16
#define XR 12
#define PFW 0
#define KEYM 3
#define DIAG 0
#define NEARX 0
#define MIXH 1
#define SKEWH 10
#define SKEWP 8
#define NEARY 0
#define CS1 0x8000u
#define CS2 0x10000u
#define X_SLP 2
#define X_DEL 6
#define FASTP 0
#define CAL_O 0u
#define H_SLP 0
#define CAL_N 3
#define STEP_MIN 180u

struct Params {
    const bf16_t *Aq, *Ak, *Av, *shift_k, *ssm_k, *ssm_D, *mem0, *Wq, *Wk, *Wv, *Wo, *gate_w;
    bf16_t* out; unsigned char* ws;
};
__host__ __device__ __forceinline__ unsigned hx_base(const void* ws) { const size_t a = (size_t)ws; return (unsigned)((((a + W_TOTAL + 0x7FFFFFu) >> 23) << 23) - a) + HX_SH; }

typedef short v4s __attribute__((ext_vector_type(4)));
typedef unsigned v32u __attribute__((ext_vector_type(32)));
typedef unsigned v16u __attribute__((ext_vector_type(16)));
typedef unsigned v8u __attribute__((ext_vector_type(8)));
typedef unsigned v12u __attribute__((ext_vector_type(12)));
__device__ __forceinline__ v4u mk_srd(const void* p, unsigned bytes) { const size_t a = (size_t)p; v4u d;
    d.x = __builtin_amdgcn_readfirstlane((unsigned)a); d.y = __builtin_amdgcn_readfirstlane((unsigned)(a >> 32) & 0xFFFFu); d.z = __builtin_amdgcn_readfirstlane(bytes); d.w = 0x00020000u; return d; }
__device__ __forceinline__ bf16x8 ldsa(unsigned addr) { return *(const __attribute__((address_space(3))) bf16x8*)(size_t)addr; }
__device__ __forceinline__ void rec_block(const Params& p, const int wv, const int b, const unsigned tag) {
    const int w = wv, tid = otid(), l = tid & 63, n16 = l & 15, g = l >> 4, c = n16 >> 2;
    const int x = (b & 7) >> 1, i32 = (b >> 3) * 2 + (b & 1);
    const unsigned OOB = 0xF0000000u;
    const v4u rhd = mk_srd(p.ws + hx_base(p.ws), U_END * 16384), wsd = mk_srd(p.ws, W_TOTAL);
    const int G = x - 1, vi = G * 32 + i32, h = x == 0 ? i32 : vi >> 1;
    const unsigned pr_a = F_PROG + h * 128 + (x == 0 ? 0 : 8 + (vi & 1) * 8), cal = F_CAL - w * 1024, kvo = HO_KVM + h * 4096 + (x ? 2048 : 0);
    if (CPF) {
        unsigned lo, hi, l2; asm volatile("s_getpc_b64 s[98:99]\n.Lpc%=:\n\ts_add_u32 %2, s98, rec_v_code-.Lpc%=\n\ts_add_u32 %0, s98, rec_s_code-.Lpc%=\n\ts_addc_u32 %1, s99, 0" : "=s"(lo), "=s"(hi), "=s"(l2) :: "s98", "s99", "scc");
        const size_t a = (((size_t)hi << 32) | lo) + (size_t)(long long)((x ? (int)(l2 - lo) : 0) + ((i32 * 4 + w) % 10) * 1024 - 4096 + l * 16);
        asm volatile("s_mov_b32 m0, %1\n\ts_nop 0\n\tglobal_load_lds_dwordx4 %0, off" :: "v"(a & ~(size_t)15), "s"(x ? L_V + 16384 : 0) : "memory", "m0");
    }
    typedef __attribute__((address_space(3))) volatile unsigned lu32;
    auto cpy = [](int k) -> unsigned { return k == 1 ? 0u : k * 32768u + 16384u; };
    auto cun = [](int u) -> unsigned { return ((u & 1) ? CS1 : 0u) ^ ((u & 2) ? CS2 : 0u); };
    unsigned nA = cpy(x), nW = cpy(w), nT = 64 * 16384 + (x ? 0 : h * 8), nS = 64 * 16384 + x * 1024, nC = 32 * 16384 + (x == 2 ? 16384 : 0), b0 = 65 * 16384 + (h >> 2) * 32768 + (h & 3) * 2048;
    unsigned pob = w == 0 ? nA + (i32 >> 4) * 32768 + (i32 & 15) * 512 : (w == 1 && x) ? (nC ^ cun(i32 >> 3)) + (i32 & 7) * 1024 : (w == 2 && i32 == 0) ? nS : OOB;
    if (x == 0) {
        const v4u sq = mk_srd(p.Wq, 1024 * 4096 * 2), sk = mk_srd(p.Wk, 1024 * 4096 * 2);
        const unsigned m0b = L_W + w * 1024, f = (c & 1) | ((g & 1) << 1) | ((c >> 1) << 2);
        const unsigned t0 = L_W + (16 * (g >> 1) + 4 * (g & 1) + (c & 1) + 8 * (c >> 1)) * 256 + (((2 * w) ^ f) << 5) + (n16 & 3) * 8;
        const unsigned dv = (4 * w + g) * 8192 + h * 256 + ((n16 ^ (((g & 1) | (w << 1)) << 1)) << 4);
        v8u pa;
#pragma unroll
        for (int q = 0; q < 8; ++q) pa[q] = (t0 + ((w + (q >> 1)) & 3) * 32768) ^ ((q & 1) << 5);
        const unsigned kx = W_KX + h * 8192, vx = W_VF + h * 8192 + w * 1024, kf = F_KVQ + h * 64;
        const unsigned aq = img(8 * g + (n16 >> 2)) + (n16 & 1) * 8;
        const v8u pc = {(unsigned)tid * 16, (unsigned)(L_W + w * 32768 + l * 16), (n16 & 2) ? 0xEEEEu : 0x4444u, aq,
                  (unsigned)(((w + 1) & 3) * 1024 + l * 16), (unsigned)(((w + 2) & 3) * 1024 + l * 16), (unsigned)(((w + 3) & 3) * 1024 + l * 16), img(32 * w + n16) + g * 4};
        const v2u pd = {(n16 & 2) ? (unsigned)L_Z : aq + w * 768, (n16 & 2) ? aq + w * 768 : (unsigned)L_Z};
        *(__attribute__((address_space(3))) v4u*)(size_t)(L_TAB + tid * 16) = (v4u){(unsigned)(L_QK + 3072 + g * 384 + n16 * 8),
                  n16 < 8 ? (unsigned)((((n16 >> 2) * 16384) ^ cun((h >> 1) & 3)) + ((h & 1) * 4 + (h >> 3)) * 1024 + w * 256 + (g >> 1) * 128 + (n16 & 2) * 32 + (g & 1) * 32 + (n16 & 1) * 16) : OOB,
                  (unsigned)((32 * w + 16 * (g & 1) + n16) * 16 + (g >> 1) * 8), (unsigned)(L_QK + 3072 + w * 768 + ((n16 & 1) + 2 * (n16 >> 2)) * 32 + g * 8 + (n16 & 2) * 2)};
        if (tid < 64) ((lu32*)(size_t)L_FL)[tid] = 0u;
    asm volatile(
        "s_add_u32 s80, %[m0b], 0\n\t"
        "s_mov_b32 s81, 0\n\t"
        "s_mov_b32 s82, 4\n\t"
        ".Ld0%=:\n\t"
        "s_mov_b32 m0, s80\n\t"
        "s_add_u32 s80, s80, 0x8000\n\t"
        "buffer_load_dwordx4 v96, %[sq], s81 offen lds\n\t"
        "s_add_u32 m0, m0, 0x1000\n\t"
        "s_add_u32 s81, s81, 0x20000\n\t"
        "buffer_load_dwordx4 v96, %[sq], s81 offen lds\n\t"
        "s_add_u32 m0, m0, 0x1000\n\t"
        "s_add_u32 s81, s81, 0x20000\n\t"
        "buffer_load_dwordx4 v96, %[sq], s81 offen lds\n\t"
        "s_add_u32 m0, m0, 0x1000\n\t"
        "s_add_u32 s81, s81, 0x20000\n\t"
        "buffer_load_dwordx4 v96, %[sq], s81 offen lds\n\t"
        "s_add_u32 m0, m0, 0x1000\n\t"
        "s_add_u32 s81, s81, 0x20000\n\t"
        "s_sub_u32 s82, s82, 1\n\t"
        "s_cmp_lg_u32 s82, 0\n\t"
        "s_cbranch_scc1 .Ld0%=\n\t"
        "s_add_u32 s80, %[m0b], 16384\n\t"
        "s_mov_b32 s81, 0\n\t"
        "s_mov_b32 s82, 4\n\t"
        ".Ld1%=:\n\t"
        "s_mov_b32 m0, s80\n\t"
        "s_add_u32 s80, s80, 0x8000\n\t"
        "buffer_load_dwordx4 v96, %[sk], s81 offen lds\n\t"
        "s_add_u32 m0, m0, 0x1000\n\t"
        "s_add_u32 s81, s81, 0x20000\n\t"
        "buffer_load_dwordx4 v96, %[sk], s81 offen lds\n\t"
        "s_add_u32 m0, m0, 0x1000\n\t"
        "s_add_u32 s81, s81, 0x20000\n\t"
        "buffer_load_dwordx4 v96, %[sk], s81 offen lds\n\t"
        "s_add_u32 m0, m0, 0x1000\n\t"
        "s_add_u32 s81, s81, 0x20000\n\t"
        "buffer_load_dwordx4 v96, %[sk], s81 offen lds\n\t"
        "s_add_u32 m0, m0, 0x1000\n\t"
        "s_add_u32 s81, s81, 0x20000\n\t"
        "s_sub_u32 s82, s82, 1\n\t"
        "s_cmp_lg_u32 s82, 0\n\t"
        "s_cbranch_scc1 .Ld1%=\n\t"
        "s_waitcnt vmcnt(16)\n\t"
        "s_barrier\n\t"
        "ds_read_b64_tr_b16 v[0:1], v98 offset:0\n\t"
        "ds_read_b64_tr_b16 v[2:3], v98 offset:512\n\t"
        "ds_read_b64_tr_b16 v[4:5], v98 offset:8192\n\t"
        "ds_read_b64_tr_b16 v[6:7], v98 offset:8704\n\t"
        "ds_read_b64_tr_b16 v[8:9], v99 offset:0\n\t"
        "ds_read_b64_tr_b16 v[10:11], v99 offset:512\n\t"
        "ds_read_b64_tr_b16 v[12:13], v99 offset:8192\n\t"
        "ds_read_b64_tr_b16 v[14:15], v99 offset:8704\n\t"
        "ds_read_b64_tr_b16 a[16:17], v100 offset:0\n\t"
        "ds_read_b64_tr_b16 a[18:19], v100 offset:512\n\t"
        "ds_read_b64_tr_b16 a[20:21], v100 offset:8192\n\t"
        "ds_read_b64_tr_b16 a[22:23], v100 offset:8704\n\t"
        "ds_read_b64_tr_b16 a[24:25], v101 offset:0\n\t"
        "ds_read_b64_tr_b16 a[26:27], v101 offset:512\n\t"
        "ds_read_b64_tr_b16 a[28:29], v101 offset:8192\n\t"
        "ds_read_b64_tr_b16 a[30:31], v101 offset:8704\n\t"
        "ds_read_b64_tr_b16 a[32:33], v102 offset:0\n\t"
        "ds_read_b64_tr_b16 a[34:35], v102 offset:512\n\t"
        "ds_read_b64_tr_b16 a[36:37], v102 offset:8192\n\t"
        "ds_read_b64_tr_b16 a[38:39], v102 offset:8704\n\t"
        "ds_read_b64_tr_b16 a[40:41], v103 offset:0\n\t"
        "ds_read_b64_tr_b16 a[42:43], v103 offset:512\n\t"
        "ds_read_b64_tr_b16 a[44:45], v103 offset:8192\n\t"
        "ds_read_b64_tr_b16 a[46:47], v103 offset:8704\n\t"
        "ds_read_b64_tr_b16 a[48:49], v104 offset:0\n\t"
        "ds_read_b64_tr_b16 a[50:51], v104 offset:512\n\t"
        "ds_read_b64_tr_b16 a[52:53], v104 offset:8192\n\t"
        "ds_read_b64_tr_b16 a[54:55], v104 offset:8704\n\t"
        "ds_read_b64_tr_b16 a[56:57], v105 offset:0\n\t"
        "ds_read_b64_tr_b16 a[58:59], v105 offset:512\n\t"
        "ds_read_b64_tr_b16 a[60:61], v105 offset:8192\n\t"
        "ds_read_b64_tr_b16 a[62:63], v105 offset:8704\n\t"
        "s_waitcnt lgkmcnt(0)\n\t"
        "s_barrier\n\t"
        "s_add_u32 s80, %[m0b], 0\n\t"
        "s_mov_b32 s81, 2097152\n\t"
        "s_mov_b32 s82, 4\n\t"
        ".Ld2%=:\n\t"
        "s_mov_b32 m0, s80\n\t"
        "s_add_u32 s80, s80, 0x8000\n\t"
        "buffer_load_dwordx4 v96, %[sq], s81 offen lds\n\t"
        "s_add_u32 m0, m0, 0x1000\n\t"
        "s_add_u32 s81, s81, 0x20000\n\t"
        "buffer_load_dwordx4 v96, %[sq], s81 offen lds\n\t"
        "s_add_u32 m0, m0, 0x1000\n\t"
        "s_add_u32 s81, s81, 0x20000\n\t"
        "buffer_load_dwordx4 v96, %[sq], s81 offen lds\n\t"
        "s_add_u32 m0, m0, 0x1000\n\t"
        "s_add_u32 s81, s81, 0x20000\n\t"
        "buffer_load_dwordx4 v96, %[sq], s81 offen lds\n\t"
        "s_add_u32 m0, m0, 0x1000\n\t"
        "s_add_u32 s81, s81, 0x20000\n\t"
        "s_sub_u32 s82, s82, 1\n\t"
        "s_cmp_lg_u32 s82, 0\n\t"
        "s_cbranch_scc1 .Ld2%=\n\t"
        "s_waitcnt vmcnt(16)\n\t"
        "s_barrier\n\t"
        "ds_read_b64_tr_b16 v[16:17], v98 offset:16384\n\t"
        "ds_read_b64_tr_b16 v[18:19], v98 offset:16896\n\t"
        "ds_read_b64_tr_b16 v[20:21], v98 offset:24576\n\t"
        "ds_read_b64_tr_b16 v[22:23], v98 offset:25088\n\t"
        "ds_read_b64_tr_b16 v[24:25], v99 offset:16384\n\t"
        "ds_read_b64_tr_b16 v[26:27], v99 offset:16896\n\t"
        "ds_read_b64_tr_b16 v[28:29], v99 offset:24576\n\t"
        "ds_read_b64_tr_b16 v[30:31], v99 offset:25088\n\t"
        "ds_read_b64_tr_b16 a[80:81], v100 offset:16384\n\t"
        "ds_read_b64_tr_b16 a[82:83], v100 offset:16896\n\t"
        "ds_read_b64_tr_b16 a[84:85], v100 offset:24576\n\t"
        "ds_read_b64_tr_b16 a[86:87], v100 offset:25088\n\t"
        "ds_read_b64_tr_b16 a[88:89], v101 offset:16384\n\t"
        "ds_read_b64_tr_b16 a[90:91], v101 offset:16896\n\t"
        "ds_read_b64_tr_b16 a[92:93], v101 offset:24576\n\t"
        "ds_read_b64_tr_b16 a[94:95], v101 offset:25088\n\t"
        "ds_read_b64_tr_b16 a[96:97], v102 offset:16384\n\t"
        "ds_read_b64_tr_b16 a[98:99], v102 offset:16896\n\t"
        "ds_read_b64_tr_b16 a[100:101], v102 offset:24576\n\t"
        "ds_read_b64_tr_b16 a[102:103], v102 offset:25088\n\t"
        "ds_read_b64_tr_b16 a[104:105], v103 offset:16384\n\t"
        "ds_read_b64_tr_b16 a[106:107], v103 offset:16896\n\t"
        "ds_read_b64_tr_b16 a[108:109], v103 offset:24576\n\t"
        "ds_read_b64_tr_b16 a[110:111], v103 offset:25088\n\t"
        "ds_read_b64_tr_b16 a[112:113], v104 offset:16384\n\t"
        "ds_read_b64_tr_b16 a[114:115], v104 offset:16896\n\t"
        "ds_read_b64_tr_b16 a[116:117], v104 offset:24576\n\t"
        "ds_read_b64_tr_b16 a[118:119], v104 offset:25088\n\t"
        "ds_read_b64_tr_b16 a[120:121], v105 offset:16384\n\t"
        "ds_read_b64_tr_b16 a[122:123], v105 offset:16896\n\t"
        "ds_read_b64_tr_b16 a[124:125], v105 offset:24576\n\t"
        "ds_read_b64_tr_b16 a[126:127], v105 offset:25088\n\t"
        "s_waitcnt lgkmcnt(0)\n\t"
        "s_barrier\n\t"
        "s_add_u32 s80, %[m0b], 16384\n\t"
        "s_mov_b32 s81, 2097152\n\t"
        "s_mov_b32 s82, 4\n\t"
        ".Ld3%=:\n\t"
        "s_mov_b32 m0, s80\n\t"
        "s_add_u32 s80, s80, 0x8000\n\t"
        "buffer_load_dwordx4 v96, %[sk], s81 offen lds\n\t"
        "s_add_u32 m0, m0, 0x1000\n\t"
        "s_add_u32 s81, s81, 0x20000\n\t"
        "buffer_load_dwordx4 v96, %[sk], s81 offen lds\n\t"
        "s_add_u32 m0, m0, 0x1000\n\t"
        "s_add_u32 s81, s81, 0x20000\n\t"
        "buffer_load_dwordx4 v96, %[sk], s81 offen lds\n\t"
        "s_add_u32 m0, m0, 0x1000\n\t"
        "s_add_u32 s81, s81, 0x20000\n\t"
        "buffer_load_dwordx4 v96, %[sk], s81 offen lds\n\t"
        "s_add_u32 m0, m0, 0x1000\n\t"
        "s_add_u32 s81, s81, 0x20000\n\t"
        "s_sub_u32 s82, s82, 1\n\t"
        "s_cmp_lg_u32 s82, 0\n\t"
        "s_cbranch_scc1 .Ld3%=\n\t"
        "s_waitcnt vmcnt(16)\n\t"
        "s_barrier\n\t"
        "ds_read_b64_tr_b16 v[32:33], v98 offset:0\n\t"
        "ds_read_b64_tr_b16 v[34:35], v98 offset:512\n\t"
        "ds_read_b64_tr_b16 v[36:37], v98 offset:8192\n\t"
        "ds_read_b64_tr_b16 v[38:39], v98 offset:8704\n\t"
        "ds_read_b64_tr_b16 v[40:41], v99 offset:0\n\t"
        "ds_read_b64_tr_b16 v[42:43], v99 offset:512\n\t"
        "ds_read_b64_tr_b16 v[44:45], v99 offset:8192\n\t"
        "ds_read_b64_tr_b16 v[46:47], v99 offset:8704\n\t"
        "ds_read_b64_tr_b16 a[144:145], v100 offset:0\n\t"
        "ds_read_b64_tr_b16 a[146:147], v100 offset:512\n\t"
        "ds_read_b64_tr_b16 a[148:149], v100 offset:8192\n\t"
        "ds_read_b64_tr_b16 a[150:151], v100 offset:8704\n\t"
        "ds_read_b64_tr_b16 a[152:153], v101 offset:0\n\t"
        "ds_read_b64_tr_b16 a[154:155], v101 offset:512\n\t"
        "ds_read_b64_tr_b16 a[156:157], v101 offset:8192\n\t"
        "ds_read_b64_tr_b16 a[158:159], v101 offset:8704\n\t"
        "ds_read_b64_tr_b16 a[160:161], v102 offset:0\n\t"
        "ds_read_b64_tr_b16 a[162:163], v102 offset:512\n\t"
        "ds_read_b64_tr_b16 a[164:165], v102 offset:8192\n\t"
        "ds_read_b64_tr_b16 a[166:167], v102 offset:8704\n\t"
        "ds_read_b64_tr_b16 a[168:169], v103 offset:0\n\t"
        "ds_read_b64_tr_b16 a[170:171], v103 offset:512\n\t"
        "ds_read_b64_tr_b16 a[172:173], v103 offset:8192\n\t"
        "ds_read_b64_tr_b16 a[174:175], v103 offset:8704\n\t"
        "ds_read_b64_tr_b16 a[176:177], v104 offset:0\n\t"
        "ds_read_b64_tr_b16 a[178:179], v104 offset:512\n\t"
        "ds_read_b64_tr_b16 a[180:181], v104 offset:8192\n\t"
        "ds_read_b64_tr_b16 a[182:183], v104 offset:8704\n\t"
        "ds_read_b64_tr_b16 a[184:185], v105 offset:0\n\t"
        "ds_read_b64_tr_b16 a[186:187], v105 offset:512\n\t"
        "ds_read_b64_tr_b16 a[188:189], v105 offset:8192\n\t"
        "ds_read_b64_tr_b16 a[190:191], v105 offset:8704\n\t"
        "s_waitcnt lgkmcnt(0)\n\t"
        "s_barrier\n\t"
        "s_add_u32 s80, %[m0b], 0\n\t"
        "s_mov_b32 s81, 4194304\n\t"
        "s_mov_b32 s82, 4\n\t"
        ".Ld4%=:\n\t"
        "s_mov_b32 m0, s80\n\t"
        "s_add_u32 s80, s80, 0x8000\n\t"
        "buffer_load_dwordx4 v96, %[sq], s81 offen lds\n\t"
        "s_add_u32 m0, m0, 0x1000\n\t"
        "s_add_u32 s81, s81, 0x20000\n\t"
        "buffer_load_dwordx4 v96, %[sq], s81 offen lds\n\t"
        "s_add_u32 m0, m0, 0x1000\n\t"
        "s_add_u32 s81, s81, 0x20000\n\t"
        "buffer_load_dwordx4 v96, %[sq], s81 offen lds\n\t"
        "s_add_u32 m0, m0, 0x1000\n\t"
        "s_add_u32 s81, s81, 0x20000\n\t"
        "buffer_load_dwordx4 v96, %[sq], s81 offen lds\n\t"
        "s_add_u32 m0, m0, 0x1000\n\t"
        "s_add_u32 s81, s81, 0x20000\n\t"
        "s_sub_u32 s82, s82, 1\n\t"
        "s_cmp_lg_u32 s82, 0\n\t"
        "s_cbranch_scc1 .Ld4%=\n\t"
        "s_waitcnt vmcnt(16)\n\t"
        "s_barrier\n\t"
        "ds_read_b64_tr_b16 v[48:49], v98 offset:16384\n\t"
        "ds_read_b64_tr_b16 v[50:51], v98 offset:16896\n\t"
        "ds_read_b64_tr_b16 v[52:53], v98 offset:24576\n\t"
        "ds_read_b64_tr_b16 v[54:55], v98 offset:25088\n\t"
        "ds_read_b64_tr_b16 v[56:57], v99 offset:16384\n\t"
        "ds_read_b64_tr_b16 v[58:59], v99 offset:16896\n\t"
        "ds_read_b64_tr_b16 v[60:61], v99 offset:24576\n\t"
        "ds_read_b64_tr_b16 v[62:63], v99 offset:25088\n\t"
        "ds_read_b64_tr_b16 a[208:209], v100 offset:16384\n\t"
        "ds_read_b64_tr_b16 a[210:211], v100 offset:16896\n\t"
        "ds_read_b64_tr_b16 a[212:213], v100 offset:24576\n\t"
        "ds_read_b64_tr_b16 a[214:215], v100 offset:25088\n\t"
        "ds_read_b64_tr_b16 a[216:217], v101 offset:16384\n\t"
        "ds_read_b64_tr_b16 a[218:219], v101 offset:16896\n\t"
        "ds_read_b64_tr_b16 a[220:221], v101 offset:24576\n\t"
        "ds_read_b64_tr_b16 a[222:223], v101 offset:25088\n\t"
        "ds_read_b64_tr_b16 a[224:225], v102 offset:16384\n\t"
        "ds_read_b64_tr_b16 a[226:227], v102 offset:16896\n\t"
        "ds_read_b64_tr_b16 a[228:229], v102 offset:24576\n\t"
        "ds_read_b64_tr_b16 a[230:231], v102 offset:25088\n\t"
        "ds_read_b64_tr_b16 a[232:233], v103 offset:16384\n\t"
        "ds_read_b64_tr_b16 a[234:235], v103 offset:16896\n\t"
        "ds_read_b64_tr_b16 a[236:237], v103 offset:24576\n\t"
        "ds_read_b64_tr_b16 a[238:239], v103 offset:25088\n\t"
        "ds_read_b64_tr_b16 a[240:241], v104 offset:16384\n\t"
        "ds_read_b64_tr_b16 a[242:243], v104 offset:16896\n\t"
        "ds_read_b64_tr_b16 a[244:245], v104 offset:24576\n\t"
        "ds_read_b64_tr_b16 a[246:247], v104 offset:25088\n\t"
        "ds_read_b64_tr_b16 a[248:249], v105 offset:16384\n\t"
        "ds_read_b64_tr_b16 a[250:251], v105 offset:16896\n\t"
        "ds_read_b64_tr_b16 a[252:253], v105 offset:24576\n\t"
        "ds_read_b64_tr_b16 a[254:255], v105 offset:25088\n\t"
        "s_waitcnt lgkmcnt(0)\n\t"
        "s_barrier\n\t"
        "s_add_u32 s80, %[m0b], 16384\n\t"
        "s_mov_b32 s81, 4194304\n\t"
        "s_mov_b32 s82, 4\n\t"
        ".Ld5%=:\n\t"
        "s_mov_b32 m0, s80\n\t"
        "s_add_u32 s80, s80, 0x8000\n\t"
        "buffer_load_dwordx4 v96, %[sk], s81 offen lds\n\t"
        "s_add_u32 m0, m0, 0x1000\n\t"
        "s_add_u32 s81, s81, 0x20000\n\t"
        "buffer_load_dwordx4 v96, %[sk], s81 offen lds\n\t"
        "s_add_u32 m0, m0, 0x1000\n\t"
        "s_add_u32 s81, s81, 0x20000\n\t"
        "buffer_load_dwordx4 v96, %[sk], s81 offen lds\n\t"
        "s_add_u32 m0, m0, 0x1000\n\t"
        "s_add_u32 s81, s81, 0x20000\n\t"
        "buffer_load_dwordx4 v96, %[sk], s81 offen lds\n\t"
        "s_add_u32 m0, m0, 0x1000\n\t"
        "s_add_u32 s81, s81, 0x20000\n\t"
        "s_sub_u32 s82, s82, 1\n\t"
        "s_cmp_lg_u32 s82, 0\n\t"
        "s_cbranch_scc1 .Ld5%=\n\t"
        "s_waitcnt vmcnt(16)\n\t"
        "s_barrier\n\t"
        "ds_read_b64_tr_b16 v[128:129], v98 offset:0\n\t"
        "ds_read_b64_tr_b16 v[130:131], v98 offset:512\n\t"
        "ds_read_b64_tr_b16 v[132:133], v98 offset:8192\n\t"
        "ds_read_b64_tr_b16 v[134:135], v98 offset:8704\n\t"
        "ds_read_b64_tr_b16 v[136:137], v99 offset:0\n\t"
        "ds_read_b64_tr_b16 v[138:139], v99 offset:512\n\t"
        "ds_read_b64_tr_b16 v[140:141], v99 offset:8192\n\t"
        "ds_read_b64_tr_b16 v[142:143], v99 offset:8704\n\t"
        "ds_read_b64_tr_b16 v[144:145], v100 offset:0\n\t"
        "ds_read_b64_tr_b16 v[146:147], v100 offset:512\n\t"
        "ds_read_b64_tr_b16 v[148:149], v100 offset:8192\n\t"
        "ds_read_b64_tr_b16 v[150:151], v100 offset:8704\n\t"
        "ds_read_b64_tr_b16 v[152:153], v101 offset:0\n\t"
        "ds_read_b64_tr_b16 v[154:155], v101 offset:512\n\t"
        "ds_read_b64_tr_b16 v[156:157], v101 offset:8192\n\t"
        "ds_read_b64_tr_b16 v[158:159], v101 offset:8704\n\t"
        "ds_read_b64_tr_b16 v[160:161], v102 offset:0\n\t"
        "ds_read_b64_tr_b16 v[162:163], v102 offset:512\n\t"
        "ds_read_b64_tr_b16 v[164:165], v102 offset:8192\n\t"
        "ds_read_b64_tr_b16 v[166:167], v102 offset:8704\n\t"
        "ds_read_b64_tr_b16 v[168:169], v103 offset:0\n\t"
        "ds_read_b64_tr_b16 v[170:171], v103 offset:512\n\t"
        "ds_read_b64_tr_b16 v[172:173], v103 offset:8192\n\t"
        "ds_read_b64_tr_b16 v[174:175], v103 offset:8704\n\t"
        "ds_read_b64_tr_b16 v[176:177], v104 offset:0\n\t"
        "ds_read_b64_tr_b16 v[178:179], v104 offset:512\n\t"
        "ds_read_b64_tr_b16 v[180:181], v104 offset:8192\n\t"
        "ds_read_b64_tr_b16 v[182:183], v104 offset:8704\n\t"
        "ds_read_b64_tr_b16 v[184:185], v105 offset:0\n\t"
        "ds_read_b64_tr_b16 v[186:187], v105 offset:512\n\t"
        "ds_read_b64_tr_b16 v[188:189], v105 offset:8192\n\t"
        "ds_read_b64_tr_b16 v[190:191], v105 offset:8704\n\t"
        "s_waitcnt lgkmcnt(0)\n\t"
        "s_barrier\n\t"
        "s_add_u32 s80, %[m0b], 0\n\t"
        "s_mov_b32 s81, 6291456\n\t"
        "s_mov_b32 s82, 4\n\t"
        ".Ld6%=:\n\t"
        "s_mov_b32 m0, s80\n\t"
        "s_add_u32 s80, s80, 0x8000\n\t"
        "buffer_load_dwordx4 v96, %[sq], s81 offen lds\n\t"
        "s_add_u32 m0, m0, 0x1000\n\t"
        "s_add_u32 s81, s81, 0x20000\n\t"
        "buffer_load_dwordx4 v96, %[sq], s81 offen lds\n\t"
        "s_add_u32 m0, m0, 0x1000\n\t"
        "s_add_u32 s81, s81, 0x20000\n\t"
        "buffer_load_dwordx4 v96, %[sq], s81 offen lds\n\t"
        "s_add_u32 m0, m0, 0x1000\n\t"
        "s_add_u32 s81, s81, 0x20000\n\t"
        "buffer_load_dwordx4 v96, %[sq], s81 offen lds\n\t"
        "s_add_u32 m0, m0, 0x1000\n\t"
        "s_add_u32 s81, s81, 0x20000\n\t"
        "s_sub_u32 s82, s82, 1\n\t"
        "s_cmp_lg_u32 s82, 0\n\t"
        "s_cbranch_scc1 .Ld6%=\n\t"
        "s_waitcnt vmcnt(16)\n\t"
        "s_barrier\n\t"
        "ds_read_b64_tr_b16 v[192:193], v98 offset:16384\n\t"
        "ds_read_b64_tr_b16 v[194:195], v98 offset:16896\n\t"
        "ds_read_b64_tr_b16 v[196:197], v98 offset:24576\n\t"
        "ds_read_b64_tr_b16 v[198:199], v98 offset:25088\n\t"
        "ds_read_b64_tr_b16 v[200:201], v99 offset:16384\n\t"
        "ds_read_b64_tr_b16 v[202:203], v99 offset:16896\n\t"
        "ds_read_b64_tr_b16 v[204:205], v99 offset:24576\n\t"
        "ds_read_b64_tr_b16 v[206:207], v99 offset:25088\n\t"
        "ds_read_b64_tr_b16 v[208:209], v100 offset:16384\n\t"
        "ds_read_b64_tr_b16 v[210:211], v100 offset:16896\n\t"
        "ds_read_b64_tr_b16 v[212:213], v100 offset:24576\n\t"
        "ds_read_b64_tr_b16 v[214:215], v100 offset:25088\n\t"
        "ds_read_b64_tr_b16 v[216:217], v101 offset:16384\n\t"
        "ds_read_b64_tr_b16 v[218:219], v101 offset:16896\n\t"
        "ds_read_b64_tr_b16 v[220:221], v101 offset:24576\n\t"
        "ds_read_b64_tr_b16 v[222:223], v101 offset:25088\n\t"
        "ds_read_b64_tr_b16 v[224:225], v102 offset:16384\n\t"
        "ds_read_b64_tr_b16 v[226:227], v102 offset:16896\n\t"
        "ds_read_b64_tr_b16 v[228:229], v102 offset:24576\n\t"
        "ds_read_b64_tr_b16 v[230:231], v102 offset:25088\n\t"
        "ds_read_b64_tr_b16 v[232:233], v103 offset:16384\n\t"
        "ds_read_b64_tr_b16 v[234:235], v103 offset:16896\n\t"
        "ds_read_b64_tr_b16 v[236:237], v103 offset:24576\n\t"
        "ds_read_b64_tr_b16 v[238:239], v103 offset:25088\n\t"
        "ds_read_b64_tr_b16 v[240:241], v104 offset:16384\n\t"
        "ds_read_b64_tr_b16 v[242:243], v104 offset:16896\n\t"
        "ds_read_b64_tr_b16 v[244:245], v104 offset:24576\n\t"
        "ds_read_b64_tr_b16 v[246:247], v104 offset:25088\n\t"
        "ds_read_b64_tr_b16 v[248:249], v105 offset:16384\n\t"
        "ds_read_b64_tr_b16 v[250:251], v105 offset:16896\n\t"
        "ds_read_b64_tr_b16 v[252:253], v105 offset:24576\n\t"
        "ds_read_b64_tr_b16 v[254:255], v105 offset:25088\n\t"
        "s_waitcnt lgkmcnt(0)\n\t"
        "s_barrier\n\t"
        "s_add_u32 s80, %[m0b], 16384\n\t"
        "s_mov_b32 s81, 6291456\n\t"
        "s_mov_b32 s82, 4\n\t"
        ".Ld7%=:\n\t"
        "s_mov_b32 m0, s80\n\t"
        "s_add_u32 s80, s80, 0x8000\n\t"
        "buffer_load_dwordx4 v96, %[sk], s81 offen lds\n\t"
        "s_add_u32 m0, m0, 0x1000\n\t"
        "s_add_u32 s81, s81, 0x20000\n\t"
        "buffer_load_dwordx4 v96, %[sk], s81 offen lds\n\t"
        "s_add_u32 m0, m0, 0x1000\n\t"
        "s_add_u32 s81, s81, 0x20000\n\t"
        "buffer_load_dwordx4 v96, %[sk], s81 offen lds\n\t"
        "s_add_u32 m0, m0, 0x1000\n\t"
        "s_add_u32 s81, s81, 0x20000\n\t"
        "buffer_load_dwordx4 v96, %[sk], s81 offen lds\n\t"
        "s_add_u32 m0, m0, 0x1000\n\t"
        "s_add_u32 s81, s81, 0x20000\n\t"
        "s_sub_u32 s82, s82, 1\n\t"
        "s_cmp_lg_u32 s82, 0\n\t"
        "s_cbranch_scc1 .Ld7%=\n\t"
        "s_waitcnt vmcnt(16)\n\t"
        "s_barrier\n\t"
        "ds_read_b64_tr_b16 a[0:1], v98 offset:0\n\t"
        "ds_read_b64_tr_b16 a[2:3], v98 offset:512\n\t"
        "ds_read_b64_tr_b16 a[4:5], v98 offset:8192\n\t"
        "ds_read_b64_tr_b16 a[6:7], v98 offset:8704\n\t"
        "ds_read_b64_tr_b16 a[8:9], v99 offset:0\n\t"
        "ds_read_b64_tr_b16 a[10:11], v99 offset:512\n\t"
        "ds_read_b64_tr_b16 a[12:13], v99 offset:8192\n\t"
        "ds_read_b64_tr_b16 a[14:15], v99 offset:8704\n\t"
        "ds_read_b64_tr_b16 v[64:65], v100 offset:0\n\t"
        "ds_read_b64_tr_b16 v[66:67], v100 offset:512\n\t"
        "ds_read_b64_tr_b16 v[68:69], v100 offset:8192\n\t"
        "ds_read_b64_tr_b16 v[70:71], v100 offset:8704\n\t"
        "ds_read_b64_tr_b16 v[72:73], v101 offset:0\n\t"
        "ds_read_b64_tr_b16 v[74:75], v101 offset:512\n\t"
        "ds_read_b64_tr_b16 v[76:77], v101 offset:8192\n\t"
        "ds_read_b64_tr_b16 v[78:79], v101 offset:8704\n\t"
        "ds_read_b64_tr_b16 v[80:81], v102 offset:0\n\t"
        "ds_read_b64_tr_b16 v[82:83], v102 offset:512\n\t"
        "ds_read_b64_tr_b16 v[84:85], v102 offset:8192\n\t"
        "ds_read_b64_tr_b16 v[86:87], v102 offset:8704\n\t"
        "ds_read_b64_tr_b16 v[88:89], v103 offset:0\n\t"
        "ds_read_b64_tr_b16 v[90:91], v103 offset:512\n\t"
        "ds_read_b64_tr_b16 v[92:93], v103 offset:8192\n\t"
        "ds_read_b64_tr_b16 v[94:95], v103 offset:8704\n\t"
        "ds_read_b64_tr_b16 a[128:129], v104 offset:0\n\t"
        "ds_read_b64_tr_b16 a[130:131], v104 offset:512\n\t"
        "ds_read_b64_tr_b16 a[132:133], v104 offset:8192\n\t"
        "ds_read_b64_tr_b16 a[134:135], v104 offset:8704\n\t"
        "ds_read_b64_tr_b16 a[136:137], v105 offset:0\n\t"
        "ds_read_b64_tr_b16 a[138:139], v105 offset:512\n\t"
        "ds_read_b64_tr_b16 a[140:141], v105 offset:8192\n\t"
        "ds_read_b64_tr_b16 a[142:143], v105 offset:8704\n\t"
        "s_waitcnt lgkmcnt(0)\n\t"
        "s_barrier\n\t"
        "ds_write_b128 v121, v[0:3] offset:0\n\t"
        "ds_write_b128 v121, v[4:7] offset:1024\n\t"
        "ds_write_b128 v121, v[8:11] offset:2048\n\t"
        "ds_write_b128 v121, v[12:15] offset:3072\n\t"
        "ds_write_b128 v121, v[16:19] offset:4096\n\t"
        "ds_write_b128 v121, v[20:23] offset:5120\n\t"
        "ds_write_b128 v121, v[24:27] offset:6144\n\t"
        "ds_write_b128 v121, v[28:31] offset:7168\n\t"
        "ds_write_b128 v121, v[32:35] offset:8192\n\t"
        "ds_write_b128 v121, v[36:39] offset:9216\n\t"
        "ds_write_b128 v121, v[40:43] offset:10240\n\t"
        "ds_write_b128 v121, v[44:47] offset:11264\n\t"
        "ds_write_b128 v121, v[48:51] offset:12288\n\t"
        "ds_write_b128 v121, v[52:55] offset:13312\n\t"
        "ds_write_b128 v121, v[56:59] offset:14336\n\t"
        "ds_write_b128 v121, v[60:63] offset:15360\n\t"
        "s_waitcnt lgkmcnt(0)\n\t"
        "s_mov_b32 s64, %[kf]\n\t"
        "s_mov_b32 s36, 0xf0000000\n\t"
        "buffer_load_dwordx4 v[116:119], v120, %[wsd], %[cal] offen sc1\n\t"
        "buffer_load_dwordx2 v[114:115], off, %[wsd], s64 sc1\n\t"
        "s_waitcnt vmcnt(2)\n\t"
        "s_barrier\n\t"
        "ds_read_b64_tr_b16 a[64:65], v98 offset:16384\n\t"
        "ds_read_b64_tr_b16 a[66:67], v98 offset:16896\n\t"
        "ds_read_b64_tr_b16 a[68:69], v98 offset:24576\n\t"
        "ds_read_b64_tr_b16 a[70:71], v98 offset:25088\n\t"
        "ds_read_b64_tr_b16 a[72:73], v99 offset:16384\n\t"
        "ds_read_b64_tr_b16 a[74:75], v99 offset:16896\n\t"
        "ds_read_b64_tr_b16 a[76:77], v99 offset:24576\n\t"
        "ds_read_b64_tr_b16 a[78:79], v99 offset:25088\n\t"
        "ds_read_b64_tr_b16 v[0:1], v100 offset:16384\n\t"
        "ds_read_b64_tr_b16 v[2:3], v100 offset:16896\n\t"
        "ds_read_b64_tr_b16 v[4:5], v100 offset:24576\n\t"
        "ds_read_b64_tr_b16 v[6:7], v100 offset:25088\n\t"
        "ds_read_b64_tr_b16 v[8:9], v101 offset:16384\n\t"
        "ds_read_b64_tr_b16 v[10:11], v101 offset:16896\n\t"
        "ds_read_b64_tr_b16 v[12:13], v101 offset:24576\n\t"
        "ds_read_b64_tr_b16 v[14:15], v101 offset:25088\n\t"
        "ds_read_b64_tr_b16 v[16:17], v102 offset:16384\n\t"
        "ds_read_b64_tr_b16 v[18:19], v102 offset:16896\n\t"
        "ds_read_b64_tr_b16 v[20:21], v102 offset:24576\n\t"
        "ds_read_b64_tr_b16 v[22:23], v102 offset:25088\n\t"
        "ds_read_b64_tr_b16 v[24:25], v103 offset:16384\n\t"
        "ds_read_b64_tr_b16 v[26:27], v103 offset:16896\n\t"
        "ds_read_b64_tr_b16 v[28:29], v103 offset:24576\n\t"
        "ds_read_b64_tr_b16 v[30:31], v103 offset:25088\n\t"
        "ds_read_b64_tr_b16 a[192:193], v104 offset:16384\n\t"
        "ds_read_b64_tr_b16 a[194:195], v104 offset:16896\n\t"
        "ds_read_b64_tr_b16 a[196:197], v104 offset:24576\n\t"
        "ds_read_b64_tr_b16 a[198:199], v104 offset:25088\n\t"
        "ds_read_b64_tr_b16 a[200:201], v105 offset:16384\n\t"
        "ds_read_b64_tr_b16 a[202:203], v105 offset:16896\n\t"
        "ds_read_b64_tr_b16 a[204:205], v105 offset:24576\n\t"
        "ds_read_b64_tr_b16 a[206:207], v105 offset:25088\n\t"
        "s_waitcnt vmcnt(0)\n\t"
        "s_mov_b32 s97, 0\n\t"
        ".Lg0%=:\n\t"
        "v_cmp_ne_u32_e32 vcc, %[tag], v116\n\t"
        "v_cmp_ne_u32_e64 s[82:83], %[tag], v118\n\t"
        "s_nop 1\n\t"
        "s_or_b64 vcc, vcc, s[82:83]\n\t"
        "s_cbranch_scc0 .Lg1%=\n\t"
        "s_sleep 4\n\t"
        "buffer_load_dwordx4 v[116:119], v120, %[wsd], %[cal] offen sc1\n\t"
        "s_add_u32 s97, s97, 1\n\t"
        "s_cmp_lt_u32 s97, 0x100000\n\t"
        "s_cbranch_scc0 .Lgu%=\n\t"
        "s_waitcnt vmcnt(0)\n\t"
        "s_branch .Lg0%=\n\t"
        ".Lg1%=:\n\t"
        "s_mov_b32 s84, 0x55555555\n\t"
        "s_mov_b32 s85, s84\n\t"
        "v_and_b32_e32 v116, 1, v117\n\t"
        "v_and_b32_e32 v118, 1, v119\n\t"
        "v_cmp_ne_u32_e32 vcc, 0, v116\n\t"
        "v_cmp_ne_u32_e64 s[82:83], 0, v118\n\t"
        "s_nop 0\n\t"
        "s_and_b64 s[82:83], s[82:83], s[84:85]\n\t"
        "s_bcnt1_i32_b64 s87, s[82:83]\n\t"
        "s_and_b64 s[82:83], vcc, s[84:85]\n\t"
        "s_bcnt1_i32_b64 s86, s[82:83]\n\t"
        "s_andn2_b64 s[82:83], vcc, s[84:85]\n\t"
        "s_bcnt1_i32_b64 s88, s[82:83]\n\t"
        "v_and_b32_e32 v116, 2, v117\n\t"
        "v_and_b32_e32 v118, 2, v119\n\t"
        "v_cmp_ne_u32_e32 vcc, 0, v116\n\t"
        "v_cmp_ne_u32_e64 s[82:83], 0, v118\n\t"
        "s_nop 0\n\t"
        "s_and_b64 s[82:83], s[82:83], s[84:85]\n\t"
        "s_bcnt1_i32_b64 s90, s[82:83]\n\t"
        "s_and_b64 s[82:83], vcc, s[84:85]\n\t"
        "s_bcnt1_i32_b64 s89, s[82:83]\n\t"
        "s_andn2_b64 s[82:83], vcc, s[84:85]\n\t"
        "s_bcnt1_i32_b64 s91, s[82:83]\n\t"
        "s_add_u32 s80, s86, s87\n\t"
        "s_add_u32 s80, s80, s88\n\t"
        "s_cmp_ge_u32 s80, 48\n\t"
        "s_cselect_b32 s80, 8, 24\n\t"
        "s_cmp_gt_u32 s86, s80\n\t"
        "s_cselect_b32 s86, %c[NX1], %c[NX0]\n\t"
        "s_cmp_gt_u32 s89, 16\n\t"
        "s_cselect_b32 s81, %c[NY1], %c[NY0]\n\t"
        "s_or_b32 s86, s86, s81\n\t"
        "s_cmp_gt_u32 s87, s80\n\t"
        "s_cselect_b32 s87, %c[NX1], %c[NX0]\n\t"
        "s_cmp_gt_u32 s90, 16\n\t"
        "s_cselect_b32 s81, %c[NY1], %c[NY0]\n\t"
        "s_or_b32 s87, s87, s81\n\t"
        "s_cmp_gt_u32 s88, s80\n\t"
        "s_cselect_b32 s88, %c[NX1], %c[NX0]\n\t"
        "s_cmp_gt_u32 s91, 16\n\t"
        "s_cselect_b32 s81, %c[NY1], %c[NY0]\n\t"
        "s_or_b32 s88, s88, s81\n\t"
        "s_xor_b32 %[nA], %[nA], s86\n\t"
        "s_xor_b32 %[nW], %[nW], s86\n\t"
        "s_xor_b32 %[nC], %[nC], s87\n\t"
        "s_xor_b32 %[nS], %[nS], s88\n\t"
        "s_xor_b32 %[nT], %[nT], s88\n\t"
        "s_xor_b32 %[b0], %[b0], s88\n\t"
        "s_cmp_eq_u32 %[w], 1\n\t"
        "s_cselect_b32 s80, s87, s86\n\t"
        "s_cmp_eq_u32 %[w], 2\n\t"
        "s_cselect_b32 s80, s88, s80\n\t"
        "s_xor_b32 %[pob], %[pob], s80\n\t"
        "s_lshl_b32 s80, %[w], 10\n\t"
        "s_sub_u32 %[nS], %[nS], s80\n\t"
        "s_sub_u32 %[pob], %[pob], s80\n\t"
        "ds_read_b32 v116, v120 offset:20484\n\t"
        "s_and_b32 s80, s87, 0x4000\n\t"
        "s_xor_b32 %[nC], %[nC], s80\n\t"
        "s_lshl_b32 s81, %[w], 8\n\t"
        "s_add_u32 s81, s81, 24576\n\t"
        "s_sub_u32 %[b0], %[b0], s81\n\t"
        "s_waitcnt lgkmcnt(0)\n\t"
        "v_xor_b32_e32 v116, s80, v116\n\t"
        "ds_write_b32 v120, v116 offset:20484\n\t"
        "s_mov_b32 s36, 0xf0000000\n\t"
        "s_mov_b32 s38, 0xff00ff00\n\t"
        "s_mov_b32 s39, 0xff00ff00\n\t"
        "s_mov_b32 s40, 0\n\t"
        "s_mov_b32 s37, 0\n\t"
        "s_brev_b32 s54, 1\n\t"
        "s_mov_b32 s46, %c[iD1]\n\t"
        "s_mov_b32 s45, %c[iD1]\n\t"
        "s_mov_b32 s47, %c[iD2]\n\t"
        "s_mov_b32 s48, 1\n\t"
        "s_mov_b32 s50, %c[LC]\n\t"
        "s_mov_b32 s51, %c[LA]\n\t"
        "s_mov_b32 s52, %c[E1]\n\t"
        "s_mov_b32 s53, %c[E2]\n\t"
        "s_mov_b32 s43, 0\n\t"
        "s_mov_b32 s61, %[b0]\n\t"
        "s_add_u32 s62, %[b0], 0x40000\n\t"
        "s_mov_b32 s75, 31744\n\t"
        "s_cmp_eq_u32 s64, s36\n\t"
        "s_cbranch_scc1 .Lk10%=\n\t"
        "s_mov_b32 s97, 0\n\t"
        ".Lk00%=:\n\t"
        "v_readfirstlane_b32 s82, v114\n\t"
        "v_readfirstlane_b32 s83, v115\n\t"
        "s_xor_b32 s82, s82, %[tag]\n\t"
        "s_xor_b32 s83, s83, %[tag]\n\t"
        "s_or_b32 s82, s82, s83\n\t"
        "s_cbranch_scc0 .Lk10%=\n\t"
        "s_sleep 3\n\t"
        "buffer_load_dwordx2 v[114:115], off, %[wsd], s64 sc1\n\t"
        "s_add_u32 s97, s97, 1\n\t"
        "s_cmp_lt_u32 s97, 0x200000\n\t"
        "s_cbranch_scc0 .Lgu%=\n\t"
        "s_waitcnt vmcnt(0)\n\t"
        "s_branch .Lk00%=\n\t"
        ".Lk10%=:\n\t"
        "s_add_u32 s80, s40, 0\n\t"
        "s_min_u32 s80, s80, 63\n\t"
        "s_lshl_b32 s80, s80, 18\n\t"
        "s_add_u32 s69, s80, %[kx]\n\t"
        "s_add_u32 s70, s69, 0x1000\n\t"
        "s_lshl_b32 s80, s40, 18\n\t"
        "s_add_u32 s71, s80, %[vx]\n\t"
        "s_add_u32 s80, s40, 1\n\t"
        "s_lshl_b32 s81, s80, 9\n\t"
        "s_add_u32 s81, s81, %[kf]\n\t"
        "s_and_b32 s80, s80, 0x43\n\t"
        "s_cselect_b32 s64, s36, s81\n\t"
        "buffer_load_dwordx4 v[104:107], v120, %[wsd], s69 offen sc1\n\t"
        "buffer_load_dwordx4 v[108:111], v120, %[wsd], s70 offen sc1\n\t"
        "s_waitcnt lgkmcnt(0)\n\t"
        "s_barrier\n\t"
        "ds_write_b128 v121, v[64:67] offset:16384\n\t"
        "ds_write_b128 v121, v[68:71] offset:17408\n\t"
        "ds_write_b128 v121, v[72:75] offset:18432\n\t"
        "ds_write_b128 v121, v[76:79] offset:19456\n\t"
        "ds_write_b128 v121, v[80:83] offset:20480\n\t"
        "ds_write_b128 v121, v[84:87] offset:21504\n\t"
        "ds_write_b128 v121, v[88:91] offset:22528\n\t"
        "ds_write_b128 v121, v[92:95] offset:23552\n\t"
        "ds_write_b128 v121, v[0:3] offset:24576\n\t"
        "ds_write_b128 v121, v[4:7] offset:25600\n\t"
        "ds_write_b128 v121, v[8:11] offset:26624\n\t"
        "ds_write_b128 v121, v[12:15] offset:27648\n\t"
        "ds_write_b128 v121, v[16:19] offset:28672\n\t"
        "ds_write_b128 v121, v[20:23] offset:29696\n\t"
        "ds_write_b128 v121, v[24:27] offset:30720\n\t"
        "ds_write_b128 v121, v[28:31] offset:31744\n\t"
        "s_waitcnt lgkmcnt(0)\n\t"
        "rec_s_code:\n\t"
        "ds_read_b128 v[16:19], v121 offset:0\n\t"
        "ds_read_b128 v[20:23], v121 offset:1024\n\t"
        "ds_read_b128 v[24:27], v121 offset:2048\n\t"
        "ds_read_b128 v[28:31], v121 offset:3072\n\t"
        "ds_read_b128 v[32:35], v121 offset:4096\n\t"
        "ds_read_b128 v[36:39], v121 offset:5120\n\t"
        "ds_read_b128 v[40:43], v121 offset:6144\n\t"
        "ds_read_b128 v[44:47], v121 offset:7168\n\t"
        "ds_read_b128 v[56:59], v121 offset:8192\n\t"
        "ds_read_b128 v[60:63], v121 offset:9216\n\t"
        "ds_read_b128 v[64:67], v121 offset:10240\n\t"
        "ds_read_b128 v[68:71], v121 offset:11264\n\t"
        "ds_read_b128 v[72:75], v121 offset:12288\n\t"
        "ds_read_b128 v[76:79], v121 offset:13312\n\t"
        "ds_read_b128 v[80:83], v121 offset:14336\n\t"
        "ds_read_b128 v[84:87], v121 offset:15360\n\t"
        "s_and_b32 s80, s40, 3\n\t"
        "s_lshl_b32 s80, s80, 17\n\t"
        "s_add_u32 s57, s80, %[nA]\n\t"
        "s_add_u32 s58, s57, 0x1000\n\t"
        "s_add_u32 s59, s57, 0x8000\n\t"
        "s_add_u32 s60, s57, 0x9000\n\t"
        "s_and_b32 s80, s40, 3\n\t"
        "s_lshl_b32 s80, s80, 17\n\t"
        "s_add_u32 s63, s80, %[nC]\n\t"
        "s_add_u32 s66, s80, %[nS]\n\t"
        "s_add_u32 s81, s80, 0x20000\n\t"
        "s_and_b32 s81, s81, 0x60000\n\t"
        "s_add_u32 s67, s81, %[nW]\n\t"
        "s_add_u32 s79, s81, %[nT]\n\t"
        "s_cmp_eq_u32 %[w], 2\n\t"
        "s_cselect_b32 s81, s80, s81\n\t"
        "s_xor_b32 s81, s81, 0x40000\n\t"
        "s_add_u32 s68, s81, %[pob]\n\t"
        "s_lshl_b32 s72, s40, 17\n\t"
        "s_add_u32 s72, s72, %[kvo]\n\t"
        "s_add_u32 s80, s40, 1\n\t"
        "s_min_u32 s80, s80, 63\n\t"
        "s_lshl_b32 s80, s80, 18\n\t"
        "s_add_u32 s69, s80, %[kx]\n\t"
        "s_add_u32 s70, s69, 0x1000\n\t"
        "s_lshl_b32 s80, s40, 18\n\t"
        "s_add_u32 s71, s80, %[vx]\n\t"
        "s_add_u32 s80, s40, 2\n\t"
        "s_lshl_b32 s81, s80, 9\n\t"
        "s_add_u32 s81, s81, %[kf]\n\t"
        "s_and_b32 s80, s80, 0x43\n\t"
        "s_cselect_b32 s64, s36, s81\n\t"
        "v_mov_b32_e32 v88, 0\n\t"
        "v_mov_b32_e32 v89, 0\n\t"
        "v_mov_b32_e32 v90, 0\n\t"
        "v_mov_b32_e32 v91, 0\n\t"
        "v_mov_b32_e32 v92, 0\n\t"
        "v_mov_b32_e32 v93, 0\n\t"
        "v_mov_b32_e32 v94, 0\n\t"
        "v_mov_b32_e32 v95, 0\n\t"
        "v_mov_b32_e32 v96, 0\n\t"
        "v_mov_b32_e32 v97, 0\n\t"
        "v_mov_b32_e32 v98, 0\n\t"
        "v_mov_b32_e32 v99, 0\n\t"
        "v_mov_b32_e32 v100, 0\n\t"
        "v_mov_b32_e32 v101, 0\n\t"
        "v_mov_b32_e32 v102, 0\n\t"
        "v_mov_b32_e32 v103, 0\n\t"
        "v_mov_b32_e32 v52, v112\n\t"
        "v_mov_b32_e32 v53, v113\n\t"
        "v_mov_b32_e32 v49, 0\n\t"
        "v_lshlrev_b32_e32 v51, 6, v120\n\t"
        "s_cmp_eq_u32 %[w], 0\n\t"
        "s_cselect_b32 s73, %[nT], s36\n\t"
        "s_cselect_b32 s74, %[pra], s36\n\t"
        "v_mov_b32_e32 v50, s40\n\t"
        "s_cmp_ge_u32 s40, 60\n\t"
        "s_cselect_b32 s65, s74, s36\n\t"
        "s_waitcnt lgkmcnt(0)\n\t"
        "s_barrier\n\t"
        "s_memrealtime s[98:99]\n\t"
        "s_waitcnt lgkmcnt(0)\n\t"
        "s_mov_b32 s41, s98\n\t"
        "v_mov_b32_e32 v48, s98\n\t"
        ".p2align 6\n\t"
        ".Lloop%=:\n\t"
        "s_mov_b32 s97, 0\n\t"
        "s_movk_i32 s96, 0x1000\n\t"
        ".Lwa%=:\n\t"
        "s_memrealtime s[98:99]\n\t"
        "s_addk_i32 s96, -1\n\t"
        "s_cmp_lt_i32 s96, 0\n\t"
        "s_cbranch_scc1 .Lta%=\n\t"
        "s_waitcnt lgkmcnt(0)\n\t"
        "s_sub_i32 s98, s98, s41\n\t"
        "s_cmp_lt_i32 s98, %c[iFT]\n\t"
        "s_cbranch_scc1 .Lwa%=\n\t"
        "s_cmp_ge_i32 s98, 0\n\t"
        "s_cbranch_scc1 .Lxa%=\n\t"
        ".Lqa%=:\n\t"
        "s_nop %c[iFN]\n\t"
        "s_addk_i32 s98, 1\n\t"
        "s_cmp_lt_i32 s98, 0\n\t"
        "s_cbranch_scc1 .Lqa%=\n\t"
        ".Lxa%=:\n\t"
        "buffer_load_dwordx4 v[0:3], v120, %[srd], s57 offen sc1\n\t"
        "s_nop 7\n\t"
        "buffer_load_dwordx4 v[4:7], v120, %[srd], s58 offen sc1\n\t"
        "s_nop 7\n\t"
        "buffer_load_dwordx4 v[8:11], v120, %[srd], s59 offen sc1\n\t"
        "s_nop 7\n\t"
        "buffer_load_dwordx4 v[12:15], v120, %[srd], s60 offen sc1\n\t"
        "s_nop 15\n\t"
        "s_nop 15\n\t"
        "s_nop 3\n\t"
        "s_mov_b64 exec, 7\n\t"
        "buffer_store_dwordx2 v[48:49], v51, %[srd], s73 offen sc1\n\t"
        "s_mov_b64 exec, -1\n\t"
        "buffer_load_dwordx4 v[112:115], v120, %[wsd], s71 offen offset:0 sc1\n\t"
        "buffer_load_dwordx4 v[116:119], v120, %[wsd], s71 offen offset:1024 sc1\n\t"
        ".Lgo%=:\n\t"
        "s_waitcnt vmcnt(6)\n\t"
        "v_smfmac_f32_16x16x64_bf16 v[88:91], v[0:3], v[16:23], v122\n\t"
        "ds_write_b128 v120, v[0:3] offset:0\n\t"
        "v_smfmac_f32_16x16x64_bf16 v[92:95], v[0:3], v[24:31], v122\n\t"
        "v_smfmac_f32_16x16x64_bf16 v[96:99], v[0:3], v[32:39], v122\n\t"
        "v_smfmac_f32_16x16x64_bf16 v[100:103], v[0:3], v[40:47], v122\n\t"
        "s_waitcnt vmcnt(5)\n\t"
        "v_smfmac_f32_16x16x64_bf16 v[88:91], v[4:7], v[56:63], v122\n\t"
        "ds_write_b128 v120, v[4:7] offset:4096\n\t"
        "v_smfmac_f32_16x16x64_bf16 v[92:95], v[4:7], v[64:71], v122\n\t"
        "v_smfmac_f32_16x16x64_bf16 v[96:99], v[4:7], v[72:79], v122\n\t"
        "v_smfmac_f32_16x16x64_bf16 v[100:103], v[4:7], v[80:87], v122\n\t"
        "s_waitcnt vmcnt(4)\n\t"
        "v_smfmac_f32_16x16x64_bf16 v[88:91], v[8:11], v[128:135], v122\n\t"
        "ds_write_b128 v120, v[8:11] offset:8192\n\t"
        "v_smfmac_f32_16x16x64_bf16 v[92:95], v[8:11], v[136:143], v122\n\t"
        "s_waitcnt lgkmcnt(1)\n\t"
        "s_barrier\n\t"
        "v_smfmac_f32_16x16x64_bf16 v[96:99], v[8:11], v[192:199], v122\n\t"
        "ds_read_b128 v[16:19], v124 offset:0\n\t"
        "v_smfmac_f32_16x16x64_bf16 v[100:103], v[8:11], v[200:207], v122\n\t"
        "ds_read_b128 v[20:23], v125 offset:0\n\t"
        "s_waitcnt vmcnt(3)\n\t"
        "v_smfmac_f32_16x16x64_bf16 v[88:91], v[12:15], a[0:7], v122\n\t"
        "ds_write_b128 v120, v[12:15] offset:12288\n\t"
        "v_smfmac_f32_16x16x64_bf16 v[92:95], v[12:15], a[8:15], v122\n\t"
        "buffer_store_dword v50, off, %[wsd], s65 sc1\n\t"
        "v_smfmac_f32_16x16x64_bf16 v[96:99], v[12:15], a[64:71], v122\n\t"
        "ds_read_b128 v[24:27], v126 offset:0\n\t"
        "v_smfmac_f32_16x16x64_bf16 v[100:103], v[12:15], a[72:79], v122\n\t"
        "ds_read_b128 v[28:31], v124 offset:4096\n\t"
        "s_waitcnt lgkmcnt(4)\n\t"
        "v_smfmac_f32_16x16x64_bf16 v[88:91], v[16:19], a[16:23], v122\n\t"
        "ds_read_b128 v[32:35], v125 offset:4096\n\t"
        "v_smfmac_f32_16x16x64_bf16 v[92:95], v[16:19], a[24:31], v122\n\t"
        "ds_read_b128 v[36:39], v126 offset:4096\n\t"
        "s_waitcnt lgkmcnt(5)\n\t"
        "v_smfmac_f32_16x16x64_bf16 v[88:91], v[20:23], a[32:39], v122\n\t"
        "ds_read_b128 v[72:75], v121 offset:16384\n\t"
        "v_smfmac_f32_16x16x64_bf16 v[92:95], v[20:23], a[40:47], v122\n\t"
        "s_waitcnt lgkmcnt(5)\n\t"
        "s_barrier\n\t"
        "ds_read_b128 v[76:79], v121 offset:17408\n\t"
        "s_waitcnt lgkmcnt(5)\n\t"
        "v_smfmac_f32_16x16x64_bf16 v[88:91], v[24:27], a[48:55], v122\n\t"
        "ds_read_b128 v[0:3], v124 offset:12288\n\t"
        "v_smfmac_f32_16x16x64_bf16 v[92:95], v[24:27], a[56:63], v122\n\t"
        "ds_read_b128 v[80:83], v121 offset:18432\n\t"
        "s_waitcnt lgkmcnt(6)\n\t"
        "v_smfmac_f32_16x16x64_bf16 v[88:91], v[28:31], a[144:151], v122\n\t"
        "ds_read_b128 v[84:87], v121 offset:19456\n\t"
        "v_smfmac_f32_16x16x64_bf16 v[92:95], v[28:31], a[152:159], v122\n\t"
        "ds_read_b128 v[40:43], v124 offset:8192\n\t"
        "s_waitcnt lgkmcnt(7)\n\t"
        "v_smfmac_f32_16x16x64_bf16 v[88:91], v[32:35], a[160:167], v122\n\t"
        "ds_read_b128 v[44:47], v125 offset:8192\n\t"
        "v_mov_b32_e32 v12, 0x3f803f80\n\t"
        "v_smfmac_f32_16x16x64_bf16 v[92:95], v[32:35], a[168:175], v122\n\t"
        "ds_read_b128 v[56:59], v121 offset:20480\n\t"
        "v_mov_b32_e32 v13, 0x3f803f80\n\t"
        "s_waitcnt lgkmcnt(8)\n\t"
        "v_smfmac_f32_16x16x64_bf16 v[88:91], v[36:39], a[176:183], v122\n\t"
        "ds_read_b128 v[60:63], v121 offset:21504\n\t"
        "v_mov_b32_e32 v14, 0x3f803f80\n\t"
        "v_smfmac_f32_16x16x64_bf16 v[92:95], v[36:39], a[184:191], v122\n\t"
        "ds_read_b128 v[4:7], v125 offset:12288\n\t"
        "v_mov_b32_e32 v15, 0x3f803f80\n\t"
        "s_waitcnt lgkmcnt(8)\n\t"
        "s_waitcnt lgkmcnt(7)\n\t"
        "v_smfmac_f32_16x16x64_bf16 v[88:91], v[0:3], v[72:79], v122\n\t"
        "ds_read_b128 v[64:67], v121 offset:22528\n\t"
        "s_waitcnt lgkmcnt(6)\n\t"
        "v_smfmac_f32_16x16x64_bf16 v[92:95], v[0:3], v[80:87], v122\n\t"
        "ds_read_b128 v[68:71], v121 offset:23552\n\t"
        "s_waitcnt lgkmcnt(6)\n\t"
        "v_smfmac_f32_16x16x64_bf16 v[88:91], v[40:43], v[144:151], v122\n\t"
        "ds_read_b128 v[48:51], v126 offset:8192\n\t"
        "v_smfmac_f32_16x16x64_bf16 v[92:95], v[40:43], v[152:159], v122\n\t"
        "ds_read_b128 v[8:11], v126 offset:12288\n\t"
        "s_waitcnt lgkmcnt(7)\n\t"
        "v_smfmac_f32_16x16x64_bf16 v[88:91], v[44:47], v[160:167], v122\n\t"
        "ds_read_b128 v[72:75], v121 offset:24576\n\t"
        "v_smfmac_f32_16x16x64_bf16 v[92:95], v[44:47], v[168:175], v122\n\t"
        "ds_read_b128 v[76:79], v121 offset:25600\n\t"
        "s_waitcnt lgkmcnt(7)\n\t"
        "s_waitcnt lgkmcnt(6)\n\t"
        "v_smfmac_f32_16x16x64_bf16 v[88:91], v[4:7], v[56:63], v122\n\t"
        "ds_read_b128 v[80:83], v121 offset:26624\n\t"
        "s_waitcnt lgkmcnt(5)\n\t"
        "v_smfmac_f32_16x16x64_bf16 v[92:95], v[4:7], v[64:71], v122\n\t"
        "ds_read_b128 v[84:87], v121 offset:27648\n\t"
        "s_waitcnt lgkmcnt(5)\n\t"
        "v_smfmac_f32_16x16x64_bf16 v[88:91], v[48:51], v[176:183], v122\n\t"
        "v_mov_b32_e32 v64, 0\n\t"
        "v_smfmac_f32_16x16x64_bf16 v[92:95], v[48:51], v[184:191], v122\n\t"
        "v_mov_b32_e32 v65, 0\n\t"
        "s_waitcnt lgkmcnt(4)\n\t"
        "v_smfmac_f32_16x16x64_bf16 v[88:91], v[8:11], a[128:135], v122\n\t"
        "v_mov_b32_e32 v66, 0\n\t"
        "v_smfmac_f32_16x16x64_bf16 v[92:95], v[8:11], a[136:143], v122\n\t"
        "v_mov_b32_e32 v67, 0\n\t"
        "s_waitcnt lgkmcnt(2)\n\t"
        "v_smfmac_f32_16x16x64_bf16 v[96:99], v[0:3], v[72:79], v122\n\t"
        "ds_read_b128 v[72:75], v121 offset:28672\n\t"
        "s_waitcnt lgkmcnt(1)\n\t"
        "v_smfmac_f32_16x16x64_bf16 v[100:103], v[0:3], v[80:87], v122\n\t"
        "ds_read_b128 v[76:79], v121 offset:29696\n\t"
        "v_smfmac_f32_16x16x64_bf16 v[96:99], v[16:19], a[80:87], v122\n\t"
        "v_add_f32_e32 v88, v88, v90\n\t"
        "v_add_f32_e32 v89, v89, v91\n\t"
        "v_smfmac_f32_16x16x64_bf16 v[100:103], v[16:19], a[88:95], v122\n\t"
        "v_cvt_pk_bf16_f32 v90, v88, v89\n\t"
        "v_add_f32_e32 v92, v92, v94\n\t"
        "v_smfmac_f32_16x16x64_bf16 v[96:99], v[20:23], a[96:103], v122\n\t"
        "ds_write_b32 v127, v90\n\t"
        "v_add_f32_e32 v93, v93, v95\n\t"
        "v_cvt_pk_bf16_f32 v91, v92, v93\n\t"
        "v_smfmac_f32_16x16x64_bf16 v[100:103], v[20:23], a[104:111], v122\n\t"
        "ds_write_b32 v127, v91 offset:384\n\t"
        "v_cmp_u_f32_e32 vcc, v88, v89\n\t"
        "v_smfmac_f32_16x16x64_bf16 v[96:99], v[24:27], a[112:119], v122\n\t"
        "ds_read_b64_tr_b16 v[56:57], v52 offset:0\n\t"
        "ds_read_b64_tr_b16 v[58:59], v52 offset:64\n\t"
        "v_smfmac_f32_16x16x64_bf16 v[100:103], v[24:27], a[120:127], v122\n\t"
        "ds_read_b64_tr_b16 v[60:61], v53 offset:0\n\t"
        "ds_read_b64_tr_b16 v[62:63], v53 offset:64\n\t"
        "v_smfmac_f32_16x16x64_bf16 v[96:99], v[28:31], a[208:215], v122\n\t"
        "s_cbranch_vccnz .Lf1%=\n\t"
        ".Lr1%=:\n\t"
        "ds_read_b128 v[80:83], v121 offset:30720\n\t"
        "v_smfmac_f32_16x16x64_bf16 v[100:103], v[28:31], a[216:223], v122\n\t"
        "ds_read_b128 v[84:87], v121 offset:31744\n\t"
        "v_mov_b32_e32 v91, 0\n\t"
        "v_smfmac_f32_16x16x64_bf16 v[96:99], v[32:35], a[224:231], v122\n\t"
        "v_mov_b32_e32 v94, 0\n\t"
        "v_smfmac_f32_16x16x64_bf16 v[100:103], v[32:35], a[232:239], v122\n\t"
        "ds_read_b128 v[32:35], v120 offset:20480\n\t"
        "v_smfmac_f32_16x16x64_bf16 v[96:99], v[36:39], a[240:247], v122\n\t"
        "s_waitcnt lgkmcnt(3)\n\t"
        "s_barrier\n\t"
        "v_mfma_f32_16x16x32_bf16 v[68:71], v[104:107], v[56:59], 0\n\t"
        "ds_read_b64_tr_b16 v[16:17], v123 offset:0\n\t"
        "v_mfma_f32_16x16x32_bf16 v[68:71], v[108:111], v[60:63], v[68:71]\n\t"
        "ds_read_b64_tr_b16 v[18:19], v123 offset:64\n\t"
        "v_smfmac_f32_16x16x64_bf16 v[100:103], v[36:39], a[248:255], v122\n\t"
        "ds_read_b64_tr_b16 v[20:21], v123 offset:768\n\t"
        "v_smfmac_f32_16x16x64_bf16 v[96:99], v[8:11], a[192:199], v122\n\t"
        "ds_read_b64_tr_b16 v[22:23], v123 offset:832\n\t"
        "v_smfmac_f32_16x16x64_bf16 v[100:103], v[8:11], a[200:207], v122\n\t"
        "s_nop 1\n\t"
        "ds_write_b128 v120, v[68:71] offset:16384\n\t"
        "v_smfmac_f32_16x16x64_bf16 v[96:99], v[4:7], v[72:79], v122\n\t"
        "buffer_load_dword v88, v127, %[srd], s61 offen offset:0 sc1\n\t"
        "s_waitcnt lgkmcnt(6)\n\t"
        "v_smfmac_f32_16x16x64_bf16 v[100:103], v[4:7], v[80:87], v122\n\t"
        "buffer_load_dword v89, v127, %[srd], s61 offen offset:256 sc1\n\t"
        "v_smfmac_f32_16x16x64_bf16 v[96:99], v[40:43], v[208:215], v122\n\t"
        "ds_read_b64_tr_b16 v[24:25], v123 offset:1536\n\t"
        "v_smfmac_f32_16x16x64_bf16 v[100:103], v[40:43], v[216:223], v122\n\t"
        "ds_read_b64_tr_b16 v[26:27], v123 offset:1600\n\t"
        "v_smfmac_f32_16x16x64_bf16 v[96:99], v[44:47], v[224:231], v122\n\t"
        "s_waitcnt lgkmcnt(2)\n\t"
        "s_barrier\n\t"
        "ds_read_b128 v[36:39], v124 offset:16384\n\t"
        "v_smfmac_f32_16x16x64_bf16 v[100:103], v[44:47], v[232:239], v122\n\t"
        "ds_read_b128 v[56:59], v125 offset:16384\n\t"
        "v_smfmac_f32_16x16x64_bf16 v[96:99], v[48:51], v[240:247], v122\n\t"
        "ds_read_b128 v[60:63], v126 offset:16384\n\t"
        "v_smfmac_f32_16x16x64_bf16 v[100:103], v[48:51], v[248:255], v122\n\t"
        "ds_read_b64_tr_b16 v[28:29], v123 offset:2304\n\t"
        "ds_read_b64_tr_b16 v[30:31], v123 offset:2368\n\t"
        "buffer_load_dwordx4 v[84:87], v120, %[srd], s66 offen sc1\n\t"
        "s_waitcnt lgkmcnt(4)\n\t"
        "s_cmp_eq_u32 s40, 63\n\t"
        "v_add_f32_e32 v68, v68, v36\n\t"
        "v_add_f32_e32 v69, v69, v37\n\t"
        "v_add_f32_e32 v70, v70, v38\n\t"
        "v_add_f32_e32 v71, v71, v39\n\t"
        "v_add_f32_e32 v96, v96, v98\n\t"
        "v_add_f32_e32 v97, v97, v99\n\t"
        "s_waitcnt lgkmcnt(2)\n\t"
        "v_cvt_pk_bf16_f32 v50, v96, v97\n\t"
        "v_pk_add_f32 v[100:101], v[100:101], v[102:103]\n\t"
        "ds_write_b32 v35, v50\n\t"
        "v_cvt_pk_bf16_f32 v51, v100, v101\n\t"
        "ds_write_b32 v35, v51 offset:384\n\t"
        "v_pk_add_f32 v[56:57], v[56:57], v[60:61]\n\t"
        "s_cbranch_scc1 .Lend%=\n\t"
        "v_pk_add_f32 v[58:59], v[58:59], v[62:63]\n\t"
        "v_pk_add_f32 v[68:69], v[68:69], v[56:57]\n\t"
        "v_pk_add_f32 v[70:71], v[70:71], v[58:59]\n\t"
        "v_max3_f32 v2, v68, v69, v70\n\t"
        "v_max_f32_e32 v2, v2, v71\n\t"
        "s_nop 1\n\t"
        "v_max_f32_dpp v3, v2, v2 row_ror:8 row_mask:0xf bank_mask:0xf\n\t"
        "v_max_f32_dpp v1, v2, v2 row_ror:8 row_mask:0xf bank_mask:0xf\n\t"
        "s_nop 1\n\t"
        "v_permlane16_swap_b32_e32 v3, v1\n\t"
        "v_max_f32_e32 v2, v3, v1\n\t"
        "v_max_f32_e32 v3, v3, v1\n\t"
        "s_waitcnt lgkmcnt(0)\n\t"
        "s_barrier\n\t"
        "ds_read_b64_tr_b16 v[72:73], v32 offset:0\n\t"
        "v_permlane32_swap_b32_e32 v2, v3\n\t"
        "ds_read_b64_tr_b16 v[74:75], v32 offset:128\n\t"
        "v_max_f32_e32 v0, v2, v3\n\t"
        "ds_read_b64_tr_b16 v[76:77], v32 offset:1536\n\t"
        "v_mul_f32_e32 v2, 0xbe0293ee, v0\n\t"
        "ds_read_b64_tr_b16 v[78:79], v32 offset:1664\n\t"
        "v_fmamk_f32 v68, v68, 0x3e0293ee, v2\n\t"
        "v_fmamk_f32 v69, v69, 0x3e0293ee, v2\n\t"
        "v_fmamk_f32 v70, v70, 0x3e0293ee, v2\n\t"
        "v_fmamk_f32 v71, v71, 0x3e0293ee, v2\n\t"
        "v_exp_f32_e32 v68, v68\n\t"
        "v_exp_f32_e32 v69, v69\n\t"
        "v_exp_f32_e32 v70, v70\n\t"
        "s_waitcnt lgkmcnt(2)\n\t"
        "v_smfmac_f32_16x16x64_bf16 v[64:67], v[72:75], v[16:23], v122\n\t"
        "s_waitcnt lgkmcnt(0)\n\t"
        "v_smfmac_f32_16x16x64_bf16 v[64:67], v[76:79], v[24:31], v122\n\t"
        "s_waitcnt vmcnt(4)\n\t"
        "v_cvt_pk_bf16_f32 v36, v68, v69\n\t"
        "v_exp_f32_e32 v71, v71\n\t"
        "s_nop 0\n\t"
        "v_mov_b32_dpp v38, v36 row_ror:8 row_mask:0xf bank_mask:0xf\n\t"
        "v_cvt_pk_bf16_f32 v37, v70, v71\n\t"
        "s_nop 1\n\t"
        "v_mov_b32_dpp v39, v37 row_ror:8 row_mask:0xf bank_mask:0xf\n\t"
        "v_pk_add_f32 v[64:65], v[64:65], v[66:67]\n\t"
        "v_max3_f32 v3, v0, v64, v65\n\t"
        "v_max3_f32 v1, v0, v64, v65\n\t"
        "v_mfma_f32_16x16x32_bf16 v[40:43], v[112:115], v[36:39], 0\n\t"
        "s_nop 0\n\t"
        "v_permlane16_swap_b32_e32 v3, v1\n\t"
        "v_max_f32_e32 v2, v3, v1\n\t"
        "v_max_f32_e32 v3, v3, v1\n\t"
        "v_mfma_f32_16x16x32_bf16 v[44:47], v[116:119], v[36:39], 0\n\t"
        "s_nop 0\n\t"
        "v_permlane32_swap_b32_e32 v2, v3\n\t"
        "v_max_f32_e32 v2, v2, v3\n\t"
        "v_mfma_f32_16x16x32_bf16 v[96:99], v[12:15], v[36:39], 0\n\t"
        "v_mul_f32_e32 v2, 0xbe0293ee, v2\n\t"
        "v_fmamk_f32 v10, v0, 0x3e0293ee, v2\n\t"
        "v_fmamk_f32 v64, v64, 0x3e0293ee, v2\n\t"
        "v_exp_f32_e32 v10, v10\n\t"
        "v_fmamk_f32 v65, v65, 0x3e0293ee, v2\n\t"
        "v_exp_f32_e32 v64, v64\n\t"
        "s_nop 1\n\t"
        "v_mul_f32_e32 v96, v96, v10\n\t"
        "v_exp_f32_e32 v65, v65\n\t"
        "v_pk_mul_f32 v[40:41], v[40:41], v[10:11] op_sel_hi:[1,0]\n\t"
        "v_cvt_pk_bf16_f32 v90, v64, v65\n\t"
        "v_pk_mul_f32 v[42:43], v[42:43], v[10:11] op_sel_hi:[1,0]\n\t"
        "v_cvt_pk_bf16_f32 v95, v64, v65\n\t"
        "v_pk_mul_f32 v[44:45], v[44:45], v[10:11] op_sel_hi:[1,0]\n\t"
        "v_mfma_f32_16x16x16_bf16 v[96:99], v[12:13], v[90:91], v[96:99]\n\t"
        "s_waitcnt vmcnt(1)\n\t"
        "v_mul_f32_e32 v46, v46, v10\n\t"
        "v_mul_f32_e32 v47, v47, v10\n\t"
        "v_mfma_f32_16x16x16_bf16 v[40:43], v[88:89], v[90:91], v[40:43]\n\t"
        "v_max_u32_e32 v54, v88, v89\n\t"
        "v_cmp_eq_u32_e32 vcc, -1, v54\n\t"
        "v_mfma_f32_16x16x16_bf16 v[44:47], v[88:89], v[94:95], v[44:47]\n\t"
        "s_nop 0\n\t"
        "v_rcp_f32_e32 v2, v96\n\t"
        "s_nop 0\n\t"
        "s_cbranch_vccnz .Lfb%=\n\t"
        ".Lrb%=:\n\t"
        "s_nop 0\n\t"
        "v_pk_mul_f32 v[40:41], v[40:41], v[2:3] op_sel_hi:[1,0]\n\t"
        "v_pk_mul_f32 v[42:43], v[42:43], v[2:3] op_sel_hi:[1,0]\n\t"
        "v_cvt_pk_bf16_f32 v100, v40, v41\n\t"
        "v_pk_mul_f32 v[44:45], v[44:45], v[2:3] op_sel_hi:[1,0]\n\t"
        "v_pk_mul_f32 v[46:47], v[46:47], v[2:3] op_sel_hi:[1,0]\n\t"
        "v_cvt_pk_bf16_f32 v101, v42, v43\n\t"
        "v_cvt_pk_bf16_f32 v102, v44, v45\n\t"
        "v_min_u32_e32 v100, -2, v100\n\t"
        "v_cvt_pk_bf16_f32 v103, v46, v47\n\t"
        "v_min_u32_e32 v102, -2, v102\n\t"
        "buffer_store_dwordx4 v[100:103], v33, %[srd], s63 offen sc1\n\t"
        "s_memrealtime s[76:77]\n\t"
        "v_mov_b32_e32 v8, -1\n\t"
        "v_mov_b32_e32 v9, -1\n\t"
        "v_mov_b32_e32 v10, -1\n\t"
        "v_mov_b32_e32 v11, -1\n\t"
        "v_permlane16_swap_b32_e32 v50, v51\n\t"
        "v_permlane16_swap_b32_e32 v88, v89\n\t"
        "v_mov_b32_e32 v14, s40\n\t"
        "s_waitcnt lgkmcnt(0)\n\t"
        "s_waitcnt vmcnt(1)\n\t"
        "s_mov_b32 s97, 0\n\t"
        ".Lsr%=:\n\t"
        "s_mov_b32 exec_hi, 0xffff\n\t"
        "v_subrev_u32_e32 v0, s41, v84\n\t"
        "v_subrev_u32_e32 v1, s41, v86\n\t"
        "v_max_i32_e32 v0, v0, v1\n\t"
        "v_max_u32_e32 v1, v85, v87\n\t"
        "v_cmp_eq_u32_e32 vcc, -1, v1\n\t"
        "v_max_i32_dpp v0, v0, v0 quad_perm:[1,0,3,2] row_mask:0xf bank_mask:0xf\n\t"
        "v_or_b32_e32 v2, v85, v87\n\t"
        "s_nop 0\n\t"
        "v_max_i32_dpp v0, v0, v0 quad_perm:[2,3,0,1] row_mask:0xf bank_mask:0xf\n\t"
        "s_nop 1\n\t"
        "v_max_i32_dpp v0, v0, v0 row_ror:4 row_mask:0xf bank_mask:0xf\n\t"
        "s_nop 1\n\t"
        "v_max_i32_dpp v0, v0, v0 row_ror:8 row_mask:0xf bank_mask:0xf\n\t"
        "s_cbranch_vccnz .Lsf%=\n\t"
        "v_readlane_b32 s85, v0, 0\n\t"
        "v_readlane_b32 s81, v0, 16\n\t"
        "v_readlane_b32 s56, v0, 32\n\t"
        "v_and_b32_e32 v1, 1, v2\n\t"
        "v_cmp_ne_u32_e64 s[86:87], 0, v1\n\t"
        "v_and_b32_e32 v1, 2, v2\n\t"
        "v_cmp_ne_u32_e64 s[88:89], 0, v1\n\t"
        "v_and_b32_e32 v1, 4, v2\n\t"
        "v_cmp_ne_u32_e64 s[90:91], 0, v1\n\t"
        "s_max_i32 s56, s56, s81\n\t"
        "s_add_u32 s92, s85, s41\n\t"
        "s_sub_u32 s92, s92, s43\n\t"
        "s_add_i32 s93, s56, s47\n\t"
        "s_mov_b32 s44, s46\n\t"
        "s_mov_b32 s46, s45\n\t"
        "s_mov_b32 s94, s48\n\t"
        "s_mov_b32 s43, s41\n\t"
        "s_cmp_gt_i32 s56, s54\n\t"
        "s_cbranch_scc1 .Lmv%=\n\t"
        "s_mov_b32 s37, 0\n\t"
        "s_mov_b32 s48, 0\n\t"
        "s_mov_b32 s95, 0\n\t"
        ".Lsd%=:\n\t"
        "s_mov_b32 exec_hi, -1\n\t"
        "s_add_u32 s42, s43, s46\n\t"
        "s_sleep %c[HSLP]\n\t"
        "buffer_load_dwordx2 v[48:49], off, %[wsd], s64 sc1\n\t"
        "buffer_load_dwordx4 v[104:107], v120, %[wsd], s69 offen sc1\n\t"
        "buffer_load_dwordx4 v[108:111], v120, %[wsd], s70 offen sc1\n\t"
        "buffer_store_dwordx2 v[50:51], v34, %[wsd], s72 offen sc1\n\t"
        "buffer_store_dwordx2 v[88:89], v34, %[wsd], s72 offen offset:2048 sc1\n\t"
        "s_lshl_b32 s80, %[w], 8\n\t"
        "s_add_u32 s80, s80, s61\n\t"
        "s_add_u32 s80, s80, 24576\n\t"
        "buffer_store_dwordx2 v[8:9], v34, %[srd], s80 offen sc1\n\t"
        "s_cmp_eq_u32 %[w], 0\n\t"
        "s_cselect_b32 exec_hi, 0, -1\n\t"
        "buffer_store_dwordx4 v[8:11], v120, %[srd], s68 offen sc1\n\t"
        "s_mov_b32 exec_hi, -1\n\t"
        "s_mov_b64 exec, 1\n\t"
        "buffer_store_dword v14, off, %[wsd], s74 sc1\n\t"
        "s_mov_b64 exec, -1\n\t"
        "s_cmp_ge_u32 s40, %c[iCT0]\n\t"
        "s_cselect_b32 s85, 1, 0\n\t"
        "s_cmp_lg_u64 s[86:87], 0\n\t"
        "s_cselect_b32 s86, s85, 0\n\t"
        "s_cmp_lg_u64 s[88:89], 0\n\t"
        "s_cselect_b32 s87, s85, 0\n\t"
        "s_cmp_lg_u64 s[90:91], 0\n\t"
        "s_cselect_b32 s88, s85, 0\n\t"
        "s_or_b32 s80, s94, s86\n\t"
        "s_or_b32 s80, s80, s87\n\t"
        "s_andn2_b32 s90, s85, s80\n\t"
        "s_cmp_le_i32 s92, s44\n\t"
        "s_cselect_b32 s80, s85, 0\n\t"
        "s_andn2_b32 s91, s80, s88\n\t"
        "s_and_b32 s80, s40, %c[LDEC]\n\t"
        "s_cselect_b32 s89, 0, 1\n\t"
        "s_mov_b32 s80, s88\n\t"
        "s_add_i32 s81, s92, s50\n\t"
        "s_sub_i32 s81, s81, 4\n\t"
        "s_cmp_le_i32 s81, s44\n\t"
        "s_cselect_b32 s80, s80, 0\n\t"
        "s_cmp_lt_i32 s50, %c[LC24]\n\t"
        "s_cselect_b32 s80, s80, 0\n\t"
        "s_lshl2_add_u32 s50, s80, s50\n\t"
        "s_sub_i32 s50, s50, s89\n\t"
        "s_max_i32 s50, s50, %c[LC]\n\t"
        "s_lshl_b32 s80, s92, 1\n\t"
        "s_sub_i32 s81, s80, s52\n\t"
        "s_ashr_i32 s82, s81, %c[UPSH]\n\t"
        "s_max_i32 s82, s82, %c[TRUP]\n\t"
        "s_add_i32 s82, s82, s52\n\t"
        "s_min_i32 s82, s82, s80\n\t"
        "s_ashr_i32 s83, s81, 1\n\t"
        "s_min_i32 s83, s83, -%c[TRDN]\n\t"
        "s_add_i32 s83, s83, s52\n\t"
        "s_max_i32 s83, s83, s80\n\t"
        "s_cmp_gt_i32 s81, 0\n\t"
        "s_cselect_b32 s82, s82, s83\n\t"
        "s_cmp_lg_u32 s90, 0\n\t"
        "s_cselect_b32 s52, s82, s52\n\t"
        "s_add_i32 s80, s52, 1\n\t"
        "s_ashr_i32 s80, s80, 1\n\t"
        "s_add_i32 s45, s80, s50\n\t"
        "s_andn2_b32 s80, s86, s94\n\t"
        "s_cmp_lt_i32 s51, %c[LA24]\n\t"
        "s_cselect_b32 s80, s80, 0\n\t"
        "s_lshl2_add_u32 s51, s80, s51\n\t"
        "s_sub_i32 s51, s51, s89\n\t"
        "s_max_i32 s51, s51, %c[LA]\n\t"
        "s_lshl_b32 s80, s93, 1\n\t"
        "s_sub_i32 s81, s80, s53\n\t"
        "s_ashr_i32 s82, s81, %c[UPSH]\n\t"
        "s_max_i32 s82, s82, %c[TRUP]\n\t"
        "s_add_i32 s82, s82, s53\n\t"
        "s_min_i32 s82, s82, s80\n\t"
        "s_ashr_i32 s83, s81, 1\n\t"
        "s_min_i32 s83, s83, -%c[TRDN]\n\t"
        "s_add_i32 s83, s83, s53\n\t"
        "s_max_i32 s83, s83, s80\n\t"
        "s_cmp_gt_i32 s81, 0\n\t"
        "s_cselect_b32 s82, s82, s83\n\t"
        "s_cmp_lg_u32 s91, 0\n\t"
        "s_cselect_b32 s53, s82, s53\n\t"
        "s_add_i32 s80, s53, 1\n\t"
        "s_ashr_i32 s80, s80, 1\n\t"
        "s_add_i32 s47, s80, s51\n\t"
        "s_add_u32 s81, s47, %c[AUP]\n\t"
        "s_min_i32 s80, s95, s81\n\t"
        "s_max_i32 s47, s47, s80\n\t"
        "s_add_u32 s41, s42, s47\n\t"
        "s_add_u32 s40, s40, 1\n\t"
        "v_mov_b32_e32 v88, 0\n\t"
        "v_mov_b32_e32 v89, 0\n\t"
        "v_mov_b32_e32 v90, 0\n\t"
        "v_mov_b32_e32 v91, 0\n\t"
        "v_mov_b32_e32 v92, 0\n\t"
        "v_mov_b32_e32 v93, 0\n\t"
        "v_mov_b32_e32 v94, 0\n\t"
        "v_mov_b32_e32 v95, 0\n\t"
        "v_mov_b32_e32 v96, 0\n\t"
        "v_mov_b32_e32 v97, 0\n\t"
        "v_mov_b32_e32 v98, 0\n\t"
        "v_mov_b32_e32 v99, 0\n\t"
        "v_mov_b32_e32 v100, 0\n\t"
        "v_mov_b32_e32 v101, 0\n\t"
        "v_mov_b32_e32 v102, 0\n\t"
        "v_mov_b32_e32 v103, 0\n\t"
        "ds_read_b128 v[16:19], v121 offset:0\n\t"
        "ds_read_b128 v[20:23], v121 offset:1024\n\t"
        "ds_read_b128 v[24:27], v121 offset:2048\n\t"
        "ds_read_b128 v[28:31], v121 offset:3072\n\t"
        "ds_read_b128 v[32:35], v121 offset:4096\n\t"
        "ds_read_b128 v[36:39], v121 offset:5120\n\t"
        "ds_read_b128 v[40:43], v121 offset:6144\n\t"
        "ds_read_b128 v[44:47], v121 offset:7168\n\t"
        "ds_read_b128 v[56:59], v121 offset:8192\n\t"
        "ds_read_b128 v[60:63], v121 offset:9216\n\t"
        "ds_read_b128 v[64:67], v121 offset:10240\n\t"
        "ds_read_b128 v[68:71], v121 offset:11264\n\t"
        "ds_read_b128 v[72:75], v121 offset:12288\n\t"
        "ds_read_b128 v[76:79], v121 offset:13312\n\t"
        "ds_read_b128 v[80:83], v121 offset:14336\n\t"
        "ds_read_b128 v[84:87], v121 offset:15360\n\t"
        "v_mov_b32_e32 v6, 30720\n\t"
        "ds_read_b64 v[6:7], v6\n\t"
        "s_waitcnt lgkmcnt(0)\n\t"
        "v_readfirstlane_b32 s80, v6\n\t"
        "v_readfirstlane_b32 s81, v7\n\t"
        "s_cmp_eq_u32 s80, s40\n\t"
        "s_cselect_b32 s84, 1, 0\n\t"
        "s_cmp_eq_u32 s81, s40\n\t"
        "s_cselect_b32 s81, 2, 0\n\t"
        "s_or_b32 s84, s84, s81\n\t"
        "s_waitcnt vmcnt(7)\n\t"
        "s_cmp_eq_u32 s64, s36\n\t"
        "s_cbranch_scc1 .Lk11%=\n\t"
        "s_mov_b32 s97, 0\n\t"
        ".Lk01%=:\n\t"
        "v_readfirstlane_b32 s82, v48\n\t"
        "v_readfirstlane_b32 s83, v49\n\t"
        "s_xor_b32 s82, s82, %[tag]\n\t"
        "s_xor_b32 s83, s83, %[tag]\n\t"
        "s_or_b32 s82, s82, s83\n\t"
        "s_cbranch_scc0 .Lk11%=\n\t"
        "s_sleep 3\n\t"
        "buffer_load_dwordx2 v[48:49], off, %[wsd], s64 sc1\n\t"
        "s_add_u32 s97, s97, 1\n\t"
        "s_cmp_lt_u32 s97, 0x200000\n\t"
        "s_cbranch_scc0 .Lgu%=\n\t"
        "s_waitcnt vmcnt(0)\n\t"
        "s_branch .Lk01%=\n\t"
        ".Lk11%=:\n\t"
        "v_mov_b32_e32 v48, s76\n\t"
        "v_mov_b32_e32 v49, s84\n\t"
        "s_mov_b32 s85, s79\n\t"
        "s_and_b32 s80, s40, 3\n\t"
        "s_lshl_b32 s80, s80, 17\n\t"
        "s_add_u32 s57, s80, %[nA]\n\t"
        "s_add_u32 s58, s57, 0x1000\n\t"
        "s_add_u32 s59, s57, 0x8000\n\t"
        "s_add_u32 s60, s57, 0x9000\n\t"
        "s_and_b32 s80, s40, 3\n\t"
        "s_lshl_b32 s80, s80, 17\n\t"
        "s_add_u32 s63, s80, %[nC]\n\t"
        "s_add_u32 s66, s80, %[nS]\n\t"
        "s_add_u32 s81, s80, 0x20000\n\t"
        "s_and_b32 s81, s81, 0x60000\n\t"
        "s_add_u32 s67, s81, %[nW]\n\t"
        "s_add_u32 s79, s81, %[nT]\n\t"
        "s_cmp_eq_u32 %[w], 2\n\t"
        "s_cselect_b32 s81, s80, s81\n\t"
        "s_xor_b32 s81, s81, 0x40000\n\t"
        "s_add_u32 s68, s81, %[pob]\n\t"
        "s_lshl_b32 s72, s40, 17\n\t"
        "s_add_u32 s72, s72, %[kvo]\n\t"
        "v_lshlrev_b32_e32 v51, 6, v120\n\t"
        "s_cmp_eq_u32 %[w], 0\n\t"
        "s_cselect_b32 s73, s85, s36\n\t"
        "s_cselect_b32 s74, %[pra], s36\n\t"
        "v_mov_b32_e32 v50, s40\n\t"
        "s_cmp_ge_u32 s40, 60\n\t"
        "s_cselect_b32 s65, s74, s36\n\t"
        "s_add_u32 s80, s40, 1\n\t"
        "s_min_u32 s80, s80, 63\n\t"
        "s_lshl_b32 s80, s80, 18\n\t"
        "s_add_u32 s69, s80, %[kx]\n\t"
        "s_add_u32 s70, s69, 0x1000\n\t"
        "s_lshl_b32 s80, s40, 18\n\t"
        "s_add_u32 s71, s80, %[vx]\n\t"
        "s_add_u32 s80, s40, 2\n\t"
        "s_lshl_b32 s81, s80, 9\n\t"
        "s_add_u32 s81, s81, %[kf]\n\t"
        "s_and_b32 s80, s80, 0x43\n\t"
        "s_cselect_b32 s64, s36, s81\n\t"
        "s_mov_b32 s80, s61\n\t"
        "s_mov_b32 s61, s62\n\t"
        "s_mov_b32 s62, s80\n\t"
        "s_waitcnt lgkmcnt(0)\n\t"
        "s_branch .Lloop%=\n\t"
        ".Lmv%=:\n\t"
        "s_mov_b32 s48, 1\n\t"
        "s_cmp_eq_u32 s54, %c[iTOL]\n\t"
        "s_cbranch_scc1 .Lm1%=\n\t"
        "s_max_i32 s80, s85, s56\n\t"
        "s_add_u32 s43, s41, s80\n\t"
        "s_add_u32 s43, s43, %c[iPEN0]\n\t"
        "s_mov_b32 s54, %c[iTOL]\n\t"
        "s_add_u32 s46, s46, %c[iX0C]\n\t"
        "s_add_u32 s95, s47, %c[iX0A]\n\t"
        "v_and_b32_e32 v0, 3, v84\n\t"
        "v_and_b32_e32 v1, 3, v86\n\t"
        "v_lshlrev_b32_e64 v0, v0, 1\n\t"
        "v_lshlrev_b32_e64 v1, v1, 1\n\t"
        "v_or_b32_e32 v0, v0, v1\n\t"
        "s_mov_b32 s80, 0\n\t"
        "v_and_b32_e32 v1, 1, v0\n\t"
        "v_cmp_ne_u32_e32 vcc, 0, v1\n\t"
        "s_nop 1\n\t"
        "s_cmp_lg_u64 vcc, 0\n\t"
        "s_cselect_b32 s81, 2, 0\n\t"
        "s_or_b32 s80, s80, s81\n\t"
        "v_and_b32_e32 v1, 2, v0\n\t"
        "v_cmp_ne_u32_e32 vcc, 0, v1\n\t"
        "s_nop 1\n\t"
        "s_cmp_lg_u64 vcc, 0\n\t"
        "s_cselect_b32 s81, 4, 0\n\t"
        "s_or_b32 s80, s80, s81\n\t"
        "v_and_b32_e32 v1, 4, v0\n\t"
        "v_cmp_ne_u32_e32 vcc, 0, v1\n\t"
        "s_nop 1\n\t"
        "s_cmp_lg_u64 vcc, 0\n\t"
        "s_cselect_b32 s81, 8, 0\n\t"
        "s_or_b32 s80, s80, s81\n\t"
        "v_and_b32_e32 v1, 8, v0\n\t"
        "v_cmp_ne_u32_e32 vcc, 0, v1\n\t"
        "s_nop 1\n\t"
        "s_cmp_lg_u64 vcc, 0\n\t"
        "s_cselect_b32 s81, 16, 0\n\t"
        "s_or_b32 s80, s80, s81\n\t"
        "s_lshr_b32 s80, 0x1adf1210, s80\n\t"
        "s_and_b32 s55, s80, 3\n\t"
        "s_branch .Lal%=\n\t"
        ".Lm1%=:\n\t"
        "s_add_u32 s80, s37, %c[iPEN2]\n\t"
        "s_min_i32 s80, s80, %c[iPENM]\n\t"
        "s_cmp_lg_u32 s37, 0\n\t"
        "s_cselect_b32 s37, s80, %c[iPEN]\n\t"
        "s_add_u32 s80, s56, s37\n\t"
        "s_max_i32 s80, s80, %c[iPMIN]\n\t"
        "s_add_u32 s43, s41, s80\n\t"
        "s_add_u32 s95, s93, %c[LA]\n\t"
        "s_add_u32 s46, s46, %c[iXR]\n\t"
        ".Lal%=:\n\t"
        "s_sub_u32 s80, s55, s43\n\t"
        "s_and_b32 s80, s80, %c[iPHM]\n\t"
        "s_add_u32 s43, s43, s80\n\t"
        "s_branch .Lsd%=\n\t"
        ".Lsf%=:\n\t"
        "s_mov_b32 exec_hi, -1\n\t"
        "s_sleep 2\n\t"
        "buffer_load_dwordx4 v[84:87], v120, %[srd], s66 offen sc1\n\t"
        "s_add_u32 s97, s97, 1\n\t"
        "s_cmp_lt_u32 s97, 0x100000\n\t"
        "s_cbranch_scc0 .Lgu%=\n\t"
        "s_waitcnt vmcnt(0)\n\t"
        "s_branch .Lsr%=\n\t"
        ".Lf1%=:\n\t"
        "ds_read_b128 v[92:95], v120 offset:0\n\t"
        "ds_read_b128 v[68:71], v120 offset:4096\n\t"
        "v_max_u32_e32 v54, v16, v18\n\t"
        "v_max3_u32 v54, v54, v17, v19\n\t"
        "v_max3_u32 v54, v54, v20, v22\n\t"
        "v_max3_u32 v54, v54, v21, v23\n\t"
        "v_max3_u32 v54, v54, v24, v26\n\t"
        "v_max3_u32 v54, v54, v25, v27\n\t"
        "v_max3_u32 v54, v54, v28, v30\n\t"
        "v_max3_u32 v54, v54, v29, v31\n\t"
        "v_max3_u32 v54, v54, v32, v34\n\t"
        "v_max3_u32 v54, v54, v33, v35\n\t"
        "v_max3_u32 v54, v54, v36, v38\n\t"
        "v_max3_u32 v54, v54, v37, v39\n\t"
        "v_max3_u32 v54, v54, v40, v42\n\t"
        "v_max3_u32 v54, v54, v41, v43\n\t"
        "v_max3_u32 v54, v54, v44, v46\n\t"
        "v_max3_u32 v54, v54, v45, v47\n\t"
        "v_max3_u32 v54, v54, v48, v50\n\t"
        "v_max3_u32 v54, v54, v49, v51\n\t"
        "v_max3_u32 v54, v54, v0, v2\n\t"
        "v_max3_u32 v54, v54, v1, v3\n\t"
        "v_max3_u32 v54, v54, v4, v6\n\t"
        "v_max3_u32 v54, v54, v5, v7\n\t"
        "v_max3_u32 v54, v54, v8, v10\n\t"
        "v_max3_u32 v54, v54, v9, v11\n\t"
        "s_waitcnt lgkmcnt(0)\n\t"
        "v_max3_u32 v54, v54, v92, v94\n\t"
        "v_max3_u32 v54, v54, v93, v95\n\t"
        "v_max3_u32 v54, v54, v68, v70\n\t"
        "v_max3_u32 v54, v54, v69, v71\n\t"
        "ds_read_b128 v[92:95], v120 offset:8192\n\t"
        "ds_read_b128 v[68:71], v120 offset:12288\n\t"
        "s_waitcnt lgkmcnt(0)\n\t"
        "v_max3_u32 v54, v54, v92, v94\n\t"
        "v_max3_u32 v54, v54, v93, v95\n\t"
        "v_max3_u32 v54, v54, v68, v70\n\t"
        "v_max3_u32 v54, v54, v69, v71\n\t"
        "v_cmp_eq_u32_e32 vcc, -1, v54\n\t"
        "s_nop 1\n\t"
        "v_mov_b32_e32 v55, v55\n\t"
        "s_nop 0\n\t"
        "s_cbranch_vccz .Lr1%=\n\t"
        "buffer_load_dwordx4 v[0:3], v120, %[srd], s57 offen sc1\n\t"
        "buffer_load_dwordx4 v[4:7], v120, %[srd], s58 offen sc1\n\t"
        "buffer_load_dwordx4 v[8:11], v120, %[srd], s59 offen sc1\n\t"
        "buffer_load_dwordx4 v[12:15], v120, %[srd], s60 offen sc1\n\t"
        "s_add_u32 s80, s40, 1\n\t"
        "v_mov_b32_e32 v54, 30720\n\t"
        "v_mov_b32_e32 v55, s80\n\t"
        "ds_write_b32 v54, v55\n\t"
        "ds_read_b128 v[16:19], v121 offset:0\n\t"
        "ds_read_b128 v[20:23], v121 offset:1024\n\t"
        "ds_read_b128 v[24:27], v121 offset:2048\n\t"
        "ds_read_b128 v[28:31], v121 offset:3072\n\t"
        "ds_read_b128 v[32:35], v121 offset:4096\n\t"
        "ds_read_b128 v[36:39], v121 offset:5120\n\t"
        "ds_read_b128 v[40:43], v121 offset:6144\n\t"
        "ds_read_b128 v[44:47], v121 offset:7168\n\t"
        "ds_read_b128 v[56:59], v121 offset:8192\n\t"
        "ds_read_b128 v[60:63], v121 offset:9216\n\t"
        "ds_read_b128 v[64:67], v121 offset:10240\n\t"
        "ds_read_b128 v[68:71], v121 offset:11264\n\t"
        "ds_read_b128 v[72:75], v121 offset:12288\n\t"
        "ds_read_b128 v[76:79], v121 offset:13312\n\t"
        "ds_read_b128 v[80:83], v121 offset:14336\n\t"
        "ds_read_b128 v[84:87], v121 offset:15360\n\t"
        "s_mov_b32 s65, s36\n\t"
        "v_mov_b32_e32 v88, 0\n\t"
        "v_mov_b32_e32 v89, 0\n\t"
        "v_mov_b32_e32 v90, 0\n\t"
        "v_mov_b32_e32 v91, 0\n\t"
        "v_mov_b32_e32 v92, 0\n\t"
        "v_mov_b32_e32 v93, 0\n\t"
        "v_mov_b32_e32 v94, 0\n\t"
        "v_mov_b32_e32 v95, 0\n\t"
        "v_mov_b32_e32 v96, 0\n\t"
        "v_mov_b32_e32 v97, 0\n\t"
        "v_mov_b32_e32 v98, 0\n\t"
        "v_mov_b32_e32 v99, 0\n\t"
        "v_mov_b32_e32 v100, 0\n\t"
        "v_mov_b32_e32 v101, 0\n\t"
        "v_mov_b32_e32 v102, 0\n\t"
        "v_mov_b32_e32 v103, 0\n\t"
        "s_waitcnt lgkmcnt(0)\n\t"
        "s_barrier\n\t"
        ".Lfa%=:\n\t"
        "s_waitcnt vmcnt(0)\n\t"
        "v_max_u32_e32 v54, v0, v2\n\t"
        "v_max3_u32 v54, v54, v1, v3\n\t"
        "v_max3_u32 v54, v54, v4, v6\n\t"
        "v_max3_u32 v54, v54, v5, v7\n\t"
        "v_max3_u32 v54, v54, v8, v10\n\t"
        "v_max3_u32 v54, v54, v9, v11\n\t"
        "v_max3_u32 v54, v54, v12, v14\n\t"
        "v_max3_u32 v54, v54, v13, v15\n\t"
        "v_cmp_eq_u32_e32 vcc, -1, v54\n\t"
        "s_nop 1\n\t"
        "v_mov_b32_e32 v55, v55\n\t"
        "s_nop 0\n\t"
        "s_cbranch_vccz .Lfg%=\n\t"
        "s_sleep %c[ASLP]\n\t"
        "buffer_load_dwordx4 v[0:3], v120, %[srd], s57 offen sc1\n\t"
        "buffer_load_dwordx4 v[4:7], v120, %[srd], s58 offen sc1\n\t"
        "buffer_load_dwordx4 v[8:11], v120, %[srd], s59 offen sc1\n\t"
        "buffer_load_dwordx4 v[12:15], v120, %[srd], s60 offen sc1\n\t"
        "s_add_u32 s97, s97, 1\n\t"
        "s_cmp_lt_u32 s97, 0x100000\n\t"
        "s_cbranch_scc0 .Lgu%=\n\t"
        "s_branch .Lfa%=\n\t"
        ".Lfg%=:\n\t"
        "s_sleep %c[RSLP]\n\t"
        "s_branch .Lgo%=\n\t"
        ".Lfb%=:\n\t"
        "s_nop 7\n\t"
        "s_mov_b32 s97, 0\n\t"
        "s_add_u32 s80, s40, 1\n\t"
        "v_mov_b32_e32 v54, 30724\n\t"
        "v_mov_b32_e32 v55, s80\n\t"
        "ds_write_b32 v54, v55\n\t"
        ".Lfp%=:\n\t"
        "s_sleep %c[ASLP]\n\t"
        "buffer_load_dword v88, v127, %[srd], s61 offen offset:0 sc1\n\t"
        "buffer_load_dword v89, v127, %[srd], s61 offen offset:256 sc1\n\t"
        "s_add_u32 s97, s97, 1\n\t"
        "s_cmp_lt_u32 s97, 0x100000\n\t"
        "s_cbranch_scc0 .Lgu%=\n\t"
        "s_waitcnt vmcnt(0)\n\t"
        "v_max_u32_e32 v54, v88, v89\n\t"
        "v_cmp_eq_u32_e32 vcc, -1, v54\n\t"
        "s_nop 1\n\t"
        "v_mov_b32_e32 v55, v55\n\t"
        "s_nop 0\n\t"
        "s_cbranch_vccnz .Lfp%=\n\t"
        "v_mfma_f32_16x16x32_bf16 v[40:43], v[112:115], v[36:39], 0\n\t"
        "v_mfma_f32_16x16x32_bf16 v[44:47], v[116:119], v[36:39], 0\n\t"
        "s_nop 1\n\t"
        "v_mov_b32_e32 v55, v55\n\t"
        "s_nop 1\n\t"
        "v_mov_b32_e32 v55, v55\n\t"
        "s_nop 0\n\t"
        "v_pk_mul_f32 v[40:41], v[40:41], v[10:11] op_sel_hi:[1,0]\n\t"
        "v_pk_mul_f32 v[42:43], v[42:43], v[10:11] op_sel_hi:[1,0]\n\t"
        "v_pk_mul_f32 v[44:45], v[44:45], v[10:11] op_sel_hi:[1,0]\n\t"
        "v_pk_mul_f32 v[46:47], v[46:47], v[10:11] op_sel_hi:[1,0]\n\t"
        "v_mfma_f32_16x16x16_bf16 v[40:43], v[88:89], v[90:91], v[40:43]\n\t"
        "s_nop 0\n\t"
        "v_mfma_f32_16x16x16_bf16 v[44:47], v[88:89], v[94:95], v[44:47]\n\t"
        "s_nop 7\n\t"
        "s_branch .Lrb%=\n\t"
        ".Lta%=:\n\t"
        "s_waitcnt lgkmcnt(0)\n\t"
        "s_branch .Lxa%=\n\t"
        ".Lgu%=:\n\t"
        "s_mov_b64 exec, -1\n\t"
        "v_mov_b32_e32 v54, %c[ferr]\n\t"
        "v_mov_b32_e32 v55, 1\n\t"
        "buffer_store_dword v55, v54, %[wsd], 0 offen sc1\n\t"
        "s_endpgm\n\t"
        ".Lend%=:\n\t"
        "s_nop 0\n\t"
        "v_permlane16_swap_b32_e32 v50, v51\n\t"
        "v_mov_b32_e32 v0, 64\n\t"
        "buffer_store_dwordx2 v[50:51], v34, %[wsd], s72 offen sc1\n\t"
        "s_cmp_lg_u32 %[w], 0\n\t"
        "s_waitcnt vmcnt(0)\n\t"
        "s_barrier\n\t"
        "s_cbranch_scc1 .Le2%=\n\t"
        "s_mov_b64 exec, 1\n\t"
        "buffer_store_dword v0, off, %[wsd], %[pra] sc1\n\t"
        "s_mov_b64 exec, -1\n\t"
        ".Le2%=:\n\t"
        "s_endpgm"
        : [nA] "+s"(nA), [nS] "+s"(nS), [nT] "+s"(nT), [nW] "+s"(nW), [nC] "+s"(nC), [b0] "+s"(b0), [pob] "+s"(pob)
        : [srd] "s"(rhd), [wsd] "s"(wsd), [w] "s"(w), [tag] "s"(tag), [kx] "s"(kx), [vx] "s"(vx), [kf] "s"(kf), [kvo] "s"(kvo), [cal] "s"(cal), [sq] "s"(sq), [sk] "s"(sk), [m0b] "s"(m0b),
          [pra] "s"(pr_a), [ferr] "i"(F_ERR), [iD1] "i"(D1), [iD2] "i"(D2), [LC] "i"(LC0), [LA] "i"(LA0), [LC24] "i"(LC0 + 24), [LA24] "i"(LA0 + 24), [E1] "i"(2 * (D1 - LC0)), [E2] "i"(2 * (D2 - LA0)),
          [iPEN] "i"(PEN), [iPEN2] "i"(PEN2), [iPENM] "i"(PEN + 2 * PEN2), [iPEN0] "i"(PEN0), [iTOL] "i"(TOL), [iCT0] "i"(CT0), [iX0C] "i"(X0C), [iX0A] "i"(X0A), [iPHM] "i"(PH_M),
          [iFT] "i"(-3), [iFN] "i"(FINE_N), [AUP] "i"(A_UP), [ASLP] "i"(A_SLP), [LDEC] "i"(L_DEC), [UPSH] "i"(UP_SH), [TRUP] "i"(TR_UP), [TRDN] "i"(TR_DN), [HSLP] "i"(H_SLP), [iPMIN] "i"(PMIN), [VSLP] "i"(V_SLP), [RSLP] "i"(R_SLP), [iVD] "i"(V_DEL), [iXR] "i"(XR), [NX0] "i"(NEARX), [NX1] "i"(8192 - NEARX), [iCS1] "i"(CS1), [iCS2] "i"(CS2), [NY0] "i"(NEARY), [NY1] "i"(16384 - NEARY),
          "{v[120:127]}"(pc), "{v[112:113]}"(pd), "{v96}"(dv), "{v[98:105]}"(pa)
        : "memory", "vcc", "scc", "m0", "s36", "s37", "s38", "s39", "s40", "s41", "s42", "s43", "s44", "s45", "s46", "s47", "s48", "s49", "s50", "s51", "s52", "s53", "s54", "s55", "s56", "s57", "s58", "s59", "s60", "s61", "s62", "s63", "s64", "s65", "s66", "s67", "s68", "s69", "s70", "s71", "s72", "s73", "s74", "s75", "s76", "s77", "s78", "s79", "s80", "s81", "s82", "s83", "s84", "s85", "s86", "s87", "s88", "s89", "s90", "s91", "s92", "s93", "s94", "s95", "s96", "s97", "s98", "s99");
    } else {
        const int cb = i32;
        const v4u sv = mk_srd(p.Wv, 1024 * 4096 * 2), so = mk_srd(p.Wo, 1024 * 4096 * 2);
        const unsigned key = KEYM & ((cb & 1) * 2 + G), tu = cb >> 2, m0b = w * 32768, f = (g & 1) | ((c >> 1) << 1);
        const unsigned t0 = m0b + (16 * (g >> 1) + 4 * (g & 1) + (c & 1) + 8 * (c >> 1)) * 128 + (f << 5) + (n16 & 3) * 8, t1 = m0b + (16 * g + 4 * (c >> 1) + (c & 1)) * 64 + ((c >> 1) << 5) + (n16 & 3) * 8;
        v8u pa;
        pa[0] = (64 * w + (l >> 3)) * 8192 + vi * 128 + (((l & 7) ^ ((l >> 5) << 1)) << 4); pa[1] = pa[0] ^ 64u;
        pa[2] = (1024 * w + (l >> 2)) * 2048 + cb * 64 + (((l & 3) ^ ((g & 1) << 1)) << 4);
#pragma unroll
        for (int q = 0; q < 4; ++q) pa[3 + q] = t0 ^ (((w + q) & 3) << 5);
        pa[7] = t1;
        const v2u pb = {t1 ^ 32u, (unsigned)((w * 1024 + cb * 32) * 2048 + l * 1024) + (((3u - (so.x >> 7)) & 7u) << 7)};
        const v16u pc = {(unsigned)tid * 16, (n16 & 2) ? 0xEEEEu : 0x4444u, (unsigned)((vi & 1) * 1024 + w * 256 + n16 * 16 + g * 4), (unsigned)(4096 + tid * 16),
                   w < 3 ? (unsigned)((cb >> 4) * 32768 + ((cb >> 1) & 7) * 1024 + ((cb & 1) * 2 + (l >> 5)) * 256 + G * 128 + (l & 31) * 4) : OOB,
                   tid < 3 ? (unsigned)(l * 1024 + (32 + vi) * 8) : OOB, tid == 192 ? pr_a : OOB, (unsigned)(L_V + 8192 + w * 512 + (g & 1) * 256 + (g >> 1) * 64 + n16 * 4),
                   (unsigned)(L_V + (((l >> 2) & 1) + ((l >> 3) & 2)) * 128 + (l >> 5) * 64 + (l & 8) * 4 + (l & 3) * 8), 0u,
                   (unsigned)(L_V + w * 2048 + l * 8), (unsigned)(L_V + ((w + 1) & 3) * 2048 + 512 + l * 8), (unsigned)(L_V + ((w + 2) & 3) * 2048 + 1024 + l * 8), (unsigned)(L_V + ((w + 3) & 3) * 2048 + 1536 + l * 8), 0u, 0u};
        if (tid < 2) ((lu32*)(size_t)(L_V + L_FL))[tid] = ~0u;
    asm volatile(
        "s_mov_b32 s82, 3\n\t"
        ".Ld0%=:\n\t"
        "s_xor_b32 s80, s82, %[key]\n\t"
        "s_mul_i32 s81, s80, 131072\n\t"
        "s_lshl_b32 s80, s80, 11\n\t"
        "s_add_u32 s80, s80, %[m0b]\n\t"
        "s_add_u32 m0, s80, 0\n\t"
        "s_add_u32 s81, s81, 0\n\t"
        "buffer_load_dwordx4 v96, %[sv], s81 offen lds\n\t"
        "s_add_u32 m0, m0, 0x400\n\t"
        "s_add_u32 s81, s81, 65536\n\t"
        "buffer_load_dwordx4 v97, %[sv], s81 offen lds\n\t"
        "s_sub_u32 s82, s82, 1\n\t"
        "s_cbranch_scc0 .Ld0%=\n\t"
        "s_mov_b32 s82, 3\n\t"
        ".Ld1%=:\n\t"
        "s_xor_b32 s80, s82, %[key]\n\t"
        "s_mul_i32 s81, s80, 131072\n\t"
        "s_lshl_b32 s80, s80, 11\n\t"
        "s_add_u32 s80, s80, %[m0b]\n\t"
        "s_add_u32 m0, s80, 8192\n\t"
        "s_add_u32 s81, s81, 2097152\n\t"
        "buffer_load_dwordx4 v96, %[sv], s81 offen lds\n\t"
        "s_add_u32 m0, m0, 0x400\n\t"
        "s_add_u32 s81, s81, 65536\n\t"
        "buffer_load_dwordx4 v97, %[sv], s81 offen lds\n\t"
        "s_sub_u32 s82, s82, 1\n\t"
        "s_cbranch_scc0 .Ld1%=\n\t"
        "s_cmp_eq_u32 %[tu], 0\n\t"
        "s_cselect_b32 s81, 0, 0x7fff0000\n\t"
        "buffer_load_dword v110, v105, %[so], s81 offen\n\t"
        "s_mov_b32 s82, 3\n\t"
        ".Ld2%=:\n\t"
        "s_xor_b32 s80, s82, %[key]\n\t"
        "s_mul_i32 s81, s80, 131072\n\t"
        "s_lshl_b32 s80, s80, 11\n\t"
        "s_add_u32 s80, s80, %[m0b]\n\t"
        "s_add_u32 m0, s80, 16384\n\t"
        "s_add_u32 s81, s81, 4194304\n\t"
        "buffer_load_dwordx4 v96, %[sv], s81 offen lds\n\t"
        "s_add_u32 m0, m0, 0x400\n\t"
        "s_add_u32 s81, s81, 65536\n\t"
        "buffer_load_dwordx4 v97, %[sv], s81 offen lds\n\t"
        "s_sub_u32 s82, s82, 1\n\t"
        "s_cbranch_scc0 .Ld2%=\n\t"
        "s_cmp_eq_u32 %[tu], 1\n\t"
        "s_cselect_b32 s81, 0, 0x7fff0000\n\t"
        "buffer_load_dword v110, v105, %[so], s81 offen\n\t"
        "s_mov_b32 s82, 3\n\t"
        ".Ld3%=:\n\t"
        "s_xor_b32 s80, s82, %[key]\n\t"
        "s_mul_i32 s81, s80, 131072\n\t"
        "s_lshl_b32 s80, s80, 11\n\t"
        "s_add_u32 s80, s80, %[m0b]\n\t"
        "s_add_u32 m0, s80, 24576\n\t"
        "s_add_u32 s81, s81, 6291456\n\t"
        "buffer_load_dwordx4 v96, %[sv], s81 offen lds\n\t"
        "s_add_u32 m0, m0, 0x400\n\t"
        "s_add_u32 s81, s81, 65536\n\t"
        "buffer_load_dwordx4 v97, %[sv], s81 offen lds\n\t"
        "s_sub_u32 s82, s82, 1\n\t"
        "s_cbranch_scc0 .Ld3%=\n\t"
        "s_waitcnt vmcnt(26)\n\t"
        "ds_read_b64_tr_b16 v[128:129], v99 offset:0\n\t"
        "ds_read_b64_tr_b16 v[130:131], v99 offset:256\n\t"
        "ds_read_b64_tr_b16 v[132:133], v99 offset:4096\n\t"
        "ds_read_b64_tr_b16 v[134:135], v99 offset:4352\n\t"
        "ds_read_b64_tr_b16 v[136:137], v100 offset:0\n\t"
        "ds_read_b64_tr_b16 v[138:139], v100 offset:256\n\t"
        "ds_read_b64_tr_b16 v[140:141], v100 offset:4096\n\t"
        "ds_read_b64_tr_b16 v[142:143], v100 offset:4352\n\t"
        "ds_read_b64_tr_b16 v[144:145], v101 offset:0\n\t"
        "ds_read_b64_tr_b16 v[146:147], v101 offset:256\n\t"
        "ds_read_b64_tr_b16 v[148:149], v101 offset:4096\n\t"
        "ds_read_b64_tr_b16 v[150:151], v101 offset:4352\n\t"
        "ds_read_b64_tr_b16 v[152:153], v102 offset:0\n\t"
        "ds_read_b64_tr_b16 v[154:155], v102 offset:256\n\t"
        "ds_read_b64_tr_b16 v[156:157], v102 offset:4096\n\t"
        "ds_read_b64_tr_b16 v[158:159], v102 offset:4352\n\t"
        "s_waitcnt lgkmcnt(0)\n\t"
        "s_cmp_eq_u32 %[tu], 2\n\t"
        "s_cselect_b32 s81, 0, 0x7fff0000\n\t"
        "buffer_load_dword v110, v105, %[so], s81 offen\n\t"
        "s_mov_b32 s82, 3\n\t"
        ".Ld4%=:\n\t"
        "s_xor_b32 s80, s82, %[key]\n\t"
        "s_mul_i32 s81, s80, 65536\n\t"
        "s_lshl_b32 s80, s80, 11\n\t"
        "s_add_u32 s80, s80, %[m0b]\n\t"
        "s_add_u32 m0, s80, 0\n\t"
        "s_add_u32 s81, s81, 0\n\t"
        "buffer_load_dwordx4 v98, %[so], s81 offen lds\n\t"
        "s_add_u32 m0, m0, 0x400\n\t"
        "s_add_u32 s81, s81, 32768\n\t"
        "buffer_load_dwordx4 v98, %[so], s81 offen lds\n\t"
        "s_sub_u32 s82, s82, 1\n\t"
        "s_cbranch_scc0 .Ld4%=\n\t"
        "s_waitcnt vmcnt(27)\n\t"
        "ds_read_b64_tr_b16 v[160:161], v99 offset:8192\n\t"
        "ds_read_b64_tr_b16 v[162:163], v99 offset:8448\n\t"
        "ds_read_b64_tr_b16 v[164:165], v99 offset:12288\n\t"
        "ds_read_b64_tr_b16 v[166:167], v99 offset:12544\n\t"
        "ds_read_b64_tr_b16 v[168:169], v100 offset:8192\n\t"
        "ds_read_b64_tr_b16 v[170:171], v100 offset:8448\n\t"
        "ds_read_b64_tr_b16 v[172:173], v100 offset:12288\n\t"
        "ds_read_b64_tr_b16 v[174:175], v100 offset:12544\n\t"
        "ds_read_b64_tr_b16 v[176:177], v101 offset:8192\n\t"
        "ds_read_b64_tr_b16 v[178:179], v101 offset:8448\n\t"
        "ds_read_b64_tr_b16 v[180:181], v101 offset:12288\n\t"
        "ds_read_b64_tr_b16 v[182:183], v101 offset:12544\n\t"
        "ds_read_b64_tr_b16 v[184:185], v102 offset:8192\n\t"
        "ds_read_b64_tr_b16 v[186:187], v102 offset:8448\n\t"
        "ds_read_b64_tr_b16 v[188:189], v102 offset:12288\n\t"
        "ds_read_b64_tr_b16 v[190:191], v102 offset:12544\n\t"
        "s_waitcnt lgkmcnt(0)\n\t"
        "s_cmp_eq_u32 %[tu], 3\n\t"
        "s_cselect_b32 s81, 0, 0x7fff0000\n\t"
        "buffer_load_dword v110, v105, %[so], s81 offen\n\t"
        "s_mov_b32 s82, 3\n\t"
        ".Ld5%=:\n\t"
        "s_xor_b32 s80, s82, %[key]\n\t"
        "s_mul_i32 s81, s80, 65536\n\t"
        "s_lshl_b32 s80, s80, 11\n\t"
        "s_add_u32 s80, s80, %[m0b]\n\t"
        "s_add_u32 m0, s80, 8192\n\t"
        "s_add_u32 s81, s81, 262144\n\t"
        "buffer_load_dwordx4 v98, %[so], s81 offen lds\n\t"
        "s_add_u32 m0, m0, 0x400\n\t"
        "s_add_u32 s81, s81, 32768\n\t"
        "buffer_load_dwordx4 v98, %[so], s81 offen lds\n\t"
        "s_sub_u32 s82, s82, 1\n\t"
        "s_cbranch_scc0 .Ld5%=\n\t"
        "s_waitcnt vmcnt(27)\n\t"
        "ds_read_b64_tr_b16 v[192:193], v99 offset:16384\n\t"
        "ds_read_b64_tr_b16 v[194:195], v99 offset:16640\n\t"
        "ds_read_b64_tr_b16 v[196:197], v99 offset:20480\n\t"
        "ds_read_b64_tr_b16 v[198:199], v99 offset:20736\n\t"
        "ds_read_b64_tr_b16 v[200:201], v100 offset:16384\n\t"
        "ds_read_b64_tr_b16 v[202:203], v100 offset:16640\n\t"
        "ds_read_b64_tr_b16 v[204:205], v100 offset:20480\n\t"
        "ds_read_b64_tr_b16 v[206:207], v100 offset:20736\n\t"
        "ds_read_b64_tr_b16 v[208:209], v101 offset:16384\n\t"
        "ds_read_b64_tr_b16 v[210:211], v101 offset:16640\n\t"
        "ds_read_b64_tr_b16 v[212:213], v101 offset:20480\n\t"
        "ds_read_b64_tr_b16 v[214:215], v101 offset:20736\n\t"
        "ds_read_b64_tr_b16 v[216:217], v102 offset:16384\n\t"
        "ds_read_b64_tr_b16 v[218:219], v102 offset:16640\n\t"
        "ds_read_b64_tr_b16 v[220:221], v102 offset:20480\n\t"
        "ds_read_b64_tr_b16 v[222:223], v102 offset:20736\n\t"
        "s_waitcnt lgkmcnt(0)\n\t"
        "s_cmp_eq_u32 %[tu], 4\n\t"
        "s_cselect_b32 s81, 0, 0x7fff0000\n\t"
        "buffer_load_dword v110, v105, %[so], s81 offen\n\t"
        "s_mov_b32 s82, 3\n\t"
        ".Ld6%=:\n\t"
        "s_xor_b32 s80, s82, %[key]\n\t"
        "s_mul_i32 s81, s80, 65536\n\t"
        "s_lshl_b32 s80, s80, 11\n\t"
        "s_add_u32 s80, s80, %[m0b]\n\t"
        "s_add_u32 m0, s80, 16384\n\t"
        "s_add_u32 s81, s81, 524288\n\t"
        "buffer_load_dwordx4 v98, %[so], s81 offen lds\n\t"
        "s_add_u32 m0, m0, 0x400\n\t"
        "s_add_u32 s81, s81, 32768\n\t"
        "buffer_load_dwordx4 v98, %[so], s81 offen lds\n\t"
        "s_sub_u32 s82, s82, 1\n\t"
        "s_cbranch_scc0 .Ld6%=\n\t"
        "s_waitcnt vmcnt(27)\n\t"
        "ds_read_b64_tr_b16 v[224:225], v99 offset:24576\n\t"
        "ds_read_b64_tr_b16 v[226:227], v99 offset:24832\n\t"
        "ds_read_b64_tr_b16 v[228:229], v99 offset:28672\n\t"
        "ds_read_b64_tr_b16 v[230:231], v99 offset:28928\n\t"
        "ds_read_b64_tr_b16 v[232:233], v100 offset:24576\n\t"
        "ds_read_b64_tr_b16 v[234:235], v100 offset:24832\n\t"
        "ds_read_b64_tr_b16 v[236:237], v100 offset:28672\n\t"
        "ds_read_b64_tr_b16 v[238:239], v100 offset:28928\n\t"
        "ds_read_b64_tr_b16 v[240:241], v101 offset:24576\n\t"
        "ds_read_b64_tr_b16 v[242:243], v101 offset:24832\n\t"
        "ds_read_b64_tr_b16 v[244:245], v101 offset:28672\n\t"
        "ds_read_b64_tr_b16 v[246:247], v101 offset:28928\n\t"
        "ds_read_b64_tr_b16 v[248:249], v102 offset:24576\n\t"
        "ds_read_b64_tr_b16 v[250:251], v102 offset:24832\n\t"
        "ds_read_b64_tr_b16 v[252:253], v102 offset:28672\n\t"
        "ds_read_b64_tr_b16 v[254:255], v102 offset:28928\n\t"
        "s_waitcnt lgkmcnt(0)\n\t"
        "s_cmp_eq_u32 %[tu], 5\n\t"
        "s_cselect_b32 s81, 0, 0x7fff0000\n\t"
        "buffer_load_dword v110, v105, %[so], s81 offen\n\t"
        "s_mov_b32 s82, 3\n\t"
        ".Ld7%=:\n\t"
        "s_xor_b32 s80, s82, %[key]\n\t"
        "s_mul_i32 s81, s80, 65536\n\t"
        "s_lshl_b32 s80, s80, 11\n\t"
        "s_add_u32 s80, s80, %[m0b]\n\t"
        "s_add_u32 m0, s80, 24576\n\t"
        "s_add_u32 s81, s81, 786432\n\t"
        "buffer_load_dwordx4 v98, %[so], s81 offen lds\n\t"
        "s_add_u32 m0, m0, 0x400\n\t"
        "s_add_u32 s81, s81, 32768\n\t"
        "buffer_load_dwordx4 v98, %[so], s81 offen lds\n\t"
        "s_sub_u32 s82, s82, 1\n\t"
        "s_cbranch_scc0 .Ld7%=\n\t"
        "s_waitcnt vmcnt(27)\n\t"
        "ds_read_b64_tr_b16 a[0:1], v103 offset:0\n\t"
        "ds_read_b64_tr_b16 a[2:3], v103 offset:128\n\t"
        "ds_read_b64_tr_b16 a[4:5], v103 offset:4096\n\t"
        "ds_read_b64_tr_b16 a[6:7], v103 offset:4224\n\t"
        "ds_read_b64_tr_b16 a[8:9], v104 offset:0\n\t"
        "ds_read_b64_tr_b16 a[10:11], v104 offset:128\n\t"
        "ds_read_b64_tr_b16 a[12:13], v104 offset:4096\n\t"
        "ds_read_b64_tr_b16 a[14:15], v104 offset:4224\n\t"
        "ds_read_b64_tr_b16 a[16:17], v103 offset:512\n\t"
        "ds_read_b64_tr_b16 a[18:19], v103 offset:640\n\t"
        "ds_read_b64_tr_b16 a[20:21], v103 offset:4608\n\t"
        "ds_read_b64_tr_b16 a[22:23], v103 offset:4736\n\t"
        "ds_read_b64_tr_b16 a[24:25], v104 offset:512\n\t"
        "ds_read_b64_tr_b16 a[26:27], v104 offset:640\n\t"
        "ds_read_b64_tr_b16 a[28:29], v104 offset:4608\n\t"
        "ds_read_b64_tr_b16 a[30:31], v104 offset:4736\n\t"
        "s_waitcnt lgkmcnt(0)\n\t"
        "s_cmp_eq_u32 %[tu], 6\n\t"
        "s_cselect_b32 s81, 0, 0x7fff0000\n\t"
        "buffer_load_dword v110, v105, %[so], s81 offen\n\t"
        "s_mov_b32 s82, 3\n\t"
        ".Ld8%=:\n\t"
        "s_xor_b32 s80, s82, %[key]\n\t"
        "s_mul_i32 s81, s80, 65536\n\t"
        "s_lshl_b32 s80, s80, 11\n\t"
        "s_add_u32 s80, s80, %[m0b]\n\t"
        "s_add_u32 m0, s80, 0\n\t"
        "s_add_u32 s81, s81, 1048576\n\t"
        "buffer_load_dwordx4 v98, %[so], s81 offen lds\n\t"
        "s_add_u32 m0, m0, 0x400\n\t"
        "s_add_u32 s81, s81, 32768\n\t"
        "buffer_load_dwordx4 v98, %[so], s81 offen lds\n\t"
        "s_sub_u32 s82, s82, 1\n\t"
        "s_cbranch_scc0 .Ld8%=\n\t"
        "s_waitcnt vmcnt(27)\n\t"
        "ds_read_b64_tr_b16 a[32:33], v103 offset:8192\n\t"
        "ds_read_b64_tr_b16 a[34:35], v103 offset:8320\n\t"
        "ds_read_b64_tr_b16 a[36:37], v103 offset:12288\n\t"
        "ds_read_b64_tr_b16 a[38:39], v103 offset:12416\n\t"
        "ds_read_b64_tr_b16 a[40:41], v104 offset:8192\n\t"
        "ds_read_b64_tr_b16 a[42:43], v104 offset:8320\n\t"
        "ds_read_b64_tr_b16 a[44:45], v104 offset:12288\n\t"
        "ds_read_b64_tr_b16 a[46:47], v104 offset:12416\n\t"
        "ds_read_b64_tr_b16 a[48:49], v103 offset:8704\n\t"
        "ds_read_b64_tr_b16 a[50:51], v103 offset:8832\n\t"
        "ds_read_b64_tr_b16 a[52:53], v103 offset:12800\n\t"
        "ds_read_b64_tr_b16 a[54:55], v103 offset:12928\n\t"
        "ds_read_b64_tr_b16 a[56:57], v104 offset:8704\n\t"
        "ds_read_b64_tr_b16 a[58:59], v104 offset:8832\n\t"
        "ds_read_b64_tr_b16 a[60:61], v104 offset:12800\n\t"
        "ds_read_b64_tr_b16 a[62:63], v104 offset:12928\n\t"
        "s_waitcnt lgkmcnt(0)\n\t"
        "s_cmp_eq_u32 %[tu], 7\n\t"
        "s_cselect_b32 s81, 0, 0x7fff0000\n\t"
        "buffer_load_dword v110, v105, %[so], s81 offen\n\t"
        "s_mov_b32 s82, 3\n\t"
        ".Ld9%=:\n\t"
        "s_xor_b32 s80, s82, %[key]\n\t"
        "s_mul_i32 s81, s80, 65536\n\t"
        "s_lshl_b32 s80, s80, 11\n\t"
        "s_add_u32 s80, s80, %[m0b]\n\t"
        "s_add_u32 m0, s80, 8192\n\t"
        "s_add_u32 s81, s81, 1310720\n\t"
        "buffer_load_dwordx4 v98, %[so], s81 offen lds\n\t"
        "s_add_u32 m0, m0, 0x400\n\t"
        "s_add_u32 s81, s81, 32768\n\t"
        "buffer_load_dwordx4 v98, %[so], s81 offen lds\n\t"
        "s_sub_u32 s82, s82, 1\n\t"
        "s_cbranch_scc0 .Ld9%=\n\t"
        "s_waitcnt vmcnt(27)\n\t"
        "ds_read_b64_tr_b16 a[64:65], v103 offset:16384\n\t"
        "ds_read_b64_tr_b16 a[66:67], v103 offset:16512\n\t"
        "ds_read_b64_tr_b16 a[68:69], v103 offset:20480\n\t"
        "ds_read_b64_tr_b16 a[70:71], v103 offset:20608\n\t"
        "ds_read_b64_tr_b16 a[72:73], v104 offset:16384\n\t"
        "ds_read_b64_tr_b16 a[74:75], v104 offset:16512\n\t"
        "ds_read_b64_tr_b16 a[76:77], v104 offset:20480\n\t"
        "ds_read_b64_tr_b16 a[78:79], v104 offset:20608\n\t"
        "ds_read_b64_tr_b16 a[80:81], v103 offset:16896\n\t"
        "ds_read_b64_tr_b16 a[82:83], v103 offset:17024\n\t"
        "ds_read_b64_tr_b16 a[84:85], v103 offset:20992\n\t"
        "ds_read_b64_tr_b16 a[86:87], v103 offset:21120\n\t"
        "ds_read_b64_tr_b16 a[88:89], v104 offset:16896\n\t"
        "ds_read_b64_tr_b16 a[90:91], v104 offset:17024\n\t"
        "ds_read_b64_tr_b16 a[92:93], v104 offset:20992\n\t"
        "ds_read_b64_tr_b16 a[94:95], v104 offset:21120\n\t"
        "buffer_load_dwordx4 v[0:3], v112, %[wsd], %[cal] offen sc1\n\t"
        "s_waitcnt lgkmcnt(0)\n\t"
        "s_mov_b32 s82, 3\n\t"
        ".Ld10%=:\n\t"
        "s_xor_b32 s80, s82, %[key]\n\t"
        "s_mul_i32 s81, s80, 65536\n\t"
        "s_lshl_b32 s80, s80, 11\n\t"
        "s_add_u32 s80, s80, %[m0b]\n\t"
        "s_add_u32 m0, s80, 16384\n\t"
        "s_add_u32 s81, s81, 1572864\n\t"
        "buffer_load_dwordx4 v98, %[so], s81 offen lds\n\t"
        "s_add_u32 m0, m0, 0x400\n\t"
        "s_add_u32 s81, s81, 32768\n\t"
        "buffer_load_dwordx4 v98, %[so], s81 offen lds\n\t"
        "s_sub_u32 s82, s82, 1\n\t"
        "s_cbranch_scc0 .Ld10%=\n\t"
        "s_waitcnt vmcnt(27)\n\t"
        "ds_read_b64_tr_b16 a[96:97], v103 offset:24576\n\t"
        "ds_read_b64_tr_b16 a[98:99], v103 offset:24704\n\t"
        "ds_read_b64_tr_b16 a[100:101], v103 offset:28672\n\t"
        "ds_read_b64_tr_b16 a[102:103], v103 offset:28800\n\t"
        "ds_read_b64_tr_b16 a[104:105], v104 offset:24576\n\t"
        "ds_read_b64_tr_b16 a[106:107], v104 offset:24704\n\t"
        "ds_read_b64_tr_b16 a[108:109], v104 offset:28672\n\t"
        "ds_read_b64_tr_b16 a[110:111], v104 offset:28800\n\t"
        "ds_read_b64_tr_b16 a[112:113], v103 offset:25088\n\t"
        "ds_read_b64_tr_b16 a[114:115], v103 offset:25216\n\t"
        "ds_read_b64_tr_b16 a[116:117], v103 offset:29184\n\t"
        "ds_read_b64_tr_b16 a[118:119], v103 offset:29312\n\t"
        "ds_read_b64_tr_b16 a[120:121], v104 offset:25088\n\t"
        "ds_read_b64_tr_b16 a[122:123], v104 offset:25216\n\t"
        "ds_read_b64_tr_b16 a[124:125], v104 offset:29184\n\t"
        "ds_read_b64_tr_b16 a[126:127], v104 offset:29312\n\t"
        "s_waitcnt lgkmcnt(0)\n\t"
        "s_mov_b32 s82, 3\n\t"
        ".Ld11%=:\n\t"
        "s_xor_b32 s80, s82, %[key]\n\t"
        "s_mul_i32 s81, s80, 65536\n\t"
        "s_lshl_b32 s80, s80, 11\n\t"
        "s_add_u32 s80, s80, %[m0b]\n\t"
        "s_add_u32 m0, s80, 24576\n\t"
        "s_add_u32 s81, s81, 1835008\n\t"
        "buffer_load_dwordx4 v98, %[so], s81 offen lds\n\t"
        "s_add_u32 m0, m0, 0x400\n\t"
        "s_add_u32 s81, s81, 32768\n\t"
        "buffer_load_dwordx4 v98, %[so], s81 offen lds\n\t"
        "s_sub_u32 s82, s82, 1\n\t"
        "s_cbranch_scc0 .Ld11%=\n\t"
        "s_waitcnt vmcnt(26)\n\t"
        "ds_read_b64_tr_b16 a[128:129], v103 offset:0\n\t"
        "ds_read_b64_tr_b16 a[130:131], v103 offset:128\n\t"
        "ds_read_b64_tr_b16 a[132:133], v103 offset:4096\n\t"
        "ds_read_b64_tr_b16 a[134:135], v103 offset:4224\n\t"
        "ds_read_b64_tr_b16 a[136:137], v104 offset:0\n\t"
        "ds_read_b64_tr_b16 a[138:139], v104 offset:128\n\t"
        "ds_read_b64_tr_b16 a[140:141], v104 offset:4096\n\t"
        "ds_read_b64_tr_b16 a[142:143], v104 offset:4224\n\t"
        "ds_read_b64_tr_b16 a[144:145], v103 offset:512\n\t"
        "ds_read_b64_tr_b16 a[146:147], v103 offset:640\n\t"
        "ds_read_b64_tr_b16 a[148:149], v103 offset:4608\n\t"
        "ds_read_b64_tr_b16 a[150:151], v103 offset:4736\n\t"
        "ds_read_b64_tr_b16 a[152:153], v104 offset:512\n\t"
        "ds_read_b64_tr_b16 a[154:155], v104 offset:640\n\t"
        "ds_read_b64_tr_b16 a[156:157], v104 offset:4608\n\t"
        "ds_read_b64_tr_b16 a[158:159], v104 offset:4736\n\t"
        "s_waitcnt lgkmcnt(0)\n\t"
        "s_waitcnt vmcnt(17)\n\t"
        "ds_read_b64_tr_b16 a[160:161], v103 offset:8192\n\t"
        "ds_read_b64_tr_b16 a[162:163], v103 offset:8320\n\t"
        "ds_read_b64_tr_b16 a[164:165], v103 offset:12288\n\t"
        "ds_read_b64_tr_b16 a[166:167], v103 offset:12416\n\t"
        "ds_read_b64_tr_b16 a[168:169], v104 offset:8192\n\t"
        "ds_read_b64_tr_b16 a[170:171], v104 offset:8320\n\t"
        "ds_read_b64_tr_b16 a[172:173], v104 offset:12288\n\t"
        "ds_read_b64_tr_b16 a[174:175], v104 offset:12416\n\t"
        "ds_read_b64_tr_b16 a[176:177], v103 offset:8704\n\t"
        "ds_read_b64_tr_b16 a[178:179], v103 offset:8832\n\t"
        "ds_read_b64_tr_b16 a[180:181], v103 offset:12800\n\t"
        "ds_read_b64_tr_b16 a[182:183], v103 offset:12928\n\t"
        "ds_read_b64_tr_b16 a[184:185], v104 offset:8704\n\t"
        "ds_read_b64_tr_b16 a[186:187], v104 offset:8832\n\t"
        "ds_read_b64_tr_b16 a[188:189], v104 offset:12800\n\t"
        "ds_read_b64_tr_b16 a[190:191], v104 offset:12928\n\t"
        "s_waitcnt lgkmcnt(0)\n\t"
        "s_waitcnt vmcnt(8)\n\t"
        "ds_read_b64_tr_b16 a[192:193], v103 offset:16384\n\t"
        "ds_read_b64_tr_b16 a[194:195], v103 offset:16512\n\t"
        "ds_read_b64_tr_b16 a[196:197], v103 offset:20480\n\t"
        "ds_read_b64_tr_b16 a[198:199], v103 offset:20608\n\t"
        "ds_read_b64_tr_b16 a[200:201], v104 offset:16384\n\t"
        "ds_read_b64_tr_b16 a[202:203], v104 offset:16512\n\t"
        "ds_read_b64_tr_b16 a[204:205], v104 offset:20480\n\t"
        "ds_read_b64_tr_b16 a[206:207], v104 offset:20608\n\t"
        "ds_read_b64_tr_b16 a[208:209], v103 offset:16896\n\t"
        "ds_read_b64_tr_b16 a[210:211], v103 offset:17024\n\t"
        "ds_read_b64_tr_b16 a[212:213], v103 offset:20992\n\t"
        "ds_read_b64_tr_b16 a[214:215], v103 offset:21120\n\t"
        "ds_read_b64_tr_b16 a[216:217], v104 offset:16896\n\t"
        "ds_read_b64_tr_b16 a[218:219], v104 offset:17024\n\t"
        "ds_read_b64_tr_b16 a[220:221], v104 offset:20992\n\t"
        "ds_read_b64_tr_b16 a[222:223], v104 offset:21120\n\t"
        "s_mov_b32 s97, 0\n\t"
        ".Lg0%=:\n\t"
        "v_cmp_ne_u32_e32 vcc, %[tag], v0\n\t"
        "v_cmp_ne_u32_e64 s[82:83], %[tag], v2\n\t"
        "s_nop 1\n\t"
        "s_or_b64 vcc, vcc, s[82:83]\n\t"
        "s_cbranch_scc0 .Lg1%=\n\t"
        "s_sleep 4\n\t"
        "buffer_load_dwordx4 v[0:3], v112, %[wsd], %[cal] offen sc1\n\t"
        "s_add_u32 s97, s97, 1\n\t"
        "s_cmp_lt_u32 s97, 0x100000\n\t"
        "s_cbranch_scc0 .Lgu%=\n\t"
        "s_waitcnt vmcnt(0)\n\t"
        "s_branch .Lg0%=\n\t"
        ".Lg1%=:\n\t"
        "s_mov_b32 s84, 0x55555555\n\t"
        "s_mov_b32 s85, s84\n\t"
        "v_and_b32_e32 v0, 1, v1\n\t"
        "v_and_b32_e32 v2, 1, v3\n\t"
        "v_cmp_ne_u32_e32 vcc, 0, v0\n\t"
        "v_cmp_ne_u32_e64 s[82:83], 0, v2\n\t"
        "s_nop 0\n\t"
        "s_and_b64 s[82:83], s[82:83], s[84:85]\n\t"
        "s_bcnt1_i32_b64 s87, s[82:83]\n\t"
        "s_and_b64 s[82:83], vcc, s[84:85]\n\t"
        "s_bcnt1_i32_b64 s86, s[82:83]\n\t"
        "s_andn2_b64 s[82:83], vcc, s[84:85]\n\t"
        "s_bcnt1_i32_b64 s88, s[82:83]\n\t"
        "v_and_b32_e32 v0, 2, v1\n\t"
        "v_and_b32_e32 v2, 2, v3\n\t"
        "v_cmp_ne_u32_e32 vcc, 0, v0\n\t"
        "v_cmp_ne_u32_e64 s[82:83], 0, v2\n\t"
        "s_nop 0\n\t"
        "s_and_b64 s[82:83], s[82:83], s[84:85]\n\t"
        "s_bcnt1_i32_b64 s90, s[82:83]\n\t"
        "s_and_b64 s[82:83], vcc, s[84:85]\n\t"
        "s_bcnt1_i32_b64 s89, s[82:83]\n\t"
        "s_andn2_b64 s[82:83], vcc, s[84:85]\n\t"
        "s_bcnt1_i32_b64 s91, s[82:83]\n\t"
        "s_add_u32 s80, s86, s87\n\t"
        "s_add_u32 s80, s80, s88\n\t"
        "s_cmp_ge_u32 s80, 48\n\t"
        "s_cselect_b32 s80, 8, 24\n\t"
        "s_cmp_gt_u32 s86, s80\n\t"
        "s_cselect_b32 s86, %c[NX1], %c[NX0]\n\t"
        "s_cmp_gt_u32 s89, 16\n\t"
        "s_cselect_b32 s81, %c[NY1], %c[NY0]\n\t"
        "s_or_b32 s86, s86, s81\n\t"
        "s_cmp_gt_u32 s87, s80\n\t"
        "s_cselect_b32 s87, %c[NX1], %c[NX0]\n\t"
        "s_cmp_gt_u32 s90, 16\n\t"
        "s_cselect_b32 s81, %c[NY1], %c[NY0]\n\t"
        "s_or_b32 s87, s87, s81\n\t"
        "s_cmp_gt_u32 s88, s80\n\t"
        "s_cselect_b32 s88, %c[NX1], %c[NX0]\n\t"
        "s_cmp_gt_u32 s91, 16\n\t"
        "s_cselect_b32 s81, %c[NY1], %c[NY0]\n\t"
        "s_or_b32 s88, s88, s81\n\t"
        "s_xor_b32 %[nA], %[nA], s86\n\t"
        "s_xor_b32 %[nW], %[nW], s86\n\t"
        "s_xor_b32 %[nC], %[nC], s87\n\t"
        "s_xor_b32 %[nS], %[nS], s88\n\t"
        "s_xor_b32 %[nT], %[nT], s88\n\t"
        "s_xor_b32 %[b0], %[b0], s88\n\t"
        "s_cmp_eq_u32 %[w], 1\n\t"
        "s_cselect_b32 s80, s87, s86\n\t"
        "s_cmp_eq_u32 %[w], 2\n\t"
        "s_cselect_b32 s80, s88, s80\n\t"
        "s_xor_b32 %[pob], %[pob], s80\n\t"
        "s_lshl_b32 s80, %[w], 10\n\t"
        "s_sub_u32 %[nS], %[nS], s80\n\t"
        "s_sub_u32 %[pob], %[pob], s80\n\t"
        "s_mov_b32 s36, 0xf0000000\n\t"
        "s_mov_b32 s38, 0xff00ff00\n\t"
        "s_mov_b32 s39, 0xff00ff00\n\t"
        "s_mov_b32 s40, 0\n\t"
        "s_mov_b32 s37, 0\n\t"
        "s_brev_b32 s54, 1\n\t"
        "s_mov_b32 s46, %c[iD1]\n\t"
        "s_mov_b32 s45, %c[iD1]\n\t"
        "s_mov_b32 s47, %c[iD2]\n\t"
        "s_mov_b32 s48, 1\n\t"
        "s_mov_b32 s50, %c[LC]\n\t"
        "s_mov_b32 s51, %c[LA]\n\t"
        "s_mov_b32 s52, %c[E1]\n\t"
        "s_mov_b32 s53, %c[E2]\n\t"
        "s_mov_b32 s43, 0\n\t"
        "s_mov_b32 s61, %[b0]\n\t"
        "s_add_u32 s62, %[b0], 0x40000\n\t"
        "s_mov_b32 s75, 162816\n\t"
        "s_waitcnt lgkmcnt(0)\n\t"
        "s_waitcnt vmcnt(0)\n\t"
        "ds_read_b64_tr_b16 a[224:225], v103 offset:24576\n\t"
        "ds_read_b64_tr_b16 a[226:227], v103 offset:24704\n\t"
        "ds_read_b64_tr_b16 a[228:229], v103 offset:28672\n\t"
        "ds_read_b64_tr_b16 a[230:231], v103 offset:28800\n\t"
        "ds_read_b64_tr_b16 a[232:233], v104 offset:24576\n\t"
        "ds_read_b64_tr_b16 a[234:235], v104 offset:24704\n\t"
        "ds_read_b64_tr_b16 a[236:237], v104 offset:28672\n\t"
        "ds_read_b64_tr_b16 a[238:239], v104 offset:28800\n\t"
        "ds_read_b64_tr_b16 a[240:241], v103 offset:25088\n\t"
        "ds_read_b64_tr_b16 a[242:243], v103 offset:25216\n\t"
        "ds_read_b64_tr_b16 a[244:245], v103 offset:29184\n\t"
        "ds_read_b64_tr_b16 a[246:247], v103 offset:29312\n\t"
        "ds_read_b64_tr_b16 a[248:249], v104 offset:25088\n\t"
        "ds_read_b64_tr_b16 a[250:251], v104 offset:25216\n\t"
        "ds_read_b64_tr_b16 a[252:253], v104 offset:29184\n\t"
        "ds_read_b64_tr_b16 a[254:255], v104 offset:29312\n\t"
        "s_waitcnt lgkmcnt(0)\n\t"
        "rec_v_code:\n\t"
        "s_and_b32 s80, s40, 3\n\t"
        "s_lshl_b32 s80, s80, 17\n\t"
        "s_add_u32 s57, s80, %[nA]\n\t"
        "s_add_u32 s58, s57, 0x1000\n\t"
        "s_add_u32 s59, s57, 0x8000\n\t"
        "s_add_u32 s60, s57, 0x9000\n\t"
        "v_mov_b32_e32 v92, -1\n\t"
        "v_mov_b32_e32 v93, -1\n\t"
        "v_mov_b32_e32 v94, -1\n\t"
        "v_mov_b32_e32 v95, -1\n\t"
        "v_mov_b32_e32 v48, 0\n\t"
        "v_mov_b32_e32 v49, 0\n\t"
        "v_mov_b32_e32 v50, 0\n\t"
        "v_mov_b32_e32 v51, 0\n\t"
        "v_mov_b32_e32 v52, 0\n\t"
        "v_mov_b32_e32 v53, 0\n\t"
        "v_mov_b32_e32 v54, 0\n\t"
        "v_mov_b32_e32 v55, 0\n\t"
        "v_mov_b32_e32 v56, 0\n\t"
        "v_mov_b32_e32 v57, 0\n\t"
        "v_mov_b32_e32 v58, 0\n\t"
        "v_mov_b32_e32 v59, 0\n\t"
        "v_mov_b32_e32 v60, 0\n\t"
        "v_mov_b32_e32 v61, 0\n\t"
        "v_mov_b32_e32 v62, 0\n\t"
        "v_mov_b32_e32 v63, 0\n\t"
        "s_barrier\n\t"
        "s_memrealtime s[76:77]\n\t"
        "s_mov_b32 s79, %[nT]\n\t"
        "v_mov_b32_e32 v86, 64\n\t"
        "s_add_u32 s61, %[b0], 0x40000\n\t"
        "s_mov_b32 s62, %[b0]\n\t"
        "s_sub_u32 s75, s75, 16\n\t"
        "s_waitcnt lgkmcnt(0)\n\t"
        "s_mov_b32 s41, s76\n\t"
        "s_mov_b32 s69, s76\n\t"
        ".p2align 6\n\t"
        ".Lloop%=:\n\t"
        "s_mov_b32 s97, 0\n\t"
        "s_movk_i32 s96, 0x1000\n\t"
        ".Lwa%=:\n\t"
        "s_memrealtime s[98:99]\n\t"
        "s_addk_i32 s96, -1\n\t"
        "s_cmp_lt_i32 s96, 0\n\t"
        "s_cbranch_scc1 .Lta%=\n\t"
        "s_waitcnt lgkmcnt(0)\n\t"
        "s_sub_i32 s98, s98, s69\n\t"
        "s_cmp_lt_i32 s98, %c[iFT]\n\t"
        "s_cbranch_scc1 .Lwa%=\n\t"
        "s_cmp_ge_i32 s98, 0\n\t"
        "s_cbranch_scc1 .Lxa%=\n\t"
        ".Lqa%=:\n\t"
        "s_nop %c[iFN]\n\t"
        "s_addk_i32 s98, 1\n\t"
        "s_cmp_lt_i32 s98, 0\n\t"
        "s_cbranch_scc1 .Lqa%=\n\t"
        ".Lxa%=:\n\t"
        "buffer_load_dwordx4 v[0:3], v112, %[srd], s57 offen sc1\n\t"
        "s_nop 7\n\t"
        "buffer_load_dwordx4 v[4:7], v112, %[srd], s58 offen sc1\n\t"
        "s_nop 7\n\t"
        "buffer_load_dwordx4 v[8:11], v112, %[srd], s59 offen sc1\n\t"
        "s_nop 7\n\t"
        "buffer_load_dwordx4 v[12:15], v112, %[srd], s60 offen sc1\n\t"
        "v_mov_b32_e32 v80, 161792\n\t"
        "ds_read_b64 v[80:81], v80\n\t"
        "s_waitcnt lgkmcnt(0)\n\t"
        "v_readfirstlane_b32 s80, v80\n\t"
        "v_readfirstlane_b32 s81, v81\n\t"
        "s_cmp_eq_u32 s80, s40\n\t"
        "s_cselect_b32 s84, 1, 0\n\t"
        "s_cmp_eq_u32 s81, s40\n\t"
        "s_cselect_b32 s81, 4, 0\n\t"
        "s_or_b32 s84, s84, s81\n\t"
        "v_mov_b32_e32 v106, s76\n\t"
        "v_mov_b32_e32 v107, s84\n\t"
        "buffer_store_dwordx2 v[106:107], v117, %[srd], s79 offen sc1\n\t"
        "s_and_b32 s80, s40, 3\n\t"
        "s_lshl_b32 s80, s80, 17\n\t"
        "s_add_u32 s63, s80, %[nC]\n\t"
        "s_add_u32 s66, s80, %[nS]\n\t"
        "s_add_u32 s81, s80, 0x20000\n\t"
        "s_and_b32 s81, s81, 0x60000\n\t"
        "s_add_u32 s67, s81, %[nW]\n\t"
        "s_add_u32 s79, s81, %[nT]\n\t"
        "s_cmp_eq_u32 %[w], 2\n\t"
        "s_cselect_b32 s81, s80, s81\n\t"
        "s_xor_b32 s81, s81, 0x40000\n\t"
        "s_add_u32 s68, s81, %[pob]\n\t"
        "s_lshl_b32 s72, s40, 17\n\t"
        "s_add_u32 s72, s72, %[kvo]\n\t"
        "s_mov_b32 s80, s61\n\t"
        "s_mov_b32 s61, s62\n\t"
        "s_mov_b32 s62, s80\n\t"
        ".Lgo%=:\n\t"
        "s_waitcnt vmcnt(4)\n\t"
        "v_smfmac_f32_16x16x64_bf16 v[52:55], v[0:3], v[136:143], v113\n\t"
        "v_smfmac_f32_16x16x64_bf16 v[56:59], v[0:3], v[144:151], v113\n\t"
        "v_smfmac_f32_16x16x64_bf16 v[60:63], v[0:3], v[152:159], v113\n\t"
        "v_smfmac_f32_16x16x64_bf16 v[48:51], v[0:3], v[128:135], v113\n\t"
        "s_waitcnt vmcnt(3)\n\t"
        "v_smfmac_f32_16x16x64_bf16 v[52:55], v[4:7], v[168:175], v113\n\t"
        "v_smfmac_f32_16x16x64_bf16 v[56:59], v[4:7], v[176:183], v113\n\t"
        "v_smfmac_f32_16x16x64_bf16 v[60:63], v[4:7], v[184:191], v113\n\t"
        "v_smfmac_f32_16x16x64_bf16 v[48:51], v[4:7], v[160:167], v113\n\t"
        "s_waitcnt vmcnt(2)\n\t"
        "v_smfmac_f32_16x16x64_bf16 v[52:55], v[8:11], v[200:207], v113\n\t"
        "v_smfmac_f32_16x16x64_bf16 v[56:59], v[8:11], v[208:215], v113\n\t"
        "v_smfmac_f32_16x16x64_bf16 v[60:63], v[8:11], v[216:223], v113\n\t"
        "v_smfmac_f32_16x16x64_bf16 v[48:51], v[8:11], v[192:199], v113\n\t"
        "s_waitcnt vmcnt(1)\n\t"
        "v_smfmac_f32_16x16x64_bf16 v[52:55], v[12:15], v[232:239], v113\n\t"
        "v_smfmac_f32_16x16x64_bf16 v[56:59], v[12:15], v[240:247], v113\n\t"
        "v_smfmac_f32_16x16x64_bf16 v[60:63], v[12:15], v[248:255], v113\n\t"
        "v_smfmac_f32_16x16x64_bf16 v[48:51], v[12:15], v[224:231], v113\n\t"
        "s_nop 1\n\t"
        "v_mov_b32_e32 v104, v104\n\t"
        "s_nop 1\n\t"
        "v_add_f32_e32 v98, v52, v54\n\t"
        "v_add_f32_e32 v99, v53, v55\n\t"
        "ds_write_b64 v123, v[98:99]\n\t"
        "v_add_f32_e32 v100, v56, v58\n\t"
        "v_add_f32_e32 v101, v57, v59\n\t"
        "ds_write_b64 v124, v[100:101]\n\t"
        "v_add_f32_e32 v102, v60, v62\n\t"
        "v_add_f32_e32 v103, v61, v63\n\t"
        "ds_write_b64 v125, v[102:103]\n\t"
        "v_pk_add_f32 v[96:97], v[48:49], v[50:51]\n\t"
        "v_cmp_u_f32_e32 vcc, v96, v97\n\t"
        "s_nop 2\n\t"
        "v_mov_b32_e32 v104, v104\n\t"
        "s_cbranch_vccnz .Lfa%=\n\t"
        ".Lra%=:\n\t"
        "v_mov_b32_e32 v104, v104\n\t"
        "s_nop 2\n\t"
        "v_mov_b32_e32 v104, v104\n\t"
        "s_nop 2\n\t"
        "v_mov_b32_e32 v104, v104\n\t"
        "s_nop 2\n\t"
        "v_mov_b32_e32 v104, v104\n\t"
        "s_waitcnt lgkmcnt(0)\n\t"
        "s_barrier\n\t"
        "v_mov_b32_e32 v104, v104\n\t"
        "ds_read_b64 v[98:99], v122 offset:512\n\t"
        "ds_read_b64 v[100:101], v122 offset:1024\n\t"
        "ds_read_b64 v[102:103], v122 offset:1536\n\t"
        "v_mov_b32_e32 v104, v104\n\t"
        "s_nop 2\n\t"
        "v_mov_b32_e32 v104, v104\n\t"
        "s_nop 2\n\t"
        "v_mov_b32_e32 v104, v104\n\t"
        "s_waitcnt lgkmcnt(2)\n\t"
        "v_pk_add_f32 v[96:97], v[96:97], v[98:99]\n\t"
        "s_waitcnt lgkmcnt(0)\n\t"
        "v_pk_add_f32 v[100:101], v[100:101], v[102:103]\n\t"
        "v_pk_add_f32 v[96:97], v[96:97], v[100:101]\n\t"
        "v_cvt_pk_bf16_f32 v80, v96, v97\n\t"
        "v_min_u32_e32 v80, -2, v80\n\t"
        "buffer_store_dword v80, v114, %[srd], s61 offen sc1\n\t"
        "s_cmp_eq_u32 s40, 63\n\t"
        "s_cbranch_scc1 .Lend%=\n\t"
        "v_mov_b32_e32 v64, 0\n\t"
        "v_mov_b32_e32 v65, 0\n\t"
        "v_mov_b32_e32 v66, 0\n\t"
        "v_mov_b32_e32 v67, 0\n\t"
        "v_mov_b32_e32 v68, 0\n\t"
        "v_mov_b32_e32 v69, 0\n\t"
        "v_mov_b32_e32 v70, 0\n\t"
        "v_mov_b32_e32 v71, 0\n\t"
        "v_mov_b32_e32 v72, 0\n\t"
        "v_mov_b32_e32 v73, 0\n\t"
        "v_mov_b32_e32 v74, 0\n\t"
        "v_mov_b32_e32 v75, 0\n\t"
        "v_mov_b32_e32 v76, 0\n\t"
        "v_mov_b32_e32 v77, 0\n\t"
        "v_mov_b32_e32 v78, 0\n\t"
        "v_mov_b32_e32 v79, 0\n\t"
        "s_xor_b32 s64, s63, %c[iCS1]\n\t"
        "s_xor_b32 s65, s63, %c[iCS2]\n\t"
        "s_xor_b32 s73, s64, %c[iCS2]\n\t"
        "s_sleep %c[VSLP]\n\t"
        "buffer_load_dwordx4 v[88:91], v112, %[srd], s66 offen sc1\n\t"
        "s_cmp_eq_u32 %[w], 0\n\t"
        "s_cselect_b32 exec_hi, 0, -1\n\t"
        "buffer_store_dwordx4 v[92:95], v112, %[srd], s68 offen sc1\n\t"
        "s_mov_b32 exec_hi, -1\n\t"
        "s_waitcnt lgkmcnt(0)\n\t"
        "v_mov_b32_e32 v48, 0\n\t"
        "v_mov_b32_e32 v49, 0\n\t"
        "v_mov_b32_e32 v50, 0\n\t"
        "v_mov_b32_e32 v51, 0\n\t"
        "v_mov_b32_e32 v52, 0\n\t"
        "v_mov_b32_e32 v53, 0\n\t"
        "v_mov_b32_e32 v54, 0\n\t"
        "v_mov_b32_e32 v55, 0\n\t"
        "v_mov_b32_e32 v56, 0\n\t"
        "v_mov_b32_e32 v57, 0\n\t"
        "v_mov_b32_e32 v58, 0\n\t"
        "v_mov_b32_e32 v59, 0\n\t"
        "v_mov_b32_e32 v60, 0\n\t"
        "v_mov_b32_e32 v61, 0\n\t"
        "v_mov_b32_e32 v62, 0\n\t"
        "v_mov_b32_e32 v63, 0\n\t"
        "s_add_u32 s81, s40, 1\n\t"
        "s_and_b32 s80, s81, 3\n\t"
        "s_lshl_b32 s80, s80, 17\n\t"
        "s_add_u32 s57, s80, %[nA]\n\t"
        "s_add_u32 s58, s57, 0x1000\n\t"
        "s_add_u32 s59, s57, 0x8000\n\t"
        "s_add_u32 s60, s57, 0x9000\n\t"
        "s_waitcnt vmcnt(1)\n\t"
        "s_mov_b32 s97, 0\n\t"
        ".Lsr%=:\n\t"
        "s_mov_b32 exec_hi, 0xffff\n\t"
        "v_subrev_u32_e32 v4, s41, v88\n\t"
        "v_subrev_u32_e32 v5, s41, v90\n\t"
        "v_max_i32_e32 v4, v4, v5\n\t"
        "v_max_u32_e32 v5, v89, v91\n\t"
        "v_cmp_eq_u32_e32 vcc, -1, v5\n\t"
        "v_max_i32_dpp v4, v4, v4 quad_perm:[1,0,3,2] row_mask:0xf bank_mask:0xf\n\t"
        "v_or_b32_e32 v6, v89, v91\n\t"
        "s_nop 0\n\t"
        "v_max_i32_dpp v4, v4, v4 quad_perm:[2,3,0,1] row_mask:0xf bank_mask:0xf\n\t"
        "s_nop 1\n\t"
        "v_max_i32_dpp v4, v4, v4 row_ror:4 row_mask:0xf bank_mask:0xf\n\t"
        "s_nop 1\n\t"
        "v_max_i32_dpp v4, v4, v4 row_ror:8 row_mask:0xf bank_mask:0xf\n\t"
        "s_cbranch_vccnz .Lsf%=\n\t"
        "v_readlane_b32 s85, v4, 0\n\t"
        "v_readlane_b32 s81, v4, 16\n\t"
        "v_readlane_b32 s56, v4, 32\n\t"
        "v_and_b32_e32 v5, 1, v6\n\t"
        "v_cmp_ne_u32_e64 s[86:87], 0, v5\n\t"
        "v_and_b32_e32 v5, 2, v6\n\t"
        "v_cmp_ne_u32_e64 s[88:89], 0, v5\n\t"
        "v_and_b32_e32 v5, 4, v6\n\t"
        "v_cmp_ne_u32_e64 s[90:91], 0, v5\n\t"
        "s_max_i32 s56, s56, s81\n\t"
        "s_add_u32 s92, s85, s41\n\t"
        "s_sub_u32 s92, s92, s43\n\t"
        "s_add_i32 s93, s56, s47\n\t"
        "s_mov_b32 s44, s46\n\t"
        "s_mov_b32 s46, s45\n\t"
        "s_mov_b32 s94, s48\n\t"
        "s_mov_b32 s43, s41\n\t"
        "s_cmp_gt_i32 s56, s54\n\t"
        "s_cbranch_scc1 .Lmv%=\n\t"
        "s_mov_b32 s37, 0\n\t"
        "s_mov_b32 s48, 0\n\t"
        "s_mov_b32 s95, 0\n\t"
        ".Lsd%=:\n\t"
        "s_mov_b32 exec_hi, -1\n\t"
        "s_add_u32 s42, s43, s46\n\t"
        "s_mov_b32 s97, 0\n\t"
        "s_movk_i32 s96, 0x1000\n\t"
        ".Lwc%=:\n\t"
        "s_memrealtime s[98:99]\n\t"
        "s_addk_i32 s96, -1\n\t"
        "s_cmp_lt_i32 s96, 0\n\t"
        "s_cbranch_scc1 .Ltc%=\n\t"
        "s_waitcnt lgkmcnt(0)\n\t"
        "s_sub_i32 s98, s98, s42\n\t"
        "s_cmp_lt_i32 s98, %c[iFT]\n\t"
        "s_cbranch_scc1 .Lwc%=\n\t"
        "s_cmp_ge_i32 s98, 0\n\t"
        "s_cbranch_scc1 .Lxc%=\n\t"
        ".Lqc%=:\n\t"
        "s_nop %c[iFN]\n\t"
        "s_addk_i32 s98, 1\n\t"
        "s_cmp_lt_i32 s98, 0\n\t"
        "s_cbranch_scc1 .Lqc%=\n\t"
        ".Lxc%=:\n\t"
        "buffer_load_dwordx4 v[16:19], v112, %[srd], s63 offen sc1\n\t"
        "s_cmp_ge_u32 s40, %c[iCT0]\n\t"
        "s_cselect_b32 s85, 1, 0\n\t"
        "s_cmp_lg_u64 s[86:87], 0\n\t"
        "s_cselect_b32 s86, s85, 0\n\t"
        "s_cmp_lg_u64 s[88:89], 0\n\t"
        "s_cselect_b32 s87, s85, 0\n\t"
        "s_cmp_lg_u64 s[90:91], 0\n\t"
        "s_cselect_b32 s88, s85, 0\n\t"
        "buffer_load_dwordx4 v[20:23], v115, %[srd], s63 offen sc1\n\t"
        "s_or_b32 s80, s94, s86\n\t"
        "s_or_b32 s80, s80, s87\n\t"
        "s_andn2_b32 s90, s85, s80\n\t"
        "s_cmp_le_i32 s92, s44\n\t"
        "s_cselect_b32 s80, s85, 0\n\t"
        "s_andn2_b32 s91, s80, s88\n\t"
        "s_and_b32 s80, s40, %c[LDEC]\n\t"
        "s_cselect_b32 s89, 0, 1\n\t"
        "buffer_load_dwordx4 v[24:27], v112, %[srd], s64 offen sc1\n\t"
        "s_mov_b32 s80, s88\n\t"
        "s_add_i32 s81, s92, s50\n\t"
        "s_sub_i32 s81, s81, 4\n\t"
        "s_cmp_le_i32 s81, s44\n\t"
        "s_cselect_b32 s80, s80, 0\n\t"
        "s_cmp_lt_i32 s50, %c[LC24]\n\t"
        "s_cselect_b32 s80, s80, 0\n\t"
        "s_lshl2_add_u32 s50, s80, s50\n\t"
        "buffer_load_dwordx4 v[28:31], v115, %[srd], s64 offen sc1\n\t"
        "s_sub_i32 s50, s50, s89\n\t"
        "s_max_i32 s50, s50, %c[LC]\n\t"
        "s_lshl_b32 s80, s92, 1\n\t"
        "s_sub_i32 s81, s80, s52\n\t"
        "s_ashr_i32 s82, s81, %c[UPSH]\n\t"
        "s_max_i32 s82, s82, %c[TRUP]\n\t"
        "s_add_i32 s82, s82, s52\n\t"
        "s_min_i32 s82, s82, s80\n\t"
        "buffer_load_dwordx4 v[32:35], v112, %[srd], s65 offen sc1\n\t"
        "s_ashr_i32 s83, s81, 1\n\t"
        "s_min_i32 s83, s83, -%c[TRDN]\n\t"
        "s_add_i32 s83, s83, s52\n\t"
        "s_max_i32 s83, s83, s80\n\t"
        "s_cmp_gt_i32 s81, 0\n\t"
        "s_cselect_b32 s82, s82, s83\n\t"
        "s_cmp_lg_u32 s90, 0\n\t"
        "s_cselect_b32 s52, s82, s52\n\t"
        "buffer_load_dwordx4 v[36:39], v115, %[srd], s65 offen sc1\n\t"
        "s_add_i32 s80, s52, 1\n\t"
        "s_ashr_i32 s80, s80, 1\n\t"
        "s_add_i32 s45, s80, s50\n\t"
        "s_andn2_b32 s80, s86, s94\n\t"
        "s_cmp_lt_i32 s51, %c[LA24]\n\t"
        "s_cselect_b32 s80, s80, 0\n\t"
        "s_lshl2_add_u32 s51, s80, s51\n\t"
        "s_sub_i32 s51, s51, s89\n\t"
        "buffer_load_dwordx4 v[40:43], v112, %[srd], s73 offen sc1\n\t"
        "s_max_i32 s51, s51, %c[LA]\n\t"
        "s_lshl_b32 s80, s93, 1\n\t"
        "s_sub_i32 s81, s80, s53\n\t"
        "s_ashr_i32 s82, s81, %c[UPSH]\n\t"
        "s_max_i32 s82, s82, %c[TRUP]\n\t"
        "s_add_i32 s82, s82, s53\n\t"
        "s_min_i32 s82, s82, s80\n\t"
        "s_ashr_i32 s83, s81, 1\n\t"
        "buffer_load_dwordx4 v[44:47], v115, %[srd], s73 offen sc1\n\t"
        "s_min_i32 s83, s83, -%c[TRDN]\n\t"
        "s_add_i32 s83, s83, s53\n\t"
        "s_max_i32 s83, s83, s80\n\t"
        "s_cmp_gt_i32 s81, 0\n\t"
        "s_cselect_b32 s82, s82, s83\n\t"
        "s_cmp_lg_u32 s91, 0\n\t"
        "s_cselect_b32 s53, s82, s53\n\t"
        "s_add_i32 s80, s53, 1\n\t"
        "s_ashr_i32 s80, s80, 1\n\t"
        "s_add_i32 s47, s80, s51\n\t"
        "s_add_u32 s81, s47, %c[AUP]\n\t"
        "s_min_i32 s80, s95, s81\n\t"
        "s_max_i32 s47, s47, s80\n\t"
        "s_add_u32 s41, s42, s47\n\t"
        ".Lrc%=:\n\t"
        "s_waitcnt vmcnt(7)\n\t"
        "v_smfmac_f32_16x16x64_bf16 v[64:67], v[16:19], a[0:7], v113\n\t"
        "v_smfmac_f32_16x16x64_bf16 v[68:71], v[16:19], a[8:15], v113\n\t"
        "v_smfmac_f32_16x16x64_bf16 v[72:75], v[16:19], a[16:23], v113\n\t"
        "v_smfmac_f32_16x16x64_bf16 v[76:79], v[16:19], a[24:31], v113\n\t"
        "s_waitcnt vmcnt(6)\n\t"
        "v_smfmac_f32_16x16x64_bf16 v[64:67], v[20:23], a[32:39], v113\n\t"
        "v_smfmac_f32_16x16x64_bf16 v[68:71], v[20:23], a[40:47], v113\n\t"
        "v_smfmac_f32_16x16x64_bf16 v[72:75], v[20:23], a[48:55], v113\n\t"
        "v_smfmac_f32_16x16x64_bf16 v[76:79], v[20:23], a[56:63], v113\n\t"
        "s_waitcnt vmcnt(5)\n\t"
        "v_smfmac_f32_16x16x64_bf16 v[64:67], v[24:27], a[64:71], v113\n\t"
        "v_smfmac_f32_16x16x64_bf16 v[68:71], v[24:27], a[72:79], v113\n\t"
        "v_smfmac_f32_16x16x64_bf16 v[72:75], v[24:27], a[80:87], v113\n\t"
        "v_smfmac_f32_16x16x64_bf16 v[76:79], v[24:27], a[88:95], v113\n\t"
        "s_waitcnt vmcnt(4)\n\t"
        "v_smfmac_f32_16x16x64_bf16 v[64:67], v[28:31], a[96:103], v113\n\t"
        "v_smfmac_f32_16x16x64_bf16 v[68:71], v[28:31], a[104:111], v113\n\t"
        "v_smfmac_f32_16x16x64_bf16 v[72:75], v[28:31], a[112:119], v113\n\t"
        "v_smfmac_f32_16x16x64_bf16 v[76:79], v[28:31], a[120:127], v113\n\t"
        "s_waitcnt vmcnt(3)\n\t"
        "v_smfmac_f32_16x16x64_bf16 v[64:67], v[32:35], a[128:135], v113\n\t"
        "v_smfmac_f32_16x16x64_bf16 v[68:71], v[32:35], a[136:143], v113\n\t"
        "v_smfmac_f32_16x16x64_bf16 v[72:75], v[32:35], a[144:151], v113\n\t"
        "v_smfmac_f32_16x16x64_bf16 v[76:79], v[32:35], a[152:159], v113\n\t"
        "s_waitcnt vmcnt(2)\n\t"
        "v_smfmac_f32_16x16x64_bf16 v[64:67], v[36:39], a[160:167], v113\n\t"
        "v_smfmac_f32_16x16x64_bf16 v[68:71], v[36:39], a[168:175], v113\n\t"
        "v_smfmac_f32_16x16x64_bf16 v[72:75], v[36:39], a[176:183], v113\n\t"
        "v_smfmac_f32_16x16x64_bf16 v[76:79], v[36:39], a[184:191], v113\n\t"
        "s_waitcnt vmcnt(1)\n\t"
        "v_smfmac_f32_16x16x64_bf16 v[64:67], v[40:43], a[192:199], v113\n\t"
        "v_smfmac_f32_16x16x64_bf16 v[68:71], v[40:43], a[200:207], v113\n\t"
        "v_smfmac_f32_16x16x64_bf16 v[72:75], v[40:43], a[208:215], v113\n\t"
        "v_smfmac_f32_16x16x64_bf16 v[76:79], v[40:43], a[216:223], v113\n\t"
        "s_waitcnt vmcnt(0)\n\t"
        "v_smfmac_f32_16x16x64_bf16 v[64:67], v[44:47], a[224:231], v113\n\t"
        "s_nop 2\n\t"
        "v_smfmac_f32_16x16x64_bf16 v[68:71], v[44:47], a[232:239], v113\n\t"
        "s_cmp_gt_u32 %[w], 2\n\t"
        "s_nop 1\n\t"
        "v_smfmac_f32_16x16x64_bf16 v[72:75], v[44:47], a[240:247], v113\n\t"
        "v_add_f32_e32 v80, v64, v66\n\t"
        "s_nop 0\n\t"
        "v_smfmac_f32_16x16x64_bf16 v[76:79], v[44:47], a[248:255], v113\n\t"
        "v_add_f32_e32 v81, v65, v67\n\t"
        "v_add_f32_e32 v82, v68, v70\n\t"
        "v_add_f32_e32 v83, v69, v71\n\t"
        "s_mov_b32 exec_lo, 0\n\t"
        "s_nop 0\n\t"
        "v_add_f32_e32 v80, v72, v74\n\t"
        "v_add_f32_e32 v81, v73, v75\n\t"
        "s_nop 0\n\t"
        "v_pk_add_f32 v[82:83], v[76:77], v[78:79]\n\t"
        "s_mov_b32 exec_lo, -1\n\t"
        "s_nop 0\n\t"
        "v_permlane32_swap_b32_e32 v80, v82\n\t"
        "v_add_f32_e32 v80, v80, v82\n\t"
        "ds_write_b32 v119, v80\n\t"
        "v_permlane32_swap_b32_e32 v81, v83\n\t"
        "v_add_f32_e32 v81, v81, v83\n\t"
        "ds_write_b32 v119, v81 offset:128\n\t"
        "v_cmp_u_f32_e32 vcc, v80, v81\n\t"
        "s_nop 2\n\t"
        "v_mov_b32_e32 v104, v104\n\t"
        "s_cbranch_vccnz .Lfc%=\n\t"
        ".Lrd%=:\n\t"
        "v_mov_b32_e32 v104, v104\n\t"
        "s_cbranch_scc1 .Lno%=\n\t"
        "v_mov_b32_e32 v104, v104\n\t"
        "s_nop 2\n\t"
        "v_mov_b32_e32 v104, v104\n\t"
        "s_waitcnt lgkmcnt(0)\n\t"
        "s_barrier\n\t"
        "v_mov_b32_e32 v104, v104\n\t"
        "ds_read_b64 v[0:1], v120 offset:8192\n\t"
        "ds_read_b64 v[2:3], v120 offset:8704\n\t"
        "ds_read_b64 v[4:5], v120 offset:9216\n\t"
        "ds_read_b64 v[6:7], v120 offset:9728\n\t"
        "v_mov_b32_e32 v104, v104\n\t"
        "s_nop 2\n\t"
        "v_mov_b32_e32 v104, v104\n\t"
        "s_nop 2\n\t"
        "v_mov_b32_e32 v104, v104\n\t"
        "s_waitcnt lgkmcnt(2)\n\t"
        "v_pk_add_f32 v[0:1], v[0:1], v[2:3]\n\t"
        "s_waitcnt lgkmcnt(0)\n\t"
        "v_pk_add_f32 v[4:5], v[4:5], v[6:7]\n\t"
        "v_pk_add_f32 v[0:1], v[0:1], v[4:5]\n\t"
        "v_cvt_pk_bf16_f32 v0, v0, v1\n\t"
        "v_min_u32_e32 v0, -2, v0\n\t"
        "buffer_store_dword v0, v116, %[srd], s67 offen sc1\n\t"
        ".Lnp%=:\n\t"
        "s_memrealtime s[76:77]\n\t"
        "s_add_u32 s40, s40, 1\n\t"
        "s_waitcnt lgkmcnt(0)\n\t"
        "s_add_u32 s81, s76, %c[iVD]\n\t"
        "s_sub_u32 s80, s76, s41\n\t"
        "s_cmp_gt_i32 s80, %c[iTOL]\n\t"
        "s_cselect_b32 s69, s81, s41\n\t"
        "s_branch .Lloop%=\n\t"
        ".Lmv%=:\n\t"
        "s_mov_b32 s48, 1\n\t"
        "s_cmp_eq_u32 s54, %c[iTOL]\n\t"
        "s_cbranch_scc1 .Lm1%=\n\t"
        "s_max_i32 s80, s85, s56\n\t"
        "s_add_u32 s43, s41, s80\n\t"
        "s_add_u32 s43, s43, %c[iPEN0]\n\t"
        "s_mov_b32 s54, %c[iTOL]\n\t"
        "s_add_u32 s46, s46, %c[iX0C]\n\t"
        "s_add_u32 s95, s47, %c[iX0A]\n\t"
        "v_and_b32_e32 v4, 3, v88\n\t"
        "v_and_b32_e32 v5, 3, v90\n\t"
        "v_lshlrev_b32_e64 v4, v4, 1\n\t"
        "v_lshlrev_b32_e64 v5, v5, 1\n\t"
        "v_or_b32_e32 v4, v4, v5\n\t"
        "s_mov_b32 s80, 0\n\t"
        "v_and_b32_e32 v5, 1, v4\n\t"
        "v_cmp_ne_u32_e32 vcc, 0, v5\n\t"
        "s_nop 1\n\t"
        "s_cmp_lg_u64 vcc, 0\n\t"
        "s_cselect_b32 s81, 2, 0\n\t"
        "s_or_b32 s80, s80, s81\n\t"
        "v_and_b32_e32 v5, 2, v4\n\t"
        "v_cmp_ne_u32_e32 vcc, 0, v5\n\t"
        "s_nop 1\n\t"
        "s_cmp_lg_u64 vcc, 0\n\t"
        "s_cselect_b32 s81, 4, 0\n\t"
        "s_or_b32 s80, s80, s81\n\t"
        "v_and_b32_e32 v5, 4, v4\n\t"
        "v_cmp_ne_u32_e32 vcc, 0, v5\n\t"
        "s_nop 1\n\t"
        "s_cmp_lg_u64 vcc, 0\n\t"
        "s_cselect_b32 s81, 8, 0\n\t"
        "s_or_b32 s80, s80, s81\n\t"
        "v_and_b32_e32 v5, 8, v4\n\t"
        "v_cmp_ne_u32_e32 vcc, 0, v5\n\t"
        "s_nop 1\n\t"
        "s_cmp_lg_u64 vcc, 0\n\t"
        "s_cselect_b32 s81, 16, 0\n\t"
        "s_or_b32 s80, s80, s81\n\t"
        "s_lshr_b32 s80, 0x1adf1210, s80\n\t"
        "s_and_b32 s55, s80, 3\n\t"
        "s_branch .Lal%=\n\t"
        ".Lm1%=:\n\t"
        "s_add_u32 s80, s37, %c[iPEN2]\n\t"
        "s_min_i32 s80, s80, %c[iPENM]\n\t"
        "s_cmp_lg_u32 s37, 0\n\t"
        "s_cselect_b32 s37, s80, %c[iPEN]\n\t"
        "s_add_u32 s80, s56, s37\n\t"
        "s_max_i32 s80, s80, %c[iPMIN]\n\t"
        "s_add_u32 s43, s41, s80\n\t"
        "s_add_u32 s95, s93, %c[LA]\n\t"
        "s_add_u32 s46, s46, %c[iXR]\n\t"
        ".Lal%=:\n\t"
        "s_sub_u32 s80, s55, s43\n\t"
        "s_and_b32 s80, s80, %c[iPHM]\n\t"
        "s_add_u32 s43, s43, s80\n\t"
        "s_branch .Lsd%=\n\t"
        ".Lsf%=:\n\t"
        "s_mov_b32 exec_hi, -1\n\t"
        "s_sleep 2\n\t"
        "buffer_load_dwordx4 v[88:91], v112, %[srd], s66 offen sc1\n\t"
        "s_add_u32 s97, s97, 1\n\t"
        "s_cmp_lt_u32 s97, 0x100000\n\t"
        "s_cbranch_scc0 .Lgu%=\n\t"
        "s_waitcnt vmcnt(0)\n\t"
        "s_branch .Lsr%=\n\t"
        ".Lfa%=:\n\t"
        "v_max_u32_e32 v84, v0, v2\n\t"
        "v_max3_u32 v84, v84, v1, v3\n\t"
        "v_max3_u32 v84, v84, v4, v6\n\t"
        "v_max3_u32 v84, v84, v5, v7\n\t"
        "v_max3_u32 v84, v84, v8, v10\n\t"
        "v_max3_u32 v84, v84, v9, v11\n\t"
        "v_max3_u32 v84, v84, v12, v14\n\t"
        "v_max3_u32 v84, v84, v13, v15\n\t"
        "v_cmp_eq_u32_e32 vcc, -1, v84\n\t"
        "s_nop 1\n\t"
        "v_mov_b32_e32 v104, v104\n\t"
        "s_nop 0\n\t"
        "s_cbranch_vccz .Lra%=\n\t"
        "s_add_u32 s80, s40, 1\n\t"
        "v_mov_b32_e32 v80, 161792\n\t"
        "v_mov_b32_e32 v81, s80\n\t"
        "ds_write_b32 v80, v81\n\t"
        ".Lfp%=:\n\t"
        "s_sleep %c[ASLP]\n\t"
        "buffer_load_dwordx4 v[0:3], v112, %[srd], s57 offen sc1\n\t"
        "buffer_load_dwordx4 v[4:7], v112, %[srd], s58 offen sc1\n\t"
        "buffer_load_dwordx4 v[8:11], v112, %[srd], s59 offen sc1\n\t"
        "buffer_load_dwordx4 v[12:15], v112, %[srd], s60 offen sc1\n\t"
        "s_add_u32 s97, s97, 1\n\t"
        "s_cmp_lt_u32 s97, 0x100000\n\t"
        "s_cbranch_scc0 .Lgu%=\n\t"
        "s_waitcnt vmcnt(0)\n\t"
        "v_max_u32_e32 v84, v0, v2\n\t"
        "v_max3_u32 v84, v84, v1, v3\n\t"
        "v_max3_u32 v84, v84, v4, v6\n\t"
        "v_max3_u32 v84, v84, v5, v7\n\t"
        "v_max3_u32 v84, v84, v8, v10\n\t"
        "v_max3_u32 v84, v84, v9, v11\n\t"
        "v_max3_u32 v84, v84, v12, v14\n\t"
        "v_max3_u32 v84, v84, v13, v15\n\t"
        "v_cmp_eq_u32_e32 vcc, -1, v84\n\t"
        "s_nop 1\n\t"
        "v_mov_b32_e32 v104, v104\n\t"
        "s_nop 0\n\t"
        "s_cbranch_vccnz .Lfp%=\n\t"
        "v_mov_b32_e32 v48, 0\n\t"
        "v_mov_b32_e32 v49, 0\n\t"
        "v_mov_b32_e32 v50, 0\n\t"
        "v_mov_b32_e32 v51, 0\n\t"
        "v_mov_b32_e32 v52, 0\n\t"
        "v_mov_b32_e32 v53, 0\n\t"
        "v_mov_b32_e32 v54, 0\n\t"
        "v_mov_b32_e32 v55, 0\n\t"
        "v_mov_b32_e32 v56, 0\n\t"
        "v_mov_b32_e32 v57, 0\n\t"
        "v_mov_b32_e32 v58, 0\n\t"
        "v_mov_b32_e32 v59, 0\n\t"
        "v_mov_b32_e32 v60, 0\n\t"
        "v_mov_b32_e32 v61, 0\n\t"
        "v_mov_b32_e32 v62, 0\n\t"
        "v_mov_b32_e32 v63, 0\n\t"
        "s_nop 1\n\t"
        "s_branch .Lgo%=\n\t"
        ".Lfc%=:\n\t"
        "v_max_u32_e32 v84, v16, v18\n\t"
        "v_max3_u32 v84, v84, v20, v22\n\t"
        "v_max3_u32 v84, v84, v24, v26\n\t"
        "v_max3_u32 v84, v84, v28, v30\n\t"
        "v_max3_u32 v84, v84, v32, v34\n\t"
        "v_max3_u32 v84, v84, v36, v38\n\t"
        "v_max3_u32 v84, v84, v40, v42\n\t"
        "v_max3_u32 v84, v84, v44, v46\n\t"
        "v_cmp_eq_u32_e32 vcc, -1, v84\n\t"
        "s_nop 1\n\t"
        "v_mov_b32_e32 v104, v104\n\t"
        "s_nop 0\n\t"
        "s_cbranch_vccz .Lrd%=\n\t"
        "s_add_u32 s80, s40, 1\n\t"
        "v_mov_b32_e32 v80, 161796\n\t"
        "v_mov_b32_e32 v81, s80\n\t"
        "ds_write_b32 v80, v81\n\t"
        ".Lfq%=:\n\t"
        "s_sleep %c[ASLP]\n\t"
        "buffer_load_dwordx4 v[16:19], v112, %[srd], s63 offen sc1\n\t"
        "buffer_load_dwordx4 v[20:23], v115, %[srd], s63 offen sc1\n\t"
        "buffer_load_dwordx4 v[24:27], v112, %[srd], s64 offen sc1\n\t"
        "buffer_load_dwordx4 v[28:31], v115, %[srd], s64 offen sc1\n\t"
        "buffer_load_dwordx4 v[32:35], v112, %[srd], s65 offen sc1\n\t"
        "buffer_load_dwordx4 v[36:39], v115, %[srd], s65 offen sc1\n\t"
        "buffer_load_dwordx4 v[40:43], v112, %[srd], s73 offen sc1\n\t"
        "buffer_load_dwordx4 v[44:47], v115, %[srd], s73 offen sc1\n\t"
        "s_add_u32 s97, s97, 1\n\t"
        "s_cmp_lt_u32 s97, 0x100000\n\t"
        "s_cbranch_scc0 .Lgu%=\n\t"
        "s_waitcnt vmcnt(0)\n\t"
        "v_max_u32_e32 v84, v16, v18\n\t"
        "v_max3_u32 v84, v84, v20, v22\n\t"
        "v_max3_u32 v84, v84, v24, v26\n\t"
        "v_max3_u32 v84, v84, v28, v30\n\t"
        "v_max3_u32 v84, v84, v32, v34\n\t"
        "v_max3_u32 v84, v84, v36, v38\n\t"
        "v_max3_u32 v84, v84, v40, v42\n\t"
        "v_max3_u32 v84, v84, v44, v46\n\t"
        "v_cmp_eq_u32_e32 vcc, -1, v84\n\t"
        "s_nop 1\n\t"
        "v_mov_b32_e32 v104, v104\n\t"
        "s_nop 0\n\t"
        "s_cbranch_vccnz .Lfq%=\n\t"
        "v_mov_b32_e32 v64, 0\n\t"
        "v_mov_b32_e32 v65, 0\n\t"
        "v_mov_b32_e32 v66, 0\n\t"
        "v_mov_b32_e32 v67, 0\n\t"
        "v_mov_b32_e32 v68, 0\n\t"
        "v_mov_b32_e32 v69, 0\n\t"
        "v_mov_b32_e32 v70, 0\n\t"
        "v_mov_b32_e32 v71, 0\n\t"
        "v_mov_b32_e32 v72, 0\n\t"
        "v_mov_b32_e32 v73, 0\n\t"
        "v_mov_b32_e32 v74, 0\n\t"
        "v_mov_b32_e32 v75, 0\n\t"
        "v_mov_b32_e32 v76, 0\n\t"
        "v_mov_b32_e32 v77, 0\n\t"
        "v_mov_b32_e32 v78, 0\n\t"
        "v_mov_b32_e32 v79, 0\n\t"
        "s_nop 1\n\t"
        "s_branch .Lrc%=\n\t"
        ".Lno%=:\n\t"
        "s_waitcnt lgkmcnt(0)\n\t"
        "s_barrier\n\t"
        "s_branch .Lnp%=\n\t"
        ".Lta%=:\n\t"
        "s_waitcnt lgkmcnt(0)\n\t"
        "s_branch .Lxa%=\n\t"
        ".Ltc%=:\n\t"
        "s_waitcnt lgkmcnt(0)\n\t"
        "s_branch .Lxc%=\n\t"
        ".Lgu%=:\n\t"
        "s_mov_b64 exec, -1\n\t"
        "v_mov_b32_e32 v80, %c[ferr]\n\t"
        "v_mov_b32_e32 v81, 2\n\t"
        "buffer_store_dword v81, v80, %[wsd], 0 offen sc1\n\t"
        "s_endpgm\n\t"
        ".Lend%=:\n\t"
        "buffer_store_dword v80, v114, %[wsd], s72 offen sc1\n\t"
        "s_waitcnt vmcnt(0)\n\t"
        "s_barrier\n\t"
        "buffer_store_dword v86, v118, %[wsd], 0 offen sc1\n\t"
        "s_endpgm"
        : [nA] "+s"(nA), [nS] "+s"(nS), [nT] "+s"(nT), [nW] "+s"(nW), [nC] "+s"(nC), [b0] "+s"(b0), [pob] "+s"(pob)
        : [srd] "s"(rhd), [wsd] "s"(wsd), [w] "s"(w), [tag] "s"(tag), [kvo] "s"(kvo), [cal] "s"(cal), [sv] "s"(sv), [so] "s"(so), [m0b] "s"(m0b), [key] "s"(key), [tu] "s"(tu),
          [ferr] "i"(F_ERR), [iD1] "i"(D1), [iD2] "i"(D2), [LC] "i"(LC0), [LA] "i"(LA0), [LC24] "i"(LC0 + 24), [LA24] "i"(LA0 + 24), [E1] "i"(2 * (D1 - LC0)), [E2] "i"(2 * (D2 - LA0)),
          [iPEN] "i"(PEN), [iPEN2] "i"(PEN2), [iPENM] "i"(PEN + 2 * PEN2), [iPEN0] "i"(PEN0), [iTOL] "i"(TOL), [iCT0] "i"(CT0), [iX0C] "i"(X0C), [iX0A] "i"(X0A), [iPHM] "i"(PH_M),
          [iFT] "i"(-3), [iFN] "i"(FINE_N), [AUP] "i"(A_UP), [ASLP] "i"(A_SLP), [LDEC] "i"(L_DEC), [UPSH] "i"(UP_SH), [TRUP] "i"(TR_UP), [TRDN] "i"(TR_DN), [HSLP] "i"(H_SLP), [iPMIN] "i"(PMIN), [VSLP] "i"(V_SLP), [RSLP] "i"(R_SLP), [iVD] "i"(V_DEL), [iXR] "i"(XR), [NX0] "i"(NEARX), [NX1] "i"(8192 - NEARX), [iCS1] "i"(CS1), [iCS2] "i"(CS2), [NY0] "i"(NEARY), [NY1] "i"(16384 - NEARY),
          "{v[112:127]}"(pc), "{v[96:103]}"(pa), "{v[104:105]}"(pb)
        : "memory", "vcc", "scc", "m0", "s36", "s37", "s38", "s39", "s40", "s41", "s42", "s43", "s44", "s45", "s46", "s47", "s48", "s49", "s50", "s51", "s52", "s53", "s54", "s55", "s56", "s57", "s58", "s59", "s60", "s61", "s62", "s63", "s64", "s65", "s66", "s67", "s68", "s69", "s70", "s71", "s72", "s73", "s74", "s75", "s76", "s77", "s78", "s79", "s80", "s81", "s82", "s83", "s84", "s85", "s86", "s87", "s88", "s89", "s90", "s91", "s92", "s93", "s94", "s95", "s96", "s97", "s98", "s99");
    }
}

using f32x16 = __attribute__((ext_vector_type(16))) float;
constexpr int CV_RP = 80;
constexpr int CV_U = 96 * CV_RP;
constexpr int CV_KP = 4416;
constexpr int CV_K0 = 8 * CV_U;
__device__ __forceinline__ float lo16(unsigned x) { return __uint_as_float(x << 16); }
__device__ __forceinline__ float hi16(unsigned x) { return __uint_as_float(x & 0xFFFF0000u); }
__device__ __forceinline__ void prep_block(const Params& p, rsrc_t r, int k4, const int wv, const unsigned tag) {
    const int tid = otid(), w = wv, l = tid & 63;
    const int cu = (k4 & 3) * 32 + w * 8 + (l >> 3), oc0 = (k4 >> 2) * 8 + (l & 7);
    const rsrc_t rk = __builtin_amdgcn_make_buffer_rsrc((void*)p.Ak, 0, S * D * 2, 0x00020000), rv = __builtin_amdgcn_make_buffer_rsrc((void*)p.Av, 0, S * D * 2, 0x00020000);
    v4u sk[4], kn[11], vn[8], gw[2];
    auto fetch = [&](int oc) {
#pragma unroll
        for (int j = 0; j < 11; ++j) kn[j] = __builtin_amdgcn_raw_buffer_load_b128(rk, (unsigned)((oc * 8 + j - 3) * 2048 + cu * 16), 0, 2);
#pragma unroll
        for (int i = 0; i < 8; ++i) vn[i] = __builtin_amdgcn_raw_buffer_load_b128(rv, (unsigned)((oc * 8 + i) * 2048 + cu * 16), 0, 2); };
    fetch(oc0);
#pragma unroll
    for (int q = 0; q < 4; ++q) sk[q] = *(const v4u*)(p.shift_k + cu * 32 + q * 8);
#pragma unroll
    for (int q = 0; q < 2; ++q) gw[q] = *(const v4u*)(p.gate_w + q * 512 + l * 8);
    st4(r, tid < 128 ? F_RDY + k4 * 512 + tid * 4 : tid < 132 ? F_ATT + (k4 * 4 + tid - 128) * 64 : (tid == 192 && k4 == 0) ? F_CONV : 0xF0000000u, 0u);
#pragma unroll 1
    for (int it = 0; it < 4; ++it) {
        const int oc = oc0 + 64 * it, tk = (it * 32 + k4) * 16 + w;
        v4u kr[11], vr[8], xg[4][2];
#pragma unroll
        for (int j = 0; j < 11; ++j) kr[j] = kn[j];
#pragma unroll
        for (int i = 0; i < 8; ++i) vr[i] = vn[i];
        __builtin_amdgcn_sched_barrier(0);
#pragma unroll
        for (int i = 0; i < 4; ++i)
#pragma unroll
            for (int q = 0; q < 2; ++q) xg[i][q] = *(const v4u*)(p.Aq + (size_t)(tk + 4 * i) * D + q * 512 + l * 8);
        fetch(it < 3 ? oc + 64 : 4096);
        __builtin_amdgcn_sched_barrier(0);
#pragma unroll
        for (int c8 = 0; c8 < 8; ++c8) {
            const unsigned s01 = sk[c8 >> 1][(c8 & 1) * 2], s23 = sk[c8 >> 1][(c8 & 1) * 2 + 1];
            const float sv[4] = {hi16(s23), lo16(s23), hi16(s01), lo16(s01)};
            float kk[11], u[8];
#pragma unroll
            for (int j = 0; j < 11; ++j) kk[j] = (c8 & 1) ? hi16(kr[j][c8 >> 1]) : lo16(kr[j][c8 >> 1]);
            f32x2 acc[4];
#pragma unroll
            for (int j = 0; j < 4; ++j)
#pragma unroll
                for (int i = 0; i < 4; ++i) { if (FASTP) { const f32x2 t = (f32x2){kk[i + j], kk[i + j + 4]} * sv[j]; acc[i] = j == 0 ? t : j == 3 ? rbf2(acc[i] + t) : acc[i] + t; }
                    else { const f32x2 t = rbf2((f32x2){kk[i + j], kk[i + j + 4]} * sv[j]); acc[i] = j == 0 ? t : rbf2(acc[i] + t); } }
#pragma unroll
            for (int i = 0; i < 8; ++i) u[i] = acc[i & 3][i >> 2] * ((c8 & 1) ? hi16(vr[i][c8 >> 1]) : lo16(vr[i][c8 >> 1]));
            st16(r, W_U + (cu * 8 + c8) * 4096 + oc * 16, (v4u){pack2(u[7], u[6]), pack2(u[5], u[4]), pack2(u[3], u[2]), pack2(u[1], u[0])});
        }
        float ga[4];
#pragma unroll
        for (int i = 0; i < 4; ++i) { float acc = 0.f;
#pragma unroll
            for (int q = 0; q < 2; ++q)
#pragma unroll
                for (int e = 0; e < 4; ++e) { const unsigned xa = xg[i][q][e], gb = gw[q][e]; acc += rbfh(lo16(xa) * lo16(gb)); acc += rbfh(hi16(xa) * hi16(gb)); }
            ga[i] = acc; }
        for (int o = 32; o > 0; o >>= 1)
#pragma unroll
            for (int i = 0; i < 4; ++i) ga[i] += __shfl_xor(ga[i], o);
        const float z = rbfh(l == 0 ? ga[0] : l == 1 ? ga[1] : l == 2 ? ga[2] : ga[3]);
        st4(r, l < 4 ? W_GATE + (tk + 4 * l) * 4 : 0xF0000000u, __float_as_uint(rbfh(1.f / (1.f + expf(-z)))));
    }
    DRAIN(); __syncthreads();
    if (tid == 0) st4(r, F_PREP + k4 * 4, tag);
}
__device__ __forceinline__ bool conv_block(const Params& p, rsrc_t r, int k4, unsigned char* lds, const int wv, const int j, const unsigned tag) {
    (void)j;
    const int tid = otid(), w = wv, l = tid & 63;
    const rsrc_t rs = __builtin_amdgcn_make_buffer_rsrc((void*)p.ssm_k, 0, S * D * 2, 0x00020000);
    v4u kraw[2][4], uv[8]; unsigned dn;
    auto fetch_k = [&](int d0) {
#pragma unroll
        for (int ci = 0; ci < 2; ++ci)
#pragma unroll
            for (int i = 0; i < 4; ++i) kraw[ci][i] = __builtin_amdgcn_raw_buffer_load_b128(rs, (unsigned)((d0 + 2 * w + ci) * 4096 + (i * 64 + l) * 16), 0, 2);
        dn = *(const unsigned*)(p.ssm_D + (d0 & 1023) + 2 * w); };
    auto fetch_u = [&](unsigned o) {
#pragma unroll
        for (int i = 0; i < 8; ++i) uv[i] = ld16(r, o + i * 4096 + tid * 16); };
    fetch_k(k4 * 32);
    if (!wait_prep(r, l, tag)) return false;
    if (SKEWH) wait_long(rtnow() + (unsigned)k4 * SKEWH);
    fetch_u(W_U + k4 * 32 * 4096);
#pragma unroll 1
    for (int it = 0; it < 4; ++it) {
    const int bb = k4 * 4 + it; const unsigned dd = dn;
    { const v4u z = {0u, 0u, 0u, 0u};
#pragma unroll
      for (int i = 0; i < 5; ++i) { const int q = tid + 256 * i; *(v4u*)(lds + (q / 160) * CV_U + (q % 160) * 16) = z; } }
#pragma unroll
    for (int i = 0; i < 8; ++i) *(v4u*)(lds + i * CV_U + (32 + (tid >> 2)) * CV_RP + (tid & 3) * 16) = uv[i];
    unsigned char* kc = lds + CV_K0 + w * 4 * CV_KP;
    auto copies = [&](int ci) {
        if (l < 32) *(unsigned*)(kc + l * 4) = 0u;
        if (l < 48) *(unsigned*)(kc + 128 + 4096 + l * 4) = 0u;
#pragma unroll
        for (int i = 0; i < 4; ++i) *(v4u*)(kc + 128 + (i * 64 + l) * 16) = kraw[ci][i];
        __builtin_amdgcn_wave_barrier();
#pragma unroll
        for (int it = 0; it < 9; ++it) { const int i = min(l + 64 * it, CV_KP / 8 - 2);
            const v2u e01 = *(const v2u*)(kc + i * 8), e23 = *(const v2u*)(kc + i * 8 + 8);
            const unsigned s0 = (e01.x >> 16) | (e01.y << 16), s1 = (e01.y >> 16) | (e23.x << 16), s2 = (e23.x >> 16) | (e23.y << 16);
            *(v2u*)(kc + CV_KP + i * 8) = (v2u){s0, s1};
            *(v2u*)(kc + 2 * CV_KP + i * 8) = (v2u){e01.y, e23.x};
            *(v2u*)(kc + 3 * CV_KP + i * 8) = (v2u){s1, s2}; }
        __builtin_amdgcn_wave_barrier(); };
    copies(0);
    __syncthreads();
    const int b = l & 31, kg = l >> 5, cc = (b + 1) & 3;
    float ys[32];
#pragma unroll
    for (int ci = 0; ci < 2; ++ci) {
        const int c8 = 2 * w + ci;
        if (ci == 1) { copies(1); __builtin_amdgcn_sched_barrier(0); fetch_k(it < 3 ? bb * 8 + 8 : 1024); fetch_u(it < 3 ? W_U + (bb * 8 + 8) * 4096 : 0xF0000000u); __builtin_amdgcn_sched_barrier(0); }
        f32x16 acc0, acc1, ad0, ad1;
#pragma unroll
        for (int i = 0; i < 16; ++i) { acc0[i] = 0.f; acc1[i] = 0.f; ad0[i] = 0.f; ad1[i] = 0.f; }
        typedef const __attribute__((address_space(3))) v2u* l2p;
        unsigned kb = (unsigned)(size_t)(kc + cc * CV_KP + (b - 8 * kg - 7 + 64 - cc) * 2) - 32, kb8 = kb + 8;
        asm volatile("" : "+v"(kb), "+v"(kb8));
        const unsigned char* ub = lds + c8 * CV_U + (32 + b) * CV_RP + kg * 16;
        v2u ka[4][2], kz[4][2]; bf16x8 fa[4], fz[4];
        auto ld_k = [&](int set, int un, int hi) { const int dl = un >> 1, kh = un & 1; const unsigned o = (unsigned)((32 * dl - 16 * kh + 16) * 2 + hi * 2048);
            if (hi) { kz[set][0] = *(l2p)(size_t)(kb + o); kz[set][1] = *(l2p)(size_t)(kb8 + o); } else { ka[set][0] = *(l2p)(size_t)(kb + o); ka[set][1] = *(l2p)(size_t)(kb8 + o); } };
        auto ld_f = [&](int set, int un) { const int dl = un >> 1, kh = un & 1;
            fa[set] = *(const bf16x8*)(ub + (32 - dl) * CV_RP + kh * 32); fz[set] = *(const bf16x8*)(ub - dl * CV_RP + kh * 32); };
#pragma unroll
        for (int q = 0; q < 2; ++q) { ld_k(q, q, 0); ld_f(q, q); ld_k(q, q, 1); }
        {
            const unsigned dv = ci ? dd >> 16 : dd & 0xFFFFu;
#pragma unroll
            for (int kh = 0; kh < 2; ++kh) { const int e = 7 - (b - 16 * kh - 8 * kg); const unsigned x = dv << ((e & 1) * 16); const int q = e >> 1;
                const v4u di = {q == 0 ? x : 0u, q == 1 ? x : 0u, q == 2 ? x : 0u, q == 3 ? x : 0u};
                asm volatile("s_nop 1\n\tv_mfma_f32_32x32x16_bf16 %0, %2, %4, %0\n\tv_mfma_f32_32x32x16_bf16 %1, %3, %4, %1" : "+v"(ad1), "+v"(ad0) : "v"(fa[kh]), "v"(fz[kh]), "v"(di)); }
        }
#pragma unroll 1
        for (int u0 = 0; u0 < 64; u0 += 4) {
#pragma unroll
            for (int q = 0; q < 4; ++q) {
                const int un = u0 + q + 2, ns = (q + 2) & 3;
                const v4u kav = {ka[q][0].x, ka[q][0].y, ka[q][1].x, ka[q][1].y}, kzv = {kz[q][0].x, kz[q][0].y, kz[q][1].x, kz[q][1].y};
                __builtin_amdgcn_sched_barrier(0);
                asm volatile("s_nop 1\n\tv_mfma_f32_32x32x16_bf16 %0, %1, %2, %0" : "+v"(acc1) : "v"(fa[q]), "v"(kav));
                ld_k(ns, un, 0);
                __builtin_amdgcn_sched_barrier(0);
                asm volatile("v_mfma_f32_32x32x16_bf16 %0, %1, %2, %0" : "+v"(acc0) : "v"(fz[q]), "v"(kav));
                ld_f(ns, un);
                __builtin_amdgcn_sched_barrier(0);
                asm volatile("v_mfma_f32_32x32x16_bf16 %0, %1, %2, %0" : "+v"(acc1) : "v"(fz[q]), "v"(kzv));
                ld_k(ns, un, 1);
            }
        }
        asm volatile("s_nop 7\n\ts_nop 7" : "+v"(acc0), "+v"(acc1), "+v"(ad0), "+v"(ad1));
#pragma unroll
        for (int i = 0; i < 32; ++i) { const float y = rbfh(i < 16 ? acc0[i] : acc1[i - 16]) + rbfh(i < 16 ? ad0[i] : ad1[i - 16]);
            ys[i] = ci == 0 ? y : __uint_as_float(pack2(ys[i], y)); }
    }
    __syncthreads();
#pragma unroll
    for (int i = 0; i < 32; ++i) { const int row = (i >> 4) * 32 + (i & 3) + 8 * ((i & 15) >> 2) + 4 * kg; *(float*)(lds + (row * 32 + b) * 16 + w * 4) = ys[i]; }
    __syncthreads();
#pragma unroll
    for (int i = 0; i < 8; ++i) { const int t = tid + 256 * i; st16(r, W_A + bb * 32768 + t * 16, *(const v4u*)(lds + t * 16)); }
    __syncthreads();
    }
    DRAIN(); __syncthreads();
    if (tid == 0) __hip_atomic_fetch_add((unsigned*)(p.ws + F_CONV), 1u, __ATOMIC_RELAXED, __HIP_MEMORY_SCOPE_AGENT);
    return true;
}

__device__ __forceinline__ void dma16(unsigned ldsdst, unsigned voff, const v4u& srd, unsigned soff) {
    asm volatile("s_mov_b32 m0, %0\n\ts_nop 0\n\tbuffer_load_dwordx4 %1, %2, %3 offen lds" :: "s"(ldsdst), "v"(voff), "s"(srd), "s"(soff) : "m0", "memory"); }
__device__ __forceinline__ void dma16n(unsigned ldsdst, unsigned voff, const v4u& srd, unsigned soff) {
    asm volatile("s_mov_b32 m0, %0\n\ts_nop 0\n\tbuffer_load_dwordx4 %1, %2, %3 offen nt lds" :: "s"(ldsdst), "v"(voff), "s"(srd), "s"(soff) : "m0", "memory"); }
__device__ __forceinline__ v4u pack_acc(const f32x4& a, const f32x4& b) {
    float x0, x1, x2, x3, y0, y1, y2, y3;
    asm volatile("v_accvgpr_read_b32 %0, %8\n\tv_accvgpr_read_b32 %1, %9\n\tv_accvgpr_read_b32 %2, %10\n\tv_accvgpr_read_b32 %3, %11\n\t"
                 "v_accvgpr_read_b32 %4, %12\n\tv_accvgpr_read_b32 %5, %13\n\tv_accvgpr_read_b32 %6, %14\n\tv_accvgpr_read_b32 %7, %15"
                 : "=v"(x0), "=v"(x1), "=v"(x2), "=v"(x3), "=v"(y0), "=v"(y1), "=v"(y2), "=v"(y3)
                 : "a"(a[0]), "a"(a[1]), "a"(a[2]), "a"(a[3]), "a"(b[0]), "a"(b[1]), "a"(b[2]), "a"(b[3]));
    return (v4u){pack2(x0, x1), pack2(x2, x3), pack2(y0, y1), pack2(y2, y3)}; }
#define SB() __builtin_amdgcn_sched_barrier(0)
#define WO_REGS "a128", "a129", "a130", "a131", "a132", "a133", "a134", "a135", "a136", "a137", "a138", "a139", "a140", "a141", "a142", "a143", "a144", "a145", "a146", "a147", "a148", "a149", "a150", "a151", "a152", "a153", "a154", "a155", "a156", "a157", "a158", "a159", "a160", "a161", "a162", "a163", "a164", "a165", "a166", "a167", "a168", "a169", "a170", "a171", "a172", "a173", "a174", "a175", "a176", "a177", "a178", "a179", "a180", "a181", "a182", "a183", "a184", "a185", "a186", "a187", "a188", "a189", "a190", "a191", "a192", "a193", "a194", "a195", "a196", "a197", "a198", "a199", "a200", "a201", "a202", "a203", "a204", "a205", "a206", "a207", "a208", "a209", "a210", "a211", "a212", "a213", "a214", "a215", "a216", "a217", "a218", "a219", "a220", "a221", "a222", "a223", "a224", "a225", "a226", "a227", "a228", "a229", "a230", "a231", "a232", "a233", "a234", "a235", "a236", "a237", "a238", "a239", "a240", "a241", "a242", "a243", "a244", "a245", "a246", "a247", "a248", "a249", "a250", "a251", "a252", "a253", "a254", "a255", "v192", "v193", "v194", "v195", "v196", "v197", "v198", "v199", "v200", "v201", "v202", "v203", "v204", "v205", "v206", "v207", "v208", "v209", "v210", "v211", "v212", "v213", "v214", "v215", "v216", "v217", "v218", "v219", "v220", "v221", "v222", "v223", "v224", "v225", "v226", "v227", "v228", "v229", "v230", "v231", "v232", "v233", "v234", "v235", "v236", "v237", "v238", "v239", "v240", "v241", "v242", "v243", "v244", "v245", "v246", "v247", "v248", "v249", "v250", "v251", "v252", "v253", "v254", "v255"
struct NoHook { __device__ void operator()(int) const {} };
template <int MODE, int MI, int NJ, int NS, int HX = 0, class HOOK = NoHook, int MN = MI>
__device__ __forceinline__ void gemm(rsrc_t r, const bf16_t* __restrict__ A, const bf16_t* __restrict__ B, int m0, int n0, unsigned char* lds, unsigned coff, const int wv,
                                     const int rot, const void* tk = nullptr,
                                     const bool have = false, const bf16_t* Bn = nullptr, int m0n = 0, int n0n = 0, HOOK&& hook = NoHook()) {
    constexpr int AB = MN * 4096, BB = NJ * 2048, B0 = (NS / 2) * AB, NB = NJ / 2, NM = MI * NJ, NR = MI + NJ, BP = NJ * 64;
    static_assert(NS % 2 == 0 && 32 % NS == 0 && B0 <= 65536 && NS * BB <= 65536 && (NJ == 4 || NJ == 8), "ring");
    const int tid = otid();
    const int w = wv, l = tid & 63, n16 = l & 15, g = l >> 4;
    const int wm = w >> 1, wn = w & 1;
    const unsigned lb = __builtin_amdgcn_readfirstlane((unsigned)(size_t)lds);
    const v4u sa = mk_srd(A, 2048 * 1024 * 2); v4u sb = mk_srd(B, 1024 * 4096 * 2);
    f32x4 acc[MI][NJ];
#pragma unroll
    for (int i = 0; i < MI; ++i)
#pragma unroll
        for (int j = 0; j < NJ; ++j) acc[i][j] = (f32x4){0.f, 0.f, 0.f, 0.f};
    unsigned va = (unsigned)((m0 + 8 * MI * w + (l >> 3)) * 2048 + (((l & 7) ^ (l >> 3)) << 4));
    unsigned vb[2];
    if (NJ == 8) { vb[0] = (unsigned)((8 * w + (l >> 5)) * 8192 + n0 * 2) + (unsigned)(((l & 31) << 4) ^ ((l >> 5) << 5) ^ (wn << 7)); vb[1] = vb[0] ^ 64u; }
    else { vb[0] = (unsigned)((8 * w + g) * 8192 + n0 * 2) + (unsigned)((n16 << 4) ^ (g << 5) ^ (wn << 7)); vb[1] = vb[0]; }
    auto rk = [&](int kt) { return (kt & ~31) | ((kt + rot) & 31); };
    unsigned la = lb + w * MI * 1024;
    auto st_a = [&](int buf, int kt, int q) { dma16(la + buf * AB + q * 1024, va, sa, (unsigned)(rk(kt) * 64 + q * 16384)); };
    auto st_b = [&](int buf, int kt, int x) { if ((B_NT >> (NJ == 4 ? 0 : MI == 4 ? 1 : 2)) & 1) dma16n(lb + B0 + buf * BB + (w * NB + x) * 1024, vb[x & 1], sb, (unsigned)((rk(kt) * 32 + x * (8 / NB)) * 8192));
        else dma16(lb + B0 + buf * BB + (w * NB + x) * 1024, vb[x & 1], sb, (unsigned)((rk(kt) * 32 + x * (8 / NB)) * 8192)); };
    const unsigned f = (unsigned)((n16 >> 2) | ((g & 1) << 2));
    constexpr int NQ = MODE == 1 ? NJ : NJ / 2;
    unsigned ra[2], rb[NQ];
    ra[0] = lb + (16 * MI * wm + n16) * 128 + ((g ^ (n16 & 7)) << 4); ra[1] = ra[0] ^ 64u;
    {   const unsigned row = lb + B0 + (8 * g + (n16 >> 2)) * BP + (NJ == 8 ? 256 * wn : 0), g0 = NJ == 8 ? 0u : 4u * wn;
#pragma unroll
        for (int j = 0; j < NQ; ++j) {
            if (MODE == 1) rb[j] = row + (((g0 + j) ^ f) << 5) + 8 * (n16 & 3);
            else rb[j] = row + (((g0 + 2 * j + ((n16 & 3) >> 1)) ^ f) << 5) + (n16 & 1) * 16;
            asm volatile("" : "+v"(rb[j])); } }
    bf16x8 af[2][MI], bfr[2][NJ];
    auto rd = [&](int set, int buf, int x) {
        if (x < NJ) { const unsigned a = rb[MODE == 1 ? x : (x >> 1)] + (MODE == 1 ? 0 : (x & 1) * 8) + buf * BB;
            const v2u lo = trb(a), hi = trb(a + 4 * BP); v4u v = {lo.x, lo.y, hi.x, hi.y}; bfr[set][x] = as_frag(v); }
        else af[set][x - NJ] = ldsa(ra[buf & 1] + (buf >> 1) * AB + (x - NJ) * 2048); };
    auto mf = [&](int set, int i, int j) {
        if (HX) asm volatile("v_mfma_f32_16x16x32_bf16 %0, %1, %2, %0" : "+a"(acc[i][j]) : "v"(MODE == 1 ? af[set][i] : bfr[set][j]), "v"(MODE == 1 ? bfr[set][j] : af[set][i]) : WO_REGS);
        else if (MODE == 1) asm volatile("v_mfma_f32_16x16x32_bf16 %0, %1, %2, %0" : "+a"(acc[i][j]) : "v"(af[set][i]), "v"(bfr[set][j]));
        else asm volatile("v_mfma_f32_16x16x32_bf16 %0, %1, %2, %0" : "+a"(acc[i][j]) : "v"(bfr[set][j]), "v"(af[set][i])); };
    if (!have) {
#pragma unroll
        for (int t = 0; t < NS; ++t) {
            if (t & 1) {
#pragma unroll
                for (int q = 0; q < MI; ++q) st_a(t >> 1, t - 1, q); }
#pragma unroll
            for (int q = 0; q < NB; ++q) st_b(t, t, q);
            if (HX) hook(t); }
    }
#pragma unroll
    for (int i = 0; i < MI; ++i)
#pragma unroll
        for (int j = 0; j < NJ; ++j) asm volatile("" : "+a"(acc[i][j]));
    asm volatile("s_nop 1" ::: "memory");
    if (!have) {
        asm volatile("s_waitcnt vmcnt(%c0)\n\ts_barrier" :: "i"((NS - 1) * (NB + HX) + (NS / 2 - 1) * MI) : "memory");
    }
#pragma unroll
    for (int x = 0; x < NR; ++x) rd(0, 0, x);
    constexpr int HEAD = NM / 8;
    const v4u st = mk_srd(tk, tk ? 32768 : 0); unsigned tdum = 0u;
#pragma unroll 1
    for (int k0 = 0; k0 < 32; k0 += NS) {
        int kb = k0 + NS;
        if (kb == 32) {
            kb = Bn ? 0 : 0x10100;
            if (Bn) { va += (unsigned)((m0n - m0 + 8 * (MN - MI) * w) * 2048); la = lb + w * MN * 1024; vb[0] += (unsigned)((n0n - n0) * 2); vb[1] += (unsigned)((n0n - n0) * 2); sb = mk_srd(Bn, 1024 * 4096 * 2); } }
        if (NS == 4) asm volatile("buffer_load_dword %0, %1, %2, %3 offen" : "+v"(tdum) : "v"(l < 32 ? (unsigned)l * 128u : 0xF0000000u), "s"(st), "s"((unsigned)k0 * 1024u) : "memory");
#pragma unroll
        for (int u = 0; u < NS; ++u) {
            SB();
#pragma unroll
            for (int q = 0; q < HEAD; ++q) mf(u & 1, q / NJ, q % NJ);
            SB();
            asm volatile("s_waitcnt vmcnt(%c0) lgkmcnt(0)\n\ts_barrier" :: "i"((NS - 2) * (NB + HX) + (NS / 2 - 1 - (u & 1)) * MI) : "memory");
            SB();
            constexpr int span = NM - HEAD;
#pragma unroll
            for (int q = HEAD; q < NM; ++q) {
                mf(u & 1, q / NJ, q % NJ);
                const int x = q - HEAD, nd = NB + ((u & 1) ? MI : 0);
#pragma unroll
                for (int y = 0; y < NR; ++y) if ((y * (span - HEAD)) / NR == x) rd((u & 1) ^ 1, (u + 1) % NS, y);
#pragma unroll
                for (int y = 0; y < nd; ++y) if ((y * span + span / 2) / nd == x) { if ((u & 1) && y < MI) st_a(u >> 1, kb + u - 1, y); else st_b(u, kb + u, y - ((u & 1) ? MI : 0)); }
                SB(); }
            if (MN > MI && (u & 1) && k0 == 32 - NS) {
#pragma unroll
                for (int y = MI; y < MN; ++y) st_a(u >> 1, kb + u - 1, y); }
            if (HX) hook(NS + k0 + u);
        }
    }
    asm volatile("s_nop 7\n\ts_nop 3" : "+v"(tdum) :: "memory");
    SB();
    const int col = n0 + 16 * NJ * wn, head = col >> 7, sub = (col & 127) >> 4, seg0 = (m0 >> 5) + (MI / 2) * wm;
#pragma unroll
    for (int sg = 0; sg < MI / 2; ++sg) {
        const unsigned base = coff + (unsigned)(((seg0 + sg) * 32 + head) * 8192 + l * 16);
#pragma unroll
        for (int q = 0; q < NJ; ++q) {
            if (HX) asm volatile("" ::: WO_REGS);
            if (MODE == 0) { const int tt = q / (NJ / 2), kk = q % (NJ / 2); st16(r, base + (tt * 4 + (sub >> 1) + kk) * 1024, pack_acc(acc[2 * sg + tt][2 * kk], acc[2 * sg + tt][2 * kk + 1])); }
            else st16(r, base + (sub + q) * 1024, pack_acc(acc[2 * sg][q], acc[2 * sg + 1][q])); }
    }
}

__device__ __forceinline__ unsigned min4(unsigned v) { return min(min(__builtin_amdgcn_readlane(v, 0), __builtin_amdgcn_readlane(v, 1)), min(__builtin_amdgcn_readlane(v, 2), __builtin_amdgcn_readlane(v, 3))); }
__device__ __forceinline__ void attn_block(const Params& p, rsrc_t r, unsigned char* lds, const int wv, const int h, const int j, const unsigned tag) {
    (void)j;
    const int l = otid() & 63, n16 = l & 15, g = l >> 4;
    const unsigned OOB = 0xF0000000u;
    v4u qv[2][4], kv[2][4], va[8];
    int nev = 0; (void)nev;
    auto ld_q = [&](unsigned o) {
#pragma unroll
        for (int i = 0; i < 8; ++i) qv[i >> 2][i & 3] = ld16(r, W_QX + o + i * 1024); };
    auto ld_k = [&](unsigned o) {
#pragma unroll
        for (int i = 0; i < 8; ++i) kv[i >> 2][i & 3] = ld16(r, W_KX + o + i * 1024); };
    auto ld_v = [&](unsigned o) {
#pragma unroll
        for (int i = 0; i < 8; ++i) va[i] = ld16(r, W_VF + o + i * 1024); };
    auto count = [&](int seg, int qm) -> unsigned { unsigned old = 0u;
        if ((l & 31) == 0 && ((qm >> (l >> 5)) & 1)) old = __hip_atomic_fetch_add((unsigned*)(p.ws + F_ATT + (2 * seg + (l >> 5)) * 64), 1u, __ATOMIC_RELAXED, __HIP_MEMORY_SCOPE_AGENT);
        return old; };
    auto tell = [&](int seg, unsigned old) { const int tt = 2 * seg + (l >> 5);
        const unsigned long long last = __ballot(old == 31u);
        const int gr = tt < 120 ? (tt >> 1) & 3 : tt & 3, wd = tt < 120 ? (tt >> 3) * 2 + (tt & 1) : tt >> 2;
        st4(r, ((last >> (l & 32)) & 1ull) ? F_RDY + ((gr * 32 + (l & 31)) * 32 + wd) * 4 : OOB, 32u); };
    unsigned prog = 0u, tseen = rtnow() - 100000u;
    unsigned long long tiles = 0ull;
    bool have = false; int pend = -1;
    const int nit = wv == 0 ? 17 : 16;
    auto task = [&](int it) -> int { return it == 16 ? 62 : (it == 15 && wv == 2) ? 63 : 4 * it + wv; };
#pragma unroll 1
    for (int it = 0; it < nit; ++it) {
        const int seg = task(it), qm = seg == 63 ? 1 << (wv & 1) : 3;
        if (!have) {
            for (unsigned spin = 0; ((tiles >> (seg & 60)) & 15ull) != 15ull; ++spin) { MEMBAR();
                if (spin) __builtin_amdgcn_s_sleep(16);
                tiles = __ballot(ld4(r, F_KVQ + ((l >> 2) * 32 + h) * 64 + min(l & 3, 2) * 4) == tag);
                if (spin > (1u << 20)) { st4(r, F_ERR, 64u); return; } }
            MEMBAR();
            const unsigned o = (unsigned)((seg * 32 + h) * 8192 + l * 16);
            ld_k(o); ld_q(o); ld_v(o);
        }
        const bool early = prog > (unsigned)seg;
        v4u k0, k1; v2u pm[8];
        auto ld_m = [&](unsigned km) {
            k0 = ld16(r, km + l * 16); k1 = ld16(r, km + 1024 + l * 16);
#pragma unroll
            for (int t = 0; t < 8; ++t) pm[t] = ld8(r, km + 2048 + t * 256 + n16 * 16 + (g & 1) * 8); };
        ld_m(early ? HO_KVM + (seg * 32 + h) * 4096 : OOB);
        f32x4 sc[2][3]; float mx[2];
#pragma unroll
        for (int qt = 0; qt < 2; ++qt)
            asm volatile("s_nop 1\n\t"
                "v_mfma_f32_16x16x32_bf16 %0, %2, %10, 0\n\tv_mfma_f32_16x16x32_bf16 %1, %6, %10, 0\n\tv_mfma_f32_16x16x32_bf16 %0, %3, %11, %0\n\tv_mfma_f32_16x16x32_bf16 %1, %7, %11, %1\n\t"
                "v_mfma_f32_16x16x32_bf16 %0, %4, %12, %0\n\tv_mfma_f32_16x16x32_bf16 %1, %8, %12, %1\n\tv_mfma_f32_16x16x32_bf16 %0, %5, %13, %0\n\tv_mfma_f32_16x16x32_bf16 %1, %9, %13, %1"
                : "=&v"(sc[qt][0]), "=&v"(sc[qt][1])
                : "v"(kv[0][0]), "v"(kv[0][1]), "v"(kv[0][2]), "v"(kv[0][3]), "v"(kv[1][0]), "v"(kv[1][1]), "v"(kv[1][2]), "v"(kv[1][3]), "v"(qv[qt][0]), "v"(qv[qt][1]), "v"(qv[qt][2]), "v"(qv[qt][3]));
        asm volatile("s_nop 7\n\ts_waitcnt vmcnt(0)" : "+v"(sc[0][0]), "+v"(sc[0][1]), "+v"(sc[1][0]), "+v"(sc[1][1]), "+v"(k0), "+v"(k1), "+v"(pm[0]), "+v"(pm[1]), "+v"(pm[2]), "+v"(pm[3]), "+v"(pm[4]), "+v"(pm[5]), "+v"(pm[6]), "+v"(pm[7]),
                     "+v"(va[0]), "+v"(va[1]), "+v"(va[2]), "+v"(va[3]), "+v"(va[4]), "+v"(va[5]), "+v"(va[6]), "+v"(va[7]) :: "memory");
        const unsigned po = count(pend, pend >= 0 ? 3 : 0);
        const int sn = task(it + 1);
        have = it + 1 < nit && ((tiles >> (sn & 60)) & 15ull) == 15ull;
        const unsigned on = have ? (unsigned)((sn * 32 + h) * 8192 + l * 16) : OOB;
        ld_k(on);
#pragma unroll
        for (int qt = 0; qt < 2; ++qt) { const int qi = 16 * qt + n16; float m = -INFINITY;
#pragma unroll
            for (int t = 0; t < 2; ++t)
#pragma unroll
                for (int rr = 0; rr < 4; ++rr) { const float v = (16 * t + 4 * g + rr <= qi) ? rbfh(rbfh(sc[qt][t][rr]) * 0.08838834764831845f) : -1000000000.0f; sc[qt][t][rr] = v; m = fmaxf(m, v); }
            mx[qt] = m; }
        tell(pend, po);
        if (!early) {
            for (unsigned spin = 0; prog <= (unsigned)seg; ++spin) {
                const unsigned now = rtnow();
                if (spin > (1u << 22)) { st4(r, F_ERR, 32u); return; }
                if (prog + (seg >= EARLY_FROM ? 2u : 1u) + (now - tseen) / STEP_MIN <= (unsigned)seg) { __builtin_amdgcn_s_sleep(16); continue; }
                MEMBAR();
                const unsigned pg = min4(ld4(r, F_PROG + h * 128 + min(l, 2) * 8));
                if (pg != prog) { prog = pg; tseen = now; }
                if (prog <= (unsigned)seg) __builtin_amdgcn_s_sleep(P_SLP);
            }
            MEMBAR();
            ld_m(HO_KVM + (seg * 32 + h) * 4096);
            asm volatile("" : "+v"(k0), "+v"(k1), "+v"(pm[0]), "+v"(pm[1]), "+v"(pm[2]), "+v"(pm[3]), "+v"(pm[4]), "+v"(pm[5]), "+v"(pm[6]), "+v"(pm[7]));
        }
        v4u kmv[4];
        *(v4u*)(lds + wv * 2048 + l * 16) = k0; *(v4u*)(lds + wv * 2048 + 1024 + l * 16) = k1;
        __builtin_amdgcn_wave_barrier();
#pragma unroll
        for (int ks = 0; ks < 4; ++ks) { const unsigned a = (unsigned)(size_t)lds + wv * 2048 + (32 * ks + 8 * g + (n16 >> 2)) * 16 + (n16 & 1) * 8; const v2u x0 = trb(a), x1 = trb(a + 64); kmv[ks] = (v4u){x0.x, x0.y, x1.x, x1.y}; }
        asm volatile("s_nop 1\n\t"
            "v_mfma_f32_16x16x32_bf16 %0, %2, %6, 0\n\tv_mfma_f32_16x16x32_bf16 %1, %2, %10, 0\n\tv_mfma_f32_16x16x32_bf16 %0, %3, %7, %0\n\tv_mfma_f32_16x16x32_bf16 %1, %3, %11, %1\n\t"
            "v_mfma_f32_16x16x32_bf16 %0, %4, %8, %0\n\tv_mfma_f32_16x16x32_bf16 %1, %4, %12, %1\n\tv_mfma_f32_16x16x32_bf16 %0, %5, %9, %0\n\tv_mfma_f32_16x16x32_bf16 %1, %5, %13, %1\n\t"
            "s_nop 2\n\tv_mov_b32 %14, %14\n\ts_nop 2\n\tv_mov_b32 %14, %14"
            : "=&v"(sc[0][2]), "=&v"(sc[1][2])
            : "v"(kmv[0]), "v"(kmv[1]), "v"(kmv[2]), "v"(kmv[3]), "v"(qv[0][0]), "v"(qv[0][1]), "v"(qv[0][2]), "v"(qv[0][3]), "v"(qv[1][0]), "v"(qv[1][1]), "v"(qv[1][2]), "v"(qv[1][3]), "v"(l));
        v4u pk[2][4];
#pragma unroll
        for (int qt = 0; qt < 2; ++qt) {
            if (!((qm >> qt) & 1)) continue;
            float m = mx[qt];
#pragma unroll
            for (int rr = 0; rr < 4; ++rr) { const float v = g >= 2 ? -INFINITY : rbfh(rbfh(sc[qt][2][rr]) * 0.08838834764831845f); sc[qt][2][rr] = v; m = fmaxf(m, v); }
            m = xor16_max(m); m = xor32_max(m); float sum = 0.f;
#pragma unroll
            for (int t = 0; t < 3; ++t)
#pragma unroll
                for (int rr = 0; rr < 4; ++rr) { const float e = __expf(sc[qt][t][rr] - m); sc[qt][t][rr] = e; sum += e; }
            sum = xor16_add(sum); sum = xor32_add(sum); const float inv = __builtin_amdgcn_rcpf(sum);
            const v4u p0 = {pack2(sc[qt][0][0] * inv, sc[qt][0][1] * inv), pack2(sc[qt][0][2] * inv, sc[qt][0][3] * inv), pack2(sc[qt][1][0] * inv, sc[qt][1][1] * inv), pack2(sc[qt][1][2] * inv, sc[qt][1][3] * inv)};
            const v2u p1 = {pack2(sc[qt][2][0] * inv, sc[qt][2][1] * inv), pack2(sc[qt][2][2] * inv, sc[qt][2][3] * inv)};
            f32x4 oa[8];
            asm volatile("s_nop 1\n\t"
                "v_mfma_f32_16x16x32_bf16 %0, %8, %24, 0\n\tv_mfma_f32_16x16x32_bf16 %1, %9, %24, 0\n\tv_mfma_f32_16x16x32_bf16 %2, %10, %24, 0\n\tv_mfma_f32_16x16x32_bf16 %3, %11, %24, 0\n\t"
                "v_mfma_f32_16x16x32_bf16 %4, %12, %24, 0\n\tv_mfma_f32_16x16x32_bf16 %5, %13, %24, 0\n\tv_mfma_f32_16x16x32_bf16 %6, %14, %24, 0\n\tv_mfma_f32_16x16x32_bf16 %7, %15, %24, 0\n\t"
                "v_mfma_f32_16x16x16_bf16 %0, %16, %25, %0\n\tv_mfma_f32_16x16x16_bf16 %1, %17, %25, %1\n\tv_mfma_f32_16x16x16_bf16 %2, %18, %25, %2\n\tv_mfma_f32_16x16x16_bf16 %3, %19, %25, %3\n\t"
                "v_mfma_f32_16x16x16_bf16 %4, %20, %25, %4\n\tv_mfma_f32_16x16x16_bf16 %5, %21, %25, %5\n\tv_mfma_f32_16x16x16_bf16 %6, %22, %25, %6\n\tv_mfma_f32_16x16x16_bf16 %7, %23, %25, %7\n\t"
                "s_nop 2\n\tv_mov_b32 %26, %26\n\ts_nop 2\n\tv_mov_b32 %26, %26"
                : "=&v"(oa[0]), "=&v"(oa[1]), "=&v"(oa[2]), "=&v"(oa[3]), "=&v"(oa[4]), "=&v"(oa[5]), "=&v"(oa[6]), "=&v"(oa[7])
                : "v"(va[0]), "v"(va[1]), "v"(va[2]), "v"(va[3]), "v"(va[4]), "v"(va[5]), "v"(va[6]), "v"(va[7]),
                  "v"(pm[0]), "v"(pm[1]), "v"(pm[2]), "v"(pm[3]), "v"(pm[4]), "v"(pm[5]), "v"(pm[6]), "v"(pm[7]), "v"(p0), "v"(p1), "v"(l));
#pragma unroll
            for (int u = 0; u < 4; ++u) {
                const f32x4 o0 = oa[2 * u], o1 = oa[2 * u + 1];
                pk[qt][u] = (v4u){pack2(o0[0], o0[1]), pack2(o0[2], o0[3]), pack2(o1[0], o1[1]), pack2(o1[2], o1[3])};
                st16(r, W_OX + (((((seg * 2 + qt) * 2 + (h >> 4)) * 4 + (h & 3)) * 4 + u) * 4 + ((h >> 2) & 3)) * 1024 + l * 16, pk[qt][u]);
            }
        }
        pend = (early && have) ? seg : -1;
        if (pend < 0) { DRAIN(); tell(seg, count(seg, qm)); }
        MEMBAR();
        ld_q(on); ld_v(on);
    }
}

constexpr int OP_P = 144;
__device__ __forceinline__ void oproj_block(const Params& p, const int wv, const int nt, const int rho, const int j, const unsigned tag, const unsigned g_t0) {
    const int w = wv, l = otid() & 63, n16 = l & 15, g = l >> 4;
    const v4u rd = mk_srd(p.ws, W_TOTAL), ro = mk_srd(p.out, S * D * 2), rx = mk_srd(p.Aq, S * D * 2);
    const unsigned snd = (unsigned)((w >> 1) ^ nt) << 31, own = snd ^ 0x80000000u;
    const unsigned ep = (nt == 0 && rho == 0 && w == 0) ? F_EP : 0xF0000000u;
    const int n0 = (nt >> 1) * 64 + 16 * w + 4 * g;
    const unsigned a_b = W_OX + (nt & 1) * 65536 + w * 1024 + l * 16, tr = (4 * g + (n16 >> 2)), tc = (n16 & 3) * 8;
    const v16u pc = {a_b, a_b + 4096, F_RDY + (rho * 32 + nt) * 128 + (l & 31) * 4, W_A + (n0 >> 3) * 32768 + n16 * 16 + (g & 1) * 8, W_GATE + n16 * 4, (unsigned)((n16 * 1024 + n0) * 2),
                     W_XC + ((nt >> 1) * 4 + w) * 2048 + l * 16, (unsigned)(w * 8192 + l * 16), (unsigned)(w * 1024 + l * 16),
                     (unsigned)(w * 10240 + (l >> 3) * OP_P + (l & 7) * 16), w * 10240 + tr * OP_P + tc, WO_L + w * 16384 + tr * 128 + tc, 0u, 0u, 0u, 0u};
    asm volatile(
        "ds_write_b128 v185, v[192:195] offset:0\n\t"
        "ds_write_b128 v185, v[196:199] offset:1152\n\t"
        "ds_write_b128 v185, v[200:203] offset:2304\n\t"
        "ds_write_b128 v185, v[204:207] offset:3456\n\t"
        "ds_read_b64_tr_b16 a[0:1], v186 offset:0\n\t"
        "ds_read_b64_tr_b16 a[2:3], v186 offset:2304\n\t"
        "ds_read_b64_tr_b16 a[4:5], v186 offset:32\n\t"
        "ds_read_b64_tr_b16 a[6:7], v186 offset:2336\n\t"
        "ds_read_b64_tr_b16 a[8:9], v186 offset:64\n\t"
        "ds_read_b64_tr_b16 a[10:11], v186 offset:2368\n\t"
        "ds_read_b64_tr_b16 a[12:13], v186 offset:96\n\t"
        "ds_read_b64_tr_b16 a[14:15], v186 offset:2400\n\t"
        "ds_write_b128 v185, v[208:211] offset:5120\n\t"
        "ds_write_b128 v185, v[212:215] offset:6272\n\t"
        "ds_write_b128 v185, v[216:219] offset:7424\n\t"
        "ds_write_b128 v185, v[220:223] offset:8576\n\t"
        "ds_read_b64_tr_b16 a[16:17], v186 offset:5120\n\t"
        "ds_read_b64_tr_b16 a[18:19], v186 offset:7424\n\t"
        "ds_read_b64_tr_b16 a[20:21], v186 offset:5152\n\t"
        "ds_read_b64_tr_b16 a[22:23], v186 offset:7456\n\t"
        "ds_read_b64_tr_b16 a[24:25], v186 offset:5184\n\t"
        "ds_read_b64_tr_b16 a[26:27], v186 offset:7488\n\t"
        "ds_read_b64_tr_b16 a[28:29], v186 offset:5216\n\t"
        "ds_read_b64_tr_b16 a[30:31], v186 offset:7520\n\t"
        "ds_write_b128 v185, v[224:227] offset:0\n\t"
        "ds_write_b128 v185, v[228:231] offset:1152\n\t"
        "ds_write_b128 v185, v[232:235] offset:2304\n\t"
        "ds_write_b128 v185, v[236:239] offset:3456\n\t"
        "ds_read_b64_tr_b16 a[32:33], v186 offset:0\n\t"
        "ds_read_b64_tr_b16 a[34:35], v186 offset:2304\n\t"
        "ds_read_b64_tr_b16 a[36:37], v186 offset:32\n\t"
        "ds_read_b64_tr_b16 a[38:39], v186 offset:2336\n\t"
        "ds_read_b64_tr_b16 a[40:41], v186 offset:64\n\t"
        "ds_read_b64_tr_b16 a[42:43], v186 offset:2368\n\t"
        "ds_read_b64_tr_b16 a[44:45], v186 offset:96\n\t"
        "ds_read_b64_tr_b16 a[46:47], v186 offset:2400\n\t"
        "ds_write_b128 v185, v[240:243] offset:5120\n\t"
        "ds_write_b128 v185, v[244:247] offset:6272\n\t"
        "ds_write_b128 v185, v[248:251] offset:7424\n\t"
        "ds_write_b128 v185, v[252:255] offset:8576\n\t"
        "ds_read_b64_tr_b16 a[48:49], v186 offset:5120\n\t"
        "ds_read_b64_tr_b16 a[50:51], v186 offset:7424\n\t"
        "ds_read_b64_tr_b16 a[52:53], v186 offset:5152\n\t"
        "ds_read_b64_tr_b16 a[54:55], v186 offset:7456\n\t"
        "ds_read_b64_tr_b16 a[56:57], v186 offset:5184\n\t"
        "ds_read_b64_tr_b16 a[58:59], v186 offset:7488\n\t"
        "ds_read_b64_tr_b16 a[60:61], v186 offset:5216\n\t"
        "ds_read_b64_tr_b16 a[62:63], v186 offset:7520\n\t"
        "ds_read_b64_tr_b16 a[64:65], v187 offset:0\n\t"
        "ds_read_b64_tr_b16 a[66:67], v187 offset:2048\n\t"
        "ds_read_b64_tr_b16 a[68:69], v187 offset:32\n\t"
        "ds_read_b64_tr_b16 a[70:71], v187 offset:2080\n\t"
        "ds_read_b64_tr_b16 a[72:73], v187 offset:64\n\t"
        "ds_read_b64_tr_b16 a[74:75], v187 offset:2112\n\t"
        "ds_read_b64_tr_b16 a[76:77], v187 offset:96\n\t"
        "ds_read_b64_tr_b16 a[78:79], v187 offset:2144\n\t"
        "ds_read_b64_tr_b16 a[80:81], v187 offset:4096\n\t"
        "ds_read_b64_tr_b16 a[82:83], v187 offset:6144\n\t"
        "ds_read_b64_tr_b16 a[84:85], v187 offset:4128\n\t"
        "ds_read_b64_tr_b16 a[86:87], v187 offset:6176\n\t"
        "ds_read_b64_tr_b16 a[88:89], v187 offset:4160\n\t"
        "ds_read_b64_tr_b16 a[90:91], v187 offset:6208\n\t"
        "ds_read_b64_tr_b16 a[92:93], v187 offset:4192\n\t"
        "ds_read_b64_tr_b16 a[94:95], v187 offset:6240\n\t"
        "ds_read_b64_tr_b16 a[96:97], v187 offset:8192\n\t"
        "ds_read_b64_tr_b16 a[98:99], v187 offset:10240\n\t"
        "ds_read_b64_tr_b16 a[100:101], v187 offset:8224\n\t"
        "ds_read_b64_tr_b16 a[102:103], v187 offset:10272\n\t"
        "ds_read_b64_tr_b16 a[104:105], v187 offset:8256\n\t"
        "ds_read_b64_tr_b16 a[106:107], v187 offset:10304\n\t"
        "ds_read_b64_tr_b16 a[108:109], v187 offset:8288\n\t"
        "ds_read_b64_tr_b16 a[110:111], v187 offset:10336\n\t"
        "ds_read_b64_tr_b16 a[112:113], v187 offset:12288\n\t"
        "ds_read_b64_tr_b16 a[114:115], v187 offset:14336\n\t"
        "ds_read_b64_tr_b16 a[116:117], v187 offset:12320\n\t"
        "ds_read_b64_tr_b16 a[118:119], v187 offset:14368\n\t"
        "ds_read_b64_tr_b16 a[120:121], v187 offset:12352\n\t"
        "ds_read_b64_tr_b16 a[122:123], v187 offset:14400\n\t"
        "ds_read_b64_tr_b16 a[124:125], v187 offset:12384\n\t"
        "ds_read_b64_tr_b16 a[126:127], v187 offset:14432\n\t"
        "ds_write_b128 v185, a[128:131] offset:0\n\t"
        "ds_write_b128 v185, a[132:135] offset:1152\n\t"
        "ds_write_b128 v185, a[136:139] offset:2304\n\t"
        "ds_write_b128 v185, a[140:143] offset:3456\n\t"
        "s_waitcnt lgkmcnt(0)\n\t"
        "ds_read_b64_tr_b16 a[128:129], v186 offset:0\n\t"
        "ds_read_b64_tr_b16 a[130:131], v186 offset:2304\n\t"
        "ds_read_b64_tr_b16 a[132:133], v186 offset:32\n\t"
        "ds_read_b64_tr_b16 a[134:135], v186 offset:2336\n\t"
        "ds_read_b64_tr_b16 a[136:137], v186 offset:64\n\t"
        "ds_read_b64_tr_b16 a[138:139], v186 offset:2368\n\t"
        "ds_read_b64_tr_b16 a[140:141], v186 offset:96\n\t"
        "ds_read_b64_tr_b16 a[142:143], v186 offset:2400\n\t"
        "ds_write_b128 v185, a[144:147] offset:5120\n\t"
        "ds_write_b128 v185, a[148:151] offset:6272\n\t"
        "ds_write_b128 v185, a[152:155] offset:7424\n\t"
        "ds_write_b128 v185, a[156:159] offset:8576\n\t"
        "s_waitcnt lgkmcnt(0)\n\t"
        "ds_read_b64_tr_b16 a[144:145], v186 offset:5120\n\t"
        "ds_read_b64_tr_b16 a[146:147], v186 offset:7424\n\t"
        "ds_read_b64_tr_b16 a[148:149], v186 offset:5152\n\t"
        "ds_read_b64_tr_b16 a[150:151], v186 offset:7456\n\t"
        "ds_read_b64_tr_b16 a[152:153], v186 offset:5184\n\t"
        "ds_read_b64_tr_b16 a[154:155], v186 offset:7488\n\t"
        "ds_read_b64_tr_b16 a[156:157], v186 offset:5216\n\t"
        "ds_read_b64_tr_b16 a[158:159], v186 offset:7520\n\t"
        "ds_write_b128 v185, a[160:163] offset:0\n\t"
        "ds_write_b128 v185, a[164:167] offset:1152\n\t"
        "ds_write_b128 v185, a[168:171] offset:2304\n\t"
        "ds_write_b128 v185, a[172:175] offset:3456\n\t"
        "s_waitcnt lgkmcnt(0)\n\t"
        "ds_read_b64_tr_b16 a[160:161], v186 offset:0\n\t"
        "ds_read_b64_tr_b16 a[162:163], v186 offset:2304\n\t"
        "ds_read_b64_tr_b16 a[164:165], v186 offset:32\n\t"
        "ds_read_b64_tr_b16 a[166:167], v186 offset:2336\n\t"
        "ds_read_b64_tr_b16 a[168:169], v186 offset:64\n\t"
        "ds_read_b64_tr_b16 a[170:171], v186 offset:2368\n\t"
        "ds_read_b64_tr_b16 a[172:173], v186 offset:96\n\t"
        "ds_read_b64_tr_b16 a[174:175], v186 offset:2400\n\t"
        "ds_write_b128 v185, a[176:179] offset:5120\n\t"
        "ds_write_b128 v185, a[180:183] offset:6272\n\t"
        "ds_write_b128 v185, a[184:187] offset:7424\n\t"
        "ds_write_b128 v185, a[188:191] offset:8576\n\t"
        "s_waitcnt lgkmcnt(0)\n\t"
        "ds_read_b64_tr_b16 a[176:177], v186 offset:5120\n\t"
        "ds_read_b64_tr_b16 a[178:179], v186 offset:7424\n\t"
        "ds_read_b64_tr_b16 a[180:181], v186 offset:5152\n\t"
        "ds_read_b64_tr_b16 a[182:183], v186 offset:7456\n\t"
        "ds_read_b64_tr_b16 a[184:185], v186 offset:5184\n\t"
        "ds_read_b64_tr_b16 a[186:187], v186 offset:7488\n\t"
        "ds_read_b64_tr_b16 a[188:189], v186 offset:5216\n\t"
        "ds_read_b64_tr_b16 a[190:191], v186 offset:7520\n\t"
        "ds_write_b128 v185, a[192:195] offset:0\n\t"
        "ds_write_b128 v185, a[196:199] offset:1152\n\t"
        "ds_write_b128 v185, a[200:203] offset:2304\n\t"
        "ds_write_b128 v185, a[204:207] offset:3456\n\t"
        "s_waitcnt lgkmcnt(0)\n\t"
        "ds_read_b64_tr_b16 a[192:193], v186 offset:0\n\t"
        "ds_read_b64_tr_b16 a[194:195], v186 offset:2304\n\t"
        "ds_read_b64_tr_b16 a[196:197], v186 offset:32\n\t"
        "ds_read_b64_tr_b16 a[198:199], v186 offset:2336\n\t"
        "ds_read_b64_tr_b16 a[200:201], v186 offset:64\n\t"
        "ds_read_b64_tr_b16 a[202:203], v186 offset:2368\n\t"
        "ds_read_b64_tr_b16 a[204:205], v186 offset:96\n\t"
        "ds_read_b64_tr_b16 a[206:207], v186 offset:2400\n\t"
        "ds_write_b128 v185, a[208:211] offset:5120\n\t"
        "ds_write_b128 v185, a[212:215] offset:6272\n\t"
        "ds_write_b128 v185, a[216:219] offset:7424\n\t"
        "ds_write_b128 v185, a[220:223] offset:8576\n\t"
        "s_waitcnt lgkmcnt(0)\n\t"
        "ds_read_b64_tr_b16 a[208:209], v186 offset:5120\n\t"
        "ds_read_b64_tr_b16 a[210:211], v186 offset:7424\n\t"
        "ds_read_b64_tr_b16 a[212:213], v186 offset:5152\n\t"
        "ds_read_b64_tr_b16 a[214:215], v186 offset:7456\n\t"
        "ds_read_b64_tr_b16 a[216:217], v186 offset:5184\n\t"
        "ds_read_b64_tr_b16 a[218:219], v186 offset:7488\n\t"
        "ds_read_b64_tr_b16 a[220:221], v186 offset:5216\n\t"
        "ds_read_b64_tr_b16 a[222:223], v186 offset:7520\n\t"
        "ds_write_b128 v185, a[224:227] offset:0\n\t"
        "ds_write_b128 v185, a[228:231] offset:1152\n\t"
        "ds_write_b128 v185, a[232:235] offset:2304\n\t"
        "ds_write_b128 v185, a[236:239] offset:3456\n\t"
        "s_waitcnt lgkmcnt(0)\n\t"
        "ds_read_b64_tr_b16 a[224:225], v186 offset:0\n\t"
        "ds_read_b64_tr_b16 a[226:227], v186 offset:2304\n\t"
        "ds_read_b64_tr_b16 a[228:229], v186 offset:32\n\t"
        "ds_read_b64_tr_b16 a[230:231], v186 offset:2336\n\t"
        "ds_read_b64_tr_b16 a[232:233], v186 offset:64\n\t"
        "ds_read_b64_tr_b16 a[234:235], v186 offset:2368\n\t"
        "ds_read_b64_tr_b16 a[236:237], v186 offset:96\n\t"
        "ds_read_b64_tr_b16 a[238:239], v186 offset:2400\n\t"
        "ds_write_b128 v185, a[240:243] offset:5120\n\t"
        "ds_write_b128 v185, a[244:247] offset:6272\n\t"
        "ds_write_b128 v185, a[248:251] offset:7424\n\t"
        "ds_write_b128 v185, a[252:255] offset:8576\n\t"
        "s_waitcnt lgkmcnt(0)\n\t"
        "ds_read_b64_tr_b16 a[240:241], v186 offset:5120\n\t"
        "ds_read_b64_tr_b16 a[242:243], v186 offset:7424\n\t"
        "ds_read_b64_tr_b16 a[244:245], v186 offset:5152\n\t"
        "ds_read_b64_tr_b16 a[246:247], v186 offset:7456\n\t"
        "ds_read_b64_tr_b16 a[248:249], v186 offset:5184\n\t"
        "ds_read_b64_tr_b16 a[250:251], v186 offset:7488\n\t"
        "ds_read_b64_tr_b16 a[252:253], v186 offset:5216\n\t"
        "ds_read_b64_tr_b16 a[254:255], v186 offset:7520\n\t"
        "s_waitcnt lgkmcnt(0)\n\t"
        "v_mov_b32_e32 v240, v176\n\t"
        "v_mov_b32_e32 v241, v177\n\t"
        "v_mov_b32_e32 v242, v178\n\t"
        "v_mov_b32_e32 v243, v179\n\t"
        "v_mov_b32_e32 v244, v180\n\t"
        "v_mov_b32_e32 v245, v181\n\t"
        "v_mov_b32_e32 v246, v182\n\t"
        "v_mov_b32_e32 v247, v183\n\t"
        "v_mov_b32_e32 v248, v184\n\t"
        "v_mov_b32_e32 v205, %[tag]\n\t"
        "v_mov_b32_e32 v207, %[tag]\n\t"
        "v_mov_b32_e32 v209, %[tag]\n\t"
        "v_mov_b32_e32 v211, %[tag]\n\t"
        "v_mov_b32_e32 v213, %[tag]\n\t"
        "v_mov_b32_e32 v215, %[tag]\n\t"
        "v_mov_b32_e32 v217, %[tag]\n\t"
        "v_mov_b32_e32 v219, %[tag]\n\t"
        "s_mov_b32 s36, 0xf0000000\n\t"
        "s_mov_b32 s37, 0\n\t"
        "s_mov_b32 s38, -1\n\t"
        "s_mov_b32 s39, -1\n\t"
        "s_mov_b32 s40, 0\n\t"
        "s_mov_b32 s43, 0\n\t"
        "s_lshl_b32 s45, %[w], 10\n\t"
        "s_add_u32 s45, s45, 65536\n\t"
        "s_lshl_b32 s44, %[rho], 1\n\t"
        "s_barrier\n\t"
        "s_mov_b32 s97, 0\n\t"
        "v_and_b32_e32 v186, 0x7c, v242\n\t"
        "s_mov_b32 s80, %c[fprep]\n\t"
        "s_mov_b32 s81, %c[fconv]\n\t"
        ".Lo1%=:\n\t"
        "buffer_load_dword v187, v186, %[rd], s80 offen sc1\n\t"
        "s_add_u32 s97, s97, 1\n\t"
        "s_cmp_lt_u32 s97, 0x200000\n\t"
        "s_cbranch_scc0 .Lgu%=\n\t"
        "s_waitcnt vmcnt(0)\n\t"
        "v_cmp_ne_u32_e32 vcc, %[tag], v187\n\t"
        "s_nop 3\n\t"
        "s_cbranch_vccz .Lo2%=\n\t"
        "s_sleep 8\n\t"
        "s_branch .Lo1%=\n\t"
        ".Lo2%=:\n\t"
        "buffer_load_dword v187, off, %[rd], s81 sc1\n\t"
        "s_add_u32 s97, s97, 1\n\t"
        "s_cmp_lt_u32 s97, 0x200000\n\t"
        "s_cbranch_scc0 .Lgu%=\n\t"
        "s_waitcnt vmcnt(0)\n\t"
        "v_cmp_gt_u32_e32 vcc, 32, v187\n\t"
        "s_nop 3\n\t"
        "s_cbranch_vccz .Lo3%=\n\t"
        "s_sleep 8\n\t"
        "s_branch .Lo2%=\n\t"
        ".Lo3%=:\n\t"
        ".Lnew%=:\n\t"
        "s_lshl_b32 s85, s37, 1\n\t"
        "s_and_b32 s80, s40, 0x3fffffff\n\t"
        "s_lshr_b32 s80, s80, s85\n\t"
        "s_andn2_b32 s80, 3, s80\n\t"
        "s_cbranch_scc0 .Lgo%=\n\t"
        "s_cmp_lt_i32 s39, 0\n\t"
        "s_cbranch_scc1 .Lo4%=\n\t"
        "s_add_i32 s80, s39, 0\n\t"
        "s_lshl_b32 s80, s80, 6\n\t"
        "s_or_b32 s81, s39, %[snd]\n\t"
        "s_cmp_lt_i32 s81, 0\n\t"
        "s_cselect_b32 s82, s36, s80\n\t"
        "buffer_load_dword v184, v244, %[rd], s82 offen sc1\n\t"
        "s_lshl_b32 s80, s80, 2\n\t"
        "s_cmp_lt_i32 s81, 0\n\t"
        "s_cselect_b32 s82, s36, s80\n\t"
        "buffer_load_dwordx2 v[180:181], v243, %[rd], s82 offen sc1\n\t"
        "s_lshl_b32 s80, s80, 7\n\t"
        "s_cmp_lt_i32 s81, 0\n\t"
        "s_cselect_b32 s82, s36, s80\n\t"
        "buffer_load_dwordx2 v[176:177], v245, %[rx], s82 offen\n\t"
        "s_add_i32 s80, s39, 0\n\t"
        "s_lshl_b32 s80, s80, 17\n\t"
        "s_or_b32 s81, s39, %[snd]\n\t"
        "s_cmp_lt_i32 s81, 0\n\t"
        "s_cselect_b32 s82, s36, s80\n\t"
        "buffer_load_dwordx4 v[160:163], v246, %[rd], s82 offen offset:0 sc1\n\t"
        "buffer_load_dwordx4 v[164:167], v246, %[rd], s82 offen offset:1024 sc1\n\t"
        "s_add_i32 s80, s39, 1\n\t"
        "s_lshl_b32 s80, s80, 6\n\t"
        "s_or_b32 s81, s39, %[snd]\n\t"
        "s_cmp_lt_i32 s81, 0\n\t"
        "s_cselect_b32 s82, s36, s80\n\t"
        "buffer_load_dword v185, v244, %[rd], s82 offen sc1\n\t"
        "s_lshl_b32 s80, s80, 2\n\t"
        "s_cmp_lt_i32 s81, 0\n\t"
        "s_cselect_b32 s82, s36, s80\n\t"
        "buffer_load_dwordx2 v[182:183], v243, %[rd], s82 offen sc1\n\t"
        "s_lshl_b32 s80, s80, 7\n\t"
        "s_cmp_lt_i32 s81, 0\n\t"
        "s_cselect_b32 s82, s36, s80\n\t"
        "buffer_load_dwordx2 v[178:179], v245, %[rx], s82 offen\n\t"
        "s_add_i32 s80, s39, 1\n\t"
        "s_lshl_b32 s80, s80, 17\n\t"
        "s_or_b32 s81, s39, %[snd]\n\t"
        "s_cmp_lt_i32 s81, 0\n\t"
        "s_cselect_b32 s82, s36, s80\n\t"
        "buffer_load_dwordx4 v[168:171], v246, %[rd], s82 offen offset:0 sc1\n\t"
        "buffer_load_dwordx4 v[172:175], v246, %[rd], s82 offen offset:1024 sc1\n\t"
        "s_waitcnt vmcnt(0)\n\t"
        "s_add_i32 s80, s39, 0\n\t"
        "s_lshl_b32 s80, s80, 15\n\t"
        "s_or_b32 s81, s39, %[snd]\n\t"
        "s_cmp_lt_i32 s81, 0\n\t"
        "s_cselect_b32 s83, s36, s80\n\t"
        "s_cbranch_scc1 .Lo7%=\n\t"
        "s_mov_b32 s97, 0\n\t"
        ".Lo5%=:\n\t"
        "v_xor_b32_e32 v186, %[tag], v161\n\t"
        "v_xor_b32_e32 v187, %[tag], v163\n\t"
        "v_xor_b32_e32 v188, %[tag], v165\n\t"
        "v_or3_b32 v186, v186, v187, v188\n\t"
        "v_xor_b32_e32 v187, %[tag], v167\n\t"
        "v_or_b32_e32 v186, v186, v187\n\t"
        "v_cmp_ne_u32_e32 vcc, 0, v186\n\t"
        "v_sub_f32_e32 v198, 1.0, v184\n\t"
        "v_add_f32_e32 v190, v152, v160\n\t"
        "v_add_f32_e32 v191, v153, v162\n\t"
        "v_add_f32_e32 v192, v154, v164\n\t"
        "v_add_f32_e32 v193, v155, v166\n\t"
        "s_cbranch_vccz .Lo6%=\n\t"
        "s_sleep %c[xslp]\n\t"
        "s_lshl_b32 s82, s83, 2\n\t"
        "buffer_load_dwordx4 v[160:163], v246, %[rd], s82 offen offset:0 sc1\n\t"
        "buffer_load_dwordx4 v[164:167], v246, %[rd], s82 offen offset:1024 sc1\n\t"
        "s_add_u32 s97, s97, 1\n\t"
        "s_cmp_lt_u32 s97, 0x200000\n\t"
        "s_cbranch_scc0 .Lgu%=\n\t"
        "s_waitcnt vmcnt(0)\n\t"
        "s_branch .Lo5%=\n\t"
        ".Lo6%=:\n\t"
        "v_cvt_pk_bf16_f32 v198, 0, v198\n\t"
        "v_cvt_pk_bf16_f32 v190, 0, v190\n\t"
        "v_cvt_pk_bf16_f32 v191, 0, v191\n\t"
        "v_cvt_pk_bf16_f32 v192, 0, v192\n\t"
        "v_cvt_pk_bf16_f32 v193, 0, v193\n\t"
        "v_lshlrev_b32_e32 v186, 16, v180\n\t"
        "v_lshlrev_b32_e32 v194, 16, v176\n\t"
        "v_and_b32_e32 v187, 0xffff0000, v180\n\t"
        "v_and_b32_e32 v195, 0xffff0000, v176\n\t"
        "v_lshlrev_b32_e32 v188, 16, v181\n\t"
        "v_lshlrev_b32_e32 v196, 16, v177\n\t"
        "v_and_b32_e32 v189, 0xffff0000, v181\n\t"
        "v_and_b32_e32 v197, 0xffff0000, v177\n\t"
        "v_mul_f32_e32 v186, v186, v194\n\t"
        "v_mul_f32_e32 v187, v187, v195\n\t"
        "v_mul_f32_e32 v188, v188, v196\n\t"
        "v_mul_f32_e32 v189, v189, v197\n\t"
        "v_mul_f32_e32 v190, v198, v190\n\t"
        "v_mul_f32_e32 v191, v198, v191\n\t"
        "v_mul_f32_e32 v192, v198, v192\n\t"
        "v_mul_f32_e32 v193, v198, v193\n\t"
        "v_cvt_pk_bf16_f32 v186, 0, v186\n\t"
        "v_cvt_pk_bf16_f32 v187, 0, v187\n\t"
        "v_cvt_pk_bf16_f32 v188, 0, v188\n\t"
        "v_cvt_pk_bf16_f32 v189, 0, v189\n\t"
        "v_cvt_pk_bf16_f32 v190, 0, v190\n\t"
        "v_cvt_pk_bf16_f32 v191, 0, v191\n\t"
        "v_cvt_pk_bf16_f32 v192, 0, v192\n\t"
        "v_cvt_pk_bf16_f32 v193, 0, v193\n\t"
        "v_mul_f32_e32 v186, v184, v186\n\t"
        "v_mul_f32_e32 v187, v184, v187\n\t"
        "v_mul_f32_e32 v188, v184, v188\n\t"
        "v_mul_f32_e32 v189, v184, v189\n\t"
        "v_cvt_pk_bf16_f32 v186, 0, v186\n\t"
        "v_cvt_pk_bf16_f32 v187, 0, v187\n\t"
        "v_cvt_pk_bf16_f32 v188, 0, v188\n\t"
        "v_cvt_pk_bf16_f32 v189, 0, v189\n\t"
        "v_add_f32_e32 v186, v186, v190\n\t"
        "v_add_f32_e32 v187, v187, v191\n\t"
        "v_add_f32_e32 v188, v188, v192\n\t"
        "v_add_f32_e32 v189, v189, v193\n\t"
        "v_cvt_pk_bf16_f32 v200, v186, v187\n\t"
        "v_cvt_pk_bf16_f32 v201, v188, v189\n\t"
        ".Lo7%=:\n\t"
        "buffer_store_dwordx2 v[200:201], v245, %[ro], s83 offen\n\t"
        "s_add_i32 s80, s39, 1\n\t"
        "s_lshl_b32 s80, s80, 15\n\t"
        "s_or_b32 s81, s39, %[snd]\n\t"
        "s_cmp_lt_i32 s81, 0\n\t"
        "s_cselect_b32 s83, s36, s80\n\t"
        "s_cbranch_scc1 .Lo10%=\n\t"
        "s_mov_b32 s97, 0\n\t"
        ".Lo8%=:\n\t"
        "v_xor_b32_e32 v186, %[tag], v169\n\t"
        "v_xor_b32_e32 v187, %[tag], v171\n\t"
        "v_xor_b32_e32 v188, %[tag], v173\n\t"
        "v_or3_b32 v186, v186, v187, v188\n\t"
        "v_xor_b32_e32 v187, %[tag], v175\n\t"
        "v_or_b32_e32 v186, v186, v187\n\t"
        "v_cmp_ne_u32_e32 vcc, 0, v186\n\t"
        "v_sub_f32_e32 v198, 1.0, v185\n\t"
        "v_add_f32_e32 v190, v156, v168\n\t"
        "v_add_f32_e32 v191, v157, v170\n\t"
        "v_add_f32_e32 v192, v158, v172\n\t"
        "v_add_f32_e32 v193, v159, v174\n\t"
        "s_cbranch_vccz .Lo9%=\n\t"
        "s_sleep %c[xslp]\n\t"
        "s_lshl_b32 s82, s83, 2\n\t"
        "buffer_load_dwordx4 v[168:171], v246, %[rd], s82 offen offset:0 sc1\n\t"
        "buffer_load_dwordx4 v[172:175], v246, %[rd], s82 offen offset:1024 sc1\n\t"
        "s_add_u32 s97, s97, 1\n\t"
        "s_cmp_lt_u32 s97, 0x200000\n\t"
        "s_cbranch_scc0 .Lgu%=\n\t"
        "s_waitcnt vmcnt(0)\n\t"
        "s_branch .Lo8%=\n\t"
        ".Lo9%=:\n\t"
        "v_cvt_pk_bf16_f32 v198, 0, v198\n\t"
        "v_cvt_pk_bf16_f32 v190, 0, v190\n\t"
        "v_cvt_pk_bf16_f32 v191, 0, v191\n\t"
        "v_cvt_pk_bf16_f32 v192, 0, v192\n\t"
        "v_cvt_pk_bf16_f32 v193, 0, v193\n\t"
        "v_lshlrev_b32_e32 v186, 16, v182\n\t"
        "v_lshlrev_b32_e32 v194, 16, v178\n\t"
        "v_and_b32_e32 v187, 0xffff0000, v182\n\t"
        "v_and_b32_e32 v195, 0xffff0000, v178\n\t"
        "v_lshlrev_b32_e32 v188, 16, v183\n\t"
        "v_lshlrev_b32_e32 v196, 16, v179\n\t"
        "v_and_b32_e32 v189, 0xffff0000, v183\n\t"
        "v_and_b32_e32 v197, 0xffff0000, v179\n\t"
        "v_mul_f32_e32 v186, v186, v194\n\t"
        "v_mul_f32_e32 v187, v187, v195\n\t"
        "v_mul_f32_e32 v188, v188, v196\n\t"
        "v_mul_f32_e32 v189, v189, v197\n\t"
        "v_mul_f32_e32 v190, v198, v190\n\t"
        "v_mul_f32_e32 v191, v198, v191\n\t"
        "v_mul_f32_e32 v192, v198, v192\n\t"
        "v_mul_f32_e32 v193, v198, v193\n\t"
        "v_cvt_pk_bf16_f32 v186, 0, v186\n\t"
        "v_cvt_pk_bf16_f32 v187, 0, v187\n\t"
        "v_cvt_pk_bf16_f32 v188, 0, v188\n\t"
        "v_cvt_pk_bf16_f32 v189, 0, v189\n\t"
        "v_cvt_pk_bf16_f32 v190, 0, v190\n\t"
        "v_cvt_pk_bf16_f32 v191, 0, v191\n\t"
        "v_cvt_pk_bf16_f32 v192, 0, v192\n\t"
        "v_cvt_pk_bf16_f32 v193, 0, v193\n\t"
        "v_mul_f32_e32 v186, v185, v186\n\t"
        "v_mul_f32_e32 v187, v185, v187\n\t"
        "v_mul_f32_e32 v188, v185, v188\n\t"
        "v_mul_f32_e32 v189, v185, v189\n\t"
        "v_cvt_pk_bf16_f32 v186, 0, v186\n\t"
        "v_cvt_pk_bf16_f32 v187, 0, v187\n\t"
        "v_cvt_pk_bf16_f32 v188, 0, v188\n\t"
        "v_cvt_pk_bf16_f32 v189, 0, v189\n\t"
        "v_add_f32_e32 v186, v186, v190\n\t"
        "v_add_f32_e32 v187, v187, v191\n\t"
        "v_add_f32_e32 v188, v188, v192\n\t"
        "v_add_f32_e32 v189, v189, v193\n\t"
        "v_cvt_pk_bf16_f32 v200, v186, v187\n\t"
        "v_cvt_pk_bf16_f32 v201, v188, v189\n\t"
        ".Lo10%=:\n\t"
        "buffer_store_dwordx2 v[200:201], v245, %[ro], s83 offen\n\t"
        "s_mov_b32 s39, -1\n\t"
        ".Lo4%=:\n\t"
        "s_cmp_eq_u32 s37, 15\n\t"
        "s_cbranch_scc1 .Lsol%=\n\t"
        "s_mov_b32 s97, 0\n\t"
        ".Lo11%=:\n\t"
        "s_lshr_b32 s80, s40, s85\n\t"
        "s_andn2_b32 s80, 3, s80\n\t"
        "s_cbranch_scc0 .Lo12%=\n\t"
        "s_cmp_eq_u32 s97, 0\n\t"
        "s_cbranch_scc1 .Lo11%=x\n\t"
        "s_sleep %c[oslp]\n\t"
        ".Lo11%=x:\n\t"
        "buffer_load_dword v220, v242, %[rd], 0 offen sc1\n\t"
        "s_add_u32 s97, s97, 1\n\t"
        "s_cmp_lt_u32 s97, 0x200000\n\t"
        "s_cbranch_scc0 .Lgu%=\n\t"
        "s_waitcnt vmcnt(0)\n\t"
        "v_cmp_eq_u32_e32 vcc, 32, v220\n\t"
        "s_nop 1\n\t"
        "s_or_b32 s40, s40, vcc_lo\n\t"
        "s_branch .Lo11%=\n\t"
        ".Lo12%=:\n\t"
        ".Lgo%=:\n\t"
        "s_lshl_b32 s84, s44, 17\n\t"
        "buffer_load_dwordx4 v[0:3], v240, %[rd], s84 offen sc1\n\t"
        "buffer_load_dwordx4 v[4:7], v241, %[rd], s84 offen sc1\n\t"
        "s_add_u32 s84, s84, 8192\n\t"
        "buffer_load_dwordx4 v[8:11], v240, %[rd], s84 offen sc1\n\t"
        "buffer_load_dwordx4 v[12:15], v241, %[rd], s84 offen sc1\n\t"
        "s_add_u32 s84, s84, 8192\n\t"
        "buffer_load_dwordx4 v[16:19], v240, %[rd], s84 offen sc1\n\t"
        "buffer_load_dwordx4 v[20:23], v241, %[rd], s84 offen sc1\n\t"
        "s_add_u32 s84, s84, 8192\n\t"
        "buffer_load_dwordx4 v[24:27], v240, %[rd], s84 offen sc1\n\t"
        "buffer_load_dwordx4 v[28:31], v241, %[rd], s84 offen sc1\n\t"
        "s_add_u32 s84, s84, 8192\n\t"
        "buffer_load_dwordx4 v[32:35], v240, %[rd], s84 offen sc1\n\t"
        "buffer_load_dwordx4 v[36:39], v241, %[rd], s84 offen sc1\n\t"
        "s_add_u32 s84, s84, 8192\n\t"
        "buffer_load_dwordx4 v[40:43], v240, %[rd], s84 offen sc1\n\t"
        "buffer_load_dwordx4 v[44:47], v241, %[rd], s84 offen sc1\n\t"
        "s_add_u32 s84, s84, 8192\n\t"
        "buffer_load_dwordx4 v[48:51], v240, %[rd], s84 offen sc1\n\t"
        "buffer_load_dwordx4 v[52:55], v241, %[rd], s84 offen sc1\n\t"
        "s_add_u32 s84, s84, 8192\n\t"
        "buffer_load_dwordx4 v[56:59], v240, %[rd], s84 offen sc1\n\t"
        "buffer_load_dwordx4 v[60:63], v241, %[rd], s84 offen sc1\n\t"
        "s_add_u32 s84, s84, 73728\n\t"
        "buffer_load_dwordx4 v[64:67], v240, %[rd], s84 offen sc1\n\t"
        "buffer_load_dwordx4 v[68:71], v241, %[rd], s84 offen sc1\n\t"
        "s_add_u32 s84, s84, 8192\n\t"
        "buffer_load_dwordx4 v[72:75], v240, %[rd], s84 offen sc1\n\t"
        "buffer_load_dwordx4 v[76:79], v241, %[rd], s84 offen sc1\n\t"
        "s_add_u32 s84, s84, 8192\n\t"
        "buffer_load_dwordx4 v[80:83], v240, %[rd], s84 offen sc1\n\t"
        "buffer_load_dwordx4 v[84:87], v241, %[rd], s84 offen sc1\n\t"
        "s_add_u32 s84, s84, 8192\n\t"
        "buffer_load_dwordx4 v[88:91], v240, %[rd], s84 offen sc1\n\t"
        "buffer_load_dwordx4 v[92:95], v241, %[rd], s84 offen sc1\n\t"
        "s_add_u32 s84, s84, 8192\n\t"
        "buffer_load_dwordx4 v[96:99], v240, %[rd], s84 offen sc1\n\t"
        "buffer_load_dwordx4 v[100:103], v241, %[rd], s84 offen sc1\n\t"
        "s_add_u32 s84, s84, 8192\n\t"
        "buffer_load_dwordx4 v[104:107], v240, %[rd], s84 offen sc1\n\t"
        "buffer_load_dwordx4 v[108:111], v241, %[rd], s84 offen sc1\n\t"
        "s_add_u32 s84, s84, 8192\n\t"
        "buffer_load_dwordx4 v[112:115], v240, %[rd], s84 offen sc1\n\t"
        "buffer_load_dwordx4 v[116:119], v241, %[rd], s84 offen sc1\n\t"
        "s_add_u32 s84, s84, 8192\n\t"
        "buffer_load_dwordx4 v[120:123], v240, %[rd], s84 offen sc1\n\t"
        "buffer_load_dwordx4 v[124:127], v241, %[rd], s84 offen sc1\n\t"
        "s_add_u32 s84, s84, 8192\n\t"
        "buffer_store_dword v0, off, %[ro], s36\n\t"
        "buffer_store_dword v0, off, %[ro], s36\n\t"
        "buffer_store_dword v0, off, %[ro], s36\n\t"
        "buffer_store_dword v0, off, %[ro], s36\n\t"
        "buffer_store_dword v0, off, %[ro], s36\n\t"
        "buffer_store_dword v0, off, %[ro], s36\n\t"
        "buffer_store_dword v0, off, %[ro], s36\n\t"
        ".Lrnd%=:\n\t"
        "s_lshl_b32 s85, s37, 1\n\t"
        "s_and_b32 s86, s40, 0x3fffffff\n\t"
        "s_lshr_b32 s86, s86, s85\n\t"
        "s_andn2_b32 s80, 12, s86\n\t"
        "s_cselect_b32 s41, 0, 1\n\t"
        "s_add_u32 s84, s44, 8\n\t"
        "s_lshl_b32 s84, s84, 17\n\t"
        "s_cmp_eq_u32 s41, 0\n\t"
        "s_cselect_b32 s84, s36, s84\n\t"
        "s_cselect_b32 s87, 0, s36\n\t"
        "s_andn2_b32 s80, 48, s86\n\t"
        "s_cselect_b32 s80, 0, s36\n\t"
        "v_add_u32_e32 v222, s43, v247\n\t"
        "s_xor_b32 s81, s43, 0x8000\n\t"
        "v_add_u32_e32 v223, s81, v248\n\t"
        "buffer_load_dword v220, v242, %[rd], s80 offen sc1\n\t"
        "s_add_i32 s80, s39, 0\n\t"
        "s_lshl_b32 s80, s80, 6\n\t"
        "s_or_b32 s81, s39, %[snd]\n\t"
        "s_cmp_lt_i32 s81, 0\n\t"
        "s_cselect_b32 s82, s36, s80\n\t"
        "buffer_load_dword v184, v244, %[rd], s82 offen sc1\n\t"
        "s_lshl_b32 s80, s80, 2\n\t"
        "s_cmp_lt_i32 s81, 0\n\t"
        "s_cselect_b32 s82, s36, s80\n\t"
        "buffer_load_dwordx2 v[180:181], v243, %[rd], s82 offen sc1\n\t"
        "s_lshl_b32 s80, s80, 7\n\t"
        "s_cmp_lt_i32 s81, 0\n\t"
        "s_cselect_b32 s82, s36, s80\n\t"
        "buffer_load_dwordx2 v[176:177], v245, %[rx], s82 offen\n\t"
        "s_add_i32 s80, s39, 0\n\t"
        "s_lshl_b32 s80, s80, 17\n\t"
        "s_or_b32 s81, s39, %[snd]\n\t"
        "s_cmp_lt_i32 s81, 0\n\t"
        "s_cselect_b32 s82, s36, s80\n\t"
        "buffer_load_dwordx4 v[160:163], v246, %[rd], s82 offen offset:0 sc1\n\t"
        "buffer_load_dwordx4 v[164:167], v246, %[rd], s82 offen offset:1024 sc1\n\t"
        "s_add_i32 s80, s39, 1\n\t"
        "s_lshl_b32 s80, s80, 6\n\t"
        "s_or_b32 s81, s39, %[snd]\n\t"
        "s_cmp_lt_i32 s81, 0\n\t"
        "s_cselect_b32 s82, s36, s80\n\t"
        "buffer_load_dword v185, v244, %[rd], s82 offen sc1\n\t"
        "s_lshl_b32 s80, s80, 2\n\t"
        "s_cmp_lt_i32 s81, 0\n\t"
        "s_cselect_b32 s82, s36, s80\n\t"
        "buffer_load_dwordx2 v[182:183], v243, %[rd], s82 offen sc1\n\t"
        "s_lshl_b32 s80, s80, 7\n\t"
        "s_cmp_lt_i32 s81, 0\n\t"
        "s_cselect_b32 s82, s36, s80\n\t"
        "buffer_load_dwordx2 v[178:179], v245, %[rx], s82 offen\n\t"
        "s_add_i32 s80, s39, 1\n\t"
        "s_lshl_b32 s80, s80, 17\n\t"
        "s_or_b32 s81, s39, %[snd]\n\t"
        "s_cmp_lt_i32 s81, 0\n\t"
        "s_cselect_b32 s82, s36, s80\n\t"
        "buffer_load_dwordx4 v[168:171], v246, %[rd], s82 offen offset:0 sc1\n\t"
        "buffer_load_dwordx4 v[172:175], v246, %[rd], s82 offen offset:1024 sc1\n\t"
        "s_waitcnt vmcnt(49)\n\t"
        "v_mfma_f32_16x16x32_bf16 v[128:131], a[128:131], v[0:3], 0\n\t"
        "v_mfma_f32_16x16x32_bf16 v[132:135], a[132:135], v[0:3], 0\n\t"
        "v_mfma_f32_16x16x32_bf16 v[136:139], a[136:139], v[0:3], 0\n\t"
        "v_mfma_f32_16x16x32_bf16 v[140:143], a[140:143], v[0:3], 0\n\t"
        "buffer_load_dwordx4 v[0:3], v240, %[rd], s84 offen sc1\n\t"
        "s_waitcnt vmcnt(49)\n\t"
        "v_mfma_f32_16x16x32_bf16 v[128:131], a[144:147], v[4:7], v[128:131]\n\t"
        "v_mfma_f32_16x16x32_bf16 v[132:135], a[148:151], v[4:7], v[132:135]\n\t"
        "v_mfma_f32_16x16x32_bf16 v[136:139], a[152:155], v[4:7], v[136:139]\n\t"
        "v_mfma_f32_16x16x32_bf16 v[140:143], a[156:159], v[4:7], v[140:143]\n\t"
        "buffer_load_dwordx4 v[4:7], v241, %[rd], s84 offen sc1\n\t"
        "s_add_u32 s84, s84, 8192\n\t"
        "ds_read_b128 v[186:189], v223 offset:0\n\t"
        "ds_read_b128 v[190:193], v223 offset:8192\n\t"
        "ds_read_b128 v[194:197], v223 offset:16384\n\t"
        "ds_read_b128 v[198:201], v223 offset:24576\n\t"
        "s_waitcnt vmcnt(49)\n\t"
        "v_mfma_f32_16x16x32_bf16 v[128:131], a[160:163], v[8:11], v[128:131]\n\t"
        "v_mfma_f32_16x16x32_bf16 v[132:135], a[164:167], v[8:11], v[132:135]\n\t"
        "v_mfma_f32_16x16x32_bf16 v[136:139], a[168:171], v[8:11], v[136:139]\n\t"
        "v_mfma_f32_16x16x32_bf16 v[140:143], a[172:175], v[8:11], v[140:143]\n\t"
        "buffer_load_dwordx4 v[8:11], v240, %[rd], s84 offen sc1\n\t"
        "s_waitcnt vmcnt(49)\n\t"
        "v_mfma_f32_16x16x32_bf16 v[128:131], a[176:179], v[12:15], v[128:131]\n\t"
        "v_mfma_f32_16x16x32_bf16 v[132:135], a[180:183], v[12:15], v[132:135]\n\t"
        "v_mfma_f32_16x16x32_bf16 v[136:139], a[184:187], v[12:15], v[136:139]\n\t"
        "v_mfma_f32_16x16x32_bf16 v[140:143], a[188:191], v[12:15], v[140:143]\n\t"
        "buffer_load_dwordx4 v[12:15], v241, %[rd], s84 offen sc1\n\t"
        "s_add_u32 s84, s84, 8192\n\t"
        "s_waitcnt lgkmcnt(0)\n\t"
        "v_add_f32_e32 v186, v186, v190\n\t"
        "v_add_f32_e32 v187, v187, v191\n\t"
        "v_add_f32_e32 v188, v188, v192\n\t"
        "v_add_f32_e32 v189, v189, v193\n\t"
        "v_add_f32_e32 v194, v194, v198\n\t"
        "v_add_f32_e32 v195, v195, v199\n\t"
        "v_add_f32_e32 v196, v196, v200\n\t"
        "v_add_f32_e32 v197, v197, v201\n\t"
        "v_add_f32_e32 v204, v186, v194\n\t"
        "v_add_f32_e32 v206, v187, v195\n\t"
        "v_add_f32_e32 v208, v188, v196\n\t"
        "v_add_f32_e32 v210, v189, v197\n\t"
        "s_add_i32 s80, s38, 0\n\t"
        "s_lshl_b32 s80, s80, 17\n\t"
        "s_or_b32 s81, s38, %[own]\n\t"
        "s_cmp_lt_i32 s81, 0\n\t"
        "s_cselect_b32 s82, s36, s80\n\t"
        "buffer_store_dwordx4 v[204:207], v246, %[rd], s82 offen offset:0 sc1\n\t"
        "buffer_store_dwordx4 v[208:211], v246, %[rd], s82 offen offset:1024 sc1\n\t"
        "v_mov_b32_e32 v144, v204\n\t"
        "v_mov_b32_e32 v145, v206\n\t"
        "v_mov_b32_e32 v146, v208\n\t"
        "v_mov_b32_e32 v147, v210\n\t"
        "s_waitcnt vmcnt(49)\n\t"
        "v_mfma_f32_16x16x32_bf16 v[128:131], a[192:195], v[16:19], v[128:131]\n\t"
        "v_mfma_f32_16x16x32_bf16 v[132:135], a[196:199], v[16:19], v[132:135]\n\t"
        "v_mfma_f32_16x16x32_bf16 v[136:139], a[200:203], v[16:19], v[136:139]\n\t"
        "v_mfma_f32_16x16x32_bf16 v[140:143], a[204:207], v[16:19], v[140:143]\n\t"
        "buffer_load_dwordx4 v[16:19], v240, %[rd], s84 offen sc1\n\t"
        "s_waitcnt vmcnt(49)\n\t"
        "v_mfma_f32_16x16x32_bf16 v[128:131], a[208:211], v[20:23], v[128:131]\n\t"
        "v_mfma_f32_16x16x32_bf16 v[132:135], a[212:215], v[20:23], v[132:135]\n\t"
        "v_mfma_f32_16x16x32_bf16 v[136:139], a[216:219], v[20:23], v[136:139]\n\t"
        "v_mfma_f32_16x16x32_bf16 v[140:143], a[220:223], v[20:23], v[140:143]\n\t"
        "buffer_load_dwordx4 v[20:23], v241, %[rd], s84 offen sc1\n\t"
        "s_add_u32 s84, s84, 8192\n\t"
        "ds_read_b128 v[186:189], v223 offset:4096\n\t"
        "ds_read_b128 v[190:193], v223 offset:12288\n\t"
        "ds_read_b128 v[194:197], v223 offset:20480\n\t"
        "ds_read_b128 v[198:201], v223 offset:28672\n\t"
        "s_waitcnt vmcnt(49)\n\t"
        "v_mfma_f32_16x16x32_bf16 v[128:131], a[224:227], v[24:27], v[128:131]\n\t"
        "v_mfma_f32_16x16x32_bf16 v[132:135], a[228:231], v[24:27], v[132:135]\n\t"
        "v_mfma_f32_16x16x32_bf16 v[136:139], a[232:235], v[24:27], v[136:139]\n\t"
        "v_mfma_f32_16x16x32_bf16 v[140:143], a[236:239], v[24:27], v[140:143]\n\t"
        "buffer_load_dwordx4 v[24:27], v240, %[rd], s84 offen sc1\n\t"
        "s_waitcnt vmcnt(49)\n\t"
        "v_mfma_f32_16x16x32_bf16 v[128:131], a[240:243], v[28:31], v[128:131]\n\t"
        "v_mfma_f32_16x16x32_bf16 v[132:135], a[244:247], v[28:31], v[132:135]\n\t"
        "v_mfma_f32_16x16x32_bf16 v[136:139], a[248:251], v[28:31], v[136:139]\n\t"
        "v_mfma_f32_16x16x32_bf16 v[140:143], a[252:255], v[28:31], v[140:143]\n\t"
        "buffer_load_dwordx4 v[28:31], v241, %[rd], s84 offen sc1\n\t"
        "s_add_u32 s84, s84, 8192\n\t"
        "s_waitcnt lgkmcnt(0)\n\t"
        "v_add_f32_e32 v186, v186, v190\n\t"
        "v_add_f32_e32 v187, v187, v191\n\t"
        "v_add_f32_e32 v188, v188, v192\n\t"
        "v_add_f32_e32 v189, v189, v193\n\t"
        "v_add_f32_e32 v194, v194, v198\n\t"
        "v_add_f32_e32 v195, v195, v199\n\t"
        "v_add_f32_e32 v196, v196, v200\n\t"
        "v_add_f32_e32 v197, v197, v201\n\t"
        "v_add_f32_e32 v212, v186, v194\n\t"
        "v_add_f32_e32 v214, v187, v195\n\t"
        "v_add_f32_e32 v216, v188, v196\n\t"
        "v_add_f32_e32 v218, v189, v197\n\t"
        "s_add_i32 s80, s38, 1\n\t"
        "s_lshl_b32 s80, s80, 17\n\t"
        "s_or_b32 s81, s38, %[own]\n\t"
        "s_cmp_lt_i32 s81, 0\n\t"
        "s_cselect_b32 s82, s36, s80\n\t"
        "buffer_store_dwordx4 v[212:215], v246, %[rd], s82 offen offset:0 sc1\n\t"
        "buffer_store_dwordx4 v[216:219], v246, %[rd], s82 offen offset:1024 sc1\n\t"
        "v_mov_b32_e32 v148, v212\n\t"
        "v_mov_b32_e32 v149, v214\n\t"
        "v_mov_b32_e32 v150, v216\n\t"
        "v_mov_b32_e32 v151, v218\n\t"
        "s_waitcnt vmcnt(49)\n\t"
        "v_mfma_f32_16x16x32_bf16 v[128:131], a[0:3], v[32:35], v[128:131]\n\t"
        "v_mfma_f32_16x16x32_bf16 v[132:135], a[4:7], v[32:35], v[132:135]\n\t"
        "v_mfma_f32_16x16x32_bf16 v[136:139], a[8:11], v[32:35], v[136:139]\n\t"
        "v_mfma_f32_16x16x32_bf16 v[140:143], a[12:15], v[32:35], v[140:143]\n\t"
        "buffer_load_dwordx4 v[32:35], v240, %[rd], s84 offen sc1\n\t"
        "s_waitcnt vmcnt(49)\n\t"
        "v_mfma_f32_16x16x32_bf16 v[128:131], a[16:19], v[36:39], v[128:131]\n\t"
        "v_mfma_f32_16x16x32_bf16 v[132:135], a[20:23], v[36:39], v[132:135]\n\t"
        "v_mfma_f32_16x16x32_bf16 v[136:139], a[24:27], v[36:39], v[136:139]\n\t"
        "v_mfma_f32_16x16x32_bf16 v[140:143], a[28:31], v[36:39], v[140:143]\n\t"
        "buffer_load_dwordx4 v[36:39], v241, %[rd], s84 offen sc1\n\t"
        "s_add_u32 s84, s84, 8192\n\t"
        "s_waitcnt vmcnt(49)\n\t"
        "v_mfma_f32_16x16x32_bf16 v[128:131], a[32:35], v[40:43], v[128:131]\n\t"
        "v_mfma_f32_16x16x32_bf16 v[132:135], a[36:39], v[40:43], v[132:135]\n\t"
        "v_mfma_f32_16x16x32_bf16 v[136:139], a[40:43], v[40:43], v[136:139]\n\t"
        "v_mfma_f32_16x16x32_bf16 v[140:143], a[44:47], v[40:43], v[140:143]\n\t"
        "buffer_load_dwordx4 v[40:43], v240, %[rd], s84 offen sc1\n\t"
        "s_waitcnt vmcnt(49)\n\t"
        "v_mfma_f32_16x16x32_bf16 v[128:131], a[48:51], v[44:47], v[128:131]\n\t"
        "v_mfma_f32_16x16x32_bf16 v[132:135], a[52:55], v[44:47], v[132:135]\n\t"
        "v_mfma_f32_16x16x32_bf16 v[136:139], a[56:59], v[44:47], v[136:139]\n\t"
        "v_mfma_f32_16x16x32_bf16 v[140:143], a[60:63], v[44:47], v[140:143]\n\t"
        "buffer_load_dwordx4 v[44:47], v241, %[rd], s84 offen sc1\n\t"
        "s_add_u32 s84, s84, 8192\n\t"
        "s_waitcnt vmcnt(49)\n\t"
        "v_mfma_f32_16x16x32_bf16 v[128:131], a[64:67], v[48:51], v[128:131]\n\t"
        "v_mfma_f32_16x16x32_bf16 v[132:135], a[68:71], v[48:51], v[132:135]\n\t"
        "v_mfma_f32_16x16x32_bf16 v[136:139], a[72:75], v[48:51], v[136:139]\n\t"
        "v_mfma_f32_16x16x32_bf16 v[140:143], a[76:79], v[48:51], v[140:143]\n\t"
        "buffer_load_dwordx4 v[48:51], v240, %[rd], s84 offen sc1\n\t"
        "s_waitcnt vmcnt(49)\n\t"
        "v_mfma_f32_16x16x32_bf16 v[128:131], a[80:83], v[52:55], v[128:131]\n\t"
        "v_mfma_f32_16x16x32_bf16 v[132:135], a[84:87], v[52:55], v[132:135]\n\t"
        "v_mfma_f32_16x16x32_bf16 v[136:139], a[88:91], v[52:55], v[136:139]\n\t"
        "v_mfma_f32_16x16x32_bf16 v[140:143], a[92:95], v[52:55], v[140:143]\n\t"
        "buffer_load_dwordx4 v[52:55], v241, %[rd], s84 offen sc1\n\t"
        "s_add_u32 s84, s84, 8192\n\t"
        "s_waitcnt vmcnt(49)\n\t"
        "v_mfma_f32_16x16x32_bf16 v[128:131], a[96:99], v[56:59], v[128:131]\n\t"
        "v_mfma_f32_16x16x32_bf16 v[132:135], a[100:103], v[56:59], v[132:135]\n\t"
        "v_mfma_f32_16x16x32_bf16 v[136:139], a[104:107], v[56:59], v[136:139]\n\t"
        "v_mfma_f32_16x16x32_bf16 v[140:143], a[108:111], v[56:59], v[140:143]\n\t"
        "buffer_load_dwordx4 v[56:59], v240, %[rd], s84 offen sc1\n\t"
        "s_waitcnt vmcnt(49)\n\t"
        "v_mfma_f32_16x16x32_bf16 v[128:131], a[112:115], v[60:63], v[128:131]\n\t"
        "v_mfma_f32_16x16x32_bf16 v[132:135], a[116:119], v[60:63], v[132:135]\n\t"
        "v_mfma_f32_16x16x32_bf16 v[136:139], a[120:123], v[60:63], v[136:139]\n\t"
        "v_mfma_f32_16x16x32_bf16 v[140:143], a[124:127], v[60:63], v[140:143]\n\t"
        "buffer_load_dwordx4 v[60:63], v241, %[rd], s84 offen sc1\n\t"
        "s_add_u32 s84, s84, 73728\n\t"
        "s_nop 2\n\t"
        "ds_write_b128 v222, v[128:131] offset:0\n\t"
        "ds_write_b128 v222, v[132:135] offset:1024\n\t"
        "ds_write_b128 v222, v[136:139] offset:2048\n\t"
        "ds_write_b128 v222, v[140:143] offset:3072\n\t"
        "s_waitcnt vmcnt(49)\n\t"
        "v_mfma_f32_16x16x32_bf16 v[128:131], a[128:131], v[64:67], 0\n\t"
        "v_mfma_f32_16x16x32_bf16 v[132:135], a[132:135], v[64:67], 0\n\t"
        "v_mfma_f32_16x16x32_bf16 v[136:139], a[136:139], v[64:67], 0\n\t"
        "v_mfma_f32_16x16x32_bf16 v[140:143], a[140:143], v[64:67], 0\n\t"
        "buffer_load_dwordx4 v[64:67], v240, %[rd], s84 offen sc1\n\t"
        "s_waitcnt vmcnt(49)\n\t"
        "v_mfma_f32_16x16x32_bf16 v[128:131], a[144:147], v[68:71], v[128:131]\n\t"
        "v_mfma_f32_16x16x32_bf16 v[132:135], a[148:151], v[68:71], v[132:135]\n\t"
        "v_mfma_f32_16x16x32_bf16 v[136:139], a[152:155], v[68:71], v[136:139]\n\t"
        "v_mfma_f32_16x16x32_bf16 v[140:143], a[156:159], v[68:71], v[140:143]\n\t"
        "buffer_load_dwordx4 v[68:71], v241, %[rd], s84 offen sc1\n\t"
        "s_add_u32 s84, s84, 8192\n\t"
        "s_waitcnt vmcnt(49)\n\t"
        "v_mfma_f32_16x16x32_bf16 v[128:131], a[160:163], v[72:75], v[128:131]\n\t"
        "v_mfma_f32_16x16x32_bf16 v[132:135], a[164:167], v[72:75], v[132:135]\n\t"
        "v_mfma_f32_16x16x32_bf16 v[136:139], a[168:171], v[72:75], v[136:139]\n\t"
        "v_mfma_f32_16x16x32_bf16 v[140:143], a[172:175], v[72:75], v[140:143]\n\t"
        "buffer_load_dwordx4 v[72:75], v240, %[rd], s84 offen sc1\n\t"
        "s_waitcnt vmcnt(49)\n\t"
        "v_mfma_f32_16x16x32_bf16 v[128:131], a[176:179], v[76:79], v[128:131]\n\t"
        "v_mfma_f32_16x16x32_bf16 v[132:135], a[180:183], v[76:79], v[132:135]\n\t"
        "v_mfma_f32_16x16x32_bf16 v[136:139], a[184:187], v[76:79], v[136:139]\n\t"
        "v_mfma_f32_16x16x32_bf16 v[140:143], a[188:191], v[76:79], v[140:143]\n\t"
        "buffer_load_dwordx4 v[76:79], v241, %[rd], s84 offen sc1\n\t"
        "s_add_u32 s84, s84, 8192\n\t"
        "s_waitcnt vmcnt(49)\n\t"
        "v_mfma_f32_16x16x32_bf16 v[128:131], a[192:195], v[80:83], v[128:131]\n\t"
        "v_mfma_f32_16x16x32_bf16 v[132:135], a[196:199], v[80:83], v[132:135]\n\t"
        "v_mfma_f32_16x16x32_bf16 v[136:139], a[200:203], v[80:83], v[136:139]\n\t"
        "v_mfma_f32_16x16x32_bf16 v[140:143], a[204:207], v[80:83], v[140:143]\n\t"
        "buffer_load_dwordx4 v[80:83], v240, %[rd], s84 offen sc1\n\t"
        "s_waitcnt vmcnt(49)\n\t"
        "v_mfma_f32_16x16x32_bf16 v[128:131], a[208:211], v[84:87], v[128:131]\n\t"
        "v_mfma_f32_16x16x32_bf16 v[132:135], a[212:215], v[84:87], v[132:135]\n\t"
        "v_mfma_f32_16x16x32_bf16 v[136:139], a[216:219], v[84:87], v[136:139]\n\t"
        "v_mfma_f32_16x16x32_bf16 v[140:143], a[220:223], v[84:87], v[140:143]\n\t"
        "buffer_load_dwordx4 v[84:87], v241, %[rd], s84 offen sc1\n\t"
        "s_add_u32 s84, s84, 8192\n\t"
        "s_waitcnt vmcnt(49)\n\t"
        "v_mfma_f32_16x16x32_bf16 v[128:131], a[224:227], v[88:91], v[128:131]\n\t"
        "v_mfma_f32_16x16x32_bf16 v[132:135], a[228:231], v[88:91], v[132:135]\n\t"
        "v_mfma_f32_16x16x32_bf16 v[136:139], a[232:235], v[88:91], v[136:139]\n\t"
        "v_mfma_f32_16x16x32_bf16 v[140:143], a[236:239], v[88:91], v[140:143]\n\t"
        "buffer_load_dwordx4 v[88:91], v240, %[rd], s84 offen sc1\n\t"
        "s_waitcnt vmcnt(49)\n\t"
        "v_mfma_f32_16x16x32_bf16 v[128:131], a[240:243], v[92:95], v[128:131]\n\t"
        "v_mfma_f32_16x16x32_bf16 v[132:135], a[244:247], v[92:95], v[132:135]\n\t"
        "v_mfma_f32_16x16x32_bf16 v[136:139], a[248:251], v[92:95], v[136:139]\n\t"
        "v_mfma_f32_16x16x32_bf16 v[140:143], a[252:255], v[92:95], v[140:143]\n\t"
        "buffer_load_dwordx4 v[92:95], v241, %[rd], s84 offen sc1\n\t"
        "s_add_u32 s84, s84, 8192\n\t"
        "buffer_load_dword v221, v242, %[rd], s87 offen sc1\n\t"
        "s_waitcnt vmcnt(49)\n\t"
        "v_mfma_f32_16x16x32_bf16 v[128:131], a[0:3], v[96:99], v[128:131]\n\t"
        "v_mfma_f32_16x16x32_bf16 v[132:135], a[4:7], v[96:99], v[132:135]\n\t"
        "v_mfma_f32_16x16x32_bf16 v[136:139], a[8:11], v[96:99], v[136:139]\n\t"
        "v_mfma_f32_16x16x32_bf16 v[140:143], a[12:15], v[96:99], v[140:143]\n\t"
        "buffer_load_dwordx4 v[96:99], v240, %[rd], s84 offen sc1\n\t"
        "s_waitcnt vmcnt(49)\n\t"
        "v_mfma_f32_16x16x32_bf16 v[128:131], a[16:19], v[100:103], v[128:131]\n\t"
        "v_mfma_f32_16x16x32_bf16 v[132:135], a[20:23], v[100:103], v[132:135]\n\t"
        "v_mfma_f32_16x16x32_bf16 v[136:139], a[24:27], v[100:103], v[136:139]\n\t"
        "v_mfma_f32_16x16x32_bf16 v[140:143], a[28:31], v[100:103], v[140:143]\n\t"
        "buffer_load_dwordx4 v[100:103], v241, %[rd], s84 offen sc1\n\t"
        "s_add_u32 s84, s84, 8192\n\t"
        "s_waitcnt vmcnt(49)\n\t"
        "v_mfma_f32_16x16x32_bf16 v[128:131], a[32:35], v[104:107], v[128:131]\n\t"
        "v_mfma_f32_16x16x32_bf16 v[132:135], a[36:39], v[104:107], v[132:135]\n\t"
        "v_mfma_f32_16x16x32_bf16 v[136:139], a[40:43], v[104:107], v[136:139]\n\t"
        "v_mfma_f32_16x16x32_bf16 v[140:143], a[44:47], v[104:107], v[140:143]\n\t"
        "buffer_load_dwordx4 v[104:107], v240, %[rd], s84 offen sc1\n\t"
        "s_waitcnt vmcnt(49)\n\t"
        "v_mfma_f32_16x16x32_bf16 v[128:131], a[48:51], v[108:111], v[128:131]\n\t"
        "v_mfma_f32_16x16x32_bf16 v[132:135], a[52:55], v[108:111], v[132:135]\n\t"
        "v_mfma_f32_16x16x32_bf16 v[136:139], a[56:59], v[108:111], v[136:139]\n\t"
        "v_mfma_f32_16x16x32_bf16 v[140:143], a[60:63], v[108:111], v[140:143]\n\t"
        "buffer_load_dwordx4 v[108:111], v241, %[rd], s84 offen sc1\n\t"
        "s_add_u32 s84, s84, 8192\n\t"
        "s_waitcnt vmcnt(49)\n\t"
        "v_mfma_f32_16x16x32_bf16 v[128:131], a[64:67], v[112:115], v[128:131]\n\t"
        "v_mfma_f32_16x16x32_bf16 v[132:135], a[68:71], v[112:115], v[132:135]\n\t"
        "v_mfma_f32_16x16x32_bf16 v[136:139], a[72:75], v[112:115], v[136:139]\n\t"
        "v_mfma_f32_16x16x32_bf16 v[140:143], a[76:79], v[112:115], v[140:143]\n\t"
        "buffer_load_dwordx4 v[112:115], v240, %[rd], s84 offen sc1\n\t"
        "s_waitcnt vmcnt(49)\n\t"
        "v_mfma_f32_16x16x32_bf16 v[128:131], a[80:83], v[116:119], v[128:131]\n\t"
        "v_mfma_f32_16x16x32_bf16 v[132:135], a[84:87], v[116:119], v[132:135]\n\t"
        "v_mfma_f32_16x16x32_bf16 v[136:139], a[88:91], v[116:119], v[136:139]\n\t"
        "v_mfma_f32_16x16x32_bf16 v[140:143], a[92:95], v[116:119], v[140:143]\n\t"
        "buffer_load_dwordx4 v[116:119], v241, %[rd], s84 offen sc1\n\t"
        "s_add_u32 s84, s84, 8192\n\t"
        "s_waitcnt vmcnt(49)\n\t"
        "v_mfma_f32_16x16x32_bf16 v[128:131], a[96:99], v[120:123], v[128:131]\n\t"
        "v_mfma_f32_16x16x32_bf16 v[132:135], a[100:103], v[120:123], v[132:135]\n\t"
        "v_mfma_f32_16x16x32_bf16 v[136:139], a[104:107], v[120:123], v[136:139]\n\t"
        "v_mfma_f32_16x16x32_bf16 v[140:143], a[108:111], v[120:123], v[140:143]\n\t"
        "buffer_load_dwordx4 v[120:123], v240, %[rd], s84 offen sc1\n\t"
        "s_waitcnt vmcnt(49)\n\t"
        "v_mfma_f32_16x16x32_bf16 v[128:131], a[112:115], v[124:127], v[128:131]\n\t"
        "v_mfma_f32_16x16x32_bf16 v[132:135], a[116:119], v[124:127], v[132:135]\n\t"
        "v_mfma_f32_16x16x32_bf16 v[136:139], a[120:123], v[124:127], v[136:139]\n\t"
        "v_mfma_f32_16x16x32_bf16 v[140:143], a[124:127], v[124:127], v[140:143]\n\t"
        "buffer_load_dwordx4 v[124:127], v241, %[rd], s84 offen sc1\n\t"
        "s_add_u32 s84, s84, 8192\n\t"
        "s_nop 2\n\t"
        "ds_write_b128 v222, v[128:131] offset:4096\n\t"
        "ds_write_b128 v222, v[132:135] offset:5120\n\t"
        "ds_write_b128 v222, v[136:139] offset:6144\n\t"
        "ds_write_b128 v222, v[140:143] offset:7168\n\t"
        "s_waitcnt lgkmcnt(0)\n\t"
        "s_barrier\n\t"
        "s_cmp_eq_u32 s41, 0\n\t"
        "s_cbranch_scc1 .Lfl%=\n\t"
        "s_waitcnt vmcnt(37)\n\t"
        "v_cmp_eq_u32_e32 vcc, 32, v220\n\t"
        "s_nop 1\n\t"
        "s_or_b32 s40, s40, vcc_lo\n\t"
        "s_add_i32 s80, s39, 0\n\t"
        "s_lshl_b32 s80, s80, 15\n\t"
        "s_or_b32 s81, s39, %[snd]\n\t"
        "s_cmp_lt_i32 s81, 0\n\t"
        "s_cselect_b32 s83, s36, s80\n\t"
        "s_cbranch_scc1 .Lo15%=\n\t"
        "s_mov_b32 s97, 0\n\t"
        ".Lo13%=:\n\t"
        "v_xor_b32_e32 v186, %[tag], v161\n\t"
        "v_xor_b32_e32 v187, %[tag], v163\n\t"
        "v_xor_b32_e32 v188, %[tag], v165\n\t"
        "v_or3_b32 v186, v186, v187, v188\n\t"
        "v_xor_b32_e32 v187, %[tag], v167\n\t"
        "v_or_b32_e32 v186, v186, v187\n\t"
        "v_cmp_ne_u32_e32 vcc, 0, v186\n\t"
        "v_sub_f32_e32 v198, 1.0, v184\n\t"
        "v_add_f32_e32 v190, v152, v160\n\t"
        "v_add_f32_e32 v191, v153, v162\n\t"
        "v_add_f32_e32 v192, v154, v164\n\t"
        "v_add_f32_e32 v193, v155, v166\n\t"
        "s_cbranch_vccz .Lo14%=\n\t"
        "s_sleep %c[xslp]\n\t"
        "s_lshl_b32 s82, s83, 2\n\t"
        "buffer_load_dwordx4 v[160:163], v246, %[rd], s82 offen offset:0 sc1\n\t"
        "buffer_load_dwordx4 v[164:167], v246, %[rd], s82 offen offset:1024 sc1\n\t"
        "s_add_u32 s97, s97, 1\n\t"
        "s_cmp_lt_u32 s97, 0x200000\n\t"
        "s_cbranch_scc0 .Lgu%=\n\t"
        "s_waitcnt vmcnt(0)\n\t"
        "s_branch .Lo13%=\n\t"
        ".Lo14%=:\n\t"
        "v_cvt_pk_bf16_f32 v198, 0, v198\n\t"
        "v_cvt_pk_bf16_f32 v190, 0, v190\n\t"
        "v_cvt_pk_bf16_f32 v191, 0, v191\n\t"
        "v_cvt_pk_bf16_f32 v192, 0, v192\n\t"
        "v_cvt_pk_bf16_f32 v193, 0, v193\n\t"
        "v_lshlrev_b32_e32 v186, 16, v180\n\t"
        "v_lshlrev_b32_e32 v194, 16, v176\n\t"
        "v_and_b32_e32 v187, 0xffff0000, v180\n\t"
        "v_and_b32_e32 v195, 0xffff0000, v176\n\t"
        "v_lshlrev_b32_e32 v188, 16, v181\n\t"
        "v_lshlrev_b32_e32 v196, 16, v177\n\t"
        "v_and_b32_e32 v189, 0xffff0000, v181\n\t"
        "v_and_b32_e32 v197, 0xffff0000, v177\n\t"
        "v_mul_f32_e32 v186, v186, v194\n\t"
        "v_mul_f32_e32 v187, v187, v195\n\t"
        "v_mul_f32_e32 v188, v188, v196\n\t"
        "v_mul_f32_e32 v189, v189, v197\n\t"
        "v_mul_f32_e32 v190, v198, v190\n\t"
        "v_mul_f32_e32 v191, v198, v191\n\t"
        "v_mul_f32_e32 v192, v198, v192\n\t"
        "v_mul_f32_e32 v193, v198, v193\n\t"
        "v_cvt_pk_bf16_f32 v186, 0, v186\n\t"
        "v_cvt_pk_bf16_f32 v187, 0, v187\n\t"
        "v_cvt_pk_bf16_f32 v188, 0, v188\n\t"
        "v_cvt_pk_bf16_f32 v189, 0, v189\n\t"
        "v_cvt_pk_bf16_f32 v190, 0, v190\n\t"
        "v_cvt_pk_bf16_f32 v191, 0, v191\n\t"
        "v_cvt_pk_bf16_f32 v192, 0, v192\n\t"
        "v_cvt_pk_bf16_f32 v193, 0, v193\n\t"
        "v_mul_f32_e32 v186, v184, v186\n\t"
        "v_mul_f32_e32 v187, v184, v187\n\t"
        "v_mul_f32_e32 v188, v184, v188\n\t"
        "v_mul_f32_e32 v189, v184, v189\n\t"
        "v_cvt_pk_bf16_f32 v186, 0, v186\n\t"
        "v_cvt_pk_bf16_f32 v187, 0, v187\n\t"
        "v_cvt_pk_bf16_f32 v188, 0, v188\n\t"
        "v_cvt_pk_bf16_f32 v189, 0, v189\n\t"
        "v_add_f32_e32 v186, v186, v190\n\t"
        "v_add_f32_e32 v187, v187, v191\n\t"
        "v_add_f32_e32 v188, v188, v192\n\t"
        "v_add_f32_e32 v189, v189, v193\n\t"
        "v_cvt_pk_bf16_f32 v200, v186, v187\n\t"
        "v_cvt_pk_bf16_f32 v201, v188, v189\n\t"
        ".Lo15%=:\n\t"
        "buffer_store_dwordx2 v[200:201], v245, %[ro], s83 offen\n\t"
        "s_add_i32 s80, s39, 1\n\t"
        "s_lshl_b32 s80, s80, 15\n\t"
        "s_or_b32 s81, s39, %[snd]\n\t"
        "s_cmp_lt_i32 s81, 0\n\t"
        "s_cselect_b32 s83, s36, s80\n\t"
        "s_cbranch_scc1 .Lo18%=\n\t"
        "s_mov_b32 s97, 0\n\t"
        ".Lo16%=:\n\t"
        "v_xor_b32_e32 v186, %[tag], v169\n\t"
        "v_xor_b32_e32 v187, %[tag], v171\n\t"
        "v_xor_b32_e32 v188, %[tag], v173\n\t"
        "v_or3_b32 v186, v186, v187, v188\n\t"
        "v_xor_b32_e32 v187, %[tag], v175\n\t"
        "v_or_b32_e32 v186, v186, v187\n\t"
        "v_cmp_ne_u32_e32 vcc, 0, v186\n\t"
        "v_sub_f32_e32 v198, 1.0, v185\n\t"
        "v_add_f32_e32 v190, v156, v168\n\t"
        "v_add_f32_e32 v191, v157, v170\n\t"
        "v_add_f32_e32 v192, v158, v172\n\t"
        "v_add_f32_e32 v193, v159, v174\n\t"
        "s_cbranch_vccz .Lo17%=\n\t"
        "s_sleep %c[xslp]\n\t"
        "s_lshl_b32 s82, s83, 2\n\t"
        "buffer_load_dwordx4 v[168:171], v246, %[rd], s82 offen offset:0 sc1\n\t"
        "buffer_load_dwordx4 v[172:175], v246, %[rd], s82 offen offset:1024 sc1\n\t"
        "s_add_u32 s97, s97, 1\n\t"
        "s_cmp_lt_u32 s97, 0x200000\n\t"
        "s_cbranch_scc0 .Lgu%=\n\t"
        "s_waitcnt vmcnt(0)\n\t"
        "s_branch .Lo16%=\n\t"
        ".Lo17%=:\n\t"
        "v_cvt_pk_bf16_f32 v198, 0, v198\n\t"
        "v_cvt_pk_bf16_f32 v190, 0, v190\n\t"
        "v_cvt_pk_bf16_f32 v191, 0, v191\n\t"
        "v_cvt_pk_bf16_f32 v192, 0, v192\n\t"
        "v_cvt_pk_bf16_f32 v193, 0, v193\n\t"
        "v_lshlrev_b32_e32 v186, 16, v182\n\t"
        "v_lshlrev_b32_e32 v194, 16, v178\n\t"
        "v_and_b32_e32 v187, 0xffff0000, v182\n\t"
        "v_and_b32_e32 v195, 0xffff0000, v178\n\t"
        "v_lshlrev_b32_e32 v188, 16, v183\n\t"
        "v_lshlrev_b32_e32 v196, 16, v179\n\t"
        "v_and_b32_e32 v189, 0xffff0000, v183\n\t"
        "v_and_b32_e32 v197, 0xffff0000, v179\n\t"
        "v_mul_f32_e32 v186, v186, v194\n\t"
        "v_mul_f32_e32 v187, v187, v195\n\t"
        "v_mul_f32_e32 v188, v188, v196\n\t"
        "v_mul_f32_e32 v189, v189, v197\n\t"
        "v_mul_f32_e32 v190, v198, v190\n\t"
        "v_mul_f32_e32 v191, v198, v191\n\t"
        "v_mul_f32_e32 v192, v198, v192\n\t"
        "v_mul_f32_e32 v193, v198, v193\n\t"
        "v_cvt_pk_bf16_f32 v186, 0, v186\n\t"
        "v_cvt_pk_bf16_f32 v187, 0, v187\n\t"
        "v_cvt_pk_bf16_f32 v188, 0, v188\n\t"
        "v_cvt_pk_bf16_f32 v189, 0, v189\n\t"
        "v_cvt_pk_bf16_f32 v190, 0, v190\n\t"
        "v_cvt_pk_bf16_f32 v191, 0, v191\n\t"
        "v_cvt_pk_bf16_f32 v192, 0, v192\n\t"
        "v_cvt_pk_bf16_f32 v193, 0, v193\n\t"
        "v_mul_f32_e32 v186, v185, v186\n\t"
        "v_mul_f32_e32 v187, v185, v187\n\t"
        "v_mul_f32_e32 v188, v185, v188\n\t"
        "v_mul_f32_e32 v189, v185, v189\n\t"
        "v_cvt_pk_bf16_f32 v186, 0, v186\n\t"
        "v_cvt_pk_bf16_f32 v187, 0, v187\n\t"
        "v_cvt_pk_bf16_f32 v188, 0, v188\n\t"
        "v_cvt_pk_bf16_f32 v189, 0, v189\n\t"
        "v_add_f32_e32 v186, v186, v190\n\t"
        "v_add_f32_e32 v187, v187, v191\n\t"
        "v_add_f32_e32 v188, v188, v192\n\t"
        "v_add_f32_e32 v189, v189, v193\n\t"
        "v_cvt_pk_bf16_f32 v200, v186, v187\n\t"
        "v_cvt_pk_bf16_f32 v201, v188, v189\n\t"
        ".Lo18%=:\n\t"
        "buffer_store_dwordx2 v[200:201], v245, %[ro], s83 offen\n\t"
        "v_mov_b32_e32 v152, v144\n\t"
        "v_mov_b32_e32 v153, v145\n\t"
        "v_mov_b32_e32 v154, v146\n\t"
        "v_mov_b32_e32 v155, v147\n\t"
        "v_mov_b32_e32 v156, v148\n\t"
        "v_mov_b32_e32 v157, v149\n\t"
        "v_mov_b32_e32 v158, v150\n\t"
        "v_mov_b32_e32 v159, v151\n\t"
        "s_mov_b32 s39, s38\n\t"
        "s_mov_b32 s38, s44\n\t"
        "s_add_u32 s37, s37, 1\n\t"
        "s_add_u32 s44, s44, 8\n\t"
        "s_xor_b32 s43, s43, 0x8000\n\t"
        "s_branch .Lrnd%=\n\t"
        ".Lfl%=:\n\t"
        "v_add_u32_e32 v223, s43, v248\n\t"
        "ds_read_b128 v[186:189], v223 offset:0\n\t"
        "ds_read_b128 v[190:193], v223 offset:8192\n\t"
        "ds_read_b128 v[194:197], v223 offset:16384\n\t"
        "ds_read_b128 v[198:201], v223 offset:24576\n\t"
        "s_waitcnt lgkmcnt(0)\n\t"
        "v_add_f32_e32 v186, v186, v190\n\t"
        "v_add_f32_e32 v187, v187, v191\n\t"
        "v_add_f32_e32 v188, v188, v192\n\t"
        "v_add_f32_e32 v189, v189, v193\n\t"
        "v_add_f32_e32 v194, v194, v198\n\t"
        "v_add_f32_e32 v195, v195, v199\n\t"
        "v_add_f32_e32 v196, v196, v200\n\t"
        "v_add_f32_e32 v197, v197, v201\n\t"
        "v_add_f32_e32 v204, v186, v194\n\t"
        "v_add_f32_e32 v206, v187, v195\n\t"
        "v_add_f32_e32 v208, v188, v196\n\t"
        "v_add_f32_e32 v210, v189, v197\n\t"
        "s_add_i32 s80, s44, 0\n\t"
        "s_lshl_b32 s80, s80, 17\n\t"
        "s_or_b32 s81, s44, %[own]\n\t"
        "s_cmp_lt_i32 s81, 0\n\t"
        "s_cselect_b32 s82, s36, s80\n\t"
        "buffer_store_dwordx4 v[204:207], v246, %[rd], s82 offen offset:0 sc1\n\t"
        "buffer_store_dwordx4 v[208:211], v246, %[rd], s82 offen offset:1024 sc1\n\t"
        "v_mov_b32_e32 v128, v204\n\t"
        "v_mov_b32_e32 v129, v206\n\t"
        "v_mov_b32_e32 v130, v208\n\t"
        "v_mov_b32_e32 v131, v210\n\t"
        "ds_read_b128 v[186:189], v223 offset:4096\n\t"
        "ds_read_b128 v[190:193], v223 offset:12288\n\t"
        "ds_read_b128 v[194:197], v223 offset:20480\n\t"
        "ds_read_b128 v[198:201], v223 offset:28672\n\t"
        "s_waitcnt lgkmcnt(0)\n\t"
        "v_add_f32_e32 v186, v186, v190\n\t"
        "v_add_f32_e32 v187, v187, v191\n\t"
        "v_add_f32_e32 v188, v188, v192\n\t"
        "v_add_f32_e32 v189, v189, v193\n\t"
        "v_add_f32_e32 v194, v194, v198\n\t"
        "v_add_f32_e32 v195, v195, v199\n\t"
        "v_add_f32_e32 v196, v196, v200\n\t"
        "v_add_f32_e32 v197, v197, v201\n\t"
        "v_add_f32_e32 v212, v186, v194\n\t"
        "v_add_f32_e32 v214, v187, v195\n\t"
        "v_add_f32_e32 v216, v188, v196\n\t"
        "v_add_f32_e32 v218, v189, v197\n\t"
        "s_add_i32 s80, s44, 1\n\t"
        "s_lshl_b32 s80, s80, 17\n\t"
        "s_or_b32 s81, s44, %[own]\n\t"
        "s_cmp_lt_i32 s81, 0\n\t"
        "s_cselect_b32 s82, s36, s80\n\t"
        "buffer_store_dwordx4 v[212:215], v246, %[rd], s82 offen offset:0 sc1\n\t"
        "buffer_store_dwordx4 v[216:219], v246, %[rd], s82 offen offset:1024 sc1\n\t"
        "v_mov_b32_e32 v132, v212\n\t"
        "v_mov_b32_e32 v133, v214\n\t"
        "v_mov_b32_e32 v134, v216\n\t"
        "v_mov_b32_e32 v135, v218\n\t"
        "s_waitcnt vmcnt(4)\n\t"
        "v_cmp_eq_u32_e32 vcc, 32, v220\n\t"
        "s_nop 1\n\t"
        "s_or_b32 s40, s40, vcc_lo\n\t"
        "v_cmp_eq_u32_e32 vcc, 32, v221\n\t"
        "s_nop 1\n\t"
        "s_or_b32 s40, s40, vcc_lo\n\t"
        "s_add_i32 s80, s39, 0\n\t"
        "s_lshl_b32 s80, s80, 15\n\t"
        "s_or_b32 s81, s39, %[snd]\n\t"
        "s_cmp_lt_i32 s81, 0\n\t"
        "s_cselect_b32 s83, s36, s80\n\t"
        "s_cbranch_scc1 .Lo21%=\n\t"
        "s_mov_b32 s97, 0\n\t"
        ".Lo19%=:\n\t"
        "v_xor_b32_e32 v186, %[tag], v161\n\t"
        "v_xor_b32_e32 v187, %[tag], v163\n\t"
        "v_xor_b32_e32 v188, %[tag], v165\n\t"
        "v_or3_b32 v186, v186, v187, v188\n\t"
        "v_xor_b32_e32 v187, %[tag], v167\n\t"
        "v_or_b32_e32 v186, v186, v187\n\t"
        "v_cmp_ne_u32_e32 vcc, 0, v186\n\t"
        "v_sub_f32_e32 v198, 1.0, v184\n\t"
        "v_add_f32_e32 v190, v152, v160\n\t"
        "v_add_f32_e32 v191, v153, v162\n\t"
        "v_add_f32_e32 v192, v154, v164\n\t"
        "v_add_f32_e32 v193, v155, v166\n\t"
        "s_cbranch_vccz .Lo20%=\n\t"
        "s_sleep %c[xslp]\n\t"
        "s_lshl_b32 s82, s83, 2\n\t"
        "buffer_load_dwordx4 v[160:163], v246, %[rd], s82 offen offset:0 sc1\n\t"
        "buffer_load_dwordx4 v[164:167], v246, %[rd], s82 offen offset:1024 sc1\n\t"
        "s_add_u32 s97, s97, 1\n\t"
        "s_cmp_lt_u32 s97, 0x200000\n\t"
        "s_cbranch_scc0 .Lgu%=\n\t"
        "s_waitcnt vmcnt(0)\n\t"
        "s_branch .Lo19%=\n\t"
        ".Lo20%=:\n\t"
        "v_cvt_pk_bf16_f32 v198, 0, v198\n\t"
        "v_cvt_pk_bf16_f32 v190, 0, v190\n\t"
        "v_cvt_pk_bf16_f32 v191, 0, v191\n\t"
        "v_cvt_pk_bf16_f32 v192, 0, v192\n\t"
        "v_cvt_pk_bf16_f32 v193, 0, v193\n\t"
        "v_lshlrev_b32_e32 v186, 16, v180\n\t"
        "v_lshlrev_b32_e32 v194, 16, v176\n\t"
        "v_and_b32_e32 v187, 0xffff0000, v180\n\t"
        "v_and_b32_e32 v195, 0xffff0000, v176\n\t"
        "v_lshlrev_b32_e32 v188, 16, v181\n\t"
        "v_lshlrev_b32_e32 v196, 16, v177\n\t"
        "v_and_b32_e32 v189, 0xffff0000, v181\n\t"
        "v_and_b32_e32 v197, 0xffff0000, v177\n\t"
        "v_mul_f32_e32 v186, v186, v194\n\t"
        "v_mul_f32_e32 v187, v187, v195\n\t"
        "v_mul_f32_e32 v188, v188, v196\n\t"
        "v_mul_f32_e32 v189, v189, v197\n\t"
        "v_mul_f32_e32 v190, v198, v190\n\t"
        "v_mul_f32_e32 v191, v198, v191\n\t"
        "v_mul_f32_e32 v192, v198, v192\n\t"
        "v_mul_f32_e32 v193, v198, v193\n\t"
        "v_cvt_pk_bf16_f32 v186, 0, v186\n\t"
        "v_cvt_pk_bf16_f32 v187, 0, v187\n\t"
        "v_cvt_pk_bf16_f32 v188, 0, v188\n\t"
        "v_cvt_pk_bf16_f32 v189, 0, v189\n\t"
        "v_cvt_pk_bf16_f32 v190, 0, v190\n\t"
        "v_cvt_pk_bf16_f32 v191, 0, v191\n\t"
        "v_cvt_pk_bf16_f32 v192, 0, v192\n\t"
        "v_cvt_pk_bf16_f32 v193, 0, v193\n\t"
        "v_mul_f32_e32 v186, v184, v186\n\t"
        "v_mul_f32_e32 v187, v184, v187\n\t"
        "v_mul_f32_e32 v188, v184, v188\n\t"
        "v_mul_f32_e32 v189, v184, v189\n\t"
        "v_cvt_pk_bf16_f32 v186, 0, v186\n\t"
        "v_cvt_pk_bf16_f32 v187, 0, v187\n\t"
        "v_cvt_pk_bf16_f32 v188, 0, v188\n\t"
        "v_cvt_pk_bf16_f32 v189, 0, v189\n\t"
        "v_add_f32_e32 v186, v186, v190\n\t"
        "v_add_f32_e32 v187, v187, v191\n\t"
        "v_add_f32_e32 v188, v188, v192\n\t"
        "v_add_f32_e32 v189, v189, v193\n\t"
        "v_cvt_pk_bf16_f32 v200, v186, v187\n\t"
        "v_cvt_pk_bf16_f32 v201, v188, v189\n\t"
        ".Lo21%=:\n\t"
        "buffer_store_dwordx2 v[200:201], v245, %[ro], s83 offen\n\t"
        "s_add_i32 s80, s39, 1\n\t"
        "s_lshl_b32 s80, s80, 15\n\t"
        "s_or_b32 s81, s39, %[snd]\n\t"
        "s_cmp_lt_i32 s81, 0\n\t"
        "s_cselect_b32 s83, s36, s80\n\t"
        "s_cbranch_scc1 .Lo24%=\n\t"
        "s_mov_b32 s97, 0\n\t"
        ".Lo22%=:\n\t"
        "v_xor_b32_e32 v186, %[tag], v169\n\t"
        "v_xor_b32_e32 v187, %[tag], v171\n\t"
        "v_xor_b32_e32 v188, %[tag], v173\n\t"
        "v_or3_b32 v186, v186, v187, v188\n\t"
        "v_xor_b32_e32 v187, %[tag], v175\n\t"
        "v_or_b32_e32 v186, v186, v187\n\t"
        "v_cmp_ne_u32_e32 vcc, 0, v186\n\t"
        "v_sub_f32_e32 v198, 1.0, v185\n\t"
        "v_add_f32_e32 v190, v156, v168\n\t"
        "v_add_f32_e32 v191, v157, v170\n\t"
        "v_add_f32_e32 v192, v158, v172\n\t"
        "v_add_f32_e32 v193, v159, v174\n\t"
        "s_cbranch_vccz .Lo23%=\n\t"
        "s_sleep %c[xslp]\n\t"
        "s_lshl_b32 s82, s83, 2\n\t"
        "buffer_load_dwordx4 v[168:171], v246, %[rd], s82 offen offset:0 sc1\n\t"
        "buffer_load_dwordx4 v[172:175], v246, %[rd], s82 offen offset:1024 sc1\n\t"
        "s_add_u32 s97, s97, 1\n\t"
        "s_cmp_lt_u32 s97, 0x200000\n\t"
        "s_cbranch_scc0 .Lgu%=\n\t"
        "s_waitcnt vmcnt(0)\n\t"
        "s_branch .Lo22%=\n\t"
        ".Lo23%=:\n\t"
        "v_cvt_pk_bf16_f32 v198, 0, v198\n\t"
        "v_cvt_pk_bf16_f32 v190, 0, v190\n\t"
        "v_cvt_pk_bf16_f32 v191, 0, v191\n\t"
        "v_cvt_pk_bf16_f32 v192, 0, v192\n\t"
        "v_cvt_pk_bf16_f32 v193, 0, v193\n\t"
        "v_lshlrev_b32_e32 v186, 16, v182\n\t"
        "v_lshlrev_b32_e32 v194, 16, v178\n\t"
        "v_and_b32_e32 v187, 0xffff0000, v182\n\t"
        "v_and_b32_e32 v195, 0xffff0000, v178\n\t"
        "v_lshlrev_b32_e32 v188, 16, v183\n\t"
        "v_lshlrev_b32_e32 v196, 16, v179\n\t"
        "v_and_b32_e32 v189, 0xffff0000, v183\n\t"
        "v_and_b32_e32 v197, 0xffff0000, v179\n\t"
        "v_mul_f32_e32 v186, v186, v194\n\t"
        "v_mul_f32_e32 v187, v187, v195\n\t"
        "v_mul_f32_e32 v188, v188, v196\n\t"
        "v_mul_f32_e32 v189, v189, v197\n\t"
        "v_mul_f32_e32 v190, v198, v190\n\t"
        "v_mul_f32_e32 v191, v198, v191\n\t"
        "v_mul_f32_e32 v192, v198, v192\n\t"
        "v_mul_f32_e32 v193, v198, v193\n\t"
        "v_cvt_pk_bf16_f32 v186, 0, v186\n\t"
        "v_cvt_pk_bf16_f32 v187, 0, v187\n\t"
        "v_cvt_pk_bf16_f32 v188, 0, v188\n\t"
        "v_cvt_pk_bf16_f32 v189, 0, v189\n\t"
        "v_cvt_pk_bf16_f32 v190, 0, v190\n\t"
        "v_cvt_pk_bf16_f32 v191, 0, v191\n\t"
        "v_cvt_pk_bf16_f32 v192, 0, v192\n\t"
        "v_cvt_pk_bf16_f32 v193, 0, v193\n\t"
        "v_mul_f32_e32 v186, v185, v186\n\t"
        "v_mul_f32_e32 v187, v185, v187\n\t"
        "v_mul_f32_e32 v188, v185, v188\n\t"
        "v_mul_f32_e32 v189, v185, v189\n\t"
        "v_cvt_pk_bf16_f32 v186, 0, v186\n\t"
        "v_cvt_pk_bf16_f32 v187, 0, v187\n\t"
        "v_cvt_pk_bf16_f32 v188, 0, v188\n\t"
        "v_cvt_pk_bf16_f32 v189, 0, v189\n\t"
        "v_add_f32_e32 v186, v186, v190\n\t"
        "v_add_f32_e32 v187, v187, v191\n\t"
        "v_add_f32_e32 v188, v188, v192\n\t"
        "v_add_f32_e32 v189, v189, v193\n\t"
        "v_cvt_pk_bf16_f32 v200, v186, v187\n\t"
        "v_cvt_pk_bf16_f32 v201, v188, v189\n\t"
        ".Lo24%=:\n\t"
        "buffer_store_dwordx2 v[200:201], v245, %[ro], s83 offen\n\t"
        "s_cmp_lt_i32 s38, 0\n\t"
        "s_cbranch_scc1 .Lo25%=\n\t"
        "s_add_i32 s80, s38, 0\n\t"
        "s_lshl_b32 s80, s80, 6\n\t"
        "s_or_b32 s81, s38, %[snd]\n\t"
        "s_cmp_lt_i32 s81, 0\n\t"
        "s_cselect_b32 s82, s36, s80\n\t"
        "buffer_load_dword v184, v244, %[rd], s82 offen sc1\n\t"
        "s_lshl_b32 s80, s80, 2\n\t"
        "s_cmp_lt_i32 s81, 0\n\t"
        "s_cselect_b32 s82, s36, s80\n\t"
        "buffer_load_dwordx2 v[180:181], v243, %[rd], s82 offen sc1\n\t"
        "s_lshl_b32 s80, s80, 7\n\t"
        "s_cmp_lt_i32 s81, 0\n\t"
        "s_cselect_b32 s82, s36, s80\n\t"
        "buffer_load_dwordx2 v[176:177], v245, %[rx], s82 offen\n\t"
        "s_add_i32 s80, s38, 0\n\t"
        "s_lshl_b32 s80, s80, 17\n\t"
        "s_or_b32 s81, s38, %[snd]\n\t"
        "s_cmp_lt_i32 s81, 0\n\t"
        "s_cselect_b32 s82, s36, s80\n\t"
        "buffer_load_dwordx4 v[160:163], v246, %[rd], s82 offen offset:0 sc1\n\t"
        "buffer_load_dwordx4 v[164:167], v246, %[rd], s82 offen offset:1024 sc1\n\t"
        "s_add_i32 s80, s38, 1\n\t"
        "s_lshl_b32 s80, s80, 6\n\t"
        "s_or_b32 s81, s38, %[snd]\n\t"
        "s_cmp_lt_i32 s81, 0\n\t"
        "s_cselect_b32 s82, s36, s80\n\t"
        "buffer_load_dword v185, v244, %[rd], s82 offen sc1\n\t"
        "s_lshl_b32 s80, s80, 2\n\t"
        "s_cmp_lt_i32 s81, 0\n\t"
        "s_cselect_b32 s82, s36, s80\n\t"
        "buffer_load_dwordx2 v[182:183], v243, %[rd], s82 offen sc1\n\t"
        "s_lshl_b32 s80, s80, 7\n\t"
        "s_cmp_lt_i32 s81, 0\n\t"
        "s_cselect_b32 s82, s36, s80\n\t"
        "buffer_load_dwordx2 v[178:179], v245, %[rx], s82 offen\n\t"
        "s_add_i32 s80, s38, 1\n\t"
        "s_lshl_b32 s80, s80, 17\n\t"
        "s_or_b32 s81, s38, %[snd]\n\t"
        "s_cmp_lt_i32 s81, 0\n\t"
        "s_cselect_b32 s82, s36, s80\n\t"
        "buffer_load_dwordx4 v[168:171], v246, %[rd], s82 offen offset:0 sc1\n\t"
        "buffer_load_dwordx4 v[172:175], v246, %[rd], s82 offen offset:1024 sc1\n\t"
        "s_waitcnt vmcnt(0)\n\t"
        "s_add_i32 s80, s38, 0\n\t"
        "s_lshl_b32 s80, s80, 15\n\t"
        "s_or_b32 s81, s38, %[snd]\n\t"
        "s_cmp_lt_i32 s81, 0\n\t"
        "s_cselect_b32 s83, s36, s80\n\t"
        "s_cbranch_scc1 .Lo28%=\n\t"
        "s_mov_b32 s97, 0\n\t"
        ".Lo26%=:\n\t"
        "v_xor_b32_e32 v186, %[tag], v161\n\t"
        "v_xor_b32_e32 v187, %[tag], v163\n\t"
        "v_xor_b32_e32 v188, %[tag], v165\n\t"
        "v_or3_b32 v186, v186, v187, v188\n\t"
        "v_xor_b32_e32 v187, %[tag], v167\n\t"
        "v_or_b32_e32 v186, v186, v187\n\t"
        "v_cmp_ne_u32_e32 vcc, 0, v186\n\t"
        "v_sub_f32_e32 v198, 1.0, v184\n\t"
        "v_add_f32_e32 v190, v144, v160\n\t"
        "v_add_f32_e32 v191, v145, v162\n\t"
        "v_add_f32_e32 v192, v146, v164\n\t"
        "v_add_f32_e32 v193, v147, v166\n\t"
        "s_cbranch_vccz .Lo27%=\n\t"
        "s_sleep %c[xslp]\n\t"
        "s_lshl_b32 s82, s83, 2\n\t"
        "buffer_load_dwordx4 v[160:163], v246, %[rd], s82 offen offset:0 sc1\n\t"
        "buffer_load_dwordx4 v[164:167], v246, %[rd], s82 offen offset:1024 sc1\n\t"
        "s_add_u32 s97, s97, 1\n\t"
        "s_cmp_lt_u32 s97, 0x200000\n\t"
        "s_cbranch_scc0 .Lgu%=\n\t"
        "s_waitcnt vmcnt(0)\n\t"
        "s_branch .Lo26%=\n\t"
        ".Lo27%=:\n\t"
        "v_cvt_pk_bf16_f32 v198, 0, v198\n\t"
        "v_cvt_pk_bf16_f32 v190, 0, v190\n\t"
        "v_cvt_pk_bf16_f32 v191, 0, v191\n\t"
        "v_cvt_pk_bf16_f32 v192, 0, v192\n\t"
        "v_cvt_pk_bf16_f32 v193, 0, v193\n\t"
        "v_lshlrev_b32_e32 v186, 16, v180\n\t"
        "v_lshlrev_b32_e32 v194, 16, v176\n\t"
        "v_and_b32_e32 v187, 0xffff0000, v180\n\t"
        "v_and_b32_e32 v195, 0xffff0000, v176\n\t"
        "v_lshlrev_b32_e32 v188, 16, v181\n\t"
        "v_lshlrev_b32_e32 v196, 16, v177\n\t"
        "v_and_b32_e32 v189, 0xffff0000, v181\n\t"
        "v_and_b32_e32 v197, 0xffff0000, v177\n\t"
        "v_mul_f32_e32 v186, v186, v194\n\t"
        "v_mul_f32_e32 v187, v187, v195\n\t"
        "v_mul_f32_e32 v188, v188, v196\n\t"
        "v_mul_f32_e32 v189, v189, v197\n\t"
        "v_mul_f32_e32 v190, v198, v190\n\t"
        "v_mul_f32_e32 v191, v198, v191\n\t"
        "v_mul_f32_e32 v192, v198, v192\n\t"
        "v_mul_f32_e32 v193, v198, v193\n\t"
        "v_cvt_pk_bf16_f32 v186, 0, v186\n\t"
        "v_cvt_pk_bf16_f32 v187, 0, v187\n\t"
        "v_cvt_pk_bf16_f32 v188, 0, v188\n\t"
        "v_cvt_pk_bf16_f32 v189, 0, v189\n\t"
        "v_cvt_pk_bf16_f32 v190, 0, v190\n\t"
        "v_cvt_pk_bf16_f32 v191, 0, v191\n\t"
        "v_cvt_pk_bf16_f32 v192, 0, v192\n\t"
        "v_cvt_pk_bf16_f32 v193, 0, v193\n\t"
        "v_mul_f32_e32 v186, v184, v186\n\t"
        "v_mul_f32_e32 v187, v184, v187\n\t"
        "v_mul_f32_e32 v188, v184, v188\n\t"
        "v_mul_f32_e32 v189, v184, v189\n\t"
        "v_cvt_pk_bf16_f32 v186, 0, v186\n\t"
        "v_cvt_pk_bf16_f32 v187, 0, v187\n\t"
        "v_cvt_pk_bf16_f32 v188, 0, v188\n\t"
        "v_cvt_pk_bf16_f32 v189, 0, v189\n\t"
        "v_add_f32_e32 v186, v186, v190\n\t"
        "v_add_f32_e32 v187, v187, v191\n\t"
        "v_add_f32_e32 v188, v188, v192\n\t"
        "v_add_f32_e32 v189, v189, v193\n\t"
        "v_cvt_pk_bf16_f32 v200, v186, v187\n\t"
        "v_cvt_pk_bf16_f32 v201, v188, v189\n\t"
        ".Lo28%=:\n\t"
        "buffer_store_dwordx2 v[200:201], v245, %[ro], s83 offen\n\t"
        "s_add_i32 s80, s38, 1\n\t"
        "s_lshl_b32 s80, s80, 15\n\t"
        "s_or_b32 s81, s38, %[snd]\n\t"
        "s_cmp_lt_i32 s81, 0\n\t"
        "s_cselect_b32 s83, s36, s80\n\t"
        "s_cbranch_scc1 .Lo31%=\n\t"
        "s_mov_b32 s97, 0\n\t"
        ".Lo29%=:\n\t"
        "v_xor_b32_e32 v186, %[tag], v169\n\t"
        "v_xor_b32_e32 v187, %[tag], v171\n\t"
        "v_xor_b32_e32 v188, %[tag], v173\n\t"
        "v_or3_b32 v186, v186, v187, v188\n\t"
        "v_xor_b32_e32 v187, %[tag], v175\n\t"
        "v_or_b32_e32 v186, v186, v187\n\t"
        "v_cmp_ne_u32_e32 vcc, 0, v186\n\t"
        "v_sub_f32_e32 v198, 1.0, v185\n\t"
        "v_add_f32_e32 v190, v148, v168\n\t"
        "v_add_f32_e32 v191, v149, v170\n\t"
        "v_add_f32_e32 v192, v150, v172\n\t"
        "v_add_f32_e32 v193, v151, v174\n\t"
        "s_cbranch_vccz .Lo30%=\n\t"
        "s_sleep %c[xslp]\n\t"
        "s_lshl_b32 s82, s83, 2\n\t"
        "buffer_load_dwordx4 v[168:171], v246, %[rd], s82 offen offset:0 sc1\n\t"
        "buffer_load_dwordx4 v[172:175], v246, %[rd], s82 offen offset:1024 sc1\n\t"
        "s_add_u32 s97, s97, 1\n\t"
        "s_cmp_lt_u32 s97, 0x200000\n\t"
        "s_cbranch_scc0 .Lgu%=\n\t"
        "s_waitcnt vmcnt(0)\n\t"
        "s_branch .Lo29%=\n\t"
        ".Lo30%=:\n\t"
        "v_cvt_pk_bf16_f32 v198, 0, v198\n\t"
        "v_cvt_pk_bf16_f32 v190, 0, v190\n\t"
        "v_cvt_pk_bf16_f32 v191, 0, v191\n\t"
        "v_cvt_pk_bf16_f32 v192, 0, v192\n\t"
        "v_cvt_pk_bf16_f32 v193, 0, v193\n\t"
        "v_lshlrev_b32_e32 v186, 16, v182\n\t"
        "v_lshlrev_b32_e32 v194, 16, v178\n\t"
        "v_and_b32_e32 v187, 0xffff0000, v182\n\t"
        "v_and_b32_e32 v195, 0xffff0000, v178\n\t"
        "v_lshlrev_b32_e32 v188, 16, v183\n\t"
        "v_lshlrev_b32_e32 v196, 16, v179\n\t"
        "v_and_b32_e32 v189, 0xffff0000, v183\n\t"
        "v_and_b32_e32 v197, 0xffff0000, v179\n\t"
        "v_mul_f32_e32 v186, v186, v194\n\t"
        "v_mul_f32_e32 v187, v187, v195\n\t"
        "v_mul_f32_e32 v188, v188, v196\n\t"
        "v_mul_f32_e32 v189, v189, v197\n\t"
        "v_mul_f32_e32 v190, v198, v190\n\t"
        "v_mul_f32_e32 v191, v198, v191\n\t"
        "v_mul_f32_e32 v192, v198, v192\n\t"
        "v_mul_f32_e32 v193, v198, v193\n\t"
        "v_cvt_pk_bf16_f32 v186, 0, v186\n\t"
        "v_cvt_pk_bf16_f32 v187, 0, v187\n\t"
        "v_cvt_pk_bf16_f32 v188, 0, v188\n\t"
        "v_cvt_pk_bf16_f32 v189, 0, v189\n\t"
        "v_cvt_pk_bf16_f32 v190, 0, v190\n\t"
        "v_cvt_pk_bf16_f32 v191, 0, v191\n\t"
        "v_cvt_pk_bf16_f32 v192, 0, v192\n\t"
        "v_cvt_pk_bf16_f32 v193, 0, v193\n\t"
        "v_mul_f32_e32 v186, v185, v186\n\t"
        "v_mul_f32_e32 v187, v185, v187\n\t"
        "v_mul_f32_e32 v188, v185, v188\n\t"
        "v_mul_f32_e32 v189, v185, v189\n\t"
        "v_cvt_pk_bf16_f32 v186, 0, v186\n\t"
        "v_cvt_pk_bf16_f32 v187, 0, v187\n\t"
        "v_cvt_pk_bf16_f32 v188, 0, v188\n\t"
        "v_cvt_pk_bf16_f32 v189, 0, v189\n\t"
        "v_add_f32_e32 v186, v186, v190\n\t"
        "v_add_f32_e32 v187, v187, v191\n\t"
        "v_add_f32_e32 v188, v188, v192\n\t"
        "v_add_f32_e32 v189, v189, v193\n\t"
        "v_cvt_pk_bf16_f32 v200, v186, v187\n\t"
        "v_cvt_pk_bf16_f32 v201, v188, v189\n\t"
        ".Lo31%=:\n\t"
        "buffer_store_dwordx2 v[200:201], v245, %[ro], s83 offen\n\t"
        "s_mov_b32 s38, -1\n\t"
        ".Lo25%=:\n\t"
        "v_mov_b32_e32 v152, v128\n\t"
        "v_mov_b32_e32 v153, v129\n\t"
        "v_mov_b32_e32 v154, v130\n\t"
        "v_mov_b32_e32 v155, v131\n\t"
        "v_mov_b32_e32 v156, v132\n\t"
        "v_mov_b32_e32 v157, v133\n\t"
        "v_mov_b32_e32 v158, v134\n\t"
        "v_mov_b32_e32 v159, v135\n\t"
        "s_mov_b32 s39, s44\n\t"
        "s_add_u32 s37, s37, 1\n\t"
        "s_add_u32 s44, s44, 8\n\t"
        "s_xor_b32 s43, s43, 0x8000\n\t"
        "s_branch .Lnew%=\n\t"
        ".Lsol%=:\n\t"
        "s_add_u32 s44, %[rho], 120\n\t"
        "s_mov_b32 s37, 30\n\t"
        ".Lone%=:\n\t"
        "s_add_i32 s80, s44, 0\n\t"
        "s_lshl_b32 s80, s80, 6\n\t"
        "s_or_b32 s81, s44, %[snd]\n\t"
        "s_cmp_lt_i32 s81, 0\n\t"
        "s_cselect_b32 s82, s36, s80\n\t"
        "buffer_load_dword v184, v244, %[rd], s82 offen sc1\n\t"
        "s_lshl_b32 s80, s80, 2\n\t"
        "s_cmp_lt_i32 s81, 0\n\t"
        "s_cselect_b32 s82, s36, s80\n\t"
        "buffer_load_dwordx2 v[180:181], v243, %[rd], s82 offen sc1\n\t"
        "s_lshl_b32 s80, s80, 7\n\t"
        "s_cmp_lt_i32 s81, 0\n\t"
        "s_cselect_b32 s82, s36, s80\n\t"
        "buffer_load_dwordx2 v[176:177], v245, %[rx], s82 offen\n\t"
        "v_add_u32_e32 v222, s43, v247\n\t"
        "v_add_u32_e32 v223, s43, v248\n\t"
        "s_mov_b32 s97, 0\n\t"
        ".Lo32%=:\n\t"
        "s_lshr_b32 s80, s40, s37\n\t"
        "s_andn2_b32 s80, 1, s80\n\t"
        "s_cbranch_scc0 .Lo33%=\n\t"
        "s_cmp_eq_u32 s97, 0\n\t"
        "s_cbranch_scc1 .Lo32%=x\n\t"
        "s_sleep %c[oslp]\n\t"
        ".Lo32%=x:\n\t"
        "buffer_load_dword v220, v242, %[rd], 0 offen sc1\n\t"
        "s_add_u32 s97, s97, 1\n\t"
        "s_cmp_lt_u32 s97, 0x200000\n\t"
        "s_cbranch_scc0 .Lgu%=\n\t"
        "s_waitcnt vmcnt(0)\n\t"
        "v_cmp_eq_u32_e32 vcc, 32, v220\n\t"
        "s_nop 1\n\t"
        "s_or_b32 s40, s40, vcc_lo\n\t"
        "s_branch .Lo32%=\n\t"
        ".Lo33%=:\n\t"
        "s_lshl_b32 s84, s44, 17\n\t"
        "buffer_load_dwordx4 v[0:3], v240, %[rd], s84 offen sc1\n\t"
        "buffer_load_dwordx4 v[4:7], v241, %[rd], s84 offen sc1\n\t"
        "s_add_u32 s84, s84, 8192\n\t"
        "buffer_load_dwordx4 v[8:11], v240, %[rd], s84 offen sc1\n\t"
        "buffer_load_dwordx4 v[12:15], v241, %[rd], s84 offen sc1\n\t"
        "s_add_u32 s84, s84, 8192\n\t"
        "buffer_load_dwordx4 v[16:19], v240, %[rd], s84 offen sc1\n\t"
        "buffer_load_dwordx4 v[20:23], v241, %[rd], s84 offen sc1\n\t"
        "s_add_u32 s84, s84, 8192\n\t"
        "buffer_load_dwordx4 v[24:27], v240, %[rd], s84 offen sc1\n\t"
        "buffer_load_dwordx4 v[28:31], v241, %[rd], s84 offen sc1\n\t"
        "s_add_u32 s84, s84, 8192\n\t"
        "buffer_load_dwordx4 v[32:35], v240, %[rd], s84 offen sc1\n\t"
        "buffer_load_dwordx4 v[36:39], v241, %[rd], s84 offen sc1\n\t"
        "s_add_u32 s84, s84, 8192\n\t"
        "buffer_load_dwordx4 v[40:43], v240, %[rd], s84 offen sc1\n\t"
        "buffer_load_dwordx4 v[44:47], v241, %[rd], s84 offen sc1\n\t"
        "s_add_u32 s84, s84, 8192\n\t"
        "buffer_load_dwordx4 v[48:51], v240, %[rd], s84 offen sc1\n\t"
        "buffer_load_dwordx4 v[52:55], v241, %[rd], s84 offen sc1\n\t"
        "s_add_u32 s84, s84, 8192\n\t"
        "buffer_load_dwordx4 v[56:59], v240, %[rd], s84 offen sc1\n\t"
        "buffer_load_dwordx4 v[60:63], v241, %[rd], s84 offen sc1\n\t"
        "s_add_u32 s84, s84, 73728\n\t"
        "s_waitcnt vmcnt(15)\n\t"
        "v_mfma_f32_16x16x32_bf16 v[128:131], a[128:131], v[0:3], 0\n\t"
        "v_mfma_f32_16x16x32_bf16 v[132:135], a[132:135], v[0:3], 0\n\t"
        "v_mfma_f32_16x16x32_bf16 v[136:139], a[136:139], v[0:3], 0\n\t"
        "v_mfma_f32_16x16x32_bf16 v[140:143], a[140:143], v[0:3], 0\n\t"
        "s_waitcnt vmcnt(14)\n\t"
        "v_mfma_f32_16x16x32_bf16 v[128:131], a[144:147], v[4:7], v[128:131]\n\t"
        "v_mfma_f32_16x16x32_bf16 v[132:135], a[148:151], v[4:7], v[132:135]\n\t"
        "v_mfma_f32_16x16x32_bf16 v[136:139], a[152:155], v[4:7], v[136:139]\n\t"
        "v_mfma_f32_16x16x32_bf16 v[140:143], a[156:159], v[4:7], v[140:143]\n\t"
        "s_waitcnt vmcnt(13)\n\t"
        "v_mfma_f32_16x16x32_bf16 v[128:131], a[160:163], v[8:11], v[128:131]\n\t"
        "v_mfma_f32_16x16x32_bf16 v[132:135], a[164:167], v[8:11], v[132:135]\n\t"
        "v_mfma_f32_16x16x32_bf16 v[136:139], a[168:171], v[8:11], v[136:139]\n\t"
        "v_mfma_f32_16x16x32_bf16 v[140:143], a[172:175], v[8:11], v[140:143]\n\t"
        "s_waitcnt vmcnt(12)\n\t"
        "v_mfma_f32_16x16x32_bf16 v[128:131], a[176:179], v[12:15], v[128:131]\n\t"
        "v_mfma_f32_16x16x32_bf16 v[132:135], a[180:183], v[12:15], v[132:135]\n\t"
        "v_mfma_f32_16x16x32_bf16 v[136:139], a[184:187], v[12:15], v[136:139]\n\t"
        "v_mfma_f32_16x16x32_bf16 v[140:143], a[188:191], v[12:15], v[140:143]\n\t"
        "s_waitcnt vmcnt(11)\n\t"
        "v_mfma_f32_16x16x32_bf16 v[128:131], a[192:195], v[16:19], v[128:131]\n\t"
        "v_mfma_f32_16x16x32_bf16 v[132:135], a[196:199], v[16:19], v[132:135]\n\t"
        "v_mfma_f32_16x16x32_bf16 v[136:139], a[200:203], v[16:19], v[136:139]\n\t"
        "v_mfma_f32_16x16x32_bf16 v[140:143], a[204:207], v[16:19], v[140:143]\n\t"
        "s_waitcnt vmcnt(10)\n\t"
        "v_mfma_f32_16x16x32_bf16 v[128:131], a[208:211], v[20:23], v[128:131]\n\t"
        "v_mfma_f32_16x16x32_bf16 v[132:135], a[212:215], v[20:23], v[132:135]\n\t"
        "v_mfma_f32_16x16x32_bf16 v[136:139], a[216:219], v[20:23], v[136:139]\n\t"
        "v_mfma_f32_16x16x32_bf16 v[140:143], a[220:223], v[20:23], v[140:143]\n\t"
        "s_waitcnt vmcnt(9)\n\t"
        "v_mfma_f32_16x16x32_bf16 v[128:131], a[224:227], v[24:27], v[128:131]\n\t"
        "v_mfma_f32_16x16x32_bf16 v[132:135], a[228:231], v[24:27], v[132:135]\n\t"
        "v_mfma_f32_16x16x32_bf16 v[136:139], a[232:235], v[24:27], v[136:139]\n\t"
        "v_mfma_f32_16x16x32_bf16 v[140:143], a[236:239], v[24:27], v[140:143]\n\t"
        "s_waitcnt vmcnt(8)\n\t"
        "v_mfma_f32_16x16x32_bf16 v[128:131], a[240:243], v[28:31], v[128:131]\n\t"
        "v_mfma_f32_16x16x32_bf16 v[132:135], a[244:247], v[28:31], v[132:135]\n\t"
        "v_mfma_f32_16x16x32_bf16 v[136:139], a[248:251], v[28:31], v[136:139]\n\t"
        "v_mfma_f32_16x16x32_bf16 v[140:143], a[252:255], v[28:31], v[140:143]\n\t"
        "s_waitcnt vmcnt(7)\n\t"
        "v_mfma_f32_16x16x32_bf16 v[128:131], a[0:3], v[32:35], v[128:131]\n\t"
        "v_mfma_f32_16x16x32_bf16 v[132:135], a[4:7], v[32:35], v[132:135]\n\t"
        "v_mfma_f32_16x16x32_bf16 v[136:139], a[8:11], v[32:35], v[136:139]\n\t"
        "v_mfma_f32_16x16x32_bf16 v[140:143], a[12:15], v[32:35], v[140:143]\n\t"
        "s_waitcnt vmcnt(6)\n\t"
        "v_mfma_f32_16x16x32_bf16 v[128:131], a[16:19], v[36:39], v[128:131]\n\t"
        "v_mfma_f32_16x16x32_bf16 v[132:135], a[20:23], v[36:39], v[132:135]\n\t"
        "v_mfma_f32_16x16x32_bf16 v[136:139], a[24:27], v[36:39], v[136:139]\n\t"
        "v_mfma_f32_16x16x32_bf16 v[140:143], a[28:31], v[36:39], v[140:143]\n\t"
        "s_waitcnt vmcnt(5)\n\t"
        "v_mfma_f32_16x16x32_bf16 v[128:131], a[32:35], v[40:43], v[128:131]\n\t"
        "v_mfma_f32_16x16x32_bf16 v[132:135], a[36:39], v[40:43], v[132:135]\n\t"
        "v_mfma_f32_16x16x32_bf16 v[136:139], a[40:43], v[40:43], v[136:139]\n\t"
        "v_mfma_f32_16x16x32_bf16 v[140:143], a[44:47], v[40:43], v[140:143]\n\t"
        "s_waitcnt vmcnt(4)\n\t"
        "v_mfma_f32_16x16x32_bf16 v[128:131], a[48:51], v[44:47], v[128:131]\n\t"
        "v_mfma_f32_16x16x32_bf16 v[132:135], a[52:55], v[44:47], v[132:135]\n\t"
        "v_mfma_f32_16x16x32_bf16 v[136:139], a[56:59], v[44:47], v[136:139]\n\t"
        "v_mfma_f32_16x16x32_bf16 v[140:143], a[60:63], v[44:47], v[140:143]\n\t"
        "s_waitcnt vmcnt(3)\n\t"
        "v_mfma_f32_16x16x32_bf16 v[128:131], a[64:67], v[48:51], v[128:131]\n\t"
        "v_mfma_f32_16x16x32_bf16 v[132:135], a[68:71], v[48:51], v[132:135]\n\t"
        "v_mfma_f32_16x16x32_bf16 v[136:139], a[72:75], v[48:51], v[136:139]\n\t"
        "v_mfma_f32_16x16x32_bf16 v[140:143], a[76:79], v[48:51], v[140:143]\n\t"
        "s_waitcnt vmcnt(2)\n\t"
        "v_mfma_f32_16x16x32_bf16 v[128:131], a[80:83], v[52:55], v[128:131]\n\t"
        "v_mfma_f32_16x16x32_bf16 v[132:135], a[84:87], v[52:55], v[132:135]\n\t"
        "v_mfma_f32_16x16x32_bf16 v[136:139], a[88:91], v[52:55], v[136:139]\n\t"
        "v_mfma_f32_16x16x32_bf16 v[140:143], a[92:95], v[52:55], v[140:143]\n\t"
        "s_waitcnt vmcnt(1)\n\t"
        "v_mfma_f32_16x16x32_bf16 v[128:131], a[96:99], v[56:59], v[128:131]\n\t"
        "v_mfma_f32_16x16x32_bf16 v[132:135], a[100:103], v[56:59], v[132:135]\n\t"
        "v_mfma_f32_16x16x32_bf16 v[136:139], a[104:107], v[56:59], v[136:139]\n\t"
        "v_mfma_f32_16x16x32_bf16 v[140:143], a[108:111], v[56:59], v[140:143]\n\t"
        "s_waitcnt vmcnt(0)\n\t"
        "v_mfma_f32_16x16x32_bf16 v[128:131], a[112:115], v[60:63], v[128:131]\n\t"
        "v_mfma_f32_16x16x32_bf16 v[132:135], a[116:119], v[60:63], v[132:135]\n\t"
        "v_mfma_f32_16x16x32_bf16 v[136:139], a[120:123], v[60:63], v[136:139]\n\t"
        "v_mfma_f32_16x16x32_bf16 v[140:143], a[124:127], v[60:63], v[140:143]\n\t"
        "s_nop 4\n\t"
        "ds_write_b128 v222, v[128:131] offset:0\n\t"
        "ds_write_b128 v222, v[132:135] offset:1024\n\t"
        "ds_write_b128 v222, v[136:139] offset:2048\n\t"
        "ds_write_b128 v222, v[140:143] offset:3072\n\t"
        "s_waitcnt lgkmcnt(0)\n\t"
        "s_barrier\n\t"
        "ds_read_b128 v[186:189], v223 offset:0\n\t"
        "ds_read_b128 v[190:193], v223 offset:8192\n\t"
        "ds_read_b128 v[194:197], v223 offset:16384\n\t"
        "ds_read_b128 v[198:201], v223 offset:24576\n\t"
        "s_waitcnt lgkmcnt(0)\n\t"
        "v_add_f32_e32 v186, v186, v190\n\t"
        "v_add_f32_e32 v187, v187, v191\n\t"
        "v_add_f32_e32 v188, v188, v192\n\t"
        "v_add_f32_e32 v189, v189, v193\n\t"
        "v_add_f32_e32 v194, v194, v198\n\t"
        "v_add_f32_e32 v195, v195, v199\n\t"
        "v_add_f32_e32 v196, v196, v200\n\t"
        "v_add_f32_e32 v197, v197, v201\n\t"
        "v_add_f32_e32 v204, v186, v194\n\t"
        "v_add_f32_e32 v206, v187, v195\n\t"
        "v_add_f32_e32 v208, v188, v196\n\t"
        "v_add_f32_e32 v210, v189, v197\n\t"
        "s_add_i32 s80, s44, 0\n\t"
        "s_lshl_b32 s80, s80, 17\n\t"
        "s_or_b32 s81, s44, %[own]\n\t"
        "s_cmp_lt_i32 s81, 0\n\t"
        "s_cselect_b32 s82, s36, s80\n\t"
        "buffer_store_dwordx4 v[204:207], v246, %[rd], s82 offen offset:0 sc1\n\t"
        "buffer_store_dwordx4 v[208:211], v246, %[rd], s82 offen offset:1024 sc1\n\t"
        "v_mov_b32_e32 v144, v204\n\t"
        "v_mov_b32_e32 v145, v206\n\t"
        "v_mov_b32_e32 v146, v208\n\t"
        "v_mov_b32_e32 v147, v210\n\t"
        "s_sleep %c[xdel]\n\t"
        "s_add_i32 s80, s44, 0\n\t"
        "s_lshl_b32 s80, s80, 17\n\t"
        "s_or_b32 s81, s44, %[snd]\n\t"
        "s_cmp_lt_i32 s81, 0\n\t"
        "s_cselect_b32 s82, s36, s80\n\t"
        "buffer_load_dwordx4 v[160:163], v246, %[rd], s82 offen offset:0 sc1\n\t"
        "buffer_load_dwordx4 v[164:167], v246, %[rd], s82 offen offset:1024 sc1\n\t"
        "s_waitcnt vmcnt(0)\n\t"
        "s_add_i32 s80, s44, 0\n\t"
        "s_lshl_b32 s80, s80, 15\n\t"
        "s_or_b32 s81, s44, %[snd]\n\t"
        "s_cmp_lt_i32 s81, 0\n\t"
        "s_cselect_b32 s83, s36, s80\n\t"
        "s_cbranch_scc1 .Lo36%=\n\t"
        "s_mov_b32 s97, 0\n\t"
        ".Lo34%=:\n\t"
        "v_xor_b32_e32 v186, %[tag], v161\n\t"
        "v_xor_b32_e32 v187, %[tag], v163\n\t"
        "v_xor_b32_e32 v188, %[tag], v165\n\t"
        "v_or3_b32 v186, v186, v187, v188\n\t"
        "v_xor_b32_e32 v187, %[tag], v167\n\t"
        "v_or_b32_e32 v186, v186, v187\n\t"
        "v_cmp_ne_u32_e32 vcc, 0, v186\n\t"
        "v_sub_f32_e32 v198, 1.0, v184\n\t"
        "v_add_f32_e32 v190, v144, v160\n\t"
        "v_add_f32_e32 v191, v145, v162\n\t"
        "v_add_f32_e32 v192, v146, v164\n\t"
        "v_add_f32_e32 v193, v147, v166\n\t"
        "s_cbranch_vccz .Lo35%=\n\t"
        "s_sleep %c[xslp]\n\t"
        "s_lshl_b32 s82, s83, 2\n\t"
        "buffer_load_dwordx4 v[160:163], v246, %[rd], s82 offen offset:0 sc1\n\t"
        "buffer_load_dwordx4 v[164:167], v246, %[rd], s82 offen offset:1024 sc1\n\t"
        "s_add_u32 s97, s97, 1\n\t"
        "s_cmp_lt_u32 s97, 0x200000\n\t"
        "s_cbranch_scc0 .Lgu%=\n\t"
        "s_waitcnt vmcnt(0)\n\t"
        "s_branch .Lo34%=\n\t"
        ".Lo35%=:\n\t"
        "v_cvt_pk_bf16_f32 v198, 0, v198\n\t"
        "v_cvt_pk_bf16_f32 v190, 0, v190\n\t"
        "v_cvt_pk_bf16_f32 v191, 0, v191\n\t"
        "v_cvt_pk_bf16_f32 v192, 0, v192\n\t"
        "v_cvt_pk_bf16_f32 v193, 0, v193\n\t"
        "v_lshlrev_b32_e32 v186, 16, v180\n\t"
        "v_lshlrev_b32_e32 v194, 16, v176\n\t"
        "v_and_b32_e32 v187, 0xffff0000, v180\n\t"
        "v_and_b32_e32 v195, 0xffff0000, v176\n\t"
        "v_lshlrev_b32_e32 v188, 16, v181\n\t"
        "v_lshlrev_b32_e32 v196, 16, v177\n\t"
        "v_and_b32_e32 v189, 0xffff0000, v181\n\t"
        "v_and_b32_e32 v197, 0xffff0000, v177\n\t"
        "v_mul_f32_e32 v186, v186, v194\n\t"
        "v_mul_f32_e32 v187, v187, v195\n\t"
        "v_mul_f32_e32 v188, v188, v196\n\t"
        "v_mul_f32_e32 v189, v189, v197\n\t"
        "v_mul_f32_e32 v190, v198, v190\n\t"
        "v_mul_f32_e32 v191, v198, v191\n\t"
        "v_mul_f32_e32 v192, v198, v192\n\t"
        "v_mul_f32_e32 v193, v198, v193\n\t"
        "v_cvt_pk_bf16_f32 v186, 0, v186\n\t"
        "v_cvt_pk_bf16_f32 v187, 0, v187\n\t"
        "v_cvt_pk_bf16_f32 v188, 0, v188\n\t"
        "v_cvt_pk_bf16_f32 v189, 0, v189\n\t"
        "v_cvt_pk_bf16_f32 v190, 0, v190\n\t"
        "v_cvt_pk_bf16_f32 v191, 0, v191\n\t"
        "v_cvt_pk_bf16_f32 v192, 0, v192\n\t"
        "v_cvt_pk_bf16_f32 v193, 0, v193\n\t"
        "v_mul_f32_e32 v186, v184, v186\n\t"
        "v_mul_f32_e32 v187, v184, v187\n\t"
        "v_mul_f32_e32 v188, v184, v188\n\t"
        "v_mul_f32_e32 v189, v184, v189\n\t"
        "v_cvt_pk_bf16_f32 v186, 0, v186\n\t"
        "v_cvt_pk_bf16_f32 v187, 0, v187\n\t"
        "v_cvt_pk_bf16_f32 v188, 0, v188\n\t"
        "v_cvt_pk_bf16_f32 v189, 0, v189\n\t"
        "v_add_f32_e32 v186, v186, v190\n\t"
        "v_add_f32_e32 v187, v187, v191\n\t"
        "v_add_f32_e32 v188, v188, v192\n\t"
        "v_add_f32_e32 v189, v189, v193\n\t"
        "v_cvt_pk_bf16_f32 v200, v186, v187\n\t"
        "v_cvt_pk_bf16_f32 v201, v188, v189\n\t"
        ".Lo36%=:\n\t"
        "buffer_store_dwordx2 v[200:201], v245, %[ro], s83 offen\n\t"
        "s_add_u32 s37, s37, 1\n\t"
        "s_add_u32 s44, s44, 4\n\t"
        "s_xor_b32 s43, s43, 0x8000\n\t"
        "s_cmp_lt_u32 s37, 32\n\t"
        "s_cbranch_scc1 .Lone%=\n\t"
        "buffer_store_dword v205, off, %[rd], %[ep] sc1\n\t"
        "s_endpgm\n\t"
        ".Lgu%=:\n\t"
        "v_mov_b32_e32 v0, 128\n\t"
        "s_mov_b32 s80, %c[ferr]\n\t"
        "buffer_store_dword v0, off, %[rd], s80 sc1\n\t"
        "s_endpgm"
        :: [rd] "s"(rd), [ro] "s"(ro), [rx] "s"(rx), [w] "s"(w), [rho] "s"(rho), [tag] "s"(tag), [own] "s"(own), [snd] "s"(snd), [ep] "s"(ep), [blk] "s"(j), [t0] "s"(g_t0),
          [ferr] "i"(F_ERR), [fprep] "i"(F_PREP), [fconv] "i"(F_CONV), [fprog] "i"(F_PROG), [dbg] "i"(W_DBG), [oslp] "i"(O_SLP), [xslp] "i"(X_SLP), [xdel] "i"(X_DEL), "{v[176:191]}"(pc)
        : "memory", "vcc", "scc", "s36", "s37", "s38", "s39", "s40", "s41", "s42", "s43", "s44", "s45", "s46", "s47", "s80", "s81", "s82", "s83", "s84", "s85", "s86", "s87", "s88", "s89", "s90", "s91", "s92", "s93", "s94", "s95", "s96", "s97", "s98", "s99", WO_REGS);
}

__device__ __forceinline__ void bulk_block(const Params& p, rsrc_t r, unsigned char* lds, const int wv, const int j, const unsigned tag, const unsigned g_t0) {
    const int x = (j & 7) >> 1, i32 = (j >> 3) * 2 + (j & 1);
    auto flags = [&](int mt, int nmt, int h0, int nh, int mat) {
        DRAIN(); __syncthreads();
        const int tid = otid(); if (tid < nmt * nh) st4(r, F_KVQ + ((mt + tid / nh) * 32 + h0 + tid % nh) * 64 + mat * 4, tag); };
#pragma unroll 1
    for (int t = 0; t < 2; ++t) {
        const int mt = 2 * t + (i32 & 1), mat = (i32 >> 1) & 1, hd = 8 * x + (i32 >> 2), rot = ((i32 >> 1) & 7) * 2;
        const bf16_t* Bm = mat ? p.Wv : p.Wk;
        if (mat) gemm<1, 4, 4, 8>(r, p.Aq, Bm, mt * 128, hd * 128, lds, W_VF, wv, rot, nullptr, t == 1, t == 0 ? Bm : nullptr, (mt + 2) * 128, hd * 128);
        else gemm<0, 4, 4, 8>(r, p.Aq, Bm, mt * 128, hd * 128, lds, W_KX, wv, rot, nullptr, t == 1, t == 0 ? Bm : nullptr, (mt + 2) * 128, hd * 128);
        flags(mt, 1, hd, 1, mat);
    }
    const int hp = 4 * x + (i32 >> 3), q8 = i32 & 7, rot = ROT2 * (i32 >> 2);
    if (SKEW) wait_long(rtnow() + (unsigned)((q8 & 3) * 4 + x) * SKEW);
    {
        const int m = q8 < 4 ? 4 + (q8 & 1) : q8 - 4, mat = q8 < 4 ? q8 >> 1 : 2;
        const bf16_t* Bn = mat == 2 ? p.Wq : mat == 1 ? p.Wv : p.Wk;
        const int mt = 4 + (q8 & 3), mv = q8 >> 2;
        const bf16_t* tk = (wv == 1 && j < 32) ? p.Aq + (64 + j) * 16384 : nullptr;
        if (mv) gemm<1, 4, 8, 4, 0, NoHook, 8>(r, p.Aq, p.Wv, mt * 128, hp * 256, lds, W_VF, wv, rot, tk, false, Bn, m * 256, hp * 256);
        else gemm<0, 4, 8, 4, 0, NoHook, 8>(r, p.Aq, p.Wk, mt * 128, hp * 256, lds, W_KX, wv, rot, tk, false, Bn, m * 256, hp * 256);
        flags(mt, 1, 2 * hp, 2, mv);
        const bf16_t* tl = (wv == 1 && j < 32) ? p.Aq + (96 + j) * 16384 : nullptr;
        if (mat == 1) gemm<1, 8, 8, 4>(r, p.Aq, Bn, m * 256, hp * 256, lds, W_VF, wv, rot, tl, true);
        else gemm<0, 8, 8, 4>(r, p.Aq, Bn, m * 256, hp * 256, lds, mat == 2 ? W_QX : W_KX, wv, rot, tl, true);
        flags(2 * m, 2, 2 * hp, 2, mat);
    }
    {
        unsigned wi = 0u;
        const int l = otid() & 63; const v4u sw = mk_srd(p.Wo, 4096 * 1024 * 2); const unsigned wa = (unsigned)(((i32 & 1) * 2048 + wv * 512 + (l >> 3)) * 2048 + (i32 >> 1) * 128 + (l & 7) * 16);
        unsigned long long tab;
        asm volatile("s_getpc_b64 %0\n\ts_branch .Lwo%=\n\t"
                     "buffer_load_dwordx4 a[128:131], v191, s[92:95], s91 offen\n\ts_setpc_b64 s[96:97]\n\t" "buffer_load_dwordx4 a[132:135], v191, s[92:95], s91 offen\n\ts_setpc_b64 s[96:97]\n\t"
                     "buffer_load_dwordx4 a[136:139], v191, s[92:95], s91 offen\n\ts_setpc_b64 s[96:97]\n\t" "buffer_load_dwordx4 a[140:143], v191, s[92:95], s91 offen\n\ts_setpc_b64 s[96:97]\n\t"
                     "buffer_load_dwordx4 a[144:147], v191, s[92:95], s91 offen\n\ts_setpc_b64 s[96:97]\n\t" "buffer_load_dwordx4 a[148:151], v191, s[92:95], s91 offen\n\ts_setpc_b64 s[96:97]\n\t"
                     "buffer_load_dwordx4 a[152:155], v191, s[92:95], s91 offen\n\ts_setpc_b64 s[96:97]\n\t" "buffer_load_dwordx4 a[156:159], v191, s[92:95], s91 offen\n\ts_setpc_b64 s[96:97]\n\t"
                     "buffer_load_dwordx4 a[160:163], v191, s[92:95], s91 offen\n\ts_setpc_b64 s[96:97]\n\t" "buffer_load_dwordx4 a[164:167], v191, s[92:95], s91 offen\n\ts_setpc_b64 s[96:97]\n\t"
                     "buffer_load_dwordx4 a[168:171], v191, s[92:95], s91 offen\n\ts_setpc_b64 s[96:97]\n\t" "buffer_load_dwordx4 a[172:175], v191, s[92:95], s91 offen\n\ts_setpc_b64 s[96:97]\n\t"
                     "buffer_load_dwordx4 a[176:179], v191, s[92:95], s91 offen\n\ts_setpc_b64 s[96:97]\n\t" "buffer_load_dwordx4 a[180:183], v191, s[92:95], s91 offen\n\ts_setpc_b64 s[96:97]\n\t"
                     "buffer_load_dwordx4 a[184:187], v191, s[92:95], s91 offen\n\ts_setpc_b64 s[96:97]\n\t" "buffer_load_dwordx4 a[188:191], v191, s[92:95], s91 offen\n\ts_setpc_b64 s[96:97]\n\t"
                     "buffer_load_dwordx4 a[192:195], v191, s[92:95], s91 offen\n\ts_setpc_b64 s[96:97]\n\t" "buffer_load_dwordx4 a[196:199], v191, s[92:95], s91 offen\n\ts_setpc_b64 s[96:97]\n\t"
                     "buffer_load_dwordx4 a[200:203], v191, s[92:95], s91 offen\n\ts_setpc_b64 s[96:97]\n\t" "buffer_load_dwordx4 a[204:207], v191, s[92:95], s91 offen\n\ts_setpc_b64 s[96:97]\n\t"
                     "buffer_load_dwordx4 a[208:211], v191, s[92:95], s91 offen\n\ts_setpc_b64 s[96:97]\n\t" "buffer_load_dwordx4 a[212:215], v191, s[92:95], s91 offen\n\ts_setpc_b64 s[96:97]\n\t"
                     "buffer_load_dwordx4 a[216:219], v191, s[92:95], s91 offen\n\ts_setpc_b64 s[96:97]\n\t" "buffer_load_dwordx4 a[220:223], v191, s[92:95], s91 offen\n\ts_setpc_b64 s[96:97]\n\t"
                     "buffer_load_dwordx4 a[224:227], v191, s[92:95], s91 offen\n\ts_setpc_b64 s[96:97]\n\t" "buffer_load_dwordx4 a[228:231], v191, s[92:95], s91 offen\n\ts_setpc_b64 s[96:97]\n\t"
                     "buffer_load_dwordx4 a[232:235], v191, s[92:95], s91 offen\n\ts_setpc_b64 s[96:97]\n\t" "buffer_load_dwordx4 a[236:239], v191, s[92:95], s91 offen\n\ts_setpc_b64 s[96:97]\n\t"
                     "buffer_load_dwordx4 a[240:243], v191, s[92:95], s91 offen\n\ts_setpc_b64 s[96:97]\n\t" "buffer_load_dwordx4 a[244:247], v191, s[92:95], s91 offen\n\ts_setpc_b64 s[96:97]\n\t"
                     "buffer_load_dwordx4 a[248:251], v191, s[92:95], s91 offen\n\ts_setpc_b64 s[96:97]\n\t" "buffer_load_dwordx4 a[252:255], v191, s[92:95], s91 offen\n\ts_setpc_b64 s[96:97]\n\t"
                     "buffer_load_dwordx4 v[192:195], v191, s[92:95], s91 offen\n\ts_setpc_b64 s[96:97]\n\t" "buffer_load_dwordx4 v[196:199], v191, s[92:95], s91 offen\n\ts_setpc_b64 s[96:97]\n\t"
                     "buffer_load_dwordx4 v[200:203], v191, s[92:95], s91 offen\n\ts_setpc_b64 s[96:97]\n\t" "buffer_load_dwordx4 v[204:207], v191, s[92:95], s91 offen\n\ts_setpc_b64 s[96:97]\n\t"
                     "buffer_load_dwordx4 v[208:211], v191, s[92:95], s91 offen\n\ts_setpc_b64 s[96:97]\n\t" "buffer_load_dwordx4 v[212:215], v191, s[92:95], s91 offen\n\ts_setpc_b64 s[96:97]\n\t"
                     "buffer_load_dwordx4 v[216:219], v191, s[92:95], s91 offen\n\ts_setpc_b64 s[96:97]\n\t" "buffer_load_dwordx4 v[220:223], v191, s[92:95], s91 offen\n\ts_setpc_b64 s[96:97]\n\t"
                     "buffer_load_dwordx4 v[224:227], v191, s[92:95], s91 offen\n\ts_setpc_b64 s[96:97]\n\t" "buffer_load_dwordx4 v[228:231], v191, s[92:95], s91 offen\n\ts_setpc_b64 s[96:97]\n\t"
                     "buffer_load_dwordx4 v[232:235], v191, s[92:95], s91 offen\n\ts_setpc_b64 s[96:97]\n\t" "buffer_load_dwordx4 v[236:239], v191, s[92:95], s91 offen\n\ts_setpc_b64 s[96:97]\n\t"
                     "buffer_load_dwordx4 v[240:243], v191, s[92:95], s91 offen\n\ts_setpc_b64 s[96:97]\n\t" "buffer_load_dwordx4 v[244:247], v191, s[92:95], s91 offen\n\ts_setpc_b64 s[96:97]\n\t"
                     "buffer_load_dwordx4 v[248:251], v191, s[92:95], s91 offen\n\ts_setpc_b64 s[96:97]\n\t" "buffer_load_dwordx4 v[252:255], v191, s[92:95], s91 offen\n\ts_setpc_b64 s[96:97]\n\t"
                     "buffer_load_dwordx4 v191, s[92:95], s91 offen lds\n\ts_setpc_b64 s[96:97]\n.Lwo%=:" : "=s"(tab));
        auto hook = [&](int) {
            const unsigned q = min(wi, 63u);
            asm volatile("s_mov_b32 m0, %1\n\ts_swappc_b64 s[96:97], %0"
                         :: "s"(tab + (min(q, 48u) * 12u + 4u)), "s"(WO_L + wv * 16384 + (q - 48u) * 1024u), "{v191}"(wa), "{s[92:95]}"(sw), "{s91}"(q * 16384u) : "memory", "m0", "s96", "s97", WO_REGS);
            ++wi; };
#pragma unroll 1
        for (int t = 0; t < 2; ++t) {
            const int mt = t ? 8 + q8 : 12 + (q8 & 3), mv = t ? 2 : q8 >> 2;
            if (mv == 1) gemm<1, 4, 8, 4, 1>(r, p.Aq, p.Wv, mt * 128, hp * 256, lds, W_VF, wv, rot, nullptr, false, p.Wq, (8 + q8) * 128, hp * 256, hook);
            else gemm<0, 4, 8, 4, 1>(r, p.Aq, t ? p.Wq : p.Wk, mt * 128, hp * 256, lds, t ? W_QX : W_KX, wv, rot, nullptr, t == 1, t ? nullptr : p.Wq, (8 + q8) * 128, hp * 256, hook);
            flags(mt, 1, 2 * hp, 2, mv);
        }
        oproj_block(p, wv, i32, x, j, tag, g_t0);
    }
}

__device__ __forceinline__ void help_block(const Params& p, rsrc_t r, unsigned char* lds, const int wv, const int k, const unsigned tag, const unsigned far) {
    const int j = -32 + k; (void)j;
    {
        const unsigned th = rtnow() + HELP_D + (unsigned)k * SKEWP, OOB = 0xF0000000u;
        const int l = otid() & 63, n16 = l & 15;
        const rsrc_t rh = __builtin_amdgcn_make_buffer_rsrc(p.ws + hx_base(p.ws), 0, U_END * 16384, 0x00020000);
        const unsigned un = (unsigned)(wv * 32 + (k & 30)) * 16384u + (k & 1) * 4096u, ca = l == 0 ? un : OOB;
        v4u z[4]; unsigned best[4] = {~0u, ~0u, ~0u, ~0u};
#pragma unroll
        for (int i = 0; i < 4; ++i) z[i] = (v4u){~0u, ~0u, ~0u, ~0u};
        for (int i = 0; i < CAL_N; ++i)
#pragma unroll
            for (int q = 0; q < 4; ++q) {
                const unsigned t0 = (unsigned)__builtin_amdgcn_s_memtime(); __builtin_amdgcn_s_waitcnt(0);
                st16(rh, ca + q * 8192, z[0]); DRAIN();
                const unsigned t1 = (unsigned)__builtin_amdgcn_s_memtime(); __builtin_amdgcn_s_waitcnt(0);
                best[q] = min(best[q], t1 - t0); }
        const unsigned up = min(best[1], best[3]) < min(best[0], best[2]) ? 1u : 0u, vote = (up ^ far) | ((up ? best[2] < best[0] : best[3] < best[1]) ? 2u : 0u);
        if (wv == 0 && k < 8) {
#pragma unroll
            for (int i = 0; i < 4; ++i) z[i] = __builtin_elementwise_min(*(const v4u*)(p.mem0 + ((n16 & 1) + 2 * (n16 >> 2)) * 1024 + ((k & 3) * 4 + i) * 64 + (l >> 4) * 16 + (n16 & 2) * 4), (v4u){~1u, ~1u, ~1u, ~1u}); }
#pragma unroll
        for (int i = 0; i < 16; ++i) st16(rh, un + (i >> 2) * 8192 + (i & 3) * 1024 + l * 16, z[i & 3]);
        st4(r, (wv == 3 && l < 3) ? F_PROG + k * 128 + l * 8 : OOB, l ? 63u : 0u);
        DRAIN();
        st8(r, l == 0 ? F_CAL + k * 32 + wv * 8 : OOB, (v2u){tag, vote});
        wait_long(th);
    }
    prep_block(p, r, k, wv, tag);
    if (!conv_block(p, r, k, lds, wv, j, tag)) return;
    attn_block(p, r, lds, wv, k, j, tag);
}

__global__ void __launch_bounds__(256, 1) k_main(Params p) {
    extern __shared__ __attribute__((aligned(16))) unsigned char lds[];
    const unsigned g_t0 = DIAG ? rtnow() : 0u;
    rsrc_t r = __builtin_amdgcn_make_buffer_rsrc(p.ws, 0, W_TOTAL, 0x00020000);
    const int wv = __builtin_amdgcn_readfirstlane(threadIdx.x >> 6);
    int role = blockIdx.x;
    unsigned xcc; asm volatile("s_getreg_b32 %0, hwreg(HW_REG_XCC_ID)" : "=s"(xcc)); xcc &= 7u;
    unsigned old = 0u, xr = xcc, rk = blockIdx.x >> 3;
    if (MIXH) { if (xcc == 3u) { xr = 4u + (rk >> 3); rk = 24u + (rk & 7u); } else if (xcc > 3u && rk >= 24u) { xr = 3u; rk = (xcc - 4u) * 8u + (rk & 7u); } }
    unsigned slot = (xr >> 2) * 128u + (rk >> 1) * 8u + (xr & 3u) * 2u + (rk & 1u);
    const unsigned tag = __builtin_amdgcn_readfirstlane(ld4(r, (threadIdx.x & 63) == 1 ? F_CLM + slot * 64 + 4 : F_EP)) * 2654435761u + 0x9E3779B9u;
    if (threadIdx.x == 0) old = __hip_atomic_exchange((unsigned*)(p.ws + F_CLM + slot * 64), tag, __ATOMIC_RELAXED, __HIP_MEMORY_SCOPE_AGENT);
    if (threadIdx.x == 0) {
        for (int i = 0; i < 256 && old == tag; ++i) { slot = (slot + 1u) & 255u; old = __hip_atomic_exchange((unsigned*)(p.ws + F_CLM + slot * 64), tag, __ATOMIC_RELAXED, __HIP_MEMORY_SCOPE_AGENT); }
        *(volatile __attribute__((address_space(3))) int*)(size_t)(unsigned)(size_t)lds = (int)slot;
    }
    __syncthreads();
    role = __builtin_amdgcn_readfirstlane(*(volatile __attribute__((address_space(3))) int*)(size_t)(unsigned)(size_t)lds) & 255;
    __syncthreads();
    if (role >= 128) bulk_block(p, r, lds, wv, role - 128, tag, g_t0);
    else if ((role & 6) == 6) help_block(p, r, lds, wv, (role >> 3) * 2 + (role & 1), tag, xcc >> 2);
    else rec_block(p, wv, role, tag);
}

extern "C" void kernel_launch(void* const* d_in, const int* in_sizes, int n_in, void* d_out, int out_size, void* d_ws, size_t ws_size, hipStream_t stream) {
    Params p;
    p.Aq = (const bf16_t*)d_in[0]; p.Ak = (const bf16_t*)d_in[1]; p.Av = (const bf16_t*)d_in[2];
    p.shift_k = (const bf16_t*)d_in[3]; p.ssm_k = (const bf16_t*)d_in[4]; p.ssm_D = (const bf16_t*)d_in[5];
    p.mem0 = (const bf16_t*)d_in[7]; p.Wq = (const bf16_t*)d_in[8]; p.Wk = (const bf16_t*)d_in[9];
    p.Wv = (const bf16_t*)d_in[10]; p.Wo = (const bf16_t*)d_in[11]; p.gate_w = (const bf16_t*)d_in[13];
    p.out = (bf16_t*)d_out; p.ws = (unsigned char*)d_ws;
    (void)hipFuncSetAttribute((const void*)k_main, hipFuncAttributeMaxDynamicSharedMemorySize, L_TOTAL);
    k_main<<<256, 256, L_TOTAL, stream>>>(p);
}
```

```cpp
#include <hip/hip_runtime.h>
#include <stdint.h>
#include <type_traits>
typedef unsigned short bf16_t;
using bf16x8 = __attribute__((ext_vector_type(8))) short;
using f32x4 = __attribute__((ext_vector_type(4))) float;
typedef unsigned v4u __attribute__((ext_vector_type(4)));
typedef unsigned v2u __attribute__((ext_vector_type(2)));
typedef __amdgpu_buffer_rsrc_t rsrc_t;

typedef __bf16 hbf2 __attribute__((ext_vector_type(2)));
typedef float f32x2 __attribute__((ext_vector_type(2)));
__device__ __forceinline__ unsigned pack2(float a, float b) { f32x2 v = {a, b}; return __builtin_bit_cast(unsigned, __builtin_convertvector(v, hbf2)); }
__device__ __forceinline__ float rbfh(float f) { return __uint_as_float(pack2(0.f, f)); }
__device__ __forceinline__ f32x2 rbf2(f32x2 f) { return (f32x2){rbfh(f.x), rbfh(f.y)}; }
__device__ __forceinline__ unsigned nosent(unsigned x) {
    typedef unsigned short us2 __attribute__((ext_vector_type(2)));
    const us2 a = __builtin_bit_cast(us2, x), c = {(unsigned short)0xFFFE, (unsigned short)0xFFFE}; return __builtin_bit_cast(unsigned, __builtin_elementwise_min(a, c)); }

constexpr int S = 2048, D = 1024, H = 32, DH = 128, M = 8, SEG = 32, NSEG = 64;

constexpr unsigned W_FLG = 0;
constexpr unsigned F_KVQ = W_FLG;
constexpr unsigned F_EP = W_FLG + 32768;
constexpr unsigned F_ERR = W_FLG + 32768 + 256;
constexpr unsigned F_PREP = W_FLG + 32768 + 512;
constexpr unsigned F_CAL = W_FLG + 73728;
constexpr unsigned F_CLM = W_FLG + 77824;
constexpr unsigned F_CONV = W_FLG + 33792;
constexpr unsigned F_PROG = W_FLG + 36864;
constexpr unsigned F_ATT = W_FLG + 40960;
constexpr unsigned F_RDY = W_FLG + 49152;
constexpr unsigned FLG_BYTES = 94208;
constexpr unsigned HO_KVM = W_FLG + FLG_BYTES;
constexpr unsigned W_A = HO_KVM + 64 * 32 * 4096;
constexpr unsigned W_GATE = W_A + S * D * 2;
constexpr unsigned W_QX = W_GATE + 8192;
constexpr unsigned W_KX = W_QX + S * 4096 * 2;
constexpr unsigned W_VF = W_KX + S * 4096 * 2;
constexpr unsigned W_OX = W_VF + 64 * 32 * 8192;
constexpr unsigned DBG_BYTES = 0;
constexpr unsigned W_DBG = W_OX + S * 4096 * 2;
constexpr unsigned W_U = W_DBG + DBG_BYTES;
constexpr unsigned W_XC = W_U + S * D * 2;
constexpr unsigned W_TOTAL = W_XC + 128 * 131072;

__device__ __forceinline__ v4u ld16(rsrc_t r, unsigned off) { return __builtin_amdgcn_raw_buffer_load_b128(r, off, 0, 16); }
__device__ __forceinline__ v2u ld8(rsrc_t r, unsigned off) { return __builtin_amdgcn_raw_buffer_load_b64(r, off, 0, 16); }
__device__ __forceinline__ unsigned ld4(rsrc_t r, unsigned off) { return __builtin_amdgcn_raw_buffer_load_b32(r, off, 0, 16); }
__device__ __forceinline__ void st16(rsrc_t r, unsigned off, v4u v) { __builtin_amdgcn_raw_buffer_store_b128(v, r, off, 0, 16); }
__device__ __forceinline__ void st8(rsrc_t r, unsigned off, v2u v) { __builtin_amdgcn_raw_buffer_store_b64(v, r, off, 0, 16); }
__device__ __forceinline__ void st4(rsrc_t r, unsigned off, unsigned v) { __builtin_amdgcn_raw_buffer_store_b32(v, r, off, 0, 16); }
__device__ __forceinline__ bf16x8 as_frag(v4u v) { return __builtin_bit_cast(bf16x8, v); }
#define D1 134
#define D2 90
#define D3 64
#define LB0 16
#define LC0 7
#define LA0 8
#define TR_UP 4
#define TR_DN 2
#define L_DEC 15
#define STAG 1
#define UP_SH 2
#define EARLY_FROM 58
#define OUT_WARM 0
#define AROT 0
#define P_SLP 2
#define O_SLP 2
#define W_NT 0
#define TOL 2
#define A_SLP 0
#define PEN2 20
#define A_UP 6
#define CT0 2
#define CPF 1
#define PH_M 3
#define SKEW 20
#define SKEW1 0
#define XT 1
#define TRK 3
#define WO_NT 0
#define WO_TRK 1
#define WIN 3
#define ROT0 3
#define ROT1 2
#define ROT2 2
#define B_NT 0
#define CALM 15
#define R4M 1
#define HX_SH 0u
#define FINE_T 1
#define FINE_N 0
#define FINE_A 0
#define O_ND 12
#define PEN0 32
#define X0C 20
#define X0A 16
#define X1A 8
#define PEN 40
#define MEMBAR() asm volatile("" ::: "memory")
#define ENDPGM() asm volatile("s_endpgm" ::: "memory")
#define DRAIN() asm volatile("s_waitcnt vmcnt(0)" ::: "memory")

__device__ __forceinline__ float xor16_max(float x) { auto r = __builtin_amdgcn_permlane16_swap(__float_as_uint(x), __float_as_uint(x), false, false); return fmaxf(__uint_as_float(r[0]), __uint_as_float(r[1])); }
__device__ __forceinline__ float xor32_max(float x) { auto r = __builtin_amdgcn_permlane32_swap(__float_as_uint(x), __float_as_uint(x), false, false); return fmaxf(__uint_as_float(r[0]), __uint_as_float(r[1])); }
__device__ __forceinline__ float xor16_add(float x) { auto r = __builtin_amdgcn_permlane16_swap(__float_as_uint(x), __float_as_uint(x), false, false); return __uint_as_float(r[0]) + __uint_as_float(r[1]); }
__device__ __forceinline__ float xor32_add(float x) { auto r = __builtin_amdgcn_permlane32_swap(__float_as_uint(x), __float_as_uint(x), false, false); return __uint_as_float(r[0]) + __uint_as_float(r[1]); }

typedef short v4i16_t __attribute__((ext_vector_type(4)));
__device__ __forceinline__ v2u trb(unsigned lds_addr) { return __builtin_bit_cast(v2u, __builtin_amdgcn_ds_read_tr16_b64_v4i16((__attribute__((address_space(3))) v4i16_t*)(size_t)lds_addr)); }
__device__ __forceinline__ int otid_(int wv) { int t; asm volatile("v_mbcnt_lo_u32_b32 %0, -1, 0\n\tv_mbcnt_hi_u32_b32 %0, -1, %0" : "=v"(t)); return wv * 64 + t; }
#define otid() otid_(wv)
__device__ __forceinline__ bool wait_word(rsrc_t r, unsigned off, unsigned want, unsigned code) {
    for (unsigned spin = 0;; ++spin) { MEMBAR();
        const unsigned v = __builtin_amdgcn_readfirstlane(ld4(r, off));
        if (v >= want) return true;
        if (spin > (1u << 20)) { st4(r, F_ERR, code); return false; }
        __builtin_amdgcn_s_sleep(8); }
}

__device__ __forceinline__ bool wait_prep(rsrc_t r, int l, unsigned tag) {
    for (unsigned spin = 0;; ++spin) { MEMBAR();
        const unsigned v = ld4(r, F_PREP + (l & 31) * 4);
        if (__all(v == tag)) return true;
        if (spin > (1u << 20)) { st4(r, F_ERR, 512u); return false; }
        __builtin_amdgcn_s_sleep(8); }
}
__device__ __forceinline__ unsigned rtnow() { return (unsigned)__builtin_amdgcn_s_memrealtime(); }
__device__ __forceinline__ void wait_long(unsigned target) { for (int i = 0; i < 4096; ++i) { if ((int)(rtnow() - target) >= 0) break; __builtin_amdgcn_s_sleep(8); } }
constexpr int L_TAB = 20480;
constexpr int L_QK = 24576;
__device__ __forceinline__ unsigned img(int f) { return (unsigned)(L_QK + f * 16 + (f >> 4) * 128); }
constexpr int L_FL = 30720, L_Z = L_FL + 128, L_LOG = 31744, L_W = 32768, L_V = 131072;
constexpr int L_TOTAL = 163840;
constexpr unsigned U_END = 96;
#define PMIN 82
#define V_SLP 2
#define R_SLP 0
constexpr unsigned WO_L = 98304;
#define HELP_D 200
#define V_DEL 24
#define XR 8
#define PFW 0
#define KEYM 3
#define DIAG 0
#define NEARX 0
#define MIXH 1
#define SKEWH 10
#define SKEWP 8
#define NEARY 0
#define CS1 0x8000u
#define CS2 0x10000u
#define X_SLP 2
#define X_DEL 6
#define FASTP 0
#define CV1 3
#define AT1 7
#define CVH 8
#define DN_SH 1
#define EPI_SLP 0
#define AT_SLP 18
#define VA_SLP 0
#define C_SLP 0
#define ATD 0
#define S0B 40
#define BT_SLP 0
#define CAL_O 0u
#define H_SLP 0
#define CAL_N 3
#define STEP_MIN 180u

struct Params {
    const bf16_t *Aq, *Ak, *Av, *shift_k, *ssm_k, *ssm_D, *mem0, *Wq, *Wk, *Wv, *Wo, *gate_w;
    bf16_t* out; unsigned char* ws;
};
__host__ __device__ __forceinline__ unsigned hx_base(const void* ws) { const size_t a = (size_t)ws; return (unsigned)((((a + W_TOTAL + 0x7FFFFFu) >> 23) << 23) - a) + HX_SH; }

typedef short v4s __attribute__((ext_vector_type(4)));
typedef unsigned v32u __attribute__((ext_vector_type(32)));
typedef unsigned v16u __attribute__((ext_vector_type(16)));
typedef unsigned v8u __attribute__((ext_vector_type(8)));
typedef unsigned v12u __attribute__((ext_vector_type(12)));
__device__ __forceinline__ v4u mk_srd(const void* p, unsigned bytes) { const size_t a = (size_t)p; v4u d;
    d.x = __builtin_amdgcn_readfirstlane((unsigned)a); d.y = __builtin_amdgcn_readfirstlane((unsigned)(a >> 32) & 0xFFFFu); d.z = __builtin_amdgcn_readfirstlane(bytes); d.w = 0x00020000u; return d; }
__device__ __forceinline__ bf16x8 ldsa(unsigned addr) { return *(const __attribute__((address_space(3))) bf16x8*)(size_t)addr; }
__device__ __forceinline__ void rec_block(const Params& p, const int wv, const int b, const unsigned tag) {
    const int w = wv, tid = otid(), l = tid & 63, n16 = l & 15, g = l >> 4, c = n16 >> 2;
    const int x = (b & 7) >> 1, i32 = (b >> 3) * 2 + (b & 1);
    const unsigned OOB = 0xF0000000u;
    const v4u rhd = mk_srd(p.ws + hx_base(p.ws), U_END * 16384), wsd = mk_srd(p.ws, W_TOTAL);
    const int G = x - 1, vi = G * 32 + i32, h = x == 0 ? i32 : vi >> 1;
    const unsigned pr_a = F_PROG + h * 128 + (x == 0 ? 0 : 8 + (vi & 1) * 8), cal = F_CAL - w * 1024, kvo = HO_KVM + h * 4096 + (x ? 2048 : 0);
    if (CPF) {
        unsigned lo, hi, l2; asm volatile("s_getpc_b64 s[98:99]\n.Lpc%=:\n\ts_add_u32 %2, s98, rec_v_code-.Lpc%=\n\ts_add_u32 %0, s98, rec_s_code-.Lpc%=\n\ts_addc_u32 %1, s99, 0" : "=s"(lo), "=s"(hi), "=s"(l2) :: "s98", "s99", "scc");
        const size_t a = (((size_t)hi << 32) | lo) + (size_t)(long long)((x ? (int)(l2 - lo) : 0) + ((i32 * 4 + w) % 12) * 1024 - 4096 + l * 16);
        asm volatile("s_mov_b32 m0, %1\n\ts_nop 0\n\tglobal_load_lds_dwordx4 %0, off" :: "v"(a & ~(size_t)15), "s"(x ? L_V + 16384 : 0) : "memory", "m0");
    }
    typedef __attribute__((address_space(3))) volatile unsigned lu32;
    auto cpy = [](int k) -> unsigned { return k == 1 ? 0u : k * 32768u + 16384u; };
    auto cun = [](int u) -> unsigned { return ((u & 1) ? CS1 : 0u) ^ ((u & 2) ? CS2 : 0u); };
    unsigned nA = cpy(x), nW = cpy(w), nT = 64 * 16384 + (x ? 0 : h * 8), nS = 64 * 16384 + x * 1024, nC = 32 * 16384 + (x == 2 ? 16384 : 0), b0 = 65 * 16384 + (h >> 2) * 32768 + (h & 3) * 2048;
    unsigned pob = w == 0 ? nA + (i32 >> 4) * 32768 + (i32 & 15) * 512 : (w == 1 && x) ? (nC ^ cun(i32 >> 3)) + (i32 & 7) * 1024 : (w == 2 && i32 == 0) ? nS : OOB;
    if (x == 0) {
        const v4u sq = mk_srd(p.Wq, 1024 * 4096 * 2), sk = mk_srd(p.Wk, 1024 * 4096 * 2);
        const unsigned m0b = L_W + w * 1024, f = (c & 1) | ((g & 1) << 1) | ((c >> 1) << 2);
        const unsigned t0 = L_W + (16 * (g >> 1) + 4 * (g & 1) + (c & 1) + 8 * (c >> 1)) * 256 + (((2 * w) ^ f) << 5) + (n16 & 3) * 8;
        const unsigned dv = (4 * w + g) * 8192 + h * 256 + ((n16 ^ (((g & 1) | (w << 1)) << 1)) << 4);
        v8u pa;
#pragma unroll
        for (int q = 0; q < 8; ++q) pa[q] = (t0 + ((w + (q >> 1)) & 3) * 32768) ^ ((q & 1) << 5);
        const unsigned kx = W_KX + h * 8192, vx = W_VF + h * 8192 + w * 1024, kf = F_KVQ + h * 64;
        const unsigned aq = img(8 * g + (n16 >> 2)) + (n16 & 1) * 8;
        const v8u pc = {(unsigned)tid * 16, (unsigned)(L_W + w * 32768 + l * 16), (n16 & 2) ? 0xEEEEu : 0x4444u, aq,
                  (unsigned)(((w + 1) & 3) * 1024 + l * 16), (unsigned)(((w + 2) & 3) * 1024 + l * 16), (unsigned)(((w + 3) & 3) * 1024 + l * 16), img(32 * w + n16) + g * 4};
        const v2u pd = {(n16 & 2) ? (unsigned)L_Z : aq + w * 768, (n16 & 2) ? aq + w * 768 : (unsigned)L_Z};
        *(__attribute__((address_space(3))) v4u*)(size_t)(L_TAB + tid * 16) = (v4u){(unsigned)(L_QK + 3072 + g * 384 + n16 * 8),
                  n16 < 8 ? (unsigned)((((n16 >> 2) * 16384) ^ cun((h >> 1) & 3)) + ((h & 1) * 4 + (h >> 3)) * 1024 + w * 256 + (g >> 1) * 128 + (n16 & 2) * 32 + (g & 1) * 32 + (n16 & 1) * 16) : OOB,
                  (unsigned)((32 * w + 16 * (g & 1) + n16) * 16 + (g >> 1) * 8), (unsigned)(L_QK + 3072 + w * 768 + ((n16 & 1) + 2 * (n16 >> 2)) * 32 + g * 8 + (n16 & 2) * 2)};
        if (tid < 64) ((lu32*)(size_t)L_FL)[tid] = 0u;
    asm volatile(
        "s_add_u32 s80, %[m0b], 0\n\t"
        "s_mov_b32 s81, 0\n\t"
        "s_mov_b32 s82, 4\n\t"
        ".Ld0%=:\n\t"
        "s_mov_b32 m0, s80\n\t"
        "s_add_u32 s80, s80, 0x8000\n\t"
        "buffer_load_dwordx4 v96, %[sq], s81 offen lds\n\t"
        "s_add_u32 m0, m0, 0x1000\n\t"
        "s_add_u32 s81, s81, 0x20000\n\t"
        "buffer_load_dwordx4 v96, %[sq], s81 offen lds\n\t"
        "s_add_u32 m0, m0, 0x1000\n\t"
        "s_add_u32 s81, s81, 0x20000\n\t"
        "buffer_load_dwordx4 v96, %[sq], s81 offen lds\n\t"
        "s_add_u32 m0, m0, 0x1000\n\t"
        "s_add_u32 s81, s81, 0x20000\n\t"
        "buffer_load_dwordx4 v96, %[sq], s81 offen lds\n\t"
        "s_add_u32 m0, m0, 0x1000\n\t"
        "s_add_u32 s81, s81, 0x20000\n\t"
        "s_sub_u32 s82, s82, 1\n\t"
        "s_cmp_lg_u32 s82, 0\n\t"
        "s_cbranch_scc1 .Ld0%=\n\t"
        "s_add_u32 s80, %[m0b], 16384\n\t"
        "s_mov_b32 s81, 0\n\t"
        "s_mov_b32 s82, 4\n\t"
        ".Ld1%=:\n\t"
        "s_mov_b32 m0, s80\n\t"
        "s_add_u32 s80, s80, 0x8000\n\t"
        "buffer_load_dwordx4 v96, %[sk], s81 offen lds\n\t"
        "s_add_u32 m0, m0, 0x1000\n\t"
        "s_add_u32 s81, s81, 0x20000\n\t"
        "buffer_load_dwordx4 v96, %[sk], s81 offen lds\n\t"
        "s_add_u32 m0, m0, 0x1000\n\t"
        "s_add_u32 s81, s81, 0x20000\n\t"
        "buffer_load_dwordx4 v96, %[sk], s81 offen lds\n\t"
        "s_add_u32 m0, m0, 0x1000\n\t"
        "s_add_u32 s81, s81, 0x20000\n\t"
        "buffer_load_dwordx4 v96, %[sk], s81 offen lds\n\t"
        "s_add_u32 m0, m0, 0x1000\n\t"
        "s_add_u32 s81, s81, 0x20000\n\t"
        "s_sub_u32 s82, s82, 1\n\t"
        "s_cmp_lg_u32 s82, 0\n\t"
        "s_cbranch_scc1 .Ld1%=\n\t"
        "s_waitcnt vmcnt(16)\n\t"
        "s_barrier\n\t"
        "ds_read_b64_tr_b16 v[0:1], v98 offset:0\n\t"
        "ds_read_b64_tr_b16 v[2:3], v98 offset:512\n\t"
        "ds_read_b64_tr_b16 v[4:5], v98 offset:8192\n\t"
        "ds_read_b64_tr_b16 v[6:7], v98 offset:8704\n\t"
        "ds_read_b64_tr_b16 v[8:9], v99 offset:0\n\t"
        "ds_read_b64_tr_b16 v[10:11], v99 offset:512\n\t"
        "ds_read_b64_tr_b16 v[12:13], v99 offset:8192\n\t"
        "ds_read_b64_tr_b16 v[14:15], v99 offset:8704\n\t"
        "ds_read_b64_tr_b16 a[16:17], v100 offset:0\n\t"
        "ds_read_b64_tr_b16 a[18:19], v100 offset:512\n\t"
        "ds_read_b64_tr_b16 a[20:21], v100 offset:8192\n\t"
        "ds_read_b64_tr_b16 a[22:23], v100 offset:8704\n\t"
        "ds_read_b64_tr_b16 a[24:25], v101 offset:0\n\t"
        "ds_read_b64_tr_b16 a[26:27], v101 offset:512\n\t"
        "ds_read_b64_tr_b16 a[28:29], v101 offset:8192\n\t"
        "ds_read_b64_tr_b16 a[30:31], v101 offset:8704\n\t"
        "ds_read_b64_tr_b16 a[32:33], v102 offset:0\n\t"
        "ds_read_b64_tr_b16 a[34:35], v102 offset:512\n\t"
        "ds_read_b64_tr_b16 a[36:37], v102 offset:8192\n\t"
        "ds_read_b64_tr_b16 a[38:39], v102 offset:8704\n\t"
        "ds_read_b64_tr_b16 a[40:41], v103 offset:0\n\t"
        "ds_read_b64_tr_b16 a[42:43], v103 offset:512\n\t"
        "ds_read_b64_tr_b16 a[44:45], v103 offset:8192\n\t"
        "ds_read_b64_tr_b16 a[46:47], v103 offset:8704\n\t"
        "ds_read_b64_tr_b16 a[48:49], v104 offset:0\n\t"
        "ds_read_b64_tr_b16 a[50:51], v104 offset:512\n\t"
        "ds_read_b64_tr_b16 a[52:53], v104 offset:8192\n\t"
        "ds_read_b64_tr_b16 a[54:55], v104 offset:8704\n\t"
        "ds_read_b64_tr_b16 a[56:57], v105 offset:0\n\t"
        "ds_read_b64_tr_b16 a[58:59], v105 offset:512\n\t"
        "ds_read_b64_tr_b16 a[60:61], v105 offset:8192\n\t"
        "ds_read_b64_tr_b16 a[62:63], v105 offset:8704\n\t"
        "s_waitcnt lgkmcnt(0)\n\t"
        "s_barrier\n\t"
        "s_add_u32 s80, %[m0b], 0\n\t"
        "s_mov_b32 s81, 2097152\n\t"
        "s_mov_b32 s82, 4\n\t"
        ".Ld2%=:\n\t"
        "s_mov_b32 m0, s80\n\t"
        "s_add_u32 s80, s80, 0x8000\n\t"
        "buffer_load_dwordx4 v96, %[sq], s81 offen lds\n\t"
        "s_add_u32 m0, m0, 0x1000\n\t"
        "s_add_u32 s81, s81, 0x20000\n\t"
        "buffer_load_dwordx4 v96, %[sq], s81 offen lds\n\t"
        "s_add_u32 m0, m0, 0x1000\n\t"
        "s_add_u32 s81, s81, 0x20000\n\t"
        "buffer_load_dwordx4 v96, %[sq], s81 offen lds\n\t"
        "s_add_u32 m0, m0, 0x1000\n\t"
        "s_add_u32 s81, s81, 0x20000\n\t"
        "buffer_load_dwordx4 v96, %[sq], s81 offen lds\n\t"
        "s_add_u32 m0, m0, 0x1000\n\t"
        "s_add_u32 s81, s81, 0x20000\n\t"
        "s_sub_u32 s82, s82, 1\n\t"
        "s_cmp_lg_u32 s82, 0\n\t"
        "s_cbranch_scc1 .Ld2%=\n\t"
        "s_waitcnt vmcnt(16)\n\t"
        "s_barrier\n\t"
        "ds_read_b64_tr_b16 v[16:17], v98 offset:16384\n\t"
        "ds_read_b64_tr_b16 v[18:19], v98 offset:16896\n\t"
        "ds_read_b64_tr_b16 v[20:21], v98 offset:24576\n\t"
        "ds_read_b64_tr_b16 v[22:23], v98 offset:25088\n\t"
        "ds_read_b64_tr_b16 v[24:25], v99 offset:16384\n\t"
        "ds_read_b64_tr_b16 v[26:27], v99 offset:16896\n\t"
        "ds_read_b64_tr_b16 v[28:29], v99 offset:24576\n\t"
        "ds_read_b64_tr_b16 v[30:31], v99 offset:25088\n\t"
        "ds_read_b64_tr_b16 a[80:81], v100 offset:16384\n\t"
        "ds_read_b64_tr_b16 a[82:83], v100 offset:16896\n\t"
        "ds_read_b64_tr_b16 a[84:85], v100 offset:24576\n\t"
        "ds_read_b64_tr_b16 a[86:87], v100 offset:25088\n\t"
        "ds_read_b64_tr_b16 a[88:89], v101 offset:16384\n\t"
        "ds_read_b64_tr_b16 a[90:91], v101 offset:16896\n\t"
        "ds_read_b64_tr_b16 a[92:93], v101 offset:24576\n\t"
        "ds_read_b64_tr_b16 a[94:95], v101 offset:25088\n\t"
        "ds_read_b64_tr_b16 a[96:97], v102 offset:16384\n\t"
        "ds_read_b64_tr_b16 a[98:99], v102 offset:16896\n\t"
        "ds_read_b64_tr_b16 a[100:101], v102 offset:24576\n\t"
        "ds_read_b64_tr_b16 a[102:103], v102 offset:25088\n\t"
        "ds_read_b64_tr_b16 a[104:105], v103 offset:16384\n\t"
        "ds_read_b64_tr_b16 a[106:107], v103 offset:16896\n\t"
        "ds_read_b64_tr_b16 a[108:109], v103 offset:24576\n\t"
        "ds_read_b64_tr_b16 a[110:111], v103 offset:25088\n\t"
        "ds_read_b64_tr_b16 a[112:113], v104 offset:16384\n\t"
        "ds_read_b64_tr_b16 a[114:115], v104 offset:16896\n\t"
        "ds_read_b64_tr_b16 a[116:117], v104 offset:24576\n\t"
        "ds_read_b64_tr_b16 a[118:119], v104 offset:25088\n\t"
        "ds_read_b64_tr_b16 a[120:121], v105 offset:16384\n\t"
        "ds_read_b64_tr_b16 a[122:123], v105 offset:16896\n\t"
        "ds_read_b64_tr_b16 a[124:125], v105 offset:24576\n\t"
        "ds_read_b64_tr_b16 a[126:127], v105 offset:25088\n\t"
        "s_waitcnt lgkmcnt(0)\n\t"
        "s_barrier\n\t"
        "s_add_u32 s80, %[m0b], 16384\n\t"
        "s_mov_b32 s81, 2097152\n\t"
        "s_mov_b32 s82, 4\n\t"
        ".Ld3%=:\n\t"
        "s_mov_b32 m0, s80\n\t"
        "s_add_u32 s80, s80, 0x8000\n\t"
        "buffer_load_dwordx4 v96, %[sk], s81 offen lds\n\t"
        "s_add_u32 m0, m0, 0x1000\n\t"
        "s_add_u32 s81, s81, 0x20000\n\t"
        "buffer_load_dwordx4 v96, %[sk], s81 offen lds\n\t"
        "s_add_u32 m0, m0, 0x1000\n\t"
        "s_add_u32 s81, s81, 0x20000\n\t"
        "buffer_load_dwordx4 v96, %[sk], s81 offen lds\n\t"
        "s_add_u32 m0, m0, 0x1000\n\t"
        "s_add_u32 s81, s81, 0x20000\n\t"
        "buffer_load_dwordx4 v96, %[sk], s81 offen lds\n\t"
        "s_add_u32 m0, m0, 0x1000\n\t"
        "s_add_u32 s81, s81, 0x20000\n\t"
        "s_sub_u32 s82, s82, 1\n\t"
        "s_cmp_lg_u32 s82, 0\n\t"
        "s_cbranch_scc1 .Ld3%=\n\t"
        "s_waitcnt vmcnt(16)\n\t"
        "s_barrier\n\t"
        "ds_read_b64_tr_b16 v[32:33], v98 offset:0\n\t"
        "ds_read_b64_tr_b16 v[34:35], v98 offset:512\n\t"
        "ds_read_b64_tr_b16 v[36:37], v98 offset:8192\n\t"
        "ds_read_b64_tr_b16 v[38:39], v98 offset:8704\n\t"
        "ds_read_b64_tr_b16 v[40:41], v99 offset:0\n\t"
        "ds_read_b64_tr_b16 v[42:43], v99 offset:512\n\t"
        "ds_read_b64_tr_b16 v[44:45], v99 offset:8192\n\t"
        "ds_read_b64_tr_b16 v[46:47], v99 offset:8704\n\t"
        "ds_read_b64_tr_b16 a[144:145], v100 offset:0\n\t"
        "ds_read_b64_tr_b16 a[146:147], v100 offset:512\n\t"
        "ds_read_b64_tr_b16 a[148:149], v100 offset:8192\n\t"
        "ds_read_b64_tr_b16 a[150:151], v100 offset:8704\n\t"
        "ds_read_b64_tr_b16 a[152:153], v101 offset:0\n\t"
        "ds_read_b64_tr_b16 a[154:155], v101 offset:512\n\t"
        "ds_read_b64_tr_b16 a[156:157], v101 offset:8192\n\t"
        "ds_read_b64_tr_b16 a[158:159], v101 offset:8704\n\t"
        "ds_read_b64_tr_b16 a[160:161], v102 offset:0\n\t"
        "ds_read_b64_tr_b16 a[162:163], v102 offset:512\n\t"
        "ds_read_b64_tr_b16 a[164:165], v102 offset:8192\n\t"
        "ds_read_b64_tr_b16 a[166:167], v102 offset:8704\n\t"
        "ds_read_b64_tr_b16 a[168:169], v103 offset:0\n\t"
        "ds_read_b64_tr_b16 a[170:171], v103 offset:512\n\t"
        "ds_read_b64_tr_b16 a[172:173], v103 offset:8192\n\t"
        "ds_read_b64_tr_b16 a[174:175], v103 offset:8704\n\t"
        "ds_read_b64_tr_b16 a[176:177], v104 offset:0\n\t"
        "ds_read_b64_tr_b16 a[178:179], v104 offset:512\n\t"
        "ds_read_b64_tr_b16 a[180:181], v104 offset:8192\n\t"
        "ds_read_b64_tr_b16 a[182:183], v104 offset:8704\n\t"
        "ds_read_b64_tr_b16 a[184:185], v105 offset:0\n\t"
        "ds_read_b64_tr_b16 a[186:187], v105 offset:512\n\t"
        "ds_read_b64_tr_b16 a[188:189], v105 offset:8192\n\t"
        "ds_read_b64_tr_b16 a[190:191], v105 offset:8704\n\t"
        "s_waitcnt lgkmcnt(0)\n\t"
        "s_barrier\n\t"
        "s_add_u32 s80, %[m0b], 0\n\t"
        "s_mov_b32 s81, 4194304\n\t"
        "s_mov_b32 s82, 4\n\t"
        ".Ld4%=:\n\t"
        "s_mov_b32 m0, s80\n\t"
        "s_add_u32 s80, s80, 0x8000\n\t"
        "buffer_load_dwordx4 v96, %[sq], s81 offen lds\n\t"
        "s_add_u32 m0, m0, 0x1000\n\t"
        "s_add_u32 s81, s81, 0x20000\n\t"
        "buffer_load_dwordx4 v96, %[sq], s81 offen lds\n\t"
        "s_add_u32 m0, m0, 0x1000\n\t"
        "s_add_u32 s81, s81, 0x20000\n\t"
        "buffer_load_dwordx4 v96, %[sq], s81 offen lds\n\t"
        "s_add_u32 m0, m0, 0x1000\n\t"
        "s_add_u32 s81, s81, 0x20000\n\t"
        "buffer_load_dwordx4 v96, %[sq], s81 offen lds\n\t"
        "s_add_u32 m0, m0, 0x1000\n\t"
        "s_add_u32 s81, s81, 0x20000\n\t"
        "s_sub_u32 s82, s82, 1\n\t"
        "s_cmp_lg_u32 s82, 0\n\t"
        "s_cbranch_scc1 .Ld4%=\n\t"
        "s_mov_b64 exec, 0\n\t"
        "s_branch .Lhk0%=\n\t"
        ".Lhr%=:\n\t"
        "s_mov_b64 exec, -1\n\t"
        "s_waitcnt vmcnt(16)\n\t"
        "s_barrier\n\t"
        "ds_read_b64_tr_b16 v[48:49], v98 offset:16384\n\t"
        "ds_read_b64_tr_b16 v[50:51], v98 offset:16896\n\t"
        "ds_read_b64_tr_b16 v[52:53], v98 offset:24576\n\t"
        "ds_read_b64_tr_b16 v[54:55], v98 offset:25088\n\t"
        "ds_read_b64_tr_b16 v[56:57], v99 offset:16384\n\t"
        "ds_read_b64_tr_b16 v[58:59], v99 offset:16896\n\t"
        "ds_read_b64_tr_b16 v[60:61], v99 offset:24576\n\t"
        "ds_read_b64_tr_b16 v[62:63], v99 offset:25088\n\t"
        "ds_read_b64_tr_b16 a[208:209], v100 offset:16384\n\t"
        "ds_read_b64_tr_b16 a[210:211], v100 offset:16896\n\t"
        "ds_read_b64_tr_b16 a[212:213], v100 offset:24576\n\t"
        "ds_read_b64_tr_b16 a[214:215], v100 offset:25088\n\t"
        "ds_read_b64_tr_b16 a[216:217], v101 offset:16384\n\t"
        "ds_read_b64_tr_b16 a[218:219], v101 offset:16896\n\t"
        "ds_read_b64_tr_b16 a[220:221], v101 offset:24576\n\t"
        "ds_read_b64_tr_b16 a[222:223], v101 offset:25088\n\t"
        "ds_read_b64_tr_b16 a[224:225], v102 offset:16384\n\t"
        "ds_read_b64_tr_b16 a[226:227], v102 offset:16896\n\t"
        "ds_read_b64_tr_b16 a[228:229], v102 offset:24576\n\t"
        "ds_read_b64_tr_b16 a[230:231], v102 offset:25088\n\t"
        "ds_read_b64_tr_b16 a[232:233], v103 offset:16384\n\t"
        "ds_read_b64_tr_b16 a[234:235], v103 offset:16896\n\t"
        "ds_read_b64_tr_b16 a[236:237], v103 offset:24576\n\t"
        "ds_read_b64_tr_b16 a[238:239], v103 offset:25088\n\t"
        "ds_read_b64_tr_b16 a[240:241], v104 offset:16384\n\t"
        "ds_read_b64_tr_b16 a[242:243], v104 offset:16896\n\t"
        "ds_read_b64_tr_b16 a[244:245], v104 offset:24576\n\t"
        "ds_read_b64_tr_b16 a[246:247], v104 offset:25088\n\t"
        "ds_read_b64_tr_b16 a[248:249], v105 offset:16384\n\t"
        "ds_read_b64_tr_b16 a[250:251], v105 offset:16896\n\t"
        "ds_read_b64_tr_b16 a[252:253], v105 offset:24576\n\t"
        "ds_read_b64_tr_b16 a[254:255], v105 offset:25088\n\t"
        "s_waitcnt lgkmcnt(0)\n\t"
        "s_barrier\n\t"
        "s_add_u32 s80, %[m0b], 16384\n\t"
        "s_mov_b32 s81, 4194304\n\t"
        "s_mov_b32 s82, 4\n\t"
        ".Ld5%=:\n\t"
        "s_mov_b32 m0, s80\n\t"
        "s_add_u32 s80, s80, 0x8000\n\t"
        "buffer_load_dwordx4 v96, %[sk], s81 offen lds\n\t"
        "s_add_u32 m0, m0, 0x1000\n\t"
        "s_add_u32 s81, s81, 0x20000\n\t"
        "buffer_load_dwordx4 v96, %[sk], s81 offen lds\n\t"
        "s_add_u32 m0, m0, 0x1000\n\t"
        "s_add_u32 s81, s81, 0x20000\n\t"
        "buffer_load_dwordx4 v96, %[sk], s81 offen lds\n\t"
        "s_add_u32 m0, m0, 0x1000\n\t"
        "s_add_u32 s81, s81, 0x20000\n\t"
        "buffer_load_dwordx4 v96, %[sk], s81 offen lds\n\t"
        "s_add_u32 m0, m0, 0x1000\n\t"
        "s_add_u32 s81, s81, 0x20000\n\t"
        "s_sub_u32 s82, s82, 1\n\t"
        "s_cmp_lg_u32 s82, 0\n\t"
        "s_cbranch_scc1 .Ld5%=\n\t"
        "s_waitcnt vmcnt(16)\n\t"
        "s_barrier\n\t"
        "ds_read_b64_tr_b16 v[128:129], v98 offset:0\n\t"
        "ds_read_b64_tr_b16 v[130:131], v98 offset:512\n\t"
        "ds_read_b64_tr_b16 v[132:133], v98 offset:8192\n\t"
        "ds_read_b64_tr_b16 v[134:135], v98 offset:8704\n\t"
        "ds_read_b64_tr_b16 v[136:137], v99 offset:0\n\t"
        "ds_read_b64_tr_b16 v[138:139], v99 offset:512\n\t"
        "ds_read_b64_tr_b16 v[140:141], v99 offset:8192\n\t"
        "ds_read_b64_tr_b16 v[142:143], v99 offset:8704\n\t"
        "ds_read_b64_tr_b16 v[144:145], v100 offset:0\n\t"
        "ds_read_b64_tr_b16 v[146:147], v100 offset:512\n\t"
        "ds_read_b64_tr_b16 v[148:149], v100 offset:8192\n\t"
        "ds_read_b64_tr_b16 v[150:151], v100 offset:8704\n\t"
        "ds_read_b64_tr_b16 v[152:153], v101 offset:0\n\t"
        "ds_read_b64_tr_b16 v[154:155], v101 offset:512\n\t"
        "ds_read_b64_tr_b16 v[156:157], v101 offset:8192\n\t"
        "ds_read_b64_tr_b16 v[158:159], v101 offset:8704\n\t"
        "ds_read_b64_tr_b16 v[160:161], v102 offset:0\n\t"
        "ds_read_b64_tr_b16 v[162:163], v102 offset:512\n\t"
        "ds_read_b64_tr_b16 v[164:165], v102 offset:8192\n\t"
        "ds_read_b64_tr_b16 v[166:167], v102 offset:8704\n\t"
        "ds_read_b64_tr_b16 v[168:169], v103 offset:0\n\t"
        "ds_read_b64_tr_b16 v[170:171], v103 offset:512\n\t"
        "ds_read_b64_tr_b16 v[172:173], v103 offset:8192\n\t"
        "ds_read_b64_tr_b16 v[174:175], v103 offset:8704\n\t"
        "ds_read_b64_tr_b16 v[176:177], v104 offset:0\n\t"
        "ds_read_b64_tr_b16 v[178:179], v104 offset:512\n\t"
        "ds_read_b64_tr_b16 v[180:181], v104 offset:8192\n\t"
        "ds_read_b64_tr_b16 v[182:183], v104 offset:8704\n\t"
        "ds_read_b64_tr_b16 v[184:185], v105 offset:0\n\t"
        "ds_read_b64_tr_b16 v[186:187], v105 offset:512\n\t"
        "ds_read_b64_tr_b16 v[188:189], v105 offset:8192\n\t"
        "ds_read_b64_tr_b16 v[190:191], v105 offset:8704\n\t"
        "s_waitcnt lgkmcnt(0)\n\t"
        "s_barrier\n\t"
        "s_add_u32 s80, %[m0b], 0\n\t"
        "s_mov_b32 s81, 6291456\n\t"
        "s_mov_b32 s82, 4\n\t"
        ".Ld6%=:\n\t"
        "s_mov_b32 m0, s80\n\t"
        "s_add_u32 s80, s80, 0x8000\n\t"
        "buffer_load_dwordx4 v96, %[sq], s81 offen lds\n\t"
        "s_add_u32 m0, m0, 0x1000\n\t"
        "s_add_u32 s81, s81, 0x20000\n\t"
        "buffer_load_dwordx4 v96, %[sq], s81 offen lds\n\t"
        "s_add_u32 m0, m0, 0x1000\n\t"
        "s_add_u32 s81, s81, 0x20000\n\t"
        "buffer_load_dwordx4 v96, %[sq], s81 offen lds\n\t"
        "s_add_u32 m0, m0, 0x1000\n\t"
        "s_add_u32 s81, s81, 0x20000\n\t"
        "buffer_load_dwordx4 v96, %[sq], s81 offen lds\n\t"
        "s_add_u32 m0, m0, 0x1000\n\t"
        "s_add_u32 s81, s81, 0x20000\n\t"
        "s_sub_u32 s82, s82, 1\n\t"
        "s_cmp_lg_u32 s82, 0\n\t"
        "s_cbranch_scc1 .Ld6%=\n\t"
        "s_waitcnt vmcnt(16)\n\t"
        "s_barrier\n\t"
        "ds_read_b64_tr_b16 v[192:193], v98 offset:16384\n\t"
        "ds_read_b64_tr_b16 v[194:195], v98 offset:16896\n\t"
        "ds_read_b64_tr_b16 v[196:197], v98 offset:24576\n\t"
        "ds_read_b64_tr_b16 v[198:199], v98 offset:25088\n\t"
        "ds_read_b64_tr_b16 v[200:201], v99 offset:16384\n\t"
        "ds_read_b64_tr_b16 v[202:203], v99 offset:16896\n\t"
        "ds_read_b64_tr_b16 v[204:205], v99 offset:24576\n\t"
        "ds_read_b64_tr_b16 v[206:207], v99 offset:25088\n\t"
        "ds_read_b64_tr_b16 v[208:209], v100 offset:16384\n\t"
        "ds_read_b64_tr_b16 v[210:211], v100 offset:16896\n\t"
        "ds_read_b64_tr_b16 v[212:213], v100 offset:24576\n\t"
        "ds_read_b64_tr_b16 v[214:215], v100 offset:25088\n\t"
        "ds_read_b64_tr_b16 v[216:217], v101 offset:16384\n\t"
        "ds_read_b64_tr_b16 v[218:219], v101 offset:16896\n\t"
        "ds_read_b64_tr_b16 v[220:221], v101 offset:24576\n\t"
        "ds_read_b64_tr_b16 v[222:223], v101 offset:25088\n\t"
        "ds_read_b64_tr_b16 v[224:225], v102 offset:16384\n\t"
        "ds_read_b64_tr_b16 v[226:227], v102 offset:16896\n\t"
        "ds_read_b64_tr_b16 v[228:229], v102 offset:24576\n\t"
        "ds_read_b64_tr_b16 v[230:231], v102 offset:25088\n\t"
        "ds_read_b64_tr_b16 v[232:233], v103 offset:16384\n\t"
        "ds_read_b64_tr_b16 v[234:235], v103 offset:16896\n\t"
        "ds_read_b64_tr_b16 v[236:237], v103 offset:24576\n\t"
        "ds_read_b64_tr_b16 v[238:239], v103 offset:25088\n\t"
        "ds_read_b64_tr_b16 v[240:241], v104 offset:16384\n\t"
        "ds_read_b64_tr_b16 v[242:243], v104 offset:16896\n\t"
        "ds_read_b64_tr_b16 v[244:245], v104 offset:24576\n\t"
        "ds_read_b64_tr_b16 v[246:247], v104 offset:25088\n\t"
        "ds_read_b64_tr_b16 v[248:249], v105 offset:16384\n\t"
        "ds_read_b64_tr_b16 v[250:251], v105 offset:16896\n\t"
        "ds_read_b64_tr_b16 v[252:253], v105 offset:24576\n\t"
        "ds_read_b64_tr_b16 v[254:255], v105 offset:25088\n\t"
        "s_waitcnt lgkmcnt(0)\n\t"
        "s_barrier\n\t"
        "s_add_u32 s80, %[m0b], 16384\n\t"
        "s_mov_b32 s81, 6291456\n\t"
        "s_mov_b32 s82, 4\n\t"
        ".Ld7%=:\n\t"
        "s_mov_b32 m0, s80\n\t"
        "s_add_u32 s80, s80, 0x8000\n\t"
        "buffer_load_dwordx4 v96, %[sk], s81 offen lds\n\t"
        "s_add_u32 m0, m0, 0x1000\n\t"
        "s_add_u32 s81, s81, 0x20000\n\t"
        "buffer_load_dwordx4 v96, %[sk], s81 offen lds\n\t"
        "s_add_u32 m0, m0, 0x1000\n\t"
        "s_add_u32 s81, s81, 0x20000\n\t"
        "buffer_load_dwordx4 v96, %[sk], s81 offen lds\n\t"
        "s_add_u32 m0, m0, 0x1000\n\t"
        "s_add_u32 s81, s81, 0x20000\n\t"
        "buffer_load_dwordx4 v96, %[sk], s81 offen lds\n\t"
        "s_add_u32 m0, m0, 0x1000\n\t"
        "s_add_u32 s81, s81, 0x20000\n\t"
        "s_sub_u32 s82, s82, 1\n\t"
        "s_cmp_lg_u32 s82, 0\n\t"
        "s_cbranch_scc1 .Ld7%=\n\t"
        "s_waitcnt vmcnt(16)\n\t"
        "s_barrier\n\t"
        "ds_read_b64_tr_b16 a[0:1], v98 offset:0\n\t"
        "ds_read_b64_tr_b16 a[2:3], v98 offset:512\n\t"
        "ds_read_b64_tr_b16 a[4:5], v98 offset:8192\n\t"
        "ds_read_b64_tr_b16 a[6:7], v98 offset:8704\n\t"
        "ds_read_b64_tr_b16 a[8:9], v99 offset:0\n\t"
        "ds_read_b64_tr_b16 a[10:11], v99 offset:512\n\t"
        "ds_read_b64_tr_b16 a[12:13], v99 offset:8192\n\t"
        "ds_read_b64_tr_b16 a[14:15], v99 offset:8704\n\t"
        "ds_read_b64_tr_b16 v[64:65], v100 offset:0\n\t"
        "ds_read_b64_tr_b16 v[66:67], v100 offset:512\n\t"
        "ds_read_b64_tr_b16 v[68:69], v100 offset:8192\n\t"
        "ds_read_b64_tr_b16 v[70:71], v100 offset:8704\n\t"
        "ds_read_b64_tr_b16 v[72:73], v101 offset:0\n\t"
        "ds_read_b64_tr_b16 v[74:75], v101 offset:512\n\t"
        "ds_read_b64_tr_b16 v[76:77], v101 offset:8192\n\t"
        "ds_read_b64_tr_b16 v[78:79], v101 offset:8704\n\t"
        "ds_read_b64_tr_b16 v[80:81], v102 offset:0\n\t"
        "ds_read_b64_tr_b16 v[82:83], v102 offset:512\n\t"
        "ds_read_b64_tr_b16 v[84:85], v102 offset:8192\n\t"
        "ds_read_b64_tr_b16 v[86:87], v102 offset:8704\n\t"
        "ds_read_b64_tr_b16 v[88:89], v103 offset:0\n\t"
        "ds_read_b64_tr_b16 v[90:91], v103 offset:512\n\t"
        "ds_read_b64_tr_b16 v[92:93], v103 offset:8192\n\t"
        "ds_read_b64_tr_b16 v[94:95], v103 offset:8704\n\t"
        "ds_read_b64_tr_b16 a[128:129], v104 offset:0\n\t"
        "ds_read_b64_tr_b16 a[130:131], v104 offset:512\n\t"
        "ds_read_b64_tr_b16 a[132:133], v104 offset:8192\n\t"
        "ds_read_b64_tr_b16 a[134:135], v104 offset:8704\n\t"
        "ds_read_b64_tr_b16 a[136:137], v105 offset:0\n\t"
        "ds_read_b64_tr_b16 a[138:139], v105 offset:512\n\t"
        "ds_read_b64_tr_b16 a[140:141], v105 offset:8192\n\t"
        "ds_read_b64_tr_b16 a[142:143], v105 offset:8704\n\t"
        "s_waitcnt lgkmcnt(0)\n\t"
        "s_barrier\n\t"
        "ds_write_b128 v121, v[0:3] offset:0\n\t"
        "ds_write_b128 v121, v[4:7] offset:1024\n\t"
        "ds_write_b128 v121, v[8:11] offset:2048\n\t"
        "ds_write_b128 v121, v[12:15] offset:3072\n\t"
        "ds_write_b128 v121, v[16:19] offset:4096\n\t"
        "ds_write_b128 v121, v[20:23] offset:5120\n\t"
        "ds_write_b128 v121, v[24:27] offset:6144\n\t"
        "ds_write_b128 v121, v[28:31] offset:7168\n\t"
        "ds_write_b128 v121, v[32:35] offset:8192\n\t"
        "ds_write_b128 v121, v[36:39] offset:9216\n\t"
        "ds_write_b128 v121, v[40:43] offset:10240\n\t"
        "ds_write_b128 v121, v[44:47] offset:11264\n\t"
        "ds_write_b128 v121, v[48:51] offset:12288\n\t"
        "ds_write_b128 v121, v[52:55] offset:13312\n\t"
        "ds_write_b128 v121, v[56:59] offset:14336\n\t"
        "ds_write_b128 v121, v[60:63] offset:15360\n\t"
        "s_waitcnt lgkmcnt(0)\n\t"
        "s_mov_b32 s64, %[kf]\n\t"
        "s_mov_b32 s36, 0xf0000000\n\t"
        "buffer_load_dwordx4 v[116:119], v120, %[wsd], %[cal] offen sc1\n\t"
        "buffer_load_dwordx2 v[114:115], off, %[wsd], s64 sc1\n\t"
        "s_waitcnt vmcnt(2)\n\t"
        "s_barrier\n\t"
        "ds_read_b64_tr_b16 a[64:65], v98 offset:16384\n\t"
        "ds_read_b64_tr_b16 a[66:67], v98 offset:16896\n\t"
        "ds_read_b64_tr_b16 a[68:69], v98 offset:24576\n\t"
        "ds_read_b64_tr_b16 a[70:71], v98 offset:25088\n\t"
        "ds_read_b64_tr_b16 a[72:73], v99 offset:16384\n\t"
        "ds_read_b64_tr_b16 a[74:75], v99 offset:16896\n\t"
        "ds_read_b64_tr_b16 a[76:77], v99 offset:24576\n\t"
        "ds_read_b64_tr_b16 a[78:79], v99 offset:25088\n\t"
        "ds_read_b64_tr_b16 v[0:1], v100 offset:16384\n\t"
        "ds_read_b64_tr_b16 v[2:3], v100 offset:16896\n\t"
        "ds_read_b64_tr_b16 v[4:5], v100 offset:24576\n\t"
        "ds_read_b64_tr_b16 v[6:7], v100 offset:25088\n\t"
        "ds_read_b64_tr_b16 v[8:9], v101 offset:16384\n\t"
        "ds_read_b64_tr_b16 v[10:11], v101 offset:16896\n\t"
        "ds_read_b64_tr_b16 v[12:13], v101 offset:24576\n\t"
        "ds_read_b64_tr_b16 v[14:15], v101 offset:25088\n\t"
        "ds_read_b64_tr_b16 v[16:17], v102 offset:16384\n\t"
        "ds_read_b64_tr_b16 v[18:19], v102 offset:16896\n\t"
        "ds_read_b64_tr_b16 v[20:21], v102 offset:24576\n\t"
        "ds_read_b64_tr_b16 v[22:23], v102 offset:25088\n\t"
        "ds_read_b64_tr_b16 v[24:25], v103 offset:16384\n\t"
        "ds_read_b64_tr_b16 v[26:27], v103 offset:16896\n\t"
        "ds_read_b64_tr_b16 v[28:29], v103 offset:24576\n\t"
        "ds_read_b64_tr_b16 v[30:31], v103 offset:25088\n\t"
        "ds_read_b64_tr_b16 a[192:193], v104 offset:16384\n\t"
        "ds_read_b64_tr_b16 a[194:195], v104 offset:16896\n\t"
        "ds_read_b64_tr_b16 a[196:197], v104 offset:24576\n\t"
        "ds_read_b64_tr_b16 a[198:199], v104 offset:25088\n\t"
        "ds_read_b64_tr_b16 a[200:201], v105 offset:16384\n\t"
        "ds_read_b64_tr_b16 a[202:203], v105 offset:16896\n\t"
        "ds_read_b64_tr_b16 a[204:205], v105 offset:24576\n\t"
        "ds_read_b64_tr_b16 a[206:207], v105 offset:25088\n\t"
        "s_waitcnt vmcnt(0)\n\t"
        "s_mov_b32 s97, 0\n\t"
        ".Lg0%=:\n\t"
        "v_cmp_ne_u32_e32 vcc, %[tag], v116\n\t"
        "v_cmp_ne_u32_e64 s[82:83], %[tag], v118\n\t"
        "s_nop 1\n\t"
        "s_or_b64 vcc, vcc, s[82:83]\n\t"
        "s_cbranch_scc0 .Lg1%=\n\t"
        "s_sleep 4\n\t"
        "buffer_load_dwordx4 v[116:119], v120, %[wsd], %[cal] offen sc1\n\t"
        "s_add_u32 s97, s97, 1\n\t"
        "s_cmp_lt_u32 s97, 0x100000\n\t"
        "s_cbranch_scc0 .Lgu%=\n\t"
        "s_waitcnt vmcnt(0)\n\t"
        "s_branch .Lg0%=\n\t"
        ".Lg1%=:\n\t"
        "s_mov_b32 s84, 0x55555555\n\t"
        "s_mov_b32 s85, s84\n\t"
        "v_and_b32_e32 v116, 1, v117\n\t"
        "v_and_b32_e32 v118, 1, v119\n\t"
        "v_cmp_ne_u32_e32 vcc, 0, v116\n\t"
        "v_cmp_ne_u32_e64 s[82:83], 0, v118\n\t"
        "s_nop 0\n\t"
        "s_and_b64 s[82:83], s[82:83], s[84:85]\n\t"
        "s_bcnt1_i32_b64 s87, s[82:83]\n\t"
        "s_and_b64 s[82:83], vcc, s[84:85]\n\t"
        "s_bcnt1_i32_b64 s86, s[82:83]\n\t"
        "s_andn2_b64 s[82:83], vcc, s[84:85]\n\t"
        "s_bcnt1_i32_b64 s88, s[82:83]\n\t"
        "v_and_b32_e32 v116, 2, v117\n\t"
        "v_and_b32_e32 v118, 2, v119\n\t"
        "v_cmp_ne_u32_e32 vcc, 0, v116\n\t"
        "v_cmp_ne_u32_e64 s[82:83], 0, v118\n\t"
        "s_nop 0\n\t"
        "s_and_b64 s[82:83], s[82:83], s[84:85]\n\t"
        "s_bcnt1_i32_b64 s90, s[82:83]\n\t"
        "s_and_b64 s[82:83], vcc, s[84:85]\n\t"
        "s_bcnt1_i32_b64 s89, s[82:83]\n\t"
        "s_andn2_b64 s[82:83], vcc, s[84:85]\n\t"
        "s_bcnt1_i32_b64 s91, s[82:83]\n\t"
        "s_add_u32 s80, s86, s87\n\t"
        "s_add_u32 s80, s80, s88\n\t"
        "s_cmp_ge_u32 s80, 48\n\t"
        "s_cselect_b32 s80, 8, 24\n\t"
        "s_cmp_gt_u32 s86, s80\n\t"
        "s_cselect_b32 s86, %c[NX1], %c[NX0]\n\t"
        "s_cmp_gt_u32 s89, 16\n\t"
        "s_cselect_b32 s81, %c[NY1], %c[NY0]\n\t"
        "s_or_b32 s86, s86, s81\n\t"
        "s_cmp_gt_u32 s87, s80\n\t"
        "s_cselect_b32 s87, %c[NX1], %c[NX0]\n\t"
        "s_cmp_gt_u32 s90, 16\n\t"
        "s_cselect_b32 s81, %c[NY1], %c[NY0]\n\t"
        "s_or_b32 s87, s87, s81\n\t"
        "s_cmp_gt_u32 s88, s80\n\t"
        "s_cselect_b32 s88, %c[NX1], %c[NX0]\n\t"
        "s_cmp_gt_u32 s91, 16\n\t"
        "s_cselect_b32 s81, %c[NY1], %c[NY0]\n\t"
        "s_or_b32 s88, s88, s81\n\t"
        "s_xor_b32 %[nA], %[nA], s86\n\t"
        "s_xor_b32 %[nW], %[nW], s86\n\t"
        "s_xor_b32 %[nC], %[nC], s87\n\t"
        "s_xor_b32 %[nS], %[nS], s88\n\t"
        "s_xor_b32 %[nT], %[nT], s88\n\t"
        "s_xor_b32 %[b0], %[b0], s88\n\t"
        "s_cmp_eq_u32 %[w], 1\n\t"
        "s_cselect_b32 s80, s87, s86\n\t"
        "s_cmp_eq_u32 %[w], 2\n\t"
        "s_cselect_b32 s80, s88, s80\n\t"
        "s_xor_b32 %[pob], %[pob], s80\n\t"
        "s_lshl_b32 s80, %[w], 10\n\t"
        "s_sub_u32 %[nS], %[nS], s80\n\t"
        "s_sub_u32 %[pob], %[pob], s80\n\t"
        "ds_read_b32 v116, v120 offset:20484\n\t"
        "s_and_b32 s80, s87, 0x4000\n\t"
        "s_xor_b32 %[nC], %[nC], s80\n\t"
        "s_lshl_b32 s81, %[w], 8\n\t"
        "s_add_u32 s81, s81, 24576\n\t"
        "s_sub_u32 %[b0], %[b0], s81\n\t"
        "s_waitcnt lgkmcnt(0)\n\t"
        "v_xor_b32_e32 v116, s80, v116\n\t"
        "ds_write_b32 v120, v116 offset:20484\n\t"
        "s_mov_b32 s36, 0xf0000000\n\t"
        "s_mov_b32 s38, 0xff00ff00\n\t"
        "s_mov_b32 s39, 0xff00ff00\n\t"
        "s_mov_b32 s40, 0\n\t"
        "s_mov_b32 s37, 0\n\t"
        "s_brev_b32 s54, 1\n\t"
        "s_mov_b32 s46, %c[iD1]\n\t"
        "s_mov_b32 s45, %c[iD1]\n\t"
        "s_mov_b32 s47, %c[iD2]\n\t"
        "s_mov_b32 s48, 1\n\t"
        "s_mov_b32 s50, %c[LC]\n\t"
        "s_mov_b32 s51, %c[LA]\n\t"
        "s_mov_b32 s52, %c[E1]\n\t"
        "s_mov_b32 s53, %c[E2]\n\t"
        "s_mov_b32 s43, 0\n\t"
        "s_mov_b32 s61, %[b0]\n\t"
        "s_add_u32 s62, %[b0], 0x40000\n\t"
        "s_mov_b32 s75, 31744\n\t"
        "s_cmp_eq_u32 s64, s36\n\t"
        "s_cbranch_scc1 .Lk10%=\n\t"
        "s_mov_b32 s97, 0\n\t"
        ".Lk00%=:\n\t"
        "v_readfirstlane_b32 s82, v114\n\t"
        "v_readfirstlane_b32 s83, v115\n\t"
        "s_xor_b32 s82, s82, %[tag]\n\t"
        "s_xor_b32 s83, s83, %[tag]\n\t"
        "s_or_b32 s82, s82, s83\n\t"
        "s_cbranch_scc0 .Lk10%=\n\t"
        "s_sleep 3\n\t"
        "buffer_load_dwordx2 v[114:115], off, %[wsd], s64 sc1\n\t"
        "s_add_u32 s97, s97, 1\n\t"
        "s_cmp_lt_u32 s97, 0x200000\n\t"
        "s_cbranch_scc0 .Lgu%=\n\t"
        "s_waitcnt vmcnt(0)\n\t"
        "s_branch .Lk00%=\n\t"
        ".Lk10%=:\n\t"
        "s_add_u32 s80, s40, 0\n\t"
        "s_min_u32 s80, s80, 63\n\t"
        "s_lshl_b32 s80, s80, 18\n\t"
        "s_add_u32 s69, s80, %[kx]\n\t"
        "s_add_u32 s70, s69, 0x1000\n\t"
        "s_lshl_b32 s80, s40, 18\n\t"
        "s_add_u32 s71, s80, %[vx]\n\t"
        "s_add_u32 s80, s40, 1\n\t"
        "s_lshl_b32 s81, s80, 9\n\t"
        "s_add_u32 s81, s81, %[kf]\n\t"
        "s_and_b32 s80, s80, 0x43\n\t"
        "s_cselect_b32 s64, s36, s81\n\t"
        "buffer_load_dwordx4 v[104:107], v120, %[wsd], s69 offen sc1\n\t"
        "buffer_load_dwordx4 v[108:111], v120, %[wsd], s70 offen sc1\n\t"
        "s_waitcnt lgkmcnt(0)\n\t"
        "s_barrier\n\t"
        "ds_write_b128 v121, v[64:67] offset:16384\n\t"
        "ds_write_b128 v121, v[68:71] offset:17408\n\t"
        "ds_write_b128 v121, v[72:75] offset:18432\n\t"
        "ds_write_b128 v121, v[76:79] offset:19456\n\t"
        "ds_write_b128 v121, v[80:83] offset:20480\n\t"
        "ds_write_b128 v121, v[84:87] offset:21504\n\t"
        "ds_write_b128 v121, v[88:91] offset:22528\n\t"
        "ds_write_b128 v121, v[92:95] offset:23552\n\t"
        "ds_write_b128 v121, v[0:3] offset:24576\n\t"
        "ds_write_b128 v121, v[4:7] offset:25600\n\t"
        "ds_write_b128 v121, v[8:11] offset:26624\n\t"
        "ds_write_b128 v121, v[12:15] offset:27648\n\t"
        "ds_write_b128 v121, v[16:19] offset:28672\n\t"
        "ds_write_b128 v121, v[20:23] offset:29696\n\t"
        "ds_write_b128 v121, v[24:27] offset:30720\n\t"
        "ds_write_b128 v121, v[28:31] offset:31744\n\t"
        "s_waitcnt lgkmcnt(0)\n\t"
        "rec_s_code:\n\t"
        "ds_read_b128 v[16:19], v121 offset:0\n\t"
        "ds_read_b128 v[20:23], v121 offset:1024\n\t"
        "ds_read_b128 v[24:27], v121 offset:2048\n\t"
        "ds_read_b128 v[28:31], v121 offset:3072\n\t"
        "ds_read_b128 v[32:35], v121 offset:4096\n\t"
        "ds_read_b128 v[36:39], v121 offset:5120\n\t"
        "ds_read_b128 v[40:43], v121 offset:6144\n\t"
        "ds_read_b128 v[44:47], v121 offset:7168\n\t"
        "ds_read_b128 v[56:59], v121 offset:8192\n\t"
        "ds_read_b128 v[60:63], v121 offset:9216\n\t"
        "ds_read_b128 v[64:67], v121 offset:10240\n\t"
        "ds_read_b128 v[68:71], v121 offset:11264\n\t"
        "ds_read_b128 v[72:75], v121 offset:12288\n\t"
        "ds_read_b128 v[76:79], v121 offset:13312\n\t"
        "ds_read_b128 v[80:83], v121 offset:14336\n\t"
        "ds_read_b128 v[84:87], v121 offset:15360\n\t"
        "s_and_b32 s80, s40, 3\n\t"
        "s_lshl_b32 s80, s80, 17\n\t"
        "s_add_u32 s57, s80, %[nA]\n\t"
        "s_add_u32 s58, s57, 0x1000\n\t"
        "s_add_u32 s59, s57, 0x8000\n\t"
        "s_add_u32 s60, s57, 0x9000\n\t"
        "s_and_b32 s80, s40, 3\n\t"
        "s_lshl_b32 s80, s80, 17\n\t"
        "s_add_u32 s63, s80, %[nC]\n\t"
        "s_add_u32 s66, s80, %[nS]\n\t"
        "s_add_u32 s81, s80, 0x20000\n\t"
        "s_and_b32 s81, s81, 0x60000\n\t"
        "s_add_u32 s67, s81, %[nW]\n\t"
        "s_add_u32 s79, s81, %[nT]\n\t"
        "s_cmp_eq_u32 %[w], 2\n\t"
        "s_cselect_b32 s81, s80, s81\n\t"
        "s_xor_b32 s81, s81, 0x40000\n\t"
        "s_add_u32 s68, s81, %[pob]\n\t"
        "s_lshl_b32 s72, s40, 17\n\t"
        "s_add_u32 s72, s72, %[kvo]\n\t"
        "s_mov_b32 s88, 0\n\t"
        "s_mov_b32 s89, 0\n\t"
        "s_mov_b32 s90, 0\n\t"
        "v_mov_b32_e32 v88, 0\n\t"
        "v_mov_b32_e32 v89, 0\n\t"
        "v_mov_b32_e32 v90, 0\n\t"
        "v_mov_b32_e32 v91, 0\n\t"
        "v_mov_b32_e32 v92, 0\n\t"
        "v_mov_b32_e32 v93, 0\n\t"
        "v_mov_b32_e32 v94, 0\n\t"
        "v_mov_b32_e32 v95, 0\n\t"
        "v_mov_b32_e32 v96, 0\n\t"
        "v_mov_b32_e32 v97, 0\n\t"
        "v_mov_b32_e32 v98, 0\n\t"
        "v_mov_b32_e32 v99, 0\n\t"
        "v_mov_b32_e32 v100, 0\n\t"
        "v_mov_b32_e32 v101, 0\n\t"
        "v_mov_b32_e32 v102, 0\n\t"
        "v_mov_b32_e32 v103, 0\n\t"
        "v_mov_b32_e32 v52, v112\n\t"
        "v_mov_b32_e32 v53, v113\n\t"
        "v_mov_b32_e32 v49, 0\n\t"
        "v_lshlrev_b32_e32 v51, 6, v120\n\t"
        "s_cmp_eq_u32 %[w], 0\n\t"
        "s_cselect_b32 s73, %[nT], s36\n\t"
        "s_cselect_b32 s74, %[pra], s36\n\t"
        "v_mov_b32_e32 v50, s40\n\t"
        "s_cmp_ge_u32 s40, 60\n\t"
        "s_cselect_b32 s65, s74, s36\n\t"
        "s_waitcnt lgkmcnt(0)\n\t"
        "s_barrier\n\t"
        "s_memrealtime s[98:99]\n\t"
        "s_waitcnt lgkmcnt(0)\n\t"
        "s_mov_b32 s41, s98\n\t"
        "v_mov_b32_e32 v48, s98\n\t"
        ".p2align 6\n\t"
        ".Lloop%=:\n\t"
        "s_mov_b32 s97, 0\n\t"
        "s_movk_i32 s96, 0x1000\n\t"
        ".Lwa%=:\n\t"
        "s_memrealtime s[98:99]\n\t"
        "s_addk_i32 s96, -1\n\t"
        "s_cmp_lt_i32 s96, 0\n\t"
        "s_cbranch_scc1 .Lta%=\n\t"
        "s_waitcnt lgkmcnt(0)\n\t"
        "s_sub_i32 s98, s98, s41\n\t"
        "s_cmp_lt_i32 s98, %c[iFT]\n\t"
        "s_cbranch_scc1 .Lwa%=\n\t"
        "s_cmp_ge_i32 s98, 0\n\t"
        "s_cbranch_scc1 .Lxa%=\n\t"
        ".Lqa%=:\n\t"
        "s_nop %c[iFN]\n\t"
        "s_addk_i32 s98, 1\n\t"
        "s_cmp_lt_i32 s98, 0\n\t"
        "s_cbranch_scc1 .Lqa%=\n\t"
        ".Lxa%=:\n\t"
        "buffer_load_dwordx4 v[0:3], v120, %[srd], s57 offen sc1\n\t"
        "s_mov_b32 s80, s88\n\t"
        "s_add_i32 s81, s92, s50\n\t"
        "s_sub_i32 s81, s81, 4\n\t"
        "s_cmp_le_i32 s81, s44\n\t"
        "s_cselect_b32 s80, s80, 0\n\t"
        "s_cmp_lt_i32 s50, %c[LC24]\n\t"
        "s_cselect_b32 s80, s80, 0\n\t"
        "s_lshl2_add_u32 s50, s80, s50\n\t"
        "buffer_load_dwordx4 v[4:7], v120, %[srd], s58 offen sc1\n\t"
        "s_sub_i32 s50, s50, s89\n\t"
        "s_max_i32 s50, s50, %c[LC]\n\t"
        "s_lshl_b32 s80, s92, 1\n\t"
        "s_sub_i32 s81, s80, s52\n\t"
        "s_ashr_i32 s82, s81, %c[UPSH]\n\t"
        "s_max_i32 s82, s82, %c[TRUP]\n\t"
        "s_add_i32 s82, s82, s52\n\t"
        "s_min_i32 s82, s82, s80\n\t"
        "buffer_load_dwordx4 v[8:11], v120, %[srd], s59 offen sc1\n\t"
        "s_ashr_i32 s83, s81, %c[DNSH]\n\t"
        "s_min_i32 s83, s83, -%c[TRDN]\n\t"
        "s_add_i32 s83, s83, s52\n\t"
        "s_max_i32 s83, s83, s80\n\t"
        "s_cmp_gt_i32 s81, 0\n\t"
        "s_cselect_b32 s82, s82, s83\n\t"
        "s_cmp_lg_u32 s90, 0\n\t"
        "s_cselect_b32 s52, s82, s52\n\t"
        "buffer_load_dwordx4 v[12:15], v120, %[srd], s60 offen sc1\n\t"
        "s_add_i32 s80, s52, 1\n\t"
        "s_ashr_i32 s80, s80, 1\n\t"
        "s_add_i32 s45, s80, s50\n\t"
        "s_add_u32 s80, s40, 1\n\t"
        "s_min_u32 s80, s80, 63\n\t"
        "s_lshl_b32 s80, s80, 18\n\t"
        "s_add_u32 s69, s80, %[kx]\n\t"
        "s_add_u32 s70, s69, 0x1000\n\t"
        "s_lshl_b32 s80, s40, 18\n\t"
        "s_add_u32 s71, s80, %[vx]\n\t"
        "s_add_u32 s80, s40, 2\n\t"
        "s_lshl_b32 s81, s80, 9\n\t"
        "s_add_u32 s81, s81, %[kf]\n\t"
        "s_and_b32 s80, s80, 0x43\n\t"
        "s_cselect_b32 s64, s36, s81\n\t"
        "s_nop 15\n\t"
        "s_nop 7\n\t"
        "s_mov_b64 exec, 7\n\t"
        "buffer_store_dwordx2 v[48:49], v51, %[srd], s73 offen sc1\n\t"
        "s_mov_b64 exec, -1\n\t"
        "buffer_load_dwordx4 v[112:115], v120, %[wsd], s71 offen offset:0 sc1\n\t"
        "buffer_load_dwordx4 v[116:119], v120, %[wsd], s71 offen offset:1024 sc1\n\t"
        ".Lgo%=:\n\t"
        "s_waitcnt vmcnt(6)\n\t"
        "v_smfmac_f32_16x16x64_bf16 v[88:91], v[0:3], v[16:23], v122\n\t"
        "ds_write_b128 v120, v[0:3] offset:0\n\t"
        "v_smfmac_f32_16x16x64_bf16 v[92:95], v[0:3], v[24:31], v122\n\t"
        "v_smfmac_f32_16x16x64_bf16 v[96:99], v[0:3], v[32:39], v122\n\t"
        "v_smfmac_f32_16x16x64_bf16 v[100:103], v[0:3], v[40:47], v122\n\t"
        "s_waitcnt vmcnt(5)\n\t"
        "v_smfmac_f32_16x16x64_bf16 v[88:91], v[4:7], v[56:63], v122\n\t"
        "ds_write_b128 v120, v[4:7] offset:4096\n\t"
        "v_smfmac_f32_16x16x64_bf16 v[92:95], v[4:7], v[64:71], v122\n\t"
        "v_smfmac_f32_16x16x64_bf16 v[96:99], v[4:7], v[72:79], v122\n\t"
        "v_smfmac_f32_16x16x64_bf16 v[100:103], v[4:7], v[80:87], v122\n\t"
        "s_waitcnt vmcnt(4)\n\t"
        "v_smfmac_f32_16x16x64_bf16 v[88:91], v[8:11], v[128:135], v122\n\t"
        "ds_write_b128 v120, v[8:11] offset:8192\n\t"
        "v_smfmac_f32_16x16x64_bf16 v[92:95], v[8:11], v[136:143], v122\n\t"
        "s_waitcnt lgkmcnt(1)\n\t"
        "s_barrier\n\t"
        "v_smfmac_f32_16x16x64_bf16 v[96:99], v[8:11], v[192:199], v122\n\t"
        "ds_read_b128 v[16:19], v124 offset:0\n\t"
        "v_smfmac_f32_16x16x64_bf16 v[100:103], v[8:11], v[200:207], v122\n\t"
        "ds_read_b128 v[20:23], v125 offset:0\n\t"
        "s_waitcnt vmcnt(3)\n\t"
        "v_smfmac_f32_16x16x64_bf16 v[88:91], v[12:15], a[0:7], v122\n\t"
        "ds_write_b128 v120, v[12:15] offset:12288\n\t"
        "v_smfmac_f32_16x16x64_bf16 v[92:95], v[12:15], a[8:15], v122\n\t"
        "buffer_store_dword v50, off, %[wsd], s65 sc1\n\t"
        "v_smfmac_f32_16x16x64_bf16 v[96:99], v[12:15], a[64:71], v122\n\t"
        "ds_read_b128 v[24:27], v126 offset:0\n\t"
        "v_smfmac_f32_16x16x64_bf16 v[100:103], v[12:15], a[72:79], v122\n\t"
        "ds_read_b128 v[28:31], v124 offset:4096\n\t"
        "s_waitcnt lgkmcnt(4)\n\t"
        "v_smfmac_f32_16x16x64_bf16 v[88:91], v[16:19], a[16:23], v122\n\t"
        "ds_read_b128 v[32:35], v125 offset:4096\n\t"
        "v_smfmac_f32_16x16x64_bf16 v[92:95], v[16:19], a[24:31], v122\n\t"
        "ds_read_b128 v[36:39], v126 offset:4096\n\t"
        "s_waitcnt lgkmcnt(5)\n\t"
        "v_smfmac_f32_16x16x64_bf16 v[88:91], v[20:23], a[32:39], v122\n\t"
        "ds_read_b128 v[72:75], v121 offset:16384\n\t"
        "v_smfmac_f32_16x16x64_bf16 v[92:95], v[20:23], a[40:47], v122\n\t"
        "s_waitcnt lgkmcnt(5)\n\t"
        "s_barrier\n\t"
        "ds_read_b128 v[76:79], v121 offset:17408\n\t"
        "s_waitcnt lgkmcnt(5)\n\t"
        "v_smfmac_f32_16x16x64_bf16 v[88:91], v[24:27], a[48:55], v122\n\t"
        "ds_read_b128 v[0:3], v124 offset:12288\n\t"
        "v_smfmac_f32_16x16x64_bf16 v[92:95], v[24:27], a[56:63], v122\n\t"
        "ds_read_b128 v[80:83], v121 offset:18432\n\t"
        "s_waitcnt lgkmcnt(6)\n\t"
        "v_smfmac_f32_16x16x64_bf16 v[88:91], v[28:31], a[144:151], v122\n\t"
        "ds_read_b128 v[84:87], v121 offset:19456\n\t"
        "v_smfmac_f32_16x16x64_bf16 v[92:95], v[28:31], a[152:159], v122\n\t"
        "ds_read_b128 v[40:43], v124 offset:8192\n\t"
        "s_waitcnt lgkmcnt(7)\n\t"
        "v_smfmac_f32_16x16x64_bf16 v[88:91], v[32:35], a[160:167], v122\n\t"
        "ds_read_b128 v[44:47], v125 offset:8192\n\t"
        "v_mov_b32_e32 v12, 0x3f803f80\n\t"
        "v_smfmac_f32_16x16x64_bf16 v[92:95], v[32:35], a[168:175], v122\n\t"
        "ds_read_b128 v[56:59], v121 offset:20480\n\t"
        "v_mov_b32_e32 v13, 0x3f803f80\n\t"
        "s_waitcnt lgkmcnt(8)\n\t"
        "v_smfmac_f32_16x16x64_bf16 v[88:91], v[36:39], a[176:183], v122\n\t"
        "ds_read_b128 v[60:63], v121 offset:21504\n\t"
        "v_mov_b32_e32 v14, 0x3f803f80\n\t"
        "v_smfmac_f32_16x16x64_bf16 v[92:95], v[36:39], a[184:191], v122\n\t"
        "ds_read_b128 v[4:7], v125 offset:12288\n\t"
        "v_mov_b32_e32 v15, 0x3f803f80\n\t"
        "s_waitcnt lgkmcnt(8)\n\t"
        "s_waitcnt lgkmcnt(7)\n\t"
        "v_smfmac_f32_16x16x64_bf16 v[88:91], v[0:3], v[72:79], v122\n\t"
        "ds_read_b128 v[64:67], v121 offset:22528\n\t"
        "s_waitcnt lgkmcnt(6)\n\t"
        "v_smfmac_f32_16x16x64_bf16 v[92:95], v[0:3], v[80:87], v122\n\t"
        "ds_read_b128 v[68:71], v121 offset:23552\n\t"
        "s_waitcnt lgkmcnt(6)\n\t"
        "v_smfmac_f32_16x16x64_bf16 v[88:91], v[40:43], v[144:151], v122\n\t"
        "ds_read_b128 v[48:51], v126 offset:8192\n\t"
        "v_smfmac_f32_16x16x64_bf16 v[92:95], v[40:43], v[152:159], v122\n\t"
        "ds_read_b128 v[8:11], v126 offset:12288\n\t"
        "s_waitcnt lgkmcnt(7)\n\t"
        "v_smfmac_f32_16x16x64_bf16 v[88:91], v[44:47], v[160:167], v122\n\t"
        "ds_read_b128 v[72:75], v121 offset:24576\n\t"
        "v_smfmac_f32_16x16x64_bf16 v[92:95], v[44:47], v[168:175], v122\n\t"
        "ds_read_b128 v[76:79], v121 offset:25600\n\t"
        "s_waitcnt lgkmcnt(7)\n\t"
        "s_waitcnt lgkmcnt(6)\n\t"
        "v_smfmac_f32_16x16x64_bf16 v[88:91], v[4:7], v[56:63], v122\n\t"
        "ds_read_b128 v[80:83], v121 offset:26624\n\t"
        "s_waitcnt lgkmcnt(5)\n\t"
        "v_smfmac_f32_16x16x64_bf16 v[92:95], v[4:7], v[64:71], v122\n\t"
        "ds_read_b128 v[84:87], v121 offset:27648\n\t"
        "s_waitcnt lgkmcnt(5)\n\t"
        "v_smfmac_f32_16x16x64_bf16 v[88:91], v[48:51], v[176:183], v122\n\t"
        "v_mov_b32_e32 v64, 0\n\t"
        "v_smfmac_f32_16x16x64_bf16 v[92:95], v[48:51], v[184:191], v122\n\t"
        "v_mov_b32_e32 v65, 0\n\t"
        "s_waitcnt lgkmcnt(4)\n\t"
        "v_smfmac_f32_16x16x64_bf16 v[88:91], v[8:11], a[128:135], v122\n\t"
        "v_mov_b32_e32 v66, 0\n\t"
        "v_smfmac_f32_16x16x64_bf16 v[92:95], v[8:11], a[136:143], v122\n\t"
        "v_mov_b32_e32 v67, 0\n\t"
        "s_waitcnt lgkmcnt(2)\n\t"
        "v_smfmac_f32_16x16x64_bf16 v[96:99], v[0:3], v[72:79], v122\n\t"
        "ds_read_b128 v[72:75], v121 offset:28672\n\t"
        "s_waitcnt lgkmcnt(1)\n\t"
        "v_smfmac_f32_16x16x64_bf16 v[100:103], v[0:3], v[80:87], v122\n\t"
        "ds_read_b128 v[76:79], v121 offset:29696\n\t"
        "v_smfmac_f32_16x16x64_bf16 v[96:99], v[16:19], a[80:87], v122\n\t"
        "v_add_f32_e32 v88, v88, v90\n\t"
        "v_add_f32_e32 v89, v89, v91\n\t"
        "v_smfmac_f32_16x16x64_bf16 v[100:103], v[16:19], a[88:95], v122\n\t"
        "v_cvt_pk_bf16_f32 v90, v88, v89\n\t"
        "v_add_f32_e32 v92, v92, v94\n\t"
        "v_smfmac_f32_16x16x64_bf16 v[96:99], v[20:23], a[96:103], v122\n\t"
        "ds_write_b32 v127, v90\n\t"
        "v_add_f32_e32 v93, v93, v95\n\t"
        "v_cvt_pk_bf16_f32 v91, v92, v93\n\t"
        "v_smfmac_f32_16x16x64_bf16 v[100:103], v[20:23], a[104:111], v122\n\t"
        "ds_write_b32 v127, v91 offset:384\n\t"
        "v_cmp_u_f32_e32 vcc, v88, v89\n\t"
        "v_smfmac_f32_16x16x64_bf16 v[96:99], v[24:27], a[112:119], v122\n\t"
        "ds_read_b64_tr_b16 v[56:57], v52 offset:0\n\t"
        "ds_read_b64_tr_b16 v[58:59], v52 offset:64\n\t"
        "v_smfmac_f32_16x16x64_bf16 v[100:103], v[24:27], a[120:127], v122\n\t"
        "ds_read_b64_tr_b16 v[60:61], v53 offset:0\n\t"
        "ds_read_b64_tr_b16 v[62:63], v53 offset:64\n\t"
        "v_smfmac_f32_16x16x64_bf16 v[96:99], v[28:31], a[208:215], v122\n\t"
        "s_cbranch_vccnz .Lf1%=\n\t"
        ".Lr1%=:\n\t"
        "ds_read_b128 v[80:83], v121 offset:30720\n\t"
        "v_smfmac_f32_16x16x64_bf16 v[100:103], v[28:31], a[216:223], v122\n\t"
        "ds_read_b128 v[84:87], v121 offset:31744\n\t"
        "v_mov_b32_e32 v91, 0\n\t"
        "v_smfmac_f32_16x16x64_bf16 v[96:99], v[32:35], a[224:231], v122\n\t"
        "v_mov_b32_e32 v94, 0\n\t"
        "v_smfmac_f32_16x16x64_bf16 v[100:103], v[32:35], a[232:239], v122\n\t"
        "ds_read_b128 v[32:35], v120 offset:20480\n\t"
        "v_smfmac_f32_16x16x64_bf16 v[96:99], v[36:39], a[240:247], v122\n\t"
        "s_waitcnt lgkmcnt(3)\n\t"
        "s_barrier\n\t"
        "v_mfma_f32_16x16x32_bf16 v[68:71], v[104:107], v[56:59], 0\n\t"
        "ds_read_b64_tr_b16 v[16:17], v123 offset:0\n\t"
        "v_mfma_f32_16x16x32_bf16 v[68:71], v[108:111], v[60:63], v[68:71]\n\t"
        "ds_read_b64_tr_b16 v[18:19], v123 offset:64\n\t"
        "v_smfmac_f32_16x16x64_bf16 v[100:103], v[36:39], a[248:255], v122\n\t"
        "buffer_load_dword v88, v127, %[srd], s61 offen offset:0 sc1\n\t"
        "v_smfmac_f32_16x16x64_bf16 v[96:99], v[8:11], a[192:199], v122\n\t"
        "buffer_load_dword v89, v127, %[srd], s61 offen offset:256 sc1\n\t"
        "v_smfmac_f32_16x16x64_bf16 v[100:103], v[8:11], a[200:207], v122\n\t"
        "s_nop 1\n\t"
        "ds_write_b128 v120, v[68:71] offset:16384\n\t"
        "v_smfmac_f32_16x16x64_bf16 v[96:99], v[4:7], v[72:79], v122\n\t"
        "ds_read_b64_tr_b16 v[20:21], v123 offset:768\n\t"
        "s_waitcnt lgkmcnt(5)\n\t"
        "v_smfmac_f32_16x16x64_bf16 v[100:103], v[4:7], v[80:87], v122\n\t"
        "ds_read_b64_tr_b16 v[22:23], v123 offset:832\n\t"
        "v_smfmac_f32_16x16x64_bf16 v[96:99], v[40:43], v[208:215], v122\n\t"
        "ds_read_b64_tr_b16 v[24:25], v123 offset:1536\n\t"
        "ds_read_b64_tr_b16 v[26:27], v123 offset:1600\n\t"
        "v_smfmac_f32_16x16x64_bf16 v[100:103], v[40:43], v[216:223], v122\n\t"
        "buffer_load_dwordx4 v[84:87], v120, %[srd], s66 offen sc1\n\t"
        "v_smfmac_f32_16x16x64_bf16 v[96:99], v[44:47], v[224:231], v122\n\t"
        "s_waitcnt lgkmcnt(4)\n\t"
        "s_barrier\n\t"
        "ds_read_b128 v[36:39], v124 offset:16384\n\t"
        "v_smfmac_f32_16x16x64_bf16 v[100:103], v[44:47], v[232:239], v122\n\t"
        "ds_read_b128 v[56:59], v125 offset:16384\n\t"
        "v_smfmac_f32_16x16x64_bf16 v[96:99], v[48:51], v[240:247], v122\n\t"
        "ds_read_b128 v[60:63], v126 offset:16384\n\t"
        "v_smfmac_f32_16x16x64_bf16 v[100:103], v[48:51], v[248:255], v122\n\t"
        "ds_read_b64_tr_b16 v[28:29], v123 offset:2304\n\t"
        "ds_read_b64_tr_b16 v[30:31], v123 offset:2368\n\t"
        "s_waitcnt lgkmcnt(4)\n\t"
        "s_cmp_eq_u32 s40, 63\n\t"
        "s_nop 1\n\t"
        "v_add_f32_e32 v96, v96, v98\n\t"
        "v_add_f32_e32 v97, v97, v99\n\t"
        "s_waitcnt lgkmcnt(2)\n\t"
        "v_cvt_pk_bf16_f32 v50, v96, v97\n\t"
        "v_pk_add_f32 v[100:101], v[100:101], v[102:103]\n\t"
        "ds_write_b32 v35, v50\n\t"
        "v_cvt_pk_bf16_f32 v51, v100, v101\n\t"
        "ds_write_b32 v35, v51 offset:384\n\t"
        "v_pk_add_f32 v[56:57], v[56:57], v[60:61]\n\t"
        "s_cbranch_scc1 .Lend%=\n\t"
        "v_pk_add_f32 v[58:59], v[58:59], v[62:63]\n\t"
        "v_pk_add_f32 v[68:69], v[68:69], v[36:37]\n\t"
        "v_pk_add_f32 v[70:71], v[70:71], v[38:39]\n\t"
        "v_pk_add_f32 v[68:69], v[68:69], v[56:57]\n\t"
        "v_pk_add_f32 v[70:71], v[70:71], v[58:59]\n\t"
        "v_max3_f32 v2, v68, v69, v70\n\t"
        "v_max_f32_e32 v2, v2, v71\n\t"
        "s_nop 1\n\t"
        "v_max_f32_dpp v3, v2, v2 row_ror:8 row_mask:0xf bank_mask:0xf\n\t"
        "v_max_f32_dpp v1, v2, v2 row_ror:8 row_mask:0xf bank_mask:0xf\n\t"
        "s_nop 1\n\t"
        "v_permlane16_swap_b32_e32 v3, v1\n\t"
        "s_waitcnt lgkmcnt(0)\n\t"
        "s_barrier\n\t"
        "ds_read_b64_tr_b16 v[72:73], v32 offset:0\n\t"
        "v_max_f32_e32 v2, v3, v1\n\t"
        "ds_read_b64_tr_b16 v[74:75], v32 offset:128\n\t"
        "v_max_f32_e32 v3, v3, v1\n\t"
        "ds_read_b64_tr_b16 v[76:77], v32 offset:1536\n\t"
        "s_nop 0\n\t"
        "v_permlane32_swap_b32_e32 v2, v3\n\t"
        "ds_read_b64_tr_b16 v[78:79], v32 offset:1664\n\t"
        "v_max_f32_e32 v0, v2, v3\n\t"
        "v_mul_f32_e32 v2, 0xbe0293ee, v0\n\t"
        "v_fmamk_f32 v68, v68, 0x3e0293ee, v2\n\t"
        "v_fmamk_f32 v69, v69, 0x3e0293ee, v2\n\t"
        "v_fmamk_f32 v70, v70, 0x3e0293ee, v2\n\t"
        "v_fmamk_f32 v71, v71, 0x3e0293ee, v2\n\t"
        "v_exp_f32_e32 v68, v68\n\t"
        "s_waitcnt lgkmcnt(2)\n\t"
        "v_smfmac_f32_16x16x64_bf16 v[64:67], v[72:75], v[16:23], v122\n\t"
        "s_waitcnt lgkmcnt(0)\n\t"
        "v_smfmac_f32_16x16x64_bf16 v[64:67], v[76:79], v[24:31], v122\n\t"
        "s_waitcnt vmcnt(4)\n\t"
        "v_exp_f32_e32 v69, v69\n\t"
        "v_exp_f32_e32 v70, v70\n\t"
        "v_cvt_pk_bf16_f32 v36, v68, v69\n\t"
        "v_exp_f32_e32 v71, v71\n\t"
        "s_nop 0\n\t"
        "v_mov_b32_dpp v38, v36 row_ror:8 row_mask:0xf bank_mask:0xf\n\t"
        "v_cvt_pk_bf16_f32 v37, v70, v71\n\t"
        "s_nop 1\n\t"
        "v_mov_b32_dpp v39, v37 row_ror:8 row_mask:0xf bank_mask:0xf\n\t"
        "v_pk_add_f32 v[64:65], v[64:65], v[66:67]\n\t"
        "s_nop 0\n\t"
        "v_mfma_f32_16x16x32_bf16 v[96:99], v[12:15], v[36:39], 0\n\t"
        "v_fmamk_f32 v66, v64, 0x3e0293ee, v2\n\t"
        "v_fmamk_f32 v67, v65, 0x3e0293ee, v2\n\t"
        "v_mfma_f32_16x16x32_bf16 v[40:43], v[112:115], v[36:39], 0\n\t"
        "v_max_f32_e32 v3, v66, v67\n\t"
        "v_exp_f32_e32 v66, v66\n\t"
        "v_mfma_f32_16x16x32_bf16 v[44:47], v[116:119], v[36:39], 0\n\t"
        "v_cmp_nge_f32_e32 vcc, 0x41a00000, v3\n\t"
        "v_exp_f32_e32 v67, v67\n\t"
        "v_mov_b32_e32 v10, 1.0\n\t"
        "v_cvt_pk_bf16_f32 v90, v66, v67\n\t"
        "v_cvt_pk_bf16_f32 v95, v66, v67\n\t"
        "s_cbranch_vccnz .Lfm%=\n\t"
        ".Lrm%=:\n\t"
        "s_waitcnt vmcnt(1)\n\t"
        "v_mfma_f32_16x16x16_bf16 v[96:99], v[12:13], v[90:91], v[96:99]\n\t"
        "v_max_u32_e32 v54, v88, v89\n\t"
        "v_mfma_f32_16x16x16_bf16 v[40:43], v[88:89], v[90:91], v[40:43]\n\t"
        "v_cmp_eq_u32_e32 vcc, -1, v54\n\t"
        "v_mfma_f32_16x16x16_bf16 v[44:47], v[88:89], v[94:95], v[44:47]\n\t"
        "s_nop 1\n\t"
        "v_mov_b32_e32 v55, v55\n\t"
        "s_nop 0\n\t"
        "v_rcp_f32_e32 v2, v96\n\t"
        "s_cbranch_vccnz .Lfb%=\n\t"
        ".Lrb%=:\n\t"
        "v_pk_mul_f32 v[40:41], v[40:41], v[2:3] op_sel_hi:[1,0]\n\t"
        "v_pk_mul_f32 v[42:43], v[42:43], v[2:3] op_sel_hi:[1,0]\n\t"
        "v_cvt_pk_bf16_f32 v100, v40, v41\n\t"
        "v_pk_mul_f32 v[44:45], v[44:45], v[2:3] op_sel_hi:[1,0]\n\t"
        "v_pk_mul_f32 v[46:47], v[46:47], v[2:3] op_sel_hi:[1,0]\n\t"
        "v_cvt_pk_bf16_f32 v101, v42, v43\n\t"
        "v_cvt_pk_bf16_f32 v102, v44, v45\n\t"
        "v_min_u32_e32 v100, -2, v100\n\t"
        "v_cvt_pk_bf16_f32 v103, v46, v47\n\t"
        "v_min_u32_e32 v102, -2, v102\n\t"
        "buffer_store_dwordx4 v[100:103], v33, %[srd], s63 offen sc1\n\t"
        "s_memrealtime s[76:77]\n\t"
        "v_mov_b32_e32 v8, -1\n\t"
        "v_mov_b32_e32 v9, -1\n\t"
        "v_mov_b32_e32 v10, -1\n\t"
        "v_mov_b32_e32 v11, -1\n\t"
        "v_permlane16_swap_b32_e32 v50, v51\n\t"
        "v_permlane16_swap_b32_e32 v88, v89\n\t"
        "v_mov_b32_e32 v14, s40\n\t"
        "s_waitcnt lgkmcnt(0)\n\t"
        "s_waitcnt vmcnt(1)\n\t"
        "s_mov_b32 s97, 0\n\t"
        ".Lsr%=:\n\t"
        "s_mov_b32 exec_hi, 0xffff\n\t"
        "v_subrev_u32_e32 v0, s41, v84\n\t"
        "v_subrev_u32_e32 v1, s41, v86\n\t"
        "v_max_i32_e32 v0, v0, v1\n\t"
        "v_max_u32_e32 v1, v85, v87\n\t"
        "v_cmp_eq_u32_e32 vcc, -1, v1\n\t"
        "v_max_i32_dpp v0, v0, v0 quad_perm:[1,0,3,2] row_mask:0xf bank_mask:0xf\n\t"
        "v_or_b32_e32 v2, v85, v87\n\t"
        "s_nop 0\n\t"
        "v_max_i32_dpp v0, v0, v0 quad_perm:[2,3,0,1] row_mask:0xf bank_mask:0xf\n\t"
        "s_nop 1\n\t"
        "v_max_i32_dpp v0, v0, v0 row_ror:4 row_mask:0xf bank_mask:0xf\n\t"
        "s_nop 1\n\t"
        "v_max_i32_dpp v0, v0, v0 row_ror:8 row_mask:0xf bank_mask:0xf\n\t"
        "s_cbranch_vccnz .Lsf%=\n\t"
        "v_readlane_b32 s85, v0, 0\n\t"
        "v_readlane_b32 s81, v0, 16\n\t"
        "v_readlane_b32 s56, v0, 32\n\t"
        "v_and_b32_e32 v1, 1, v2\n\t"
        "v_cmp_ne_u32_e64 s[86:87], 0, v1\n\t"
        "v_and_b32_e32 v1, 2, v2\n\t"
        "v_cmp_ne_u32_e64 s[88:89], 0, v1\n\t"
        "v_and_b32_e32 v1, 4, v2\n\t"
        "v_cmp_ne_u32_e64 s[90:91], 0, v1\n\t"
        "s_max_i32 s56, s56, s81\n\t"
        "s_add_u32 s92, s85, s41\n\t"
        "s_sub_u32 s92, s92, s43\n\t"
        "s_add_i32 s93, s56, s47\n\t"
        "s_mov_b32 s44, s46\n\t"
        "s_mov_b32 s46, s45\n\t"
        "s_mov_b32 s94, s48\n\t"
        "s_mov_b32 s43, s41\n\t"
        "s_cmp_gt_i32 s56, s54\n\t"
        "s_cbranch_scc1 .Lmv%=\n\t"
        "s_mov_b32 s37, 0\n\t"
        "s_mov_b32 s48, 0\n\t"
        "s_mov_b32 s95, 0\n\t"
        ".Lsd%=:\n\t"
        "s_mov_b32 exec_hi, -1\n\t"
        "s_add_u32 s42, s43, s46\n\t"
        "s_sleep %c[HSLP]\n\t"
        "buffer_load_dwordx2 v[48:49], off, %[wsd], s64 sc1\n\t"
        "buffer_load_dwordx4 v[104:107], v120, %[wsd], s69 offen sc1\n\t"
        "buffer_load_dwordx4 v[108:111], v120, %[wsd], s70 offen sc1\n\t"
        "buffer_store_dwordx2 v[50:51], v34, %[wsd], s72 offen sc1\n\t"
        "buffer_store_dwordx2 v[88:89], v34, %[wsd], s72 offen offset:2048 sc1\n\t"
        "s_lshl_b32 s80, %[w], 8\n\t"
        "s_add_u32 s80, s80, s61\n\t"
        "s_add_u32 s80, s80, 24576\n\t"
        "buffer_store_dwordx2 v[8:9], v34, %[srd], s80 offen sc1\n\t"
        "s_cmp_eq_u32 %[w], 0\n\t"
        "s_cselect_b32 exec_hi, 0, -1\n\t"
        "buffer_store_dwordx4 v[8:11], v120, %[srd], s68 offen sc1\n\t"
        "s_mov_b32 exec_hi, -1\n\t"
        "s_mov_b64 exec, 1\n\t"
        "buffer_store_dword v14, off, %[wsd], s74 sc1\n\t"
        "s_mov_b64 exec, -1\n\t"
        "ds_read_b128 v[16:19], v121 offset:0\n\t"
        "ds_read_b128 v[20:23], v121 offset:1024\n\t"
        "ds_read_b128 v[24:27], v121 offset:2048\n\t"
        "ds_read_b128 v[28:31], v121 offset:3072\n\t"
        "ds_read_b128 v[32:35], v121 offset:4096\n\t"
        "ds_read_b128 v[36:39], v121 offset:5120\n\t"
        "ds_read_b128 v[40:43], v121 offset:6144\n\t"
        "ds_read_b128 v[44:47], v121 offset:7168\n\t"
        "ds_read_b128 v[56:59], v121 offset:8192\n\t"
        "ds_read_b128 v[60:63], v121 offset:9216\n\t"
        "ds_read_b128 v[64:67], v121 offset:10240\n\t"
        "ds_read_b128 v[68:71], v121 offset:11264\n\t"
        "ds_read_b128 v[72:75], v121 offset:12288\n\t"
        "ds_read_b128 v[76:79], v121 offset:13312\n\t"
        "ds_read_b128 v[80:83], v121 offset:14336\n\t"
        "ds_read_b128 v[84:87], v121 offset:15360\n\t"
        "s_cmp_ge_u32 s40, %c[iCT0]\n\t"
        "s_cselect_b32 s85, 1, 0\n\t"
        "s_cmp_lg_u64 s[86:87], 0\n\t"
        "s_cselect_b32 s86, s85, 0\n\t"
        "s_cmp_lg_u64 s[88:89], 0\n\t"
        "s_cselect_b32 s87, s85, 0\n\t"
        "s_cmp_lg_u64 s[90:91], 0\n\t"
        "s_cselect_b32 s88, s85, 0\n\t"
        "s_or_b32 s80, s94, s86\n\t"
        "s_or_b32 s80, s80, s87\n\t"
        "s_andn2_b32 s90, s85, s80\n\t"
        "s_cmp_le_i32 s92, s44\n\t"
        "s_cselect_b32 s80, s85, 0\n\t"
        "s_andn2_b32 s91, s80, s88\n\t"
        "s_and_b32 s80, s40, %c[LDEC]\n\t"
        "s_cselect_b32 s89, 0, 1\n\t"
        "s_andn2_b32 s80, s86, s94\n\t"
        "s_cmp_lt_i32 s51, %c[LA24]\n\t"
        "s_cselect_b32 s80, s80, 0\n\t"
        "s_lshl2_add_u32 s51, s80, s51\n\t"
        "s_sub_i32 s51, s51, s89\n\t"
        "s_max_i32 s51, s51, %c[LA]\n\t"
        "s_lshl_b32 s80, s93, 1\n\t"
        "s_sub_i32 s81, s80, s53\n\t"
        "s_ashr_i32 s82, s81, %c[UPSH]\n\t"
        "s_max_i32 s82, s82, %c[TRUP]\n\t"
        "s_add_i32 s82, s82, s53\n\t"
        "s_min_i32 s82, s82, s80\n\t"
        "s_ashr_i32 s83, s81, %c[DNSH]\n\t"
        "s_min_i32 s83, s83, -%c[TRDN]\n\t"
        "s_add_i32 s83, s83, s53\n\t"
        "s_max_i32 s83, s83, s80\n\t"
        "s_cmp_gt_i32 s81, 0\n\t"
        "s_cselect_b32 s82, s82, s83\n\t"
        "s_cmp_lg_u32 s91, 0\n\t"
        "s_cselect_b32 s53, s82, s53\n\t"
        "s_add_i32 s80, s53, 1\n\t"
        "s_ashr_i32 s80, s80, 1\n\t"
        "s_add_i32 s47, s80, s51\n\t"
        "s_add_u32 s81, s47, %c[AUP]\n\t"
        "s_min_i32 s80, s95, s81\n\t"
        "s_max_i32 s47, s47, s80\n\t"
        "s_add_u32 s41, s42, s47\n\t"
        "s_cmp_eq_u32 s40, 0\n\t"
        "s_cselect_b32 s51, %c[LA], s51\n\t"
        "v_mov_b32_e32 v88, 0\n\t"
        "v_mov_b32_e32 v89, 0\n\t"
        "v_mov_b32_e32 v90, 0\n\t"
        "v_mov_b32_e32 v91, 0\n\t"
        "v_mov_b32_e32 v92, 0\n\t"
        "v_mov_b32_e32 v93, 0\n\t"
        "v_mov_b32_e32 v94, 0\n\t"
        "v_mov_b32_e32 v95, 0\n\t"
        "v_mov_b32_e32 v96, 0\n\t"
        "v_mov_b32_e32 v97, 0\n\t"
        "v_mov_b32_e32 v98, 0\n\t"
        "v_mov_b32_e32 v99, 0\n\t"
        "v_mov_b32_e32 v100, 0\n\t"
        "v_mov_b32_e32 v101, 0\n\t"
        "v_mov_b32_e32 v102, 0\n\t"
        "v_mov_b32_e32 v103, 0\n\t"
        "s_add_u32 s40, s40, 1\n\t"
        "v_mov_b32_e32 v6, 30720\n\t"
        "ds_read_b64 v[6:7], v6\n\t"
        "s_waitcnt lgkmcnt(0)\n\t"
        "v_readfirstlane_b32 s80, v6\n\t"
        "v_readfirstlane_b32 s81, v7\n\t"
        "s_cmp_eq_u32 s80, s40\n\t"
        "s_cselect_b32 s84, 1, 0\n\t"
        "s_cmp_eq_u32 s81, s40\n\t"
        "s_cselect_b32 s81, 2, 0\n\t"
        "s_or_b32 s84, s84, s81\n\t"
        "s_mov_b32 s85, s79\n\t"
        "s_and_b32 s80, s40, 3\n\t"
        "s_lshl_b32 s80, s80, 17\n\t"
        "s_add_u32 s57, s80, %[nA]\n\t"
        "s_add_u32 s58, s57, 0x1000\n\t"
        "s_add_u32 s59, s57, 0x8000\n\t"
        "s_add_u32 s60, s57, 0x9000\n\t"
        "s_and_b32 s80, s40, 3\n\t"
        "s_lshl_b32 s80, s80, 17\n\t"
        "s_add_u32 s63, s80, %[nC]\n\t"
        "s_add_u32 s66, s80, %[nS]\n\t"
        "s_add_u32 s81, s80, 0x20000\n\t"
        "s_and_b32 s81, s81, 0x60000\n\t"
        "s_add_u32 s67, s81, %[nW]\n\t"
        "s_add_u32 s79, s81, %[nT]\n\t"
        "s_cmp_eq_u32 %[w], 2\n\t"
        "s_cselect_b32 s81, s80, s81\n\t"
        "s_xor_b32 s81, s81, 0x40000\n\t"
        "s_add_u32 s68, s81, %[pob]\n\t"
        "s_lshl_b32 s72, s40, 17\n\t"
        "s_add_u32 s72, s72, %[kvo]\n\t"
        "v_lshlrev_b32_e32 v51, 6, v120\n\t"
        "s_cmp_eq_u32 %[w], 0\n\t"
        "s_cselect_b32 s73, s85, s36\n\t"
        "s_cselect_b32 s74, %[pra], s36\n\t"
        "v_mov_b32_e32 v50, s40\n\t"
        "s_cmp_ge_u32 s40, 60\n\t"
        "s_cselect_b32 s65, s74, s36\n\t"
        "s_mov_b32 s80, s61\n\t"
        "s_mov_b32 s61, s62\n\t"
        "s_mov_b32 s62, s80\n\t"
        "s_waitcnt vmcnt(7)\n\t"
        "s_cmp_eq_u32 s64, s36\n\t"
        "s_cbranch_scc1 .Lk11%=\n\t"
        "s_mov_b32 s97, 0\n\t"
        ".Lk01%=:\n\t"
        "v_readfirstlane_b32 s82, v48\n\t"
        "v_readfirstlane_b32 s83, v49\n\t"
        "s_xor_b32 s82, s82, %[tag]\n\t"
        "s_xor_b32 s83, s83, %[tag]\n\t"
        "s_or_b32 s82, s82, s83\n\t"
        "s_cbranch_scc0 .Lk11%=\n\t"
        "s_sleep 3\n\t"
        "buffer_load_dwordx2 v[48:49], off, %[wsd], s64 sc1\n\t"
        "s_add_u32 s97, s97, 1\n\t"
        "s_cmp_lt_u32 s97, 0x200000\n\t"
        "s_cbranch_scc0 .Lgu%=\n\t"
        "s_waitcnt vmcnt(0)\n\t"
        "s_branch .Lk01%=\n\t"
        ".Lk11%=:\n\t"
        "v_mov_b32_e32 v48, s76\n\t"
        "v_mov_b32_e32 v49, s84\n\t"
        "s_branch .Lloop%=\n\t"
        ".Lmv%=:\n\t"
        ".Lhk0%=:\n\t"
        "s_cbranch_execz .Lhk1%=\n\t"
        "s_mov_b32 s48, 1\n\t"
        "s_cmp_eq_u32 s54, %c[iTOL]\n\t"
        "s_cbranch_scc1 .Lm1%=\n\t"
        "s_max_i32 s80, s85, s56\n\t"
        "s_add_u32 s43, s41, s80\n\t"
        "s_add_u32 s43, s43, %c[iPEN0]\n\t"
        ".Lhk1%=:\n\t"
        "s_cbranch_execz .Lhk2%=\n\t"
        "s_mov_b32 s54, %c[iTOL]\n\t"
        "s_add_u32 s46, s46, %c[iX0C]\n\t"
        "s_mov_b32 s95, 0\n\t"
        "s_add_u32 s51, s51, %c[iX0A]\n\t"
        "v_and_b32_e32 v0, 3, v84\n\t"
        "v_and_b32_e32 v1, 3, v86\n\t"
        ".Lhk2%=:\n\t"
        "s_cbranch_execz .Lhk3%=\n\t"
        "v_lshlrev_b32_e64 v0, v0, 1\n\t"
        "v_lshlrev_b32_e64 v1, v1, 1\n\t"
        "v_or_b32_e32 v0, v0, v1\n\t"
        "s_mov_b32 s80, 0\n\t"
        "v_and_b32_e32 v1, 1, v0\n\t"
        "v_cmp_ne_u32_e32 vcc, 0, v1\n\t"
        ".Lhk3%=:\n\t"
        "s_cbranch_execz .Lhk4%=\n\t"
        "s_nop 1\n\t"
        "s_cmp_lg_u64 vcc, 0\n\t"
        "s_cselect_b32 s81, 2, 0\n\t"
        "s_or_b32 s80, s80, s81\n\t"
        "v_and_b32_e32 v1, 2, v0\n\t"
        "v_cmp_ne_u32_e32 vcc, 0, v1\n\t"
        ".Lhk4%=:\n\t"
        "s_cbranch_execz .Lhk5%=\n\t"
        "s_nop 1\n\t"
        "s_cmp_lg_u64 vcc, 0\n\t"
        "s_cselect_b32 s81, 4, 0\n\t"
        "s_or_b32 s80, s80, s81\n\t"
        "v_and_b32_e32 v1, 4, v0\n\t"
        "v_cmp_ne_u32_e32 vcc, 0, v1\n\t"
        ".Lhk5%=:\n\t"
        "s_cbranch_execz .Lhk6%=\n\t"
        "s_nop 1\n\t"
        "s_cmp_lg_u64 vcc, 0\n\t"
        "s_cselect_b32 s81, 8, 0\n\t"
        "s_or_b32 s80, s80, s81\n\t"
        "v_and_b32_e32 v1, 8, v0\n\t"
        "v_cmp_ne_u32_e32 vcc, 0, v1\n\t"
        ".Lhk6%=:\n\t"
        "s_cbranch_execz .Lhk7%=\n\t"
        "s_nop 1\n\t"
        "s_cmp_lg_u64 vcc, 0\n\t"
        "s_cselect_b32 s81, 16, 0\n\t"
        "s_or_b32 s80, s80, s81\n\t"
        "s_lshr_b32 s80, 0x1adf1210, s80\n\t"
        "s_and_b32 s55, s80, 3\n\t"
        ".Lhk7%=:\n\t"
        "s_cbranch_execz .Lhk8%=\n\t"
        "s_branch .Lal%=\n\t"
        ".Lm1%=:\n\t"
        "s_add_u32 s80, s37, %c[iPEN2]\n\t"
        "s_min_i32 s80, s80, %c[iPENM]\n\t"
        "s_cmp_lg_u32 s37, 0\n\t"
        "s_cselect_b32 s37, s80, %c[iPEN]\n\t"
        "s_add_u32 s80, s56, s37\n\t"
        ".Lhk8%=:\n\t"
        "s_cbranch_execz .Lhk9%=\n\t"
        "s_max_i32 s80, s80, %c[iPMIN]\n\t"
        "s_add_u32 s43, s41, s80\n\t"
        "s_add_u32 s95, s93, %c[LA]\n\t"
        "s_add_u32 s46, s46, %c[iXR]\n\t"
        ".Lal%=:\n\t"
        "s_sub_u32 s80, s55, s43\n\t"
        "s_and_b32 s80, s80, %c[iPHM]\n\t"
        ".Lhk9%=:\n\t"
        "s_cbranch_execz .Lhk10%=\n\t"
        "s_add_u32 s43, s43, s80\n\t"
        "s_branch .Lsd%=\n\t"
        ".Lsf%=:\n\t"
        "s_mov_b32 exec_hi, -1\n\t"
        "s_sleep 2\n\t"
        "buffer_load_dwordx4 v[84:87], v120, %[srd], s66 offen sc1\n\t"
        "s_add_u32 s97, s97, 1\n\t"
        ".Lhk10%=:\n\t"
        "s_cbranch_execz .Lhk11%=\n\t"
        "s_cmp_lt_u32 s97, 0x100000\n\t"
        "s_cbranch_scc0 .Lgu%=\n\t"
        "s_waitcnt vmcnt(0)\n\t"
        "s_branch .Lsr%=\n\t"
        ".Lf1%=:\n\t"
        "v_max_u32_e32 v54, v0, v2\n\t"
        "v_max3_u32 v54, v54, v1, v3\n\t"
        ".Lhk11%=:\n\t"
        "s_cbranch_execz .Lhk12%=\n\t"
        "v_max3_u32 v54, v54, v4, v6\n\t"
        "v_max3_u32 v54, v54, v5, v7\n\t"
        "v_max3_u32 v54, v54, v8, v10\n\t"
        "v_max3_u32 v54, v54, v9, v11\n\t"
        "s_sleep %c[RSLP]\n\t"
        "buffer_load_dwordx4 v[0:3], v120, %[srd], s57 offen sc1\n\t"
        ".Lhk12%=:\n\t"
        "s_cbranch_execz .Lhk13%=\n\t"
        "buffer_load_dwordx4 v[4:7], v120, %[srd], s58 offen sc1\n\t"
        "buffer_load_dwordx4 v[8:11], v120, %[srd], s59 offen sc1\n\t"
        "buffer_load_dwordx4 v[12:15], v120, %[srd], s60 offen sc1\n\t"
        "buffer_store_dword v0, off, %[wsd], s36\n\t"
        "buffer_store_dword v0, off, %[wsd], s36\n\t"
        "buffer_store_dword v0, off, %[wsd], s36\n\t"
        ".Lhk13%=:\n\t"
        "s_cbranch_execz .Lhk14%=\n\t"
        "ds_read_b128 v[92:95], v120 offset:0\n\t"
        "ds_read_b128 v[68:71], v120 offset:4096\n\t"
        "v_max3_u32 v54, v54, v16, v18\n\t"
        "v_max3_u32 v54, v54, v17, v19\n\t"
        "v_max3_u32 v54, v54, v20, v22\n\t"
        "v_max3_u32 v54, v54, v21, v23\n\t"
        ".Lhk14%=:\n\t"
        "s_cbranch_execz .Lhk15%=\n\t"
        "v_max3_u32 v54, v54, v24, v26\n\t"
        "v_max3_u32 v54, v54, v25, v27\n\t"
        "v_max3_u32 v54, v54, v28, v30\n\t"
        "v_max3_u32 v54, v54, v29, v31\n\t"
        "v_max3_u32 v54, v54, v32, v34\n\t"
        "v_max3_u32 v54, v54, v33, v35\n\t"
        ".Lhk15%=:\n\t"
        "s_cbranch_execz .Lhk16%=\n\t"
        "v_max3_u32 v54, v54, v36, v38\n\t"
        "v_max3_u32 v54, v54, v37, v39\n\t"
        "v_max3_u32 v54, v54, v40, v42\n\t"
        "v_max3_u32 v54, v54, v41, v43\n\t"
        "v_max3_u32 v54, v54, v44, v46\n\t"
        "v_max3_u32 v54, v54, v45, v47\n\t"
        ".Lhk16%=:\n\t"
        "s_cbranch_execz .Lhk17%=\n\t"
        "v_max3_u32 v54, v54, v48, v50\n\t"
        "v_max3_u32 v54, v54, v49, v51\n\t"
        "s_waitcnt lgkmcnt(0)\n\t"
        "v_max3_u32 v54, v54, v92, v94\n\t"
        "v_max3_u32 v54, v54, v93, v95\n\t"
        "v_max3_u32 v54, v54, v68, v70\n\t"
        ".Lhk17%=:\n\t"
        "s_cbranch_execz .Lhk18%=\n\t"
        "v_max3_u32 v54, v54, v69, v71\n\t"
        "ds_read_b128 v[92:95], v120 offset:8192\n\t"
        "ds_read_b128 v[68:71], v120 offset:12288\n\t"
        "s_waitcnt lgkmcnt(0)\n\t"
        "v_max3_u32 v54, v54, v92, v94\n\t"
        "v_max3_u32 v54, v54, v93, v95\n\t"
        ".Lhk18%=:\n\t"
        "s_cbranch_execz .Lhk19%=\n\t"
        "v_max3_u32 v54, v54, v68, v70\n\t"
        "v_max3_u32 v54, v54, v69, v71\n\t"
        "v_cmp_eq_u32_e32 vcc, -1, v54\n\t"
        "s_nop 1\n\t"
        "v_mov_b32_e32 v55, v55\n\t"
        "s_nop 0\n\t"
        ".Lhk19%=:\n\t"
        "s_cbranch_execz .Lhk20%=\n\t"
        "s_cbranch_vccz .Lf2%=\n\t"
        "ds_read_b128 v[16:19], v121 offset:0\n\t"
        "ds_read_b128 v[20:23], v121 offset:1024\n\t"
        "ds_read_b128 v[24:27], v121 offset:2048\n\t"
        "ds_read_b128 v[28:31], v121 offset:3072\n\t"
        "ds_read_b128 v[32:35], v121 offset:4096\n\t"
        ".Lhk20%=:\n\t"
        "s_cbranch_execz .Lhk21%=\n\t"
        "ds_read_b128 v[36:39], v121 offset:5120\n\t"
        "ds_read_b128 v[40:43], v121 offset:6144\n\t"
        "ds_read_b128 v[44:47], v121 offset:7168\n\t"
        "ds_read_b128 v[56:59], v121 offset:8192\n\t"
        "ds_read_b128 v[60:63], v121 offset:9216\n\t"
        "ds_read_b128 v[64:67], v121 offset:10240\n\t"
        ".Lhk21%=:\n\t"
        "s_cbranch_execz .Lhk22%=\n\t"
        "ds_read_b128 v[68:71], v121 offset:11264\n\t"
        "ds_read_b128 v[72:75], v121 offset:12288\n\t"
        "ds_read_b128 v[76:79], v121 offset:13312\n\t"
        "ds_read_b128 v[80:83], v121 offset:14336\n\t"
        "ds_read_b128 v[84:87], v121 offset:15360\n\t"
        "s_add_u32 s80, s40, 1\n\t"
        ".Lhk22%=:\n\t"
        "s_cbranch_execz .Lhk23%=\n\t"
        "v_mov_b32_e32 v54, 30720\n\t"
        "v_mov_b32_e32 v55, s80\n\t"
        "ds_write_b32 v54, v55\n\t"
        "s_add_u32 s97, s97, 1\n\t"
        "s_cmp_lt_u32 s97, 0x100000\n\t"
        "s_cbranch_scc0 .Lgu%=\n\t"
        ".Lhk23%=:\n\t"
        "s_cbranch_execz .Lhk24%=\n\t"
        "s_mov_b32 s65, s36\n\t"
        "v_mov_b32_e32 v88, 0\n\t"
        "v_mov_b32_e32 v89, 0\n\t"
        "v_mov_b32_e32 v90, 0\n\t"
        "v_mov_b32_e32 v91, 0\n\t"
        "v_mov_b32_e32 v92, 0\n\t"
        ".Lhk24%=:\n\t"
        "s_cbranch_execz .Lhk25%=\n\t"
        "v_mov_b32_e32 v93, 0\n\t"
        "v_mov_b32_e32 v94, 0\n\t"
        "v_mov_b32_e32 v95, 0\n\t"
        "v_mov_b32_e32 v96, 0\n\t"
        "v_mov_b32_e32 v97, 0\n\t"
        "v_mov_b32_e32 v98, 0\n\t"
        ".Lhk25%=:\n\t"
        "s_cbranch_execz .Lhk26%=\n\t"
        "v_mov_b32_e32 v99, 0\n\t"
        "v_mov_b32_e32 v100, 0\n\t"
        "v_mov_b32_e32 v101, 0\n\t"
        "v_mov_b32_e32 v102, 0\n\t"
        "v_mov_b32_e32 v103, 0\n\t"
        "s_waitcnt lgkmcnt(0)\n\t"
        ".Lhk26%=:\n\t"
        "s_cbranch_execz .Lhk27%=\n\t"
        "s_barrier\n\t"
        "s_branch .Lgo%=\n\t"
        ".Lf2%=:\n\t"
        "s_waitcnt vmcnt(0)\n\t"
        "v_mov_b32_e32 v12, 0x3f803f80\n\t"
        "v_mov_b32_e32 v13, 0x3f803f80\n\t"
        "v_mov_b32_e32 v14, 0x3f803f80\n\t"
        ".Lhk27%=:\n\t"
        "s_cbranch_execz .Lhk28%=\n\t"
        "v_mov_b32_e32 v15, 0x3f803f80\n\t"
        "ds_read_b128 v[0:3], v124 offset:12288\n\t"
        "ds_read_b128 v[4:7], v125 offset:12288\n\t"
        "ds_read_b128 v[8:11], v126 offset:12288\n\t"
        "s_waitcnt lgkmcnt(0)\n\t"
        "s_branch .Lr1%=\n\t"
        ".Lfb%=:\n\t"
        ".Lhk28%=:\n\t"
        "s_cbranch_execz .Lhk29%=\n\t"
        "s_nop 7\n\t"
        "s_mov_b32 s97, 0\n\t"
        "s_add_u32 s80, s40, 1\n\t"
        "v_mov_b32_e32 v54, 30724\n\t"
        "v_mov_b32_e32 v55, s80\n\t"
        "ds_write_b32 v54, v55\n\t"
        ".Lfp%=:\n\t"
        ".Lhk29%=:\n\t"
        "s_cbranch_execz .Lhk30%=\n\t"
        "s_sleep %c[ASLP]\n\t"
        "buffer_load_dword v88, v127, %[srd], s61 offen offset:0 sc1\n\t"
        "buffer_load_dword v89, v127, %[srd], s61 offen offset:256 sc1\n\t"
        "s_add_u32 s97, s97, 1\n\t"
        "s_cmp_lt_u32 s97, 0x100000\n\t"
        "s_cbranch_scc0 .Lgu%=\n\t"
        ".Lhk30%=:\n\t"
        "s_cbranch_execz .Lhk31%=\n\t"
        "s_waitcnt vmcnt(0)\n\t"
        "v_max_u32_e32 v54, v88, v89\n\t"
        "v_cmp_eq_u32_e32 vcc, -1, v54\n\t"
        "s_nop 1\n\t"
        "v_mov_b32_e32 v55, v55\n\t"
        "s_nop 0\n\t"
        ".Lhk31%=:\n\t"
        "s_cbranch_execz .Lhk32%=\n\t"
        "s_cbranch_vccnz .Lfp%=\n\t"
        "v_mfma_f32_16x16x32_bf16 v[40:43], v[112:115], v[36:39], 0\n\t"
        "v_mfma_f32_16x16x32_bf16 v[44:47], v[116:119], v[36:39], 0\n\t"
        "s_nop 1\n\t"
        "v_mov_b32_e32 v55, v55\n\t"
        "s_nop 1\n\t"
        ".Lhk32%=:\n\t"
        "s_cbranch_execz .Lhk33%=\n\t"
        "v_mov_b32_e32 v55, v55\n\t"
        "s_nop 0\n\t"
        "v_pk_mul_f32 v[40:41], v[40:41], v[10:11] op_sel_hi:[1,0]\n\t"
        "v_pk_mul_f32 v[42:43], v[42:43], v[10:11] op_sel_hi:[1,0]\n\t"
        "v_pk_mul_f32 v[44:45], v[44:45], v[10:11] op_sel_hi:[1,0]\n\t"
        "v_pk_mul_f32 v[46:47], v[46:47], v[10:11] op_sel_hi:[1,0]\n\t"
        ".Lhk33%=:\n\t"
        "s_cbranch_execz .Lhk34%=\n\t"
        "v_mfma_f32_16x16x16_bf16 v[40:43], v[88:89], v[90:91], v[40:43]\n\t"
        "s_nop 0\n\t"
        "v_mfma_f32_16x16x16_bf16 v[44:47], v[88:89], v[94:95], v[44:47]\n\t"
        "s_nop 7\n\t"
        "s_branch .Lrb%=\n\t"
        ".Lfm%=:\n\t"
        "v_max3_f32 v3, v0, v64, v65\n\t"
        ".Lhk34%=:\n\t"
        "s_cbranch_execz .Lhk35%=\n\t"
        "v_max3_f32 v1, v0, v64, v65\n\t"
        "s_nop 1\n\t"
        "v_permlane16_swap_b32_e32 v3, v1\n\t"
        "v_max_f32_e32 v2, v3, v1\n\t"
        "v_max_f32_e32 v3, v3, v1\n\t"
        "s_nop 1\n\t"
        ".Lhk35%=:\n\t"
        "s_cbranch_execz .Lhk36%=\n\t"
        "v_permlane32_swap_b32_e32 v2, v3\n\t"
        "v_max_f32_e32 v2, v2, v3\n\t"
        "v_mul_f32_e32 v2, 0xbe0293ee, v2\n\t"
        "v_fmamk_f32 v10, v0, 0x3e0293ee, v2\n\t"
        "v_fmamk_f32 v66, v64, 0x3e0293ee, v2\n\t"
        "v_exp_f32_e32 v10, v10\n\t"
        ".Lhk36%=:\n\t"
        "s_cbranch_execz .Lhk37%=\n\t"
        "v_fmamk_f32 v67, v65, 0x3e0293ee, v2\n\t"
        "v_exp_f32_e32 v66, v66\n\t"
        "v_exp_f32_e32 v67, v67\n\t"
        "v_mul_f32_e32 v96, v96, v10\n\t"
        "v_pk_mul_f32 v[40:41], v[40:41], v[10:11] op_sel_hi:[1,0]\n\t"
        "v_pk_mul_f32 v[42:43], v[42:43], v[10:11] op_sel_hi:[1,0]\n\t"
        ".Lhk37%=:\n\t"
        "s_cbranch_execz .Lhk38%=\n\t"
        "v_pk_mul_f32 v[44:45], v[44:45], v[10:11] op_sel_hi:[1,0]\n\t"
        "v_pk_mul_f32 v[46:47], v[46:47], v[10:11] op_sel_hi:[1,0]\n\t"
        "v_cvt_pk_bf16_f32 v90, v66, v67\n\t"
        "v_cvt_pk_bf16_f32 v95, v66, v67\n\t"
        "s_nop 1\n\t"
        "s_branch .Lrm%=\n\t"
        ".Lta%=:\n\t"
        ".Lhk38%=:\n\t"
        "s_cbranch_execz .Lhk39%=\n\t"
        "s_waitcnt lgkmcnt(0)\n\t"
        "s_branch .Lxa%=\n\t"
        ".Lgu%=:\n\t"
        "s_mov_b64 exec, -1\n\t"
        "v_mov_b32_e32 v54, %c[ferr]\n\t"
        "v_mov_b32_e32 v55, 1\n\t"
        "buffer_store_dword v55, v54, %[wsd], 0 offen sc1\n\t"
        ".Lhk39%=:\n\t"
        "s_cbranch_execz .Lhk40%=\n\t"
        "s_endpgm\n\t"
        ".Lend%=:\n\t"
        "s_nop 0\n\t"
        "v_permlane16_swap_b32_e32 v50, v51\n\t"
        "v_mov_b32_e32 v0, 64\n\t"
        "buffer_store_dwordx2 v[50:51], v34, %[wsd], s72 offen sc1\n\t"
        "s_cmp_lg_u32 %[w], 0\n\t"
        ".Lhk40%=:\n\t"
        "s_cbranch_execz .Lhk41%=\n\t"
        "s_waitcnt vmcnt(0)\n\t"
        "s_barrier\n\t"
        "s_cbranch_scc1 .Le2%=\n\t"
        "s_mov_b64 exec, 1\n\t"
        "buffer_store_dword v0, off, %[wsd], %[pra] sc1\n\t"
        "s_mov_b64 exec, -1\n\t"
        ".Le2%=:\n\t"
        ".Lhk41%=:\n\t"
        "s_cbranch_execz .Lhk42%=\n\t"
        "s_endpgm\n\t"
        ".Lhk42%=:\n\t"
        "s_branch .Lhr%="
        : [nA] "+s"(nA), [nS] "+s"(nS), [nT] "+s"(nT), [nW] "+s"(nW), [nC] "+s"(nC), [b0] "+s"(b0), [pob] "+s"(pob)
        : [srd] "s"(rhd), [wsd] "s"(wsd), [w] "s"(w), [tag] "s"(tag), [kx] "s"(kx), [vx] "s"(vx), [kf] "s"(kf), [kvo] "s"(kvo), [cal] "s"(cal), [sq] "s"(sq), [sk] "s"(sk), [m0b] "s"(m0b),
          [pra] "s"(pr_a), [ferr] "i"(F_ERR), [iD1] "i"(D1), [iD2] "i"(D2), [LC] "i"(LC0), [LA] "i"(LA0), [LC24] "i"(LC0 + 24), [LA24] "i"(LA0 + 24), [E1] "i"(2 * (D1 - LC0)), [E2] "i"(2 * (D2 - LA0)),
          [iPEN] "i"(PEN), [iPEN2] "i"(PEN2), [iPENM] "i"(PEN + 2 * PEN2), [iPEN0] "i"(PEN0), [iTOL] "i"(TOL), [iCT0] "i"(CT0), [iX0C] "i"(X0C), [iX0A] "i"(X0A), [iPHM] "i"(PH_M),
          [iFT] "i"(-3), [iFN] "i"(FINE_N), [AUP] "i"(A_UP), [ASLP] "i"(A_SLP), [LDEC] "i"(L_DEC), [UPSH] "i"(UP_SH), [TRUP] "i"(TR_UP), [TRDN] "i"(TR_DN), [HSLP] "i"(H_SLP), [iPMIN] "i"(PMIN), [VSLP] "i"(V_SLP), [RSLP] "i"(R_SLP), [iVD] "i"(V_DEL), [iXR] "i"(XR), [VASL] "i"(VA_SLP), [CSLP] "i"(C_SLP), [DNSH] "i"(DN_SH), [NX0] "i"(NEARX), [NX1] "i"(8192 - NEARX), [iCS1] "i"(CS1), [iCS2] "i"(CS2), [NY0] "i"(NEARY), [NY1] "i"(16384 - NEARY),
          "{v[120:127]}"(pc), "{v[112:113]}"(pd), "{v96}"(dv), "{v[98:105]}"(pa)
        : "memory", "vcc", "scc", "m0", "s36", "s37", "s38", "s39", "s40", "s41", "s42", "s43", "s44", "s45", "s46", "s47", "s48", "s49", "s50", "s51", "s52", "s53", "s54", "s55", "s56", "s57", "s58", "s59", "s60", "s61", "s62", "s63", "s64", "s65", "s66", "s67", "s68", "s69", "s70", "s71", "s72", "s73", "s74", "s75", "s76", "s77", "s78", "s79", "s80", "s81", "s82", "s83", "s84", "s85", "s86", "s87", "s88", "s89", "s90", "s91", "s92", "s93", "s94", "s95", "s96", "s97", "s98", "s99");
    } else {
        const int cb = i32;
        const v4u sv = mk_srd(p.Wv, 1024 * 4096 * 2), so = mk_srd(p.Wo, 1024 * 4096 * 2);
        const unsigned key = KEYM & ((cb & 1) * 2 + G), tu = cb >> 2, m0b = w * 32768, f = (g & 1) | ((c >> 1) << 1);
        const unsigned t0 = m0b + (16 * (g >> 1) + 4 * (g & 1) + (c & 1) + 8 * (c >> 1)) * 128 + (f << 5) + (n16 & 3) * 8, t1 = m0b + (16 * g + 4 * (c >> 1) + (c & 1)) * 64 + ((c >> 1) << 5) + (n16 & 3) * 8;
        v8u pa;
        pa[0] = (64 * w + (l >> 3)) * 8192 + vi * 128 + (((l & 7) ^ ((l >> 5) << 1)) << 4); pa[1] = pa[0] ^ 64u;
        pa[2] = (1024 * w + (l >> 2)) * 2048 + cb * 64 + (((l & 3) ^ ((g & 1) << 1)) << 4);
#pragma unroll
        for (int q = 0; q < 4; ++q) pa[3 + q] = t0 ^ (((w + q) & 3) << 5);
        pa[7] = t1;
        const v2u pb = {t1 ^ 32u, (unsigned)((w * 1024 + cb * 32) * 2048 + l * 1024) + (((3u - (so.x >> 7)) & 7u) << 7)};
        const v16u pc = {(unsigned)tid * 16, (n16 & 2) ? 0xEEEEu : 0x4444u, (unsigned)((vi & 1) * 1024 + w * 256 + n16 * 16 + g * 4), (unsigned)(4096 + tid * 16),
                   w < 3 ? (unsigned)((cb >> 4) * 32768 + ((cb >> 1) & 7) * 1024 + ((cb & 1) * 2 + (l >> 5)) * 256 + G * 128 + (l & 31) * 4) : OOB,
                   tid < 3 ? (unsigned)(l * 1024 + (32 + vi) * 8) : OOB, tid == 192 ? pr_a : OOB, (unsigned)(L_V + 8192 + w * 512 + (g & 1) * 256 + (g >> 1) * 64 + n16 * 4),
                   (unsigned)(L_V + (((l >> 2) & 1) + ((l >> 3) & 2)) * 128 + (l >> 5) * 64 + (l & 8) * 4 + (l & 3) * 8), 0u,
                   (unsigned)(L_V + w * 2048 + l * 8), (unsigned)(L_V + ((w + 1) & 3) * 2048 + 512 + l * 8), (unsigned)(L_V + ((w + 2) & 3) * 2048 + 1024 + l * 8), (unsigned)(L_V + ((w + 3) & 3) * 2048 + 1536 + l * 8), 0u, 0u};
        if (tid < 2) ((lu32*)(size_t)(L_V + L_FL))[tid] = ~0u;
    asm volatile(
        "s_mov_b32 s82, 3\n\t"
        ".Ld0%=:\n\t"
        "s_xor_b32 s80, s82, %[key]\n\t"
        "s_mul_i32 s81, s80, 131072\n\t"
        "s_lshl_b32 s80, s80, 11\n\t"
        "s_add_u32 s80, s80, %[m0b]\n\t"
        "s_add_u32 m0, s80, 0\n\t"
        "s_add_u32 s81, s81, 0\n\t"
        "buffer_load_dwordx4 v96, %[sv], s81 offen lds\n\t"
        "s_add_u32 m0, m0, 0x400\n\t"
        "s_add_u32 s81, s81, 65536\n\t"
        "buffer_load_dwordx4 v97, %[sv], s81 offen lds\n\t"
        "s_sub_u32 s82, s82, 1\n\t"
        "s_cbranch_scc0 .Ld0%=\n\t"
        "s_mov_b32 s82, 3\n\t"
        ".Ld1%=:\n\t"
        "s_xor_b32 s80, s82, %[key]\n\t"
        "s_mul_i32 s81, s80, 131072\n\t"
        "s_lshl_b32 s80, s80, 11\n\t"
        "s_add_u32 s80, s80, %[m0b]\n\t"
        "s_add_u32 m0, s80, 8192\n\t"
        "s_add_u32 s81, s81, 2097152\n\t"
        "buffer_load_dwordx4 v96, %[sv], s81 offen lds\n\t"
        "s_add_u32 m0, m0, 0x400\n\t"
        "s_add_u32 s81, s81, 65536\n\t"
        "buffer_load_dwordx4 v97, %[sv], s81 offen lds\n\t"
        "s_sub_u32 s82, s82, 1\n\t"
        "s_cbranch_scc0 .Ld1%=\n\t"
        "s_cmp_eq_u32 %[tu], 0\n\t"
        "s_cselect_b32 s81, 0, 0x7fff0000\n\t"
        "buffer_load_dword v110, v105, %[so], s81 offen\n\t"
        "s_mov_b32 s82, 3\n\t"
        ".Ld2%=:\n\t"
        "s_xor_b32 s80, s82, %[key]\n\t"
        "s_mul_i32 s81, s80, 131072\n\t"
        "s_lshl_b32 s80, s80, 11\n\t"
        "s_add_u32 s80, s80, %[m0b]\n\t"
        "s_add_u32 m0, s80, 16384\n\t"
        "s_add_u32 s81, s81, 4194304\n\t"
        "buffer_load_dwordx4 v96, %[sv], s81 offen lds\n\t"
        "s_add_u32 m0, m0, 0x400\n\t"
        "s_add_u32 s81, s81, 65536\n\t"
        "buffer_load_dwordx4 v97, %[sv], s81 offen lds\n\t"
        "s_sub_u32 s82, s82, 1\n\t"
        "s_cbranch_scc0 .Ld2%=\n\t"
        "s_cmp_eq_u32 %[tu], 1\n\t"
        "s_cselect_b32 s81, 0, 0x7fff0000\n\t"
        "buffer_load_dword v110, v105, %[so], s81 offen\n\t"
        "s_mov_b32 s82, 3\n\t"
        ".Ld3%=:\n\t"
        "s_xor_b32 s80, s82, %[key]\n\t"
        "s_mul_i32 s81, s80, 131072\n\t"
        "s_lshl_b32 s80, s80, 11\n\t"
        "s_add_u32 s80, s80, %[m0b]\n\t"
        "s_add_u32 m0, s80, 24576\n\t"
        "s_add_u32 s81, s81, 6291456\n\t"
        "buffer_load_dwordx4 v96, %[sv], s81 offen lds\n\t"
        "s_add_u32 m0, m0, 0x400\n\t"
        "s_add_u32 s81, s81, 65536\n\t"
        "buffer_load_dwordx4 v97, %[sv], s81 offen lds\n\t"
        "s_sub_u32 s82, s82, 1\n\t"
        "s_cbranch_scc0 .Ld3%=\n\t"
        "s_waitcnt vmcnt(26)\n\t"
        "ds_read_b64_tr_b16 v[128:129], v99 offset:0\n\t"
        "ds_read_b64_tr_b16 v[130:131], v99 offset:256\n\t"
        "ds_read_b64_tr_b16 v[132:133], v99 offset:4096\n\t"
        "ds_read_b64_tr_b16 v[134:135], v99 offset:4352\n\t"
        "ds_read_b64_tr_b16 v[136:137], v100 offset:0\n\t"
        "ds_read_b64_tr_b16 v[138:139], v100 offset:256\n\t"
        "ds_read_b64_tr_b16 v[140:141], v100 offset:4096\n\t"
        "ds_read_b64_tr_b16 v[142:143], v100 offset:4352\n\t"
        "ds_read_b64_tr_b16 v[144:145], v101 offset:0\n\t"
        "ds_read_b64_tr_b16 v[146:147], v101 offset:256\n\t"
        "ds_read_b64_tr_b16 v[148:149], v101 offset:4096\n\t"
        "ds_read_b64_tr_b16 v[150:151], v101 offset:4352\n\t"
        "ds_read_b64_tr_b16 v[152:153], v102 offset:0\n\t"
        "ds_read_b64_tr_b16 v[154:155], v102 offset:256\n\t"
        "ds_read_b64_tr_b16 v[156:157], v102 offset:4096\n\t"
        "ds_read_b64_tr_b16 v[158:159], v102 offset:4352\n\t"
        "s_waitcnt lgkmcnt(0)\n\t"
        "s_cmp_eq_u32 %[tu], 2\n\t"
        "s_cselect_b32 s81, 0, 0x7fff0000\n\t"
        "buffer_load_dword v110, v105, %[so], s81 offen\n\t"
        "s_mov_b32 s82, 3\n\t"
        ".Ld4%=:\n\t"
        "s_xor_b32 s80, s82, %[key]\n\t"
        "s_mul_i32 s81, s80, 65536\n\t"
        "s_lshl_b32 s80, s80, 11\n\t"
        "s_add_u32 s80, s80, %[m0b]\n\t"
        "s_add_u32 m0, s80, 0\n\t"
        "s_add_u32 s81, s81, 0\n\t"
        "buffer_load_dwordx4 v98, %[so], s81 offen lds\n\t"
        "s_add_u32 m0, m0, 0x400\n\t"
        "s_add_u32 s81, s81, 32768\n\t"
        "buffer_load_dwordx4 v98, %[so], s81 offen lds\n\t"
        "s_sub_u32 s82, s82, 1\n\t"
        "s_cbranch_scc0 .Ld4%=\n\t"
        "s_waitcnt vmcnt(27)\n\t"
        "ds_read_b64_tr_b16 v[160:161], v99 offset:8192\n\t"
        "ds_read_b64_tr_b16 v[162:163], v99 offset:8448\n\t"
        "ds_read_b64_tr_b16 v[164:165], v99 offset:12288\n\t"
        "ds_read_b64_tr_b16 v[166:167], v99 offset:12544\n\t"
        "ds_read_b64_tr_b16 v[168:169], v100 offset:8192\n\t"
        "ds_read_b64_tr_b16 v[170:171], v100 offset:8448\n\t"
        "ds_read_b64_tr_b16 v[172:173], v100 offset:12288\n\t"
        "ds_read_b64_tr_b16 v[174:175], v100 offset:12544\n\t"
        "ds_read_b64_tr_b16 v[176:177], v101 offset:8192\n\t"
        "ds_read_b64_tr_b16 v[178:179], v101 offset:8448\n\t"
        "ds_read_b64_tr_b16 v[180:181], v101 offset:12288\n\t"
        "ds_read_b64_tr_b16 v[182:183], v101 offset:12544\n\t"
        "ds_read_b64_tr_b16 v[184:185], v102 offset:8192\n\t"
        "ds_read_b64_tr_b16 v[186:187], v102 offset:8448\n\t"
        "ds_read_b64_tr_b16 v[188:189], v102 offset:12288\n\t"
        "ds_read_b64_tr_b16 v[190:191], v102 offset:12544\n\t"
        "s_waitcnt lgkmcnt(0)\n\t"
        "s_cmp_eq_u32 %[tu], 3\n\t"
        "s_cselect_b32 s81, 0, 0x7fff0000\n\t"
        "buffer_load_dword v110, v105, %[so], s81 offen\n\t"
        "s_mov_b32 s82, 3\n\t"
        ".Ld5%=:\n\t"
        "s_xor_b32 s80, s82, %[key]\n\t"
        "s_mul_i32 s81, s80, 65536\n\t"
        "s_lshl_b32 s80, s80, 11\n\t"
        "s_add_u32 s80, s80, %[m0b]\n\t"
        "s_add_u32 m0, s80, 8192\n\t"
        "s_add_u32 s81, s81, 262144\n\t"
        "buffer_load_dwordx4 v98, %[so], s81 offen lds\n\t"
        "s_add_u32 m0, m0, 0x400\n\t"
        "s_add_u32 s81, s81, 32768\n\t"
        "buffer_load_dwordx4 v98, %[so], s81 offen lds\n\t"
        "s_sub_u32 s82, s82, 1\n\t"
        "s_cbranch_scc0 .Ld5%=\n\t"
        "s_waitcnt vmcnt(27)\n\t"
        "ds_read_b64_tr_b16 v[192:193], v99 offset:16384\n\t"
        "ds_read_b64_tr_b16 v[194:195], v99 offset:16640\n\t"
        "ds_read_b64_tr_b16 v[196:197], v99 offset:20480\n\t"
        "ds_read_b64_tr_b16 v[198:199], v99 offset:20736\n\t"
        "ds_read_b64_tr_b16 v[200:201], v100 offset:16384\n\t"
        "ds_read_b64_tr_b16 v[202:203], v100 offset:16640\n\t"
        "ds_read_b64_tr_b16 v[204:205], v100 offset:20480\n\t"
        "ds_read_b64_tr_b16 v[206:207], v100 offset:20736\n\t"
        "ds_read_b64_tr_b16 v[208:209], v101 offset:16384\n\t"
        "ds_read_b64_tr_b16 v[210:211], v101 offset:16640\n\t"
        "ds_read_b64_tr_b16 v[212:213], v101 offset:20480\n\t"
        "ds_read_b64_tr_b16 v[214:215], v101 offset:20736\n\t"
        "ds_read_b64_tr_b16 v[216:217], v102 offset:16384\n\t"
        "ds_read_b64_tr_b16 v[218:219], v102 offset:16640\n\t"
        "ds_read_b64_tr_b16 v[220:221], v102 offset:20480\n\t"
        "ds_read_b64_tr_b16 v[222:223], v102 offset:20736\n\t"
        "s_waitcnt lgkmcnt(0)\n\t"
        "s_cmp_eq_u32 %[tu], 4\n\t"
        "s_cselect_b32 s81, 0, 0x7fff0000\n\t"
        "buffer_load_dword v110, v105, %[so], s81 offen\n\t"
        "s_mov_b32 s82, 3\n\t"
        ".Ld6%=:\n\t"
        "s_xor_b32 s80, s82, %[key]\n\t"
        "s_mul_i32 s81, s80, 65536\n\t"
        "s_lshl_b32 s80, s80, 11\n\t"
        "s_add_u32 s80, s80, %[m0b]\n\t"
        "s_add_u32 m0, s80, 16384\n\t"
        "s_add_u32 s81, s81, 524288\n\t"
        "buffer_load_dwordx4 v98, %[so], s81 offen lds\n\t"
        "s_add_u32 m0, m0, 0x400\n\t"
        "s_add_u32 s81, s81, 32768\n\t"
        "buffer_load_dwordx4 v98, %[so], s81 offen lds\n\t"
        "s_sub_u32 s82, s82, 1\n\t"
        "s_cbranch_scc0 .Ld6%=\n\t"
        "s_waitcnt vmcnt(27)\n\t"
        "ds_read_b64_tr_b16 v[224:225], v99 offset:24576\n\t"
        "ds_read_b64_tr_b16 v[226:227], v99 offset:24832\n\t"
        "ds_read_b64_tr_b16 v[228:229], v99 offset:28672\n\t"
        "ds_read_b64_tr_b16 v[230:231], v99 offset:28928\n\t"
        "ds_read_b64_tr_b16 v[232:233], v100 offset:24576\n\t"
        "ds_read_b64_tr_b16 v[234:235], v100 offset:24832\n\t"
        "ds_read_b64_tr_b16 v[236:237], v100 offset:28672\n\t"
        "ds_read_b64_tr_b16 v[238:239], v100 offset:28928\n\t"
        "ds_read_b64_tr_b16 v[240:241], v101 offset:24576\n\t"
        "ds_read_b64_tr_b16 v[242:243], v101 offset:24832\n\t"
        "ds_read_b64_tr_b16 v[244:245], v101 offset:28672\n\t"
        "ds_read_b64_tr_b16 v[246:247], v101 offset:28928\n\t"
        "ds_read_b64_tr_b16 v[248:249], v102 offset:24576\n\t"
        "ds_read_b64_tr_b16 v[250:251], v102 offset:24832\n\t"
        "ds_read_b64_tr_b16 v[252:253], v102 offset:28672\n\t"
        "ds_read_b64_tr_b16 v[254:255], v102 offset:28928\n\t"
        "s_waitcnt lgkmcnt(0)\n\t"
        "s_cmp_eq_u32 %[tu], 5\n\t"
        "s_cselect_b32 s81, 0, 0x7fff0000\n\t"
        "buffer_load_dword v110, v105, %[so], s81 offen\n\t"
        "s_mov_b32 s82, 3\n\t"
        ".Ld7%=:\n\t"
        "s_xor_b32 s80, s82, %[key]\n\t"
        "s_mul_i32 s81, s80, 65536\n\t"
        "s_lshl_b32 s80, s80, 11\n\t"
        "s_add_u32 s80, s80, %[m0b]\n\t"
        "s_add_u32 m0, s80, 24576\n\t"
        "s_add_u32 s81, s81, 786432\n\t"
        "buffer_load_dwordx4 v98, %[so], s81 offen lds\n\t"
        "s_add_u32 m0, m0, 0x400\n\t"
        "s_add_u32 s81, s81, 32768\n\t"
        "buffer_load_dwordx4 v98, %[so], s81 offen lds\n\t"
        "s_sub_u32 s82, s82, 1\n\t"
        "s_cbranch_scc0 .Ld7%=\n\t"
        "s_mov_b64 exec, 0\n\t"
        "s_branch .Lhk0%=\n\t"
        ".Lhr%=:\n\t"
        "s_mov_b64 exec, -1\n\t"
        "s_waitcnt vmcnt(27)\n\t"
        "ds_read_b64_tr_b16 a[0:1], v103 offset:0\n\t"
        "ds_read_b64_tr_b16 a[2:3], v103 offset:128\n\t"
        "ds_read_b64_tr_b16 a[4:5], v103 offset:4096\n\t"
        "ds_read_b64_tr_b16 a[6:7], v103 offset:4224\n\t"
        "ds_read_b64_tr_b16 a[8:9], v104 offset:0\n\t"
        "ds_read_b64_tr_b16 a[10:11], v104 offset:128\n\t"
        "ds_read_b64_tr_b16 a[12:13], v104 offset:4096\n\t"
        "ds_read_b64_tr_b16 a[14:15], v104 offset:4224\n\t"
        "ds_read_b64_tr_b16 a[16:17], v103 offset:512\n\t"
        "ds_read_b64_tr_b16 a[18:19], v103 offset:640\n\t"
        "ds_read_b64_tr_b16 a[20:21], v103 offset:4608\n\t"
        "ds_read_b64_tr_b16 a[22:23], v103 offset:4736\n\t"
        "ds_read_b64_tr_b16 a[24:25], v104 offset:512\n\t"
        "ds_read_b64_tr_b16 a[26:27], v104 offset:640\n\t"
        "ds_read_b64_tr_b16 a[28:29], v104 offset:4608\n\t"
        "ds_read_b64_tr_b16 a[30:31], v104 offset:4736\n\t"
        "s_waitcnt lgkmcnt(0)\n\t"
        "s_cmp_eq_u32 %[tu], 6\n\t"
        "s_cselect_b32 s81, 0, 0x7fff0000\n\t"
        "buffer_load_dword v110, v105, %[so], s81 offen\n\t"
        "s_mov_b32 s82, 3\n\t"
        ".Ld8%=:\n\t"
        "s_xor_b32 s80, s82, %[key]\n\t"
        "s_mul_i32 s81, s80, 65536\n\t"
        "s_lshl_b32 s80, s80, 11\n\t"
        "s_add_u32 s80, s80, %[m0b]\n\t"
        "s_add_u32 m0, s80, 0\n\t"
        "s_add_u32 s81, s81, 1048576\n\t"
        "buffer_load_dwordx4 v98, %[so], s81 offen lds\n\t"
        "s_add_u32 m0, m0, 0x400\n\t"
        "s_add_u32 s81, s81, 32768\n\t"
        "buffer_load_dwordx4 v98, %[so], s81 offen lds\n\t"
        "s_sub_u32 s82, s82, 1\n\t"
        "s_cbranch_scc0 .Ld8%=\n\t"
        "s_waitcnt vmcnt(27)\n\t"
        "ds_read_b64_tr_b16 a[32:33], v103 offset:8192\n\t"
        "ds_read_b64_tr_b16 a[34:35], v103 offset:8320\n\t"
        "ds_read_b64_tr_b16 a[36:37], v103 offset:12288\n\t"
        "ds_read_b64_tr_b16 a[38:39], v103 offset:12416\n\t"
        "ds_read_b64_tr_b16 a[40:41], v104 offset:8192\n\t"
        "ds_read_b64_tr_b16 a[42:43], v104 offset:8320\n\t"
        "ds_read_b64_tr_b16 a[44:45], v104 offset:12288\n\t"
        "ds_read_b64_tr_b16 a[46:47], v104 offset:12416\n\t"
        "ds_read_b64_tr_b16 a[48:49], v103 offset:8704\n\t"
        "ds_read_b64_tr_b16 a[50:51], v103 offset:8832\n\t"
        "ds_read_b64_tr_b16 a[52:53], v103 offset:12800\n\t"
        "ds_read_b64_tr_b16 a[54:55], v103 offset:12928\n\t"
        "ds_read_b64_tr_b16 a[56:57], v104 offset:8704\n\t"
        "ds_read_b64_tr_b16 a[58:59], v104 offset:8832\n\t"
        "ds_read_b64_tr_b16 a[60:61], v104 offset:12800\n\t"
        "ds_read_b64_tr_b16 a[62:63], v104 offset:12928\n\t"
        "s_waitcnt lgkmcnt(0)\n\t"
        "s_cmp_eq_u32 %[tu], 7\n\t"
        "s_cselect_b32 s81, 0, 0x7fff0000\n\t"
        "buffer_load_dword v110, v105, %[so], s81 offen\n\t"
        "s_mov_b32 s82, 3\n\t"
        ".Ld9%=:\n\t"
        "s_xor_b32 s80, s82, %[key]\n\t"
        "s_mul_i32 s81, s80, 65536\n\t"
        "s_lshl_b32 s80, s80, 11\n\t"
        "s_add_u32 s80, s80, %[m0b]\n\t"
        "s_add_u32 m0, s80, 8192\n\t"
        "s_add_u32 s81, s81, 1310720\n\t"
        "buffer_load_dwordx4 v98, %[so], s81 offen lds\n\t"
        "s_add_u32 m0, m0, 0x400\n\t"
        "s_add_u32 s81, s81, 32768\n\t"
        "buffer_load_dwordx4 v98, %[so], s81 offen lds\n\t"
        "s_sub_u32 s82, s82, 1\n\t"
        "s_cbranch_scc0 .Ld9%=\n\t"
        "s_waitcnt vmcnt(27)\n\t"
        "ds_read_b64_tr_b16 a[64:65], v103 offset:16384\n\t"
        "ds_read_b64_tr_b16 a[66:67], v103 offset:16512\n\t"
        "ds_read_b64_tr_b16 a[68:69], v103 offset:20480\n\t"
        "ds_read_b64_tr_b16 a[70:71], v103 offset:20608\n\t"
        "ds_read_b64_tr_b16 a[72:73], v104 offset:16384\n\t"
        "ds_read_b64_tr_b16 a[74:75], v104 offset:16512\n\t"
        "ds_read_b64_tr_b16 a[76:77], v104 offset:20480\n\t"
        "ds_read_b64_tr_b16 a[78:79], v104 offset:20608\n\t"
        "ds_read_b64_tr_b16 a[80:81], v103 offset:16896\n\t"
        "ds_read_b64_tr_b16 a[82:83], v103 offset:17024\n\t"
        "ds_read_b64_tr_b16 a[84:85], v103 offset:20992\n\t"
        "ds_read_b64_tr_b16 a[86:87], v103 offset:21120\n\t"
        "ds_read_b64_tr_b16 a[88:89], v104 offset:16896\n\t"
        "ds_read_b64_tr_b16 a[90:91], v104 offset:17024\n\t"
        "ds_read_b64_tr_b16 a[92:93], v104 offset:20992\n\t"
        "ds_read_b64_tr_b16 a[94:95], v104 offset:21120\n\t"
        "buffer_load_dwordx4 v[0:3], v112, %[wsd], %[cal] offen sc1\n\t"
        "s_waitcnt lgkmcnt(0)\n\t"
        "s_mov_b32 s82, 3\n\t"
        ".Ld10%=:\n\t"
        "s_xor_b32 s80, s82, %[key]\n\t"
        "s_mul_i32 s81, s80, 65536\n\t"
        "s_lshl_b32 s80, s80, 11\n\t"
        "s_add_u32 s80, s80, %[m0b]\n\t"
        "s_add_u32 m0, s80, 16384\n\t"
        "s_add_u32 s81, s81, 1572864\n\t"
        "buffer_load_dwordx4 v98, %[so], s81 offen lds\n\t"
        "s_add_u32 m0, m0, 0x400\n\t"
        "s_add_u32 s81, s81, 32768\n\t"
        "buffer_load_dwordx4 v98, %[so], s81 offen lds\n\t"
        "s_sub_u32 s82, s82, 1\n\t"
        "s_cbranch_scc0 .Ld10%=\n\t"
        "s_waitcnt vmcnt(27)\n\t"
        "ds_read_b64_tr_b16 a[96:97], v103 offset:24576\n\t"
        "ds_read_b64_tr_b16 a[98:99], v103 offset:24704\n\t"
        "ds_read_b64_tr_b16 a[100:101], v103 offset:28672\n\t"
        "ds_read_b64_tr_b16 a[102:103], v103 offset:28800\n\t"
        "ds_read_b64_tr_b16 a[104:105], v104 offset:24576\n\t"
        "ds_read_b64_tr_b16 a[106:107], v104 offset:24704\n\t"
        "ds_read_b64_tr_b16 a[108:109], v104 offset:28672\n\t"
        "ds_read_b64_tr_b16 a[110:111], v104 offset:28800\n\t"
        "ds_read_b64_tr_b16 a[112:113], v103 offset:25088\n\t"
        "ds_read_b64_tr_b16 a[114:115], v103 offset:25216\n\t"
        "ds_read_b64_tr_b16 a[116:117], v103 offset:29184\n\t"
        "ds_read_b64_tr_b16 a[118:119], v103 offset:29312\n\t"
        "ds_read_b64_tr_b16 a[120:121], v104 offset:25088\n\t"
        "ds_read_b64_tr_b16 a[122:123], v104 offset:25216\n\t"
        "ds_read_b64_tr_b16 a[124:125], v104 offset:29184\n\t"
        "ds_read_b64_tr_b16 a[126:127], v104 offset:29312\n\t"
        "s_waitcnt lgkmcnt(0)\n\t"
        "s_mov_b32 s82, 3\n\t"
        ".Ld11%=:\n\t"
        "s_xor_b32 s80, s82, %[key]\n\t"
        "s_mul_i32 s81, s80, 65536\n\t"
        "s_lshl_b32 s80, s80, 11\n\t"
        "s_add_u32 s80, s80, %[m0b]\n\t"
        "s_add_u32 m0, s80, 24576\n\t"
        "s_add_u32 s81, s81, 1835008\n\t"
        "buffer_load_dwordx4 v98, %[so], s81 offen lds\n\t"
        "s_add_u32 m0, m0, 0x400\n\t"
        "s_add_u32 s81, s81, 32768\n\t"
        "buffer_load_dwordx4 v98, %[so], s81 offen lds\n\t"
        "s_sub_u32 s82, s82, 1\n\t"
        "s_cbranch_scc0 .Ld11%=\n\t"
        "s_waitcnt vmcnt(26)\n\t"
        "ds_read_b64_tr_b16 a[128:129], v103 offset:0\n\t"
        "ds_read_b64_tr_b16 a[130:131], v103 offset:128\n\t"
        "ds_read_b64_tr_b16 a[132:133], v103 offset:4096\n\t"
        "ds_read_b64_tr_b16 a[134:135], v103 offset:4224\n\t"
        "ds_read_b64_tr_b16 a[136:137], v104 offset:0\n\t"
        "ds_read_b64_tr_b16 a[138:139], v104 offset:128\n\t"
        "ds_read_b64_tr_b16 a[140:141], v104 offset:4096\n\t"
        "ds_read_b64_tr_b16 a[142:143], v104 offset:4224\n\t"
        "ds_read_b64_tr_b16 a[144:145], v103 offset:512\n\t"
        "ds_read_b64_tr_b16 a[146:147], v103 offset:640\n\t"
        "ds_read_b64_tr_b16 a[148:149], v103 offset:4608\n\t"
        "ds_read_b64_tr_b16 a[150:151], v103 offset:4736\n\t"
        "ds_read_b64_tr_b16 a[152:153], v104 offset:512\n\t"
        "ds_read_b64_tr_b16 a[154:155], v104 offset:640\n\t"
        "ds_read_b64_tr_b16 a[156:157], v104 offset:4608\n\t"
        "ds_read_b64_tr_b16 a[158:159], v104 offset:4736\n\t"
        "s_waitcnt lgkmcnt(0)\n\t"
        "s_waitcnt vmcnt(17)\n\t"
        "ds_read_b64_tr_b16 a[160:161], v103 offset:8192\n\t"
        "ds_read_b64_tr_b16 a[162:163], v103 offset:8320\n\t"
        "ds_read_b64_tr_b16 a[164:165], v103 offset:12288\n\t"
        "ds_read_b64_tr_b16 a[166:167], v103 offset:12416\n\t"
        "ds_read_b64_tr_b16 a[168:169], v104 offset:8192\n\t"
        "ds_read_b64_tr_b16 a[170:171], v104 offset:8320\n\t"
        "ds_read_b64_tr_b16 a[172:173], v104 offset:12288\n\t"
        "ds_read_b64_tr_b16 a[174:175], v104 offset:12416\n\t"
        "ds_read_b64_tr_b16 a[176:177], v103 offset:8704\n\t"
        "ds_read_b64_tr_b16 a[178:179], v103 offset:8832\n\t"
        "ds_read_b64_tr_b16 a[180:181], v103 offset:12800\n\t"
        "ds_read_b64_tr_b16 a[182:183], v103 offset:12928\n\t"
        "ds_read_b64_tr_b16 a[184:185], v104 offset:8704\n\t"
        "ds_read_b64_tr_b16 a[186:187], v104 offset:8832\n\t"
        "ds_read_b64_tr_b16 a[188:189], v104 offset:12800\n\t"
        "ds_read_b64_tr_b16 a[190:191], v104 offset:12928\n\t"
        "s_waitcnt lgkmcnt(0)\n\t"
        "s_waitcnt vmcnt(8)\n\t"
        "ds_read_b64_tr_b16 a[192:193], v103 offset:16384\n\t"
        "ds_read_b64_tr_b16 a[194:195], v103 offset:16512\n\t"
        "ds_read_b64_tr_b16 a[196:197], v103 offset:20480\n\t"
        "ds_read_b64_tr_b16 a[198:199], v103 offset:20608\n\t"
        "ds_read_b64_tr_b16 a[200:201], v104 offset:16384\n\t"
        "ds_read_b64_tr_b16 a[202:203], v104 offset:16512\n\t"
        "ds_read_b64_tr_b16 a[204:205], v104 offset:20480\n\t"
        "ds_read_b64_tr_b16 a[206:207], v104 offset:20608\n\t"
        "ds_read_b64_tr_b16 a[208:209], v103 offset:16896\n\t"
        "ds_read_b64_tr_b16 a[210:211], v103 offset:17024\n\t"
        "ds_read_b64_tr_b16 a[212:213], v103 offset:20992\n\t"
        "ds_read_b64_tr_b16 a[214:215], v103 offset:21120\n\t"
        "ds_read_b64_tr_b16 a[216:217], v104 offset:16896\n\t"
        "ds_read_b64_tr_b16 a[218:219], v104 offset:17024\n\t"
        "ds_read_b64_tr_b16 a[220:221], v104 offset:20992\n\t"
        "ds_read_b64_tr_b16 a[222:223], v104 offset:21120\n\t"
        "s_mov_b32 s97, 0\n\t"
        ".Lg0%=:\n\t"
        "v_cmp_ne_u32_e32 vcc, %[tag], v0\n\t"
        "v_cmp_ne_u32_e64 s[82:83], %[tag], v2\n\t"
        "s_nop 1\n\t"
        "s_or_b64 vcc, vcc, s[82:83]\n\t"
        "s_cbranch_scc0 .Lg1%=\n\t"
        "s_sleep 4\n\t"
        "buffer_load_dwordx4 v[0:3], v112, %[wsd], %[cal] offen sc1\n\t"
        "s_add_u32 s97, s97, 1\n\t"
        "s_cmp_lt_u32 s97, 0x100000\n\t"
        "s_cbranch_scc0 .Lgu%=\n\t"
        "s_waitcnt vmcnt(0)\n\t"
        "s_branch .Lg0%=\n\t"
        ".Lg1%=:\n\t"
        "s_mov_b32 s84, 0x55555555\n\t"
        "s_mov_b32 s85, s84\n\t"
        "v_and_b32_e32 v0, 1, v1\n\t"
        "v_and_b32_e32 v2, 1, v3\n\t"
        "v_cmp_ne_u32_e32 vcc, 0, v0\n\t"
        "v_cmp_ne_u32_e64 s[82:83], 0, v2\n\t"
        "s_nop 0\n\t"
        "s_and_b64 s[82:83], s[82:83], s[84:85]\n\t"
        "s_bcnt1_i32_b64 s87, s[82:83]\n\t"
        "s_and_b64 s[82:83], vcc, s[84:85]\n\t"
        "s_bcnt1_i32_b64 s86, s[82:83]\n\t"
        "s_andn2_b64 s[82:83], vcc, s[84:85]\n\t"
        "s_bcnt1_i32_b64 s88, s[82:83]\n\t"
        "v_and_b32_e32 v0, 2, v1\n\t"
        "v_and_b32_e32 v2, 2, v3\n\t"
        "v_cmp_ne_u32_e32 vcc, 0, v0\n\t"
        "v_cmp_ne_u32_e64 s[82:83], 0, v2\n\t"
        "s_nop 0\n\t"
        "s_and_b64 s[82:83], s[82:83], s[84:85]\n\t"
        "s_bcnt1_i32_b64 s90, s[82:83]\n\t"
        "s_and_b64 s[82:83], vcc, s[84:85]\n\t"
        "s_bcnt1_i32_b64 s89, s[82:83]\n\t"
        "s_andn2_b64 s[82:83], vcc, s[84:85]\n\t"
        "s_bcnt1_i32_b64 s91, s[82:83]\n\t"
        "s_add_u32 s80, s86, s87\n\t"
        "s_add_u32 s80, s80, s88\n\t"
        "s_cmp_ge_u32 s80, 48\n\t"
        "s_cselect_b32 s80, 8, 24\n\t"
        "s_cmp_gt_u32 s86, s80\n\t"
        "s_cselect_b32 s86, %c[NX1], %c[NX0]\n\t"
        "s_cmp_gt_u32 s89, 16\n\t"
        "s_cselect_b32 s81, %c[NY1], %c[NY0]\n\t"
        "s_or_b32 s86, s86, s81\n\t"
        "s_cmp_gt_u32 s87, s80\n\t"
        "s_cselect_b32 s87, %c[NX1], %c[NX0]\n\t"
        "s_cmp_gt_u32 s90, 16\n\t"
        "s_cselect_b32 s81, %c[NY1], %c[NY0]\n\t"
        "s_or_b32 s87, s87, s81\n\t"
        "s_cmp_gt_u32 s88, s80\n\t"
        "s_cselect_b32 s88, %c[NX1], %c[NX0]\n\t"
        "s_cmp_gt_u32 s91, 16\n\t"
        "s_cselect_b32 s81, %c[NY1], %c[NY0]\n\t"
        "s_or_b32 s88, s88, s81\n\t"
        "s_xor_b32 %[nA], %[nA], s86\n\t"
        "s_xor_b32 %[nW], %[nW], s86\n\t"
        "s_xor_b32 %[nC], %[nC], s87\n\t"
        "s_xor_b32 %[nS], %[nS], s88\n\t"
        "s_xor_b32 %[nT], %[nT], s88\n\t"
        "s_xor_b32 %[b0], %[b0], s88\n\t"
        "s_cmp_eq_u32 %[w], 1\n\t"
        "s_cselect_b32 s80, s87, s86\n\t"
        "s_cmp_eq_u32 %[w], 2\n\t"
        "s_cselect_b32 s80, s88, s80\n\t"
        "s_xor_b32 %[pob], %[pob], s80\n\t"
        "s_lshl_b32 s80, %[w], 10\n\t"
        "s_sub_u32 %[nS], %[nS], s80\n\t"
        "s_sub_u32 %[pob], %[pob], s80\n\t"
        "s_mov_b32 s36, 0xf0000000\n\t"
        "s_mov_b32 s38, 0xff00ff00\n\t"
        "s_mov_b32 s39, 0xff00ff00\n\t"
        "s_mov_b32 s40, 0\n\t"
        "s_mov_b32 s37, 0\n\t"
        "s_brev_b32 s54, 1\n\t"
        "s_mov_b32 s46, %c[iD1]\n\t"
        "s_mov_b32 s45, %c[iD1]\n\t"
        "s_mov_b32 s47, %c[iD2]\n\t"
        "s_mov_b32 s48, 1\n\t"
        "s_mov_b32 s50, %c[LC]\n\t"
        "s_mov_b32 s51, %c[LA]\n\t"
        "s_mov_b32 s52, %c[E1]\n\t"
        "s_mov_b32 s53, %c[E2]\n\t"
        "s_mov_b32 s43, 0\n\t"
        "s_mov_b32 s61, %[b0]\n\t"
        "s_add_u32 s62, %[b0], 0x40000\n\t"
        "s_mov_b32 s75, 162816\n\t"
        "s_waitcnt lgkmcnt(0)\n\t"
        "s_waitcnt vmcnt(0)\n\t"
        "ds_read_b64_tr_b16 a[224:225], v103 offset:24576\n\t"
        "ds_read_b64_tr_b16 a[226:227], v103 offset:24704\n\t"
        "ds_read_b64_tr_b16 a[228:229], v103 offset:28672\n\t"
        "ds_read_b64_tr_b16 a[230:231], v103 offset:28800\n\t"
        "ds_read_b64_tr_b16 a[232:233], v104 offset:24576\n\t"
        "ds_read_b64_tr_b16 a[234:235], v104 offset:24704\n\t"
        "ds_read_b64_tr_b16 a[236:237], v104 offset:28672\n\t"
        "ds_read_b64_tr_b16 a[238:239], v104 offset:28800\n\t"
        "ds_read_b64_tr_b16 a[240:241], v103 offset:25088\n\t"
        "ds_read_b64_tr_b16 a[242:243], v103 offset:25216\n\t"
        "ds_read_b64_tr_b16 a[244:245], v103 offset:29184\n\t"
        "ds_read_b64_tr_b16 a[246:247], v103 offset:29312\n\t"
        "ds_read_b64_tr_b16 a[248:249], v104 offset:25088\n\t"
        "ds_read_b64_tr_b16 a[250:251], v104 offset:25216\n\t"
        "ds_read_b64_tr_b16 a[252:253], v104 offset:29184\n\t"
        "ds_read_b64_tr_b16 a[254:255], v104 offset:29312\n\t"
        "s_waitcnt lgkmcnt(0)\n\t"
        "rec_v_code:\n\t"
        "s_and_b32 s80, s40, 3\n\t"
        "s_lshl_b32 s80, s80, 17\n\t"
        "s_add_u32 s57, s80, %[nA]\n\t"
        "s_add_u32 s58, s57, 0x1000\n\t"
        "s_add_u32 s59, s57, 0x8000\n\t"
        "s_add_u32 s60, s57, 0x9000\n\t"
        "v_mov_b32_e32 v92, -1\n\t"
        "v_mov_b32_e32 v93, -1\n\t"
        "v_mov_b32_e32 v94, -1\n\t"
        "v_mov_b32_e32 v95, -1\n\t"
        "v_mov_b32_e32 v48, 0\n\t"
        "v_mov_b32_e32 v49, 0\n\t"
        "v_mov_b32_e32 v50, 0\n\t"
        "v_mov_b32_e32 v51, 0\n\t"
        "v_mov_b32_e32 v52, 0\n\t"
        "v_mov_b32_e32 v53, 0\n\t"
        "v_mov_b32_e32 v54, 0\n\t"
        "v_mov_b32_e32 v55, 0\n\t"
        "v_mov_b32_e32 v56, 0\n\t"
        "v_mov_b32_e32 v57, 0\n\t"
        "v_mov_b32_e32 v58, 0\n\t"
        "v_mov_b32_e32 v59, 0\n\t"
        "v_mov_b32_e32 v60, 0\n\t"
        "v_mov_b32_e32 v61, 0\n\t"
        "v_mov_b32_e32 v62, 0\n\t"
        "v_mov_b32_e32 v63, 0\n\t"
        "s_barrier\n\t"
        "s_memrealtime s[76:77]\n\t"
        "s_mov_b32 s79, %[nT]\n\t"
        "v_mov_b32_e32 v86, 64\n\t"
        "s_add_u32 s61, %[b0], 0x40000\n\t"
        "s_mov_b32 s62, %[b0]\n\t"
        "s_sub_u32 s75, s75, 16\n\t"
        "s_waitcnt lgkmcnt(0)\n\t"
        "s_mov_b32 s41, s76\n\t"
        "s_mov_b32 s69, s76\n\t"
        ".p2align 6\n\t"
        ".Lloop%=:\n\t"
        "s_mov_b32 s97, 0\n\t"
        "s_movk_i32 s96, 0x1000\n\t"
        ".Lwa%=:\n\t"
        "s_memrealtime s[98:99]\n\t"
        "s_addk_i32 s96, -1\n\t"
        "s_cmp_lt_i32 s96, 0\n\t"
        "s_cbranch_scc1 .Lta%=\n\t"
        "s_waitcnt lgkmcnt(0)\n\t"
        "s_sub_i32 s98, s98, s69\n\t"
        "s_cmp_lt_i32 s98, %c[iFT]\n\t"
        "s_cbranch_scc1 .Lwa%=\n\t"
        "s_cmp_ge_i32 s98, 0\n\t"
        "s_cbranch_scc1 .Lxa%=\n\t"
        ".Lqa%=:\n\t"
        "s_nop %c[iFN]\n\t"
        "s_addk_i32 s98, 1\n\t"
        "s_cmp_lt_i32 s98, 0\n\t"
        "s_cbranch_scc1 .Lqa%=\n\t"
        ".Lxa%=:\n\t"
        "buffer_load_dwordx4 v[0:3], v112, %[srd], s57 offen sc1\n\t"
        "s_nop 7\n\t"
        "buffer_load_dwordx4 v[4:7], v112, %[srd], s58 offen sc1\n\t"
        "s_nop 7\n\t"
        "buffer_load_dwordx4 v[8:11], v112, %[srd], s59 offen sc1\n\t"
        "s_nop 7\n\t"
        "buffer_load_dwordx4 v[12:15], v112, %[srd], s60 offen sc1\n\t"
        "v_mov_b32_e32 v80, 161792\n\t"
        "ds_read_b64 v[80:81], v80\n\t"
        "s_waitcnt lgkmcnt(0)\n\t"
        "v_readfirstlane_b32 s80, v80\n\t"
        "v_readfirstlane_b32 s81, v81\n\t"
        "s_cmp_eq_u32 s80, s40\n\t"
        "s_cselect_b32 s84, 1, 0\n\t"
        "s_cmp_eq_u32 s81, s40\n\t"
        "s_cselect_b32 s81, 4, 0\n\t"
        "s_or_b32 s84, s84, s81\n\t"
        "v_mov_b32_e32 v106, s76\n\t"
        "v_mov_b32_e32 v107, s84\n\t"
        "buffer_store_dwordx2 v[106:107], v117, %[srd], s79 offen sc1\n\t"
        "s_and_b32 s80, s40, 3\n\t"
        "s_lshl_b32 s80, s80, 17\n\t"
        "s_add_u32 s63, s80, %[nC]\n\t"
        "s_add_u32 s66, s80, %[nS]\n\t"
        "s_add_u32 s81, s80, 0x20000\n\t"
        "s_and_b32 s81, s81, 0x60000\n\t"
        "s_add_u32 s67, s81, %[nW]\n\t"
        "s_add_u32 s79, s81, %[nT]\n\t"
        "s_cmp_eq_u32 %[w], 2\n\t"
        "s_cselect_b32 s81, s80, s81\n\t"
        "s_xor_b32 s81, s81, 0x40000\n\t"
        "s_add_u32 s68, s81, %[pob]\n\t"
        "s_lshl_b32 s72, s40, 17\n\t"
        "s_add_u32 s72, s72, %[kvo]\n\t"
        "s_mov_b32 s80, s61\n\t"
        "s_mov_b32 s61, s62\n\t"
        "s_mov_b32 s62, s80\n\t"
        ".Lgo%=:\n\t"
        "s_waitcnt vmcnt(4)\n\t"
        "v_smfmac_f32_16x16x64_bf16 v[52:55], v[0:3], v[136:143], v113\n\t"
        "v_smfmac_f32_16x16x64_bf16 v[56:59], v[0:3], v[144:151], v113\n\t"
        "v_smfmac_f32_16x16x64_bf16 v[60:63], v[0:3], v[152:159], v113\n\t"
        "v_smfmac_f32_16x16x64_bf16 v[48:51], v[0:3], v[128:135], v113\n\t"
        "s_waitcnt vmcnt(3)\n\t"
        "v_smfmac_f32_16x16x64_bf16 v[52:55], v[4:7], v[168:175], v113\n\t"
        "v_smfmac_f32_16x16x64_bf16 v[56:59], v[4:7], v[176:183], v113\n\t"
        "v_smfmac_f32_16x16x64_bf16 v[60:63], v[4:7], v[184:191], v113\n\t"
        "v_smfmac_f32_16x16x64_bf16 v[48:51], v[4:7], v[160:167], v113\n\t"
        "s_waitcnt vmcnt(2)\n\t"
        "v_smfmac_f32_16x16x64_bf16 v[52:55], v[8:11], v[200:207], v113\n\t"
        "v_smfmac_f32_16x16x64_bf16 v[56:59], v[8:11], v[208:215], v113\n\t"
        "v_smfmac_f32_16x16x64_bf16 v[60:63], v[8:11], v[216:223], v113\n\t"
        "v_smfmac_f32_16x16x64_bf16 v[48:51], v[8:11], v[192:199], v113\n\t"
        "s_waitcnt vmcnt(1)\n\t"
        "v_smfmac_f32_16x16x64_bf16 v[52:55], v[12:15], v[232:239], v113\n\t"
        "v_smfmac_f32_16x16x64_bf16 v[56:59], v[12:15], v[240:247], v113\n\t"
        "v_smfmac_f32_16x16x64_bf16 v[60:63], v[12:15], v[248:255], v113\n\t"
        "v_smfmac_f32_16x16x64_bf16 v[48:51], v[12:15], v[224:231], v113\n\t"
        "s_nop 1\n\t"
        "v_mov_b32_e32 v104, v104\n\t"
        "s_nop 1\n\t"
        "v_add_f32_e32 v98, v52, v54\n\t"
        "v_add_f32_e32 v99, v53, v55\n\t"
        "ds_write_b64 v123, v[98:99]\n\t"
        "v_add_f32_e32 v100, v56, v58\n\t"
        "v_add_f32_e32 v101, v57, v59\n\t"
        "ds_write_b64 v124, v[100:101]\n\t"
        "v_add_f32_e32 v102, v60, v62\n\t"
        "v_add_f32_e32 v103, v61, v63\n\t"
        "ds_write_b64 v125, v[102:103]\n\t"
        "v_pk_add_f32 v[96:97], v[48:49], v[50:51]\n\t"
        "v_cmp_u_f32_e32 vcc, v96, v97\n\t"
        "s_nop 2\n\t"
        "v_mov_b32_e32 v104, v104\n\t"
        "s_cbranch_vccnz .Lfa%=\n\t"
        ".Lra%=:\n\t"
        "v_mov_b32_e32 v104, v104\n\t"
        "s_nop 2\n\t"
        "v_mov_b32_e32 v104, v104\n\t"
        "s_nop 2\n\t"
        "v_mov_b32_e32 v104, v104\n\t"
        "s_nop 2\n\t"
        "v_mov_b32_e32 v104, v104\n\t"
        "s_waitcnt lgkmcnt(0)\n\t"
        "s_barrier\n\t"
        "v_mov_b32_e32 v104, v104\n\t"
        "ds_read_b64 v[98:99], v122 offset:512\n\t"
        "ds_read_b64 v[100:101], v122 offset:1024\n\t"
        "ds_read_b64 v[102:103], v122 offset:1536\n\t"
        "v_mov_b32_e32 v104, v104\n\t"
        "s_nop 2\n\t"
        "v_mov_b32_e32 v104, v104\n\t"
        "s_nop 2\n\t"
        "v_mov_b32_e32 v104, v104\n\t"
        "s_waitcnt lgkmcnt(2)\n\t"
        "v_pk_add_f32 v[96:97], v[96:97], v[98:99]\n\t"
        "s_waitcnt lgkmcnt(0)\n\t"
        "v_pk_add_f32 v[100:101], v[100:101], v[102:103]\n\t"
        "v_pk_add_f32 v[96:97], v[96:97], v[100:101]\n\t"
        "v_cvt_pk_bf16_f32 v80, v96, v97\n\t"
        "v_min_u32_e32 v80, -2, v80\n\t"
        "buffer_store_dword v80, v114, %[srd], s61 offen sc1\n\t"
        "s_cmp_eq_u32 s40, 63\n\t"
        "s_cbranch_scc1 .Lend%=\n\t"
        "v_mov_b32_e32 v64, 0\n\t"
        "v_mov_b32_e32 v65, 0\n\t"
        "v_mov_b32_e32 v66, 0\n\t"
        "v_mov_b32_e32 v67, 0\n\t"
        "v_mov_b32_e32 v68, 0\n\t"
        "v_mov_b32_e32 v69, 0\n\t"
        "v_mov_b32_e32 v70, 0\n\t"
        "v_mov_b32_e32 v71, 0\n\t"
        "v_mov_b32_e32 v72, 0\n\t"
        "v_mov_b32_e32 v73, 0\n\t"
        "v_mov_b32_e32 v74, 0\n\t"
        "v_mov_b32_e32 v75, 0\n\t"
        "v_mov_b32_e32 v76, 0\n\t"
        "v_mov_b32_e32 v77, 0\n\t"
        "v_mov_b32_e32 v78, 0\n\t"
        "v_mov_b32_e32 v79, 0\n\t"
        "s_xor_b32 s64, s63, %c[iCS1]\n\t"
        "s_xor_b32 s65, s63, %c[iCS2]\n\t"
        "s_xor_b32 s73, s64, %c[iCS2]\n\t"
        "s_sleep %c[VSLP]\n\t"
        "buffer_load_dwordx4 v[88:91], v112, %[srd], s66 offen sc1\n\t"
        "s_cmp_eq_u32 %[w], 0\n\t"
        "s_cselect_b32 exec_hi, 0, -1\n\t"
        "buffer_store_dwordx4 v[92:95], v112, %[srd], s68 offen sc1\n\t"
        "s_mov_b32 exec_hi, -1\n\t"
        "s_waitcnt lgkmcnt(0)\n\t"
        "v_mov_b32_e32 v48, 0\n\t"
        "v_mov_b32_e32 v49, 0\n\t"
        "v_mov_b32_e32 v50, 0\n\t"
        "v_mov_b32_e32 v51, 0\n\t"
        "v_mov_b32_e32 v52, 0\n\t"
        "v_mov_b32_e32 v53, 0\n\t"
        "v_mov_b32_e32 v54, 0\n\t"
        "v_mov_b32_e32 v55, 0\n\t"
        "v_mov_b32_e32 v56, 0\n\t"
        "v_mov_b32_e32 v57, 0\n\t"
        "v_mov_b32_e32 v58, 0\n\t"
        "v_mov_b32_e32 v59, 0\n\t"
        "v_mov_b32_e32 v60, 0\n\t"
        "v_mov_b32_e32 v61, 0\n\t"
        "v_mov_b32_e32 v62, 0\n\t"
        "v_mov_b32_e32 v63, 0\n\t"
        "s_add_u32 s81, s40, 1\n\t"
        "s_and_b32 s80, s81, 3\n\t"
        "s_lshl_b32 s80, s80, 17\n\t"
        "s_add_u32 s57, s80, %[nA]\n\t"
        "s_add_u32 s58, s57, 0x1000\n\t"
        "s_add_u32 s59, s57, 0x8000\n\t"
        "s_add_u32 s60, s57, 0x9000\n\t"
        "s_waitcnt vmcnt(1)\n\t"
        "s_mov_b32 s97, 0\n\t"
        ".Lsr%=:\n\t"
        "s_mov_b32 exec_hi, 0xffff\n\t"
        "v_subrev_u32_e32 v4, s41, v88\n\t"
        "v_subrev_u32_e32 v5, s41, v90\n\t"
        "v_max_i32_e32 v4, v4, v5\n\t"
        "v_max_u32_e32 v5, v89, v91\n\t"
        "v_cmp_eq_u32_e32 vcc, -1, v5\n\t"
        "v_max_i32_dpp v4, v4, v4 quad_perm:[1,0,3,2] row_mask:0xf bank_mask:0xf\n\t"
        "v_or_b32_e32 v6, v89, v91\n\t"
        "s_nop 0\n\t"
        "v_max_i32_dpp v4, v4, v4 quad_perm:[2,3,0,1] row_mask:0xf bank_mask:0xf\n\t"
        "s_nop 1\n\t"
        "v_max_i32_dpp v4, v4, v4 row_ror:4 row_mask:0xf bank_mask:0xf\n\t"
        "s_nop 1\n\t"
        "v_max_i32_dpp v4, v4, v4 row_ror:8 row_mask:0xf bank_mask:0xf\n\t"
        "s_cbranch_vccnz .Lsf%=\n\t"
        "v_readlane_b32 s85, v4, 0\n\t"
        "v_readlane_b32 s81, v4, 16\n\t"
        "v_readlane_b32 s56, v4, 32\n\t"
        "v_and_b32_e32 v5, 1, v6\n\t"
        "v_cmp_ne_u32_e64 s[86:87], 0, v5\n\t"
        "v_and_b32_e32 v5, 2, v6\n\t"
        "v_cmp_ne_u32_e64 s[88:89], 0, v5\n\t"
        "v_and_b32_e32 v5, 4, v6\n\t"
        "v_cmp_ne_u32_e64 s[90:91], 0, v5\n\t"
        "s_max_i32 s56, s56, s81\n\t"
        "s_add_u32 s92, s85, s41\n\t"
        "s_sub_u32 s92, s92, s43\n\t"
        "s_add_i32 s93, s56, s47\n\t"
        "s_mov_b32 s44, s46\n\t"
        "s_mov_b32 s46, s45\n\t"
        "s_mov_b32 s94, s48\n\t"
        "s_mov_b32 s43, s41\n\t"
        "s_cmp_gt_i32 s56, s54\n\t"
        "s_cbranch_scc1 .Lmv%=\n\t"
        "s_mov_b32 s37, 0\n\t"
        "s_mov_b32 s48, 0\n\t"
        "s_mov_b32 s95, 0\n\t"
        ".Lsd%=:\n\t"
        "s_mov_b32 exec_hi, -1\n\t"
        "s_add_u32 s42, s43, s46\n\t"
        "s_mov_b32 s97, 0\n\t"
        "s_movk_i32 s96, 0x1000\n\t"
        ".Lwc%=:\n\t"
        "s_memrealtime s[98:99]\n\t"
        "s_addk_i32 s96, -1\n\t"
        "s_cmp_lt_i32 s96, 0\n\t"
        "s_cbranch_scc1 .Ltc%=\n\t"
        "s_waitcnt lgkmcnt(0)\n\t"
        "s_sub_i32 s98, s98, s42\n\t"
        "s_cmp_lt_i32 s98, %c[iFT]\n\t"
        "s_cbranch_scc1 .Lwc%=\n\t"
        "s_cmp_ge_i32 s98, 0\n\t"
        "s_cbranch_scc1 .Lxc%=\n\t"
        ".Lqc%=:\n\t"
        "s_nop %c[iFN]\n\t"
        "s_addk_i32 s98, 1\n\t"
        "s_cmp_lt_i32 s98, 0\n\t"
        "s_cbranch_scc1 .Lqc%=\n\t"
        ".Lxc%=:\n\t"
        "buffer_load_dwordx4 v[16:19], v112, %[srd], s63 offen sc1\n\t"
        "s_cmp_ge_u32 s40, %c[iCT0]\n\t"
        "s_cselect_b32 s85, 1, 0\n\t"
        "s_cmp_lg_u64 s[86:87], 0\n\t"
        "s_cselect_b32 s86, s85, 0\n\t"
        "s_cmp_lg_u64 s[88:89], 0\n\t"
        "s_cselect_b32 s87, s85, 0\n\t"
        "s_cmp_lg_u64 s[90:91], 0\n\t"
        "s_cselect_b32 s88, s85, 0\n\t"
        "buffer_load_dwordx4 v[20:23], v115, %[srd], s63 offen sc1\n\t"
        "s_or_b32 s80, s94, s86\n\t"
        "s_or_b32 s80, s80, s87\n\t"
        "s_andn2_b32 s90, s85, s80\n\t"
        "s_cmp_le_i32 s92, s44\n\t"
        "s_cselect_b32 s80, s85, 0\n\t"
        "s_andn2_b32 s91, s80, s88\n\t"
        "s_and_b32 s80, s40, %c[LDEC]\n\t"
        "s_cselect_b32 s89, 0, 1\n\t"
        "buffer_load_dwordx4 v[24:27], v112, %[srd], s64 offen sc1\n\t"
        "s_andn2_b32 s80, s86, s94\n\t"
        "s_cmp_lt_i32 s51, %c[LA24]\n\t"
        "s_cselect_b32 s80, s80, 0\n\t"
        "s_lshl2_add_u32 s51, s80, s51\n\t"
        "s_sub_i32 s51, s51, s89\n\t"
        "s_max_i32 s51, s51, %c[LA]\n\t"
        "s_lshl_b32 s80, s93, 1\n\t"
        "s_sub_i32 s81, s80, s53\n\t"
        "buffer_load_dwordx4 v[28:31], v115, %[srd], s64 offen sc1\n\t"
        "s_ashr_i32 s82, s81, %c[UPSH]\n\t"
        "s_max_i32 s82, s82, %c[TRUP]\n\t"
        "s_add_i32 s82, s82, s53\n\t"
        "s_min_i32 s82, s82, s80\n\t"
        "s_ashr_i32 s83, s81, %c[DNSH]\n\t"
        "s_min_i32 s83, s83, -%c[TRDN]\n\t"
        "s_add_i32 s83, s83, s53\n\t"
        "s_max_i32 s83, s83, s80\n\t"
        "buffer_load_dwordx4 v[32:35], v112, %[srd], s65 offen sc1\n\t"
        "s_cmp_gt_i32 s81, 0\n\t"
        "s_cselect_b32 s82, s82, s83\n\t"
        "s_cmp_lg_u32 s91, 0\n\t"
        "s_cselect_b32 s53, s82, s53\n\t"
        "s_add_i32 s80, s53, 1\n\t"
        "s_ashr_i32 s80, s80, 1\n\t"
        "s_add_i32 s47, s80, s51\n\t"
        "s_add_u32 s81, s47, %c[AUP]\n\t"
        "buffer_load_dwordx4 v[36:39], v115, %[srd], s65 offen sc1\n\t"
        "s_min_i32 s80, s95, s81\n\t"
        "s_max_i32 s47, s47, s80\n\t"
        "s_add_u32 s41, s42, s47\n\t"
        "s_cmp_eq_u32 s40, 0\n\t"
        "s_cselect_b32 s51, %c[LA], s51\n\t"
        "s_mov_b32 s80, s88\n\t"
        "s_add_i32 s81, s92, s50\n\t"
        "s_sub_i32 s81, s81, 4\n\t"
        "buffer_load_dwordx4 v[40:43], v112, %[srd], s73 offen sc1\n\t"
        "s_cmp_le_i32 s81, s44\n\t"
        "s_cselect_b32 s80, s80, 0\n\t"
        "s_cmp_lt_i32 s50, %c[LC24]\n\t"
        "s_cselect_b32 s80, s80, 0\n\t"
        "s_lshl2_add_u32 s50, s80, s50\n\t"
        "s_sub_i32 s50, s50, s89\n\t"
        "s_max_i32 s50, s50, %c[LC]\n\t"
        "s_lshl_b32 s80, s92, 1\n\t"
        "buffer_load_dwordx4 v[44:47], v115, %[srd], s73 offen sc1\n\t"
        "s_sub_i32 s81, s80, s52\n\t"
        "s_ashr_i32 s82, s81, %c[UPSH]\n\t"
        "s_max_i32 s82, s82, %c[TRUP]\n\t"
        "s_add_i32 s82, s82, s52\n\t"
        "s_min_i32 s82, s82, s80\n\t"
        "s_ashr_i32 s83, s81, %c[DNSH]\n\t"
        "s_min_i32 s83, s83, -%c[TRDN]\n\t"
        "s_add_i32 s83, s83, s52\n\t"
        "s_max_i32 s83, s83, s80\n\t"
        "s_cmp_gt_i32 s81, 0\n\t"
        "s_cselect_b32 s82, s82, s83\n\t"
        "s_cmp_lg_u32 s90, 0\n\t"
        "s_cselect_b32 s52, s82, s52\n\t"
        "s_add_i32 s80, s52, 1\n\t"
        "s_ashr_i32 s80, s80, 1\n\t"
        "s_add_i32 s45, s80, s50\n\t"
        ".p2align 6\n\t"
        ".Lrc%=:\n\t"
        "s_waitcnt vmcnt(7)\n\t"
        "v_smfmac_f32_16x16x64_bf16 v[64:67], v[16:19], a[0:7], v113\n\t"
        "v_smfmac_f32_16x16x64_bf16 v[68:71], v[16:19], a[8:15], v113\n\t"
        "v_smfmac_f32_16x16x64_bf16 v[72:75], v[16:19], a[16:23], v113\n\t"
        "v_smfmac_f32_16x16x64_bf16 v[76:79], v[16:19], a[24:31], v113\n\t"
        "s_waitcnt vmcnt(6)\n\t"
        "v_smfmac_f32_16x16x64_bf16 v[64:67], v[20:23], a[32:39], v113\n\t"
        "v_smfmac_f32_16x16x64_bf16 v[68:71], v[20:23], a[40:47], v113\n\t"
        "v_smfmac_f32_16x16x64_bf16 v[72:75], v[20:23], a[48:55], v113\n\t"
        "v_smfmac_f32_16x16x64_bf16 v[76:79], v[20:23], a[56:63], v113\n\t"
        "s_waitcnt vmcnt(5)\n\t"
        "v_smfmac_f32_16x16x64_bf16 v[64:67], v[24:27], a[64:71], v113\n\t"
        "v_smfmac_f32_16x16x64_bf16 v[68:71], v[24:27], a[72:79], v113\n\t"
        "v_smfmac_f32_16x16x64_bf16 v[72:75], v[24:27], a[80:87], v113\n\t"
        "v_smfmac_f32_16x16x64_bf16 v[76:79], v[24:27], a[88:95], v113\n\t"
        "s_waitcnt vmcnt(4)\n\t"
        "v_smfmac_f32_16x16x64_bf16 v[64:67], v[28:31], a[96:103], v113\n\t"
        "v_smfmac_f32_16x16x64_bf16 v[68:71], v[28:31], a[104:111], v113\n\t"
        "v_smfmac_f32_16x16x64_bf16 v[72:75], v[28:31], a[112:119], v113\n\t"
        "v_smfmac_f32_16x16x64_bf16 v[76:79], v[28:31], a[120:127], v113\n\t"
        "s_waitcnt vmcnt(3)\n\t"
        "v_smfmac_f32_16x16x64_bf16 v[64:67], v[32:35], a[128:135], v113\n\t"
        "v_smfmac_f32_16x16x64_bf16 v[68:71], v[32:35], a[136:143], v113\n\t"
        "v_smfmac_f32_16x16x64_bf16 v[72:75], v[32:35], a[144:151], v113\n\t"
        "v_smfmac_f32_16x16x64_bf16 v[76:79], v[32:35], a[152:159], v113\n\t"
        "s_waitcnt vmcnt(2)\n\t"
        "v_smfmac_f32_16x16x64_bf16 v[64:67], v[36:39], a[160:167], v113\n\t"
        "v_smfmac_f32_16x16x64_bf16 v[68:71], v[36:39], a[168:175], v113\n\t"
        "v_smfmac_f32_16x16x64_bf16 v[72:75], v[36:39], a[176:183], v113\n\t"
        "v_smfmac_f32_16x16x64_bf16 v[76:79], v[36:39], a[184:191], v113\n\t"
        "s_waitcnt vmcnt(1)\n\t"
        "v_smfmac_f32_16x16x64_bf16 v[64:67], v[40:43], a[192:199], v113\n\t"
        "v_smfmac_f32_16x16x64_bf16 v[68:71], v[40:43], a[200:207], v113\n\t"
        "v_smfmac_f32_16x16x64_bf16 v[72:75], v[40:43], a[208:215], v113\n\t"
        "v_smfmac_f32_16x16x64_bf16 v[76:79], v[40:43], a[216:223], v113\n\t"
        "s_waitcnt vmcnt(0)\n\t"
        "v_smfmac_f32_16x16x64_bf16 v[64:67], v[44:47], a[224:231], v113\n\t"
        "s_nop 2\n\t"
        "v_smfmac_f32_16x16x64_bf16 v[68:71], v[44:47], a[232:239], v113\n\t"
        "s_cmp_gt_u32 %[w], 2\n\t"
        "s_nop 1\n\t"
        "v_smfmac_f32_16x16x64_bf16 v[72:75], v[44:47], a[240:247], v113\n\t"
        "v_add_f32_e32 v80, v64, v66\n\t"
        "s_nop 0\n\t"
        "v_smfmac_f32_16x16x64_bf16 v[76:79], v[44:47], a[248:255], v113\n\t"
        "v_add_f32_e32 v81, v65, v67\n\t"
        "v_add_f32_e32 v82, v68, v70\n\t"
        "v_add_f32_e32 v83, v69, v71\n\t"
        "s_mov_b32 exec_lo, 0\n\t"
        "s_nop 0\n\t"
        "v_add_f32_e32 v80, v72, v74\n\t"
        "v_add_f32_e32 v81, v73, v75\n\t"
        "s_nop 0\n\t"
        "v_pk_add_f32 v[82:83], v[76:77], v[78:79]\n\t"
        "s_mov_b32 exec_lo, -1\n\t"
        "s_nop 0\n\t"
        "v_permlane32_swap_b32_e32 v80, v82\n\t"
        "v_add_f32_e32 v80, v80, v82\n\t"
        "ds_write_b32 v119, v80\n\t"
        "v_permlane32_swap_b32_e32 v81, v83\n\t"
        "v_add_f32_e32 v81, v81, v83\n\t"
        "ds_write_b32 v119, v81 offset:128\n\t"
        "v_cmp_u_f32_e32 vcc, v80, v81\n\t"
        "s_nop 2\n\t"
        "v_mov_b32_e32 v104, v104\n\t"
        "s_cbranch_vccnz .Lfc%=\n\t"
        ".Lrd%=:\n\t"
        "v_mov_b32_e32 v104, v104\n\t"
        "s_cbranch_scc1 .Lno%=\n\t"
        "v_mov_b32_e32 v104, v104\n\t"
        "s_nop 2\n\t"
        "v_mov_b32_e32 v104, v104\n\t"
        "s_waitcnt lgkmcnt(0)\n\t"
        "s_barrier\n\t"
        "v_mov_b32_e32 v104, v104\n\t"
        "ds_read_b64 v[0:1], v120 offset:8192\n\t"
        "ds_read_b64 v[2:3], v120 offset:8704\n\t"
        "ds_read_b64 v[4:5], v120 offset:9216\n\t"
        "ds_read_b64 v[6:7], v120 offset:9728\n\t"
        "v_mov_b32_e32 v104, v104\n\t"
        "s_nop 2\n\t"
        "v_mov_b32_e32 v104, v104\n\t"
        "s_nop 2\n\t"
        "v_mov_b32_e32 v104, v104\n\t"
        "s_waitcnt lgkmcnt(2)\n\t"
        "v_pk_add_f32 v[0:1], v[0:1], v[2:3]\n\t"
        "s_waitcnt lgkmcnt(0)\n\t"
        "v_pk_add_f32 v[4:5], v[4:5], v[6:7]\n\t"
        "v_pk_add_f32 v[0:1], v[0:1], v[4:5]\n\t"
        "v_cvt_pk_bf16_f32 v0, v0, v1\n\t"
        "v_min_u32_e32 v0, -2, v0\n\t"
        "buffer_store_dword v0, v116, %[srd], s67 offen sc1\n\t"
        ".Lnp%=:\n\t"
        "s_memrealtime s[76:77]\n\t"
        "s_add_u32 s40, s40, 1\n\t"
        "s_waitcnt lgkmcnt(0)\n\t"
        "s_add_u32 s81, s76, %c[iVD]\n\t"
        "s_sub_u32 s80, s76, s41\n\t"
        "s_cmp_gt_i32 s80, %c[iTOL]\n\t"
        "s_cselect_b32 s69, s81, s41\n\t"
        "s_branch .Lloop%=\n\t"
        ".Lmv%=:\n\t"
        ".Lhk0%=:\n\t"
        "s_cbranch_execz .Lhk1%=\n\t"
        "s_mov_b32 s48, 1\n\t"
        "s_cmp_eq_u32 s54, %c[iTOL]\n\t"
        "s_cbranch_scc1 .Lm1%=\n\t"
        "s_max_i32 s80, s85, s56\n\t"
        "s_add_u32 s43, s41, s80\n\t"
        "s_add_u32 s43, s43, %c[iPEN0]\n\t"
        ".Lhk1%=:\n\t"
        "s_cbranch_execz .Lhk2%=\n\t"
        "s_mov_b32 s54, %c[iTOL]\n\t"
        "s_add_u32 s46, s46, %c[iX0C]\n\t"
        "s_mov_b32 s95, 0\n\t"
        "s_add_u32 s51, s51, %c[iX0A]\n\t"
        "v_and_b32_e32 v4, 3, v88\n\t"
        "v_and_b32_e32 v5, 3, v90\n\t"
        ".Lhk2%=:\n\t"
        "s_cbranch_execz .Lhk3%=\n\t"
        "v_lshlrev_b32_e64 v4, v4, 1\n\t"
        "v_lshlrev_b32_e64 v5, v5, 1\n\t"
        "v_or_b32_e32 v4, v4, v5\n\t"
        "s_mov_b32 s80, 0\n\t"
        "v_and_b32_e32 v5, 1, v4\n\t"
        "v_cmp_ne_u32_e32 vcc, 0, v5\n\t"
        ".Lhk3%=:\n\t"
        "s_cbranch_execz .Lhk4%=\n\t"
        "s_nop 1\n\t"
        "s_cmp_lg_u64 vcc, 0\n\t"
        "s_cselect_b32 s81, 2, 0\n\t"
        "s_or_b32 s80, s80, s81\n\t"
        "v_and_b32_e32 v5, 2, v4\n\t"
        "v_cmp_ne_u32_e32 vcc, 0, v5\n\t"
        ".Lhk4%=:\n\t"
        "s_cbranch_execz .Lhk5%=\n\t"
        "s_nop 1\n\t"
        "s_cmp_lg_u64 vcc, 0\n\t"
        "s_cselect_b32 s81, 4, 0\n\t"
        "s_or_b32 s80, s80, s81\n\t"
        "v_and_b32_e32 v5, 4, v4\n\t"
        "v_cmp_ne_u32_e32 vcc, 0, v5\n\t"
        ".Lhk5%=:\n\t"
        "s_cbranch_execz .Lhk6%=\n\t"
        "s_nop 1\n\t"
        "s_cmp_lg_u64 vcc, 0\n\t"
        "s_cselect_b32 s81, 8, 0\n\t"
        "s_or_b32 s80, s80, s81\n\t"
        "v_and_b32_e32 v5, 8, v4\n\t"
        "v_cmp_ne_u32_e32 vcc, 0, v5\n\t"
        ".Lhk6%=:\n\t"
        "s_cbranch_execz .Lhk7%=\n\t"
        "s_nop 1\n\t"
        "s_cmp_lg_u64 vcc, 0\n\t"
        "s_cselect_b32 s81, 16, 0\n\t"
        "s_or_b32 s80, s80, s81\n\t"
        "s_lshr_b32 s80, 0x1adf1210, s80\n\t"
        "s_and_b32 s55, s80, 3\n\t"
        ".Lhk7%=:\n\t"
        "s_cbranch_execz .Lhk8%=\n\t"
        "s_branch .Lal%=\n\t"
        ".Lm1%=:\n\t"
        "s_add_u32 s80, s37, %c[iPEN2]\n\t"
        "s_min_i32 s80, s80, %c[iPENM]\n\t"
        "s_cmp_lg_u32 s37, 0\n\t"
        "s_cselect_b32 s37, s80, %c[iPEN]\n\t"
        "s_add_u32 s80, s56, s37\n\t"
        ".Lhk8%=:\n\t"
        "s_cbranch_execz .Lhk9%=\n\t"
        "s_max_i32 s80, s80, %c[iPMIN]\n\t"
        "s_add_u32 s43, s41, s80\n\t"
        "s_add_u32 s95, s93, %c[LA]\n\t"
        "s_add_u32 s46, s46, %c[iXR]\n\t"
        ".Lal%=:\n\t"
        "s_sub_u32 s80, s55, s43\n\t"
        "s_and_b32 s80, s80, %c[iPHM]\n\t"
        ".Lhk9%=:\n\t"
        "s_cbranch_execz .Lhk10%=\n\t"
        "s_add_u32 s43, s43, s80\n\t"
        "s_branch .Lsd%=\n\t"
        ".Lsf%=:\n\t"
        "s_mov_b32 exec_hi, -1\n\t"
        "s_sleep 2\n\t"
        "buffer_load_dwordx4 v[88:91], v112, %[srd], s66 offen sc1\n\t"
        "s_add_u32 s97, s97, 1\n\t"
        ".Lhk10%=:\n\t"
        "s_cbranch_execz .Lhk11%=\n\t"
        "s_cmp_lt_u32 s97, 0x100000\n\t"
        "s_cbranch_scc0 .Lgu%=\n\t"
        "s_waitcnt vmcnt(0)\n\t"
        "s_branch .Lsr%=\n\t"
        ".Lfa%=:\n\t"
        "v_max_u32_e32 v84, v0, v2\n\t"
        "v_max3_u32 v84, v84, v1, v3\n\t"
        ".Lhk11%=:\n\t"
        "s_cbranch_execz .Lhk12%=\n\t"
        "v_max3_u32 v84, v84, v4, v6\n\t"
        "v_max3_u32 v84, v84, v5, v7\n\t"
        "v_max3_u32 v84, v84, v8, v10\n\t"
        "v_max3_u32 v84, v84, v9, v11\n\t"
        "v_max3_u32 v84, v84, v12, v14\n\t"
        "v_max3_u32 v84, v84, v13, v15\n\t"
        ".Lhk12%=:\n\t"
        "s_cbranch_execz .Lhk13%=\n\t"
        "v_cmp_eq_u32_e32 vcc, -1, v84\n\t"
        "s_nop 1\n\t"
        "v_mov_b32_e32 v104, v104\n\t"
        "s_nop 0\n\t"
        "s_cbranch_vccz .Lra%=\n\t"
        "s_add_u32 s80, s40, 1\n\t"
        ".Lhk13%=:\n\t"
        "s_cbranch_execz .Lhk14%=\n\t"
        "v_mov_b32_e32 v80, 161792\n\t"
        "v_mov_b32_e32 v81, s80\n\t"
        "ds_write_b32 v80, v81\n\t"
        "v_mov_b32_e32 v48, 0\n\t"
        "v_mov_b32_e32 v49, 0\n\t"
        "v_mov_b32_e32 v50, 0\n\t"
        ".Lhk14%=:\n\t"
        "s_cbranch_execz .Lhk15%=\n\t"
        "v_mov_b32_e32 v51, 0\n\t"
        "v_mov_b32_e32 v52, 0\n\t"
        "v_mov_b32_e32 v53, 0\n\t"
        "v_mov_b32_e32 v54, 0\n\t"
        "v_mov_b32_e32 v55, 0\n\t"
        "v_mov_b32_e32 v56, 0\n\t"
        ".Lhk15%=:\n\t"
        "s_cbranch_execz .Lhk16%=\n\t"
        "v_mov_b32_e32 v57, 0\n\t"
        "v_mov_b32_e32 v58, 0\n\t"
        "v_mov_b32_e32 v59, 0\n\t"
        "v_mov_b32_e32 v60, 0\n\t"
        "v_mov_b32_e32 v61, 0\n\t"
        "v_mov_b32_e32 v62, 0\n\t"
        ".Lhk16%=:\n\t"
        "s_cbranch_execz .Lhk17%=\n\t"
        "v_mov_b32_e32 v63, 0\n\t"
        "s_sleep %c[VASL]\n\t"
        "buffer_load_dwordx4 v[0:3], v112, %[srd], s57 offen sc1\n\t"
        "buffer_load_dwordx4 v[4:7], v112, %[srd], s58 offen sc1\n\t"
        "buffer_load_dwordx4 v[8:11], v112, %[srd], s59 offen sc1\n\t"
        "buffer_load_dwordx4 v[12:15], v112, %[srd], s60 offen sc1\n\t"
        ".Lhk17%=:\n\t"
        "s_cbranch_execz .Lhk18%=\n\t"
        "buffer_store_dword v0, off, %[wsd], s36\n\t"
        "s_add_u32 s97, s97, 1\n\t"
        "s_cmp_lt_u32 s97, 0x100000\n\t"
        "s_cbranch_scc0 .Lgu%=\n\t"
        "s_branch .Lgo%=\n\t"
        ".Lfc%=:\n\t"
        "v_max_u32_e32 v84, v16, v18\n\t"
        ".Lhk18%=:\n\t"
        "s_cbranch_execz .Lhk19%=\n\t"
        "v_max3_u32 v84, v84, v20, v22\n\t"
        "v_max3_u32 v84, v84, v24, v26\n\t"
        "v_max3_u32 v84, v84, v28, v30\n\t"
        "v_max3_u32 v84, v84, v32, v34\n\t"
        "v_max3_u32 v84, v84, v36, v38\n\t"
        "v_max3_u32 v84, v84, v40, v42\n\t"
        ".Lhk19%=:\n\t"
        "s_cbranch_execz .Lhk20%=\n\t"
        "v_max3_u32 v84, v84, v44, v46\n\t"
        "v_cmp_eq_u32_e32 vcc, -1, v84\n\t"
        "s_nop 1\n\t"
        "v_mov_b32_e32 v104, v104\n\t"
        "s_nop 0\n\t"
        "s_cbranch_vccz .Lrd%=\n\t"
        ".Lhk20%=:\n\t"
        "s_cbranch_execz .Lhk21%=\n\t"
        "s_sleep %c[CSLP]\n\t"
        "buffer_load_dwordx4 v[16:19], v112, %[srd], s63 offen sc1\n\t"
        "v_mov_b32_e32 v64, 0\n\t"
        "v_mov_b32_e32 v65, 0\n\t"
        "buffer_load_dwordx4 v[20:23], v115, %[srd], s63 offen sc1\n\t"
        "v_mov_b32_e32 v66, 0\n\t"
        ".Lhk21%=:\n\t"
        "s_cbranch_execz .Lhk22%=\n\t"
        "v_mov_b32_e32 v67, 0\n\t"
        "buffer_load_dwordx4 v[24:27], v112, %[srd], s64 offen sc1\n\t"
        "v_mov_b32_e32 v68, 0\n\t"
        "v_mov_b32_e32 v69, 0\n\t"
        "buffer_load_dwordx4 v[28:31], v115, %[srd], s64 offen sc1\n\t"
        "v_mov_b32_e32 v70, 0\n\t"
        ".Lhk22%=:\n\t"
        "s_cbranch_execz .Lhk23%=\n\t"
        "v_mov_b32_e32 v71, 0\n\t"
        "buffer_load_dwordx4 v[32:35], v112, %[srd], s65 offen sc1\n\t"
        "v_mov_b32_e32 v72, 0\n\t"
        "v_mov_b32_e32 v73, 0\n\t"
        "buffer_load_dwordx4 v[36:39], v115, %[srd], s65 offen sc1\n\t"
        "v_mov_b32_e32 v74, 0\n\t"
        ".Lhk23%=:\n\t"
        "s_cbranch_execz .Lhk24%=\n\t"
        "v_mov_b32_e32 v75, 0\n\t"
        "buffer_load_dwordx4 v[40:43], v112, %[srd], s73 offen sc1\n\t"
        "v_mov_b32_e32 v76, 0\n\t"
        "v_mov_b32_e32 v77, 0\n\t"
        "buffer_load_dwordx4 v[44:47], v115, %[srd], s73 offen sc1\n\t"
        "v_mov_b32_e32 v78, 0\n\t"
        ".Lhk24%=:\n\t"
        "s_cbranch_execz .Lhk25%=\n\t"
        "v_mov_b32_e32 v79, 0\n\t"
        "s_add_u32 s80, s40, 1\n\t"
        "v_mov_b32_e32 v80, 161796\n\t"
        "v_mov_b32_e32 v81, s80\n\t"
        "ds_write_b32 v80, v81\n\t"
        "s_add_u32 s97, s97, 1\n\t"
        ".Lhk25%=:\n\t"
        "s_cbranch_execz .Lhk26%=\n\t"
        "s_cmp_lt_u32 s97, 0x100000\n\t"
        "s_cbranch_scc0 .Lgu%=\n\t"
        "s_branch .Lrc%=\n\t"
        ".Lno%=:\n\t"
        "s_waitcnt lgkmcnt(0)\n\t"
        "s_barrier\n\t"
        "s_branch .Lnp%=\n\t"
        ".Lta%=:\n\t"
        ".Lhk26%=:\n\t"
        "s_cbranch_execz .Lhk27%=\n\t"
        "s_waitcnt lgkmcnt(0)\n\t"
        "s_branch .Lxa%=\n\t"
        ".Ltc%=:\n\t"
        "s_waitcnt lgkmcnt(0)\n\t"
        "s_branch .Lxc%=\n\t"
        ".Lgu%=:\n\t"
        "s_mov_b64 exec, -1\n\t"
        "v_mov_b32_e32 v80, %c[ferr]\n\t"
        ".Lhk27%=:\n\t"
        "s_cbranch_execz .Lhk28%=\n\t"
        "v_mov_b32_e32 v81, 2\n\t"
        "buffer_store_dword v81, v80, %[wsd], 0 offen sc1\n\t"
        "s_endpgm\n\t"
        ".Lend%=:\n\t"
        "buffer_store_dword v80, v114, %[wsd], s72 offen sc1\n\t"
        "s_waitcnt vmcnt(0)\n\t"
        "s_barrier\n\t"
        ".Lhk28%=:\n\t"
        "s_cbranch_execz .Lhk29%=\n\t"
        "buffer_store_dword v86, v118, %[wsd], 0 offen sc1\n\t"
        "s_endpgm\n\t"
        ".Lhk29%=:\n\t"
        "s_branch .Lhr%="
        : [nA] "+s"(nA), [nS] "+s"(nS), [nT] "+s"(nT), [nW] "+s"(nW), [nC] "+s"(nC), [b0] "+s"(b0), [pob] "+s"(pob)
        : [srd] "s"(rhd), [wsd] "s"(wsd), [w] "s"(w), [tag] "s"(tag), [kvo] "s"(kvo), [cal] "s"(cal), [sv] "s"(sv), [so] "s"(so), [m0b] "s"(m0b), [key] "s"(key), [tu] "s"(tu),
          [ferr] "i"(F_ERR), [iD1] "i"(D1), [iD2] "i"(D2), [LC] "i"(LC0), [LA] "i"(LA0), [LC24] "i"(LC0 + 24), [LA24] "i"(LA0 + 24), [E1] "i"(2 * (D1 - LC0)), [E2] "i"(2 * (D2 - LA0)),
          [iPEN] "i"(PEN), [iPEN2] "i"(PEN2), [iPENM] "i"(PEN + 2 * PEN2), [iPEN0] "i"(PEN0), [iTOL] "i"(TOL), [iCT0] "i"(CT0), [iX0C] "i"(X0C), [iX0A] "i"(X0A), [iPHM] "i"(PH_M),
          [iFT] "i"(-3), [iFN] "i"(FINE_N), [AUP] "i"(A_UP), [ASLP] "i"(A_SLP), [LDEC] "i"(L_DEC), [UPSH] "i"(UP_SH), [TRUP] "i"(TR_UP), [TRDN] "i"(TR_DN), [HSLP] "i"(H_SLP), [iPMIN] "i"(PMIN), [VSLP] "i"(V_SLP), [RSLP] "i"(R_SLP), [iVD] "i"(V_DEL), [iXR] "i"(XR), [VASL] "i"(VA_SLP), [CSLP] "i"(C_SLP), [DNSH] "i"(DN_SH), [NX0] "i"(NEARX), [NX1] "i"(8192 - NEARX), [iCS1] "i"(CS1), [iCS2] "i"(CS2), [NY0] "i"(NEARY), [NY1] "i"(16384 - NEARY),
          "{v[112:127]}"(pc), "{v[96:103]}"(pa), "{v[104:105]}"(pb)
        : "memory", "vcc", "scc", "m0", "s36", "s37", "s38", "s39", "s40", "s41", "s42", "s43", "s44", "s45", "s46", "s47", "s48", "s49", "s50", "s51", "s52", "s53", "s54", "s55", "s56", "s57", "s58", "s59", "s60", "s61", "s62", "s63", "s64", "s65", "s66", "s67", "s68", "s69", "s70", "s71", "s72", "s73", "s74", "s75", "s76", "s77", "s78", "s79", "s80", "s81", "s82", "s83", "s84", "s85", "s86", "s87", "s88", "s89", "s90", "s91", "s92", "s93", "s94", "s95", "s96", "s97", "s98", "s99");
    }
}

using f32x16 = __attribute__((ext_vector_type(16))) float;
constexpr int CV_RP = 80;
constexpr int CV_U = 96 * CV_RP;
constexpr int CV_KP = 4416;
constexpr int CV_K0 = 4 * CV_U;
constexpr int CV_Y = CV_K0 + 16 * CV_KP;
__device__ __forceinline__ float lo16(unsigned x) { return __uint_as_float(x << 16); }
__device__ __forceinline__ float hi16(unsigned x) { return __uint_as_float(x & 0xFFFF0000u); }
__device__ __forceinline__ void prep_block(const Params& p, rsrc_t r, int k4, const int wv, const unsigned tag) {
    const int tid = otid(), w = wv, l = tid & 63;
    const int cu = (k4 & 3) * 32 + w * 8 + (l >> 3), oc0 = (k4 >> 2) * 8 + (l & 7);
    const rsrc_t rk = __builtin_amdgcn_make_buffer_rsrc((void*)p.Ak, 0, S * D * 2, 0x00020000), rv = __builtin_amdgcn_make_buffer_rsrc((void*)p.Av, 0, S * D * 2, 0x00020000);
    v4u sk[4], kn[11], vn[8], gw[2];
    auto fetch = [&](int oc) {
#pragma unroll
        for (int j = 0; j < 11; ++j) kn[j] = __builtin_amdgcn_raw_buffer_load_b128(rk, (unsigned)((oc * 8 + j - 3) * 2048 + cu * 16), 0, 2);
#pragma unroll
        for (int i = 0; i < 8; ++i) vn[i] = __builtin_amdgcn_raw_buffer_load_b128(rv, (unsigned)((oc * 8 + i) * 2048 + cu * 16), 0, 2); };
    fetch(oc0);
#pragma unroll
    for (int q = 0; q < 4; ++q) sk[q] = *(const v4u*)(p.shift_k + cu * 32 + q * 8);
#pragma unroll
    for (int q = 0; q < 2; ++q) gw[q] = *(const v4u*)(p.gate_w + q * 512 + l * 8);
    st4(r, tid < 128 ? F_RDY + k4 * 512 + tid * 4 : tid < 132 ? F_ATT + (k4 * 4 + tid - 128) * 64 : (tid == 192 && k4 == 0) ? F_CONV : 0xF0000000u, 0u);
#pragma unroll 1
    for (int it = 0; it < 4; ++it) {
        const int oc = oc0 + 64 * it, tk = (it * 32 + k4) * 16 + w;
        v4u kr[11], vr[8], xg[4][2];
#pragma unroll
        for (int j = 0; j < 11; ++j) kr[j] = kn[j];
#pragma unroll
        for (int i = 0; i < 8; ++i) vr[i] = vn[i];
        __builtin_amdgcn_sched_barrier(0);
#pragma unroll
        for (int i = 0; i < 4; ++i)
#pragma unroll
            for (int q = 0; q < 2; ++q) xg[i][q] = *(const v4u*)(p.Aq + (size_t)(tk + 4 * i) * D + q * 512 + l * 8);
        fetch(it < 3 ? oc + 64 : 4096);
        __builtin_amdgcn_sched_barrier(0);
#pragma unroll
        for (int c8 = 0; c8 < 8; ++c8) {
            const unsigned s01 = sk[c8 >> 1][(c8 & 1) * 2], s23 = sk[c8 >> 1][(c8 & 1) * 2 + 1];
            const float sv[4] = {hi16(s23), lo16(s23), hi16(s01), lo16(s01)};
            float kk[11], u[8];
#pragma unroll
            for (int j = 0; j < 11; ++j) kk[j] = (c8 & 1) ? hi16(kr[j][c8 >> 1]) : lo16(kr[j][c8 >> 1]);
            f32x2 acc[4];
#pragma unroll
            for (int j = 0; j < 4; ++j)
#pragma unroll
                for (int i = 0; i < 4; ++i) { if (FASTP) { const f32x2 t = (f32x2){kk[i + j], kk[i + j + 4]} * sv[j]; acc[i] = j == 0 ? t : j == 3 ? rbf2(acc[i] + t) : acc[i] + t; }
                    else { const f32x2 t = rbf2((f32x2){kk[i + j], kk[i + j + 4]} * sv[j]); acc[i] = j == 0 ? t : rbf2(acc[i] + t); } }
#pragma unroll
            for (int i = 0; i < 8; ++i) u[i] = acc[i & 3][i >> 2] * ((c8 & 1) ? hi16(vr[i][c8 >> 1]) : lo16(vr[i][c8 >> 1]));
            st16(r, W_U + (cu * 8 + c8) * 4096 + oc * 16, (v4u){pack2(u[7], u[6]), pack2(u[5], u[4]), pack2(u[3], u[2]), pack2(u[1], u[0])});
        }
        float ga[4];
#pragma unroll
        for (int i = 0; i < 4; ++i) { float acc = 0.f;
#pragma unroll
            for (int q = 0; q < 2; ++q)
#pragma unroll
                for (int e = 0; e < 4; ++e) { const unsigned xa = xg[i][q][e], gb = gw[q][e]; acc += rbfh(lo16(xa) * lo16(gb)); acc += rbfh(hi16(xa) * hi16(gb)); }
            ga[i] = acc; }
        for (int o = 32; o > 0; o >>= 1)
#pragma unroll
            for (int i = 0; i < 4; ++i) ga[i] += __shfl_xor(ga[i], o);
        const float z = rbfh(l == 0 ? ga[0] : l == 1 ? ga[1] : l == 2 ? ga[2] : ga[3]);
        st4(r, l < 4 ? W_GATE + (tk + 4 * l) * 4 : 0xF0000000u, __float_as_uint(rbfh(1.f / (1.f + expf(-z)))));
    }
    DRAIN(); __syncthreads();
    if (tid == 0) st4(r, F_PREP + k4 * 4, tag);
}
__device__ __forceinline__ bool conv_block(const Params& p, rsrc_t r, const int g0, const int n, unsigned char* lds, const int wv, const int j, const unsigned tag, const unsigned skew) {
    (void)j;
    const int tid = otid(), w = wv, l = tid & 63;
    const rsrc_t rs = __builtin_amdgcn_make_buffer_rsrc((void*)p.ssm_k, 0, S * D * 2, 0x00020000);
    v4u kr[4], uv[4]; v2u kx[4]; unsigned dn;
    auto fetch_k = [&](int ch) {
#pragma unroll
        for (int i = 0; i < 4; ++i) { const unsigned o = (unsigned)(ch * 4096 + (i * 64 + l) * 16);
            kr[i] = __builtin_amdgcn_raw_buffer_load_b128(rs, o, 0, 2); kx[i] = __builtin_amdgcn_raw_buffer_load_b64(rs, (i == 3 && l == 63) ? 0xF0000000u : o + 16, 0, 2); }
        dn = p.ssm_D[ch & 1023]; };
    auto fetch_u = [&](unsigned o) {
#pragma unroll
        for (int i = 0; i < 4; ++i) uv[i] = ld16(r, o + (i * 64 + l) * 16); };
    fetch_k(g0 * 4 + w);
    unsigned char* const uc = lds + w * CV_U; unsigned char* const kc = lds + CV_K0 + w * 4 * CV_KP;
    {
        const v4u z = {0u, 0u, 0u, 0u};
#pragma unroll
        for (int i = 0; i < 3; ++i) *(v4u*)(uc + min(i * 64 + l, 159) * 16) = z;
#pragma unroll
        for (int i = 0; i < 2; ++i) { const int q = min(i * 64 + l, 79), e = q % 20; *(v4u*)(kc + (q / 20) * CV_KP + e * 16 + (e < 8 ? 0 : 4096)) = z; }
    }
    if (!wait_prep(r, l, tag)) return false;
    wait_long(rtnow() + skew);
    fetch_u(W_U + (g0 * 4 + w) * 4096);
    const int b = l & 31, kg = l >> 5, cc = (b + 1) & 3;
#pragma unroll 1
    for (int it = 0; it < n; ++it) {
        const unsigned dv = dn;
#pragma unroll
        for (int i = 0; i < 4; ++i) { const v4u d = kr[i]; const v2u e = kx[i]; unsigned char* q = kc + 128 + (i * 64 + l) * 16;
            const unsigned s0 = __builtin_amdgcn_alignbit(d.y, d.x, 16), s1 = __builtin_amdgcn_alignbit(d.z, d.y, 16), s2 = __builtin_amdgcn_alignbit(d.w, d.z, 16), s3 = __builtin_amdgcn_alignbit(e.x, d.w, 16), s4 = __builtin_amdgcn_alignbit(e.y, e.x, 16);
            *(v4u*)q = d; *(v4u*)(q + CV_KP) = (v4u){s0, s1, s2, s3}; *(v4u*)(q + 2 * CV_KP) = (v4u){d.y, d.z, d.w, e.x}; *(v4u*)(q + 3 * CV_KP) = (v4u){s1, s2, s3, s4};
            if (i == 0 && l == 0) { *(unsigned*)(q + CV_KP - 4) = d.x << 16; *(unsigned*)(q + 2 * CV_KP - 4) = d.x; *(v2u*)(q + 3 * CV_KP - 8) = (v2u){d.x << 16, s0}; } }
#pragma unroll
        for (int i = 0; i < 4; ++i) *(v4u*)(uc + (32 + i * 16 + (l >> 2)) * CV_RP + (l & 3) * 16) = uv[i];
        __builtin_amdgcn_wave_barrier();
        __builtin_amdgcn_sched_barrier(0);
        fetch_k(it + 1 < n ? (g0 + it + 1) * 4 + w : 1024); fetch_u(it + 1 < n ? W_U + ((g0 + it + 1) * 4 + w) * 4096 : 0xF0000000u);
        __builtin_amdgcn_sched_barrier(0);
        f32x16 acc0, acc1, ad0, ad1;
#pragma unroll
        for (int i = 0; i < 16; ++i) { acc0[i] = 0.f; acc1[i] = 0.f; ad0[i] = 0.f; ad1[i] = 0.f; }
        typedef const __attribute__((address_space(3))) v2u* l2p;
        unsigned kb = (unsigned)(size_t)(kc + cc * CV_KP + (b - 8 * kg - 7 + 64 - cc) * 2) - 32, kb8 = kb + 8;
        asm volatile("" : "+v"(kb), "+v"(kb8));
        const unsigned char* ub = uc + (32 + b) * CV_RP + kg * 16;
        v2u ka[4][2], kz[4][2]; bf16x8 fa[4], fz[4];
        auto ld_k = [&](int set, int un, int hi) { const int dl = un >> 1, kh = un & 1; const unsigned o = (unsigned)((32 * dl - 16 * kh + 16) * 2 + hi * 2048);
            if (hi) { kz[set][0] = *(l2p)(size_t)(kb + o); kz[set][1] = *(l2p)(size_t)(kb8 + o); } else { ka[set][0] = *(l2p)(size_t)(kb + o); ka[set][1] = *(l2p)(size_t)(kb8 + o); } };
        auto ld_f = [&](int set, int un) { const int dl = un >> 1, kh = un & 1;
            fa[set] = *(const bf16x8*)(ub + (32 - dl) * CV_RP + kh * 32); fz[set] = *(const bf16x8*)(ub - dl * CV_RP + kh * 32); };
#pragma unroll
        for (int q = 0; q < 2; ++q) { ld_k(q, q, 0); ld_f(q, q); ld_k(q, q, 1); }
#pragma unroll
        for (int kh = 0; kh < 2; ++kh) { const int e = 7 - (b - 16 * kh - 8 * kg); const unsigned x = dv << ((e & 1) * 16); const int q = e >> 1;
            const v4u di = {q == 0 ? x : 0u, q == 1 ? x : 0u, q == 2 ? x : 0u, q == 3 ? x : 0u};
            asm volatile("s_nop 1\n\tv_mfma_f32_32x32x16_bf16 %0, %2, %4, %0\n\tv_mfma_f32_32x32x16_bf16 %1, %3, %4, %1" : "+v"(ad1), "+v"(ad0) : "v"(fa[kh]), "v"(fz[kh]), "v"(di)); }
#pragma unroll 1
        for (int u0 = 0; u0 < 64; u0 += 4) {
#pragma unroll
            for (int q = 0; q < 4; ++q) {
                const int un = u0 + q + 2, ns = (q + 2) & 3;
                const v4u kav = {ka[q][0].x, ka[q][0].y, ka[q][1].x, ka[q][1].y}, kzv = {kz[q][0].x, kz[q][0].y, kz[q][1].x, kz[q][1].y};
                __builtin_amdgcn_sched_barrier(0);
                asm volatile("s_nop 1\n\tv_mfma_f32_32x32x16_bf16 %0, %1, %2, %0" : "+v"(acc1) : "v"(fa[q]), "v"(kav));
                ld_k(ns, un, 0);
                __builtin_amdgcn_sched_barrier(0);
                asm volatile("v_mfma_f32_32x32x16_bf16 %0, %1, %2, %0" : "+v"(acc0) : "v"(fz[q]), "v"(kav));
                ld_f(ns, un);
                __builtin_amdgcn_sched_barrier(0);
                asm volatile("v_mfma_f32_32x32x16_bf16 %0, %1, %2, %0" : "+v"(acc1) : "v"(fz[q]), "v"(kzv));
                ld_k(ns, un, 1);
            }
        }
        asm volatile("s_nop 7\n\ts_nop 7" : "+v"(acc0), "+v"(acc1), "+v"(ad0), "+v"(ad1));
        unsigned char* const yb = lds + CV_Y + (it & 1) * 16384;
#pragma unroll
        for (int i = 0; i < 32; i += 2) { const int row = (i >> 4) * 32 + (i & 3) + 8 * ((i & 15) >> 2) + 4 * kg;
            const unsigned y = pack2(rbfh(i < 16 ? acc0[i] : acc1[i - 16]) + rbfh(i < 16 ? ad0[i] : ad1[i - 16]), rbfh(i < 16 ? acc0[i + 1] : acc1[i - 15]) + rbfh(i < 16 ? ad0[i + 1] : ad1[i - 15]));
            *(unsigned short*)(yb + (row * 32 + b) * 8 + w * 2) = (unsigned short)y; *(unsigned short*)(yb + (row * 32 + 32 + b) * 8 + w * 2) = (unsigned short)(y >> 16); }
        asm volatile("s_waitcnt lgkmcnt(0)\n\ts_barrier" ::: "memory");
#pragma unroll
        for (int i = 0; i < 4; ++i) { const int t = tid + 256 * i; st16(r, W_A + (g0 + it) * 16384 + t * 16, *(const v4u*)(yb + t * 16)); }
    }
    DRAIN(); __syncthreads();
    if (tid == 0) __hip_atomic_fetch_add((unsigned*)(p.ws + F_CONV), (unsigned)n, __ATOMIC_RELAXED, __HIP_MEMORY_SCOPE_AGENT);
    return true;
}

__device__ __forceinline__ void dma16(unsigned ldsdst, unsigned voff, const v4u& srd, unsigned soff) {
    asm volatile("s_mov_b32 m0, %0\n\ts_nop 0\n\tbuffer_load_dwordx4 %1, %2, %3 offen lds" :: "s"(ldsdst), "v"(voff), "s"(srd), "s"(soff) : "m0", "memory"); }
__device__ __forceinline__ void dma16n(unsigned ldsdst, unsigned voff, const v4u& srd, unsigned soff) {
    asm volatile("s_mov_b32 m0, %0\n\ts_nop 0\n\tbuffer_load_dwordx4 %1, %2, %3 offen nt lds" :: "s"(ldsdst), "v"(voff), "s"(srd), "s"(soff) : "m0", "memory"); }
__device__ __forceinline__ v4u pack_acc(const f32x4& a, const f32x4& b) {
    float x0, x1, x2, x3, y0, y1, y2, y3;
    asm volatile("v_accvgpr_read_b32 %0, %8\n\tv_accvgpr_read_b32 %1, %9\n\tv_accvgpr_read_b32 %2, %10\n\tv_accvgpr_read_b32 %3, %11\n\t"
                 "v_accvgpr_read_b32 %4, %12\n\tv_accvgpr_read_b32 %5, %13\n\tv_accvgpr_read_b32 %6, %14\n\tv_accvgpr_read_b32 %7, %15"
                 : "=v"(x0), "=v"(x1), "=v"(x2), "=v"(x3), "=v"(y0), "=v"(y1), "=v"(y2), "=v"(y3)
                 : "a"(a[0]), "a"(a[1]), "a"(a[2]), "a"(a[3]), "a"(b[0]), "a"(b[1]), "a"(b[2]), "a"(b[3]));
    return (v4u){pack2(x0, x1), pack2(x2, x3), pack2(y0, y1), pack2(y2, y3)}; }
#define SB() __builtin_amdgcn_sched_barrier(0)
#define WO_REGS "a128", "a129", "a130", "a131", "a132", "a133", "a134", "a135", "a136", "a137", "a138", "a139", "a140", "a141", "a142", "a143", "a144", "a145", "a146", "a147", "a148", "a149", "a150", "a151", "a152", "a153", "a154", "a155", "a156", "a157", "a158", "a159", "a160", "a161", "a162", "a163", "a164", "a165", "a166", "a167", "a168", "a169", "a170", "a171", "a172", "a173", "a174", "a175", "a176", "a177", "a178", "a179", "a180", "a181", "a182", "a183", "a184", "a185", "a186", "a187", "a188", "a189", "a190", "a191", "a192", "a193", "a194", "a195", "a196", "a197", "a198", "a199", "a200", "a201", "a202", "a203", "a204", "a205", "a206", "a207", "a208", "a209", "a210", "a211", "a212", "a213", "a214", "a215", "a216", "a217", "a218", "a219", "a220", "a221", "a222", "a223", "a224", "a225", "a226", "a227", "a228", "a229", "a230", "a231", "a232", "a233", "a234", "a235", "a236", "a237", "a238", "a239", "a240", "a241", "a242", "a243", "a244", "a245", "a246", "a247", "a248", "a249", "a250", "a251", "a252", "a253", "a254", "a255", "v192", "v193", "v194", "v195", "v196", "v197", "v198", "v199", "v200", "v201", "v202", "v203", "v204", "v205", "v206", "v207", "v208", "v209", "v210", "v211", "v212", "v213", "v214", "v215", "v216", "v217", "v218", "v219", "v220", "v221", "v222", "v223", "v224", "v225", "v226", "v227", "v228", "v229", "v230", "v231", "v232", "v233", "v234", "v235", "v236", "v237", "v238", "v239", "v240", "v241", "v242", "v243", "v244", "v245", "v246", "v247", "v248", "v249", "v250", "v251", "v252", "v253", "v254", "v255"
struct NoHook { __device__ void operator()(int) const {} };
template <int MODE, int MI, int NJ, int NS, int HX = 0, class HOOK = NoHook, int MN = MI>
__device__ __forceinline__ void gemm(rsrc_t r, const bf16_t* __restrict__ A, const bf16_t* __restrict__ B, int m0, int n0, unsigned char* lds, unsigned coff, const int wv,
                                     const int rot, const void* tk = nullptr,
                                     const bool have = false, const bf16_t* Bn = nullptr, int m0n = 0, int n0n = 0, HOOK&& hook = NoHook()) {
    constexpr int AB = MN * 4096, BB = NJ * 2048, B0 = (NS / 2) * AB, NB = NJ / 2, NM = MI * NJ, NR = MI + NJ, BP = NJ * 64;
    static_assert(NS % 2 == 0 && 32 % NS == 0 && B0 <= 65536 && NS * BB <= 65536 && (NJ == 4 || NJ == 8), "ring");
    const int tid = otid();
    const int w = wv, l = tid & 63, n16 = l & 15, g = l >> 4;
    const int wm = w >> 1, wn = w & 1;
    const unsigned lb = __builtin_amdgcn_readfirstlane((unsigned)(size_t)lds);
    const v4u sa = mk_srd(A, 2048 * 1024 * 2); v4u sb = mk_srd(B, 1024 * 4096 * 2);
    f32x4 acc[MI][NJ];
#pragma unroll
    for (int i = 0; i < MI; ++i)
#pragma unroll
        for (int j = 0; j < NJ; ++j) acc[i][j] = (f32x4){0.f, 0.f, 0.f, 0.f};
    unsigned va = (unsigned)((m0 + 8 * MI * w + (l >> 3)) * 2048 + (((l & 7) ^ (l >> 3)) << 4));
    unsigned vb[2];
    if (NJ == 8) { vb[0] = (unsigned)((8 * w + (l >> 5)) * 8192 + n0 * 2) + (unsigned)(((l & 31) << 4) ^ ((l >> 5) << 5) ^ (wn << 7)); vb[1] = vb[0] ^ 64u; }
    else { vb[0] = (unsigned)((8 * w + g) * 8192 + n0 * 2) + (unsigned)((n16 << 4) ^ (g << 5) ^ (wn << 7)); vb[1] = vb[0]; }
    auto rk = [&](int kt) { return (kt & ~31) | ((kt + rot) & 31); };
    unsigned la = lb + w * MI * 1024;
    auto st_a = [&](int buf, int kt, int q) { dma16(la + buf * AB + q * 1024, va, sa, (unsigned)(rk(kt) * 64 + q * 16384)); };
    auto st_b = [&](int buf, int kt, int x) { if ((B_NT >> (NJ == 4 ? 0 : MI == 4 ? 1 : 2)) & 1) dma16n(lb + B0 + buf * BB + (w * NB + x) * 1024, vb[x & 1], sb, (unsigned)((rk(kt) * 32 + x * (8 / NB)) * 8192));
        else dma16(lb + B0 + buf * BB + (w * NB + x) * 1024, vb[x & 1], sb, (unsigned)((rk(kt) * 32 + x * (8 / NB)) * 8192)); };
    const unsigned f = (unsigned)((n16 >> 2) | ((g & 1) << 2));
    constexpr int NQ = MODE == 1 ? NJ : NJ / 2;
    unsigned ra[2], rb[NQ];
    ra[0] = lb + (16 * MI * wm + n16) * 128 + ((g ^ (n16 & 7)) << 4); ra[1] = ra[0] ^ 64u;
    {   const unsigned row = lb + B0 + (8 * g + (n16 >> 2)) * BP + (NJ == 8 ? 256 * wn : 0), g0 = NJ == 8 ? 0u : 4u * wn;
#pragma unroll
        for (int j = 0; j < NQ; ++j) {
            if (MODE == 1) rb[j] = row + (((g0 + j) ^ f) << 5) + 8 * (n16 & 3);
            else rb[j] = row + (((g0 + 2 * j + ((n16 & 3) >> 1)) ^ f) << 5) + (n16 & 1) * 16;
            asm volatile("" : "+v"(rb[j])); } }
    bf16x8 af[2][MI], bfr[2][NJ];
    auto rd = [&](int set, int buf, int x) {
        if (x < NJ) { const unsigned a = rb[MODE == 1 ? x : (x >> 1)] + (MODE == 1 ? 0 : (x & 1) * 8) + buf * BB;
            const v2u lo = trb(a), hi = trb(a + 4 * BP); v4u v = {lo.x, lo.y, hi.x, hi.y}; bfr[set][x] = as_frag(v); }
        else af[set][x - NJ] = ldsa(ra[buf & 1] + (buf >> 1) * AB + (x - NJ) * 2048); };
    auto mf = [&](int set, int i, int j) {
        if (HX) asm volatile("v_mfma_f32_16x16x32_bf16 %0, %1, %2, %0" : "+a"(acc[i][j]) : "v"(MODE == 1 ? af[set][i] : bfr[set][j]), "v"(MODE == 1 ? bfr[set][j] : af[set][i]) : WO_REGS);
        else if (MODE == 1) asm volatile("v_mfma_f32_16x16x32_bf16 %0, %1, %2, %0" : "+a"(acc[i][j]) : "v"(af[set][i]), "v"(bfr[set][j]));
        else asm volatile("v_mfma_f32_16x16x32_bf16 %0, %1, %2, %0" : "+a"(acc[i][j]) : "v"(bfr[set][j]), "v"(af[set][i])); };
    if (!have) {
#pragma unroll
        for (int t = 0; t < NS; ++t) {
            if (t & 1) {
#pragma unroll
                for (int q = 0; q < MI; ++q) st_a(t >> 1, t - 1, q); }
#pragma unroll
            for (int q = 0; q < NB; ++q) st_b(t, t, q);
            if (HX) hook(t); }
    }
#pragma unroll
    for (int i = 0; i < MI; ++i)
#pragma unroll
        for (int j = 0; j < NJ; ++j) asm volatile("" : "+a"(acc[i][j]));
    asm volatile("s_nop 1" ::: "memory");
    if (!have) {
        asm volatile("s_waitcnt vmcnt(%c0)\n\ts_barrier" :: "i"((NS - 1) * (NB + HX) + (NS / 2 - 1) * MI) : "memory");
    }
#pragma unroll
    for (int x = 0; x < NR; ++x) rd(0, 0, x);
    constexpr int HEAD = NM / 8;
    const v4u st = mk_srd(tk, tk ? 32768 : 0); unsigned tdum = 0u;
#pragma unroll 1
    for (int k0 = 0; k0 < 32; k0 += NS) {
        int kb = k0 + NS;
        if (kb == 32) {
            kb = Bn ? 0 : 0x10100;
            if (Bn) { va += (unsigned)((m0n - m0 + 8 * (MN - MI) * w) * 2048); la = lb + w * MN * 1024; vb[0] += (unsigned)((n0n - n0) * 2); vb[1] += (unsigned)((n0n - n0) * 2); sb = mk_srd(Bn, 1024 * 4096 * 2); } }
        if (NS == 4) asm volatile("buffer_load_dword %0, %1, %2, %3 offen" : "+v"(tdum) : "v"(l < 32 ? (unsigned)l * 128u : 0xF0000000u), "s"(st), "s"((unsigned)k0 * 1024u) : "memory");
#pragma unroll
        for (int u = 0; u < NS; ++u) {
            SB();
#pragma unroll
            for (int q = 0; q < HEAD; ++q) mf(u & 1, q / NJ, q % NJ);
            SB();
            asm volatile("s_waitcnt vmcnt(%c0) lgkmcnt(0)\n\ts_barrier" :: "i"((NS - 2) * (NB + HX) + (NS / 2 - 1 - (u & 1)) * MI) : "memory");
            SB();
            constexpr int span = NM - HEAD;
#pragma unroll
            for (int q = HEAD; q < NM; ++q) {
                mf(u & 1, q / NJ, q % NJ);
                const int x = q - HEAD, nd = NB + ((u & 1) ? MI : 0);
#pragma unroll
                for (int y = 0; y < NR; ++y) if ((y * (span - HEAD)) / NR == x) rd((u & 1) ^ 1, (u + 1) % NS, y);
#pragma unroll
                for (int y = 0; y < nd; ++y) if ((y * span + span / 2) / nd == x) { if ((u & 1) && y < MI) st_a(u >> 1, kb + u - 1, y); else st_b(u, kb + u, y - ((u & 1) ? MI : 0)); }
                SB(); }
            if (MN > MI && (u & 1) && k0 == 32 - NS) {
#pragma unroll
                for (int y = MI; y < MN; ++y) st_a(u >> 1, kb + u - 1, y); }
            if (HX) hook(NS + k0 + u);
        }
    }
    asm volatile("s_nop 7\n\ts_nop 3" : "+v"(tdum) :: "memory");
    SB();
    const int col = n0 + 16 * NJ * wn, head = col >> 7, sub = (col & 127) >> 4, seg0 = (m0 >> 5) + (MI / 2) * wm;
#pragma unroll
    for (int sg = 0; sg < MI / 2; ++sg) {
        const unsigned base = coff + (unsigned)(((seg0 + sg) * 32 + head) * 8192 + l * 16);
#pragma unroll
        for (int q = 0; q < NJ; ++q) {
            if (HX) asm volatile("" ::: WO_REGS);
            if (EPI_SLP && NJ == 8) { SB(); __builtin_amdgcn_s_sleep(EPI_SLP); SB(); }
            if (MODE == 0) { const int tt = q / (NJ / 2), kk = q % (NJ / 2); st16(r, base + (tt * 4 + (sub >> 1) + kk) * 1024, pack_acc(acc[2 * sg + tt][2 * kk], acc[2 * sg + tt][2 * kk + 1])); }
            else st16(r, base + (sub + q) * 1024, pack_acc(acc[2 * sg][q], acc[2 * sg + 1][q])); }
    }
}

__device__ __forceinline__ void attn_block(const Params& p, const int wv, const int h, const unsigned tag) {
    const int l = otid() & 63, n16 = l & 15, g = l >> 4;
    const unsigned OOB = 0x80000000u;
    const v4u rd = mk_srd(p.ws, W_TOTAL);
    const unsigned kx = W_KX + h * 8192, qx = W_QX + h * 8192, vx = W_VF + h * 8192, km = HO_KVM + h * 4096;
    const unsigned ox = W_OX + (h >> 4) * 65536 + (h & 3) * 16384 + ((h >> 2) & 3) * 1024;
    const v8u pc = {(unsigned)l * 16, (unsigned)(n16 * 16 + (g & 1) * 8), F_KVQ + ((l >> 2) * 32 + h) * 64 + min(l & 3, 2) * 4, F_PROG + h * 128 + min(l, 2) * 8, (unsigned)(wv * 2048 + l * 16),
                    (unsigned)(wv * 2048 + (8 * g + (n16 >> 2)) * 16 + (n16 & 1) * 8), (l & 31) ? OOB : F_ATT + (l >> 5) * 64, l < 32 ? F_RDY + l * 128 : OOB};
    const int dq = n16 - 4 * g;
    asm volatile(
        "s_mov_b32 s36, 0xf0000000\n\t"
        "s_mov_b32 s37, %[w]\n\t"
        "s_mov_b32 s38, s36\n\t"
        "s_mov_b32 s40, 0\n\t"
        "s_memrealtime s[98:99]\n\t"
        "s_waitcnt lgkmcnt(0)\n\t"
        "s_sub_u32 s41, s98, 0x100000\n\t"
        "s_mov_b64 s[42:43], 0\n\t"
        "s_mov_b32 s44, -1\n\t"
        "s_mov_b32 s45, -1\n\t"
        "s_mov_b32 s56, 0\n\t"
        "s_mov_b32 s57, -1\n\t"
        "s_lshl_b32 s58, %[w], 10\n\t"
        "s_add_u32 s58, s58, 16384\n\t"
        "v_cmp_le_i32_e64 s[48:49], 0, v252\n\t"
        "v_cmp_le_i32_e64 s[50:51], 1, v252\n\t"
        "v_cmp_le_i32_e64 s[52:53], 2, v252\n\t"
        "v_cmp_le_i32_e64 s[54:55], 3, v252\n\t"
        "v_mov_b32_e32 v253, 0xce6e6b28\n\t"
        "v_mov_b32_e32 v254, 0xff800000\n\t"
        ".Ltop%=:\n\t"
        "s_cmp_eq_u32 s38, 0\n\t"
        "s_cbranch_scc1 .La2%=\n\t"
        "s_mov_b32 s97, 0\n\t"
        ".La1%=:\n\t"
        "buffer_load_dword v240, v246, %[rd], 0 offen sc1\n\t"
        "buffer_load_dword v241, v247, %[rd], 0 offen sc1\n\t"
        "s_add_u32 s97, s97, 1\n\t"
        "s_cmp_lt_u32 s97, 0x200000\n\t"
        "s_cbranch_scc0 .Lgu%=\n\t"
        "s_memrealtime s[98:99]\n\t"
        "s_waitcnt vmcnt(0) lgkmcnt(0)\n\t"
        "v_cmp_eq_u32_e32 vcc, %[tag], v240\n\t"
        "s_nop 1\n\t"
        "s_or_b64 s[42:43], s[42:43], vcc\n\t"
        "v_readlane_b32 s80, v241, 0\n\t"
        "v_readlane_b32 s81, v241, 1\n\t"
        "v_readlane_b32 s82, v241, 2\n\t"
        "s_min_u32 s80, s80, s81\n\t"
        "s_min_u32 s80, s80, s82\n\t"
        "s_cmp_gt_u32 s80, s40\n\t"
        "s_cselect_b32 s41, s98, s41\n\t"
        "s_max_u32 s40, s40, s80\n\t"
        "s_and_b32 s80, s37, 60\n\t"
        "s_lshr_b64 s[80:81], s[42:43], s80\n\t"
        "s_andn2_b32 s80, 15, s80\n\t"
        "s_cbranch_scc0 .La1%=x\n\t"
        "s_sleep 16\n\t"
        "s_branch .La1%=\n\t"
        ".La1%=x:\n\t"
        "s_cmp_gt_u32 s40, s37\n\t"
        "s_cselect_b32 s39, 0, s36\n\t"
        "s_lshl_b32 s80, s37, 18\n\t"
        "s_or_b32 s80, s80, 0\n\t"
        "s_lshl_b32 s81, s37, 17\n\t"
        "s_add_u32 s81, s81, %[km]\n\t"
        "s_or_b32 s81, s81, s39\n\t"
        "s_add_u32 s82, s80, %[kx]\n\t"
        "s_add_u32 s83, s82, 0x1000\n\t"
        "buffer_load_dwordx4 v[0:3], v244, %[rd], s82 offen offset:0 sc1\n\t"
        "buffer_load_dwordx4 v[4:7], v244, %[rd], s82 offen offset:1024 sc1\n\t"
        "buffer_load_dwordx4 v[8:11], v244, %[rd], s82 offen offset:2048 sc1\n\t"
        "buffer_load_dwordx4 v[12:15], v244, %[rd], s82 offen offset:3072 sc1\n\t"
        "buffer_load_dwordx4 v[16:19], v244, %[rd], s83 offen offset:0 sc1\n\t"
        "buffer_load_dwordx4 v[20:23], v244, %[rd], s83 offen offset:1024 sc1\n\t"
        "buffer_load_dwordx4 v[24:27], v244, %[rd], s83 offen offset:2048 sc1\n\t"
        "buffer_load_dwordx4 v[28:31], v244, %[rd], s83 offen offset:3072 sc1\n\t"
        "s_cmp_eq_u64 s[42:43], -1\n\t"
        "s_cbranch_scc1 .La3%=\n\t"
        "s_sleep %c[aslp]\n\t"
        ".La3%=:\n\t"
        "s_sleep %c[bslp]\n\t"
        "s_add_u32 s82, s80, %[qx]\n\t"
        "s_add_u32 s83, s82, 0x1000\n\t"
        "buffer_load_dwordx4 v[32:35], v244, %[rd], s82 offen offset:0 sc1\n\t"
        "buffer_load_dwordx4 v[36:39], v244, %[rd], s82 offen offset:1024 sc1\n\t"
        "buffer_load_dwordx4 v[40:43], v244, %[rd], s82 offen offset:2048 sc1\n\t"
        "buffer_load_dwordx4 v[44:47], v244, %[rd], s82 offen offset:3072 sc1\n\t"
        "buffer_load_dwordx4 v[48:51], v244, %[rd], s83 offen offset:0 sc1\n\t"
        "buffer_load_dwordx4 v[52:55], v244, %[rd], s83 offen offset:1024 sc1\n\t"
        "buffer_load_dwordx4 v[56:59], v244, %[rd], s83 offen offset:2048 sc1\n\t"
        "buffer_load_dwordx4 v[60:63], v244, %[rd], s83 offen offset:3072 sc1\n\t"
        "s_cmp_eq_u64 s[42:43], -1\n\t"
        "s_cbranch_scc1 .La4%=\n\t"
        "s_sleep %c[aslp]\n\t"
        ".La4%=:\n\t"
        "s_sleep %c[bslp]\n\t"
        "buffer_load_dwordx4 v[96:99], v244, %[rd], s81 offen offset:0 sc1\n\t"
        "buffer_load_dwordx4 v[100:103], v244, %[rd], s81 offen offset:1024 sc1\n\t"
        "buffer_load_dwordx2 v[104:105], v245, %[rd], s81 offen offset:2048 sc1\n\t"
        "buffer_load_dwordx2 v[106:107], v245, %[rd], s81 offen offset:2304 sc1\n\t"
        "buffer_load_dwordx2 v[108:109], v245, %[rd], s81 offen offset:2560 sc1\n\t"
        "buffer_load_dwordx2 v[110:111], v245, %[rd], s81 offen offset:2816 sc1\n\t"
        "buffer_load_dwordx2 v[112:113], v245, %[rd], s81 offen offset:3072 sc1\n\t"
        "buffer_load_dwordx2 v[114:115], v245, %[rd], s81 offen offset:3328 sc1\n\t"
        "buffer_load_dwordx2 v[116:117], v245, %[rd], s81 offen offset:3584 sc1\n\t"
        "buffer_load_dwordx2 v[118:119], v245, %[rd], s81 offen offset:3840 sc1\n\t"
        "s_add_u32 s82, s80, %[vx]\n\t"
        "s_add_u32 s83, s82, 0x1000\n\t"
        "buffer_load_dwordx4 v[64:67], v244, %[rd], s82 offen offset:0 sc1\n\t"
        "buffer_load_dwordx4 v[68:71], v244, %[rd], s82 offen offset:1024 sc1\n\t"
        "buffer_load_dwordx4 v[72:75], v244, %[rd], s82 offen offset:2048 sc1\n\t"
        "buffer_load_dwordx4 v[76:79], v244, %[rd], s82 offen offset:3072 sc1\n\t"
        "buffer_load_dwordx4 v[80:83], v244, %[rd], s83 offen offset:0 sc1\n\t"
        "buffer_load_dwordx4 v[84:87], v244, %[rd], s83 offen offset:1024 sc1\n\t"
        "buffer_load_dwordx4 v[88:91], v244, %[rd], s83 offen offset:2048 sc1\n\t"
        "buffer_load_dwordx4 v[92:95], v244, %[rd], s83 offen offset:3072 sc1\n\t"
        "s_cmp_eq_u64 s[42:43], -1\n\t"
        "s_cbranch_scc1 .La5%=\n\t"
        "s_sleep %c[aslp]\n\t"
        ".La5%=:\n\t"
        "s_sleep %c[bslp]\n\t"
        "buffer_store_dword v0, off, %[rd], s36\n\t"
        "buffer_store_dword v0, off, %[rd], s36\n\t"
        "buffer_store_dword v0, off, %[rd], s36\n\t"
        "buffer_store_dword v0, off, %[rd], s36\n\t"
        "buffer_store_dword v0, off, %[rd], s36\n\t"
        "buffer_store_dword v0, off, %[rd], s36\n\t"
        "buffer_store_dword v0, off, %[rd], s36\n\t"
        "buffer_store_dword v0, off, %[rd], s36\n\t"
        "buffer_store_dword v0, off, %[rd], s36\n\t"
        ".La2%=:\n\t"
        "s_cmp_gt_u32 s40, s37\n\t"
        "s_cselect_b32 s80, s39, 0\n\t"
        "s_cmp_eq_u32 s80, 0\n\t"
        "s_cbranch_scc1 .La6%=\n\t"
        "s_mov_b32 s39, 0\n\t"
        "s_lshl_b32 s81, s37, 17\n\t"
        "s_add_u32 s81, s81, %[km]\n\t"
        "buffer_load_dwordx4 v[96:99], v244, %[rd], s81 offen offset:0 sc1\n\t"
        "buffer_load_dwordx4 v[100:103], v244, %[rd], s81 offen offset:1024 sc1\n\t"
        "buffer_load_dwordx2 v[104:105], v245, %[rd], s81 offen offset:2048 sc1\n\t"
        "buffer_load_dwordx2 v[106:107], v245, %[rd], s81 offen offset:2304 sc1\n\t"
        "buffer_load_dwordx2 v[108:109], v245, %[rd], s81 offen offset:2560 sc1\n\t"
        "buffer_load_dwordx2 v[110:111], v245, %[rd], s81 offen offset:2816 sc1\n\t"
        "buffer_load_dwordx2 v[112:113], v245, %[rd], s81 offen offset:3072 sc1\n\t"
        "buffer_load_dwordx2 v[114:115], v245, %[rd], s81 offen offset:3328 sc1\n\t"
        "buffer_load_dwordx2 v[116:117], v245, %[rd], s81 offen offset:3584 sc1\n\t"
        "buffer_load_dwordx2 v[118:119], v245, %[rd], s81 offen offset:3840 sc1\n\t"
        "buffer_store_dword v0, off, %[rd], s36\n\t"
        "buffer_store_dword v0, off, %[rd], s36\n\t"
        "buffer_store_dword v0, off, %[rd], s36\n\t"
        "buffer_store_dword v0, off, %[rd], s36\n\t"
        "buffer_store_dword v0, off, %[rd], s36\n\t"
        "buffer_store_dword v0, off, %[rd], s36\n\t"
        "buffer_store_dword v0, off, %[rd], s36\n\t"
        "buffer_store_dword v0, off, %[rd], s36\n\t"
        ".La6%=:\n\t"
        "s_add_u32 s86, s37, 4\n\t"
        "s_and_b32 s80, s86, 60\n\t"
        "s_lshr_b64 s[80:81], s[42:43], s80\n\t"
        "s_andn2_b32 s80, 15, s80\n\t"
        "s_cselect_b32 s46, s36, 0\n\t"
        "s_cmp_lt_u32 s86, 64\n\t"
        "s_cselect_b32 s46, s46, s36\n\t"
        "s_cmp_gt_u32 s40, s86\n\t"
        "s_cselect_b32 s47, s46, s36\n\t"
        "s_lshl_b32 s80, s86, 18\n\t"
        "s_or_b32 s80, s80, s46\n\t"
        "s_lshl_b32 s81, s86, 17\n\t"
        "s_add_u32 s81, s81, %[km]\n\t"
        "s_or_b32 s81, s81, s47\n\t"
        "s_add_u32 s82, s80, %[kx]\n\t"
        "s_add_u32 s83, s82, 0x1000\n\t"
        "buffer_load_dwordx4 a[0:3], v244, %[rd], s82 offen offset:0 sc1\n\t"
        "buffer_load_dwordx4 a[4:7], v244, %[rd], s82 offen offset:1024 sc1\n\t"
        "buffer_load_dwordx4 a[8:11], v244, %[rd], s82 offen offset:2048 sc1\n\t"
        "buffer_load_dwordx4 a[12:15], v244, %[rd], s82 offen offset:3072 sc1\n\t"
        "buffer_load_dwordx4 a[16:19], v244, %[rd], s83 offen offset:0 sc1\n\t"
        "buffer_load_dwordx4 a[20:23], v244, %[rd], s83 offen offset:1024 sc1\n\t"
        "buffer_load_dwordx4 a[24:27], v244, %[rd], s83 offen offset:2048 sc1\n\t"
        "buffer_load_dwordx4 a[28:31], v244, %[rd], s83 offen offset:3072 sc1\n\t"
        "s_cmp_eq_u64 s[42:43], -1\n\t"
        "s_cbranch_scc1 .La7%=\n\t"
        "s_sleep %c[aslp]\n\t"
        ".La7%=:\n\t"
        "s_sleep %c[bslp]\n\t"
        "s_add_u32 s82, s80, %[qx]\n\t"
        "s_add_u32 s83, s82, 0x1000\n\t"
        "buffer_load_dwordx4 a[32:35], v244, %[rd], s82 offen offset:0 sc1\n\t"
        "buffer_load_dwordx4 a[36:39], v244, %[rd], s82 offen offset:1024 sc1\n\t"
        "buffer_load_dwordx4 a[40:43], v244, %[rd], s82 offen offset:2048 sc1\n\t"
        "buffer_load_dwordx4 a[44:47], v244, %[rd], s82 offen offset:3072 sc1\n\t"
        "buffer_load_dwordx4 a[48:51], v244, %[rd], s83 offen offset:0 sc1\n\t"
        "buffer_load_dwordx4 a[52:55], v244, %[rd], s83 offen offset:1024 sc1\n\t"
        "buffer_load_dwordx4 a[56:59], v244, %[rd], s83 offen offset:2048 sc1\n\t"
        "buffer_load_dwordx4 a[60:63], v244, %[rd], s83 offen offset:3072 sc1\n\t"
        "s_cmp_eq_u64 s[42:43], -1\n\t"
        "s_cbranch_scc1 .La8%=\n\t"
        "s_sleep %c[aslp]\n\t"
        ".La8%=:\n\t"
        "s_sleep %c[bslp]\n\t"
        "buffer_load_dwordx4 a[96:99], v244, %[rd], s81 offen offset:0 sc1\n\t"
        "buffer_load_dwordx4 a[100:103], v244, %[rd], s81 offen offset:1024 sc1\n\t"
        "buffer_load_dwordx2 a[104:105], v245, %[rd], s81 offen offset:2048 sc1\n\t"
        "buffer_load_dwordx2 a[106:107], v245, %[rd], s81 offen offset:2304 sc1\n\t"
        "buffer_load_dwordx2 a[108:109], v245, %[rd], s81 offen offset:2560 sc1\n\t"
        "buffer_load_dwordx2 a[110:111], v245, %[rd], s81 offen offset:2816 sc1\n\t"
        "buffer_load_dwordx2 a[112:113], v245, %[rd], s81 offen offset:3072 sc1\n\t"
        "buffer_load_dwordx2 a[114:115], v245, %[rd], s81 offen offset:3328 sc1\n\t"
        "buffer_load_dwordx2 a[116:117], v245, %[rd], s81 offen offset:3584 sc1\n\t"
        "buffer_load_dwordx2 a[118:119], v245, %[rd], s81 offen offset:3840 sc1\n\t"
        "s_add_u32 s82, s80, %[vx]\n\t"
        "s_add_u32 s83, s82, 0x1000\n\t"
        "buffer_load_dwordx4 a[64:67], v244, %[rd], s82 offen offset:0 sc1\n\t"
        "buffer_load_dwordx4 a[68:71], v244, %[rd], s82 offen offset:1024 sc1\n\t"
        "buffer_load_dwordx4 a[72:75], v244, %[rd], s82 offen offset:2048 sc1\n\t"
        "buffer_load_dwordx4 a[76:79], v244, %[rd], s82 offen offset:3072 sc1\n\t"
        "buffer_load_dwordx4 a[80:83], v244, %[rd], s83 offen offset:0 sc1\n\t"
        "buffer_load_dwordx4 a[84:87], v244, %[rd], s83 offen offset:1024 sc1\n\t"
        "buffer_load_dwordx4 a[88:91], v244, %[rd], s83 offen offset:2048 sc1\n\t"
        "buffer_load_dwordx4 a[92:95], v244, %[rd], s83 offen offset:3072 sc1\n\t"
        "s_cmp_eq_u64 s[42:43], -1\n\t"
        "s_cbranch_scc1 .La9%=\n\t"
        "s_sleep %c[aslp]\n\t"
        ".La9%=:\n\t"
        "s_sleep %c[bslp]\n\t"
        "s_waitcnt vmcnt(42)\n\t"
        "s_memrealtime s[98:99]\n\t"
        "v_mfma_f32_16x16x32_bf16 v[136:139], v[0:3], v[32:35], 0\n\t"
        "v_mfma_f32_16x16x32_bf16 v[148:151], v[0:3], v[48:51], 0\n\t"
        "v_mfma_f32_16x16x32_bf16 v[152:155], v[16:19], v[48:51], 0\n\t"
        "v_mfma_f32_16x16x32_bf16 v[136:139], v[4:7], v[36:39], v[136:139]\n\t"
        "v_mfma_f32_16x16x32_bf16 v[148:151], v[4:7], v[52:55], v[148:151]\n\t"
        "v_mfma_f32_16x16x32_bf16 v[152:155], v[20:23], v[52:55], v[152:155]\n\t"
        "v_mfma_f32_16x16x32_bf16 v[136:139], v[8:11], v[40:43], v[136:139]\n\t"
        "v_mfma_f32_16x16x32_bf16 v[148:151], v[8:11], v[56:59], v[148:151]\n\t"
        "v_mfma_f32_16x16x32_bf16 v[152:155], v[24:27], v[56:59], v[152:155]\n\t"
        "v_mfma_f32_16x16x32_bf16 v[136:139], v[12:15], v[44:47], v[136:139]\n\t"
        "v_mfma_f32_16x16x32_bf16 v[148:151], v[12:15], v[60:63], v[148:151]\n\t"
        "v_mfma_f32_16x16x32_bf16 v[152:155], v[28:31], v[60:63], v[152:155]\n\t"
        "s_waitcnt lgkmcnt(0)\n\t"
        "v_cmp_eq_u32_e32 vcc, %[tag], v240\n\t"
        "s_nop 1\n\t"
        "s_or_b64 s[42:43], s[42:43], vcc\n\t"
        "v_readlane_b32 s80, v241, 0\n\t"
        "v_readlane_b32 s81, v241, 1\n\t"
        "v_readlane_b32 s82, v241, 2\n\t"
        "s_min_u32 s80, s80, s81\n\t"
        "s_min_u32 s80, s80, s82\n\t"
        "s_cmp_gt_u32 s80, s40\n\t"
        "s_cselect_b32 s41, s98, s41\n\t"
        "s_max_u32 s40, s40, s80\n\t"
        "v_readlane_b32 s84, v242, 0\n\t"
        "v_readlane_b32 s85, v242, 32\n\t"
        "v_mov_b32_e32 v243, 32\n\t"
        "s_and_b32 s80, s45, 3\n\t"
        "s_lshl_b32 s80, s80, 12\n\t"
        "s_lshr_b32 s81, s45, 2\n\t"
        "s_lshl_b32 s81, s81, 3\n\t"
        "s_add_u32 s80, s80, s81\n\t"
        "s_and_b32 s81, s45, 1\n\t"
        "s_lshl_b32 s81, s81, 13\n\t"
        "s_lshr_b32 s82, s45, 1\n\t"
        "s_lshl_b32 s82, s82, 2\n\t"
        "s_add_u32 s81, s81, s82\n\t"
        "s_cmp_lt_i32 s45, 60\n\t"
        "s_cselect_b32 s80, s80, s81\n\t"
        "s_cselect_b32 s81, 4, 0x1000\n\t"
        "s_add_u32 s81, s81, s80\n\t"
        "s_cmp_eq_u32 s84, 31\n\t"
        "s_cselect_b32 s80, s80, s36\n\t"
        "s_cmp_eq_u32 s85, 31\n\t"
        "s_cselect_b32 s81, s81, s36\n\t"
        "s_cmp_lt_i32 s45, 0\n\t"
        "s_cselect_b32 s80, s36, s80\n\t"
        "s_cselect_b32 s81, s36, s81\n\t"
        "buffer_store_dword v243, v251, %[rd], s80 offen sc1\n\t"
        "buffer_store_dword v243, v251, %[rd], s81 offen sc1\n\t"
        "s_add_u32 s86, s37, 8\n\t"
        "s_cmp_lg_u64 s[42:43], -1\n\t"
        "s_cselect_b32 s84, 0, s36\n\t"
        "s_cmp_gt_u32 s40, s86\n\t"
        "s_cselect_b32 s85, s36, s39\n\t"
        "buffer_load_dword v240, v246, %[rd], s84 offen sc1\n\t"
        "buffer_load_dword v241, v247, %[rd], s85 offen sc1\n\t"
        "v_cvt_pk_bf16_f32 v136, 0, v136\n\t"
        "v_cvt_pk_bf16_f32 v137, 0, v137\n\t"
        "v_cvt_pk_bf16_f32 v138, 0, v138\n\t"
        "v_cvt_pk_bf16_f32 v139, 0, v139\n\t"
        "v_mul_f32_e32 v136, 0x3db504f3, v136\n\t"
        "v_mul_f32_e32 v137, 0x3db504f3, v137\n\t"
        "v_mul_f32_e32 v138, 0x3db504f3, v138\n\t"
        "v_mul_f32_e32 v139, 0x3db504f3, v139\n\t"
        "v_cvt_pk_bf16_f32 v136, 0, v136\n\t"
        "v_cvt_pk_bf16_f32 v137, 0, v137\n\t"
        "v_cvt_pk_bf16_f32 v138, 0, v138\n\t"
        "v_cvt_pk_bf16_f32 v139, 0, v139\n\t"
        "v_cndmask_b32_e64 v136, v253, v136, s[48:49]\n\t"
        "v_cndmask_b32_e64 v137, v253, v137, s[50:51]\n\t"
        "v_cndmask_b32_e64 v138, v253, v138, s[52:53]\n\t"
        "v_cndmask_b32_e64 v139, v253, v139, s[54:55]\n\t"
        "v_cvt_pk_bf16_f32 v148, 0, v148\n\t"
        "v_cvt_pk_bf16_f32 v149, 0, v149\n\t"
        "v_cvt_pk_bf16_f32 v150, 0, v150\n\t"
        "v_cvt_pk_bf16_f32 v151, 0, v151\n\t"
        "v_mul_f32_e32 v148, 0x3db504f3, v148\n\t"
        "v_mul_f32_e32 v149, 0x3db504f3, v149\n\t"
        "v_mul_f32_e32 v150, 0x3db504f3, v150\n\t"
        "v_mul_f32_e32 v151, 0x3db504f3, v151\n\t"
        "v_cvt_pk_bf16_f32 v148, 0, v148\n\t"
        "v_cvt_pk_bf16_f32 v149, 0, v149\n\t"
        "v_cvt_pk_bf16_f32 v150, 0, v150\n\t"
        "v_cvt_pk_bf16_f32 v151, 0, v151\n\t"
        "v_cvt_pk_bf16_f32 v152, 0, v152\n\t"
        "v_cvt_pk_bf16_f32 v153, 0, v153\n\t"
        "v_cvt_pk_bf16_f32 v154, 0, v154\n\t"
        "v_cvt_pk_bf16_f32 v155, 0, v155\n\t"
        "v_mul_f32_e32 v152, 0x3db504f3, v152\n\t"
        "v_mul_f32_e32 v153, 0x3db504f3, v153\n\t"
        "v_mul_f32_e32 v154, 0x3db504f3, v154\n\t"
        "v_mul_f32_e32 v155, 0x3db504f3, v155\n\t"
        "v_cvt_pk_bf16_f32 v152, 0, v152\n\t"
        "v_cvt_pk_bf16_f32 v153, 0, v153\n\t"
        "v_cvt_pk_bf16_f32 v154, 0, v154\n\t"
        "v_cvt_pk_bf16_f32 v155, 0, v155\n\t"
        "v_cndmask_b32_e64 v152, v253, v152, s[48:49]\n\t"
        "v_cndmask_b32_e64 v153, v253, v153, s[50:51]\n\t"
        "v_cndmask_b32_e64 v154, v253, v154, s[52:53]\n\t"
        "v_cndmask_b32_e64 v155, v253, v155, s[54:55]\n\t"
        "v_max3_f32 v172, v136, v137, v253\n\t"
        "v_max3_f32 v172, v172, v138, v139\n\t"
        "v_mov_b32_e32 v140, v253\n\t"
        "v_max_f32_e32 v174, v148, v149\n\t"
        "v_max3_f32 v174, v174, v150, v151\n\t"
        "v_max3_f32 v174, v174, v152, v153\n\t"
        "v_max3_f32 v174, v174, v154, v155\n\t"
        "s_cmp_eq_u32 s39, 0\n\t"
        "s_cbranch_scc1 .La11%=\n\t"
        "s_waitcnt vmcnt(0)\n\t"
        "s_lshl_b32 s80, s44, 7\n\t"
        "s_cmp_lt_i32 s44, 0\n\t"
        "s_cselect_b32 s80, s36, s80\n\t"
        "v_mov_b32_e32 v242, 1\n\t"
        "buffer_atomic_add v242, v250, %[rd], s80 offen sc0 sc1\n\t"
        "s_waitcnt vmcnt(0)\n\t"
        "v_readlane_b32 s84, v242, 0\n\t"
        "v_readlane_b32 s85, v242, 32\n\t"
        "v_mov_b32_e32 v243, 32\n\t"
        "s_and_b32 s80, s44, 3\n\t"
        "s_lshl_b32 s80, s80, 12\n\t"
        "s_lshr_b32 s81, s44, 2\n\t"
        "s_lshl_b32 s81, s81, 3\n\t"
        "s_add_u32 s80, s80, s81\n\t"
        "s_and_b32 s81, s44, 1\n\t"
        "s_lshl_b32 s81, s81, 13\n\t"
        "s_lshr_b32 s82, s44, 1\n\t"
        "s_lshl_b32 s82, s82, 2\n\t"
        "s_add_u32 s81, s81, s82\n\t"
        "s_cmp_lt_i32 s44, 60\n\t"
        "s_cselect_b32 s80, s80, s81\n\t"
        "s_cselect_b32 s81, 4, 0x1000\n\t"
        "s_add_u32 s81, s81, s80\n\t"
        "s_cmp_eq_u32 s84, 31\n\t"
        "s_cselect_b32 s80, s80, s36\n\t"
        "s_cmp_eq_u32 s85, 31\n\t"
        "s_cselect_b32 s81, s81, s36\n\t"
        "s_cmp_lt_i32 s44, 0\n\t"
        "s_cselect_b32 s80, s36, s80\n\t"
        "s_cselect_b32 s81, s36, s81\n\t"
        "buffer_store_dword v243, v251, %[rd], s80 offen sc1\n\t"
        "buffer_store_dword v243, v251, %[rd], s81 offen sc1\n\t"
        "s_mov_b32 s44, -1\n\t"
        "s_mov_b32 s97, 0\n\t"
        ".La10%=:\n\t"
        "s_cmp_gt_u32 s40, s37\n\t"
        "s_cbranch_scc1 .La10%=x\n\t"
        "s_memrealtime s[98:99]\n\t"
        "s_add_u32 s97, s97, 1\n\t"
        "s_cmp_lt_u32 s97, 0x400000\n\t"
        "s_cbranch_scc0 .Lgu%=\n\t"
        "s_sub_u32 s80, s37, s40\n\t"
        "s_cmp_ge_u32 s37, %c[efrom]\n\t"
        "s_subb_u32 s80, s80, 0\n\t"
        "s_mul_i32 s80, s80, %c[smin]\n\t"
        "s_waitcnt lgkmcnt(0)\n\t"
        "s_sub_u32 s81, s98, s41\n\t"
        "s_cmp_ge_i32 s81, s80\n\t"
        "s_cbranch_scc1 .La10%=p\n\t"
        "s_sleep 16\n\t"
        "s_branch .La10%=\n\t"
        ".La10%=p:\n\t"
        "buffer_load_dword v241, v247, %[rd], 0 offen sc1\n\t"
        "s_waitcnt vmcnt(0)\n\t"
        "v_readlane_b32 s80, v241, 0\n\t"
        "v_readlane_b32 s81, v241, 1\n\t"
        "v_readlane_b32 s82, v241, 2\n\t"
        "s_min_u32 s80, s80, s81\n\t"
        "s_min_u32 s80, s80, s82\n\t"
        "s_cmp_gt_u32 s80, s40\n\t"
        "s_cselect_b32 s41, s98, s41\n\t"
        "s_max_u32 s40, s40, s80\n\t"
        "s_cmp_gt_u32 s40, s37\n\t"
        "s_cbranch_scc1 .La10%=x\n\t"
        "s_sleep %c[pslp]\n\t"
        "s_branch .La10%=\n\t"
        ".La10%=x:\n\t"
        "s_lshl_b32 s81, s37, 17\n\t"
        "s_add_u32 s81, s81, %[km]\n\t"
        "buffer_load_dwordx4 v[96:99], v244, %[rd], s81 offen offset:0 sc1\n\t"
        "buffer_load_dwordx4 v[100:103], v244, %[rd], s81 offen offset:1024 sc1\n\t"
        "buffer_load_dwordx2 v[104:105], v245, %[rd], s81 offen offset:2048 sc1\n\t"
        "buffer_load_dwordx2 v[106:107], v245, %[rd], s81 offen offset:2304 sc1\n\t"
        "buffer_load_dwordx2 v[108:109], v245, %[rd], s81 offen offset:2560 sc1\n\t"
        "buffer_load_dwordx2 v[110:111], v245, %[rd], s81 offen offset:2816 sc1\n\t"
        "buffer_load_dwordx2 v[112:113], v245, %[rd], s81 offen offset:3072 sc1\n\t"
        "buffer_load_dwordx2 v[114:115], v245, %[rd], s81 offen offset:3328 sc1\n\t"
        "buffer_load_dwordx2 v[116:117], v245, %[rd], s81 offen offset:3584 sc1\n\t"
        "buffer_load_dwordx2 v[118:119], v245, %[rd], s81 offen offset:3840 sc1\n\t"
        "s_waitcnt vmcnt(0)\n\t"
        ".La11%=:\n\t"
        "ds_write_b128 v248, v[96:99] offset:0\n\t"
        "ds_write_b128 v248, v[100:103] offset:1024\n\t"
        "ds_read_b64_tr_b16 v[120:121], v249 offset:0\n\t"
        "ds_read_b64_tr_b16 v[122:123], v249 offset:64\n\t"
        "ds_read_b64_tr_b16 v[124:125], v249 offset:512\n\t"
        "ds_read_b64_tr_b16 v[126:127], v249 offset:576\n\t"
        "ds_read_b64_tr_b16 v[128:129], v249 offset:1024\n\t"
        "ds_read_b64_tr_b16 v[130:131], v249 offset:1088\n\t"
        "ds_read_b64_tr_b16 v[132:133], v249 offset:1536\n\t"
        "ds_read_b64_tr_b16 v[134:135], v249 offset:1600\n\t"
        "s_waitcnt lgkmcnt(6)\n\t"
        "v_mfma_f32_16x16x32_bf16 v[144:147], v[120:123], v[32:35], 0\n\t"
        "v_mfma_f32_16x16x32_bf16 v[156:159], v[120:123], v[48:51], 0\n\t"
        "s_waitcnt lgkmcnt(4)\n\t"
        "v_mfma_f32_16x16x32_bf16 v[144:147], v[124:127], v[36:39], v[144:147]\n\t"
        "v_mfma_f32_16x16x32_bf16 v[156:159], v[124:127], v[52:55], v[156:159]\n\t"
        "s_waitcnt lgkmcnt(2)\n\t"
        "v_mfma_f32_16x16x32_bf16 v[144:147], v[128:131], v[40:43], v[144:147]\n\t"
        "v_mfma_f32_16x16x32_bf16 v[156:159], v[128:131], v[56:59], v[156:159]\n\t"
        "s_waitcnt lgkmcnt(0)\n\t"
        "v_mfma_f32_16x16x32_bf16 v[144:147], v[132:135], v[44:47], v[144:147]\n\t"
        "v_mfma_f32_16x16x32_bf16 v[156:159], v[132:135], v[60:63], v[156:159]\n\t"
        "s_nop 1\n\t"
        "v_mov_b32_e32 v252, v252\n\t"
        "s_nop 1\n\t"
        "v_mov_b32_e32 v252, v252\n\t"
        "s_nop 0\n\t"
        "v_cvt_pk_bf16_f32 v144, 0, v144\n\t"
        "v_cvt_pk_bf16_f32 v145, 0, v145\n\t"
        "v_cvt_pk_bf16_f32 v146, 0, v146\n\t"
        "v_cvt_pk_bf16_f32 v147, 0, v147\n\t"
        "v_mul_f32_e32 v144, 0x3db504f3, v144\n\t"
        "v_mul_f32_e32 v145, 0x3db504f3, v145\n\t"
        "v_mul_f32_e32 v146, 0x3db504f3, v146\n\t"
        "v_mul_f32_e32 v147, 0x3db504f3, v147\n\t"
        "v_cvt_pk_bf16_f32 v144, 0, v144\n\t"
        "v_cvt_pk_bf16_f32 v145, 0, v145\n\t"
        "v_cvt_pk_bf16_f32 v146, 0, v146\n\t"
        "v_cvt_pk_bf16_f32 v147, 0, v147\n\t"
        "v_cndmask_b32_e64 v144, v144, v254, s[56:57]\n\t"
        "v_cndmask_b32_e64 v145, v145, v254, s[56:57]\n\t"
        "v_cndmask_b32_e64 v146, v146, v254, s[56:57]\n\t"
        "v_cndmask_b32_e64 v147, v147, v254, s[56:57]\n\t"
        "v_max3_f32 v172, v172, v144, v145\n\t"
        "v_max3_f32 v173, v172, v146, v147\n\t"
        "v_max3_f32 v172, v172, v146, v147\n\t"
        "s_nop 1\n\t"
        "v_permlane16_swap_b32_e32 v172, v173\n\t"
        "v_max_f32_e32 v172, v172, v173\n\t"
        "v_mov_b32_e32 v173, v172\n\t"
        "s_nop 1\n\t"
        "v_permlane32_swap_b32_e32 v172, v173\n\t"
        "v_max_f32_e32 v172, v172, v173\n\t"
        "v_sub_f32_e32 v136, v136, v172\n\t"
        "v_sub_f32_e32 v137, v137, v172\n\t"
        "v_sub_f32_e32 v138, v138, v172\n\t"
        "v_sub_f32_e32 v139, v139, v172\n\t"
        "v_sub_f32_e32 v140, v140, v172\n\t"
        "v_sub_f32_e32 v144, v144, v172\n\t"
        "v_sub_f32_e32 v145, v145, v172\n\t"
        "v_sub_f32_e32 v146, v146, v172\n\t"
        "v_sub_f32_e32 v147, v147, v172\n\t"
        "v_mul_f32_e32 v136, 0x3fb8aa3b, v136\n\t"
        "v_mul_f32_e32 v137, 0x3fb8aa3b, v137\n\t"
        "v_mul_f32_e32 v138, 0x3fb8aa3b, v138\n\t"
        "v_mul_f32_e32 v139, 0x3fb8aa3b, v139\n\t"
        "v_mul_f32_e32 v140, 0x3fb8aa3b, v140\n\t"
        "v_mul_f32_e32 v144, 0x3fb8aa3b, v144\n\t"
        "v_mul_f32_e32 v145, 0x3fb8aa3b, v145\n\t"
        "v_mul_f32_e32 v146, 0x3fb8aa3b, v146\n\t"
        "v_mul_f32_e32 v147, 0x3fb8aa3b, v147\n\t"
        "v_exp_f32_e32 v136, v136\n\t"
        "v_exp_f32_e32 v137, v137\n\t"
        "v_exp_f32_e32 v138, v138\n\t"
        "v_exp_f32_e32 v139, v139\n\t"
        "v_exp_f32_e32 v140, v140\n\t"
        "v_exp_f32_e32 v144, v144\n\t"
        "v_exp_f32_e32 v145, v145\n\t"
        "v_exp_f32_e32 v146, v146\n\t"
        "v_exp_f32_e32 v147, v147\n\t"
        "v_mul_f32_e32 v172, 4.0, v140\n\t"
        "v_add_f32_e32 v172, v172, v136\n\t"
        "v_add_f32_e32 v172, v172, v137\n\t"
        "v_add_f32_e32 v172, v172, v138\n\t"
        "v_add_f32_e32 v172, v172, v139\n\t"
        "v_add_f32_e32 v172, v172, v144\n\t"
        "v_add_f32_e32 v172, v172, v145\n\t"
        "v_add_f32_e32 v172, v172, v146\n\t"
        "v_add_f32_e32 v172, v172, v147\n\t"
        "v_mov_b32_e32 v173, v172\n\t"
        "s_nop 1\n\t"
        "v_permlane16_swap_b32_e32 v172, v173\n\t"
        "v_add_f32_e32 v172, v172, v173\n\t"
        "v_mov_b32_e32 v173, v172\n\t"
        "s_nop 1\n\t"
        "v_permlane32_swap_b32_e32 v172, v173\n\t"
        "v_add_f32_e32 v172, v172, v173\n\t"
        "v_rcp_f32_e32 v172, v172\n\t"
        "s_nop 0\n\t"
        "v_mul_f32_e32 v136, v136, v172\n\t"
        "v_mul_f32_e32 v137, v137, v172\n\t"
        "v_mul_f32_e32 v138, v138, v172\n\t"
        "v_mul_f32_e32 v139, v139, v172\n\t"
        "v_mul_f32_e32 v140, v140, v172\n\t"
        "v_mul_f32_e32 v144, v144, v172\n\t"
        "v_mul_f32_e32 v145, v145, v172\n\t"
        "v_mul_f32_e32 v146, v146, v172\n\t"
        "v_mul_f32_e32 v147, v147, v172\n\t"
        "v_cvt_pk_bf16_f32 v160, v136, v137\n\t"
        "v_cvt_pk_bf16_f32 v161, v138, v139\n\t"
        "v_cvt_pk_bf16_f32 v162, v140, v140\n\t"
        "v_cvt_pk_bf16_f32 v163, v140, v140\n\t"
        "v_cvt_pk_bf16_f32 v164, v144, v145\n\t"
        "v_cvt_pk_bf16_f32 v165, v146, v147\n\t"
        "v_mfma_f32_16x16x32_bf16 v[176:179], v[64:67], v[160:163], 0\n\t"
        "v_cvt_pk_bf16_f32 v156, 0, v156\n\t"
        "v_cvt_pk_bf16_f32 v157, 0, v157\n\t"
        "v_cvt_pk_bf16_f32 v158, 0, v158\n\t"
        "v_mfma_f32_16x16x32_bf16 v[180:183], v[68:71], v[160:163], 0\n\t"
        "v_cvt_pk_bf16_f32 v159, 0, v159\n\t"
        "v_mul_f32_e32 v156, 0x3db504f3, v156\n\t"
        "v_mul_f32_e32 v157, 0x3db504f3, v157\n\t"
        "v_mfma_f32_16x16x32_bf16 v[184:187], v[72:75], v[160:163], 0\n\t"
        "v_mul_f32_e32 v158, 0x3db504f3, v158\n\t"
        "v_mul_f32_e32 v159, 0x3db504f3, v159\n\t"
        "v_cvt_pk_bf16_f32 v156, 0, v156\n\t"
        "v_mfma_f32_16x16x32_bf16 v[188:191], v[76:79], v[160:163], 0\n\t"
        "v_cvt_pk_bf16_f32 v157, 0, v157\n\t"
        "v_cvt_pk_bf16_f32 v158, 0, v158\n\t"
        "v_cvt_pk_bf16_f32 v159, 0, v159\n\t"
        "v_mfma_f32_16x16x32_bf16 v[192:195], v[80:83], v[160:163], 0\n\t"
        "v_cndmask_b32_e64 v156, v156, v254, s[56:57]\n\t"
        "v_cndmask_b32_e64 v157, v157, v254, s[56:57]\n\t"
        "v_cndmask_b32_e64 v158, v158, v254, s[56:57]\n\t"
        "v_mfma_f32_16x16x32_bf16 v[196:199], v[84:87], v[160:163], 0\n\t"
        "v_cndmask_b32_e64 v159, v159, v254, s[56:57]\n\t"
        "v_max3_f32 v174, v174, v156, v157\n\t"
        "v_max3_f32 v175, v174, v158, v159\n\t"
        "v_mfma_f32_16x16x32_bf16 v[200:203], v[88:91], v[160:163], 0\n\t"
        "v_max3_f32 v174, v174, v158, v159\n\t"
        "s_nop 1\n\t"
        "v_permlane16_swap_b32_e32 v174, v175\n\t"
        "v_max_f32_e32 v174, v174, v175\n\t"
        "v_mfma_f32_16x16x32_bf16 v[204:207], v[92:95], v[160:163], 0\n\t"
        "v_mov_b32_e32 v175, v174\n\t"
        "s_nop 1\n\t"
        "v_permlane32_swap_b32_e32 v174, v175\n\t"
        "v_max_f32_e32 v174, v174, v175\n\t"
        "v_mfma_f32_16x16x16_bf16 v[176:179], v[104:105], v[164:165], v[176:179]\n\t"
        "v_sub_f32_e32 v148, v148, v174\n\t"
        "v_sub_f32_e32 v149, v149, v174\n\t"
        "v_sub_f32_e32 v150, v150, v174\n\t"
        "v_mfma_f32_16x16x16_bf16 v[180:183], v[106:107], v[164:165], v[180:183]\n\t"
        "v_sub_f32_e32 v151, v151, v174\n\t"
        "v_sub_f32_e32 v152, v152, v174\n\t"
        "v_sub_f32_e32 v153, v153, v174\n\t"
        "v_mfma_f32_16x16x16_bf16 v[184:187], v[108:109], v[164:165], v[184:187]\n\t"
        "v_sub_f32_e32 v154, v154, v174\n\t"
        "v_sub_f32_e32 v155, v155, v174\n\t"
        "v_sub_f32_e32 v156, v156, v174\n\t"
        "v_mfma_f32_16x16x16_bf16 v[188:191], v[110:111], v[164:165], v[188:191]\n\t"
        "v_sub_f32_e32 v157, v157, v174\n\t"
        "v_sub_f32_e32 v158, v158, v174\n\t"
        "v_sub_f32_e32 v159, v159, v174\n\t"
        "v_mfma_f32_16x16x16_bf16 v[192:195], v[112:113], v[164:165], v[192:195]\n\t"
        "v_mul_f32_e32 v148, 0x3fb8aa3b, v148\n\t"
        "v_mul_f32_e32 v149, 0x3fb8aa3b, v149\n\t"
        "v_mul_f32_e32 v150, 0x3fb8aa3b, v150\n\t"
        "v_mfma_f32_16x16x16_bf16 v[196:199], v[114:115], v[164:165], v[196:199]\n\t"
        "v_mul_f32_e32 v151, 0x3fb8aa3b, v151\n\t"
        "v_mul_f32_e32 v152, 0x3fb8aa3b, v152\n\t"
        "v_mul_f32_e32 v153, 0x3fb8aa3b, v153\n\t"
        "v_mfma_f32_16x16x16_bf16 v[200:203], v[116:117], v[164:165], v[200:203]\n\t"
        "v_mul_f32_e32 v154, 0x3fb8aa3b, v154\n\t"
        "v_mul_f32_e32 v155, 0x3fb8aa3b, v155\n\t"
        "v_mul_f32_e32 v156, 0x3fb8aa3b, v156\n\t"
        "v_mfma_f32_16x16x16_bf16 v[204:207], v[118:119], v[164:165], v[204:207]\n\t"
        "v_mul_f32_e32 v157, 0x3fb8aa3b, v157\n\t"
        "v_mul_f32_e32 v158, 0x3fb8aa3b, v158\n\t"
        "v_mul_f32_e32 v159, 0x3fb8aa3b, v159\n\t"
        "v_exp_f32_e32 v148, v148\n\t"
        "v_exp_f32_e32 v149, v149\n\t"
        "v_exp_f32_e32 v150, v150\n\t"
        "v_exp_f32_e32 v151, v151\n\t"
        "v_exp_f32_e32 v152, v152\n\t"
        "v_exp_f32_e32 v153, v153\n\t"
        "v_exp_f32_e32 v154, v154\n\t"
        "v_exp_f32_e32 v155, v155\n\t"
        "v_exp_f32_e32 v156, v156\n\t"
        "v_exp_f32_e32 v157, v157\n\t"
        "v_exp_f32_e32 v158, v158\n\t"
        "v_exp_f32_e32 v159, v159\n\t"
        "v_add_f32_e32 v174, v148, v149\n\t"
        "v_add_f32_e32 v174, v174, v150\n\t"
        "v_add_f32_e32 v174, v174, v151\n\t"
        "v_add_f32_e32 v174, v174, v152\n\t"
        "v_add_f32_e32 v174, v174, v153\n\t"
        "v_add_f32_e32 v174, v174, v154\n\t"
        "v_add_f32_e32 v174, v174, v155\n\t"
        "v_add_f32_e32 v174, v174, v156\n\t"
        "v_add_f32_e32 v174, v174, v157\n\t"
        "v_add_f32_e32 v174, v174, v158\n\t"
        "v_add_f32_e32 v174, v174, v159\n\t"
        "v_mov_b32_e32 v175, v174\n\t"
        "s_nop 1\n\t"
        "v_permlane16_swap_b32_e32 v174, v175\n\t"
        "v_add_f32_e32 v174, v174, v175\n\t"
        "v_mov_b32_e32 v175, v174\n\t"
        "s_nop 1\n\t"
        "v_permlane32_swap_b32_e32 v174, v175\n\t"
        "v_add_f32_e32 v174, v174, v175\n\t"
        "v_rcp_f32_e32 v174, v174\n\t"
        "s_nop 0\n\t"
        "v_mul_f32_e32 v148, v148, v174\n\t"
        "v_mul_f32_e32 v149, v149, v174\n\t"
        "v_mul_f32_e32 v150, v150, v174\n\t"
        "v_mul_f32_e32 v151, v151, v174\n\t"
        "v_mul_f32_e32 v152, v152, v174\n\t"
        "v_mul_f32_e32 v153, v153, v174\n\t"
        "v_mul_f32_e32 v154, v154, v174\n\t"
        "v_mul_f32_e32 v155, v155, v174\n\t"
        "v_mul_f32_e32 v156, v156, v174\n\t"
        "v_mul_f32_e32 v157, v157, v174\n\t"
        "v_mul_f32_e32 v158, v158, v174\n\t"
        "v_mul_f32_e32 v159, v159, v174\n\t"
        "v_cvt_pk_bf16_f32 v166, v148, v149\n\t"
        "v_cvt_pk_bf16_f32 v167, v150, v151\n\t"
        "v_cvt_pk_bf16_f32 v168, v152, v153\n\t"
        "v_cvt_pk_bf16_f32 v169, v154, v155\n\t"
        "v_cvt_pk_bf16_f32 v170, v156, v157\n\t"
        "v_cvt_pk_bf16_f32 v171, v158, v159\n\t"
        "s_waitcnt vmcnt(38)\n\t"
        "s_lshl_b32 s80, s44, 7\n\t"
        "s_cmp_lt_i32 s44, 0\n\t"
        "s_cselect_b32 s80, s36, s80\n\t"
        "v_mov_b32_e32 v242, 1\n\t"
        "buffer_atomic_add v242, v250, %[rd], s80 offen sc0 sc1\n\t"
        "s_lshl_b32 s86, s37, 18\n\t"
        "s_add_u32 s86, s86, %[ox]\n\t"
        "v_mfma_f32_16x16x32_bf16 v[208:211], v[64:67], v[166:169], 0\n\t"
        "v_cvt_pk_bf16_f32 v176, v176, v177\n\t"
        "v_cvt_pk_bf16_f32 v177, v178, v179\n\t"
        "v_mfma_f32_16x16x32_bf16 v[212:215], v[68:71], v[166:169], 0\n\t"
        "v_cvt_pk_bf16_f32 v178, v180, v181\n\t"
        "v_cvt_pk_bf16_f32 v179, v182, v183\n\t"
        "v_mfma_f32_16x16x32_bf16 v[216:219], v[72:75], v[166:169], 0\n\t"
        "buffer_store_dwordx4 v[176:179], v244, %[rd], s86 offen sc1\n\t"
        "s_add_u32 s86, s86, 4096\n\t"
        "v_cvt_pk_bf16_f32 v184, v184, v185\n\t"
        "v_mfma_f32_16x16x32_bf16 v[220:223], v[76:79], v[166:169], 0\n\t"
        "v_cvt_pk_bf16_f32 v185, v186, v187\n\t"
        "v_cvt_pk_bf16_f32 v186, v188, v189\n\t"
        "v_mfma_f32_16x16x32_bf16 v[224:227], v[80:83], v[166:169], 0\n\t"
        "v_cvt_pk_bf16_f32 v187, v190, v191\n\t"
        "buffer_store_dwordx4 v[184:187], v244, %[rd], s86 offen sc1\n\t"
        "s_add_u32 s86, s86, 4096\n\t"
        "v_mfma_f32_16x16x32_bf16 v[228:231], v[84:87], v[166:169], 0\n\t"
        "v_cvt_pk_bf16_f32 v192, v192, v193\n\t"
        "v_cvt_pk_bf16_f32 v193, v194, v195\n\t"
        "v_mfma_f32_16x16x32_bf16 v[232:235], v[88:91], v[166:169], 0\n\t"
        "v_cvt_pk_bf16_f32 v194, v196, v197\n\t"
        "v_cvt_pk_bf16_f32 v195, v198, v199\n\t"
        "v_mfma_f32_16x16x32_bf16 v[236:239], v[92:95], v[166:169], 0\n\t"
        "buffer_store_dwordx4 v[192:195], v244, %[rd], s86 offen sc1\n\t"
        "s_add_u32 s86, s86, 4096\n\t"
        "v_cvt_pk_bf16_f32 v200, v200, v201\n\t"
        "v_mfma_f32_16x16x16_bf16 v[208:211], v[104:105], v[170:171], v[208:211]\n\t"
        "v_cvt_pk_bf16_f32 v201, v202, v203\n\t"
        "v_cvt_pk_bf16_f32 v202, v204, v205\n\t"
        "v_mfma_f32_16x16x16_bf16 v[212:215], v[106:107], v[170:171], v[212:215]\n\t"
        "v_cvt_pk_bf16_f32 v203, v206, v207\n\t"
        "buffer_store_dwordx4 v[200:203], v244, %[rd], s86 offen sc1\n\t"
        "s_add_u32 s86, s86, 118784\n\t"
        "v_mfma_f32_16x16x16_bf16 v[216:219], v[108:109], v[170:171], v[216:219]\n\t"
        "v_mfma_f32_16x16x16_bf16 v[220:223], v[110:111], v[170:171], v[220:223]\n\t"
        "v_mfma_f32_16x16x16_bf16 v[224:227], v[112:113], v[170:171], v[224:227]\n\t"
        "v_mfma_f32_16x16x16_bf16 v[228:231], v[114:115], v[170:171], v[228:231]\n\t"
        "v_mfma_f32_16x16x16_bf16 v[232:235], v[116:117], v[170:171], v[232:235]\n\t"
        "v_mfma_f32_16x16x16_bf16 v[236:239], v[118:119], v[170:171], v[236:239]\n\t"
        "v_cvt_pk_bf16_f32 v208, v208, v209\n\t"
        "v_cvt_pk_bf16_f32 v209, v210, v211\n\t"
        "v_cvt_pk_bf16_f32 v210, v212, v213\n\t"
        "v_cvt_pk_bf16_f32 v211, v214, v215\n\t"
        "buffer_store_dwordx4 v[208:211], v244, %[rd], s86 offen sc1\n\t"
        "s_add_u32 s86, s86, 4096\n\t"
        "v_cvt_pk_bf16_f32 v216, v216, v217\n\t"
        "v_cvt_pk_bf16_f32 v217, v218, v219\n\t"
        "v_cvt_pk_bf16_f32 v218, v220, v221\n\t"
        "v_cvt_pk_bf16_f32 v219, v222, v223\n\t"
        "buffer_store_dwordx4 v[216:219], v244, %[rd], s86 offen sc1\n\t"
        "s_add_u32 s86, s86, 4096\n\t"
        "v_cvt_pk_bf16_f32 v224, v224, v225\n\t"
        "v_cvt_pk_bf16_f32 v225, v226, v227\n\t"
        "v_cvt_pk_bf16_f32 v226, v228, v229\n\t"
        "v_cvt_pk_bf16_f32 v227, v230, v231\n\t"
        "buffer_store_dwordx4 v[224:227], v244, %[rd], s86 offen sc1\n\t"
        "s_add_u32 s86, s86, 4096\n\t"
        "v_cvt_pk_bf16_f32 v232, v232, v233\n\t"
        "v_cvt_pk_bf16_f32 v233, v234, v235\n\t"
        "v_cvt_pk_bf16_f32 v234, v236, v237\n\t"
        "v_cvt_pk_bf16_f32 v235, v238, v239\n\t"
        "buffer_store_dwordx4 v[232:235], v244, %[rd], s86 offen sc1\n\t"
        "s_add_u32 s86, s86, 118784\n\t"
        "s_mov_b32 s45, s44\n\t"
        "s_mov_b32 s44, s37\n\t"
        "s_add_u32 s80, s37, 4\n\t"
        "s_cmp_gt_u32 s40, s80\n\t"
        "s_cselect_b32 s80, s46, 1\n\t"
        "s_cmp_eq_u32 s80, 0\n\t"
        "s_cbranch_scc1 .La12%=\n\t"
        "s_waitcnt vmcnt(0)\n\t"
        "v_readlane_b32 s84, v242, 0\n\t"
        "v_readlane_b32 s85, v242, 32\n\t"
        "v_mov_b32_e32 v243, 32\n\t"
        "s_and_b32 s80, s45, 3\n\t"
        "s_lshl_b32 s80, s80, 12\n\t"
        "s_lshr_b32 s81, s45, 2\n\t"
        "s_lshl_b32 s81, s81, 3\n\t"
        "s_add_u32 s80, s80, s81\n\t"
        "s_and_b32 s81, s45, 1\n\t"
        "s_lshl_b32 s81, s81, 13\n\t"
        "s_lshr_b32 s82, s45, 1\n\t"
        "s_lshl_b32 s82, s82, 2\n\t"
        "s_add_u32 s81, s81, s82\n\t"
        "s_cmp_lt_i32 s45, 60\n\t"
        "s_cselect_b32 s80, s80, s81\n\t"
        "s_cselect_b32 s81, 4, 0x1000\n\t"
        "s_add_u32 s81, s81, s80\n\t"
        "s_cmp_eq_u32 s84, 31\n\t"
        "s_cselect_b32 s80, s80, s36\n\t"
        "s_cmp_eq_u32 s85, 31\n\t"
        "s_cselect_b32 s81, s81, s36\n\t"
        "s_cmp_lt_i32 s45, 0\n\t"
        "s_cselect_b32 s80, s36, s80\n\t"
        "s_cselect_b32 s81, s36, s81\n\t"
        "buffer_store_dword v243, v251, %[rd], s80 offen sc1\n\t"
        "buffer_store_dword v243, v251, %[rd], s81 offen sc1\n\t"
        "s_lshl_b32 s80, s44, 7\n\t"
        "s_cmp_lt_i32 s44, 0\n\t"
        "s_cselect_b32 s80, s36, s80\n\t"
        "v_mov_b32_e32 v242, 1\n\t"
        "buffer_atomic_add v242, v250, %[rd], s80 offen sc0 sc1\n\t"
        "s_waitcnt vmcnt(0)\n\t"
        "v_readlane_b32 s84, v242, 0\n\t"
        "v_readlane_b32 s85, v242, 32\n\t"
        "v_mov_b32_e32 v243, 32\n\t"
        "s_and_b32 s80, s44, 3\n\t"
        "s_lshl_b32 s80, s80, 12\n\t"
        "s_lshr_b32 s81, s44, 2\n\t"
        "s_lshl_b32 s81, s81, 3\n\t"
        "s_add_u32 s80, s80, s81\n\t"
        "s_and_b32 s81, s44, 1\n\t"
        "s_lshl_b32 s81, s81, 13\n\t"
        "s_lshr_b32 s82, s44, 1\n\t"
        "s_lshl_b32 s82, s82, 2\n\t"
        "s_add_u32 s81, s81, s82\n\t"
        "s_cmp_lt_i32 s44, 60\n\t"
        "s_cselect_b32 s80, s80, s81\n\t"
        "s_cselect_b32 s81, 4, 0x1000\n\t"
        "s_add_u32 s81, s81, s80\n\t"
        "s_cmp_eq_u32 s84, 31\n\t"
        "s_cselect_b32 s80, s80, s36\n\t"
        "s_cmp_eq_u32 s85, 31\n\t"
        "s_cselect_b32 s81, s81, s36\n\t"
        "s_cmp_lt_i32 s44, 0\n\t"
        "s_cselect_b32 s80, s36, s80\n\t"
        "s_cselect_b32 s81, s36, s81\n\t"
        "buffer_store_dword v243, v251, %[rd], s80 offen sc1\n\t"
        "buffer_store_dword v243, v251, %[rd], s81 offen sc1\n\t"
        "s_mov_b32 s44, -1\n\t"
        "s_mov_b32 s45, -1\n\t"
        ".La12%=:\n\t"
        "s_mov_b32 s38, s46\n\t"
        "s_mov_b32 s39, s47\n\t"
        "s_add_u32 s37, s37, 4\n\t"
        "s_cmp_eq_u32 s38, 0\n\t"
        "s_cbranch_scc1 .La14%=\n\t"
        "s_mov_b32 s97, 0\n\t"
        ".La13%=:\n\t"
        "buffer_load_dword v240, v246, %[rd], 0 offen sc1\n\t"
        "buffer_load_dword v241, v247, %[rd], 0 offen sc1\n\t"
        "s_add_u32 s97, s97, 1\n\t"
        "s_cmp_lt_u32 s97, 0x200000\n\t"
        "s_cbranch_scc0 .Lgu%=\n\t"
        "s_memrealtime s[98:99]\n\t"
        "s_waitcnt vmcnt(0) lgkmcnt(0)\n\t"
        "v_cmp_eq_u32_e32 vcc, %[tag], v240\n\t"
        "s_nop 1\n\t"
        "s_or_b64 s[42:43], s[42:43], vcc\n\t"
        "v_readlane_b32 s80, v241, 0\n\t"
        "v_readlane_b32 s81, v241, 1\n\t"
        "v_readlane_b32 s82, v241, 2\n\t"
        "s_min_u32 s80, s80, s81\n\t"
        "s_min_u32 s80, s80, s82\n\t"
        "s_cmp_gt_u32 s80, s40\n\t"
        "s_cselect_b32 s41, s98, s41\n\t"
        "s_max_u32 s40, s40, s80\n\t"
        "s_and_b32 s80, s37, 60\n\t"
        "s_lshr_b64 s[80:81], s[42:43], s80\n\t"
        "s_andn2_b32 s80, 15, s80\n\t"
        "s_cbranch_scc0 .La13%=x\n\t"
        "s_sleep 16\n\t"
        "s_branch .La13%=\n\t"
        ".La13%=x:\n\t"
        "s_cmp_gt_u32 s40, s37\n\t"
        "s_cselect_b32 s39, 0, s36\n\t"
        "s_lshl_b32 s80, s37, 18\n\t"
        "s_or_b32 s80, s80, 0\n\t"
        "s_lshl_b32 s81, s37, 17\n\t"
        "s_add_u32 s81, s81, %[km]\n\t"
        "s_or_b32 s81, s81, s39\n\t"
        "s_add_u32 s82, s80, %[kx]\n\t"
        "s_add_u32 s83, s82, 0x1000\n\t"
        "buffer_load_dwordx4 a[0:3], v244, %[rd], s82 offen offset:0 sc1\n\t"
        "buffer_load_dwordx4 a[4:7], v244, %[rd], s82 offen offset:1024 sc1\n\t"
        "buffer_load_dwordx4 a[8:11], v244, %[rd], s82 offen offset:2048 sc1\n\t"
        "buffer_load_dwordx4 a[12:15], v244, %[rd], s82 offen offset:3072 sc1\n\t"
        "buffer_load_dwordx4 a[16:19], v244, %[rd], s83 offen offset:0 sc1\n\t"
        "buffer_load_dwordx4 a[20:23], v244, %[rd], s83 offen offset:1024 sc1\n\t"
        "buffer_load_dwordx4 a[24:27], v244, %[rd], s83 offen offset:2048 sc1\n\t"
        "buffer_load_dwordx4 a[28:31], v244, %[rd], s83 offen offset:3072 sc1\n\t"
        "s_cmp_eq_u64 s[42:43], -1\n\t"
        "s_cbranch_scc1 .La15%=\n\t"
        "s_sleep %c[aslp]\n\t"
        ".La15%=:\n\t"
        "s_sleep %c[bslp]\n\t"
        "s_add_u32 s82, s80, %[qx]\n\t"
        "s_add_u32 s83, s82, 0x1000\n\t"
        "buffer_load_dwordx4 a[32:35], v244, %[rd], s82 offen offset:0 sc1\n\t"
        "buffer_load_dwordx4 a[36:39], v244, %[rd], s82 offen offset:1024 sc1\n\t"
        "buffer_load_dwordx4 a[40:43], v244, %[rd], s82 offen offset:2048 sc1\n\t"
        "buffer_load_dwordx4 a[44:47], v244, %[rd], s82 offen offset:3072 sc1\n\t"
        "buffer_load_dwordx4 a[48:51], v244, %[rd], s83 offen offset:0 sc1\n\t"
        "buffer_load_dwordx4 a[52:55], v244, %[rd], s83 offen offset:1024 sc1\n\t"
        "buffer_load_dwordx4 a[56:59], v244, %[rd], s83 offen offset:2048 sc1\n\t"
        "buffer_load_dwordx4 a[60:63], v244, %[rd], s83 offen offset:3072 sc1\n\t"
        "s_cmp_eq_u64 s[42:43], -1\n\t"
        "s_cbranch_scc1 .La16%=\n\t"
        "s_sleep %c[aslp]\n\t"
        ".La16%=:\n\t"
        "s_sleep %c[bslp]\n\t"
        "buffer_load_dwordx4 a[96:99], v244, %[rd], s81 offen offset:0 sc1\n\t"
        "buffer_load_dwordx4 a[100:103], v244, %[rd], s81 offen offset:1024 sc1\n\t"
        "buffer_load_dwordx2 a[104:105], v245, %[rd], s81 offen offset:2048 sc1\n\t"
        "buffer_load_dwordx2 a[106:107], v245, %[rd], s81 offen offset:2304 sc1\n\t"
        "buffer_load_dwordx2 a[108:109], v245, %[rd], s81 offen offset:2560 sc1\n\t"
        "buffer_load_dwordx2 a[110:111], v245, %[rd], s81 offen offset:2816 sc1\n\t"
        "buffer_load_dwordx2 a[112:113], v245, %[rd], s81 offen offset:3072 sc1\n\t"
        "buffer_load_dwordx2 a[114:115], v245, %[rd], s81 offen offset:3328 sc1\n\t"
        "buffer_load_dwordx2 a[116:117], v245, %[rd], s81 offen offset:3584 sc1\n\t"
        "buffer_load_dwordx2 a[118:119], v245, %[rd], s81 offen offset:3840 sc1\n\t"
        "s_add_u32 s82, s80, %[vx]\n\t"
        "s_add_u32 s83, s82, 0x1000\n\t"
        "buffer_load_dwordx4 a[64:67], v244, %[rd], s82 offen offset:0 sc1\n\t"
        "buffer_load_dwordx4 a[68:71], v244, %[rd], s82 offen offset:1024 sc1\n\t"
        "buffer_load_dwordx4 a[72:75], v244, %[rd], s82 offen offset:2048 sc1\n\t"
        "buffer_load_dwordx4 a[76:79], v244, %[rd], s82 offen offset:3072 sc1\n\t"
        "buffer_load_dwordx4 a[80:83], v244, %[rd], s83 offen offset:0 sc1\n\t"
        "buffer_load_dwordx4 a[84:87], v244, %[rd], s83 offen offset:1024 sc1\n\t"
        "buffer_load_dwordx4 a[88:91], v244, %[rd], s83 offen offset:2048 sc1\n\t"
        "buffer_load_dwordx4 a[92:95], v244, %[rd], s83 offen offset:3072 sc1\n\t"
        "s_cmp_eq_u64 s[42:43], -1\n\t"
        "s_cbranch_scc1 .La17%=\n\t"
        "s_sleep %c[aslp]\n\t"
        ".La17%=:\n\t"
        "s_sleep %c[bslp]\n\t"
        "buffer_store_dword v0, off, %[rd], s36\n\t"
        "buffer_store_dword v0, off, %[rd], s36\n\t"
        "buffer_store_dword v0, off, %[rd], s36\n\t"
        "buffer_store_dword v0, off, %[rd], s36\n\t"
        "buffer_store_dword v0, off, %[rd], s36\n\t"
        "buffer_store_dword v0, off, %[rd], s36\n\t"
        "buffer_store_dword v0, off, %[rd], s36\n\t"
        "buffer_store_dword v0, off, %[rd], s36\n\t"
        "buffer_store_dword v0, off, %[rd], s36\n\t"
        ".La14%=:\n\t"
        "s_cmp_gt_u32 s40, s37\n\t"
        "s_cselect_b32 s80, s39, 0\n\t"
        "s_cmp_eq_u32 s80, 0\n\t"
        "s_cbranch_scc1 .La18%=\n\t"
        "s_mov_b32 s39, 0\n\t"
        "s_lshl_b32 s81, s37, 17\n\t"
        "s_add_u32 s81, s81, %[km]\n\t"
        "buffer_load_dwordx4 a[96:99], v244, %[rd], s81 offen offset:0 sc1\n\t"
        "buffer_load_dwordx4 a[100:103], v244, %[rd], s81 offen offset:1024 sc1\n\t"
        "buffer_load_dwordx2 a[104:105], v245, %[rd], s81 offen offset:2048 sc1\n\t"
        "buffer_load_dwordx2 a[106:107], v245, %[rd], s81 offen offset:2304 sc1\n\t"
        "buffer_load_dwordx2 a[108:109], v245, %[rd], s81 offen offset:2560 sc1\n\t"
        "buffer_load_dwordx2 a[110:111], v245, %[rd], s81 offen offset:2816 sc1\n\t"
        "buffer_load_dwordx2 a[112:113], v245, %[rd], s81 offen offset:3072 sc1\n\t"
        "buffer_load_dwordx2 a[114:115], v245, %[rd], s81 offen offset:3328 sc1\n\t"
        "buffer_load_dwordx2 a[116:117], v245, %[rd], s81 offen offset:3584 sc1\n\t"
        "buffer_load_dwordx2 a[118:119], v245, %[rd], s81 offen offset:3840 sc1\n\t"
        "buffer_store_dword v0, off, %[rd], s36\n\t"
        "buffer_store_dword v0, off, %[rd], s36\n\t"
        "buffer_store_dword v0, off, %[rd], s36\n\t"
        "buffer_store_dword v0, off, %[rd], s36\n\t"
        "buffer_store_dword v0, off, %[rd], s36\n\t"
        "buffer_store_dword v0, off, %[rd], s36\n\t"
        "buffer_store_dword v0, off, %[rd], s36\n\t"
        "buffer_store_dword v0, off, %[rd], s36\n\t"
        ".La18%=:\n\t"
        "s_add_u32 s86, s37, 4\n\t"
        "s_and_b32 s80, s86, 60\n\t"
        "s_lshr_b64 s[80:81], s[42:43], s80\n\t"
        "s_andn2_b32 s80, 15, s80\n\t"
        "s_cselect_b32 s46, s36, 0\n\t"
        "s_cmp_lt_u32 s86, 64\n\t"
        "s_cselect_b32 s46, s46, s36\n\t"
        "s_cmp_gt_u32 s40, s86\n\t"
        "s_cselect_b32 s47, s46, s36\n\t"
        "s_lshl_b32 s80, s86, 18\n\t"
        "s_or_b32 s80, s80, s46\n\t"
        "s_lshl_b32 s81, s86, 17\n\t"
        "s_add_u32 s81, s81, %[km]\n\t"
        "s_or_b32 s81, s81, s47\n\t"
        "s_add_u32 s82, s80, %[kx]\n\t"
        "s_add_u32 s83, s82, 0x1000\n\t"
        "buffer_load_dwordx4 v[0:3], v244, %[rd], s82 offen offset:0 sc1\n\t"
        "buffer_load_dwordx4 v[4:7], v244, %[rd], s82 offen offset:1024 sc1\n\t"
        "buffer_load_dwordx4 v[8:11], v244, %[rd], s82 offen offset:2048 sc1\n\t"
        "buffer_load_dwordx4 v[12:15], v244, %[rd], s82 offen offset:3072 sc1\n\t"
        "buffer_load_dwordx4 v[16:19], v244, %[rd], s83 offen offset:0 sc1\n\t"
        "buffer_load_dwordx4 v[20:23], v244, %[rd], s83 offen offset:1024 sc1\n\t"
        "buffer_load_dwordx4 v[24:27], v244, %[rd], s83 offen offset:2048 sc1\n\t"
        "buffer_load_dwordx4 v[28:31], v244, %[rd], s83 offen offset:3072 sc1\n\t"
        "s_cmp_eq_u64 s[42:43], -1\n\t"
        "s_cbranch_scc1 .La19%=\n\t"
        "s_sleep %c[aslp]\n\t"
        ".La19%=:\n\t"
        "s_sleep %c[bslp]\n\t"
        "s_add_u32 s82, s80, %[qx]\n\t"
        "s_add_u32 s83, s82, 0x1000\n\t"
        "buffer_load_dwordx4 v[32:35], v244, %[rd], s82 offen offset:0 sc1\n\t"
        "buffer_load_dwordx4 v[36:39], v244, %[rd], s82 offen offset:1024 sc1\n\t"
        "buffer_load_dwordx4 v[40:43], v244, %[rd], s82 offen offset:2048 sc1\n\t"
        "buffer_load_dwordx4 v[44:47], v244, %[rd], s82 offen offset:3072 sc1\n\t"
        "buffer_load_dwordx4 v[48:51], v244, %[rd], s83 offen offset:0 sc1\n\t"
        "buffer_load_dwordx4 v[52:55], v244, %[rd], s83 offen offset:1024 sc1\n\t"
        "buffer_load_dwordx4 v[56:59], v244, %[rd], s83 offen offset:2048 sc1\n\t"
        "buffer_load_dwordx4 v[60:63], v244, %[rd], s83 offen offset:3072 sc1\n\t"
        "s_cmp_eq_u64 s[42:43], -1\n\t"
        "s_cbranch_scc1 .La20%=\n\t"
        "s_sleep %c[aslp]\n\t"
        ".La20%=:\n\t"
        "s_sleep %c[bslp]\n\t"
        "buffer_load_dwordx4 v[96:99], v244, %[rd], s81 offen offset:0 sc1\n\t"
        "buffer_load_dwordx4 v[100:103], v244, %[rd], s81 offen offset:1024 sc1\n\t"
        "buffer_load_dwordx2 v[104:105], v245, %[rd], s81 offen offset:2048 sc1\n\t"
        "buffer_load_dwordx2 v[106:107], v245, %[rd], s81 offen offset:2304 sc1\n\t"
        "buffer_load_dwordx2 v[108:109], v245, %[rd], s81 offen offset:2560 sc1\n\t"
        "buffer_load_dwordx2 v[110:111], v245, %[rd], s81 offen offset:2816 sc1\n\t"
        "buffer_load_dwordx2 v[112:113], v245, %[rd], s81 offen offset:3072 sc1\n\t"
        "buffer_load_dwordx2 v[114:115], v245, %[rd], s81 offen offset:3328 sc1\n\t"
        "buffer_load_dwordx2 v[116:117], v245, %[rd], s81 offen offset:3584 sc1\n\t"
        "buffer_load_dwordx2 v[118:119], v245, %[rd], s81 offen offset:3840 sc1\n\t"
        "s_add_u32 s82, s80, %[vx]\n\t"
        "s_add_u32 s83, s82, 0x1000\n\t"
        "buffer_load_dwordx4 v[64:67], v244, %[rd], s82 offen offset:0 sc1\n\t"
        "buffer_load_dwordx4 v[68:71], v244, %[rd], s82 offen offset:1024 sc1\n\t"
        "buffer_load_dwordx4 v[72:75], v244, %[rd], s82 offen offset:2048 sc1\n\t"
        "buffer_load_dwordx4 v[76:79], v244, %[rd], s82 offen offset:3072 sc1\n\t"
        "buffer_load_dwordx4 v[80:83], v244, %[rd], s83 offen offset:0 sc1\n\t"
        "buffer_load_dwordx4 v[84:87], v244, %[rd], s83 offen offset:1024 sc1\n\t"
        "buffer_load_dwordx4 v[88:91], v244, %[rd], s83 offen offset:2048 sc1\n\t"
        "buffer_load_dwordx4 v[92:95], v244, %[rd], s83 offen offset:3072 sc1\n\t"
        "s_cmp_eq_u64 s[42:43], -1\n\t"
        "s_cbranch_scc1 .La21%=\n\t"
        "s_sleep %c[aslp]\n\t"
        ".La21%=:\n\t"
        "s_sleep %c[bslp]\n\t"
        "s_waitcnt vmcnt(42)\n\t"
        "s_memrealtime s[98:99]\n\t"
        "v_mfma_f32_16x16x32_bf16 v[136:139], a[0:3], a[32:35], 0\n\t"
        "v_mfma_f32_16x16x32_bf16 v[148:151], a[0:3], a[48:51], 0\n\t"
        "v_mfma_f32_16x16x32_bf16 v[152:155], a[16:19], a[48:51], 0\n\t"
        "v_mfma_f32_16x16x32_bf16 v[136:139], a[4:7], a[36:39], v[136:139]\n\t"
        "v_mfma_f32_16x16x32_bf16 v[148:151], a[4:7], a[52:55], v[148:151]\n\t"
        "v_mfma_f32_16x16x32_bf16 v[152:155], a[20:23], a[52:55], v[152:155]\n\t"
        "v_mfma_f32_16x16x32_bf16 v[136:139], a[8:11], a[40:43], v[136:139]\n\t"
        "v_mfma_f32_16x16x32_bf16 v[148:151], a[8:11], a[56:59], v[148:151]\n\t"
        "v_mfma_f32_16x16x32_bf16 v[152:155], a[24:27], a[56:59], v[152:155]\n\t"
        "v_mfma_f32_16x16x32_bf16 v[136:139], a[12:15], a[44:47], v[136:139]\n\t"
        "v_mfma_f32_16x16x32_bf16 v[148:151], a[12:15], a[60:63], v[148:151]\n\t"
        "v_mfma_f32_16x16x32_bf16 v[152:155], a[28:31], a[60:63], v[152:155]\n\t"
        "s_waitcnt lgkmcnt(0)\n\t"
        "v_cmp_eq_u32_e32 vcc, %[tag], v240\n\t"
        "s_nop 1\n\t"
        "s_or_b64 s[42:43], s[42:43], vcc\n\t"
        "v_readlane_b32 s80, v241, 0\n\t"
        "v_readlane_b32 s81, v241, 1\n\t"
        "v_readlane_b32 s82, v241, 2\n\t"
        "s_min_u32 s80, s80, s81\n\t"
        "s_min_u32 s80, s80, s82\n\t"
        "s_cmp_gt_u32 s80, s40\n\t"
        "s_cselect_b32 s41, s98, s41\n\t"
        "s_max_u32 s40, s40, s80\n\t"
        "v_readlane_b32 s84, v242, 0\n\t"
        "v_readlane_b32 s85, v242, 32\n\t"
        "v_mov_b32_e32 v243, 32\n\t"
        "s_and_b32 s80, s45, 3\n\t"
        "s_lshl_b32 s80, s80, 12\n\t"
        "s_lshr_b32 s81, s45, 2\n\t"
        "s_lshl_b32 s81, s81, 3\n\t"
        "s_add_u32 s80, s80, s81\n\t"
        "s_and_b32 s81, s45, 1\n\t"
        "s_lshl_b32 s81, s81, 13\n\t"
        "s_lshr_b32 s82, s45, 1\n\t"
        "s_lshl_b32 s82, s82, 2\n\t"
        "s_add_u32 s81, s81, s82\n\t"
        "s_cmp_lt_i32 s45, 60\n\t"
        "s_cselect_b32 s80, s80, s81\n\t"
        "s_cselect_b32 s81, 4, 0x1000\n\t"
        "s_add_u32 s81, s81, s80\n\t"
        "s_cmp_eq_u32 s84, 31\n\t"
        "s_cselect_b32 s80, s80, s36\n\t"
        "s_cmp_eq_u32 s85, 31\n\t"
        "s_cselect_b32 s81, s81, s36\n\t"
        "s_cmp_lt_i32 s45, 0\n\t"
        "s_cselect_b32 s80, s36, s80\n\t"
        "s_cselect_b32 s81, s36, s81\n\t"
        "buffer_store_dword v243, v251, %[rd], s80 offen sc1\n\t"
        "buffer_store_dword v243, v251, %[rd], s81 offen sc1\n\t"
        "s_add_u32 s86, s37, 8\n\t"
        "s_cmp_lg_u64 s[42:43], -1\n\t"
        "s_cselect_b32 s84, 0, s36\n\t"
        "s_cmp_gt_u32 s40, s86\n\t"
        "s_cselect_b32 s85, s36, s39\n\t"
        "buffer_load_dword v240, v246, %[rd], s84 offen sc1\n\t"
        "buffer_load_dword v241, v247, %[rd], s85 offen sc1\n\t"
        "v_cvt_pk_bf16_f32 v136, 0, v136\n\t"
        "v_cvt_pk_bf16_f32 v137, 0, v137\n\t"
        "v_cvt_pk_bf16_f32 v138, 0, v138\n\t"
        "v_cvt_pk_bf16_f32 v139, 0, v139\n\t"
        "v_mul_f32_e32 v136, 0x3db504f3, v136\n\t"
        "v_mul_f32_e32 v137, 0x3db504f3, v137\n\t"
        "v_mul_f32_e32 v138, 0x3db504f3, v138\n\t"
        "v_mul_f32_e32 v139, 0x3db504f3, v139\n\t"
        "v_cvt_pk_bf16_f32 v136, 0, v136\n\t"
        "v_cvt_pk_bf16_f32 v137, 0, v137\n\t"
        "v_cvt_pk_bf16_f32 v138, 0, v138\n\t"
        "v_cvt_pk_bf16_f32 v139, 0, v139\n\t"
        "v_cndmask_b32_e64 v136, v253, v136, s[48:49]\n\t"
        "v_cndmask_b32_e64 v137, v253, v137, s[50:51]\n\t"
        "v_cndmask_b32_e64 v138, v253, v138, s[52:53]\n\t"
        "v_cndmask_b32_e64 v139, v253, v139, s[54:55]\n\t"
        "v_cvt_pk_bf16_f32 v148, 0, v148\n\t"
        "v_cvt_pk_bf16_f32 v149, 0, v149\n\t"
        "v_cvt_pk_bf16_f32 v150, 0, v150\n\t"
        "v_cvt_pk_bf16_f32 v151, 0, v151\n\t"
        "v_mul_f32_e32 v148, 0x3db504f3, v148\n\t"
        "v_mul_f32_e32 v149, 0x3db504f3, v149\n\t"
        "v_mul_f32_e32 v150, 0x3db504f3, v150\n\t"
        "v_mul_f32_e32 v151, 0x3db504f3, v151\n\t"
        "v_cvt_pk_bf16_f32 v148, 0, v148\n\t"
        "v_cvt_pk_bf16_f32 v149, 0, v149\n\t"
        "v_cvt_pk_bf16_f32 v150, 0, v150\n\t"
        "v_cvt_pk_bf16_f32 v151, 0, v151\n\t"
        "v_cvt_pk_bf16_f32 v152, 0, v152\n\t"
        "v_cvt_pk_bf16_f32 v153, 0, v153\n\t"
        "v_cvt_pk_bf16_f32 v154, 0, v154\n\t"
        "v_cvt_pk_bf16_f32 v155, 0, v155\n\t"
        "v_mul_f32_e32 v152, 0x3db504f3, v152\n\t"
        "v_mul_f32_e32 v153, 0x3db504f3, v153\n\t"
        "v_mul_f32_e32 v154, 0x3db504f3, v154\n\t"
        "v_mul_f32_e32 v155, 0x3db504f3, v155\n\t"
        "v_cvt_pk_bf16_f32 v152, 0, v152\n\t"
        "v_cvt_pk_bf16_f32 v153, 0, v153\n\t"
        "v_cvt_pk_bf16_f32 v154, 0, v154\n\t"
        "v_cvt_pk_bf16_f32 v155, 0, v155\n\t"
        "v_cndmask_b32_e64 v152, v253, v152, s[48:49]\n\t"
        "v_cndmask_b32_e64 v153, v253, v153, s[50:51]\n\t"
        "v_cndmask_b32_e64 v154, v253, v154, s[52:53]\n\t"
        "v_cndmask_b32_e64 v155, v253, v155, s[54:55]\n\t"
        "v_max3_f32 v172, v136, v137, v253\n\t"
        "v_max3_f32 v172, v172, v138, v139\n\t"
        "v_mov_b32_e32 v140, v253\n\t"
        "v_max_f32_e32 v174, v148, v149\n\t"
        "v_max3_f32 v174, v174, v150, v151\n\t"
        "v_max3_f32 v174, v174, v152, v153\n\t"
        "v_max3_f32 v174, v174, v154, v155\n\t"
        "s_cmp_eq_u32 s39, 0\n\t"
        "s_cbranch_scc1 .La23%=\n\t"
        "s_waitcnt vmcnt(0)\n\t"
        "s_lshl_b32 s80, s44, 7\n\t"
        "s_cmp_lt_i32 s44, 0\n\t"
        "s_cselect_b32 s80, s36, s80\n\t"
        "v_mov_b32_e32 v242, 1\n\t"
        "buffer_atomic_add v242, v250, %[rd], s80 offen sc0 sc1\n\t"
        "s_waitcnt vmcnt(0)\n\t"
        "v_readlane_b32 s84, v242, 0\n\t"
        "v_readlane_b32 s85, v242, 32\n\t"
        "v_mov_b32_e32 v243, 32\n\t"
        "s_and_b32 s80, s44, 3\n\t"
        "s_lshl_b32 s80, s80, 12\n\t"
        "s_lshr_b32 s81, s44, 2\n\t"
        "s_lshl_b32 s81, s81, 3\n\t"
        "s_add_u32 s80, s80, s81\n\t"
        "s_and_b32 s81, s44, 1\n\t"
        "s_lshl_b32 s81, s81, 13\n\t"
        "s_lshr_b32 s82, s44, 1\n\t"
        "s_lshl_b32 s82, s82, 2\n\t"
        "s_add_u32 s81, s81, s82\n\t"
        "s_cmp_lt_i32 s44, 60\n\t"
        "s_cselect_b32 s80, s80, s81\n\t"
        "s_cselect_b32 s81, 4, 0x1000\n\t"
        "s_add_u32 s81, s81, s80\n\t"
        "s_cmp_eq_u32 s84, 31\n\t"
        "s_cselect_b32 s80, s80, s36\n\t"
        "s_cmp_eq_u32 s85, 31\n\t"
        "s_cselect_b32 s81, s81, s36\n\t"
        "s_cmp_lt_i32 s44, 0\n\t"
        "s_cselect_b32 s80, s36, s80\n\t"
        "s_cselect_b32 s81, s36, s81\n\t"
        "buffer_store_dword v243, v251, %[rd], s80 offen sc1\n\t"
        "buffer_store_dword v243, v251, %[rd], s81 offen sc1\n\t"
        "s_mov_b32 s44, -1\n\t"
        "s_mov_b32 s97, 0\n\t"
        ".La22%=:\n\t"
        "s_cmp_gt_u32 s40, s37\n\t"
        "s_cbranch_scc1 .La22%=x\n\t"
        "s_memrealtime s[98:99]\n\t"
        "s_add_u32 s97, s97, 1\n\t"
        "s_cmp_lt_u32 s97, 0x400000\n\t"
        "s_cbranch_scc0 .Lgu%=\n\t"
        "s_sub_u32 s80, s37, s40\n\t"
        "s_cmp_ge_u32 s37, %c[efrom]\n\t"
        "s_subb_u32 s80, s80, 0\n\t"
        "s_mul_i32 s80, s80, %c[smin]\n\t"
        "s_waitcnt lgkmcnt(0)\n\t"
        "s_sub_u32 s81, s98, s41\n\t"
        "s_cmp_ge_i32 s81, s80\n\t"
        "s_cbranch_scc1 .La22%=p\n\t"
        "s_sleep 16\n\t"
        "s_branch .La22%=\n\t"
        ".La22%=p:\n\t"
        "buffer_load_dword v241, v247, %[rd], 0 offen sc1\n\t"
        "s_waitcnt vmcnt(0)\n\t"
        "v_readlane_b32 s80, v241, 0\n\t"
        "v_readlane_b32 s81, v241, 1\n\t"
        "v_readlane_b32 s82, v241, 2\n\t"
        "s_min_u32 s80, s80, s81\n\t"
        "s_min_u32 s80, s80, s82\n\t"
        "s_cmp_gt_u32 s80, s40\n\t"
        "s_cselect_b32 s41, s98, s41\n\t"
        "s_max_u32 s40, s40, s80\n\t"
        "s_cmp_gt_u32 s40, s37\n\t"
        "s_cbranch_scc1 .La22%=x\n\t"
        "s_sleep %c[pslp]\n\t"
        "s_branch .La22%=\n\t"
        ".La22%=x:\n\t"
        "s_lshl_b32 s81, s37, 17\n\t"
        "s_add_u32 s81, s81, %[km]\n\t"
        "buffer_load_dwordx4 a[96:99], v244, %[rd], s81 offen offset:0 sc1\n\t"
        "buffer_load_dwordx4 a[100:103], v244, %[rd], s81 offen offset:1024 sc1\n\t"
        "buffer_load_dwordx2 a[104:105], v245, %[rd], s81 offen offset:2048 sc1\n\t"
        "buffer_load_dwordx2 a[106:107], v245, %[rd], s81 offen offset:2304 sc1\n\t"
        "buffer_load_dwordx2 a[108:109], v245, %[rd], s81 offen offset:2560 sc1\n\t"
        "buffer_load_dwordx2 a[110:111], v245, %[rd], s81 offen offset:2816 sc1\n\t"
        "buffer_load_dwordx2 a[112:113], v245, %[rd], s81 offen offset:3072 sc1\n\t"
        "buffer_load_dwordx2 a[114:115], v245, %[rd], s81 offen offset:3328 sc1\n\t"
        "buffer_load_dwordx2 a[116:117], v245, %[rd], s81 offen offset:3584 sc1\n\t"
        "buffer_load_dwordx2 a[118:119], v245, %[rd], s81 offen offset:3840 sc1\n\t"
        "s_waitcnt vmcnt(0)\n\t"
        ".La23%=:\n\t"
        "ds_write_b128 v248, a[96:99] offset:0\n\t"
        "ds_write_b128 v248, a[100:103] offset:1024\n\t"
        "ds_read_b64_tr_b16 v[120:121], v249 offset:0\n\t"
        "ds_read_b64_tr_b16 v[122:123], v249 offset:64\n\t"
        "ds_read_b64_tr_b16 v[124:125], v249 offset:512\n\t"
        "ds_read_b64_tr_b16 v[126:127], v249 offset:576\n\t"
        "ds_read_b64_tr_b16 v[128:129], v249 offset:1024\n\t"
        "ds_read_b64_tr_b16 v[130:131], v249 offset:1088\n\t"
        "ds_read_b64_tr_b16 v[132:133], v249 offset:1536\n\t"
        "ds_read_b64_tr_b16 v[134:135], v249 offset:1600\n\t"
        "s_waitcnt lgkmcnt(6)\n\t"
        "v_mfma_f32_16x16x32_bf16 v[144:147], v[120:123], a[32:35], 0\n\t"
        "v_mfma_f32_16x16x32_bf16 v[156:159], v[120:123], a[48:51], 0\n\t"
        "s_waitcnt lgkmcnt(4)\n\t"
        "v_mfma_f32_16x16x32_bf16 v[144:147], v[124:127], a[36:39], v[144:147]\n\t"
        "v_mfma_f32_16x16x32_bf16 v[156:159], v[124:127], a[52:55], v[156:159]\n\t"
        "s_waitcnt lgkmcnt(2)\n\t"
        "v_mfma_f32_16x16x32_bf16 v[144:147], v[128:131], a[40:43], v[144:147]\n\t"
        "v_mfma_f32_16x16x32_bf16 v[156:159], v[128:131], a[56:59], v[156:159]\n\t"
        "s_waitcnt lgkmcnt(0)\n\t"
        "v_mfma_f32_16x16x32_bf16 v[144:147], v[132:135], a[44:47], v[144:147]\n\t"
        "v_mfma_f32_16x16x32_bf16 v[156:159], v[132:135], a[60:63], v[156:159]\n\t"
        "s_nop 1\n\t"
        "v_mov_b32_e32 v252, v252\n\t"
        "s_nop 1\n\t"
        "v_mov_b32_e32 v252, v252\n\t"
        "s_nop 0\n\t"
        "v_cvt_pk_bf16_f32 v144, 0, v144\n\t"
        "v_cvt_pk_bf16_f32 v145, 0, v145\n\t"
        "v_cvt_pk_bf16_f32 v146, 0, v146\n\t"
        "v_cvt_pk_bf16_f32 v147, 0, v147\n\t"
        "v_mul_f32_e32 v144, 0x3db504f3, v144\n\t"
        "v_mul_f32_e32 v145, 0x3db504f3, v145\n\t"
        "v_mul_f32_e32 v146, 0x3db504f3, v146\n\t"
        "v_mul_f32_e32 v147, 0x3db504f3, v147\n\t"
        "v_cvt_pk_bf16_f32 v144, 0, v144\n\t"
        "v_cvt_pk_bf16_f32 v145, 0, v145\n\t"
        "v_cvt_pk_bf16_f32 v146, 0, v146\n\t"
        "v_cvt_pk_bf16_f32 v147, 0, v147\n\t"
        "v_cndmask_b32_e64 v144, v144, v254, s[56:57]\n\t"
        "v_cndmask_b32_e64 v145, v145, v254, s[56:57]\n\t"
        "v_cndmask_b32_e64 v146, v146, v254, s[56:57]\n\t"
        "v_cndmask_b32_e64 v147, v147, v254, s[56:57]\n\t"
        "v_max3_f32 v172, v172, v144, v145\n\t"
        "v_max3_f32 v173, v172, v146, v147\n\t"
        "v_max3_f32 v172, v172, v146, v147\n\t"
        "s_nop 1\n\t"
        "v_permlane16_swap_b32_e32 v172, v173\n\t"
        "v_max_f32_e32 v172, v172, v173\n\t"
        "v_mov_b32_e32 v173, v172\n\t"
        "s_nop 1\n\t"
        "v_permlane32_swap_b32_e32 v172, v173\n\t"
        "v_max_f32_e32 v172, v172, v173\n\t"
        "v_sub_f32_e32 v136, v136, v172\n\t"
        "v_sub_f32_e32 v137, v137, v172\n\t"
        "v_sub_f32_e32 v138, v138, v172\n\t"
        "v_sub_f32_e32 v139, v139, v172\n\t"
        "v_sub_f32_e32 v140, v140, v172\n\t"
        "v_sub_f32_e32 v144, v144, v172\n\t"
        "v_sub_f32_e32 v145, v145, v172\n\t"
        "v_sub_f32_e32 v146, v146, v172\n\t"
        "v_sub_f32_e32 v147, v147, v172\n\t"
        "v_mul_f32_e32 v136, 0x3fb8aa3b, v136\n\t"
        "v_mul_f32_e32 v137, 0x3fb8aa3b, v137\n\t"
        "v_mul_f32_e32 v138, 0x3fb8aa3b, v138\n\t"
        "v_mul_f32_e32 v139, 0x3fb8aa3b, v139\n\t"
        "v_mul_f32_e32 v140, 0x3fb8aa3b, v140\n\t"
        "v_mul_f32_e32 v144, 0x3fb8aa3b, v144\n\t"
        "v_mul_f32_e32 v145, 0x3fb8aa3b, v145\n\t"
        "v_mul_f32_e32 v146, 0x3fb8aa3b, v146\n\t"
        "v_mul_f32_e32 v147, 0x3fb8aa3b, v147\n\t"
        "v_exp_f32_e32 v136, v136\n\t"
        "v_exp_f32_e32 v137, v137\n\t"
        "v_exp_f32_e32 v138, v138\n\t"
        "v_exp_f32_e32 v139, v139\n\t"
        "v_exp_f32_e32 v140, v140\n\t"
        "v_exp_f32_e32 v144, v144\n\t"
        "v_exp_f32_e32 v145, v145\n\t"
        "v_exp_f32_e32 v146, v146\n\t"
        "v_exp_f32_e32 v147, v147\n\t"
        "v_mul_f32_e32 v172, 4.0, v140\n\t"
        "v_add_f32_e32 v172, v172, v136\n\t"
        "v_add_f32_e32 v172, v172, v137\n\t"
        "v_add_f32_e32 v172, v172, v138\n\t"
        "v_add_f32_e32 v172, v172, v139\n\t"
        "v_add_f32_e32 v172, v172, v144\n\t"
        "v_add_f32_e32 v172, v172, v145\n\t"
        "v_add_f32_e32 v172, v172, v146\n\t"
        "v_add_f32_e32 v172, v172, v147\n\t"
        "v_mov_b32_e32 v173, v172\n\t"
        "s_nop 1\n\t"
        "v_permlane16_swap_b32_e32 v172, v173\n\t"
        "v_add_f32_e32 v172, v172, v173\n\t"
        "v_mov_b32_e32 v173, v172\n\t"
        "s_nop 1\n\t"
        "v_permlane32_swap_b32_e32 v172, v173\n\t"
        "v_add_f32_e32 v172, v172, v173\n\t"
        "v_rcp_f32_e32 v172, v172\n\t"
        "s_nop 0\n\t"
        "v_mul_f32_e32 v136, v136, v172\n\t"
        "v_mul_f32_e32 v137, v137, v172\n\t"
        "v_mul_f32_e32 v138, v138, v172\n\t"
        "v_mul_f32_e32 v139, v139, v172\n\t"
        "v_mul_f32_e32 v140, v140, v172\n\t"
        "v_mul_f32_e32 v144, v144, v172\n\t"
        "v_mul_f32_e32 v145, v145, v172\n\t"
        "v_mul_f32_e32 v146, v146, v172\n\t"
        "v_mul_f32_e32 v147, v147, v172\n\t"
        "v_cvt_pk_bf16_f32 v160, v136, v137\n\t"
        "v_cvt_pk_bf16_f32 v161, v138, v139\n\t"
        "v_cvt_pk_bf16_f32 v162, v140, v140\n\t"
        "v_cvt_pk_bf16_f32 v163, v140, v140\n\t"
        "v_cvt_pk_bf16_f32 v164, v144, v145\n\t"
        "v_cvt_pk_bf16_f32 v165, v146, v147\n\t"
        "v_mfma_f32_16x16x32_bf16 v[176:179], a[64:67], v[160:163], 0\n\t"
        "v_cvt_pk_bf16_f32 v156, 0, v156\n\t"
        "v_cvt_pk_bf16_f32 v157, 0, v157\n\t"
        "v_cvt_pk_bf16_f32 v158, 0, v158\n\t"
        "v_mfma_f32_16x16x32_bf16 v[180:183], a[68:71], v[160:163], 0\n\t"
        "v_cvt_pk_bf16_f32 v159, 0, v159\n\t"
        "v_mul_f32_e32 v156, 0x3db504f3, v156\n\t"
        "v_mul_f32_e32 v157, 0x3db504f3, v157\n\t"
        "v_mfma_f32_16x16x32_bf16 v[184:187], a[72:75], v[160:163], 0\n\t"
        "v_mul_f32_e32 v158, 0x3db504f3, v158\n\t"
        "v_mul_f32_e32 v159, 0x3db504f3, v159\n\t"
        "v_cvt_pk_bf16_f32 v156, 0, v156\n\t"
        "v_mfma_f32_16x16x32_bf16 v[188:191], a[76:79], v[160:163], 0\n\t"
        "v_cvt_pk_bf16_f32 v157, 0, v157\n\t"
        "v_cvt_pk_bf16_f32 v158, 0, v158\n\t"
        "v_cvt_pk_bf16_f32 v159, 0, v159\n\t"
        "v_mfma_f32_16x16x32_bf16 v[192:195], a[80:83], v[160:163], 0\n\t"
        "v_cndmask_b32_e64 v156, v156, v254, s[56:57]\n\t"
        "v_cndmask_b32_e64 v157, v157, v254, s[56:57]\n\t"
        "v_cndmask_b32_e64 v158, v158, v254, s[56:57]\n\t"
        "v_mfma_f32_16x16x32_bf16 v[196:199], a[84:87], v[160:163], 0\n\t"
        "v_cndmask_b32_e64 v159, v159, v254, s[56:57]\n\t"
        "v_max3_f32 v174, v174, v156, v157\n\t"
        "v_max3_f32 v175, v174, v158, v159\n\t"
        "v_mfma_f32_16x16x32_bf16 v[200:203], a[88:91], v[160:163], 0\n\t"
        "v_max3_f32 v174, v174, v158, v159\n\t"
        "s_nop 1\n\t"
        "v_permlane16_swap_b32_e32 v174, v175\n\t"
        "v_max_f32_e32 v174, v174, v175\n\t"
        "v_mfma_f32_16x16x32_bf16 v[204:207], a[92:95], v[160:163], 0\n\t"
        "v_mov_b32_e32 v175, v174\n\t"
        "s_nop 1\n\t"
        "v_permlane32_swap_b32_e32 v174, v175\n\t"
        "v_max_f32_e32 v174, v174, v175\n\t"
        "v_mfma_f32_16x16x16_bf16 v[176:179], a[104:105], v[164:165], v[176:179]\n\t"
        "v_sub_f32_e32 v148, v148, v174\n\t"
        "v_sub_f32_e32 v149, v149, v174\n\t"
        "v_sub_f32_e32 v150, v150, v174\n\t"
        "v_mfma_f32_16x16x16_bf16 v[180:183], a[106:107], v[164:165], v[180:183]\n\t"
        "v_sub_f32_e32 v151, v151, v174\n\t"
        "v_sub_f32_e32 v152, v152, v174\n\t"
        "v_sub_f32_e32 v153, v153, v174\n\t"
        "v_mfma_f32_16x16x16_bf16 v[184:187], a[108:109], v[164:165], v[184:187]\n\t"
        "v_sub_f32_e32 v154, v154, v174\n\t"
        "v_sub_f32_e32 v155, v155, v174\n\t"
        "v_sub_f32_e32 v156, v156, v174\n\t"
        "v_mfma_f32_16x16x16_bf16 v[188:191], a[110:111], v[164:165], v[188:191]\n\t"
        "v_sub_f32_e32 v157, v157, v174\n\t"
        "v_sub_f32_e32 v158, v158, v174\n\t"
        "v_sub_f32_e32 v159, v159, v174\n\t"
        "v_mfma_f32_16x16x16_bf16 v[192:195], a[112:113], v[164:165], v[192:195]\n\t"
        "v_mul_f32_e32 v148, 0x3fb8aa3b, v148\n\t"
        "v_mul_f32_e32 v149, 0x3fb8aa3b, v149\n\t"
        "v_mul_f32_e32 v150, 0x3fb8aa3b, v150\n\t"
        "v_mfma_f32_16x16x16_bf16 v[196:199], a[114:115], v[164:165], v[196:199]\n\t"
        "v_mul_f32_e32 v151, 0x3fb8aa3b, v151\n\t"
        "v_mul_f32_e32 v152, 0x3fb8aa3b, v152\n\t"
        "v_mul_f32_e32 v153, 0x3fb8aa3b, v153\n\t"
        "v_mfma_f32_16x16x16_bf16 v[200:203], a[116:117], v[164:165], v[200:203]\n\t"
        "v_mul_f32_e32 v154, 0x3fb8aa3b, v154\n\t"
        "v_mul_f32_e32 v155, 0x3fb8aa3b, v155\n\t"
        "v_mul_f32_e32 v156, 0x3fb8aa3b, v156\n\t"
        "v_mfma_f32_16x16x16_bf16 v[204:207], a[118:119], v[164:165], v[204:207]\n\t"
        "v_mul_f32_e32 v157, 0x3fb8aa3b, v157\n\t"
        "v_mul_f32_e32 v158, 0x3fb8aa3b, v158\n\t"
        "v_mul_f32_e32 v159, 0x3fb8aa3b, v159\n\t"
        "v_exp_f32_e32 v148, v148\n\t"
        "v_exp_f32_e32 v149, v149\n\t"
        "v_exp_f32_e32 v150, v150\n\t"
        "v_exp_f32_e32 v151, v151\n\t"
        "v_exp_f32_e32 v152, v152\n\t"
        "v_exp_f32_e32 v153, v153\n\t"
        "v_exp_f32_e32 v154, v154\n\t"
        "v_exp_f32_e32 v155, v155\n\t"
        "v_exp_f32_e32 v156, v156\n\t"
        "v_exp_f32_e32 v157, v157\n\t"
        "v_exp_f32_e32 v158, v158\n\t"
        "v_exp_f32_e32 v159, v159\n\t"
        "v_add_f32_e32 v174, v148, v149\n\t"
        "v_add_f32_e32 v174, v174, v150\n\t"
        "v_add_f32_e32 v174, v174, v151\n\t"
        "v_add_f32_e32 v174, v174, v152\n\t"
        "v_add_f32_e32 v174, v174, v153\n\t"
        "v_add_f32_e32 v174, v174, v154\n\t"
        "v_add_f32_e32 v174, v174, v155\n\t"
        "v_add_f32_e32 v174, v174, v156\n\t"
        "v_add_f32_e32 v174, v174, v157\n\t"
        "v_add_f32_e32 v174, v174, v158\n\t"
        "v_add_f32_e32 v174, v174, v159\n\t"
        "v_mov_b32_e32 v175, v174\n\t"
        "s_nop 1\n\t"
        "v_permlane16_swap_b32_e32 v174, v175\n\t"
        "v_add_f32_e32 v174, v174, v175\n\t"
        "v_mov_b32_e32 v175, v174\n\t"
        "s_nop 1\n\t"
        "v_permlane32_swap_b32_e32 v174, v175\n\t"
        "v_add_f32_e32 v174, v174, v175\n\t"
        "v_rcp_f32_e32 v174, v174\n\t"
        "s_nop 0\n\t"
        "v_mul_f32_e32 v148, v148, v174\n\t"
        "v_mul_f32_e32 v149, v149, v174\n\t"
        "v_mul_f32_e32 v150, v150, v174\n\t"
        "v_mul_f32_e32 v151, v151, v174\n\t"
        "v_mul_f32_e32 v152, v152, v174\n\t"
        "v_mul_f32_e32 v153, v153, v174\n\t"
        "v_mul_f32_e32 v154, v154, v174\n\t"
        "v_mul_f32_e32 v155, v155, v174\n\t"
        "v_mul_f32_e32 v156, v156, v174\n\t"
        "v_mul_f32_e32 v157, v157, v174\n\t"
        "v_mul_f32_e32 v158, v158, v174\n\t"
        "v_mul_f32_e32 v159, v159, v174\n\t"
        "v_cvt_pk_bf16_f32 v166, v148, v149\n\t"
        "v_cvt_pk_bf16_f32 v167, v150, v151\n\t"
        "v_cvt_pk_bf16_f32 v168, v152, v153\n\t"
        "v_cvt_pk_bf16_f32 v169, v154, v155\n\t"
        "v_cvt_pk_bf16_f32 v170, v156, v157\n\t"
        "v_cvt_pk_bf16_f32 v171, v158, v159\n\t"
        "s_waitcnt vmcnt(38)\n\t"
        "s_lshl_b32 s80, s44, 7\n\t"
        "s_cmp_lt_i32 s44, 0\n\t"
        "s_cselect_b32 s80, s36, s80\n\t"
        "v_mov_b32_e32 v242, 1\n\t"
        "buffer_atomic_add v242, v250, %[rd], s80 offen sc0 sc1\n\t"
        "s_lshl_b32 s86, s37, 18\n\t"
        "s_add_u32 s86, s86, %[ox]\n\t"
        "v_mfma_f32_16x16x32_bf16 v[208:211], a[64:67], v[166:169], 0\n\t"
        "v_cvt_pk_bf16_f32 v176, v176, v177\n\t"
        "v_cvt_pk_bf16_f32 v177, v178, v179\n\t"
        "v_mfma_f32_16x16x32_bf16 v[212:215], a[68:71], v[166:169], 0\n\t"
        "v_cvt_pk_bf16_f32 v178, v180, v181\n\t"
        "v_cvt_pk_bf16_f32 v179, v182, v183\n\t"
        "v_mfma_f32_16x16x32_bf16 v[216:219], a[72:75], v[166:169], 0\n\t"
        "buffer_store_dwordx4 v[176:179], v244, %[rd], s86 offen sc1\n\t"
        "s_add_u32 s86, s86, 4096\n\t"
        "v_cvt_pk_bf16_f32 v184, v184, v185\n\t"
        "v_mfma_f32_16x16x32_bf16 v[220:223], a[76:79], v[166:169], 0\n\t"
        "v_cvt_pk_bf16_f32 v185, v186, v187\n\t"
        "v_cvt_pk_bf16_f32 v186, v188, v189\n\t"
        "v_mfma_f32_16x16x32_bf16 v[224:227], a[80:83], v[166:169], 0\n\t"
        "v_cvt_pk_bf16_f32 v187, v190, v191\n\t"
        "buffer_store_dwordx4 v[184:187], v244, %[rd], s86 offen sc1\n\t"
        "s_add_u32 s86, s86, 4096\n\t"
        "v_mfma_f32_16x16x32_bf16 v[228:231], a[84:87], v[166:169], 0\n\t"
        "v_cvt_pk_bf16_f32 v192, v192, v193\n\t"
        "v_cvt_pk_bf16_f32 v193, v194, v195\n\t"
        "v_mfma_f32_16x16x32_bf16 v[232:235], a[88:91], v[166:169], 0\n\t"
        "v_cvt_pk_bf16_f32 v194, v196, v197\n\t"
        "v_cvt_pk_bf16_f32 v195, v198, v199\n\t"
        "v_mfma_f32_16x16x32_bf16 v[236:239], a[92:95], v[166:169], 0\n\t"
        "buffer_store_dwordx4 v[192:195], v244, %[rd], s86 offen sc1\n\t"
        "s_add_u32 s86, s86, 4096\n\t"
        "v_cvt_pk_bf16_f32 v200, v200, v201\n\t"
        "v_mfma_f32_16x16x16_bf16 v[208:211], a[104:105], v[170:171], v[208:211]\n\t"
        "v_cvt_pk_bf16_f32 v201, v202, v203\n\t"
        "v_cvt_pk_bf16_f32 v202, v204, v205\n\t"
        "v_mfma_f32_16x16x16_bf16 v[212:215], a[106:107], v[170:171], v[212:215]\n\t"
        "v_cvt_pk_bf16_f32 v203, v206, v207\n\t"
        "buffer_store_dwordx4 v[200:203], v244, %[rd], s86 offen sc1\n\t"
        "s_add_u32 s86, s86, 118784\n\t"
        "v_mfma_f32_16x16x16_bf16 v[216:219], a[108:109], v[170:171], v[216:219]\n\t"
        "v_mfma_f32_16x16x16_bf16 v[220:223], a[110:111], v[170:171], v[220:223]\n\t"
        "v_mfma_f32_16x16x16_bf16 v[224:227], a[112:113], v[170:171], v[224:227]\n\t"
        "v_mfma_f32_16x16x16_bf16 v[228:231], a[114:115], v[170:171], v[228:231]\n\t"
        "v_mfma_f32_16x16x16_bf16 v[232:235], a[116:117], v[170:171], v[232:235]\n\t"
        "v_mfma_f32_16x16x16_bf16 v[236:239], a[118:119], v[170:171], v[236:239]\n\t"
        "v_cvt_pk_bf16_f32 v208, v208, v209\n\t"
        "v_cvt_pk_bf16_f32 v209, v210, v211\n\t"
        "v_cvt_pk_bf16_f32 v210, v212, v213\n\t"
        "v_cvt_pk_bf16_f32 v211, v214, v215\n\t"
        "buffer_store_dwordx4 v[208:211], v244, %[rd], s86 offen sc1\n\t"
        "s_add_u32 s86, s86, 4096\n\t"
        "v_cvt_pk_bf16_f32 v216, v216, v217\n\t"
        "v_cvt_pk_bf16_f32 v217, v218, v219\n\t"
        "v_cvt_pk_bf16_f32 v218, v220, v221\n\t"
        "v_cvt_pk_bf16_f32 v219, v222, v223\n\t"
        "buffer_store_dwordx4 v[216:219], v244, %[rd], s86 offen sc1\n\t"
        "s_add_u32 s86, s86, 4096\n\t"
        "v_cvt_pk_bf16_f32 v224, v224, v225\n\t"
        "v_cvt_pk_bf16_f32 v225, v226, v227\n\t"
        "v_cvt_pk_bf16_f32 v226, v228, v229\n\t"
        "v_cvt_pk_bf16_f32 v227, v230, v231\n\t"
        "buffer_store_dwordx4 v[224:227], v244, %[rd], s86 offen sc1\n\t"
        "s_add_u32 s86, s86, 4096\n\t"
        "v_cvt_pk_bf16_f32 v232, v232, v233\n\t"
        "v_cvt_pk_bf16_f32 v233, v234, v235\n\t"
        "v_cvt_pk_bf16_f32 v234, v236, v237\n\t"
        "v_cvt_pk_bf16_f32 v235, v238, v239\n\t"
        "buffer_store_dwordx4 v[232:235], v244, %[rd], s86 offen sc1\n\t"
        "s_add_u32 s86, s86, 118784\n\t"
        "s_mov_b32 s45, s44\n\t"
        "s_mov_b32 s44, s37\n\t"
        "s_add_u32 s80, s37, 4\n\t"
        "s_cmp_gt_u32 s40, s80\n\t"
        "s_cselect_b32 s80, s46, 1\n\t"
        "s_cmp_eq_u32 s80, 0\n\t"
        "s_cbranch_scc1 .La24%=\n\t"
        "s_waitcnt vmcnt(0)\n\t"
        "v_readlane_b32 s84, v242, 0\n\t"
        "v_readlane_b32 s85, v242, 32\n\t"
        "v_mov_b32_e32 v243, 32\n\t"
        "s_and_b32 s80, s45, 3\n\t"
        "s_lshl_b32 s80, s80, 12\n\t"
        "s_lshr_b32 s81, s45, 2\n\t"
        "s_lshl_b32 s81, s81, 3\n\t"
        "s_add_u32 s80, s80, s81\n\t"
        "s_and_b32 s81, s45, 1\n\t"
        "s_lshl_b32 s81, s81, 13\n\t"
        "s_lshr_b32 s82, s45, 1\n\t"
        "s_lshl_b32 s82, s82, 2\n\t"
        "s_add_u32 s81, s81, s82\n\t"
        "s_cmp_lt_i32 s45, 60\n\t"
        "s_cselect_b32 s80, s80, s81\n\t"
        "s_cselect_b32 s81, 4, 0x1000\n\t"
        "s_add_u32 s81, s81, s80\n\t"
        "s_cmp_eq_u32 s84, 31\n\t"
        "s_cselect_b32 s80, s80, s36\n\t"
        "s_cmp_eq_u32 s85, 31\n\t"
        "s_cselect_b32 s81, s81, s36\n\t"
        "s_cmp_lt_i32 s45, 0\n\t"
        "s_cselect_b32 s80, s36, s80\n\t"
        "s_cselect_b32 s81, s36, s81\n\t"
        "buffer_store_dword v243, v251, %[rd], s80 offen sc1\n\t"
        "buffer_store_dword v243, v251, %[rd], s81 offen sc1\n\t"
        "s_lshl_b32 s80, s44, 7\n\t"
        "s_cmp_lt_i32 s44, 0\n\t"
        "s_cselect_b32 s80, s36, s80\n\t"
        "v_mov_b32_e32 v242, 1\n\t"
        "buffer_atomic_add v242, v250, %[rd], s80 offen sc0 sc1\n\t"
        "s_waitcnt vmcnt(0)\n\t"
        "v_readlane_b32 s84, v242, 0\n\t"
        "v_readlane_b32 s85, v242, 32\n\t"
        "v_mov_b32_e32 v243, 32\n\t"
        "s_and_b32 s80, s44, 3\n\t"
        "s_lshl_b32 s80, s80, 12\n\t"
        "s_lshr_b32 s81, s44, 2\n\t"
        "s_lshl_b32 s81, s81, 3\n\t"
        "s_add_u32 s80, s80, s81\n\t"
        "s_and_b32 s81, s44, 1\n\t"
        "s_lshl_b32 s81, s81, 13\n\t"
        "s_lshr_b32 s82, s44, 1\n\t"
        "s_lshl_b32 s82, s82, 2\n\t"
        "s_add_u32 s81, s81, s82\n\t"
        "s_cmp_lt_i32 s44, 60\n\t"
        "s_cselect_b32 s80, s80, s81\n\t"
        "s_cselect_b32 s81, 4, 0x1000\n\t"
        "s_add_u32 s81, s81, s80\n\t"
        "s_cmp_eq_u32 s84, 31\n\t"
        "s_cselect_b32 s80, s80, s36\n\t"
        "s_cmp_eq_u32 s85, 31\n\t"
        "s_cselect_b32 s81, s81, s36\n\t"
        "s_cmp_lt_i32 s44, 0\n\t"
        "s_cselect_b32 s80, s36, s80\n\t"
        "s_cselect_b32 s81, s36, s81\n\t"
        "buffer_store_dword v243, v251, %[rd], s80 offen sc1\n\t"
        "buffer_store_dword v243, v251, %[rd], s81 offen sc1\n\t"
        "s_mov_b32 s44, -1\n\t"
        "s_mov_b32 s45, -1\n\t"
        ".La24%=:\n\t"
        "s_mov_b32 s38, s46\n\t"
        "s_mov_b32 s39, s47\n\t"
        "s_add_u32 s37, s37, 4\n\t"
        "s_cmp_lt_u32 s37, 64\n\t"
        "s_cbranch_scc1 .Ltop%=\n\t"
        "s_endpgm\n\t"
        ".Lgu%=:\n\t"
        "v_mov_b32_e32 v0, 64\n\t"
        "s_mov_b32 s80, %c[ferr]\n\t"
        "buffer_store_dword v0, off, %[rd], s80 sc1\n\t"
        "s_endpgm"
        :: [rd] "s"(rd), [w] "s"(wv), [tag] "s"(tag), [kx] "s"(kx), [qx] "s"(qx), [vx] "s"(vx), [km] "s"(km), [ox] "s"(ox), [blk] "s"(h),
          [ferr] "i"(F_ERR), [dbg] "i"(W_DBG), [pslp] "i"(P_SLP), [aslp] "i"(AT_SLP), [bslp] "i"(BT_SLP), [efrom] "i"(EARLY_FROM), [smin] "i"(STEP_MIN), "{v[244:251]}"(pc), "{v252}"(dq)
        : "memory", "vcc", "scc", "s36", "s37", "s38", "s39", "s40", "s41", "s42", "s43", "s44", "s45", "s46", "s47", "s48", "s49", "s50", "s51", "s52", "s53", "s54", "s55", "s56", "s57", "s58", "s59", "s80", "s81", "s82", "s83", "s84", "s85", "s86", "s87", "s88", "s89", "s90", "s91", "s92", "s93", "s94", "s95", "s96", "s97", "s98", "s99");
}

constexpr int OP_P = 144;
__device__ __forceinline__ void oproj_block(const Params& p, const int wv, const int nt, const int rho, const int j, const unsigned tag, const unsigned g_t0) {
    const int w = wv, l = otid() & 63, n16 = l & 15, g = l >> 4;
    const v4u rd = mk_srd(p.ws, W_TOTAL), ro = mk_srd(p.out, S * D * 2), rx = mk_srd(p.Aq, S * D * 2);
    const unsigned snd = (unsigned)((w >> 1) ^ nt) << 31, own = snd ^ 0x80000000u;
    const unsigned ep = (nt == 0 && rho == 0 && w == 0) ? F_EP : 0xF0000000u;
    const int n0 = (nt >> 1) * 64 + 16 * w + 4 * g;
    const unsigned a_b = W_OX + (nt & 1) * 65536 + w * 1024 + l * 16, tr = (4 * g + (n16 >> 2)), tc = (n16 & 3) * 8;
    const v16u pc = {a_b, a_b + 4096, F_RDY + (rho * 32 + nt) * 128 + (l & 31) * 4, W_A + (n0 >> 2) * 16384 + n16 * 8, W_GATE + n16 * 4, (unsigned)((n16 * 1024 + n0) * 2),
                     W_XC + ((nt >> 1) * 4 + w) * 2048 + l * 16, (unsigned)(w * 8192 + l * 16), (unsigned)(w * 1024 + l * 16),
                     (unsigned)(w * 10240 + (l >> 3) * OP_P + (l & 7) * 16), w * 10240 + tr * OP_P + tc, WO_L + w * 16384 + tr * 128 + tc, 0u, 0u, 0u, 0u};
    asm volatile(
        "ds_write_b128 v185, v[192:195] offset:0\n\t"
        "ds_write_b128 v185, v[196:199] offset:1152\n\t"
        "ds_write_b128 v185, v[200:203] offset:2304\n\t"
        "ds_write_b128 v185, v[204:207] offset:3456\n\t"
        "ds_read_b64_tr_b16 a[0:1], v186 offset:0\n\t"
        "ds_read_b64_tr_b16 a[2:3], v186 offset:2304\n\t"
        "ds_read_b64_tr_b16 a[4:5], v186 offset:32\n\t"
        "ds_read_b64_tr_b16 a[6:7], v186 offset:2336\n\t"
        "ds_read_b64_tr_b16 a[8:9], v186 offset:64\n\t"
        "ds_read_b64_tr_b16 a[10:11], v186 offset:2368\n\t"
        "ds_read_b64_tr_b16 a[12:13], v186 offset:96\n\t"
        "ds_read_b64_tr_b16 a[14:15], v186 offset:2400\n\t"
        "ds_write_b128 v185, v[208:211] offset:5120\n\t"
        "ds_write_b128 v185, v[212:215] offset:6272\n\t"
        "ds_write_b128 v185, v[216:219] offset:7424\n\t"
        "ds_write_b128 v185, v[220:223] offset:8576\n\t"
        "ds_read_b64_tr_b16 a[16:17], v186 offset:5120\n\t"
        "ds_read_b64_tr_b16 a[18:19], v186 offset:7424\n\t"
        "ds_read_b64_tr_b16 a[20:21], v186 offset:5152\n\t"
        "ds_read_b64_tr_b16 a[22:23], v186 offset:7456\n\t"
        "ds_read_b64_tr_b16 a[24:25], v186 offset:5184\n\t"
        "ds_read_b64_tr_b16 a[26:27], v186 offset:7488\n\t"
        "ds_read_b64_tr_b16 a[28:29], v186 offset:5216\n\t"
        "ds_read_b64_tr_b16 a[30:31], v186 offset:7520\n\t"
        "ds_write_b128 v185, v[224:227] offset:0\n\t"
        "ds_write_b128 v185, v[228:231] offset:1152\n\t"
        "ds_write_b128 v185, v[232:235] offset:2304\n\t"
        "ds_write_b128 v185, v[236:239] offset:3456\n\t"
        "ds_read_b64_tr_b16 a[32:33], v186 offset:0\n\t"
        "ds_read_b64_tr_b16 a[34:35], v186 offset:2304\n\t"
        "ds_read_b64_tr_b16 a[36:37], v186 offset:32\n\t"
        "ds_read_b64_tr_b16 a[38:39], v186 offset:2336\n\t"
        "ds_read_b64_tr_b16 a[40:41], v186 offset:64\n\t"
        "ds_read_b64_tr_b16 a[42:43], v186 offset:2368\n\t"
        "ds_read_b64_tr_b16 a[44:45], v186 offset:96\n\t"
        "ds_read_b64_tr_b16 a[46:47], v186 offset:2400\n\t"
        "ds_write_b128 v185, v[240:243] offset:5120\n\t"
        "ds_write_b128 v185, v[244:247] offset:6272\n\t"
        "ds_write_b128 v185, v[248:251] offset:7424\n\t"
        "ds_write_b128 v185, v[252:255] offset:8576\n\t"
        "ds_read_b64_tr_b16 a[48:49], v186 offset:5120\n\t"
        "ds_read_b64_tr_b16 a[50:51], v186 offset:7424\n\t"
        "ds_read_b64_tr_b16 a[52:53], v186 offset:5152\n\t"
        "ds_read_b64_tr_b16 a[54:55], v186 offset:7456\n\t"
        "ds_read_b64_tr_b16 a[56:57], v186 offset:5184\n\t"
        "ds_read_b64_tr_b16 a[58:59], v186 offset:7488\n\t"
        "ds_read_b64_tr_b16 a[60:61], v186 offset:5216\n\t"
        "ds_read_b64_tr_b16 a[62:63], v186 offset:7520\n\t"
        "ds_read_b64_tr_b16 a[64:65], v187 offset:0\n\t"
        "ds_read_b64_tr_b16 a[66:67], v187 offset:2048\n\t"
        "ds_read_b64_tr_b16 a[68:69], v187 offset:32\n\t"
        "ds_read_b64_tr_b16 a[70:71], v187 offset:2080\n\t"
        "ds_read_b64_tr_b16 a[72:73], v187 offset:64\n\t"
        "ds_read_b64_tr_b16 a[74:75], v187 offset:2112\n\t"
        "ds_read_b64_tr_b16 a[76:77], v187 offset:96\n\t"
        "ds_read_b64_tr_b16 a[78:79], v187 offset:2144\n\t"
        "ds_read_b64_tr_b16 a[80:81], v187 offset:4096\n\t"
        "ds_read_b64_tr_b16 a[82:83], v187 offset:6144\n\t"
        "ds_read_b64_tr_b16 a[84:85], v187 offset:4128\n\t"
        "ds_read_b64_tr_b16 a[86:87], v187 offset:6176\n\t"
        "ds_read_b64_tr_b16 a[88:89], v187 offset:4160\n\t"
        "ds_read_b64_tr_b16 a[90:91], v187 offset:6208\n\t"
        "ds_read_b64_tr_b16 a[92:93], v187 offset:4192\n\t"
        "ds_read_b64_tr_b16 a[94:95], v187 offset:6240\n\t"
        "ds_read_b64_tr_b16 a[96:97], v187 offset:8192\n\t"
        "ds_read_b64_tr_b16 a[98:99], v187 offset:10240\n\t"
        "ds_read_b64_tr_b16 a[100:101], v187 offset:8224\n\t"
        "ds_read_b64_tr_b16 a[102:103], v187 offset:10272\n\t"
        "ds_read_b64_tr_b16 a[104:105], v187 offset:8256\n\t"
        "ds_read_b64_tr_b16 a[106:107], v187 offset:10304\n\t"
        "ds_read_b64_tr_b16 a[108:109], v187 offset:8288\n\t"
        "ds_read_b64_tr_b16 a[110:111], v187 offset:10336\n\t"
        "ds_read_b64_tr_b16 a[112:113], v187 offset:12288\n\t"
        "ds_read_b64_tr_b16 a[114:115], v187 offset:14336\n\t"
        "ds_read_b64_tr_b16 a[116:117], v187 offset:12320\n\t"
        "ds_read_b64_tr_b16 a[118:119], v187 offset:14368\n\t"
        "ds_read_b64_tr_b16 a[120:121], v187 offset:12352\n\t"
        "ds_read_b64_tr_b16 a[122:123], v187 offset:14400\n\t"
        "ds_read_b64_tr_b16 a[124:125], v187 offset:12384\n\t"
        "ds_read_b64_tr_b16 a[126:127], v187 offset:14432\n\t"
        "ds_write_b128 v185, a[128:131] offset:0\n\t"
        "ds_write_b128 v185, a[132:135] offset:1152\n\t"
        "ds_write_b128 v185, a[136:139] offset:2304\n\t"
        "ds_write_b128 v185, a[140:143] offset:3456\n\t"
        "s_waitcnt lgkmcnt(0)\n\t"
        "ds_read_b64_tr_b16 a[128:129], v186 offset:0\n\t"
        "ds_read_b64_tr_b16 a[130:131], v186 offset:2304\n\t"
        "ds_read_b64_tr_b16 a[132:133], v186 offset:32\n\t"
        "ds_read_b64_tr_b16 a[134:135], v186 offset:2336\n\t"
        "ds_read_b64_tr_b16 a[136:137], v186 offset:64\n\t"
        "ds_read_b64_tr_b16 a[138:139], v186 offset:2368\n\t"
        "ds_read_b64_tr_b16 a[140:141], v186 offset:96\n\t"
        "ds_read_b64_tr_b16 a[142:143], v186 offset:2400\n\t"
        "ds_write_b128 v185, a[144:147] offset:5120\n\t"
        "ds_write_b128 v185, a[148:151] offset:6272\n\t"
        "ds_write_b128 v185, a[152:155] offset:7424\n\t"
        "ds_write_b128 v185, a[156:159] offset:8576\n\t"
        "s_waitcnt lgkmcnt(0)\n\t"
        "ds_read_b64_tr_b16 a[144:145], v186 offset:5120\n\t"
        "ds_read_b64_tr_b16 a[146:147], v186 offset:7424\n\t"
        "ds_read_b64_tr_b16 a[148:149], v186 offset:5152\n\t"
        "ds_read_b64_tr_b16 a[150:151], v186 offset:7456\n\t"
        "ds_read_b64_tr_b16 a[152:153], v186 offset:5184\n\t"
        "ds_read_b64_tr_b16 a[154:155], v186 offset:7488\n\t"
        "ds_read_b64_tr_b16 a[156:157], v186 offset:5216\n\t"
        "ds_read_b64_tr_b16 a[158:159], v186 offset:7520\n\t"
        "ds_write_b128 v185, a[160:163] offset:0\n\t"
        "ds_write_b128 v185, a[164:167] offset:1152\n\t"
        "ds_write_b128 v185, a[168:171] offset:2304\n\t"
        "ds_write_b128 v185, a[172:175] offset:3456\n\t"
        "s_waitcnt lgkmcnt(0)\n\t"
        "ds_read_b64_tr_b16 a[160:161], v186 offset:0\n\t"
        "ds_read_b64_tr_b16 a[162:163], v186 offset:2304\n\t"
        "ds_read_b64_tr_b16 a[164:165], v186 offset:32\n\t"
        "ds_read_b64_tr_b16 a[166:167], v186 offset:2336\n\t"
        "ds_read_b64_tr_b16 a[168:169], v186 offset:64\n\t"
        "ds_read_b64_tr_b16 a[170:171], v186 offset:2368\n\t"
        "ds_read_b64_tr_b16 a[172:173], v186 offset:96\n\t"
        "ds_read_b64_tr_b16 a[174:175], v186 offset:2400\n\t"
        "ds_write_b128 v185, a[176:179] offset:5120\n\t"
        "ds_write_b128 v185, a[180:183] offset:6272\n\t"
        "ds_write_b128 v185, a[184:187] offset:7424\n\t"
        "ds_write_b128 v185, a[188:191] offset:8576\n\t"
        "s_waitcnt lgkmcnt(0)\n\t"
        "ds_read_b64_tr_b16 a[176:177], v186 offset:5120\n\t"
        "ds_read_b64_tr_b16 a[178:179], v186 offset:7424\n\t"
        "ds_read_b64_tr_b16 a[180:181], v186 offset:5152\n\t"
        "ds_read_b64_tr_b16 a[182:183], v186 offset:7456\n\t"
        "ds_read_b64_tr_b16 a[184:185], v186 offset:5184\n\t"
        "ds_read_b64_tr_b16 a[186:187], v186 offset:7488\n\t"
        "ds_read_b64_tr_b16 a[188:189], v186 offset:5216\n\t"
        "ds_read_b64_tr_b16 a[190:191], v186 offset:7520\n\t"
        "ds_write_b128 v185, a[192:195] offset:0\n\t"
        "ds_write_b128 v185, a[196:199] offset:1152\n\t"
        "ds_write_b128 v185, a[200:203] offset:2304\n\t"
        "ds_write_b128 v185, a[204:207] offset:3456\n\t"
        "s_waitcnt lgkmcnt(0)\n\t"
        "ds_read_b64_tr_b16 a[192:193], v186 offset:0\n\t"
        "ds_read_b64_tr_b16 a[194:195], v186 offset:2304\n\t"
        "ds_read_b64_tr_b16 a[196:197], v186 offset:32\n\t"
        "ds_read_b64_tr_b16 a[198:199], v186 offset:2336\n\t"
        "ds_read_b64_tr_b16 a[200:201], v186 offset:64\n\t"
        "ds_read_b64_tr_b16 a[202:203], v186 offset:2368\n\t"
        "ds_read_b64_tr_b16 a[204:205], v186 offset:96\n\t"
        "ds_read_b64_tr_b16 a[206:207], v186 offset:2400\n\t"
        "ds_write_b128 v185, a[208:211] offset:5120\n\t"
        "ds_write_b128 v185, a[212:215] offset:6272\n\t"
        "ds_write_b128 v185, a[216:219] offset:7424\n\t"
        "ds_write_b128 v185, a[220:223] offset:8576\n\t"
        "s_waitcnt lgkmcnt(0)\n\t"
        "ds_read_b64_tr_b16 a[208:209], v186 offset:5120\n\t"
        "ds_read_b64_tr_b16 a[210:211], v186 offset:7424\n\t"
        "ds_read_b64_tr_b16 a[212:213], v186 offset:5152\n\t"
        "ds_read_b64_tr_b16 a[214:215], v186 offset:7456\n\t"
        "ds_read_b64_tr_b16 a[216:217], v186 offset:5184\n\t"
        "ds_read_b64_tr_b16 a[218:219], v186 offset:7488\n\t"
        "ds_read_b64_tr_b16 a[220:221], v186 offset:5216\n\t"
        "ds_read_b64_tr_b16 a[222:223], v186 offset:7520\n\t"
        "ds_write_b128 v185, a[224:227] offset:0\n\t"
        "ds_write_b128 v185, a[228:231] offset:1152\n\t"
        "ds_write_b128 v185, a[232:235] offset:2304\n\t"
        "ds_write_b128 v185, a[236:239] offset:3456\n\t"
        "s_waitcnt lgkmcnt(0)\n\t"
        "ds_read_b64_tr_b16 a[224:225], v186 offset:0\n\t"
        "ds_read_b64_tr_b16 a[226:227], v186 offset:2304\n\t"
        "ds_read_b64_tr_b16 a[228:229], v186 offset:32\n\t"
        "ds_read_b64_tr_b16 a[230:231], v186 offset:2336\n\t"
        "ds_read_b64_tr_b16 a[232:233], v186 offset:64\n\t"
        "ds_read_b64_tr_b16 a[234:235], v186 offset:2368\n\t"
        "ds_read_b64_tr_b16 a[236:237], v186 offset:96\n\t"
        "ds_read_b64_tr_b16 a[238:239], v186 offset:2400\n\t"
        "ds_write_b128 v185, a[240:243] offset:5120\n\t"
        "ds_write_b128 v185, a[244:247] offset:6272\n\t"
        "ds_write_b128 v185, a[248:251] offset:7424\n\t"
        "ds_write_b128 v185, a[252:255] offset:8576\n\t"
        "s_waitcnt lgkmcnt(0)\n\t"
        "ds_read_b64_tr_b16 a[240:241], v186 offset:5120\n\t"
        "ds_read_b64_tr_b16 a[242:243], v186 offset:7424\n\t"
        "ds_read_b64_tr_b16 a[244:245], v186 offset:5152\n\t"
        "ds_read_b64_tr_b16 a[246:247], v186 offset:7456\n\t"
        "ds_read_b64_tr_b16 a[248:249], v186 offset:5184\n\t"
        "ds_read_b64_tr_b16 a[250:251], v186 offset:7488\n\t"
        "ds_read_b64_tr_b16 a[252:253], v186 offset:5216\n\t"
        "ds_read_b64_tr_b16 a[254:255], v186 offset:7520\n\t"
        "s_waitcnt lgkmcnt(0)\n\t"
        "v_mov_b32_e32 v240, v176\n\t"
        "v_mov_b32_e32 v241, v177\n\t"
        "v_mov_b32_e32 v242, v178\n\t"
        "v_mov_b32_e32 v243, v179\n\t"
        "v_mov_b32_e32 v244, v180\n\t"
        "v_mov_b32_e32 v245, v181\n\t"
        "v_mov_b32_e32 v246, v182\n\t"
        "v_mov_b32_e32 v247, v183\n\t"
        "v_mov_b32_e32 v248, v184\n\t"
        "v_mov_b32_e32 v205, %[tag]\n\t"
        "v_mov_b32_e32 v207, %[tag]\n\t"
        "v_mov_b32_e32 v209, %[tag]\n\t"
        "v_mov_b32_e32 v211, %[tag]\n\t"
        "v_mov_b32_e32 v213, %[tag]\n\t"
        "v_mov_b32_e32 v215, %[tag]\n\t"
        "v_mov_b32_e32 v217, %[tag]\n\t"
        "v_mov_b32_e32 v219, %[tag]\n\t"
        "s_mov_b32 s36, 0xf0000000\n\t"
        "s_mov_b32 s37, 0\n\t"
        "s_mov_b32 s38, -1\n\t"
        "s_mov_b32 s39, -1\n\t"
        "s_mov_b32 s40, 0\n\t"
        "s_mov_b32 s43, 0\n\t"
        "s_lshl_b32 s45, %[w], 10\n\t"
        "s_add_u32 s45, s45, 65536\n\t"
        "s_lshl_b32 s44, %[rho], 1\n\t"
        "s_barrier\n\t"
        "s_mov_b32 s97, 0\n\t"
        "v_and_b32_e32 v186, 0x7c, v242\n\t"
        "s_mov_b32 s80, %c[fprep]\n\t"
        "s_mov_b32 s81, %c[fconv]\n\t"
        ".Lo1%=:\n\t"
        "buffer_load_dword v187, v186, %[rd], s80 offen sc1\n\t"
        "s_add_u32 s97, s97, 1\n\t"
        "s_cmp_lt_u32 s97, 0x200000\n\t"
        "s_cbranch_scc0 .Lgu%=\n\t"
        "s_waitcnt vmcnt(0)\n\t"
        "v_cmp_ne_u32_e32 vcc, %[tag], v187\n\t"
        "s_nop 3\n\t"
        "s_cbranch_vccz .Lo2%=\n\t"
        "s_sleep 8\n\t"
        "s_branch .Lo1%=\n\t"
        ".Lo2%=:\n\t"
        "buffer_load_dword v187, off, %[rd], s81 sc1\n\t"
        "s_add_u32 s97, s97, 1\n\t"
        "s_cmp_lt_u32 s97, 0x200000\n\t"
        "s_cbranch_scc0 .Lgu%=\n\t"
        "s_waitcnt vmcnt(0)\n\t"
        "v_cmp_gt_u32_e32 vcc, 0x100, v187\n\t"
        "s_nop 3\n\t"
        "s_cbranch_vccz .Lo3%=\n\t"
        "s_sleep 8\n\t"
        "s_branch .Lo2%=\n\t"
        ".Lo3%=:\n\t"
        ".Lnew%=:\n\t"
        "s_lshl_b32 s85, s37, 1\n\t"
        "s_and_b32 s80, s40, 0x3fffffff\n\t"
        "s_lshr_b32 s80, s80, s85\n\t"
        "s_andn2_b32 s80, 3, s80\n\t"
        "s_cbranch_scc0 .Lgo%=\n\t"
        "s_cmp_lt_i32 s39, 0\n\t"
        "s_cbranch_scc1 .Lo4%=\n\t"
        "s_waitcnt vmcnt(0)\n\t"
        "s_add_i32 s80, s39, 0\n\t"
        "s_lshl_b32 s80, s80, 15\n\t"
        "s_or_b32 s81, s39, %[snd]\n\t"
        "s_cmp_lt_i32 s81, 0\n\t"
        "s_cselect_b32 s83, s36, s80\n\t"
        "s_cbranch_scc1 .Lo7%=\n\t"
        "s_mov_b32 s97, 0\n\t"
        ".Lo5%=:\n\t"
        "v_xor_b32_e32 v186, %[tag], v161\n\t"
        "v_xor_b32_e32 v187, %[tag], v163\n\t"
        "v_xor_b32_e32 v188, %[tag], v165\n\t"
        "v_or3_b32 v186, v186, v187, v188\n\t"
        "v_xor_b32_e32 v187, %[tag], v167\n\t"
        "v_or_b32_e32 v186, v186, v187\n\t"
        "v_cmp_ne_u32_e32 vcc, 0, v186\n\t"
        "v_sub_f32_e32 v198, 1.0, v184\n\t"
        "v_add_f32_e32 v190, v152, v160\n\t"
        "v_add_f32_e32 v191, v153, v162\n\t"
        "v_add_f32_e32 v192, v154, v164\n\t"
        "v_add_f32_e32 v193, v155, v166\n\t"
        "s_cbranch_vccz .Lo6%=\n\t"
        "s_sleep %c[xslp]\n\t"
        "s_lshl_b32 s82, s83, 2\n\t"
        "buffer_load_dwordx4 v[160:163], v246, %[rd], s82 offen offset:0 sc1\n\t"
        "buffer_load_dwordx4 v[164:167], v246, %[rd], s82 offen offset:1024 sc1\n\t"
        "s_add_u32 s97, s97, 1\n\t"
        "s_cmp_lt_u32 s97, 0x200000\n\t"
        "s_cbranch_scc0 .Lgu%=\n\t"
        "s_waitcnt vmcnt(0)\n\t"
        "s_branch .Lo5%=\n\t"
        ".Lo6%=:\n\t"
        "v_cvt_pk_bf16_f32 v198, 0, v198\n\t"
        "v_cvt_pk_bf16_f32 v190, 0, v190\n\t"
        "v_cvt_pk_bf16_f32 v191, 0, v191\n\t"
        "v_cvt_pk_bf16_f32 v192, 0, v192\n\t"
        "v_cvt_pk_bf16_f32 v193, 0, v193\n\t"
        "v_lshlrev_b32_e32 v186, 16, v180\n\t"
        "v_lshlrev_b32_e32 v194, 16, v176\n\t"
        "v_and_b32_e32 v187, 0xffff0000, v180\n\t"
        "v_and_b32_e32 v195, 0xffff0000, v176\n\t"
        "v_lshlrev_b32_e32 v188, 16, v181\n\t"
        "v_lshlrev_b32_e32 v196, 16, v177\n\t"
        "v_and_b32_e32 v189, 0xffff0000, v181\n\t"
        "v_and_b32_e32 v197, 0xffff0000, v177\n\t"
        "v_mul_f32_e32 v186, v186, v194\n\t"
        "v_mul_f32_e32 v187, v187, v195\n\t"
        "v_mul_f32_e32 v188, v188, v196\n\t"
        "v_mul_f32_e32 v189, v189, v197\n\t"
        "v_mul_f32_e32 v190, v198, v190\n\t"
        "v_mul_f32_e32 v191, v198, v191\n\t"
        "v_mul_f32_e32 v192, v198, v192\n\t"
        "v_mul_f32_e32 v193, v198, v193\n\t"
        "v_cvt_pk_bf16_f32 v186, 0, v186\n\t"
        "v_cvt_pk_bf16_f32 v187, 0, v187\n\t"
        "v_cvt_pk_bf16_f32 v188, 0, v188\n\t"
        "v_cvt_pk_bf16_f32 v189, 0, v189\n\t"
        "v_cvt_pk_bf16_f32 v190, 0, v190\n\t"
        "v_cvt_pk_bf16_f32 v191, 0, v191\n\t"
        "v_cvt_pk_bf16_f32 v192, 0, v192\n\t"
        "v_cvt_pk_bf16_f32 v193, 0, v193\n\t"
        "v_mul_f32_e32 v186, v184, v186\n\t"
        "v_mul_f32_e32 v187, v184, v187\n\t"
        "v_mul_f32_e32 v188, v184, v188\n\t"
        "v_mul_f32_e32 v189, v184, v189\n\t"
        "v_cvt_pk_bf16_f32 v186, 0, v186\n\t"
        "v_cvt_pk_bf16_f32 v187, 0, v187\n\t"
        "v_cvt_pk_bf16_f32 v188, 0, v188\n\t"
        "v_cvt_pk_bf16_f32 v189, 0, v189\n\t"
        "v_add_f32_e32 v186, v186, v190\n\t"
        "v_add_f32_e32 v187, v187, v191\n\t"
        "v_add_f32_e32 v188, v188, v192\n\t"
        "v_add_f32_e32 v189, v189, v193\n\t"
        "v_cvt_pk_bf16_f32 v200, v186, v187\n\t"
        "v_cvt_pk_bf16_f32 v201, v188, v189\n\t"
        ".Lo7%=:\n\t"
        "buffer_store_dwordx2 v[200:201], v245, %[ro], s83 offen\n\t"
        "s_add_i32 s80, s39, 1\n\t"
        "s_lshl_b32 s80, s80, 15\n\t"
        "s_or_b32 s81, s39, %[snd]\n\t"
        "s_cmp_lt_i32 s81, 0\n\t"
        "s_cselect_b32 s83, s36, s80\n\t"
        "s_cbranch_scc1 .Lo10%=\n\t"
        "s_mov_b32 s97, 0\n\t"
        ".Lo8%=:\n\t"
        "v_xor_b32_e32 v186, %[tag], v169\n\t"
        "v_xor_b32_e32 v187, %[tag], v171\n\t"
        "v_xor_b32_e32 v188, %[tag], v173\n\t"
        "v_or3_b32 v186, v186, v187, v188\n\t"
        "v_xor_b32_e32 v187, %[tag], v175\n\t"
        "v_or_b32_e32 v186, v186, v187\n\t"
        "v_cmp_ne_u32_e32 vcc, 0, v186\n\t"
        "v_sub_f32_e32 v198, 1.0, v185\n\t"
        "v_add_f32_e32 v190, v156, v168\n\t"
        "v_add_f32_e32 v191, v157, v170\n\t"
        "v_add_f32_e32 v192, v158, v172\n\t"
        "v_add_f32_e32 v193, v159, v174\n\t"
        "s_cbranch_vccz .Lo9%=\n\t"
        "s_sleep %c[xslp]\n\t"
        "s_lshl_b32 s82, s83, 2\n\t"
        "buffer_load_dwordx4 v[168:171], v246, %[rd], s82 offen offset:0 sc1\n\t"
        "buffer_load_dwordx4 v[172:175], v246, %[rd], s82 offen offset:1024 sc1\n\t"
        "s_add_u32 s97, s97, 1\n\t"
        "s_cmp_lt_u32 s97, 0x200000\n\t"
        "s_cbranch_scc0 .Lgu%=\n\t"
        "s_waitcnt vmcnt(0)\n\t"
        "s_branch .Lo8%=\n\t"
        ".Lo9%=:\n\t"
        "v_cvt_pk_bf16_f32 v198, 0, v198\n\t"
        "v_cvt_pk_bf16_f32 v190, 0, v190\n\t"
        "v_cvt_pk_bf16_f32 v191, 0, v191\n\t"
        "v_cvt_pk_bf16_f32 v192, 0, v192\n\t"
        "v_cvt_pk_bf16_f32 v193, 0, v193\n\t"
        "v_lshlrev_b32_e32 v186, 16, v182\n\t"
        "v_lshlrev_b32_e32 v194, 16, v178\n\t"
        "v_and_b32_e32 v187, 0xffff0000, v182\n\t"
        "v_and_b32_e32 v195, 0xffff0000, v178\n\t"
        "v_lshlrev_b32_e32 v188, 16, v183\n\t"
        "v_lshlrev_b32_e32 v196, 16, v179\n\t"
        "v_and_b32_e32 v189, 0xffff0000, v183\n\t"
        "v_and_b32_e32 v197, 0xffff0000, v179\n\t"
        "v_mul_f32_e32 v186, v186, v194\n\t"
        "v_mul_f32_e32 v187, v187, v195\n\t"
        "v_mul_f32_e32 v188, v188, v196\n\t"
        "v_mul_f32_e32 v189, v189, v197\n\t"
        "v_mul_f32_e32 v190, v198, v190\n\t"
        "v_mul_f32_e32 v191, v198, v191\n\t"
        "v_mul_f32_e32 v192, v198, v192\n\t"
        "v_mul_f32_e32 v193, v198, v193\n\t"
        "v_cvt_pk_bf16_f32 v186, 0, v186\n\t"
        "v_cvt_pk_bf16_f32 v187, 0, v187\n\t"
        "v_cvt_pk_bf16_f32 v188, 0, v188\n\t"
        "v_cvt_pk_bf16_f32 v189, 0, v189\n\t"
        "v_cvt_pk_bf16_f32 v190, 0, v190\n\t"
        "v_cvt_pk_bf16_f32 v191, 0, v191\n\t"
        "v_cvt_pk_bf16_f32 v192, 0, v192\n\t"
        "v_cvt_pk_bf16_f32 v193, 0, v193\n\t"
        "v_mul_f32_e32 v186, v185, v186\n\t"
        "v_mul_f32_e32 v187, v185, v187\n\t"
        "v_mul_f32_e32 v188, v185, v188\n\t"
        "v_mul_f32_e32 v189, v185, v189\n\t"
        "v_cvt_pk_bf16_f32 v186, 0, v186\n\t"
        "v_cvt_pk_bf16_f32 v187, 0, v187\n\t"
        "v_cvt_pk_bf16_f32 v188, 0, v188\n\t"
        "v_cvt_pk_bf16_f32 v189, 0, v189\n\t"
        "v_add_f32_e32 v186, v186, v190\n\t"
        "v_add_f32_e32 v187, v187, v191\n\t"
        "v_add_f32_e32 v188, v188, v192\n\t"
        "v_add_f32_e32 v189, v189, v193\n\t"
        "v_cvt_pk_bf16_f32 v200, v186, v187\n\t"
        "v_cvt_pk_bf16_f32 v201, v188, v189\n\t"
        ".Lo10%=:\n\t"
        "buffer_store_dwordx2 v[200:201], v245, %[ro], s83 offen\n\t"
        "s_mov_b32 s39, -1\n\t"
        ".Lo4%=:\n\t"
        "s_cmp_eq_u32 s37, 15\n\t"
        "s_cbranch_scc1 .Lsol%=\n\t"
        "s_mov_b32 s97, 0\n\t"
        ".Lo11%=:\n\t"
        "s_lshr_b32 s80, s40, s85\n\t"
        "s_andn2_b32 s80, 3, s80\n\t"
        "s_cbranch_scc0 .Lo12%=\n\t"
        "s_cmp_eq_u32 s97, 0\n\t"
        "s_cbranch_scc1 .Lo11%=x\n\t"
        "s_sleep %c[oslp]\n\t"
        ".Lo11%=x:\n\t"
        "buffer_load_dword v220, v242, %[rd], 0 offen sc1\n\t"
        "s_add_u32 s97, s97, 1\n\t"
        "s_cmp_lt_u32 s97, 0x200000\n\t"
        "s_cbranch_scc0 .Lgu%=\n\t"
        "s_waitcnt vmcnt(0)\n\t"
        "v_cmp_eq_u32_e32 vcc, 32, v220\n\t"
        "s_nop 1\n\t"
        "s_or_b32 s40, s40, vcc_lo\n\t"
        "s_branch .Lo11%=\n\t"
        ".Lo12%=:\n\t"
        ".Lgo%=:\n\t"
        "s_lshl_b32 s84, s44, 17\n\t"
        "buffer_load_dwordx4 v[0:3], v240, %[rd], s84 offen sc1\n\t"
        "buffer_load_dwordx4 v[4:7], v241, %[rd], s84 offen sc1\n\t"
        "s_add_u32 s84, s84, 8192\n\t"
        "buffer_load_dwordx4 v[8:11], v240, %[rd], s84 offen sc1\n\t"
        "buffer_load_dwordx4 v[12:15], v241, %[rd], s84 offen sc1\n\t"
        "s_add_u32 s84, s84, 8192\n\t"
        "buffer_load_dwordx4 v[16:19], v240, %[rd], s84 offen sc1\n\t"
        "buffer_load_dwordx4 v[20:23], v241, %[rd], s84 offen sc1\n\t"
        "s_add_u32 s84, s84, 8192\n\t"
        "buffer_load_dwordx4 v[24:27], v240, %[rd], s84 offen sc1\n\t"
        "buffer_load_dwordx4 v[28:31], v241, %[rd], s84 offen sc1\n\t"
        "s_add_u32 s84, s84, 8192\n\t"
        "buffer_load_dwordx4 v[32:35], v240, %[rd], s84 offen sc1\n\t"
        "buffer_load_dwordx4 v[36:39], v241, %[rd], s84 offen sc1\n\t"
        "s_add_u32 s84, s84, 8192\n\t"
        "buffer_load_dwordx4 v[40:43], v240, %[rd], s84 offen sc1\n\t"
        "buffer_load_dwordx4 v[44:47], v241, %[rd], s84 offen sc1\n\t"
        "s_add_u32 s84, s84, 8192\n\t"
        "buffer_load_dwordx4 v[48:51], v240, %[rd], s84 offen sc1\n\t"
        "buffer_load_dwordx4 v[52:55], v241, %[rd], s84 offen sc1\n\t"
        "s_add_u32 s84, s84, 8192\n\t"
        "buffer_load_dwordx4 v[56:59], v240, %[rd], s84 offen sc1\n\t"
        "buffer_load_dwordx4 v[60:63], v241, %[rd], s84 offen sc1\n\t"
        "s_add_u32 s84, s84, 73728\n\t"
        "buffer_load_dwordx4 v[64:67], v240, %[rd], s84 offen sc1\n\t"
        "buffer_load_dwordx4 v[68:71], v241, %[rd], s84 offen sc1\n\t"
        "s_add_u32 s84, s84, 8192\n\t"
        "buffer_load_dwordx4 v[72:75], v240, %[rd], s84 offen sc1\n\t"
        "buffer_load_dwordx4 v[76:79], v241, %[rd], s84 offen sc1\n\t"
        "s_add_u32 s84, s84, 8192\n\t"
        "buffer_load_dwordx4 v[80:83], v240, %[rd], s84 offen sc1\n\t"
        "buffer_load_dwordx4 v[84:87], v241, %[rd], s84 offen sc1\n\t"
        "s_add_u32 s84, s84, 8192\n\t"
        "buffer_load_dwordx4 v[88:91], v240, %[rd], s84 offen sc1\n\t"
        "buffer_load_dwordx4 v[92:95], v241, %[rd], s84 offen sc1\n\t"
        "s_add_u32 s84, s84, 8192\n\t"
        "buffer_load_dwordx4 v[96:99], v240, %[rd], s84 offen sc1\n\t"
        "buffer_load_dwordx4 v[100:103], v241, %[rd], s84 offen sc1\n\t"
        "s_add_u32 s84, s84, 8192\n\t"
        "buffer_load_dwordx4 v[104:107], v240, %[rd], s84 offen sc1\n\t"
        "buffer_load_dwordx4 v[108:111], v241, %[rd], s84 offen sc1\n\t"
        "s_add_u32 s84, s84, 8192\n\t"
        "buffer_load_dwordx4 v[112:115], v240, %[rd], s84 offen sc1\n\t"
        "buffer_load_dwordx4 v[116:119], v241, %[rd], s84 offen sc1\n\t"
        "s_add_u32 s84, s84, 8192\n\t"
        "buffer_load_dwordx4 v[120:123], v240, %[rd], s84 offen sc1\n\t"
        "buffer_load_dwordx4 v[124:127], v241, %[rd], s84 offen sc1\n\t"
        "s_add_u32 s84, s84, 8192\n\t"
        "buffer_store_dword v0, off, %[ro], s36\n\t"
        "buffer_store_dword v0, off, %[ro], s36\n\t"
        "buffer_store_dword v0, off, %[ro], s36\n\t"
        "buffer_store_dword v0, off, %[ro], s36\n\t"
        "buffer_store_dword v0, off, %[ro], s36\n\t"
        "buffer_store_dword v0, off, %[ro], s36\n\t"
        "buffer_store_dword v0, off, %[ro], s36\n\t"
        "buffer_store_dword v0, off, %[ro], s36\n\t"
        "buffer_store_dword v0, off, %[ro], s36\n\t"
        "buffer_store_dword v0, off, %[ro], s36\n\t"
        "buffer_store_dword v0, off, %[ro], s36\n\t"
        "buffer_store_dword v0, off, %[ro], s36\n\t"
        "buffer_store_dword v0, off, %[ro], s36\n\t"
        "buffer_store_dword v0, off, %[ro], s36\n\t"
        "buffer_store_dword v0, off, %[ro], s36\n\t"
        "buffer_store_dword v0, off, %[ro], s36\n\t"
        "buffer_store_dword v0, off, %[ro], s36\n\t"
        ".Lrnd%=:\n\t"
        "s_lshl_b32 s85, s37, 1\n\t"
        "s_and_b32 s86, s40, 0x3fffffff\n\t"
        "s_lshr_b32 s86, s86, s85\n\t"
        "s_andn2_b32 s80, 12, s86\n\t"
        "s_cselect_b32 s41, 0, 1\n\t"
        "s_add_u32 s84, s44, 8\n\t"
        "s_lshl_b32 s84, s84, 17\n\t"
        "s_cmp_eq_u32 s41, 0\n\t"
        "s_cselect_b32 s84, s36, s84\n\t"
        "s_cselect_b32 s87, 0, s36\n\t"
        "s_andn2_b32 s80, 48, s86\n\t"
        "s_cselect_b32 s80, 0, s36\n\t"
        "v_add_u32_e32 v222, s43, v247\n\t"
        "s_xor_b32 s81, s43, 0x8000\n\t"
        "v_add_u32_e32 v223, s81, v248\n\t"
        "buffer_load_dword v220, v242, %[rd], s80 offen sc1\n\t"
        "s_waitcnt vmcnt(49)\n\t"
        "v_mfma_f32_16x16x32_bf16 v[128:131], a[128:131], v[0:3], 0\n\t"
        "ds_read_b128 v[186:189], v223 offset:0\n\t"
        "ds_read_b128 v[190:193], v223 offset:8192\n\t"
        "v_mfma_f32_16x16x32_bf16 v[132:135], a[132:135], v[0:3], 0\n\t"
        "ds_read_b128 v[194:197], v223 offset:16384\n\t"
        "ds_read_b128 v[198:201], v223 offset:24576\n\t"
        "v_mfma_f32_16x16x32_bf16 v[136:139], a[136:139], v[0:3], 0\n\t"
        "s_nop 0\n\t"
        "s_nop 0\n\t"
        "v_mfma_f32_16x16x32_bf16 v[140:143], a[140:143], v[0:3], 0\n\t"
        "s_nop 0\n\t"
        "s_nop 0\n\t"
        "buffer_load_dwordx4 v[0:3], v240, %[rd], s84 offen sc1\n\t"
        "s_waitcnt vmcnt(49)\n\t"
        "v_mfma_f32_16x16x32_bf16 v[128:131], a[144:147], v[4:7], v[128:131]\n\t"
        "s_nop 0\n\t"
        "s_nop 0\n\t"
        "v_mfma_f32_16x16x32_bf16 v[132:135], a[148:151], v[4:7], v[132:135]\n\t"
        "s_waitcnt lgkmcnt(0)\n\t"
        "v_add_f32_e32 v186, v186, v190\n\t"
        "v_mfma_f32_16x16x32_bf16 v[136:139], a[152:155], v[4:7], v[136:139]\n\t"
        "v_add_f32_e32 v187, v187, v191\n\t"
        "v_add_f32_e32 v188, v188, v192\n\t"
        "v_mfma_f32_16x16x32_bf16 v[140:143], a[156:159], v[4:7], v[140:143]\n\t"
        "v_add_f32_e32 v189, v189, v193\n\t"
        "v_add_f32_e32 v194, v194, v198\n\t"
        "buffer_load_dwordx4 v[4:7], v241, %[rd], s84 offen sc1\n\t"
        "s_add_u32 s84, s84, 8192\n\t"
        "s_waitcnt vmcnt(49)\n\t"
        "v_mfma_f32_16x16x32_bf16 v[128:131], a[160:163], v[8:11], v[128:131]\n\t"
        "v_add_f32_e32 v195, v195, v199\n\t"
        "v_add_f32_e32 v196, v196, v200\n\t"
        "v_mfma_f32_16x16x32_bf16 v[132:135], a[164:167], v[8:11], v[132:135]\n\t"
        "v_add_f32_e32 v197, v197, v201\n\t"
        "v_add_f32_e32 v204, v186, v194\n\t"
        "v_mfma_f32_16x16x32_bf16 v[136:139], a[168:171], v[8:11], v[136:139]\n\t"
        "v_add_f32_e32 v206, v187, v195\n\t"
        "v_add_f32_e32 v208, v188, v196\n\t"
        "v_mfma_f32_16x16x32_bf16 v[140:143], a[172:175], v[8:11], v[140:143]\n\t"
        "v_add_f32_e32 v210, v189, v197\n\t"
        "s_add_i32 s80, s38, 0\n\t"
        "buffer_load_dwordx4 v[8:11], v240, %[rd], s84 offen sc1\n\t"
        "s_waitcnt vmcnt(47)\n\t"
        "v_mfma_f32_16x16x32_bf16 v[128:131], a[176:179], v[12:15], v[128:131]\n\t"
        "s_lshl_b32 s80, s80, 17\n\t"
        "s_or_b32 s81, s38, %[own]\n\t"
        "v_mfma_f32_16x16x32_bf16 v[132:135], a[180:183], v[12:15], v[132:135]\n\t"
        "s_cmp_lt_i32 s81, 0\n\t"
        "s_cselect_b32 s82, s36, s80\n\t"
        "buffer_store_dwordx4 v[204:207], v246, %[rd], s82 offen offset:0 sc1\n\t"
        "v_mfma_f32_16x16x32_bf16 v[136:139], a[184:187], v[12:15], v[136:139]\n\t"
        "buffer_store_dwordx4 v[208:211], v246, %[rd], s82 offen offset:1024 sc1\n\t"
        "v_mov_b32_e32 v144, v204\n\t"
        "v_mfma_f32_16x16x32_bf16 v[140:143], a[188:191], v[12:15], v[140:143]\n\t"
        "v_mov_b32_e32 v145, v206\n\t"
        "v_mov_b32_e32 v146, v208\n\t"
        "buffer_load_dwordx4 v[12:15], v241, %[rd], s84 offen sc1\n\t"
        "s_add_u32 s84, s84, 8192\n\t"
        "s_waitcnt vmcnt(49)\n\t"
        "v_mfma_f32_16x16x32_bf16 v[128:131], a[192:195], v[16:19], v[128:131]\n\t"
        "v_mov_b32_e32 v147, v210\n\t"
        "ds_read_b128 v[186:189], v223 offset:4096\n\t"
        "v_mfma_f32_16x16x32_bf16 v[132:135], a[196:199], v[16:19], v[132:135]\n\t"
        "ds_read_b128 v[190:193], v223 offset:12288\n\t"
        "ds_read_b128 v[194:197], v223 offset:20480\n\t"
        "v_mfma_f32_16x16x32_bf16 v[136:139], a[200:203], v[16:19], v[136:139]\n\t"
        "ds_read_b128 v[198:201], v223 offset:28672\n\t"
        "s_nop 0\n\t"
        "v_mfma_f32_16x16x32_bf16 v[140:143], a[204:207], v[16:19], v[140:143]\n\t"
        "s_nop 0\n\t"
        "s_nop 0\n\t"
        "buffer_load_dwordx4 v[16:19], v240, %[rd], s84 offen sc1\n\t"
        "s_waitcnt vmcnt(49)\n\t"
        "v_mfma_f32_16x16x32_bf16 v[128:131], a[208:211], v[20:23], v[128:131]\n\t"
        "s_nop 0\n\t"
        "s_nop 0\n\t"
        "v_mfma_f32_16x16x32_bf16 v[132:135], a[212:215], v[20:23], v[132:135]\n\t"
        "s_nop 0\n\t"
        "s_waitcnt lgkmcnt(0)\n\t"
        "v_mfma_f32_16x16x32_bf16 v[136:139], a[216:219], v[20:23], v[136:139]\n\t"
        "v_add_f32_e32 v186, v186, v190\n\t"
        "v_add_f32_e32 v187, v187, v191\n\t"
        "v_mfma_f32_16x16x32_bf16 v[140:143], a[220:223], v[20:23], v[140:143]\n\t"
        "v_add_f32_e32 v188, v188, v192\n\t"
        "v_add_f32_e32 v189, v189, v193\n\t"
        "buffer_load_dwordx4 v[20:23], v241, %[rd], s84 offen sc1\n\t"
        "s_add_u32 s84, s84, 8192\n\t"
        "s_waitcnt vmcnt(49)\n\t"
        "v_mfma_f32_16x16x32_bf16 v[128:131], a[224:227], v[24:27], v[128:131]\n\t"
        "v_add_f32_e32 v194, v194, v198\n\t"
        "v_add_f32_e32 v195, v195, v199\n\t"
        "v_mfma_f32_16x16x32_bf16 v[132:135], a[228:231], v[24:27], v[132:135]\n\t"
        "v_add_f32_e32 v196, v196, v200\n\t"
        "v_add_f32_e32 v197, v197, v201\n\t"
        "v_mfma_f32_16x16x32_bf16 v[136:139], a[232:235], v[24:27], v[136:139]\n\t"
        "v_add_f32_e32 v212, v186, v194\n\t"
        "v_add_f32_e32 v214, v187, v195\n\t"
        "v_mfma_f32_16x16x32_bf16 v[140:143], a[236:239], v[24:27], v[140:143]\n\t"
        "v_add_f32_e32 v216, v188, v196\n\t"
        "v_add_f32_e32 v218, v189, v197\n\t"
        "buffer_load_dwordx4 v[24:27], v240, %[rd], s84 offen sc1\n\t"
        "s_waitcnt vmcnt(47)\n\t"
        "v_mfma_f32_16x16x32_bf16 v[128:131], a[240:243], v[28:31], v[128:131]\n\t"
        "s_add_i32 s80, s38, 1\n\t"
        "s_lshl_b32 s80, s80, 17\n\t"
        "v_mfma_f32_16x16x32_bf16 v[132:135], a[244:247], v[28:31], v[132:135]\n\t"
        "s_or_b32 s81, s38, %[own]\n\t"
        "s_cmp_lt_i32 s81, 0\n\t"
        "s_cselect_b32 s82, s36, s80\n\t"
        "v_mfma_f32_16x16x32_bf16 v[136:139], a[248:251], v[28:31], v[136:139]\n\t"
        "buffer_store_dwordx4 v[212:215], v246, %[rd], s82 offen offset:0 sc1\n\t"
        "buffer_store_dwordx4 v[216:219], v246, %[rd], s82 offen offset:1024 sc1\n\t"
        "v_mfma_f32_16x16x32_bf16 v[140:143], a[252:255], v[28:31], v[140:143]\n\t"
        "v_mov_b32_e32 v148, v212\n\t"
        "v_mov_b32_e32 v149, v214\n\t"
        "buffer_load_dwordx4 v[28:31], v241, %[rd], s84 offen sc1\n\t"
        "s_add_u32 s84, s84, 8192\n\t"
        "s_waitcnt vmcnt(49)\n\t"
        "v_mfma_f32_16x16x32_bf16 v[128:131], a[0:3], v[32:35], v[128:131]\n\t"
        "v_mov_b32_e32 v150, v216\n\t"
        "v_mov_b32_e32 v151, v218\n\t"
        "v_mfma_f32_16x16x32_bf16 v[132:135], a[4:7], v[32:35], v[132:135]\n\t"
        "s_waitcnt vmcnt(34)\n\t"
        "s_add_i32 s80, s39, 0\n\t"
        "v_mfma_f32_16x16x32_bf16 v[136:139], a[8:11], v[32:35], v[136:139]\n\t"
        "s_lshl_b32 s80, s80, 15\n\t"
        "s_or_b32 s81, s39, %[snd]\n\t"
        "v_mfma_f32_16x16x32_bf16 v[140:143], a[12:15], v[32:35], v[140:143]\n\t"
        "s_cmp_lt_i32 s81, 0\n\t"
        "s_cselect_b32 s83, s36, s80\n\t"
        "s_cselect_b64 s[88:89], 0, -1\n\t"
        "v_xor_b32_e32 v224, %[tag], v161\n\t"
        "buffer_load_dwordx4 v[32:35], v240, %[rd], s84 offen sc1\n\t"
        "s_waitcnt vmcnt(49)\n\t"
        "v_mfma_f32_16x16x32_bf16 v[128:131], a[16:19], v[36:39], v[128:131]\n\t"
        "v_xor_b32_e32 v225, %[tag], v163\n\t"
        "v_xor_b32_e32 v226, %[tag], v165\n\t"
        "v_mfma_f32_16x16x32_bf16 v[132:135], a[20:23], v[36:39], v[132:135]\n\t"
        "v_or3_b32 v224, v224, v225, v226\n\t"
        "v_xor_b32_e32 v225, %[tag], v167\n\t"
        "v_mfma_f32_16x16x32_bf16 v[136:139], a[24:27], v[36:39], v[136:139]\n\t"
        "v_or_b32_e32 v224, v224, v225\n\t"
        "v_cmp_ne_u32_e32 vcc, 0, v224\n\t"
        "v_mfma_f32_16x16x32_bf16 v[140:143], a[28:31], v[36:39], v[140:143]\n\t"
        "v_sub_f32_e32 v236, 1.0, v184\n\t"
        "v_cvt_pk_bf16_f32 v236, 0, v236\n\t"
        "s_and_b64 vcc, vcc, s[88:89]\n\t"
        "s_cbranch_scc1 .Lo13%=\n\t"
        ".Lo14%=:\n\t"
        "buffer_load_dwordx4 v[36:39], v241, %[rd], s84 offen sc1\n\t"
        "s_add_u32 s84, s84, 8192\n\t"
        "s_waitcnt vmcnt(49)\n\t"
        "v_mfma_f32_16x16x32_bf16 v[128:131], a[32:35], v[40:43], v[128:131]\n\t"
        "v_add_f32_e32 v228, v152, v160\n\t"
        "v_add_f32_e32 v229, v153, v162\n\t"
        "v_mfma_f32_16x16x32_bf16 v[132:135], a[36:39], v[40:43], v[132:135]\n\t"
        "v_add_f32_e32 v230, v154, v164\n\t"
        "v_add_f32_e32 v231, v155, v166\n\t"
        "v_mfma_f32_16x16x32_bf16 v[136:139], a[40:43], v[40:43], v[136:139]\n\t"
        "v_cvt_pk_bf16_f32 v228, 0, v228\n\t"
        "v_cvt_pk_bf16_f32 v229, 0, v229\n\t"
        "v_mfma_f32_16x16x32_bf16 v[140:143], a[44:47], v[40:43], v[140:143]\n\t"
        "v_cvt_pk_bf16_f32 v230, 0, v230\n\t"
        "v_cvt_pk_bf16_f32 v231, 0, v231\n\t"
        "buffer_load_dwordx4 v[40:43], v240, %[rd], s84 offen sc1\n\t"
        "s_waitcnt vmcnt(49)\n\t"
        "v_mfma_f32_16x16x32_bf16 v[128:131], a[48:51], v[44:47], v[128:131]\n\t"
        "v_lshlrev_b32_e32 v224, 16, v180\n\t"
        "v_lshlrev_b32_e32 v232, 16, v176\n\t"
        "v_mfma_f32_16x16x32_bf16 v[132:135], a[52:55], v[44:47], v[132:135]\n\t"
        "v_and_b32_e32 v225, 0xffff0000, v180\n\t"
        "v_and_b32_e32 v233, 0xffff0000, v176\n\t"
        "v_mfma_f32_16x16x32_bf16 v[136:139], a[56:59], v[44:47], v[136:139]\n\t"
        "v_lshlrev_b32_e32 v226, 16, v181\n\t"
        "v_lshlrev_b32_e32 v234, 16, v177\n\t"
        "v_mfma_f32_16x16x32_bf16 v[140:143], a[60:63], v[44:47], v[140:143]\n\t"
        "v_and_b32_e32 v227, 0xffff0000, v181\n\t"
        "v_and_b32_e32 v235, 0xffff0000, v177\n\t"
        "buffer_load_dwordx4 v[44:47], v241, %[rd], s84 offen sc1\n\t"
        "s_add_u32 s84, s84, 8192\n\t"
        "s_waitcnt vmcnt(49)\n\t"
        "v_mfma_f32_16x16x32_bf16 v[128:131], a[64:67], v[48:51], v[128:131]\n\t"
        "v_mul_f32_e32 v224, v224, v232\n\t"
        "v_mul_f32_e32 v225, v225, v233\n\t"
        "v_mfma_f32_16x16x32_bf16 v[132:135], a[68:71], v[48:51], v[132:135]\n\t"
        "v_mul_f32_e32 v226, v226, v234\n\t"
        "v_mul_f32_e32 v227, v227, v235\n\t"
        "v_mfma_f32_16x16x32_bf16 v[136:139], a[72:75], v[48:51], v[136:139]\n\t"
        "v_mul_f32_e32 v228, v236, v228\n\t"
        "v_mul_f32_e32 v229, v236, v229\n\t"
        "v_mfma_f32_16x16x32_bf16 v[140:143], a[76:79], v[48:51], v[140:143]\n\t"
        "v_mul_f32_e32 v230, v236, v230\n\t"
        "v_mul_f32_e32 v231, v236, v231\n\t"
        "buffer_load_dwordx4 v[48:51], v240, %[rd], s84 offen sc1\n\t"
        "s_waitcnt vmcnt(49)\n\t"
        "v_mfma_f32_16x16x32_bf16 v[128:131], a[80:83], v[52:55], v[128:131]\n\t"
        "v_cvt_pk_bf16_f32 v224, 0, v224\n\t"
        "v_cvt_pk_bf16_f32 v225, 0, v225\n\t"
        "v_mfma_f32_16x16x32_bf16 v[132:135], a[84:87], v[52:55], v[132:135]\n\t"
        "v_cvt_pk_bf16_f32 v226, 0, v226\n\t"
        "v_cvt_pk_bf16_f32 v227, 0, v227\n\t"
        "v_mfma_f32_16x16x32_bf16 v[136:139], a[88:91], v[52:55], v[136:139]\n\t"
        "v_cvt_pk_bf16_f32 v228, 0, v228\n\t"
        "v_cvt_pk_bf16_f32 v229, 0, v229\n\t"
        "v_mfma_f32_16x16x32_bf16 v[140:143], a[92:95], v[52:55], v[140:143]\n\t"
        "v_cvt_pk_bf16_f32 v230, 0, v230\n\t"
        "v_cvt_pk_bf16_f32 v231, 0, v231\n\t"
        "buffer_load_dwordx4 v[52:55], v241, %[rd], s84 offen sc1\n\t"
        "s_add_u32 s84, s84, 8192\n\t"
        "s_waitcnt vmcnt(49)\n\t"
        "v_mfma_f32_16x16x32_bf16 v[128:131], a[96:99], v[56:59], v[128:131]\n\t"
        "v_mul_f32_e32 v224, v184, v224\n\t"
        "v_mul_f32_e32 v225, v184, v225\n\t"
        "v_mfma_f32_16x16x32_bf16 v[132:135], a[100:103], v[56:59], v[132:135]\n\t"
        "v_mul_f32_e32 v226, v184, v226\n\t"
        "v_mul_f32_e32 v227, v184, v227\n\t"
        "v_mfma_f32_16x16x32_bf16 v[136:139], a[104:107], v[56:59], v[136:139]\n\t"
        "v_cvt_pk_bf16_f32 v224, 0, v224\n\t"
        "v_cvt_pk_bf16_f32 v225, 0, v225\n\t"
        "v_mfma_f32_16x16x32_bf16 v[140:143], a[108:111], v[56:59], v[140:143]\n\t"
        "v_cvt_pk_bf16_f32 v226, 0, v226\n\t"
        "v_cvt_pk_bf16_f32 v227, 0, v227\n\t"
        "buffer_load_dwordx4 v[56:59], v240, %[rd], s84 offen sc1\n\t"
        "s_waitcnt vmcnt(48)\n\t"
        "v_mfma_f32_16x16x32_bf16 v[128:131], a[112:115], v[60:63], v[128:131]\n\t"
        "v_add_f32_e32 v224, v224, v228\n\t"
        "v_add_f32_e32 v225, v225, v229\n\t"
        "v_mfma_f32_16x16x32_bf16 v[132:135], a[116:119], v[60:63], v[132:135]\n\t"
        "v_add_f32_e32 v226, v226, v230\n\t"
        "v_add_f32_e32 v227, v227, v231\n\t"
        "v_mfma_f32_16x16x32_bf16 v[136:139], a[120:123], v[60:63], v[136:139]\n\t"
        "v_cvt_pk_bf16_f32 v238, v224, v225\n\t"
        "v_cvt_pk_bf16_f32 v239, v226, v227\n\t"
        "v_mfma_f32_16x16x32_bf16 v[140:143], a[124:127], v[60:63], v[140:143]\n\t"
        "buffer_store_dwordx2 v[238:239], v245, %[ro], s83 offen\n\t"
        "s_add_i32 s80, s38, 0\n\t"
        "buffer_load_dwordx4 v[60:63], v241, %[rd], s84 offen sc1\n\t"
        "s_add_u32 s84, s84, 73728\n\t"
        "ds_write_b128 v222, v[128:131] offset:0\n\t"
        "ds_write_b128 v222, v[132:135] offset:1024\n\t"
        "ds_write_b128 v222, v[136:139] offset:2048\n\t"
        "s_nop 0\n\t"
        "ds_write_b128 v222, v[140:143] offset:3072\n\t"
        "s_waitcnt vmcnt(47)\n\t"
        "v_mfma_f32_16x16x32_bf16 v[128:131], a[128:131], v[64:67], 0\n\t"
        "s_lshl_b32 s80, s80, 6\n\t"
        "s_or_b32 s81, s38, %[snd]\n\t"
        "v_mfma_f32_16x16x32_bf16 v[132:135], a[132:135], v[64:67], 0\n\t"
        "s_cmp_lt_i32 s81, 0\n\t"
        "s_cselect_b32 s82, s36, s80\n\t"
        "buffer_load_dword v184, v244, %[rd], s82 offen sc1\n\t"
        "v_mfma_f32_16x16x32_bf16 v[136:139], a[136:139], v[64:67], 0\n\t"
        "s_lshl_b32 s80, s80, 1\n\t"
        "s_cmp_lt_i32 s81, 0\n\t"
        "s_cselect_b32 s82, s36, s80\n\t"
        "v_mfma_f32_16x16x32_bf16 v[140:143], a[140:143], v[64:67], 0\n\t"
        "buffer_load_dwordx2 v[180:181], v243, %[rd], s82 offen sc1\n\t"
        "s_lshl_b32 s80, s80, 8\n\t"
        "buffer_load_dwordx4 v[64:67], v240, %[rd], s84 offen sc1\n\t"
        "s_waitcnt vmcnt(46)\n\t"
        "v_mfma_f32_16x16x32_bf16 v[128:131], a[144:147], v[68:71], v[128:131]\n\t"
        "s_cmp_lt_i32 s81, 0\n\t"
        "s_cselect_b32 s82, s36, s80\n\t"
        "buffer_load_dwordx2 v[176:177], v245, %[rx], s82 offen\n\t"
        "v_mfma_f32_16x16x32_bf16 v[132:135], a[148:151], v[68:71], v[132:135]\n\t"
        "s_add_i32 s80, s38, 0\n\t"
        "s_lshl_b32 s80, s80, 17\n\t"
        "v_mfma_f32_16x16x32_bf16 v[136:139], a[152:155], v[68:71], v[136:139]\n\t"
        "s_or_b32 s81, s38, %[snd]\n\t"
        "s_cmp_lt_i32 s81, 0\n\t"
        "s_cselect_b32 s82, s36, s80\n\t"
        "v_mfma_f32_16x16x32_bf16 v[140:143], a[156:159], v[68:71], v[140:143]\n\t"
        "buffer_load_dwordx4 v[160:163], v246, %[rd], s82 offen offset:0 sc1\n\t"
        "buffer_load_dwordx4 v[164:167], v246, %[rd], s82 offen offset:1024 sc1\n\t"
        "buffer_load_dwordx4 v[68:71], v241, %[rd], s84 offen sc1\n\t"
        "s_add_u32 s84, s84, 8192\n\t"
        "s_waitcnt vmcnt(49)\n\t"
        "v_mfma_f32_16x16x32_bf16 v[128:131], a[160:163], v[72:75], v[128:131]\n\t"
        "s_waitcnt vmcnt(34)\n\t"
        "v_mfma_f32_16x16x32_bf16 v[132:135], a[164:167], v[72:75], v[132:135]\n\t"
        "s_add_i32 s80, s39, 1\n\t"
        "s_lshl_b32 s80, s80, 15\n\t"
        "v_mfma_f32_16x16x32_bf16 v[136:139], a[168:171], v[72:75], v[136:139]\n\t"
        "s_or_b32 s81, s39, %[snd]\n\t"
        "s_cmp_lt_i32 s81, 0\n\t"
        "s_cselect_b32 s83, s36, s80\n\t"
        "s_cselect_b64 s[88:89], 0, -1\n\t"
        "v_mfma_f32_16x16x32_bf16 v[140:143], a[172:175], v[72:75], v[140:143]\n\t"
        "v_xor_b32_e32 v224, %[tag], v169\n\t"
        "v_xor_b32_e32 v225, %[tag], v171\n\t"
        "buffer_load_dwordx4 v[72:75], v240, %[rd], s84 offen sc1\n\t"
        "s_waitcnt vmcnt(49)\n\t"
        "v_mfma_f32_16x16x32_bf16 v[128:131], a[176:179], v[76:79], v[128:131]\n\t"
        "v_xor_b32_e32 v226, %[tag], v173\n\t"
        "v_or3_b32 v224, v224, v225, v226\n\t"
        "v_mfma_f32_16x16x32_bf16 v[132:135], a[180:183], v[76:79], v[132:135]\n\t"
        "v_xor_b32_e32 v225, %[tag], v175\n\t"
        "v_or_b32_e32 v224, v224, v225\n\t"
        "v_mfma_f32_16x16x32_bf16 v[136:139], a[184:187], v[76:79], v[136:139]\n\t"
        "v_cmp_ne_u32_e32 vcc, 0, v224\n\t"
        "v_sub_f32_e32 v236, 1.0, v185\n\t"
        "v_mfma_f32_16x16x32_bf16 v[140:143], a[188:191], v[76:79], v[140:143]\n\t"
        "v_cvt_pk_bf16_f32 v236, 0, v236\n\t"
        "s_and_b64 vcc, vcc, s[88:89]\n\t"
        "s_cbranch_scc1 .Lo15%=\n\t"
        ".Lo16%=:\n\t"
        "v_add_f32_e32 v228, v156, v168\n\t"
        "buffer_load_dwordx4 v[76:79], v241, %[rd], s84 offen sc1\n\t"
        "s_add_u32 s84, s84, 8192\n\t"
        "s_waitcnt vmcnt(49)\n\t"
        "v_mfma_f32_16x16x32_bf16 v[128:131], a[192:195], v[80:83], v[128:131]\n\t"
        "v_add_f32_e32 v229, v157, v170\n\t"
        "v_add_f32_e32 v230, v158, v172\n\t"
        "v_mfma_f32_16x16x32_bf16 v[132:135], a[196:199], v[80:83], v[132:135]\n\t"
        "v_add_f32_e32 v231, v159, v174\n\t"
        "v_cvt_pk_bf16_f32 v228, 0, v228\n\t"
        "v_mfma_f32_16x16x32_bf16 v[136:139], a[200:203], v[80:83], v[136:139]\n\t"
        "v_cvt_pk_bf16_f32 v229, 0, v229\n\t"
        "v_cvt_pk_bf16_f32 v230, 0, v230\n\t"
        "v_mfma_f32_16x16x32_bf16 v[140:143], a[204:207], v[80:83], v[140:143]\n\t"
        "v_cvt_pk_bf16_f32 v231, 0, v231\n\t"
        "v_lshlrev_b32_e32 v224, 16, v182\n\t"
        "buffer_load_dwordx4 v[80:83], v240, %[rd], s84 offen sc1\n\t"
        "s_waitcnt vmcnt(49)\n\t"
        "v_mfma_f32_16x16x32_bf16 v[128:131], a[208:211], v[84:87], v[128:131]\n\t"
        "v_lshlrev_b32_e32 v232, 16, v178\n\t"
        "v_and_b32_e32 v225, 0xffff0000, v182\n\t"
        "v_mfma_f32_16x16x32_bf16 v[132:135], a[212:215], v[84:87], v[132:135]\n\t"
        "v_and_b32_e32 v233, 0xffff0000, v178\n\t"
        "v_lshlrev_b32_e32 v226, 16, v183\n\t"
        "v_mfma_f32_16x16x32_bf16 v[136:139], a[216:219], v[84:87], v[136:139]\n\t"
        "v_lshlrev_b32_e32 v234, 16, v179\n\t"
        "v_and_b32_e32 v227, 0xffff0000, v183\n\t"
        "v_mfma_f32_16x16x32_bf16 v[140:143], a[220:223], v[84:87], v[140:143]\n\t"
        "v_and_b32_e32 v235, 0xffff0000, v179\n\t"
        "v_mul_f32_e32 v224, v224, v232\n\t"
        "buffer_load_dwordx4 v[84:87], v241, %[rd], s84 offen sc1\n\t"
        "s_add_u32 s84, s84, 8192\n\t"
        "s_waitcnt vmcnt(49)\n\t"
        "v_mfma_f32_16x16x32_bf16 v[128:131], a[224:227], v[88:91], v[128:131]\n\t"
        "v_mul_f32_e32 v225, v225, v233\n\t"
        "v_mul_f32_e32 v226, v226, v234\n\t"
        "v_mfma_f32_16x16x32_bf16 v[132:135], a[228:231], v[88:91], v[132:135]\n\t"
        "v_mul_f32_e32 v227, v227, v235\n\t"
        "v_mul_f32_e32 v228, v236, v228\n\t"
        "v_mfma_f32_16x16x32_bf16 v[136:139], a[232:235], v[88:91], v[136:139]\n\t"
        "v_mul_f32_e32 v229, v236, v229\n\t"
        "v_mul_f32_e32 v230, v236, v230\n\t"
        "v_mfma_f32_16x16x32_bf16 v[140:143], a[236:239], v[88:91], v[140:143]\n\t"
        "v_mul_f32_e32 v231, v236, v231\n\t"
        "v_cvt_pk_bf16_f32 v224, 0, v224\n\t"
        "buffer_load_dwordx4 v[88:91], v240, %[rd], s84 offen sc1\n\t"
        "s_waitcnt vmcnt(49)\n\t"
        "v_mfma_f32_16x16x32_bf16 v[128:131], a[240:243], v[92:95], v[128:131]\n\t"
        "v_cvt_pk_bf16_f32 v225, 0, v225\n\t"
        "v_cvt_pk_bf16_f32 v226, 0, v226\n\t"
        "v_mfma_f32_16x16x32_bf16 v[132:135], a[244:247], v[92:95], v[132:135]\n\t"
        "v_cvt_pk_bf16_f32 v227, 0, v227\n\t"
        "v_cvt_pk_bf16_f32 v228, 0, v228\n\t"
        "v_mfma_f32_16x16x32_bf16 v[136:139], a[248:251], v[92:95], v[136:139]\n\t"
        "v_cvt_pk_bf16_f32 v229, 0, v229\n\t"
        "v_cvt_pk_bf16_f32 v230, 0, v230\n\t"
        "v_mfma_f32_16x16x32_bf16 v[140:143], a[252:255], v[92:95], v[140:143]\n\t"
        "v_cvt_pk_bf16_f32 v231, 0, v231\n\t"
        "v_mul_f32_e32 v224, v185, v224\n\t"
        "buffer_load_dwordx4 v[92:95], v241, %[rd], s84 offen sc1\n\t"
        "s_add_u32 s84, s84, 8192\n\t"
        "buffer_load_dword v221, v242, %[rd], s87 offen sc1\n\t"
        "s_waitcnt vmcnt(49)\n\t"
        "v_mfma_f32_16x16x32_bf16 v[128:131], a[0:3], v[96:99], v[128:131]\n\t"
        "v_mul_f32_e32 v225, v185, v225\n\t"
        "v_mul_f32_e32 v226, v185, v226\n\t"
        "v_mfma_f32_16x16x32_bf16 v[132:135], a[4:7], v[96:99], v[132:135]\n\t"
        "v_mul_f32_e32 v227, v185, v227\n\t"
        "v_cvt_pk_bf16_f32 v224, 0, v224\n\t"
        "v_mfma_f32_16x16x32_bf16 v[136:139], a[8:11], v[96:99], v[136:139]\n\t"
        "v_cvt_pk_bf16_f32 v225, 0, v225\n\t"
        "v_cvt_pk_bf16_f32 v226, 0, v226\n\t"
        "v_mfma_f32_16x16x32_bf16 v[140:143], a[12:15], v[96:99], v[140:143]\n\t"
        "v_cvt_pk_bf16_f32 v227, 0, v227\n\t"
        "v_add_f32_e32 v224, v224, v228\n\t"
        "buffer_load_dwordx4 v[96:99], v240, %[rd], s84 offen sc1\n\t"
        "s_waitcnt vmcnt(48)\n\t"
        "v_mfma_f32_16x16x32_bf16 v[128:131], a[16:19], v[100:103], v[128:131]\n\t"
        "v_add_f32_e32 v225, v225, v229\n\t"
        "v_add_f32_e32 v226, v226, v230\n\t"
        "v_mfma_f32_16x16x32_bf16 v[132:135], a[20:23], v[100:103], v[132:135]\n\t"
        "v_add_f32_e32 v227, v227, v231\n\t"
        "v_cvt_pk_bf16_f32 v238, v224, v225\n\t"
        "v_mfma_f32_16x16x32_bf16 v[136:139], a[24:27], v[100:103], v[136:139]\n\t"
        "v_cvt_pk_bf16_f32 v239, v226, v227\n\t"
        "buffer_store_dwordx2 v[238:239], v245, %[ro], s83 offen\n\t"
        "v_mfma_f32_16x16x32_bf16 v[140:143], a[28:31], v[100:103], v[140:143]\n\t"
        "s_add_i32 s80, s38, 1\n\t"
        "s_lshl_b32 s80, s80, 6\n\t"
        "buffer_load_dwordx4 v[100:103], v241, %[rd], s84 offen sc1\n\t"
        "s_add_u32 s84, s84, 8192\n\t"
        "s_waitcnt vmcnt(47)\n\t"
        "v_mfma_f32_16x16x32_bf16 v[128:131], a[32:35], v[104:107], v[128:131]\n\t"
        "s_or_b32 s81, s38, %[snd]\n\t"
        "s_cmp_lt_i32 s81, 0\n\t"
        "s_cselect_b32 s82, s36, s80\n\t"
        "v_mfma_f32_16x16x32_bf16 v[132:135], a[36:39], v[104:107], v[132:135]\n\t"
        "buffer_load_dword v185, v244, %[rd], s82 offen sc1\n\t"
        "s_lshl_b32 s80, s80, 1\n\t"
        "v_mfma_f32_16x16x32_bf16 v[136:139], a[40:43], v[104:107], v[136:139]\n\t"
        "s_cmp_lt_i32 s81, 0\n\t"
        "s_cselect_b32 s82, s36, s80\n\t"
        "buffer_load_dwordx2 v[182:183], v243, %[rd], s82 offen sc1\n\t"
        "v_mfma_f32_16x16x32_bf16 v[140:143], a[44:47], v[104:107], v[140:143]\n\t"
        "s_lshl_b32 s80, s80, 8\n\t"
        "s_cmp_lt_i32 s81, 0\n\t"
        "s_cselect_b32 s82, s36, s80\n\t"
        "buffer_load_dwordx4 v[104:107], v240, %[rd], s84 offen sc1\n\t"
        "s_waitcnt vmcnt(46)\n\t"
        "v_mfma_f32_16x16x32_bf16 v[128:131], a[48:51], v[108:111], v[128:131]\n\t"
        "buffer_load_dwordx2 v[178:179], v245, %[rx], s82 offen\n\t"
        "s_add_i32 s80, s38, 1\n\t"
        "v_mfma_f32_16x16x32_bf16 v[132:135], a[52:55], v[108:111], v[132:135]\n\t"
        "s_lshl_b32 s80, s80, 17\n\t"
        "s_or_b32 s81, s38, %[snd]\n\t"
        "v_mfma_f32_16x16x32_bf16 v[136:139], a[56:59], v[108:111], v[136:139]\n\t"
        "s_cmp_lt_i32 s81, 0\n\t"
        "s_cselect_b32 s82, s36, s80\n\t"
        "buffer_load_dwordx4 v[168:171], v246, %[rd], s82 offen offset:0 sc1\n\t"
        "v_mfma_f32_16x16x32_bf16 v[140:143], a[60:63], v[108:111], v[140:143]\n\t"
        "buffer_load_dwordx4 v[172:175], v246, %[rd], s82 offen offset:1024 sc1\n\t"
        "buffer_load_dwordx4 v[108:111], v241, %[rd], s84 offen sc1\n\t"
        "s_add_u32 s84, s84, 8192\n\t"
        "s_waitcnt vmcnt(49)\n\t"
        "v_mfma_f32_16x16x32_bf16 v[128:131], a[64:67], v[112:115], v[128:131]\n\t"
        "v_mfma_f32_16x16x32_bf16 v[132:135], a[68:71], v[112:115], v[132:135]\n\t"
        "v_mfma_f32_16x16x32_bf16 v[136:139], a[72:75], v[112:115], v[136:139]\n\t"
        "v_mfma_f32_16x16x32_bf16 v[140:143], a[76:79], v[112:115], v[140:143]\n\t"
        "buffer_load_dwordx4 v[112:115], v240, %[rd], s84 offen sc1\n\t"
        "s_waitcnt vmcnt(49)\n\t"
        "v_mfma_f32_16x16x32_bf16 v[128:131], a[80:83], v[116:119], v[128:131]\n\t"
        "v_mfma_f32_16x16x32_bf16 v[132:135], a[84:87], v[116:119], v[132:135]\n\t"
        "v_mfma_f32_16x16x32_bf16 v[136:139], a[88:91], v[116:119], v[136:139]\n\t"
        "v_mfma_f32_16x16x32_bf16 v[140:143], a[92:95], v[116:119], v[140:143]\n\t"
        "buffer_load_dwordx4 v[116:119], v241, %[rd], s84 offen sc1\n\t"
        "s_add_u32 s84, s84, 8192\n\t"
        "s_waitcnt vmcnt(49)\n\t"
        "v_mfma_f32_16x16x32_bf16 v[128:131], a[96:99], v[120:123], v[128:131]\n\t"
        "v_mfma_f32_16x16x32_bf16 v[132:135], a[100:103], v[120:123], v[132:135]\n\t"
        "v_mfma_f32_16x16x32_bf16 v[136:139], a[104:107], v[120:123], v[136:139]\n\t"
        "v_mfma_f32_16x16x32_bf16 v[140:143], a[108:111], v[120:123], v[140:143]\n\t"
        "buffer_load_dwordx4 v[120:123], v240, %[rd], s84 offen sc1\n\t"
        "s_waitcnt vmcnt(49)\n\t"
        "v_mfma_f32_16x16x32_bf16 v[128:131], a[112:115], v[124:127], v[128:131]\n\t"
        "v_mfma_f32_16x16x32_bf16 v[132:135], a[116:119], v[124:127], v[132:135]\n\t"
        "v_mfma_f32_16x16x32_bf16 v[136:139], a[120:123], v[124:127], v[136:139]\n\t"
        "v_mfma_f32_16x16x32_bf16 v[140:143], a[124:127], v[124:127], v[140:143]\n\t"
        "buffer_load_dwordx4 v[124:127], v241, %[rd], s84 offen sc1\n\t"
        "s_add_u32 s84, s84, 8192\n\t"
        "s_nop 2\n\t"
        "ds_write_b128 v222, v[128:131] offset:4096\n\t"
        "ds_write_b128 v222, v[132:135] offset:5120\n\t"
        "ds_write_b128 v222, v[136:139] offset:6144\n\t"
        "ds_write_b128 v222, v[140:143] offset:7168\n\t"
        "s_waitcnt vmcnt(49)\n\t"
        "v_cmp_eq_u32_e32 vcc, 32, v220\n\t"
        "s_nop 1\n\t"
        "s_or_b32 s40, s40, vcc_lo\n\t"
        "s_waitcnt lgkmcnt(0)\n\t"
        "s_barrier\n\t"
        "s_cmp_eq_u32 s41, 0\n\t"
        "s_cbranch_scc1 .Lfl%=\n\t"
        "v_mov_b32_e32 v152, v144\n\t"
        "v_mov_b32_e32 v153, v145\n\t"
        "v_mov_b32_e32 v154, v146\n\t"
        "v_mov_b32_e32 v155, v147\n\t"
        "v_mov_b32_e32 v156, v148\n\t"
        "v_mov_b32_e32 v157, v149\n\t"
        "v_mov_b32_e32 v158, v150\n\t"
        "v_mov_b32_e32 v159, v151\n\t"
        "s_mov_b32 s39, s38\n\t"
        "s_mov_b32 s38, s44\n\t"
        "s_add_u32 s37, s37, 1\n\t"
        "s_add_u32 s44, s44, 8\n\t"
        "s_xor_b32 s43, s43, 0x8000\n\t"
        "s_branch .Lrnd%=\n\t"
        ".Lfl%=:\n\t"
        "v_add_u32_e32 v223, s43, v248\n\t"
        "buffer_load_dword v220, v242, %[rd], 0 offen sc1\n\t"
        "ds_read_b128 v[186:189], v223 offset:0\n\t"
        "ds_read_b128 v[190:193], v223 offset:8192\n\t"
        "ds_read_b128 v[194:197], v223 offset:16384\n\t"
        "ds_read_b128 v[198:201], v223 offset:24576\n\t"
        "s_waitcnt lgkmcnt(0)\n\t"
        "v_add_f32_e32 v186, v186, v190\n\t"
        "v_add_f32_e32 v187, v187, v191\n\t"
        "v_add_f32_e32 v188, v188, v192\n\t"
        "v_add_f32_e32 v189, v189, v193\n\t"
        "v_add_f32_e32 v194, v194, v198\n\t"
        "v_add_f32_e32 v195, v195, v199\n\t"
        "v_add_f32_e32 v196, v196, v200\n\t"
        "v_add_f32_e32 v197, v197, v201\n\t"
        "v_add_f32_e32 v204, v186, v194\n\t"
        "v_add_f32_e32 v206, v187, v195\n\t"
        "v_add_f32_e32 v208, v188, v196\n\t"
        "v_add_f32_e32 v210, v189, v197\n\t"
        "s_add_i32 s80, s44, 0\n\t"
        "s_lshl_b32 s80, s80, 17\n\t"
        "s_or_b32 s81, s44, %[own]\n\t"
        "s_cmp_lt_i32 s81, 0\n\t"
        "s_cselect_b32 s82, s36, s80\n\t"
        "buffer_store_dwordx4 v[204:207], v246, %[rd], s82 offen offset:0 sc1\n\t"
        "buffer_store_dwordx4 v[208:211], v246, %[rd], s82 offen offset:1024 sc1\n\t"
        "v_mov_b32_e32 v128, v204\n\t"
        "v_mov_b32_e32 v129, v206\n\t"
        "v_mov_b32_e32 v130, v208\n\t"
        "v_mov_b32_e32 v131, v210\n\t"
        "ds_read_b128 v[186:189], v223 offset:4096\n\t"
        "ds_read_b128 v[190:193], v223 offset:12288\n\t"
        "ds_read_b128 v[194:197], v223 offset:20480\n\t"
        "ds_read_b128 v[198:201], v223 offset:28672\n\t"
        "s_waitcnt lgkmcnt(0)\n\t"
        "v_add_f32_e32 v186, v186, v190\n\t"
        "v_add_f32_e32 v187, v187, v191\n\t"
        "v_add_f32_e32 v188, v188, v192\n\t"
        "v_add_f32_e32 v189, v189, v193\n\t"
        "v_add_f32_e32 v194, v194, v198\n\t"
        "v_add_f32_e32 v195, v195, v199\n\t"
        "v_add_f32_e32 v196, v196, v200\n\t"
        "v_add_f32_e32 v197, v197, v201\n\t"
        "v_add_f32_e32 v212, v186, v194\n\t"
        "v_add_f32_e32 v214, v187, v195\n\t"
        "v_add_f32_e32 v216, v188, v196\n\t"
        "v_add_f32_e32 v218, v189, v197\n\t"
        "s_add_i32 s80, s44, 1\n\t"
        "s_lshl_b32 s80, s80, 17\n\t"
        "s_or_b32 s81, s44, %[own]\n\t"
        "s_cmp_lt_i32 s81, 0\n\t"
        "s_cselect_b32 s82, s36, s80\n\t"
        "buffer_store_dwordx4 v[212:215], v246, %[rd], s82 offen offset:0 sc1\n\t"
        "buffer_store_dwordx4 v[216:219], v246, %[rd], s82 offen offset:1024 sc1\n\t"
        "v_mov_b32_e32 v132, v212\n\t"
        "v_mov_b32_e32 v133, v214\n\t"
        "v_mov_b32_e32 v134, v216\n\t"
        "v_mov_b32_e32 v135, v218\n\t"
        "s_waitcnt vmcnt(4)\n\t"
        "v_cmp_eq_u32_e32 vcc, 32, v220\n\t"
        "s_nop 1\n\t"
        "s_or_b32 s40, s40, vcc_lo\n\t"
        "s_add_i32 s80, s38, 0\n\t"
        "s_lshl_b32 s80, s80, 15\n\t"
        "s_or_b32 s81, s38, %[snd]\n\t"
        "s_cmp_lt_i32 s81, 0\n\t"
        "s_cselect_b32 s83, s36, s80\n\t"
        "s_cbranch_scc1 .Lo19%=\n\t"
        "s_mov_b32 s97, 0\n\t"
        ".Lo17%=:\n\t"
        "v_xor_b32_e32 v186, %[tag], v161\n\t"
        "v_xor_b32_e32 v187, %[tag], v163\n\t"
        "v_xor_b32_e32 v188, %[tag], v165\n\t"
        "v_or3_b32 v186, v186, v187, v188\n\t"
        "v_xor_b32_e32 v187, %[tag], v167\n\t"
        "v_or_b32_e32 v186, v186, v187\n\t"
        "v_cmp_ne_u32_e32 vcc, 0, v186\n\t"
        "v_sub_f32_e32 v198, 1.0, v184\n\t"
        "v_add_f32_e32 v190, v144, v160\n\t"
        "v_add_f32_e32 v191, v145, v162\n\t"
        "v_add_f32_e32 v192, v146, v164\n\t"
        "v_add_f32_e32 v193, v147, v166\n\t"
        "s_cbranch_vccz .Lo18%=\n\t"
        "s_sleep %c[xslp]\n\t"
        "s_lshl_b32 s82, s83, 2\n\t"
        "buffer_load_dwordx4 v[160:163], v246, %[rd], s82 offen offset:0 sc1\n\t"
        "buffer_load_dwordx4 v[164:167], v246, %[rd], s82 offen offset:1024 sc1\n\t"
        "s_add_u32 s97, s97, 1\n\t"
        "s_cmp_lt_u32 s97, 0x200000\n\t"
        "s_cbranch_scc0 .Lgu%=\n\t"
        "s_waitcnt vmcnt(0)\n\t"
        "s_branch .Lo17%=\n\t"
        ".Lo18%=:\n\t"
        "v_cvt_pk_bf16_f32 v198, 0, v198\n\t"
        "v_cvt_pk_bf16_f32 v190, 0, v190\n\t"
        "v_cvt_pk_bf16_f32 v191, 0, v191\n\t"
        "v_cvt_pk_bf16_f32 v192, 0, v192\n\t"
        "v_cvt_pk_bf16_f32 v193, 0, v193\n\t"
        "v_lshlrev_b32_e32 v186, 16, v180\n\t"
        "v_lshlrev_b32_e32 v194, 16, v176\n\t"
        "v_and_b32_e32 v187, 0xffff0000, v180\n\t"
        "v_and_b32_e32 v195, 0xffff0000, v176\n\t"
        "v_lshlrev_b32_e32 v188, 16, v181\n\t"
        "v_lshlrev_b32_e32 v196, 16, v177\n\t"
        "v_and_b32_e32 v189, 0xffff0000, v181\n\t"
        "v_and_b32_e32 v197, 0xffff0000, v177\n\t"
        "v_mul_f32_e32 v186, v186, v194\n\t"
        "v_mul_f32_e32 v187, v187, v195\n\t"
        "v_mul_f32_e32 v188, v188, v196\n\t"
        "v_mul_f32_e32 v189, v189, v197\n\t"
        "v_mul_f32_e32 v190, v198, v190\n\t"
        "v_mul_f32_e32 v191, v198, v191\n\t"
        "v_mul_f32_e32 v192, v198, v192\n\t"
        "v_mul_f32_e32 v193, v198, v193\n\t"
        "v_cvt_pk_bf16_f32 v186, 0, v186\n\t"
        "v_cvt_pk_bf16_f32 v187, 0, v187\n\t"
        "v_cvt_pk_bf16_f32 v188, 0, v188\n\t"
        "v_cvt_pk_bf16_f32 v189, 0, v189\n\t"
        "v_cvt_pk_bf16_f32 v190, 0, v190\n\t"
        "v_cvt_pk_bf16_f32 v191, 0, v191\n\t"
        "v_cvt_pk_bf16_f32 v192, 0, v192\n\t"
        "v_cvt_pk_bf16_f32 v193, 0, v193\n\t"
        "v_mul_f32_e32 v186, v184, v186\n\t"
        "v_mul_f32_e32 v187, v184, v187\n\t"
        "v_mul_f32_e32 v188, v184, v188\n\t"
        "v_mul_f32_e32 v189, v184, v189\n\t"
        "v_cvt_pk_bf16_f32 v186, 0, v186\n\t"
        "v_cvt_pk_bf16_f32 v187, 0, v187\n\t"
        "v_cvt_pk_bf16_f32 v188, 0, v188\n\t"
        "v_cvt_pk_bf16_f32 v189, 0, v189\n\t"
        "v_add_f32_e32 v186, v186, v190\n\t"
        "v_add_f32_e32 v187, v187, v191\n\t"
        "v_add_f32_e32 v188, v188, v192\n\t"
        "v_add_f32_e32 v189, v189, v193\n\t"
        "v_cvt_pk_bf16_f32 v200, v186, v187\n\t"
        "v_cvt_pk_bf16_f32 v201, v188, v189\n\t"
        ".Lo19%=:\n\t"
        "buffer_store_dwordx2 v[200:201], v245, %[ro], s83 offen\n\t"
        "s_add_i32 s80, s38, 1\n\t"
        "s_lshl_b32 s80, s80, 15\n\t"
        "s_or_b32 s81, s38, %[snd]\n\t"
        "s_cmp_lt_i32 s81, 0\n\t"
        "s_cselect_b32 s83, s36, s80\n\t"
        "s_cbranch_scc1 .Lo22%=\n\t"
        "s_mov_b32 s97, 0\n\t"
        ".Lo20%=:\n\t"
        "v_xor_b32_e32 v186, %[tag], v169\n\t"
        "v_xor_b32_e32 v187, %[tag], v171\n\t"
        "v_xor_b32_e32 v188, %[tag], v173\n\t"
        "v_or3_b32 v186, v186, v187, v188\n\t"
        "v_xor_b32_e32 v187, %[tag], v175\n\t"
        "v_or_b32_e32 v186, v186, v187\n\t"
        "v_cmp_ne_u32_e32 vcc, 0, v186\n\t"
        "v_sub_f32_e32 v198, 1.0, v185\n\t"
        "v_add_f32_e32 v190, v148, v168\n\t"
        "v_add_f32_e32 v191, v149, v170\n\t"
        "v_add_f32_e32 v192, v150, v172\n\t"
        "v_add_f32_e32 v193, v151, v174\n\t"
        "s_cbranch_vccz .Lo21%=\n\t"
        "s_sleep %c[xslp]\n\t"
        "s_lshl_b32 s82, s83, 2\n\t"
        "buffer_load_dwordx4 v[168:171], v246, %[rd], s82 offen offset:0 sc1\n\t"
        "buffer_load_dwordx4 v[172:175], v246, %[rd], s82 offen offset:1024 sc1\n\t"
        "s_add_u32 s97, s97, 1\n\t"
        "s_cmp_lt_u32 s97, 0x200000\n\t"
        "s_cbranch_scc0 .Lgu%=\n\t"
        "s_waitcnt vmcnt(0)\n\t"
        "s_branch .Lo20%=\n\t"
        ".Lo21%=:\n\t"
        "v_cvt_pk_bf16_f32 v198, 0, v198\n\t"
        "v_cvt_pk_bf16_f32 v190, 0, v190\n\t"
        "v_cvt_pk_bf16_f32 v191, 0, v191\n\t"
        "v_cvt_pk_bf16_f32 v192, 0, v192\n\t"
        "v_cvt_pk_bf16_f32 v193, 0, v193\n\t"
        "v_lshlrev_b32_e32 v186, 16, v182\n\t"
        "v_lshlrev_b32_e32 v194, 16, v178\n\t"
        "v_and_b32_e32 v187, 0xffff0000, v182\n\t"
        "v_and_b32_e32 v195, 0xffff0000, v178\n\t"
        "v_lshlrev_b32_e32 v188, 16, v183\n\t"
        "v_lshlrev_b32_e32 v196, 16, v179\n\t"
        "v_and_b32_e32 v189, 0xffff0000, v183\n\t"
        "v_and_b32_e32 v197, 0xffff0000, v179\n\t"
        "v_mul_f32_e32 v186, v186, v194\n\t"
        "v_mul_f32_e32 v187, v187, v195\n\t"
        "v_mul_f32_e32 v188, v188, v196\n\t"
        "v_mul_f32_e32 v189, v189, v197\n\t"
        "v_mul_f32_e32 v190, v198, v190\n\t"
        "v_mul_f32_e32 v191, v198, v191\n\t"
        "v_mul_f32_e32 v192, v198, v192\n\t"
        "v_mul_f32_e32 v193, v198, v193\n\t"
        "v_cvt_pk_bf16_f32 v186, 0, v186\n\t"
        "v_cvt_pk_bf16_f32 v187, 0, v187\n\t"
        "v_cvt_pk_bf16_f32 v188, 0, v188\n\t"
        "v_cvt_pk_bf16_f32 v189, 0, v189\n\t"
        "v_cvt_pk_bf16_f32 v190, 0, v190\n\t"
        "v_cvt_pk_bf16_f32 v191, 0, v191\n\t"
        "v_cvt_pk_bf16_f32 v192, 0, v192\n\t"
        "v_cvt_pk_bf16_f32 v193, 0, v193\n\t"
        "v_mul_f32_e32 v186, v185, v186\n\t"
        "v_mul_f32_e32 v187, v185, v187\n\t"
        "v_mul_f32_e32 v188, v185, v188\n\t"
        "v_mul_f32_e32 v189, v185, v189\n\t"
        "v_cvt_pk_bf16_f32 v186, 0, v186\n\t"
        "v_cvt_pk_bf16_f32 v187, 0, v187\n\t"
        "v_cvt_pk_bf16_f32 v188, 0, v188\n\t"
        "v_cvt_pk_bf16_f32 v189, 0, v189\n\t"
        "v_add_f32_e32 v186, v186, v190\n\t"
        "v_add_f32_e32 v187, v187, v191\n\t"
        "v_add_f32_e32 v188, v188, v192\n\t"
        "v_add_f32_e32 v189, v189, v193\n\t"
        "v_cvt_pk_bf16_f32 v200, v186, v187\n\t"
        "v_cvt_pk_bf16_f32 v201, v188, v189\n\t"
        ".Lo22%=:\n\t"
        "buffer_store_dwordx2 v[200:201], v245, %[ro], s83 offen\n\t"
        "s_add_i32 s80, s44, 0\n\t"
        "s_lshl_b32 s80, s80, 6\n\t"
        "s_or_b32 s81, s44, %[snd]\n\t"
        "s_cmp_lt_i32 s81, 0\n\t"
        "s_cselect_b32 s82, s36, s80\n\t"
        "buffer_load_dword v184, v244, %[rd], s82 offen sc1\n\t"
        "s_lshl_b32 s80, s80, 1\n\t"
        "s_cmp_lt_i32 s81, 0\n\t"
        "s_cselect_b32 s82, s36, s80\n\t"
        "buffer_load_dwordx2 v[180:181], v243, %[rd], s82 offen sc1\n\t"
        "s_lshl_b32 s80, s80, 8\n\t"
        "s_cmp_lt_i32 s81, 0\n\t"
        "s_cselect_b32 s82, s36, s80\n\t"
        "buffer_load_dwordx2 v[176:177], v245, %[rx], s82 offen\n\t"
        "s_add_i32 s80, s44, 1\n\t"
        "s_lshl_b32 s80, s80, 6\n\t"
        "s_or_b32 s81, s44, %[snd]\n\t"
        "s_cmp_lt_i32 s81, 0\n\t"
        "s_cselect_b32 s82, s36, s80\n\t"
        "buffer_load_dword v185, v244, %[rd], s82 offen sc1\n\t"
        "s_lshl_b32 s80, s80, 1\n\t"
        "s_cmp_lt_i32 s81, 0\n\t"
        "s_cselect_b32 s82, s36, s80\n\t"
        "buffer_load_dwordx2 v[182:183], v243, %[rd], s82 offen sc1\n\t"
        "s_lshl_b32 s80, s80, 8\n\t"
        "s_cmp_lt_i32 s81, 0\n\t"
        "s_cselect_b32 s82, s36, s80\n\t"
        "buffer_load_dwordx2 v[178:179], v245, %[rx], s82 offen\n\t"
        "s_add_i32 s80, s44, 0\n\t"
        "s_lshl_b32 s80, s80, 17\n\t"
        "s_or_b32 s81, s44, %[snd]\n\t"
        "s_cmp_lt_i32 s81, 0\n\t"
        "s_cselect_b32 s82, s36, s80\n\t"
        "buffer_load_dwordx4 v[160:163], v246, %[rd], s82 offen offset:0 sc1\n\t"
        "buffer_load_dwordx4 v[164:167], v246, %[rd], s82 offen offset:1024 sc1\n\t"
        "s_add_i32 s80, s44, 1\n\t"
        "s_lshl_b32 s80, s80, 17\n\t"
        "s_or_b32 s81, s44, %[snd]\n\t"
        "s_cmp_lt_i32 s81, 0\n\t"
        "s_cselect_b32 s82, s36, s80\n\t"
        "buffer_load_dwordx4 v[168:171], v246, %[rd], s82 offen offset:0 sc1\n\t"
        "buffer_load_dwordx4 v[172:175], v246, %[rd], s82 offen offset:1024 sc1\n\t"
        "s_mov_b32 s38, -1\n\t"
        "v_mov_b32_e32 v152, v128\n\t"
        "v_mov_b32_e32 v153, v129\n\t"
        "v_mov_b32_e32 v154, v130\n\t"
        "v_mov_b32_e32 v155, v131\n\t"
        "v_mov_b32_e32 v156, v132\n\t"
        "v_mov_b32_e32 v157, v133\n\t"
        "v_mov_b32_e32 v158, v134\n\t"
        "v_mov_b32_e32 v159, v135\n\t"
        "s_mov_b32 s39, s44\n\t"
        "s_add_u32 s37, s37, 1\n\t"
        "s_add_u32 s44, s44, 8\n\t"
        "s_xor_b32 s43, s43, 0x8000\n\t"
        "s_branch .Lnew%=\n\t"
        ".Lsol%=:\n\t"
        "s_add_u32 s44, %[rho], 120\n\t"
        "s_mov_b32 s37, 30\n\t"
        ".Lone%=:\n\t"
        "s_add_i32 s80, s44, 0\n\t"
        "s_lshl_b32 s80, s80, 6\n\t"
        "s_or_b32 s81, s44, %[snd]\n\t"
        "s_cmp_lt_i32 s81, 0\n\t"
        "s_cselect_b32 s82, s36, s80\n\t"
        "buffer_load_dword v184, v244, %[rd], s82 offen sc1\n\t"
        "s_lshl_b32 s80, s80, 1\n\t"
        "s_cmp_lt_i32 s81, 0\n\t"
        "s_cselect_b32 s82, s36, s80\n\t"
        "buffer_load_dwordx2 v[180:181], v243, %[rd], s82 offen sc1\n\t"
        "s_lshl_b32 s80, s80, 8\n\t"
        "s_cmp_lt_i32 s81, 0\n\t"
        "s_cselect_b32 s82, s36, s80\n\t"
        "buffer_load_dwordx2 v[176:177], v245, %[rx], s82 offen\n\t"
        "v_add_u32_e32 v222, s43, v247\n\t"
        "v_add_u32_e32 v223, s43, v248\n\t"
        "s_mov_b32 s97, 0\n\t"
        ".Lo23%=:\n\t"
        "s_lshr_b32 s80, s40, s37\n\t"
        "s_andn2_b32 s80, 1, s80\n\t"
        "s_cbranch_scc0 .Lo24%=\n\t"
        "s_cmp_eq_u32 s97, 0\n\t"
        "s_cbranch_scc1 .Lo23%=x\n\t"
        "s_sleep %c[oslp]\n\t"
        ".Lo23%=x:\n\t"
        "buffer_load_dword v220, v242, %[rd], 0 offen sc1\n\t"
        "s_add_u32 s97, s97, 1\n\t"
        "s_cmp_lt_u32 s97, 0x200000\n\t"
        "s_cbranch_scc0 .Lgu%=\n\t"
        "s_waitcnt vmcnt(0)\n\t"
        "v_cmp_eq_u32_e32 vcc, 32, v220\n\t"
        "s_nop 1\n\t"
        "s_or_b32 s40, s40, vcc_lo\n\t"
        "s_branch .Lo23%=\n\t"
        ".Lo24%=:\n\t"
        "s_lshl_b32 s84, s44, 17\n\t"
        "buffer_load_dwordx4 v[0:3], v240, %[rd], s84 offen sc1\n\t"
        "buffer_load_dwordx4 v[4:7], v241, %[rd], s84 offen sc1\n\t"
        "s_add_u32 s84, s84, 8192\n\t"
        "buffer_load_dwordx4 v[8:11], v240, %[rd], s84 offen sc1\n\t"
        "buffer_load_dwordx4 v[12:15], v241, %[rd], s84 offen sc1\n\t"
        "s_add_u32 s84, s84, 8192\n\t"
        "buffer_load_dwordx4 v[16:19], v240, %[rd], s84 offen sc1\n\t"
        "buffer_load_dwordx4 v[20:23], v241, %[rd], s84 offen sc1\n\t"
        "s_add_u32 s84, s84, 8192\n\t"
        "buffer_load_dwordx4 v[24:27], v240, %[rd], s84 offen sc1\n\t"
        "buffer_load_dwordx4 v[28:31], v241, %[rd], s84 offen sc1\n\t"
        "s_add_u32 s84, s84, 8192\n\t"
        "buffer_load_dwordx4 v[32:35], v240, %[rd], s84 offen sc1\n\t"
        "buffer_load_dwordx4 v[36:39], v241, %[rd], s84 offen sc1\n\t"
        "s_add_u32 s84, s84, 8192\n\t"
        "buffer_load_dwordx4 v[40:43], v240, %[rd], s84 offen sc1\n\t"
        "buffer_load_dwordx4 v[44:47], v241, %[rd], s84 offen sc1\n\t"
        "s_add_u32 s84, s84, 8192\n\t"
        "buffer_load_dwordx4 v[48:51], v240, %[rd], s84 offen sc1\n\t"
        "buffer_load_dwordx4 v[52:55], v241, %[rd], s84 offen sc1\n\t"
        "s_add_u32 s84, s84, 8192\n\t"
        "buffer_load_dwordx4 v[56:59], v240, %[rd], s84 offen sc1\n\t"
        "buffer_load_dwordx4 v[60:63], v241, %[rd], s84 offen sc1\n\t"
        "s_add_u32 s84, s84, 73728\n\t"
        "s_waitcnt vmcnt(15)\n\t"
        "v_mfma_f32_16x16x32_bf16 v[128:131], a[128:131], v[0:3], 0\n\t"
        "v_mfma_f32_16x16x32_bf16 v[132:135], a[132:135], v[0:3], 0\n\t"
        "v_mfma_f32_16x16x32_bf16 v[136:139], a[136:139], v[0:3], 0\n\t"
        "v_mfma_f32_16x16x32_bf16 v[140:143], a[140:143], v[0:3], 0\n\t"
        "s_waitcnt vmcnt(14)\n\t"
        "v_mfma_f32_16x16x32_bf16 v[128:131], a[144:147], v[4:7], v[128:131]\n\t"
        "v_mfma_f32_16x16x32_bf16 v[132:135], a[148:151], v[4:7], v[132:135]\n\t"
        "v_mfma_f32_16x16x32_bf16 v[136:139], a[152:155], v[4:7], v[136:139]\n\t"
        "v_mfma_f32_16x16x32_bf16 v[140:143], a[156:159], v[4:7], v[140:143]\n\t"
        "s_waitcnt vmcnt(13)\n\t"
        "v_mfma_f32_16x16x32_bf16 v[128:131], a[160:163], v[8:11], v[128:131]\n\t"
        "v_mfma_f32_16x16x32_bf16 v[132:135], a[164:167], v[8:11], v[132:135]\n\t"
        "v_mfma_f32_16x16x32_bf16 v[136:139], a[168:171], v[8:11], v[136:139]\n\t"
        "v_mfma_f32_16x16x32_bf16 v[140:143], a[172:175], v[8:11], v[140:143]\n\t"
        "s_waitcnt vmcnt(12)\n\t"
        "v_mfma_f32_16x16x32_bf16 v[128:131], a[176:179], v[12:15], v[128:131]\n\t"
        "v_mfma_f32_16x16x32_bf16 v[132:135], a[180:183], v[12:15], v[132:135]\n\t"
        "v_mfma_f32_16x16x32_bf16 v[136:139], a[184:187], v[12:15], v[136:139]\n\t"
        "v_mfma_f32_16x16x32_bf16 v[140:143], a[188:191], v[12:15], v[140:143]\n\t"
        "s_waitcnt vmcnt(11)\n\t"
        "v_mfma_f32_16x16x32_bf16 v[128:131], a[192:195], v[16:19], v[128:131]\n\t"
        "v_mfma_f32_16x16x32_bf16 v[132:135], a[196:199], v[16:19], v[132:135]\n\t"
        "v_mfma_f32_16x16x32_bf16 v[136:139], a[200:203], v[16:19], v[136:139]\n\t"
        "v_mfma_f32_16x16x32_bf16 v[140:143], a[204:207], v[16:19], v[140:143]\n\t"
        "s_waitcnt vmcnt(10)\n\t"
        "v_mfma_f32_16x16x32_bf16 v[128:131], a[208:211], v[20:23], v[128:131]\n\t"
        "v_mfma_f32_16x16x32_bf16 v[132:135], a[212:215], v[20:23], v[132:135]\n\t"
        "v_mfma_f32_16x16x32_bf16 v[136:139], a[216:219], v[20:23], v[136:139]\n\t"
        "v_mfma_f32_16x16x32_bf16 v[140:143], a[220:223], v[20:23], v[140:143]\n\t"
        "s_waitcnt vmcnt(9)\n\t"
        "v_mfma_f32_16x16x32_bf16 v[128:131], a[224:227], v[24:27], v[128:131]\n\t"
        "v_mfma_f32_16x16x32_bf16 v[132:135], a[228:231], v[24:27], v[132:135]\n\t"
        "v_mfma_f32_16x16x32_bf16 v[136:139], a[232:235], v[24:27], v[136:139]\n\t"
        "v_mfma_f32_16x16x32_bf16 v[140:143], a[236:239], v[24:27], v[140:143]\n\t"
        "s_waitcnt vmcnt(8)\n\t"
        "v_mfma_f32_16x16x32_bf16 v[128:131], a[240:243], v[28:31], v[128:131]\n\t"
        "v_mfma_f32_16x16x32_bf16 v[132:135], a[244:247], v[28:31], v[132:135]\n\t"
        "v_mfma_f32_16x16x32_bf16 v[136:139], a[248:251], v[28:31], v[136:139]\n\t"
        "v_mfma_f32_16x16x32_bf16 v[140:143], a[252:255], v[28:31], v[140:143]\n\t"
        "s_waitcnt vmcnt(7)\n\t"
        "v_mfma_f32_16x16x32_bf16 v[128:131], a[0:3], v[32:35], v[128:131]\n\t"
        "v_mfma_f32_16x16x32_bf16 v[132:135], a[4:7], v[32:35], v[132:135]\n\t"
        "v_mfma_f32_16x16x32_bf16 v[136:139], a[8:11], v[32:35], v[136:139]\n\t"
        "v_mfma_f32_16x16x32_bf16 v[140:143], a[12:15], v[32:35], v[140:143]\n\t"
        "s_waitcnt vmcnt(6)\n\t"
        "v_mfma_f32_16x16x32_bf16 v[128:131], a[16:19], v[36:39], v[128:131]\n\t"
        "v_mfma_f32_16x16x32_bf16 v[132:135], a[20:23], v[36:39], v[132:135]\n\t"
        "v_mfma_f32_16x16x32_bf16 v[136:139], a[24:27], v[36:39], v[136:139]\n\t"
        "v_mfma_f32_16x16x32_bf16 v[140:143], a[28:31], v[36:39], v[140:143]\n\t"
        "s_waitcnt vmcnt(5)\n\t"
        "v_mfma_f32_16x16x32_bf16 v[128:131], a[32:35], v[40:43], v[128:131]\n\t"
        "v_mfma_f32_16x16x32_bf16 v[132:135], a[36:39], v[40:43], v[132:135]\n\t"
        "v_mfma_f32_16x16x32_bf16 v[136:139], a[40:43], v[40:43], v[136:139]\n\t"
        "v_mfma_f32_16x16x32_bf16 v[140:143], a[44:47], v[40:43], v[140:143]\n\t"
        "s_waitcnt vmcnt(4)\n\t"
        "v_mfma_f32_16x16x32_bf16 v[128:131], a[48:51], v[44:47], v[128:131]\n\t"
        "v_mfma_f32_16x16x32_bf16 v[132:135], a[52:55], v[44:47], v[132:135]\n\t"
        "v_mfma_f32_16x16x32_bf16 v[136:139], a[56:59], v[44:47], v[136:139]\n\t"
        "v_mfma_f32_16x16x32_bf16 v[140:143], a[60:63], v[44:47], v[140:143]\n\t"
        "s_waitcnt vmcnt(3)\n\t"
        "v_mfma_f32_16x16x32_bf16 v[128:131], a[64:67], v[48:51], v[128:131]\n\t"
        "v_mfma_f32_16x16x32_bf16 v[132:135], a[68:71], v[48:51], v[132:135]\n\t"
        "v_mfma_f32_16x16x32_bf16 v[136:139], a[72:75], v[48:51], v[136:139]\n\t"
        "v_mfma_f32_16x16x32_bf16 v[140:143], a[76:79], v[48:51], v[140:143]\n\t"
        "s_waitcnt vmcnt(2)\n\t"
        "v_mfma_f32_16x16x32_bf16 v[128:131], a[80:83], v[52:55], v[128:131]\n\t"
        "v_mfma_f32_16x16x32_bf16 v[132:135], a[84:87], v[52:55], v[132:135]\n\t"
        "v_mfma_f32_16x16x32_bf16 v[136:139], a[88:91], v[52:55], v[136:139]\n\t"
        "v_mfma_f32_16x16x32_bf16 v[140:143], a[92:95], v[52:55], v[140:143]\n\t"
        "s_waitcnt vmcnt(1)\n\t"
        "v_mfma_f32_16x16x32_bf16 v[128:131], a[96:99], v[56:59], v[128:131]\n\t"
        "v_mfma_f32_16x16x32_bf16 v[132:135], a[100:103], v[56:59], v[132:135]\n\t"
        "v_mfma_f32_16x16x32_bf16 v[136:139], a[104:107], v[56:59], v[136:139]\n\t"
        "v_mfma_f32_16x16x32_bf16 v[140:143], a[108:111], v[56:59], v[140:143]\n\t"
        "s_waitcnt vmcnt(0)\n\t"
        "v_mfma_f32_16x16x32_bf16 v[128:131], a[112:115], v[60:63], v[128:131]\n\t"
        "v_mfma_f32_16x16x32_bf16 v[132:135], a[116:119], v[60:63], v[132:135]\n\t"
        "v_mfma_f32_16x16x32_bf16 v[136:139], a[120:123], v[60:63], v[136:139]\n\t"
        "v_mfma_f32_16x16x32_bf16 v[140:143], a[124:127], v[60:63], v[140:143]\n\t"
        "s_nop 4\n\t"
        "ds_write_b128 v222, v[128:131] offset:0\n\t"
        "ds_write_b128 v222, v[132:135] offset:1024\n\t"
        "ds_write_b128 v222, v[136:139] offset:2048\n\t"
        "ds_write_b128 v222, v[140:143] offset:3072\n\t"
        "s_waitcnt lgkmcnt(0)\n\t"
        "s_barrier\n\t"
        "ds_read_b128 v[186:189], v223 offset:0\n\t"
        "ds_read_b128 v[190:193], v223 offset:8192\n\t"
        "ds_read_b128 v[194:197], v223 offset:16384\n\t"
        "ds_read_b128 v[198:201], v223 offset:24576\n\t"
        "s_waitcnt lgkmcnt(0)\n\t"
        "v_add_f32_e32 v186, v186, v190\n\t"
        "v_add_f32_e32 v187, v187, v191\n\t"
        "v_add_f32_e32 v188, v188, v192\n\t"
        "v_add_f32_e32 v189, v189, v193\n\t"
        "v_add_f32_e32 v194, v194, v198\n\t"
        "v_add_f32_e32 v195, v195, v199\n\t"
        "v_add_f32_e32 v196, v196, v200\n\t"
        "v_add_f32_e32 v197, v197, v201\n\t"
        "v_add_f32_e32 v204, v186, v194\n\t"
        "v_add_f32_e32 v206, v187, v195\n\t"
        "v_add_f32_e32 v208, v188, v196\n\t"
        "v_add_f32_e32 v210, v189, v197\n\t"
        "s_add_i32 s80, s44, 0\n\t"
        "s_lshl_b32 s80, s80, 17\n\t"
        "s_or_b32 s81, s44, %[own]\n\t"
        "s_cmp_lt_i32 s81, 0\n\t"
        "s_cselect_b32 s82, s36, s80\n\t"
        "buffer_store_dwordx4 v[204:207], v246, %[rd], s82 offen offset:0 sc1\n\t"
        "buffer_store_dwordx4 v[208:211], v246, %[rd], s82 offen offset:1024 sc1\n\t"
        "v_mov_b32_e32 v144, v204\n\t"
        "v_mov_b32_e32 v145, v206\n\t"
        "v_mov_b32_e32 v146, v208\n\t"
        "v_mov_b32_e32 v147, v210\n\t"
        "s_sleep %c[xdel]\n\t"
        "s_add_i32 s80, s44, 0\n\t"
        "s_lshl_b32 s80, s80, 17\n\t"
        "s_or_b32 s81, s44, %[snd]\n\t"
        "s_cmp_lt_i32 s81, 0\n\t"
        "s_cselect_b32 s82, s36, s80\n\t"
        "buffer_load_dwordx4 v[160:163], v246, %[rd], s82 offen offset:0 sc1\n\t"
        "buffer_load_dwordx4 v[164:167], v246, %[rd], s82 offen offset:1024 sc1\n\t"
        "s_waitcnt vmcnt(0)\n\t"
        "s_add_i32 s80, s44, 0\n\t"
        "s_lshl_b32 s80, s80, 15\n\t"
        "s_or_b32 s81, s44, %[snd]\n\t"
        "s_cmp_lt_i32 s81, 0\n\t"
        "s_cselect_b32 s83, s36, s80\n\t"
        "s_cbranch_scc1 .Lo27%=\n\t"
        "s_mov_b32 s97, 0\n\t"
        ".Lo25%=:\n\t"
        "v_xor_b32_e32 v186, %[tag], v161\n\t"
        "v_xor_b32_e32 v187, %[tag], v163\n\t"
        "v_xor_b32_e32 v188, %[tag], v165\n\t"
        "v_or3_b32 v186, v186, v187, v188\n\t"
        "v_xor_b32_e32 v187, %[tag], v167\n\t"
        "v_or_b32_e32 v186, v186, v187\n\t"
        "v_cmp_ne_u32_e32 vcc, 0, v186\n\t"
        "v_sub_f32_e32 v198, 1.0, v184\n\t"
        "v_add_f32_e32 v190, v144, v160\n\t"
        "v_add_f32_e32 v191, v145, v162\n\t"
        "v_add_f32_e32 v192, v146, v164\n\t"
        "v_add_f32_e32 v193, v147, v166\n\t"
        "s_cbranch_vccz .Lo26%=\n\t"
        "s_sleep %c[xslp]\n\t"
        "s_lshl_b32 s82, s83, 2\n\t"
        "buffer_load_dwordx4 v[160:163], v246, %[rd], s82 offen offset:0 sc1\n\t"
        "buffer_load_dwordx4 v[164:167], v246, %[rd], s82 offen offset:1024 sc1\n\t"
        "s_add_u32 s97, s97, 1\n\t"
        "s_cmp_lt_u32 s97, 0x200000\n\t"
        "s_cbranch_scc0 .Lgu%=\n\t"
        "s_waitcnt vmcnt(0)\n\t"
        "s_branch .Lo25%=\n\t"
        ".Lo26%=:\n\t"
        "v_cvt_pk_bf16_f32 v198, 0, v198\n\t"
        "v_cvt_pk_bf16_f32 v190, 0, v190\n\t"
        "v_cvt_pk_bf16_f32 v191, 0, v191\n\t"
        "v_cvt_pk_bf16_f32 v192, 0, v192\n\t"
        "v_cvt_pk_bf16_f32 v193, 0, v193\n\t"
        "v_lshlrev_b32_e32 v186, 16, v180\n\t"
        "v_lshlrev_b32_e32 v194, 16, v176\n\t"
        "v_and_b32_e32 v187, 0xffff0000, v180\n\t"
        "v_and_b32_e32 v195, 0xffff0000, v176\n\t"
        "v_lshlrev_b32_e32 v188, 16, v181\n\t"
        "v_lshlrev_b32_e32 v196, 16, v177\n\t"
        "v_and_b32_e32 v189, 0xffff0000, v181\n\t"
        "v_and_b32_e32 v197, 0xffff0000, v177\n\t"
        "v_mul_f32_e32 v186, v186, v194\n\t"
        "v_mul_f32_e32 v187, v187, v195\n\t"
        "v_mul_f32_e32 v188, v188, v196\n\t"
        "v_mul_f32_e32 v189, v189, v197\n\t"
        "v_mul_f32_e32 v190, v198, v190\n\t"
        "v_mul_f32_e32 v191, v198, v191\n\t"
        "v_mul_f32_e32 v192, v198, v192\n\t"
        "v_mul_f32_e32 v193, v198, v193\n\t"
        "v_cvt_pk_bf16_f32 v186, 0, v186\n\t"
        "v_cvt_pk_bf16_f32 v187, 0, v187\n\t"
        "v_cvt_pk_bf16_f32 v188, 0, v188\n\t"
        "v_cvt_pk_bf16_f32 v189, 0, v189\n\t"
        "v_cvt_pk_bf16_f32 v190, 0, v190\n\t"
        "v_cvt_pk_bf16_f32 v191, 0, v191\n\t"
        "v_cvt_pk_bf16_f32 v192, 0, v192\n\t"
        "v_cvt_pk_bf16_f32 v193, 0, v193\n\t"
        "v_mul_f32_e32 v186, v184, v186\n\t"
        "v_mul_f32_e32 v187, v184, v187\n\t"
        "v_mul_f32_e32 v188, v184, v188\n\t"
        "v_mul_f32_e32 v189, v184, v189\n\t"
        "v_cvt_pk_bf16_f32 v186, 0, v186\n\t"
        "v_cvt_pk_bf16_f32 v187, 0, v187\n\t"
        "v_cvt_pk_bf16_f32 v188, 0, v188\n\t"
        "v_cvt_pk_bf16_f32 v189, 0, v189\n\t"
        "v_add_f32_e32 v186, v186, v190\n\t"
        "v_add_f32_e32 v187, v187, v191\n\t"
        "v_add_f32_e32 v188, v188, v192\n\t"
        "v_add_f32_e32 v189, v189, v193\n\t"
        "v_cvt_pk_bf16_f32 v200, v186, v187\n\t"
        "v_cvt_pk_bf16_f32 v201, v188, v189\n\t"
        ".Lo27%=:\n\t"
        "buffer_store_dwordx2 v[200:201], v245, %[ro], s83 offen\n\t"
        "s_add_u32 s37, s37, 1\n\t"
        "s_add_u32 s44, s44, 4\n\t"
        "s_xor_b32 s43, s43, 0x8000\n\t"
        "s_cmp_lt_u32 s37, 32\n\t"
        "s_cbranch_scc1 .Lone%=\n\t"
        "buffer_store_dword v205, off, %[rd], %[ep] sc1\n\t"
        "s_endpgm\n\t"
        ".Lo13%=:\n\t"
        "s_mov_b32 s97, 0\n\t"
        "s_lshl_b32 s82, s83, 2\n\t"
        ".Lo13%=r:\n\t"
        "s_sleep %c[xslp]\n\t"
        "buffer_load_dwordx4 v[160:163], v246, %[rd], s82 offen offset:0 sc1\n\t"
        "buffer_load_dwordx4 v[164:167], v246, %[rd], s82 offen offset:1024 sc1\n\t"
        "s_add_u32 s97, s97, 1\n\t"
        "s_cmp_lt_u32 s97, 0x200000\n\t"
        "s_cbranch_scc0 .Lgu%=\n\t"
        "s_waitcnt vmcnt(0)\n\t"
        "v_xor_b32_e32 v224, %[tag], v161\n\t"
        "v_xor_b32_e32 v225, %[tag], v163\n\t"
        "v_xor_b32_e32 v226, %[tag], v165\n\t"
        "v_or3_b32 v224, v224, v225, v226\n\t"
        "v_xor_b32_e32 v225, %[tag], v167\n\t"
        "v_or_b32_e32 v224, v224, v225\n\t"
        "v_cmp_ne_u32_e32 vcc, 0, v224\n\t"
        "s_nop 3\n\t"
        "s_cbranch_vccnz .Lo13%=r\n\t"
        "s_branch .Lo14%=\n\t"
        ".Lo15%=:\n\t"
        "s_mov_b32 s97, 0\n\t"
        "s_lshl_b32 s82, s83, 2\n\t"
        ".Lo15%=r:\n\t"
        "s_sleep %c[xslp]\n\t"
        "buffer_load_dwordx4 v[168:171], v246, %[rd], s82 offen offset:0 sc1\n\t"
        "buffer_load_dwordx4 v[172:175], v246, %[rd], s82 offen offset:1024 sc1\n\t"
        "s_add_u32 s97, s97, 1\n\t"
        "s_cmp_lt_u32 s97, 0x200000\n\t"
        "s_cbranch_scc0 .Lgu%=\n\t"
        "s_waitcnt vmcnt(0)\n\t"
        "v_xor_b32_e32 v224, %[tag], v169\n\t"
        "v_xor_b32_e32 v225, %[tag], v171\n\t"
        "v_xor_b32_e32 v226, %[tag], v173\n\t"
        "v_or3_b32 v224, v224, v225, v226\n\t"
        "v_xor_b32_e32 v225, %[tag], v175\n\t"
        "v_or_b32_e32 v224, v224, v225\n\t"
        "v_cmp_ne_u32_e32 vcc, 0, v224\n\t"
        "s_nop 3\n\t"
        "s_cbranch_vccnz .Lo15%=r\n\t"
        "s_branch .Lo16%=\n\t"
        ".Lgu%=:\n\t"
        "v_mov_b32_e32 v0, 128\n\t"
        "s_mov_b32 s80, %c[ferr]\n\t"
        "buffer_store_dword v0, off, %[rd], s80 sc1\n\t"
        "s_endpgm"
        :: [rd] "s"(rd), [ro] "s"(ro), [rx] "s"(rx), [w] "s"(w), [rho] "s"(rho), [tag] "s"(tag), [own] "s"(own), [snd] "s"(snd), [ep] "s"(ep), [blk] "s"(j), [t0] "s"(g_t0),
          [ferr] "i"(F_ERR), [fprep] "i"(F_PREP), [fconv] "i"(F_CONV), [fprog] "i"(F_PROG), [dbg] "i"(W_DBG), [oslp] "i"(O_SLP), [xslp] "i"(X_SLP), [xdel] "i"(X_DEL), "{v[176:191]}"(pc)
        : "memory", "vcc", "scc", "s36", "s37", "s38", "s39", "s40", "s41", "s42", "s43", "s44", "s45", "s46", "s47", "s80", "s81", "s82", "s83", "s84", "s85", "s86", "s87", "s88", "s89", "s90", "s91", "s92", "s93", "s94", "s95", "s96", "s97", "s98", "s99", WO_REGS);
}

__device__ __forceinline__ void bulk_block(const Params& p, rsrc_t r, unsigned char* lds, const int wv, const int j, const unsigned tag, const unsigned g_t0) {
    const int x = (j & 7) >> 1, i32 = (j >> 3) * 2 + (j & 1);
    auto flags = [&](int mt, int nmt, int h0, int nh, int mat) {
        DRAIN(); __syncthreads();
        const int tid = otid(); if (tid < nmt * nh) st4(r, F_KVQ + ((mt + tid / nh) * 32 + h0 + tid % nh) * 64 + mat * 4, tag); };
#pragma unroll 1
    for (int t = 0; t < 2; ++t) {
        const int mt = 2 * t + (i32 & 1), mat = (i32 >> 1) & 1, hd = 8 * x + (i32 >> 2), rot = ((i32 >> 1) & 7) * 2;
        const bf16_t* Bm = mat ? p.Wv : p.Wk;
        if (mat) gemm<1, 4, 4, 8>(r, p.Aq, Bm, mt * 128, hd * 128, lds, W_VF, wv, rot, nullptr, t == 1, t == 0 ? Bm : nullptr, (mt + 2) * 128, hd * 128);
        else gemm<0, 4, 4, 8>(r, p.Aq, Bm, mt * 128, hd * 128, lds, W_KX, wv, rot, nullptr, t == 1, t == 0 ? Bm : nullptr, (mt + 2) * 128, hd * 128);
        flags(mt, 1, hd, 1, mat);
    }
    const int hp = 4 * x + (i32 >> 3), q8 = i32 & 7, rot = ROT2 * (i32 >> 2);
    if (SKEW) wait_long(rtnow() + (unsigned)((q8 & 3) * 4 + x) * SKEW);
    {
        const int m = q8 < 4 ? 4 + (q8 & 1) : q8 - 4, mat = q8 < 4 ? q8 >> 1 : 2;
        const bf16_t* Bn = mat == 2 ? p.Wq : mat == 1 ? p.Wv : p.Wk;
        const int mt = 4 + (q8 & 3), mv = q8 >> 2;
        const bf16_t* tk = (wv == 1 && j < 32) ? p.Aq + (64 + j) * 16384 : nullptr;
        if (mv) gemm<1, 4, 8, 4, 0, NoHook, 8>(r, p.Aq, p.Wv, mt * 128, hp * 256, lds, W_VF, wv, rot, tk, false, Bn, m * 256, hp * 256);
        else gemm<0, 4, 8, 4, 0, NoHook, 8>(r, p.Aq, p.Wk, mt * 128, hp * 256, lds, W_KX, wv, rot, tk, false, Bn, m * 256, hp * 256);
        flags(mt, 1, 2 * hp, 2, mv);
        const bf16_t* tl = (wv == 1 && j < 32) ? p.Aq + (96 + j) * 16384 : nullptr;
        if (mat == 1) gemm<1, 8, 8, 4>(r, p.Aq, Bn, m * 256, hp * 256, lds, W_VF, wv, rot, tl, true);
        else gemm<0, 8, 8, 4>(r, p.Aq, Bn, m * 256, hp * 256, lds, mat == 2 ? W_QX : W_KX, wv, rot, tl, true);
        flags(2 * m, 2, 2 * hp, 2, mat);
    }
    if (CVH == 4 && !conv_block(p, r, 128 + j, 1, lds, wv, j, tag, 0u)) return;
    {
        unsigned wi = 0u;
        const int l = otid() & 63; const v4u sw = mk_srd(p.Wo, 4096 * 1024 * 2); const unsigned wa = (unsigned)(((i32 & 1) * 2048 + wv * 512 + (l >> 3)) * 2048 + (i32 >> 1) * 128 + (l & 7) * 16);
        unsigned long long tab;
        asm volatile("s_getpc_b64 %0\n\ts_branch .Lwo%=\n\t"
                     "buffer_load_dwordx4 a[128:131], v191, s[92:95], s91 offen\n\ts_setpc_b64 s[96:97]\n\t" "buffer_load_dwordx4 a[132:135], v191, s[92:95], s91 offen\n\ts_setpc_b64 s[96:97]\n\t"
                     "buffer_load_dwordx4 a[136:139], v191, s[92:95], s91 offen\n\ts_setpc_b64 s[96:97]\n\t" "buffer_load_dwordx4 a[140:143], v191, s[92:95], s91 offen\n\ts_setpc_b64 s[96:97]\n\t"
                     "buffer_load_dwordx4 a[144:147], v191, s[92:95], s91 offen\n\ts_setpc_b64 s[96:97]\n\t" "buffer_load_dwordx4 a[148:151], v191, s[92:95], s91 offen\n\ts_setpc_b64 s[96:97]\n\t"
                     "buffer_load_dwordx4 a[152:155], v191, s[92:95], s91 offen\n\ts_setpc_b64 s[96:97]\n\t" "buffer_load_dwordx4 a[156:159], v191, s[92:95], s91 offen\n\ts_setpc_b64 s[96:97]\n\t"
                     "buffer_load_dwordx4 a[160:163], v191, s[92:95], s91 offen\n\ts_setpc_b64 s[96:97]\n\t" "buffer_load_dwordx4 a[164:167], v191, s[92:95], s91 offen\n\ts_setpc_b64 s[96:97]\n\t"
                     "buffer_load_dwordx4 a[168:171], v191, s[92:95], s91 offen\n\ts_setpc_b64 s[96:97]\n\t" "buffer_load_dwordx4 a[172:175], v191, s[92:95], s91 offen\n\ts_setpc_b64 s[96:97]\n\t"
                     "buffer_load_dwordx4 a[176:179], v191, s[92:95], s91 offen\n\ts_setpc_b64 s[96:97]\n\t" "buffer_load_dwordx4 a[180:183], v191, s[92:95], s91 offen\n\ts_setpc_b64 s[96:97]\n\t"
                     "buffer_load_dwordx4 a[184:187], v191, s[92:95], s91 offen\n\ts_setpc_b64 s[96:97]\n\t" "buffer_load_dwordx4 a[188:191], v191, s[92:95], s91 offen\n\ts_setpc_b64 s[96:97]\n\t"
                     "buffer_load_dwordx4 a[192:195], v191, s[92:95], s91 offen\n\ts_setpc_b64 s[96:97]\n\t" "buffer_load_dwordx4 a[196:199], v191, s[92:95], s91 offen\n\ts_setpc_b64 s[96:97]\n\t"
                     "buffer_load_dwordx4 a[200:203], v191, s[92:95], s91 offen\n\ts_setpc_b64 s[96:97]\n\t" "buffer_load_dwordx4 a[204:207], v191, s[92:95], s91 offen\n\ts_setpc_b64 s[96:97]\n\t"
                     "buffer_load_dwordx4 a[208:211], v191, s[92:95], s91 offen\n\ts_setpc_b64 s[96:97]\n\t" "buffer_load_dwordx4 a[212:215], v191, s[92:95], s91 offen\n\ts_setpc_b64 s[96:97]\n\t"
                     "buffer_load_dwordx4 a[216:219], v191, s[92:95], s91 offen\n\ts_setpc_b64 s[96:97]\n\t" "buffer_load_dwordx4 a[220:223], v191, s[92:95], s91 offen\n\ts_setpc_b64 s[96:97]\n\t"
                     "buffer_load_dwordx4 a[224:227], v191, s[92:95], s91 offen\n\ts_setpc_b64 s[96:97]\n\t" "buffer_load_dwordx4 a[228:231], v191, s[92:95], s91 offen\n\ts_setpc_b64 s[96:97]\n\t"
                     "buffer_load_dwordx4 a[232:235], v191, s[92:95], s91 offen\n\ts_setpc_b64 s[96:97]\n\t" "buffer_load_dwordx4 a[236:239], v191, s[92:95], s91 offen\n\ts_setpc_b64 s[96:97]\n\t"
                     "buffer_load_dwordx4 a[240:243], v191, s[92:95], s91 offen\n\ts_setpc_b64 s[96:97]\n\t" "buffer_load_dwordx4 a[244:247], v191, s[92:95], s91 offen\n\ts_setpc_b64 s[96:97]\n\t"
                     "buffer_load_dwordx4 a[248:251], v191, s[92:95], s91 offen\n\ts_setpc_b64 s[96:97]\n\t" "buffer_load_dwordx4 a[252:255], v191, s[92:95], s91 offen\n\ts_setpc_b64 s[96:97]\n\t"
                     "buffer_load_dwordx4 v[192:195], v191, s[92:95], s91 offen\n\ts_setpc_b64 s[96:97]\n\t" "buffer_load_dwordx4 v[196:199], v191, s[92:95], s91 offen\n\ts_setpc_b64 s[96:97]\n\t"
                     "buffer_load_dwordx4 v[200:203], v191, s[92:95], s91 offen\n\ts_setpc_b64 s[96:97]\n\t" "buffer_load_dwordx4 v[204:207], v191, s[92:95], s91 offen\n\ts_setpc_b64 s[96:97]\n\t"
                     "buffer_load_dwordx4 v[208:211], v191, s[92:95], s91 offen\n\ts_setpc_b64 s[96:97]\n\t" "buffer_load_dwordx4 v[212:215], v191, s[92:95], s91 offen\n\ts_setpc_b64 s[96:97]\n\t"
                     "buffer_load_dwordx4 v[216:219], v191, s[92:95], s91 offen\n\ts_setpc_b64 s[96:97]\n\t" "buffer_load_dwordx4 v[220:223], v191, s[92:95], s91 offen\n\ts_setpc_b64 s[96:97]\n\t"
                     "buffer_load_dwordx4 v[224:227], v191, s[92:95], s91 offen\n\ts_setpc_b64 s[96:97]\n\t" "buffer_load_dwordx4 v[228:231], v191, s[92:95], s91 offen\n\ts_setpc_b64 s[96:97]\n\t"
                     "buffer_load_dwordx4 v[232:235], v191, s[92:95], s91 offen\n\ts_setpc_b64 s[96:97]\n\t" "buffer_load_dwordx4 v[236:239], v191, s[92:95], s91 offen\n\ts_setpc_b64 s[96:97]\n\t"
                     "buffer_load_dwordx4 v[240:243], v191, s[92:95], s91 offen\n\ts_setpc_b64 s[96:97]\n\t" "buffer_load_dwordx4 v[244:247], v191, s[92:95], s91 offen\n\ts_setpc_b64 s[96:97]\n\t"
                     "buffer_load_dwordx4 v[248:251], v191, s[92:95], s91 offen\n\ts_setpc_b64 s[96:97]\n\t" "buffer_load_dwordx4 v[252:255], v191, s[92:95], s91 offen\n\ts_setpc_b64 s[96:97]\n\t"
                     "buffer_load_dwordx4 v191, s[92:95], s91 offen lds\n\ts_setpc_b64 s[96:97]\n.Lwo%=:" : "=s"(tab));
        auto hook = [&](int) {
            const unsigned q = min(wi, 63u);
            asm volatile("s_mov_b32 m0, %1\n\ts_swappc_b64 s[96:97], %0"
                         :: "s"(tab + (min(q, 48u) * 12u + 4u)), "s"(WO_L + wv * 16384 + (q - 48u) * 1024u), "{v191}"(wa), "{s[92:95]}"(sw), "{s91}"(q * 16384u) : "memory", "m0", "s96", "s97", WO_REGS);
            ++wi; };
#pragma unroll 1
        for (int t = 0; t < 2; ++t) {
            const int mt = t ? 8 + q8 : 12 + (q8 & 3), mv = t ? 2 : q8 >> 2;
            if (mv == 1) gemm<1, 4, 8, 4, 1>(r, p.Aq, p.Wv, mt * 128, hp * 256, lds, W_VF, wv, rot, nullptr, false, p.Wq, (8 + q8) * 128, hp * 256, hook);
            else gemm<0, 4, 8, 4, 1>(r, p.Aq, t ? p.Wq : p.Wk, mt * 128, hp * 256, lds, t ? W_QX : W_KX, wv, rot, nullptr, t == 1, t ? nullptr : p.Wq, (8 + q8) * 128, hp * 256, hook);
            flags(mt, 1, 2 * hp, 2, mv);
        }
        oproj_block(p, wv, i32, x, j, tag, g_t0);
    }
}

__device__ __forceinline__ void help_block(const Params& p, rsrc_t r, unsigned char* lds, const int wv, const int k, const unsigned tag, const unsigned far) {
    const int j = -32 + k; (void)j;
    {
        const unsigned th = rtnow() + HELP_D + (unsigned)k * SKEWP, OOB = 0xF0000000u;
        const int l = otid() & 63, n16 = l & 15;
        const rsrc_t rh = __builtin_amdgcn_make_buffer_rsrc(p.ws + hx_base(p.ws), 0, U_END * 16384, 0x00020000);
        const unsigned un = (unsigned)(wv * 32 + (k & 30)) * 16384u + (k & 1) * 4096u, ca = l == 0 ? un : OOB;
        v4u z[4]; unsigned best[4] = {~0u, ~0u, ~0u, ~0u};
#pragma unroll
        for (int i = 0; i < 4; ++i) z[i] = (v4u){~0u, ~0u, ~0u, ~0u};
        for (int i = 0; i < CAL_N; ++i)
#pragma unroll
            for (int q = 0; q < 4; ++q) {
                const unsigned t0 = (unsigned)__builtin_amdgcn_s_memtime(); __builtin_amdgcn_s_waitcnt(0);
                st16(rh, ca + q * 8192, z[0]); DRAIN();
                const unsigned t1 = (unsigned)__builtin_amdgcn_s_memtime(); __builtin_amdgcn_s_waitcnt(0);
                best[q] = min(best[q], t1 - t0); }
        const unsigned up = min(best[1], best[3]) < min(best[0], best[2]) ? 1u : 0u, vote = (up ^ far) | ((up ? best[2] < best[0] : best[3] < best[1]) ? 2u : 0u);
        if (wv == 0 && k < 8) {
#pragma unroll
            for (int i = 0; i < 4; ++i) z[i] = __builtin_elementwise_min(*(const v4u*)(p.mem0 + ((n16 & 1) + 2 * (n16 >> 2)) * 1024 + ((k & 3) * 4 + i) * 64 + (l >> 4) * 16 + (n16 & 2) * 4), (v4u){~1u, ~1u, ~1u, ~1u}); }
#pragma unroll
        for (int i = 0; i < 16; ++i) st16(rh, un + (i >> 2) * 8192 + (i & 3) * 1024 + l * 16, z[i & 3]);
        st4(r, (wv == 3 && l < 3) ? F_PROG + k * 128 + l * 8 : OOB, l ? 63u : 0u);
        DRAIN();
        st8(r, l == 0 ? F_CAL + k * 32 + wv * 8 : OOB, (v2u){tag, vote});
        wait_long(th);
    }
    prep_block(p, r, k, wv, tag);
    if (!conv_block(p, r, k * CVH, CVH, lds, wv, j, tag, (unsigned)k * SKEWH)) return;
    if (ATD) wait_long(rtnow() + ATD);
    attn_block(p, wv, k, tag);
}

__global__ void __launch_bounds__(256, 1) k_main(Params p) {
    extern __shared__ __attribute__((aligned(16))) unsigned char lds[];
    const unsigned g_t0 = DIAG ? rtnow() : 0u;
    rsrc_t r = __builtin_amdgcn_make_buffer_rsrc(p.ws, 0, W_TOTAL, 0x00020000);
    const int wv = __builtin_amdgcn_readfirstlane(threadIdx.x >> 6);
    int role = blockIdx.x;
    unsigned xcc; asm volatile("s_getreg_b32 %0, hwreg(HW_REG_XCC_ID)" : "=s"(xcc)); xcc &= 7u;
    unsigned old = 0u, xr = xcc, rk = blockIdx.x >> 3;
    if (MIXH) { if (xcc == 3u) { xr = 4u + (rk >> 3); rk = 24u + (rk & 7u); } else if (xcc > 3u && rk >= 24u) { xr = 3u; rk = (xcc - 4u) * 8u + (rk & 7u); } }
    unsigned slot = (xr >> 2) * 128u + (rk >> 1) * 8u + (xr & 3u) * 2u + (rk & 1u);
    const unsigned tag = (__builtin_amdgcn_readfirstlane(ld4(r, (threadIdx.x & 63) == 1 ? F_CLM + slot * 64 + 4 : F_EP)) * 2654435761u + 0x9E3779B9u) | 1u;
    if (threadIdx.x == 0) old = __hip_atomic_exchange((unsigned*)(p.ws + F_CLM + slot * 64), tag, __ATOMIC_RELAXED, __HIP_MEMORY_SCOPE_AGENT);
    if (threadIdx.x == 0) {
        for (int i = 0; i < 256 && old == tag; ++i) { slot = (slot + 1u) & 255u; old = __hip_atomic_exchange((unsigned*)(p.ws + F_CLM + slot * 64), tag, __ATOMIC_RELAXED, __HIP_MEMORY_SCOPE_AGENT); }
        *(volatile __attribute__((address_space(3))) int*)(size_t)(unsigned)(size_t)lds = (int)slot;
    }
    __syncthreads();
    role = __builtin_amdgcn_readfirstlane(*(volatile __attribute__((address_space(3))) int*)(size_t)(unsigned)(size_t)lds) & 255;
    __syncthreads();
    if (role >= 128) bulk_block(p, r, lds, wv, role - 128, tag, g_t0);
    else if ((role & 6) == 6) help_block(p, r, lds, wv, (role >> 3) * 2 + (role & 1), tag, xcc >> 2);
    else rec_block(p, wv, role, tag);
}

extern "C" void kernel_launch(void* const* d_in, const int* in_sizes, int n_in, void* d_out, int out_size, void* d_ws, size_t ws_size, hipStream_t stream) {
    Params p;
    p.Aq = (const bf16_t*)d_in[0]; p.Ak = (const bf16_t*)d_in[1]; p.Av = (const bf16_t*)d_in[2];
    p.shift_k = (const bf16_t*)d_in[3]; p.ssm_k = (const bf16_t*)d_in[4]; p.ssm_D = (const bf16_t*)d_in[5];
    p.mem0 = (const bf16_t*)d_in[7]; p.Wq = (const bf16_t*)d_in[8]; p.Wk = (const bf16_t*)d_in[9];
    p.Wv = (const bf16_t*)d_in[10]; p.Wo = (const bf16_t*)d_in[11]; p.gate_w = (const bf16_t*)d_in[13];
    p.out = (bf16_t*)d_out; p.ws = (unsigned char*)d_ws;
    (void)hipFuncSetAttribute((const void*)k_main, hipFuncAttributeMaxDynamicSharedMemorySize, L_TOTAL);
    k_main<<<256, 256, L_TOTAL, stream>>>(p);
}
```

```cpp
#include <hip/hip_runtime.h>
#include <stdint.h>
#include <type_traits>
typedef unsigned short bf16_t;
using bf16x8 = __attribute__((ext_vector_type(8))) short;
using f32x4 = __attribute__((ext_vector_type(4))) float;
typedef unsigned v4u __attribute__((ext_vector_type(4)));
typedef unsigned v2u __attribute__((ext_vector_type(2)));
typedef __amdgpu_buffer_rsrc_t rsrc_t;

typedef __bf16 hbf2 __attribute__((ext_vector_type(2)));
typedef float f32x2 __attribute__((ext_vector_type(2)));
__device__ __forceinline__ unsigned pack2(float a, float b) { f32x2 v = {a, b}; return __builtin_bit_cast(unsigned, __builtin_convertvector(v, hbf2)); }
__device__ __forceinline__ float rbfh(float f) { return __uint_as_float(pack2(0.f, f)); }
__device__ __forceinline__ f32x2 rbf2(f32x2 f) { return (f32x2){rbfh(f.x), rbfh(f.y)}; }
__device__ __forceinline__ unsigned nosent(unsigned x) {
    typedef unsigned short us2 __attribute__((ext_vector_type(2)));
    const us2 a = __builtin_bit_cast(us2, x), c = {(unsigned short)0xFFFE, (unsigned short)0xFFFE}; return __builtin_bit_cast(unsigned, __builtin_elementwise_min(a, c)); }

constexpr int S = 2048, D = 1024, H = 32, DH = 128, M = 8, SEG = 32, NSEG = 64;

constexpr unsigned W_FLG = 0;
constexpr unsigned F_KVQ = W_FLG;
constexpr unsigned F_EP = W_FLG + 32768;
constexpr unsigned F_ERR = W_FLG + 32768 + 256;
constexpr unsigned F_PREP = W_FLG + 32768 + 512;
constexpr unsigned F_CAL = W_FLG + 73728;
constexpr unsigned F_CLM = W_FLG + 77824;
constexpr unsigned F_CONV = W_FLG + 33792;
constexpr unsigned F_PROG = W_FLG + 36864;
constexpr unsigned F_ATT = W_FLG + 40960;
constexpr unsigned F_RDY = W_FLG + 49152;
constexpr unsigned FLG_BYTES = 94208;
constexpr unsigned HO_KVM = W_FLG + FLG_BYTES;
constexpr unsigned W_GATE = HO_KVM + 64 * 32 * 4096;
constexpr unsigned W_QX = W_GATE + 8192;
constexpr unsigned W_KX = W_QX + S * 4096 * 2;
constexpr unsigned W_VF = W_KX + S * 4096 * 2;
constexpr unsigned DBG_BYTES = 0;
constexpr unsigned W_DBG = W_VF + 64 * 32 * 8192;
constexpr unsigned W_U = W_DBG + DBG_BYTES;
constexpr unsigned W_TOTAL = W_U + S * D * 2;

__device__ __forceinline__ v4u ld16(rsrc_t r, unsigned off) { return __builtin_amdgcn_raw_buffer_load_b128(r, off, 0, 16); }
__device__ __forceinline__ v2u ld8(rsrc_t r, unsigned off) { return __builtin_amdgcn_raw_buffer_load_b64(r, off, 0, 16); }
__device__ __forceinline__ unsigned ld4(rsrc_t r, unsigned off) { return __builtin_amdgcn_raw_buffer_load_b32(r, off, 0, 16); }
__device__ __forceinline__ void st16(rsrc_t r, unsigned off, v4u v) { __builtin_amdgcn_raw_buffer_store_b128(v, r, off, 0, 16); }
__device__ __forceinline__ void st8(rsrc_t r, unsigned off, v2u v) { __builtin_amdgcn_raw_buffer_store_b64(v, r, off, 0, 16); }
__device__ __forceinline__ void st4(rsrc_t r, unsigned off, unsigned v) { __builtin_amdgcn_raw_buffer_store_b32(v, r, off, 0, 16); }
__device__ __forceinline__ bf16x8 as_frag(v4u v) { return __builtin_bit_cast(bf16x8, v); }
#define D1 128
#define D2 82
#define D3 64
#define LB0 16
#define LC0 7
#define LA0 8
#define TR_UP 4
#define TR_DN 2
#define L_DEC 15
#define STAG 1
#define UP_SH 2
#define EARLY_FROM 58
#define OUT_WARM 0
#define AROT 0
#define P_SLP 2
#define O_SLP 2
#define W_NT 0
#define TOL 2
#define A_SLP 0
#define PEN2 20
#define A_UP 6
#define CT0 2
#define CPF 1
#define PH_M 3
#define SKEW 20
#define SKEW1 0
#define XT 1
#define TRK 3
#define WO_NT 0
#define WO_TRK 1
#define WIN 3
#define ROT0 3
#define ROT1 2
#define ROT2 2
#define B_NT 0
#define CALM 15
#define R4M 1
#define HX_SH 0u
#define FINE_T 1
#define FINE_N 0
#define FINE_A 0
#define O_ND 12
#define PEN0 32
#define X0C 24
#define X0A 20
#define X1A 8
#define PEN 40
#define MEMBAR() asm volatile("" ::: "memory")
#define ENDPGM() asm volatile("s_endpgm" ::: "memory")
#define DRAIN() asm volatile("s_waitcnt vmcnt(0)" ::: "memory")

__device__ __forceinline__ float xor16_max(float x) { auto r = __builtin_amdgcn_permlane16_swap(__float_as_uint(x), __float_as_uint(x), false, false); return fmaxf(__uint_as_float(r[0]), __uint_as_float(r[1])); }
__device__ __forceinline__ float xor32_max(float x) { auto r = __builtin_amdgcn_permlane32_swap(__float_as_uint(x), __float_as_uint(x), false, false); return fmaxf(__uint_as_float(r[0]), __uint_as_float(r[1])); }
__device__ __forceinline__ float xor16_add(float x) { auto r = __builtin_amdgcn_permlane16_swap(__float_as_uint(x), __float_as_uint(x), false, false); return __uint_as_float(r[0]) + __uint_as_float(r[1]); }
__device__ __forceinline__ float xor32_add(float x) { auto r = __builtin_amdgcn_permlane32_swap(__float_as_uint(x), __float_as_uint(x), false, false); return __uint_as_float(r[0]) + __uint_as_float(r[1]); }

typedef short v4i16_t __attribute__((ext_vector_type(4)));
__device__ __forceinline__ v2u trb(unsigned lds_addr) { return __builtin_bit_cast(v2u, __builtin_amdgcn_ds_read_tr16_b64_v4i16((__attribute__((address_space(3))) v4i16_t*)(size_t)lds_addr)); }
__device__ __forceinline__ int otid_(int wv) { int t; asm volatile("v_mbcnt_lo_u32_b32 %0, -1, 0\n\tv_mbcnt_hi_u32_b32 %0, -1, %0" : "=v"(t)); return wv * 64 + t; }
#define otid() otid_(wv)
__device__ __forceinline__ bool wait_word(rsrc_t r, unsigned off, unsigned want, unsigned code) {
    for (unsigned spin = 0;; ++spin) { MEMBAR();
        const unsigned v = __builtin_amdgcn_readfirstlane(ld4(r, off));
        if (v >= want) return true;
        if (spin > (1u << 20)) { st4(r, F_ERR, code); return false; }
        __builtin_amdgcn_s_sleep(8); }
}

__device__ __forceinline__ bool wait_prep(rsrc_t r, int l, unsigned tag) {
    for (unsigned spin = 0;; ++spin) { MEMBAR();
        const unsigned v = ld4(r, F_PREP + (l & 31) * 4);
        if (__all(v == tag)) return true;
        if (spin > (1u << 20)) { st4(r, F_ERR, 512u); return false; }
        __builtin_amdgcn_s_sleep(8); }
}
__device__ __forceinline__ unsigned rtnow() { return (unsigned)__builtin_amdgcn_s_memrealtime(); }
__device__ __forceinline__ void wait_long(unsigned target) { for (int i = 0; i < 4096; ++i) { if ((int)(rtnow() - target) >= 0) break; __builtin_amdgcn_s_sleep(8); } }
constexpr int L_TAB = 20480;
constexpr int L_QK = 24576;
__device__ __forceinline__ unsigned img(int f) { return (unsigned)(L_QK + f * 16 + (f >> 4) * 128); }
constexpr int L_FL = 30720, L_Z = L_FL + 128, L_LOG = 31744, L_W = 32768, L_V = 131072;
constexpr int L_TOTAL = 163840;
constexpr unsigned U_END = 96;
#define PMIN 82
#define V_SLP 2
#define R_SLP 0
constexpr unsigned WO_L = 98304;
#define HELP_D 200
#define V_DEL 24
#define XR 8
#define PFW 0
#define KEYM 3
#define DIAG 0
#define NEARX 0
#define MIXH 1
#define SKEWH 10
#define SKEWP 8
#define NEARY 0
#define CS1 0x8000u
#define CS2 0x10000u
#define X_SLP 2
#define X_DEL 6
#define FASTP 0
#define CV1 3
#define AT1 7
#define CVH 8
#define DN_SH 1
#define EPI_SLP 0
#define AT_SLP 18
#define VA_SLP 0
#define C_SLP 0
#define ATD 0
#define S0B 40
#define BT_SLP 0
#define CAL_O 0u
#define H_SLP 0
#define CAL_N 3
#define STEP_MIN 180u

struct Params {
    const bf16_t *Aq, *Ak, *Av, *shift_k, *ssm_k, *ssm_D, *mem0, *Wq, *Wk, *Wv, *Wo, *gate_w;
    bf16_t* out; unsigned char* ws;
};
__host__ __device__ __forceinline__ unsigned hx_base(const void* ws) { const size_t a = (size_t)ws; return (unsigned)((((a + W_TOTAL + 0x7FFFFFu) >> 23) << 23) - a) + HX_SH; }

typedef short v4s __attribute__((ext_vector_type(4)));
typedef unsigned v32u __attribute__((ext_vector_type(32)));
typedef unsigned v16u __attribute__((ext_vector_type(16)));
typedef unsigned v8u __attribute__((ext_vector_type(8)));
typedef unsigned v12u __attribute__((ext_vector_type(12)));
__device__ __forceinline__ v4u mk_srd(const void* p, unsigned bytes) { const size_t a = (size_t)p; v4u d;
    d.x = __builtin_amdgcn_readfirstlane((unsigned)a); d.y = __builtin_amdgcn_readfirstlane((unsigned)(a >> 32) & 0xFFFFu); d.z = __builtin_amdgcn_readfirstlane(bytes); d.w = 0x00020000u; return d; }
__device__ __forceinline__ bf16x8 ldsa(unsigned addr) { return *(const __attribute__((address_space(3))) bf16x8*)(size_t)addr; }
__device__ __forceinline__ void rec_block(const Params& p, const int wv, const int b, const unsigned tag) {
    const int w = wv, tid = otid(), l = tid & 63, n16 = l & 15, g = l >> 4, c = n16 >> 2;
    const int x = (b & 7) >> 1, i32 = (b >> 3) * 2 + (b & 1);
    const unsigned OOB = 0xF0000000u;
    const v4u rhd = mk_srd(p.ws + hx_base(p.ws), U_END * 16384), wsd = mk_srd(p.ws, W_TOTAL);
    const int G = x - 1, vi = G * 32 + i32, h = x == 0 ? i32 : vi >> 1;
    const unsigned pr_a = F_PROG + h * 128 + (x == 0 ? 0 : 8 + (vi & 1) * 8), cal = F_CAL - w * 1024, kvo = HO_KVM + h * 4096 + (x ? 2048 : 0);
    if (CPF) {
        unsigned lo, hi, l2; asm volatile("s_getpc_b64 s[98:99]\n.Lpc%=:\n\ts_add_u32 %2, s98, rec_v_code-.Lpc%=\n\ts_add_u32 %0, s98, rec_s_code-.Lpc%=\n\ts_addc_u32 %1, s99, 0" : "=s"(lo), "=s"(hi), "=s"(l2) :: "s98", "s99", "scc");
        const size_t a = (((size_t)hi << 32) | lo) + (size_t)(long long)((x ? (int)(l2 - lo) : 0) + ((i32 * 4 + w) % 12) * 1024 - 4096 + l * 16);
        asm volatile("s_mov_b32 m0, %1\n\ts_nop 0\n\tglobal_load_lds_dwordx4 %0, off" :: "v"(a & ~(size_t)15), "s"(x ? L_V + 16384 : 0) : "memory", "m0");
    }
    typedef __attribute__((address_space(3))) volatile unsigned lu32;
    auto cpy = [](int k) -> unsigned { return k == 1 ? 0u : k * 32768u + 16384u; };
    auto cun = [](int u) -> unsigned { return ((u & 1) ? CS1 : 0u) ^ ((u & 2) ? CS2 : 0u); };
    unsigned nA = cpy(x), nW = cpy(w), nT = 64 * 16384 + (x ? 0 : h * 8), nS = 64 * 16384 + x * 1024, nC = 32 * 16384 + (x == 2 ? 16384 : 0), b0 = 65 * 16384 + (h >> 2) * 32768 + (h & 3) * 2048;
    unsigned pob = w == 0 ? nA + (i32 >> 4) * 32768 + (i32 & 15) * 512 : (w == 1 && x) ? (nC ^ cun(i32 >> 3)) + (i32 & 7) * 1024 : (w == 2 && i32 == 0) ? nS : OOB;
    if (x == 0) {
        const v4u sq = mk_srd(p.Wq, 1024 * 4096 * 2), sk = mk_srd(p.Wk, 1024 * 4096 * 2);
        const unsigned m0b = L_W + w * 1024, f = (c & 1) | ((g & 1) << 1) | ((c >> 1) << 2);
        const unsigned t0 = L_W + (16 * (g >> 1) + 4 * (g & 1) + (c & 1) + 8 * (c >> 1)) * 256 + (((2 * w) ^ f) << 5) + (n16 & 3) * 8;
        const unsigned dv = (4 * w + g) * 8192 + h * 256 + ((n16 ^ (((g & 1) | (w << 1)) << 1)) << 4);
        v8u pa;
#pragma unroll
        for (int q = 0; q < 8; ++q) pa[q] = (t0 + ((w + (q >> 1)) & 3) * 32768) ^ ((q & 1) << 5);
        const unsigned kx = W_KX + h * 8192, vx = W_VF + h * 8192 + w * 1024, kf = F_KVQ + h * 64;
        const unsigned aq = img(8 * g + (n16 >> 2)) + (n16 & 1) * 8;
        const v8u pc = {(unsigned)tid * 16, (unsigned)(L_W + w * 32768 + l * 16), (n16 & 2) ? 0xEEEEu : 0x4444u, aq,
                  (unsigned)(((w + 1) & 3) * 1024 + l * 16), (unsigned)(((w + 2) & 3) * 1024 + l * 16), (unsigned)(((w + 3) & 3) * 1024 + l * 16), img(32 * w + n16) + g * 4};
        const v2u pd = {(n16 & 2) ? (unsigned)L_Z : aq + w * 768, (n16 & 2) ? aq + w * 768 : (unsigned)L_Z};
        *(__attribute__((address_space(3))) v4u*)(size_t)(L_TAB + tid * 16) = (v4u){(unsigned)(L_QK + 3072 + g * 384 + n16 * 8),
                  n16 < 8 ? (unsigned)((((n16 >> 2) * 16384) ^ cun((h >> 1) & 3)) + ((h & 1) * 4 + (h >> 3)) * 1024 + w * 256 + (g >> 1) * 128 + (n16 & 2) * 32 + (g & 1) * 32 + (n16 & 1) * 16) : OOB,
                  (unsigned)((32 * w + 16 * (g & 1) + n16) * 16 + (g >> 1) * 8), (unsigned)(L_QK + 3072 + w * 768 + ((n16 & 1) + 2 * (n16 >> 2)) * 32 + g * 8 + (n16 & 2) * 2)};
        if (tid < 64) ((lu32*)(size_t)L_FL)[tid] = 0u;
    asm volatile(
        "s_add_u32 s80, %[m0b], 0\n\t"
        "s_mov_b32 s81, 0\n\t"
        "s_mov_b32 s82, 4\n\t"
        ".Ld0%=:\n\t"
        "s_mov_b32 m0, s80\n\t"
        "s_add_u32 s80, s80, 0x8000\n\t"
        "buffer_load_dwordx4 v96, %[sq], s81 offen lds\n\t"
        "s_add_u32 m0, m0, 0x1000\n\t"
        "s_add_u32 s81, s81, 0x20000\n\t"
        "buffer_load_dwordx4 v96, %[sq], s81 offen lds\n\t"
        "s_add_u32 m0, m0, 0x1000\n\t"
        "s_add_u32 s81, s81, 0x20000\n\t"
        "buffer_load_dwordx4 v96, %[sq], s81 offen lds\n\t"
        "s_add_u32 m0, m0, 0x1000\n\t"
        "s_add_u32 s81, s81, 0x20000\n\t"
        "buffer_load_dwordx4 v96, %[sq], s81 offen lds\n\t"
        "s_add_u32 m0, m0, 0x1000\n\t"
        "s_add_u32 s81, s81, 0x20000\n\t"
        "s_sub_u32 s82, s82, 1\n\t"
        "s_cmp_lg_u32 s82, 0\n\t"
        "s_cbranch_scc1 .Ld0%=\n\t"
        "s_add_u32 s80, %[m0b], 16384\n\t"
        "s_mov_b32 s81, 0\n\t"
        "s_mov_b32 s82, 4\n\t"
        ".Ld1%=:\n\t"
        "s_mov_b32 m0, s80\n\t"
        "s_add_u32 s80, s80, 0x8000\n\t"
        "buffer_load_dwordx4 v96, %[sk], s81 offen lds\n\t"
        "s_add_u32 m0, m0, 0x1000\n\t"
        "s_add_u32 s81, s81, 0x20000\n\t"
        "buffer_load_dwordx4 v96, %[sk], s81 offen lds\n\t"
        "s_add_u32 m0, m0, 0x1000\n\t"
        "s_add_u32 s81, s81, 0x20000\n\t"
        "buffer_load_dwordx4 v96, %[sk], s81 offen lds\n\t"
        "s_add_u32 m0, m0, 0x1000\n\t"
        "s_add_u32 s81, s81, 0x20000\n\t"
        "buffer_load_dwordx4 v96, %[sk], s81 offen lds\n\t"
        "s_add_u32 m0, m0, 0x1000\n\t"
        "s_add_u32 s81, s81, 0x20000\n\t"
        "s_sub_u32 s82, s82, 1\n\t"
        "s_cmp_lg_u32 s82, 0\n\t"
        "s_cbranch_scc1 .Ld1%=\n\t"
        "s_waitcnt vmcnt(16)\n\t"
        "s_barrier\n\t"
        "ds_read_b64_tr_b16 v[0:1], v98 offset:0\n\t"
        "ds_read_b64_tr_b16 v[2:3], v98 offset:512\n\t"
        "ds_read_b64_tr_b16 v[4:5], v98 offset:8192\n\t"
        "ds_read_b64_tr_b16 v[6:7], v98 offset:8704\n\t"
        "ds_read_b64_tr_b16 v[8:9], v99 offset:0\n\t"
        "ds_read_b64_tr_b16 v[10:11], v99 offset:512\n\t"
        "ds_read_b64_tr_b16 v[12:13], v99 offset:8192\n\t"
        "ds_read_b64_tr_b16 v[14:15], v99 offset:8704\n\t"
        "ds_read_b64_tr_b16 a[16:17], v100 offset:0\n\t"
        "ds_read_b64_tr_b16 a[18:19], v100 offset:512\n\t"
        "ds_read_b64_tr_b16 a[20:21], v100 offset:8192\n\t"
        "ds_read_b64_tr_b16 a[22:23], v100 offset:8704\n\t"
        "ds_read_b64_tr_b16 a[24:25], v101 offset:0\n\t"
        "ds_read_b64_tr_b16 a[26:27], v101 offset:512\n\t"
        "ds_read_b64_tr_b16 a[28:29], v101 offset:8192\n\t"
        "ds_read_b64_tr_b16 a[30:31], v101 offset:8704\n\t"
        "ds_read_b64_tr_b16 a[32:33], v102 offset:0\n\t"
        "ds_read_b64_tr_b16 a[34:35], v102 offset:512\n\t"
        "ds_read_b64_tr_b16 a[36:37], v102 offset:8192\n\t"
        "ds_read_b64_tr_b16 a[38:39], v102 offset:8704\n\t"
        "ds_read_b64_tr_b16 a[40:41], v103 offset:0\n\t"
        "ds_read_b64_tr_b16 a[42:43], v103 offset:512\n\t"
        "ds_read_b64_tr_b16 a[44:45], v103 offset:8192\n\t"
        "ds_read_b64_tr_b16 a[46:47], v103 offset:8704\n\t"
        "ds_read_b64_tr_b16 a[48:49], v104 offset:0\n\t"
        "ds_read_b64_tr_b16 a[50:51], v104 offset:512\n\t"
        "ds_read_b64_tr_b16 a[52:53], v104 offset:8192\n\t"
        "ds_read_b64_tr_b16 a[54:55], v104 offset:8704\n\t"
        "ds_read_b64_tr_b16 a[56:57], v105 offset:0\n\t"
        "ds_read_b64_tr_b16 a[58:59], v105 offset:512\n\t"
        "ds_read_b64_tr_b16 a[60:61], v105 offset:8192\n\t"
        "ds_read_b64_tr_b16 a[62:63], v105 offset:8704\n\t"
        "s_waitcnt lgkmcnt(0)\n\t"
        "s_barrier\n\t"
        "s_add_u32 s80, %[m0b], 0\n\t"
        "s_mov_b32 s81, 2097152\n\t"
        "s_mov_b32 s82, 4\n\t"
        ".Ld2%=:\n\t"
        "s_mov_b32 m0, s80\n\t"
        "s_add_u32 s80, s80, 0x8000\n\t"
        "buffer_load_dwordx4 v96, %[sq], s81 offen lds\n\t"
        "s_add_u32 m0, m0, 0x1000\n\t"
        "s_add_u32 s81, s81, 0x20000\n\t"
        "buffer_load_dwordx4 v96, %[sq], s81 offen lds\n\t"
        "s_add_u32 m0, m0, 0x1000\n\t"
        "s_add_u32 s81, s81, 0x20000\n\t"
        "buffer_load_dwordx4 v96, %[sq], s81 offen lds\n\t"
        "s_add_u32 m0, m0, 0x1000\n\t"
        "s_add_u32 s81, s81, 0x20000\n\t"
        "buffer_load_dwordx4 v96, %[sq], s81 offen lds\n\t"
        "s_add_u32 m0, m0, 0x1000\n\t"
        "s_add_u32 s81, s81, 0x20000\n\t"
        "s_sub_u32 s82, s82, 1\n\t"
        "s_cmp_lg_u32 s82, 0\n\t"
        "s_cbranch_scc1 .Ld2%=\n\t"
        "s_waitcnt vmcnt(16)\n\t"
        "s_barrier\n\t"
        "ds_read_b64_tr_b16 v[16:17], v98 offset:16384\n\t"
        "ds_read_b64_tr_b16 v[18:19], v98 offset:16896\n\t"
        "ds_read_b64_tr_b16 v[20:21], v98 offset:24576\n\t"
        "ds_read_b64_tr_b16 v[22:23], v98 offset:25088\n\t"
        "ds_read_b64_tr_b16 v[24:25], v99 offset:16384\n\t"
        "ds_read_b64_tr_b16 v[26:27], v99 offset:16896\n\t"
        "ds_read_b64_tr_b16 v[28:29], v99 offset:24576\n\t"
        "ds_read_b64_tr_b16 v[30:31], v99 offset:25088\n\t"
        "ds_read_b64_tr_b16 a[80:81], v100 offset:16384\n\t"
        "ds_read_b64_tr_b16 a[82:83], v100 offset:16896\n\t"
        "ds_read_b64_tr_b16 a[84:85], v100 offset:24576\n\t"
        "ds_read_b64_tr_b16 a[86:87], v100 offset:25088\n\t"
        "ds_read_b64_tr_b16 a[88:89], v101 offset:16384\n\t"
        "ds_read_b64_tr_b16 a[90:91], v101 offset:16896\n\t"
        "ds_read_b64_tr_b16 a[92:93], v101 offset:24576\n\t"
        "ds_read_b64_tr_b16 a[94:95], v101 offset:25088\n\t"
        "ds_read_b64_tr_b16 a[96:97], v102 offset:16384\n\t"
        "ds_read_b64_tr_b16 a[98:99], v102 offset:16896\n\t"
        "ds_read_b64_tr_b16 a[100:101], v102 offset:24576\n\t"
        "ds_read_b64_tr_b16 a[102:103], v102 offset:25088\n\t"
        "ds_read_b64_tr_b16 a[104:105], v103 offset:16384\n\t"
        "ds_read_b64_tr_b16 a[106:107], v103 offset:16896\n\t"
        "ds_read_b64_tr_b16 a[108:109], v103 offset:24576\n\t"
        "ds_read_b64_tr_b16 a[110:111], v103 offset:25088\n\t"
        "ds_read_b64_tr_b16 a[112:113], v104 offset:16384\n\t"
        "ds_read_b64_tr_b16 a[114:115], v104 offset:16896\n\t"
        "ds_read_b64_tr_b16 a[116:117], v104 offset:24576\n\t"
        "ds_read_b64_tr_b16 a[118:119], v104 offset:25088\n\t"
        "ds_read_b64_tr_b16 a[120:121], v105 offset:16384\n\t"
        "ds_read_b64_tr_b16 a[122:123], v105 offset:16896\n\t"
        "ds_read_b64_tr_b16 a[124:125], v105 offset:24576\n\t"
        "ds_read_b64_tr_b16 a[126:127], v105 offset:25088\n\t"
        "s_waitcnt lgkmcnt(0)\n\t"
        "s_barrier\n\t"
        "s_add_u32 s80, %[m0b], 16384\n\t"
        "s_mov_b32 s81, 2097152\n\t"
        "s_mov_b32 s82, 4\n\t"
        ".Ld3%=:\n\t"
        "s_mov_b32 m0, s80\n\t"
        "s_add_u32 s80, s80, 0x8000\n\t"
        "buffer_load_dwordx4 v96, %[sk], s81 offen lds\n\t"
        "s_add_u32 m0, m0, 0x1000\n\t"
        "s_add_u32 s81, s81, 0x20000\n\t"
        "buffer_load_dwordx4 v96, %[sk], s81 offen lds\n\t"
        "s_add_u32 m0, m0, 0x1000\n\t"
        "s_add_u32 s81, s81, 0x20000\n\t"
        "buffer_load_dwordx4 v96, %[sk], s81 offen lds\n\t"
        "s_add_u32 m0, m0, 0x1000\n\t"
        "s_add_u32 s81, s81, 0x20000\n\t"
        "buffer_load_dwordx4 v96, %[sk], s81 offen lds\n\t"
        "s_add_u32 m0, m0, 0x1000\n\t"
        "s_add_u32 s81, s81, 0x20000\n\t"
        "s_sub_u32 s82, s82, 1\n\t"
        "s_cmp_lg_u32 s82, 0\n\t"
        "s_cbranch_scc1 .Ld3%=\n\t"
        "s_waitcnt vmcnt(16)\n\t"
        "s_barrier\n\t"
        "ds_read_b64_tr_b16 v[32:33], v98 offset:0\n\t"
        "ds_read_b64_tr_b16 v[34:35], v98 offset:512\n\t"
        "ds_read_b64_tr_b16 v[36:37], v98 offset:8192\n\t"
        "ds_read_b64_tr_b16 v[38:39], v98 offset:8704\n\t"
        "ds_read_b64_tr_b16 v[40:41], v99 offset:0\n\t"
        "ds_read_b64_tr_b16 v[42:43], v99 offset:512\n\t"
        "ds_read_b64_tr_b16 v[44:45], v99 offset:8192\n\t"
        "ds_read_b64_tr_b16 v[46:47], v99 offset:8704\n\t"
        "ds_read_b64_tr_b16 a[144:145], v100 offset:0\n\t"
        "ds_read_b64_tr_b16 a[146:147], v100 offset:512\n\t"
        "ds_read_b64_tr_b16 a[148:149], v100 offset:8192\n\t"
        "ds_read_b64_tr_b16 a[150:151], v100 offset:8704\n\t"
        "ds_read_b64_tr_b16 a[152:153], v101 offset:0\n\t"
        "ds_read_b64_tr_b16 a[154:155], v101 offset:512\n\t"
        "ds_read_b64_tr_b16 a[156:157], v101 offset:8192\n\t"
        "ds_read_b64_tr_b16 a[158:159], v101 offset:8704\n\t"
        "ds_read_b64_tr_b16 a[160:161], v102 offset:0\n\t"
        "ds_read_b64_tr_b16 a[162:163], v102 offset:512\n\t"
        "ds_read_b64_tr_b16 a[164:165], v102 offset:8192\n\t"
        "ds_read_b64_tr_b16 a[166:167], v102 offset:8704\n\t"
        "ds_read_b64_tr_b16 a[168:169], v103 offset:0\n\t"
        "ds_read_b64_tr_b16 a[170:171], v103 offset:512\n\t"
        "ds_read_b64_tr_b16 a[172:173], v103 offset:8192\n\t"
        "ds_read_b64_tr_b16 a[174:175], v103 offset:8704\n\t"
        "ds_read_b64_tr_b16 a[176:177], v104 offset:0\n\t"
        "ds_read_b64_tr_b16 a[178:179], v104 offset:512\n\t"
        "ds_read_b64_tr_b16 a[180:181], v104 offset:8192\n\t"
        "ds_read_b64_tr_b16 a[182:183], v104 offset:8704\n\t"
        "ds_read_b64_tr_b16 a[184:185], v105 offset:0\n\t"
        "ds_read_b64_tr_b16 a[186:187], v105 offset:512\n\t"
        "ds_read_b64_tr_b16 a[188:189], v105 offset:8192\n\t"
        "ds_read_b64_tr_b16 a[190:191], v105 offset:8704\n\t"
        "s_waitcnt lgkmcnt(0)\n\t"
        "s_barrier\n\t"
        "s_add_u32 s80, %[m0b], 0\n\t"
        "s_mov_b32 s81, 4194304\n\t"
        "s_mov_b32 s82, 4\n\t"
        ".Ld4%=:\n\t"
        "s_mov_b32 m0, s80\n\t"
        "s_add_u32 s80, s80, 0x8000\n\t"
        "buffer_load_dwordx4 v96, %[sq], s81 offen lds\n\t"
        "s_add_u32 m0, m0, 0x1000\n\t"
        "s_add_u32 s81, s81, 0x20000\n\t"
        "buffer_load_dwordx4 v96, %[sq], s81 offen lds\n\t"
        "s_add_u32 m0, m0, 0x1000\n\t"
        "s_add_u32 s81, s81, 0x20000\n\t"
        "buffer_load_dwordx4 v96, %[sq], s81 offen lds\n\t"
        "s_add_u32 m0, m0, 0x1000\n\t"
        "s_add_u32 s81, s81, 0x20000\n\t"
        "buffer_load_dwordx4 v96, %[sq], s81 offen lds\n\t"
        "s_add_u32 m0, m0, 0x1000\n\t"
        "s_add_u32 s81, s81, 0x20000\n\t"
        "s_sub_u32 s82, s82, 1\n\t"
        "s_cmp_lg_u32 s82, 0\n\t"
        "s_cbranch_scc1 .Ld4%=\n\t"
        "s_mov_b64 exec, 0\n\t"
        "s_branch .Lhk0%=\n\t"
        ".Lhr%=:\n\t"
        "s_mov_b64 exec, -1\n\t"
        "s_waitcnt vmcnt(16)\n\t"
        "s_barrier\n\t"
        "ds_read_b64_tr_b16 v[48:49], v98 offset:16384\n\t"
        "ds_read_b64_tr_b16 v[50:51], v98 offset:16896\n\t"
        "ds_read_b64_tr_b16 v[52:53], v98 offset:24576\n\t"
        "ds_read_b64_tr_b16 v[54:55], v98 offset:25088\n\t"
        "ds_read_b64_tr_b16 v[56:57], v99 offset:16384\n\t"
        "ds_read_b64_tr_b16 v[58:59], v99 offset:16896\n\t"
        "ds_read_b64_tr_b16 v[60:61], v99 offset:24576\n\t"
        "ds_read_b64_tr_b16 v[62:63], v99 offset:25088\n\t"
        "ds_read_b64_tr_b16 a[208:209], v100 offset:16384\n\t"
        "ds_read_b64_tr_b16 a[210:211], v100 offset:16896\n\t"
        "ds_read_b64_tr_b16 a[212:213], v100 offset:24576\n\t"
        "ds_read_b64_tr_b16 a[214:215], v100 offset:25088\n\t"
        "ds_read_b64_tr_b16 a[216:217], v101 offset:16384\n\t"
        "ds_read_b64_tr_b16 a[218:219], v101 offset:16896\n\t"
        "ds_read_b64_tr_b16 a[220:221], v101 offset:24576\n\t"
        "ds_read_b64_tr_b16 a[222:223], v101 offset:25088\n\t"
        "ds_read_b64_tr_b16 a[224:225], v102 offset:16384\n\t"
        "ds_read_b64_tr_b16 a[226:227], v102 offset:16896\n\t"
        "ds_read_b64_tr_b16 a[228:229], v102 offset:24576\n\t"
        "ds_read_b64_tr_b16 a[230:231], v102 offset:25088\n\t"
        "ds_read_b64_tr_b16 a[232:233], v103 offset:16384\n\t"
        "ds_read_b64_tr_b16 a[234:235], v103 offset:16896\n\t"
        "ds_read_b64_tr_b16 a[236:237], v103 offset:24576\n\t"
        "ds_read_b64_tr_b16 a[238:239], v103 offset:25088\n\t"
        "ds_read_b64_tr_b16 a[240:241], v104 offset:16384\n\t"
        "ds_read_b64_tr_b16 a[242:243], v104 offset:16896\n\t"
        "ds_read_b64_tr_b16 a[244:245], v104 offset:24576\n\t"
        "ds_read_b64_tr_b16 a[246:247], v104 offset:25088\n\t"
        "ds_read_b64_tr_b16 a[248:249], v105 offset:16384\n\t"
        "ds_read_b64_tr_b16 a[250:251], v105 offset:16896\n\t"
        "ds_read_b64_tr_b16 a[252:253], v105 offset:24576\n\t"
        "ds_read_b64_tr_b16 a[254:255], v105 offset:25088\n\t"
        "s_waitcnt lgkmcnt(0)\n\t"
        "s_barrier\n\t"
        "s_add_u32 s80, %[m0b], 16384\n\t"
        "s_mov_b32 s81, 4194304\n\t"
        "s_mov_b32 s82, 4\n\t"
        ".Ld5%=:\n\t"
        "s_mov_b32 m0, s80\n\t"
        "s_add_u32 s80, s80, 0x8000\n\t"
        "buffer_load_dwordx4 v96, %[sk], s81 offen lds\n\t"
        "s_add_u32 m0, m0, 0x1000\n\t"
        "s_add_u32 s81, s81, 0x20000\n\t"
        "buffer_load_dwordx4 v96, %[sk], s81 offen lds\n\t"
        "s_add_u32 m0, m0, 0x1000\n\t"
        "s_add_u32 s81, s81, 0x20000\n\t"
        "buffer_load_dwordx4 v96, %[sk], s81 offen lds\n\t"
        "s_add_u32 m0, m0, 0x1000\n\t"
        "s_add_u32 s81, s81, 0x20000\n\t"
        "buffer_load_dwordx4 v96, %[sk], s81 offen lds\n\t"
        "s_add_u32 m0, m0, 0x1000\n\t"
        "s_add_u32 s81, s81, 0x20000\n\t"
        "s_sub_u32 s82, s82, 1\n\t"
        "s_cmp_lg_u32 s82, 0\n\t"
        "s_cbranch_scc1 .Ld5%=\n\t"
        "s_waitcnt vmcnt(16)\n\t"
        "s_barrier\n\t"
        "ds_read_b64_tr_b16 v[128:129], v98 offset:0\n\t"
        "ds_read_b64_tr_b16 v[130:131], v98 offset:512\n\t"
        "ds_read_b64_tr_b16 v[132:133], v98 offset:8192\n\t"
        "ds_read_b64_tr_b16 v[134:135], v98 offset:8704\n\t"
        "ds_read_b64_tr_b16 v[136:137], v99 offset:0\n\t"
        "ds_read_b64_tr_b16 v[138:139], v99 offset:512\n\t"
        "ds_read_b64_tr_b16 v[140:141], v99 offset:8192\n\t"
        "ds_read_b64_tr_b16 v[142:143], v99 offset:8704\n\t"
        "ds_read_b64_tr_b16 v[144:145], v100 offset:0\n\t"
        "ds_read_b64_tr_b16 v[146:147], v100 offset:512\n\t"
        "ds_read_b64_tr_b16 v[148:149], v100 offset:8192\n\t"
        "ds_read_b64_tr_b16 v[150:151], v100 offset:8704\n\t"
        "ds_read_b64_tr_b16 v[152:153], v101 offset:0\n\t"
        "ds_read_b64_tr_b16 v[154:155], v101 offset:512\n\t"
        "ds_read_b64_tr_b16 v[156:157], v101 offset:8192\n\t"
        "ds_read_b64_tr_b16 v[158:159], v101 offset:8704\n\t"
        "ds_read_b64_tr_b16 v[160:161], v102 offset:0\n\t"
        "ds_read_b64_tr_b16 v[162:163], v102 offset:512\n\t"
        "ds_read_b64_tr_b16 v[164:165], v102 offset:8192\n\t"
        "ds_read_b64_tr_b16 v[166:167], v102 offset:8704\n\t"
        "ds_read_b64_tr_b16 v[168:169], v103 offset:0\n\t"
        "ds_read_b64_tr_b16 v[170:171], v103 offset:512\n\t"
        "ds_read_b64_tr_b16 v[172:173], v103 offset:8192\n\t"
        "ds_read_b64_tr_b16 v[174:175], v103 offset:8704\n\t"
        "ds_read_b64_tr_b16 v[176:177], v104 offset:0\n\t"
        "ds_read_b64_tr_b16 v[178:179], v104 offset:512\n\t"
        "ds_read_b64_tr_b16 v[180:181], v104 offset:8192\n\t"
        "ds_read_b64_tr_b16 v[182:183], v104 offset:8704\n\t"
        "ds_read_b64_tr_b16 v[184:185], v105 offset:0\n\t"
        "ds_read_b64_tr_b16 v[186:187], v105 offset:512\n\t"
        "ds_read_b64_tr_b16 v[188:189], v105 offset:8192\n\t"
        "ds_read_b64_tr_b16 v[190:191], v105 offset:8704\n\t"
        "s_waitcnt lgkmcnt(0)\n\t"
        "s_barrier\n\t"
        "s_add_u32 s80, %[m0b], 0\n\t"
        "s_mov_b32 s81, 6291456\n\t"
        "s_mov_b32 s82, 4\n\t"
        ".Ld6%=:\n\t"
        "s_mov_b32 m0, s80\n\t"
        "s_add_u32 s80, s80, 0x8000\n\t"
        "buffer_load_dwordx4 v96, %[sq], s81 offen lds\n\t"
        "s_add_u32 m0, m0, 0x1000\n\t"
        "s_add_u32 s81, s81, 0x20000\n\t"
        "buffer_load_dwordx4 v96, %[sq], s81 offen lds\n\t"
        "s_add_u32 m0, m0, 0x1000\n\t"
        "s_add_u32 s81, s81, 0x20000\n\t"
        "buffer_load_dwordx4 v96, %[sq], s81 offen lds\n\t"
        "s_add_u32 m0, m0, 0x1000\n\t"
        "s_add_u32 s81, s81, 0x20000\n\t"
        "buffer_load_dwordx4 v96, %[sq], s81 offen lds\n\t"
        "s_add_u32 m0, m0, 0x1000\n\t"
        "s_add_u32 s81, s81, 0x20000\n\t"
        "s_sub_u32 s82, s82, 1\n\t"
        "s_cmp_lg_u32 s82, 0\n\t"
        "s_cbranch_scc1 .Ld6%=\n\t"
        "s_waitcnt vmcnt(16)\n\t"
        "s_barrier\n\t"
        "ds_read_b64_tr_b16 v[192:193], v98 offset:16384\n\t"
        "ds_read_b64_tr_b16 v[194:195], v98 offset:16896\n\t"
        "ds_read_b64_tr_b16 v[196:197], v98 offset:24576\n\t"
        "ds_read_b64_tr_b16 v[198:199], v98 offset:25088\n\t"
        "ds_read_b64_tr_b16 v[200:201], v99 offset:16384\n\t"
        "ds_read_b64_tr_b16 v[202:203], v99 offset:16896\n\t"
        "ds_read_b64_tr_b16 v[204:205], v99 offset:24576\n\t"
        "ds_read_b64_tr_b16 v[206:207], v99 offset:25088\n\t"
        "ds_read_b64_tr_b16 v[208:209], v100 offset:16384\n\t"
        "ds_read_b64_tr_b16 v[210:211], v100 offset:16896\n\t"
        "ds_read_b64_tr_b16 v[212:213], v100 offset:24576\n\t"
        "ds_read_b64_tr_b16 v[214:215], v100 offset:25088\n\t"
        "ds_read_b64_tr_b16 v[216:217], v101 offset:16384\n\t"
        "ds_read_b64_tr_b16 v[218:219], v101 offset:16896\n\t"
        "ds_read_b64_tr_b16 v[220:221], v101 offset:24576\n\t"
        "ds_read_b64_tr_b16 v[222:223], v101 offset:25088\n\t"
        "ds_read_b64_tr_b16 v[224:225], v102 offset:16384\n\t"
        "ds_read_b64_tr_b16 v[226:227], v102 offset:16896\n\t"
        "ds_read_b64_tr_b16 v[228:229], v102 offset:24576\n\t"
        "ds_read_b64_tr_b16 v[230:231], v102 offset:25088\n\t"
        "ds_read_b64_tr_b16 v[232:233], v103 offset:16384\n\t"
        "ds_read_b64_tr_b16 v[234:235], v103 offset:16896\n\t"
        "ds_read_b64_tr_b16 v[236:237], v103 offset:24576\n\t"
        "ds_read_b64_tr_b16 v[238:239], v103 offset:25088\n\t"
        "ds_read_b64_tr_b16 v[240:241], v104 offset:16384\n\t"
        "ds_read_b64_tr_b16 v[242:243], v104 offset:16896\n\t"
        "ds_read_b64_tr_b16 v[244:245], v104 offset:24576\n\t"
        "ds_read_b64_tr_b16 v[246:247], v104 offset:25088\n\t"
        "ds_read_b64_tr_b16 v[248:249], v105 offset:16384\n\t"
        "ds_read_b64_tr_b16 v[250:251], v105 offset:16896\n\t"
        "ds_read_b64_tr_b16 v[252:253], v105 offset:24576\n\t"
        "ds_read_b64_tr_b16 v[254:255], v105 offset:25088\n\t"
        "s_waitcnt lgkmcnt(0)\n\t"
        "s_barrier\n\t"
        "s_add_u32 s80, %[m0b], 16384\n\t"
        "s_mov_b32 s81, 6291456\n\t"
        "s_mov_b32 s82, 4\n\t"
        ".Ld7%=:\n\t"
        "s_mov_b32 m0, s80\n\t"
        "s_add_u32 s80, s80, 0x8000\n\t"
        "buffer_load_dwordx4 v96, %[sk], s81 offen lds\n\t"
        "s_add_u32 m0, m0, 0x1000\n\t"
        "s_add_u32 s81, s81, 0x20000\n\t"
        "buffer_load_dwordx4 v96, %[sk], s81 offen lds\n\t"
        "s_add_u32 m0, m0, 0x1000\n\t"
        "s_add_u32 s81, s81, 0x20000\n\t"
        "buffer_load_dwordx4 v96, %[sk], s81 offen lds\n\t"
        "s_add_u32 m0, m0, 0x1000\n\t"
        "s_add_u32 s81, s81, 0x20000\n\t"
        "buffer_load_dwordx4 v96, %[sk], s81 offen lds\n\t"
        "s_add_u32 m0, m0, 0x1000\n\t"
        "s_add_u32 s81, s81, 0x20000\n\t"
        "s_sub_u32 s82, s82, 1\n\t"
        "s_cmp_lg_u32 s82, 0\n\t"
        "s_cbranch_scc1 .Ld7%=\n\t"
        "s_waitcnt vmcnt(16)\n\t"
        "s_barrier\n\t"
        "ds_read_b64_tr_b16 a[0:1], v98 offset:0\n\t"
        "ds_read_b64_tr_b16 a[2:3], v98 offset:512\n\t"
        "ds_read_b64_tr_b16 a[4:5], v98 offset:8192\n\t"
        "ds_read_b64_tr_b16 a[6:7], v98 offset:8704\n\t"
        "ds_read_b64_tr_b16 a[8:9], v99 offset:0\n\t"
        "ds_read_b64_tr_b16 a[10:11], v99 offset:512\n\t"
        "ds_read_b64_tr_b16 a[12:13], v99 offset:8192\n\t"
        "ds_read_b64_tr_b16 a[14:15], v99 offset:8704\n\t"
        "ds_read_b64_tr_b16 v[64:65], v100 offset:0\n\t"
        "ds_read_b64_tr_b16 v[66:67], v100 offset:512\n\t"
        "ds_read_b64_tr_b16 v[68:69], v100 offset:8192\n\t"
        "ds_read_b64_tr_b16 v[70:71], v100 offset:8704\n\t"
        "ds_read_b64_tr_b16 v[72:73], v101 offset:0\n\t"
        "ds_read_b64_tr_b16 v[74:75], v101 offset:512\n\t"
        "ds_read_b64_tr_b16 v[76:77], v101 offset:8192\n\t"
        "ds_read_b64_tr_b16 v[78:79], v101 offset:8704\n\t"
        "ds_read_b64_tr_b16 v[80:81], v102 offset:0\n\t"
        "ds_read_b64_tr_b16 v[82:83], v102 offset:512\n\t"
        "ds_read_b64_tr_b16 v[84:85], v102 offset:8192\n\t"
        "ds_read_b64_tr_b16 v[86:87], v102 offset:8704\n\t"
        "ds_read_b64_tr_b16 v[88:89], v103 offset:0\n\t"
        "ds_read_b64_tr_b16 v[90:91], v103 offset:512\n\t"
        "ds_read_b64_tr_b16 v[92:93], v103 offset:8192\n\t"
        "ds_read_b64_tr_b16 v[94:95], v103 offset:8704\n\t"
        "ds_read_b64_tr_b16 a[128:129], v104 offset:0\n\t"
        "ds_read_b64_tr_b16 a[130:131], v104 offset:512\n\t"
        "ds_read_b64_tr_b16 a[132:133], v104 offset:8192\n\t"
        "ds_read_b64_tr_b16 a[134:135], v104 offset:8704\n\t"
        "ds_read_b64_tr_b16 a[136:137], v105 offset:0\n\t"
        "ds_read_b64_tr_b16 a[138:139], v105 offset:512\n\t"
        "ds_read_b64_tr_b16 a[140:141], v105 offset:8192\n\t"
        "ds_read_b64_tr_b16 a[142:143], v105 offset:8704\n\t"
        "s_waitcnt lgkmcnt(0)\n\t"
        "s_barrier\n\t"
        "ds_write_b128 v121, v[0:3] offset:0\n\t"
        "ds_write_b128 v121, v[4:7] offset:1024\n\t"
        "ds_write_b128 v121, v[8:11] offset:2048\n\t"
        "ds_write_b128 v121, v[12:15] offset:3072\n\t"
        "ds_write_b128 v121, v[16:19] offset:4096\n\t"
        "ds_write_b128 v121, v[20:23] offset:5120\n\t"
        "ds_write_b128 v121, v[24:27] offset:6144\n\t"
        "ds_write_b128 v121, v[28:31] offset:7168\n\t"
        "ds_write_b128 v121, v[32:35] offset:8192\n\t"
        "ds_write_b128 v121, v[36:39] offset:9216\n\t"
        "ds_write_b128 v121, v[40:43] offset:10240\n\t"
        "ds_write_b128 v121, v[44:47] offset:11264\n\t"
        "ds_write_b128 v121, v[48:51] offset:12288\n\t"
        "ds_write_b128 v121, v[52:55] offset:13312\n\t"
        "ds_write_b128 v121, v[56:59] offset:14336\n\t"
        "ds_write_b128 v121, v[60:63] offset:15360\n\t"
        "s_waitcnt lgkmcnt(0)\n\t"
        "s_mov_b32 s64, %[kf]\n\t"
        "s_mov_b32 s36, 0xf0000000\n\t"
        "buffer_load_dwordx4 v[116:119], v120, %[wsd], %[cal] offen sc1\n\t"
        "buffer_load_dwordx2 v[114:115], off, %[wsd], s64 sc1\n\t"
        "s_waitcnt vmcnt(2)\n\t"
        "s_barrier\n\t"
        "ds_read_b64_tr_b16 a[64:65], v98 offset:16384\n\t"
        "ds_read_b64_tr_b16 a[66:67], v98 offset:16896\n\t"
        "ds_read_b64_tr_b16 a[68:69], v98 offset:24576\n\t"
        "ds_read_b64_tr_b16 a[70:71], v98 offset:25088\n\t"
        "ds_read_b64_tr_b16 a[72:73], v99 offset:16384\n\t"
        "ds_read_b64_tr_b16 a[74:75], v99 offset:16896\n\t"
        "ds_read_b64_tr_b16 a[76:77], v99 offset:24576\n\t"
        "ds_read_b64_tr_b16 a[78:79], v99 offset:25088\n\t"
        "ds_read_b64_tr_b16 v[0:1], v100 offset:16384\n\t"
        "ds_read_b64_tr_b16 v[2:3], v100 offset:16896\n\t"
        "ds_read_b64_tr_b16 v[4:5], v100 offset:24576\n\t"
        "ds_read_b64_tr_b16 v[6:7], v100 offset:25088\n\t"
        "ds_read_b64_tr_b16 v[8:9], v101 offset:16384\n\t"
        "ds_read_b64_tr_b16 v[10:11], v101 offset:16896\n\t"
        "ds_read_b64_tr_b16 v[12:13], v101 offset:24576\n\t"
        "ds_read_b64_tr_b16 v[14:15], v101 offset:25088\n\t"
        "ds_read_b64_tr_b16 v[16:17], v102 offset:16384\n\t"
        "ds_read_b64_tr_b16 v[18:19], v102 offset:16896\n\t"
        "ds_read_b64_tr_b16 v[20:21], v102 offset:24576\n\t"
        "ds_read_b64_tr_b16 v[22:23], v102 offset:25088\n\t"
        "ds_read_b64_tr_b16 v[24:25], v103 offset:16384\n\t"
        "ds_read_b64_tr_b16 v[26:27], v103 offset:16896\n\t"
        "ds_read_b64_tr_b16 v[28:29], v103 offset:24576\n\t"
        "ds_read_b64_tr_b16 v[30:31], v103 offset:25088\n\t"
        "ds_read_b64_tr_b16 a[192:193], v104 offset:16384\n\t"
        "ds_read_b64_tr_b16 a[194:195], v104 offset:16896\n\t"
        "ds_read_b64_tr_b16 a[196:197], v104 offset:24576\n\t"
        "ds_read_b64_tr_b16 a[198:199], v104 offset:25088\n\t"
        "ds_read_b64_tr_b16 a[200:201], v105 offset:16384\n\t"
        "ds_read_b64_tr_b16 a[202:203], v105 offset:16896\n\t"
        "ds_read_b64_tr_b16 a[204:205], v105 offset:24576\n\t"
        "ds_read_b64_tr_b16 a[206:207], v105 offset:25088\n\t"
        "s_waitcnt vmcnt(0)\n\t"
        "s_mov_b32 s97, 0\n\t"
        ".Lg0%=:\n\t"
        "v_cmp_ne_u32_e32 vcc, %[tag], v116\n\t"
        "v_cmp_ne_u32_e64 s[82:83], %[tag], v118\n\t"
        "s_nop 1\n\t"
        "s_or_b64 vcc, vcc, s[82:83]\n\t"
        "s_cbranch_scc0 .Lg1%=\n\t"
        "s_sleep 4\n\t"
        "buffer_load_dwordx4 v[116:119], v120, %[wsd], %[cal] offen sc1\n\t"
        "s_add_u32 s97, s97, 1\n\t"
        "s_cmp_lt_u32 s97, 0x100000\n\t"
        "s_cbranch_scc0 .Lgu%=\n\t"
        "s_waitcnt vmcnt(0)\n\t"
        "s_branch .Lg0%=\n\t"
        ".Lg1%=:\n\t"
        "s_mov_b32 s84, 0x55555555\n\t"
        "s_mov_b32 s85, s84\n\t"
        "v_and_b32_e32 v116, 1, v117\n\t"
        "v_and_b32_e32 v118, 1, v119\n\t"
        "v_cmp_ne_u32_e32 vcc, 0, v116\n\t"
        "v_cmp_ne_u32_e64 s[82:83], 0, v118\n\t"
        "s_nop 0\n\t"
        "s_and_b64 s[82:83], s[82:83], s[84:85]\n\t"
        "s_bcnt1_i32_b64 s87, s[82:83]\n\t"
        "s_and_b64 s[82:83], vcc, s[84:85]\n\t"
        "s_bcnt1_i32_b64 s86, s[82:83]\n\t"
        "s_andn2_b64 s[82:83], vcc, s[84:85]\n\t"
        "s_bcnt1_i32_b64 s88, s[82:83]\n\t"
        "v_and_b32_e32 v116, 2, v117\n\t"
        "v_and_b32_e32 v118, 2, v119\n\t"
        "v_cmp_ne_u32_e32 vcc, 0, v116\n\t"
        "v_cmp_ne_u32_e64 s[82:83], 0, v118\n\t"
        "s_nop 0\n\t"
        "s_and_b64 s[82:83], s[82:83], s[84:85]\n\t"
        "s_bcnt1_i32_b64 s90, s[82:83]\n\t"
        "s_and_b64 s[82:83], vcc, s[84:85]\n\t"
        "s_bcnt1_i32_b64 s89, s[82:83]\n\t"
        "s_andn2_b64 s[82:83], vcc, s[84:85]\n\t"
        "s_bcnt1_i32_b64 s91, s[82:83]\n\t"
        "s_add_u32 s80, s86, s87\n\t"
        "s_add_u32 s80, s80, s88\n\t"
        "s_cmp_ge_u32 s80, 48\n\t"
        "s_cselect_b32 s80, 8, 24\n\t"
        "s_cmp_gt_u32 s86, s80\n\t"
        "s_cselect_b32 s86, %c[NX1], %c[NX0]\n\t"
        "s_cmp_gt_u32 s89, 16\n\t"
        "s_cselect_b32 s81, %c[NY1], %c[NY0]\n\t"
        "s_or_b32 s86, s86, s81\n\t"
        "s_cmp_gt_u32 s87, s80\n\t"
        "s_cselect_b32 s87, %c[NX1], %c[NX0]\n\t"
        "s_cmp_gt_u32 s90, 16\n\t"
        "s_cselect_b32 s81, %c[NY1], %c[NY0]\n\t"
        "s_or_b32 s87, s87, s81\n\t"
        "s_cmp_gt_u32 s88, s80\n\t"
        "s_cselect_b32 s88, %c[NX1], %c[NX0]\n\t"
        "s_cmp_gt_u32 s91, 16\n\t"
        "s_cselect_b32 s81, %c[NY1], %c[NY0]\n\t"
        "s_or_b32 s88, s88, s81\n\t"
        "s_xor_b32 %[nA], %[nA], s86\n\t"
        "s_xor_b32 %[nW], %[nW], s86\n\t"
        "s_xor_b32 %[nC], %[nC], s87\n\t"
        "s_xor_b32 %[nS], %[nS], s88\n\t"
        "s_xor_b32 %[nT], %[nT], s88\n\t"
        "s_xor_b32 %[b0], %[b0], s88\n\t"
        "s_cmp_eq_u32 %[w], 1\n\t"
        "s_cselect_b32 s80, s87, s86\n\t"
        "s_cmp_eq_u32 %[w], 2\n\t"
        "s_cselect_b32 s80, s88, s80\n\t"
        "s_xor_b32 %[pob], %[pob], s80\n\t"
        "s_lshl_b32 s80, %[w], 10\n\t"
        "s_sub_u32 %[nS], %[nS], s80\n\t"
        "s_sub_u32 %[pob], %[pob], s80\n\t"
        "ds_read_b32 v116, v120 offset:20484\n\t"
        "s_and_b32 s80, s87, 0x4000\n\t"
        "s_xor_b32 %[nC], %[nC], s80\n\t"
        "s_lshl_b32 s81, %[w], 8\n\t"
        "s_add_u32 s81, s81, 24576\n\t"
        "s_sub_u32 %[b0], %[b0], s81\n\t"
        "s_waitcnt lgkmcnt(0)\n\t"
        "v_xor_b32_e32 v116, s80, v116\n\t"
        "ds_write_b32 v120, v116 offset:20484\n\t"
        "s_mov_b32 s36, 0xf0000000\n\t"
        "s_mov_b32 s38, 0xff00ff00\n\t"
        "s_mov_b32 s39, 0xff00ff00\n\t"
        "s_mov_b32 s40, 0\n\t"
        "s_mov_b32 s37, 0\n\t"
        "s_brev_b32 s54, 1\n\t"
        "s_mov_b32 s46, %c[iD1]\n\t"
        "s_mov_b32 s45, %c[iD1]\n\t"
        "s_mov_b32 s47, %c[iD2]\n\t"
        "s_mov_b32 s48, 1\n\t"
        "s_mov_b32 s50, %c[LC]\n\t"
        "s_mov_b32 s51, %c[LA]\n\t"
        "s_mov_b32 s52, %c[E1]\n\t"
        "s_mov_b32 s53, %c[E2]\n\t"
        "s_mov_b32 s43, 0\n\t"
        "s_mov_b32 s61, %[b0]\n\t"
        "s_add_u32 s62, %[b0], 0x40000\n\t"
        "s_mov_b32 s75, 31744\n\t"
        "s_cmp_eq_u32 s64, s36\n\t"
        "s_cbranch_scc1 .Lk10%=\n\t"
        "s_mov_b32 s97, 0\n\t"
        ".Lk00%=:\n\t"
        "v_readfirstlane_b32 s82, v114\n\t"
        "v_readfirstlane_b32 s83, v115\n\t"
        "s_xor_b32 s82, s82, %[tag]\n\t"
        "s_xor_b32 s83, s83, %[tag]\n\t"
        "s_or_b32 s82, s82, s83\n\t"
        "s_cbranch_scc0 .Lk10%=\n\t"
        "s_sleep 3\n\t"
        "buffer_load_dwordx2 v[114:115], off, %[wsd], s64 sc1\n\t"
        "s_add_u32 s97, s97, 1\n\t"
        "s_cmp_lt_u32 s97, 0x200000\n\t"
        "s_cbranch_scc0 .Lgu%=\n\t"
        "s_waitcnt vmcnt(0)\n\t"
        "s_branch .Lk00%=\n\t"
        ".Lk10%=:\n\t"
        "s_add_u32 s80, s40, 0\n\t"
        "s_min_u32 s80, s80, 63\n\t"
        "s_lshl_b32 s80, s80, 18\n\t"
        "s_add_u32 s69, s80, %[kx]\n\t"
        "s_add_u32 s70, s69, 0x1000\n\t"
        "s_lshl_b32 s80, s40, 18\n\t"
        "s_add_u32 s71, s80, %[vx]\n\t"
        "s_add_u32 s80, s40, 1\n\t"
        "s_lshl_b32 s81, s80, 9\n\t"
        "s_add_u32 s81, s81, %[kf]\n\t"
        "s_and_b32 s80, s80, 0x43\n\t"
        "s_cselect_b32 s64, s36, s81\n\t"
        "buffer_load_dwordx4 v[104:107], v120, %[wsd], s69 offen sc1\n\t"
        "buffer_load_dwordx4 v[108:111], v120, %[wsd], s70 offen sc1\n\t"
        "s_waitcnt lgkmcnt(0)\n\t"
        "s_barrier\n\t"
        "ds_write_b128 v121, v[64:67] offset:16384\n\t"
        "ds_write_b128 v121, v[68:71] offset:17408\n\t"
        "ds_write_b128 v121, v[72:75] offset:18432\n\t"
        "ds_write_b128 v121, v[76:79] offset:19456\n\t"
        "ds_write_b128 v121, v[80:83] offset:20480\n\t"
        "ds_write_b128 v121, v[84:87] offset:21504\n\t"
        "ds_write_b128 v121, v[88:91] offset:22528\n\t"
        "ds_write_b128 v121, v[92:95] offset:23552\n\t"
        "ds_write_b128 v121, v[0:3] offset:24576\n\t"
        "ds_write_b128 v121, v[4:7] offset:25600\n\t"
        "ds_write_b128 v121, v[8:11] offset:26624\n\t"
        "ds_write_b128 v121, v[12:15] offset:27648\n\t"
        "ds_write_b128 v121, v[16:19] offset:28672\n\t"
        "ds_write_b128 v121, v[20:23] offset:29696\n\t"
        "ds_write_b128 v121, v[24:27] offset:30720\n\t"
        "ds_write_b128 v121, v[28:31] offset:31744\n\t"
        "s_waitcnt lgkmcnt(0)\n\t"
        "rec_s_code:\n\t"
        "ds_read_b128 v[16:19], v121 offset:0\n\t"
        "ds_read_b128 v[20:23], v121 offset:1024\n\t"
        "ds_read_b128 v[24:27], v121 offset:2048\n\t"
        "ds_read_b128 v[28:31], v121 offset:3072\n\t"
        "ds_read_b128 v[32:35], v121 offset:4096\n\t"
        "ds_read_b128 v[36:39], v121 offset:5120\n\t"
        "ds_read_b128 v[40:43], v121 offset:6144\n\t"
        "ds_read_b128 v[44:47], v121 offset:7168\n\t"
        "ds_read_b128 v[56:59], v121 offset:8192\n\t"
        "ds_read_b128 v[60:63], v121 offset:9216\n\t"
        "ds_read_b128 v[64:67], v121 offset:10240\n\t"
        "ds_read_b128 v[68:71], v121 offset:11264\n\t"
        "ds_read_b128 v[72:75], v121 offset:12288\n\t"
        "ds_read_b128 v[76:79], v121 offset:13312\n\t"
        "ds_read_b128 v[80:83], v121 offset:14336\n\t"
        "ds_read_b128 v[84:87], v121 offset:15360\n\t"
        "s_and_b32 s80, s40, 3\n\t"
        "s_lshl_b32 s80, s80, 17\n\t"
        "s_add_u32 s57, s80, %[nA]\n\t"
        "s_add_u32 s58, s57, 0x1000\n\t"
        "s_add_u32 s59, s57, 0x8000\n\t"
        "s_add_u32 s60, s57, 0x9000\n\t"
        "s_and_b32 s80, s40, 3\n\t"
        "s_lshl_b32 s80, s80, 17\n\t"
        "s_add_u32 s63, s80, %[nC]\n\t"
        "s_add_u32 s66, s80, %[nS]\n\t"
        "s_add_u32 s81, s80, 0x20000\n\t"
        "s_and_b32 s81, s81, 0x60000\n\t"
        "s_add_u32 s67, s81, %[nW]\n\t"
        "s_add_u32 s79, s81, %[nT]\n\t"
        "s_cmp_eq_u32 %[w], 2\n\t"
        "s_cselect_b32 s81, s80, s81\n\t"
        "s_xor_b32 s81, s81, 0x40000\n\t"
        "s_add_u32 s68, s81, %[pob]\n\t"
        "s_lshl_b32 s72, s40, 17\n\t"
        "s_add_u32 s72, s72, %[kvo]\n\t"
        "s_mov_b32 s88, 0\n\t"
        "s_mov_b32 s89, 0\n\t"
        "s_mov_b32 s90, 0\n\t"
        "v_mov_b32_e32 v88, 0\n\t"
        "v_mov_b32_e32 v89, 0\n\t"
        "v_mov_b32_e32 v90, 0\n\t"
        "v_mov_b32_e32 v91, 0\n\t"
        "v_mov_b32_e32 v92, 0\n\t"
        "v_mov_b32_e32 v93, 0\n\t"
        "v_mov_b32_e32 v94, 0\n\t"
        "v_mov_b32_e32 v95, 0\n\t"
        "v_mov_b32_e32 v96, 0\n\t"
        "v_mov_b32_e32 v97, 0\n\t"
        "v_mov_b32_e32 v98, 0\n\t"
        "v_mov_b32_e32 v99, 0\n\t"
        "v_mov_b32_e32 v100, 0\n\t"
        "v_mov_b32_e32 v101, 0\n\t"
        "v_mov_b32_e32 v102, 0\n\t"
        "v_mov_b32_e32 v103, 0\n\t"
        "v_mov_b32_e32 v52, v112\n\t"
        "v_mov_b32_e32 v53, v113\n\t"
        "v_mov_b32_e32 v49, 0\n\t"
        "v_lshlrev_b32_e32 v51, 6, v120\n\t"
        "s_cmp_eq_u32 %[w], 0\n\t"
        "s_cselect_b32 s73, %[nT], s36\n\t"
        "s_cselect_b32 s74, %[pra], s36\n\t"
        "v_mov_b32_e32 v50, s40\n\t"
        "s_cmp_ge_u32 s40, 60\n\t"
        "s_cselect_b32 s65, s74, s36\n\t"
        "s_waitcnt lgkmcnt(0)\n\t"
        "s_barrier\n\t"
        "s_memrealtime s[98:99]\n\t"
        "s_waitcnt lgkmcnt(0)\n\t"
        "s_mov_b32 s41, s98\n\t"
        "v_mov_b32_e32 v48, s98\n\t"
        ".p2align 6\n\t"
        ".Lloop%=:\n\t"
        "s_mov_b32 s97, 0\n\t"
        "s_movk_i32 s96, 0x1000\n\t"
        ".Lwa%=:\n\t"
        "s_memrealtime s[98:99]\n\t"
        "s_addk_i32 s96, -1\n\t"
        "s_cmp_lt_i32 s96, 0\n\t"
        "s_cbranch_scc1 .Lta%=\n\t"
        "s_waitcnt lgkmcnt(0)\n\t"
        "s_sub_i32 s98, s98, s41\n\t"
        "s_cmp_lt_i32 s98, %c[iFT]\n\t"
        "s_cbranch_scc1 .Lwa%=\n\t"
        "s_cmp_ge_i32 s98, 0\n\t"
        "s_cbranch_scc1 .Lxa%=\n\t"
        ".Lqa%=:\n\t"
        "s_nop %c[iFN]\n\t"
        "s_addk_i32 s98, 1\n\t"
        "s_cmp_lt_i32 s98, 0\n\t"
        "s_cbranch_scc1 .Lqa%=\n\t"
        ".Lxa%=:\n\t"
        "buffer_load_dwordx4 v[0:3], v120, %[srd], s57 offen sc1\n\t"
        "s_mov_b32 s80, s88\n\t"
        "s_add_i32 s81, s92, s50\n\t"
        "s_sub_i32 s81, s81, 4\n\t"
        "s_cmp_le_i32 s81, s44\n\t"
        "s_cselect_b32 s80, s80, 0\n\t"
        "s_cmp_lt_i32 s50, %c[LC24]\n\t"
        "s_cselect_b32 s80, s80, 0\n\t"
        "s_lshl2_add_u32 s50, s80, s50\n\t"
        "buffer_load_dwordx4 v[4:7], v120, %[srd], s58 offen sc1\n\t"
        "s_sub_i32 s50, s50, s89\n\t"
        "s_max_i32 s50, s50, %c[LC]\n\t"
        "s_lshl_b32 s80, s92, 1\n\t"
        "s_sub_i32 s81, s80, s52\n\t"
        "s_ashr_i32 s82, s81, %c[UPSH]\n\t"
        "s_max_i32 s82, s82, %c[TRUP]\n\t"
        "s_add_i32 s82, s82, s52\n\t"
        "s_min_i32 s82, s82, s80\n\t"
        "buffer_load_dwordx4 v[8:11], v120, %[srd], s59 offen sc1\n\t"
        "s_ashr_i32 s83, s81, %c[DNSH]\n\t"
        "s_min_i32 s83, s83, -%c[TRDN]\n\t"
        "s_add_i32 s83, s83, s52\n\t"
        "s_max_i32 s83, s83, s80\n\t"
        "s_cmp_gt_i32 s81, 0\n\t"
        "s_cselect_b32 s82, s82, s83\n\t"
        "s_cmp_lg_u32 s90, 0\n\t"
        "s_cselect_b32 s52, s82, s52\n\t"
        "buffer_load_dwordx4 v[12:15], v120, %[srd], s60 offen sc1\n\t"
        "s_add_i32 s80, s52, 1\n\t"
        "s_ashr_i32 s80, s80, 1\n\t"
        "s_add_i32 s45, s80, s50\n\t"
        "s_add_u32 s80, s40, 1\n\t"
        "s_min_u32 s80, s80, 63\n\t"
        "s_lshl_b32 s80, s80, 18\n\t"
        "s_add_u32 s69, s80, %[kx]\n\t"
        "s_add_u32 s70, s69, 0x1000\n\t"
        "s_lshl_b32 s80, s40, 18\n\t"
        "s_add_u32 s71, s80, %[vx]\n\t"
        "s_add_u32 s80, s40, 2\n\t"
        "s_lshl_b32 s81, s80, 9\n\t"
        "s_add_u32 s81, s81, %[kf]\n\t"
        "s_and_b32 s80, s80, 0x43\n\t"
        "s_cselect_b32 s64, s36, s81\n\t"
        "s_nop 15\n\t"
        "s_nop 7\n\t"
        "s_mov_b64 exec, 7\n\t"
        "buffer_store_dwordx2 v[48:49], v51, %[srd], s73 offen sc1\n\t"
        "s_mov_b64 exec, -1\n\t"
        "buffer_load_dwordx4 v[112:115], v120, %[wsd], s71 offen offset:0 sc1\n\t"
        "buffer_load_dwordx4 v[116:119], v120, %[wsd], s71 offen offset:1024 sc1\n\t"
        ".Lgo%=:\n\t"
        "s_waitcnt vmcnt(6)\n\t"
        "v_smfmac_f32_16x16x64_bf16 v[88:91], v[0:3], v[16:23], v122\n\t"
        "ds_write_b128 v120, v[0:3] offset:0\n\t"
        "v_smfmac_f32_16x16x64_bf16 v[92:95], v[0:3], v[24:31], v122\n\t"
        "v_smfmac_f32_16x16x64_bf16 v[96:99], v[0:3], v[32:39], v122\n\t"
        "v_smfmac_f32_16x16x64_bf16 v[100:103], v[0:3], v[40:47], v122\n\t"
        "s_waitcnt vmcnt(5)\n\t"
        "v_smfmac_f32_16x16x64_bf16 v[88:91], v[4:7], v[56:63], v122\n\t"
        "ds_write_b128 v120, v[4:7] offset:4096\n\t"
        "v_smfmac_f32_16x16x64_bf16 v[92:95], v[4:7], v[64:71], v122\n\t"
        "v_smfmac_f32_16x16x64_bf16 v[96:99], v[4:7], v[72:79], v122\n\t"
        "v_smfmac_f32_16x16x64_bf16 v[100:103], v[4:7], v[80:87], v122\n\t"
        "s_waitcnt vmcnt(4)\n\t"
        "v_smfmac_f32_16x16x64_bf16 v[88:91], v[8:11], v[128:135], v122\n\t"
        "ds_write_b128 v120, v[8:11] offset:8192\n\t"
        "v_smfmac_f32_16x16x64_bf16 v[92:95], v[8:11], v[136:143], v122\n\t"
        "s_waitcnt lgkmcnt(1)\n\t"
        "s_barrier\n\t"
        "v_smfmac_f32_16x16x64_bf16 v[96:99], v[8:11], v[192:199], v122\n\t"
        "ds_read_b128 v[16:19], v124 offset:0\n\t"
        "v_smfmac_f32_16x16x64_bf16 v[100:103], v[8:11], v[200:207], v122\n\t"
        "ds_read_b128 v[20:23], v125 offset:0\n\t"
        "s_waitcnt vmcnt(3)\n\t"
        "v_smfmac_f32_16x16x64_bf16 v[88:91], v[12:15], a[0:7], v122\n\t"
        "ds_write_b128 v120, v[12:15] offset:12288\n\t"
        "v_smfmac_f32_16x16x64_bf16 v[92:95], v[12:15], a[8:15], v122\n\t"
        "buffer_store_dword v50, off, %[wsd], s65 sc1\n\t"
        "v_smfmac_f32_16x16x64_bf16 v[96:99], v[12:15], a[64:71], v122\n\t"
        "ds_read_b128 v[24:27], v126 offset:0\n\t"
        "v_smfmac_f32_16x16x64_bf16 v[100:103], v[12:15], a[72:79], v122\n\t"
        "ds_read_b128 v[28:31], v124 offset:4096\n\t"
        "s_waitcnt lgkmcnt(4)\n\t"
        "v_smfmac_f32_16x16x64_bf16 v[88:91], v[16:19], a[16:23], v122\n\t"
        "ds_read_b128 v[32:35], v125 offset:4096\n\t"
        "v_smfmac_f32_16x16x64_bf16 v[92:95], v[16:19], a[24:31], v122\n\t"
        "ds_read_b128 v[36:39], v126 offset:4096\n\t"
        "s_waitcnt lgkmcnt(5)\n\t"
        "v_smfmac_f32_16x16x64_bf16 v[88:91], v[20:23], a[32:39], v122\n\t"
        "ds_read_b128 v[72:75], v121 offset:16384\n\t"
        "v_smfmac_f32_16x16x64_bf16 v[92:95], v[20:23], a[40:47], v122\n\t"
        "s_waitcnt lgkmcnt(5)\n\t"
        "s_barrier\n\t"
        "ds_read_b128 v[76:79], v121 offset:17408\n\t"
        "s_waitcnt lgkmcnt(5)\n\t"
        "v_smfmac_f32_16x16x64_bf16 v[88:91], v[24:27], a[48:55], v122\n\t"
        "ds_read_b128 v[0:3], v124 offset:12288\n\t"
        "v_smfmac_f32_16x16x64_bf16 v[92:95], v[24:27], a[56:63], v122\n\t"
        "ds_read_b128 v[80:83], v121 offset:18432\n\t"
        "s_waitcnt lgkmcnt(6)\n\t"
        "v_smfmac_f32_16x16x64_bf16 v[88:91], v[28:31], a[144:151], v122\n\t"
        "ds_read_b128 v[84:87], v121 offset:19456\n\t"
        "v_smfmac_f32_16x16x64_bf16 v[92:95], v[28:31], a[152:159], v122\n\t"
        "ds_read_b128 v[40:43], v124 offset:8192\n\t"
        "s_waitcnt lgkmcnt(7)\n\t"
        "v_smfmac_f32_16x16x64_bf16 v[88:91], v[32:35], a[160:167], v122\n\t"
        "ds_read_b128 v[44:47], v125 offset:8192\n\t"
        "v_mov_b32_e32 v12, 0x3f803f80\n\t"
        "v_smfmac_f32_16x16x64_bf16 v[92:95], v[32:35], a[168:175], v122\n\t"
        "ds_read_b128 v[56:59], v121 offset:20480\n\t"
        "v_mov_b32_e32 v13, 0x3f803f80\n\t"
        "s_waitcnt lgkmcnt(8)\n\t"
        "v_smfmac_f32_16x16x64_bf16 v[88:91], v[36:39], a[176:183], v122\n\t"
        "ds_read_b128 v[60:63], v121 offset:21504\n\t"
        "v_mov_b32_e32 v14, 0x3f803f80\n\t"
        "v_smfmac_f32_16x16x64_bf16 v[92:95], v[36:39], a[184:191], v122\n\t"
        "ds_read_b128 v[4:7], v125 offset:12288\n\t"
        "v_mov_b32_e32 v15, 0x3f803f80\n\t"
        "s_waitcnt lgkmcnt(8)\n\t"
        "s_waitcnt lgkmcnt(7)\n\t"
        "v_smfmac_f32_16x16x64_bf16 v[88:91], v[0:3], v[72:79], v122\n\t"
        "ds_read_b128 v[64:67], v121 offset:22528\n\t"
        "s_waitcnt lgkmcnt(6)\n\t"
        "v_smfmac_f32_16x16x64_bf16 v[92:95], v[0:3], v[80:87], v122\n\t"
        "ds_read_b128 v[68:71], v121 offset:23552\n\t"
        "s_waitcnt lgkmcnt(6)\n\t"
        "v_smfmac_f32_16x16x64_bf16 v[88:91], v[40:43], v[144:151], v122\n\t"
        "ds_read_b128 v[48:51], v126 offset:8192\n\t"
        "v_smfmac_f32_16x16x64_bf16 v[92:95], v[40:43], v[152:159], v122\n\t"
        "ds_read_b128 v[8:11], v126 offset:12288\n\t"
        "s_waitcnt lgkmcnt(7)\n\t"
        "v_smfmac_f32_16x16x64_bf16 v[88:91], v[44:47], v[160:167], v122\n\t"
        "ds_read_b128 v[72:75], v121 offset:24576\n\t"
        "v_smfmac_f32_16x16x64_bf16 v[92:95], v[44:47], v[168:175], v122\n\t"
        "ds_read_b128 v[76:79], v121 offset:25600\n\t"
        "s_waitcnt lgkmcnt(7)\n\t"
        "s_waitcnt lgkmcnt(6)\n\t"
        "v_smfmac_f32_16x16x64_bf16 v[88:91], v[4:7], v[56:63], v122\n\t"
        "ds_read_b128 v[80:83], v121 offset:26624\n\t"
        "s_waitcnt lgkmcnt(5)\n\t"
        "v_smfmac_f32_16x16x64_bf16 v[92:95], v[4:7], v[64:71], v122\n\t"
        "ds_read_b128 v[84:87], v121 offset:27648\n\t"
        "s_waitcnt lgkmcnt(5)\n\t"
        "v_smfmac_f32_16x16x64_bf16 v[88:91], v[48:51], v[176:183], v122\n\t"
        "v_mov_b32_e32 v64, 0\n\t"
        "v_smfmac_f32_16x16x64_bf16 v[92:95], v[48:51], v[184:191], v122\n\t"
        "v_mov_b32_e32 v65, 0\n\t"
        "s_waitcnt lgkmcnt(4)\n\t"
        "v_smfmac_f32_16x16x64_bf16 v[88:91], v[8:11], a[128:135], v122\n\t"
        "v_mov_b32_e32 v66, 0\n\t"
        "v_smfmac_f32_16x16x64_bf16 v[92:95], v[8:11], a[136:143], v122\n\t"
        "v_mov_b32_e32 v67, 0\n\t"
        "s_waitcnt lgkmcnt(2)\n\t"
        "v_smfmac_f32_16x16x64_bf16 v[96:99], v[0:3], v[72:79], v122\n\t"
        "ds_read_b128 v[72:75], v121 offset:28672\n\t"
        "s_waitcnt lgkmcnt(1)\n\t"
        "v_smfmac_f32_16x16x64_bf16 v[100:103], v[0:3], v[80:87], v122\n\t"
        "ds_read_b128 v[76:79], v121 offset:29696\n\t"
        "v_smfmac_f32_16x16x64_bf16 v[96:99], v[16:19], a[80:87], v122\n\t"
        "v_add_f32_e32 v88, v88, v90\n\t"
        "v_add_f32_e32 v89, v89, v91\n\t"
        "v_smfmac_f32_16x16x64_bf16 v[100:103], v[16:19], a[88:95], v122\n\t"
        "v_cvt_pk_bf16_f32 v90, v88, v89\n\t"
        "v_add_f32_e32 v92, v92, v94\n\t"
        "v_smfmac_f32_16x16x64_bf16 v[96:99], v[20:23], a[96:103], v122\n\t"
        "ds_write_b32 v127, v90\n\t"
        "v_add_f32_e32 v93, v93, v95\n\t"
        "v_cvt_pk_bf16_f32 v91, v92, v93\n\t"
        "v_smfmac_f32_16x16x64_bf16 v[100:103], v[20:23], a[104:111], v122\n\t"
        "ds_write_b32 v127, v91 offset:384\n\t"
        "v_cmp_u_f32_e32 vcc, v88, v89\n\t"
        "v_smfmac_f32_16x16x64_bf16 v[96:99], v[24:27], a[112:119], v122\n\t"
        "ds_read_b64_tr_b16 v[56:57], v52 offset:0\n\t"
        "ds_read_b64_tr_b16 v[58:59], v52 offset:64\n\t"
        "v_smfmac_f32_16x16x64_bf16 v[100:103], v[24:27], a[120:127], v122\n\t"
        "ds_read_b64_tr_b16 v[60:61], v53 offset:0\n\t"
        "ds_read_b64_tr_b16 v[62:63], v53 offset:64\n\t"
        "v_smfmac_f32_16x16x64_bf16 v[96:99], v[28:31], a[208:215], v122\n\t"
        "s_cbranch_vccnz .Lf1%=\n\t"
        ".Lr1%=:\n\t"
        "ds_read_b128 v[80:83], v121 offset:30720\n\t"
        "v_smfmac_f32_16x16x64_bf16 v[100:103], v[28:31], a[216:223], v122\n\t"
        "ds_read_b128 v[84:87], v121 offset:31744\n\t"
        "v_mov_b32_e32 v91, 0\n\t"
        "v_smfmac_f32_16x16x64_bf16 v[96:99], v[32:35], a[224:231], v122\n\t"
        "v_mov_b32_e32 v94, 0\n\t"
        "v_smfmac_f32_16x16x64_bf16 v[100:103], v[32:35], a[232:239], v122\n\t"
        "ds_read_b128 v[32:35], v120 offset:20480\n\t"
        "v_smfmac_f32_16x16x64_bf16 v[96:99], v[36:39], a[240:247], v122\n\t"
        "s_waitcnt lgkmcnt(3)\n\t"
        "s_barrier\n\t"
        "v_mfma_f32_16x16x32_bf16 v[68:71], v[104:107], v[56:59], 0\n\t"
        "ds_read_b64_tr_b16 v[16:17], v123 offset:0\n\t"
        "v_mfma_f32_16x16x32_bf16 v[68:71], v[108:111], v[60:63], v[68:71]\n\t"
        "ds_read_b64_tr_b16 v[18:19], v123 offset:64\n\t"
        "v_smfmac_f32_16x16x64_bf16 v[100:103], v[36:39], a[248:255], v122\n\t"
        "buffer_load_dword v88, v127, %[srd], s61 offen offset:0 sc1\n\t"
        "v_smfmac_f32_16x16x64_bf16 v[96:99], v[8:11], a[192:199], v122\n\t"
        "buffer_load_dword v89, v127, %[srd], s61 offen offset:256 sc1\n\t"
        "v_smfmac_f32_16x16x64_bf16 v[100:103], v[8:11], a[200:207], v122\n\t"
        "s_nop 1\n\t"
        "ds_write_b128 v120, v[68:71] offset:16384\n\t"
        "v_smfmac_f32_16x16x64_bf16 v[96:99], v[4:7], v[72:79], v122\n\t"
        "ds_read_b64_tr_b16 v[20:21], v123 offset:768\n\t"
        "s_waitcnt lgkmcnt(5)\n\t"
        "v_smfmac_f32_16x16x64_bf16 v[100:103], v[4:7], v[80:87], v122\n\t"
        "ds_read_b64_tr_b16 v[22:23], v123 offset:832\n\t"
        "v_smfmac_f32_16x16x64_bf16 v[96:99], v[40:43], v[208:215], v122\n\t"
        "ds_read_b64_tr_b16 v[24:25], v123 offset:1536\n\t"
        "ds_read_b64_tr_b16 v[26:27], v123 offset:1600\n\t"
        "v_smfmac_f32_16x16x64_bf16 v[100:103], v[40:43], v[216:223], v122\n\t"
        "buffer_load_dwordx4 v[84:87], v120, %[srd], s66 offen sc1\n\t"
        "v_smfmac_f32_16x16x64_bf16 v[96:99], v[44:47], v[224:231], v122\n\t"
        "s_waitcnt lgkmcnt(4)\n\t"
        "s_barrier\n\t"
        "ds_read_b128 v[36:39], v124 offset:16384\n\t"
        "v_smfmac_f32_16x16x64_bf16 v[100:103], v[44:47], v[232:239], v122\n\t"
        "ds_read_b128 v[56:59], v125 offset:16384\n\t"
        "v_smfmac_f32_16x16x64_bf16 v[96:99], v[48:51], v[240:247], v122\n\t"
        "ds_read_b128 v[60:63], v126 offset:16384\n\t"
        "v_smfmac_f32_16x16x64_bf16 v[100:103], v[48:51], v[248:255], v122\n\t"
        "ds_read_b64_tr_b16 v[28:29], v123 offset:2304\n\t"
        "ds_read_b64_tr_b16 v[30:31], v123 offset:2368\n\t"
        "s_waitcnt lgkmcnt(4)\n\t"
        "s_cmp_eq_u32 s40, 63\n\t"
        "s_nop 1\n\t"
        "v_add_f32_e32 v96, v96, v98\n\t"
        "v_add_f32_e32 v97, v97, v99\n\t"
        "s_waitcnt lgkmcnt(2)\n\t"
        "v_cvt_pk_bf16_f32 v50, v96, v97\n\t"
        "v_pk_add_f32 v[100:101], v[100:101], v[102:103]\n\t"
        "ds_write_b32 v35, v50\n\t"
        "v_cvt_pk_bf16_f32 v51, v100, v101\n\t"
        "ds_write_b32 v35, v51 offset:384\n\t"
        "v_pk_add_f32 v[56:57], v[56:57], v[60:61]\n\t"
        "s_cbranch_scc1 .Lend%=\n\t"
        "v_pk_add_f32 v[58:59], v[58:59], v[62:63]\n\t"
        "v_pk_add_f32 v[68:69], v[68:69], v[36:37]\n\t"
        "v_pk_add_f32 v[70:71], v[70:71], v[38:39]\n\t"
        "v_pk_add_f32 v[68:69], v[68:69], v[56:57]\n\t"
        "v_pk_add_f32 v[70:71], v[70:71], v[58:59]\n\t"
        "v_max3_f32 v2, v68, v69, v70\n\t"
        "v_max_f32_e32 v2, v2, v71\n\t"
        "s_nop 1\n\t"
        "v_max_f32_dpp v3, v2, v2 row_ror:8 row_mask:0xf bank_mask:0xf\n\t"
        "v_max_f32_dpp v1, v2, v2 row_ror:8 row_mask:0xf bank_mask:0xf\n\t"
        "s_nop 1\n\t"
        "v_permlane16_swap_b32_e32 v3, v1\n\t"
        "s_waitcnt lgkmcnt(0)\n\t"
        "s_barrier\n\t"
        "ds_read_b64_tr_b16 v[72:73], v32 offset:0\n\t"
        "v_max_f32_e32 v2, v3, v1\n\t"
        "ds_read_b64_tr_b16 v[74:75], v32 offset:128\n\t"
        "v_max_f32_e32 v3, v3, v1\n\t"
        "ds_read_b64_tr_b16 v[76:77], v32 offset:1536\n\t"
        "s_nop 0\n\t"
        "v_permlane32_swap_b32_e32 v2, v3\n\t"
        "ds_read_b64_tr_b16 v[78:79], v32 offset:1664\n\t"
        "v_max_f32_e32 v0, v2, v3\n\t"
        "v_mul_f32_e32 v2, 0xbe0293ee, v0\n\t"
        "v_fmamk_f32 v68, v68, 0x3e0293ee, v2\n\t"
        "v_fmamk_f32 v69, v69, 0x3e0293ee, v2\n\t"
        "v_fmamk_f32 v70, v70, 0x3e0293ee, v2\n\t"
        "v_fmamk_f32 v71, v71, 0x3e0293ee, v2\n\t"
        "v_exp_f32_e32 v68, v68\n\t"
        "s_waitcnt lgkmcnt(2)\n\t"
        "v_smfmac_f32_16x16x64_bf16 v[64:67], v[72:75], v[16:23], v122\n\t"
        "s_waitcnt lgkmcnt(0)\n\t"
        "v_smfmac_f32_16x16x64_bf16 v[64:67], v[76:79], v[24:31], v122\n\t"
        "s_waitcnt vmcnt(4)\n\t"
        "v_exp_f32_e32 v69, v69\n\t"
        "v_exp_f32_e32 v70, v70\n\t"
        "v_cvt_pk_bf16_f32 v36, v68, v69\n\t"
        "v_exp_f32_e32 v71, v71\n\t"
        "s_nop 0\n\t"
        "v_mov_b32_dpp v38, v36 row_ror:8 row_mask:0xf bank_mask:0xf\n\t"
        "v_cvt_pk_bf16_f32 v37, v70, v71\n\t"
        "s_nop 1\n\t"
        "v_mov_b32_dpp v39, v37 row_ror:8 row_mask:0xf bank_mask:0xf\n\t"
        "v_pk_add_f32 v[64:65], v[64:65], v[66:67]\n\t"
        "s_nop 0\n\t"
        "v_mfma_f32_16x16x32_bf16 v[96:99], v[12:15], v[36:39], 0\n\t"
        "v_fmamk_f32 v66, v64, 0x3e0293ee, v2\n\t"
        "v_fmamk_f32 v67, v65, 0x3e0293ee, v2\n\t"
        "v_mfma_f32_16x16x32_bf16 v[40:43], v[112:115], v[36:39], 0\n\t"
        "v_max_f32_e32 v3, v66, v67\n\t"
        "v_exp_f32_e32 v66, v66\n\t"
        "v_mfma_f32_16x16x32_bf16 v[44:47], v[116:119], v[36:39], 0\n\t"
        "v_cmp_nge_f32_e32 vcc, 0x41a00000, v3\n\t"
        "v_exp_f32_e32 v67, v67\n\t"
        "v_mov_b32_e32 v10, 1.0\n\t"
        "v_cvt_pk_bf16_f32 v90, v66, v67\n\t"
        "v_cvt_pk_bf16_f32 v95, v66, v67\n\t"
        "s_cbranch_vccnz .Lfm%=\n\t"
        ".Lrm%=:\n\t"
        "s_waitcnt vmcnt(1)\n\t"
        "v_mfma_f32_16x16x16_bf16 v[96:99], v[12:13], v[90:91], v[96:99]\n\t"
        "v_max_u32_e32 v54, v88, v89\n\t"
        "v_mfma_f32_16x16x16_bf16 v[40:43], v[88:89], v[90:91], v[40:43]\n\t"
        "v_cmp_eq_u32_e32 vcc, -1, v54\n\t"
        "v_mfma_f32_16x16x16_bf16 v[44:47], v[88:89], v[94:95], v[44:47]\n\t"
        "s_nop 1\n\t"
        "v_mov_b32_e32 v55, v55\n\t"
        "s_nop 0\n\t"
        "v_rcp_f32_e32 v2, v96\n\t"
        "s_cbranch_vccnz .Lfb%=\n\t"
        ".Lrb%=:\n\t"
        "v_pk_mul_f32 v[40:41], v[40:41], v[2:3] op_sel_hi:[1,0]\n\t"
        "v_pk_mul_f32 v[42:43], v[42:43], v[2:3] op_sel_hi:[1,0]\n\t"
        "v_cvt_pk_bf16_f32 v100, v40, v41\n\t"
        "v_pk_mul_f32 v[44:45], v[44:45], v[2:3] op_sel_hi:[1,0]\n\t"
        "v_pk_mul_f32 v[46:47], v[46:47], v[2:3] op_sel_hi:[1,0]\n\t"
        "v_cvt_pk_bf16_f32 v101, v42, v43\n\t"
        "v_cvt_pk_bf16_f32 v102, v44, v45\n\t"
        "v_min_u32_e32 v100, -2, v100\n\t"
        "v_cvt_pk_bf16_f32 v103, v46, v47\n\t"
        "v_min_u32_e32 v102, -2, v102\n\t"
        "buffer_store_dwordx4 v[100:103], v33, %[srd], s63 offen sc1\n\t"
        "s_memrealtime s[76:77]\n\t"
        "v_mov_b32_e32 v8, -1\n\t"
        "v_mov_b32_e32 v9, -1\n\t"
        "v_mov_b32_e32 v10, -1\n\t"
        "v_mov_b32_e32 v11, -1\n\t"
        "v_permlane16_swap_b32_e32 v50, v51\n\t"
        "v_permlane16_swap_b32_e32 v88, v89\n\t"
        "v_mov_b32_e32 v14, s40\n\t"
        "s_waitcnt lgkmcnt(0)\n\t"
        "s_waitcnt vmcnt(1)\n\t"
        "s_mov_b32 s97, 0\n\t"
        ".Lsr%=:\n\t"
        "s_mov_b32 exec_hi, 0xffff\n\t"
        "v_subrev_u32_e32 v0, s41, v84\n\t"
        "v_subrev_u32_e32 v1, s41, v86\n\t"
        "v_max_i32_e32 v0, v0, v1\n\t"
        "v_max_u32_e32 v1, v85, v87\n\t"
        "v_cmp_eq_u32_e32 vcc, -1, v1\n\t"
        "v_max_i32_dpp v0, v0, v0 quad_perm:[1,0,3,2] row_mask:0xf bank_mask:0xf\n\t"
        "v_or_b32_e32 v2, v85, v87\n\t"
        "s_nop 0\n\t"
        "v_max_i32_dpp v0, v0, v0 quad_perm:[2,3,0,1] row_mask:0xf bank_mask:0xf\n\t"
        "s_nop 1\n\t"
        "v_max_i32_dpp v0, v0, v0 row_ror:4 row_mask:0xf bank_mask:0xf\n\t"
        "s_nop 1\n\t"
        "v_max_i32_dpp v0, v0, v0 row_ror:8 row_mask:0xf bank_mask:0xf\n\t"
        "s_cbranch_vccnz .Lsf%=\n\t"
        "v_readlane_b32 s85, v0, 0\n\t"
        "v_readlane_b32 s81, v0, 16\n\t"
        "v_readlane_b32 s56, v0, 32\n\t"
        "v_and_b32_e32 v1, 1, v2\n\t"
        "v_cmp_ne_u32_e64 s[86:87], 0, v1\n\t"
        "v_and_b32_e32 v1, 2, v2\n\t"
        "v_cmp_ne_u32_e64 s[88:89], 0, v1\n\t"
        "v_and_b32_e32 v1, 4, v2\n\t"
        "v_cmp_ne_u32_e64 s[90:91], 0, v1\n\t"
        "s_max_i32 s56, s56, s81\n\t"
        "s_add_u32 s92, s85, s41\n\t"
        "s_sub_u32 s92, s92, s43\n\t"
        "s_add_i32 s93, s56, s47\n\t"
        "s_mov_b32 s44, s46\n\t"
        "s_mov_b32 s46, s45\n\t"
        "s_mov_b32 s94, s48\n\t"
        "s_mov_b32 s43, s41\n\t"
        "s_cmp_gt_i32 s56, s54\n\t"
        "s_cbranch_scc1 .Lmv%=\n\t"
        "s_mov_b32 s37, 0\n\t"
        "s_mov_b32 s48, 0\n\t"
        "s_mov_b32 s95, 0\n\t"
        ".Lsd%=:\n\t"
        "s_mov_b32 exec_hi, -1\n\t"
        "s_add_u32 s42, s43, s46\n\t"
        "s_sleep %c[HSLP]\n\t"
        "buffer_load_dwordx2 v[48:49], off, %[wsd], s64 sc1\n\t"
        "buffer_load_dwordx4 v[104:107], v120, %[wsd], s69 offen sc1\n\t"
        "buffer_load_dwordx4 v[108:111], v120, %[wsd], s70 offen sc1\n\t"
        "buffer_store_dwordx2 v[50:51], v34, %[wsd], s72 offen sc1\n\t"
        "buffer_store_dwordx2 v[88:89], v34, %[wsd], s72 offen offset:2048 sc1\n\t"
        "s_lshl_b32 s80, %[w], 8\n\t"
        "s_add_u32 s80, s80, s61\n\t"
        "s_add_u32 s80, s80, 24576\n\t"
        "buffer_store_dwordx2 v[8:9], v34, %[srd], s80 offen sc1\n\t"
        "s_cmp_eq_u32 %[w], 0\n\t"
        "s_cselect_b32 exec_hi, 0, -1\n\t"
        "buffer_store_dwordx4 v[8:11], v120, %[srd], s68 offen sc1\n\t"
        "s_mov_b32 exec_hi, -1\n\t"
        "s_mov_b64 exec, 1\n\t"
        "buffer_store_dword v14, off, %[wsd], s74 sc1\n\t"
        "s_mov_b64 exec, -1\n\t"
        "ds_read_b128 v[16:19], v121 offset:0\n\t"
        "ds_read_b128 v[20:23], v121 offset:1024\n\t"
        "ds_read_b128 v[24:27], v121 offset:2048\n\t"
        "ds_read_b128 v[28:31], v121 offset:3072\n\t"
        "ds_read_b128 v[32:35], v121 offset:4096\n\t"
        "ds_read_b128 v[36:39], v121 offset:5120\n\t"
        "ds_read_b128 v[40:43], v121 offset:6144\n\t"
        "ds_read_b128 v[44:47], v121 offset:7168\n\t"
        "ds_read_b128 v[56:59], v121 offset:8192\n\t"
        "ds_read_b128 v[60:63], v121 offset:9216\n\t"
        "ds_read_b128 v[64:67], v121 offset:10240\n\t"
        "ds_read_b128 v[68:71], v121 offset:11264\n\t"
        "ds_read_b128 v[72:75], v121 offset:12288\n\t"
        "ds_read_b128 v[76:79], v121 offset:13312\n\t"
        "ds_read_b128 v[80:83], v121 offset:14336\n\t"
        "ds_read_b128 v[84:87], v121 offset:15360\n\t"
        "s_cmp_ge_u32 s40, %c[iCT0]\n\t"
        "s_cselect_b32 s85, 1, 0\n\t"
        "s_cmp_lg_u64 s[86:87], 0\n\t"
        "s_cselect_b32 s86, s85, 0\n\t"
        "s_cmp_lg_u64 s[88:89], 0\n\t"
        "s_cselect_b32 s87, s85, 0\n\t"
        "s_cmp_lg_u64 s[90:91], 0\n\t"
        "s_cselect_b32 s88, s85, 0\n\t"
        "s_or_b32 s80, s94, s86\n\t"
        "s_or_b32 s80, s80, s87\n\t"
        "s_andn2_b32 s90, s85, s80\n\t"
        "s_cmp_le_i32 s92, s44\n\t"
        "s_cselect_b32 s80, s85, 0\n\t"
        "s_andn2_b32 s91, s80, s88\n\t"
        "s_and_b32 s80, s40, %c[LDEC]\n\t"
        "s_cselect_b32 s89, 0, 1\n\t"
        "s_andn2_b32 s80, s86, s94\n\t"
        "s_cmp_lt_i32 s51, %c[LA24]\n\t"
        "s_cselect_b32 s80, s80, 0\n\t"
        "s_lshl2_add_u32 s51, s80, s51\n\t"
        "s_sub_i32 s51, s51, s89\n\t"
        "s_max_i32 s51, s51, %c[LA]\n\t"
        "s_lshl_b32 s80, s93, 1\n\t"
        "s_sub_i32 s81, s80, s53\n\t"
        "s_ashr_i32 s82, s81, %c[UPSH]\n\t"
        "s_max_i32 s82, s82, %c[TRUP]\n\t"
        "s_add_i32 s82, s82, s53\n\t"
        "s_min_i32 s82, s82, s80\n\t"
        "s_ashr_i32 s83, s81, %c[DNSH]\n\t"
        "s_min_i32 s83, s83, -%c[TRDN]\n\t"
        "s_add_i32 s83, s83, s53\n\t"
        "s_max_i32 s83, s83, s80\n\t"
        "s_cmp_gt_i32 s81, 0\n\t"
        "s_cselect_b32 s82, s82, s83\n\t"
        "s_cmp_lg_u32 s91, 0\n\t"
        "s_cselect_b32 s53, s82, s53\n\t"
        "s_add_i32 s80, s53, 1\n\t"
        "s_ashr_i32 s80, s80, 1\n\t"
        "s_add_i32 s47, s80, s51\n\t"
        "s_add_u32 s81, s47, %c[AUP]\n\t"
        "s_min_i32 s80, s95, s81\n\t"
        "s_max_i32 s47, s47, s80\n\t"
        "s_add_u32 s41, s42, s47\n\t"
        "s_cmp_eq_u32 s40, 0\n\t"
        "s_cselect_b32 s51, %c[LA], s51\n\t"
        "v_mov_b32_e32 v88, 0\n\t"
        "v_mov_b32_e32 v89, 0\n\t"
        "v_mov_b32_e32 v90, 0\n\t"
        "v_mov_b32_e32 v91, 0\n\t"
        "v_mov_b32_e32 v92, 0\n\t"
        "v_mov_b32_e32 v93, 0\n\t"
        "v_mov_b32_e32 v94, 0\n\t"
        "v_mov_b32_e32 v95, 0\n\t"
        "v_mov_b32_e32 v96, 0\n\t"
        "v_mov_b32_e32 v97, 0\n\t"
        "v_mov_b32_e32 v98, 0\n\t"
        "v_mov_b32_e32 v99, 0\n\t"
        "v_mov_b32_e32 v100, 0\n\t"
        "v_mov_b32_e32 v101, 0\n\t"
        "v_mov_b32_e32 v102, 0\n\t"
        "v_mov_b32_e32 v103, 0\n\t"
        "s_add_u32 s40, s40, 1\n\t"
        "v_mov_b32_e32 v6, 30720\n\t"
        "ds_read_b64 v[6:7], v6\n\t"
        "s_waitcnt lgkmcnt(0)\n\t"
        "v_readfirstlane_b32 s80, v6\n\t"
        "v_readfirstlane_b32 s81, v7\n\t"
        "s_cmp_eq_u32 s80, s40\n\t"
        "s_cselect_b32 s84, 1, 0\n\t"
        "s_cmp_eq_u32 s81, s40\n\t"
        "s_cselect_b32 s81, 2, 0\n\t"
        "s_or_b32 s84, s84, s81\n\t"
        "s_mov_b32 s85, s79\n\t"
        "s_and_b32 s80, s40, 3\n\t"
        "s_lshl_b32 s80, s80, 17\n\t"
        "s_add_u32 s57, s80, %[nA]\n\t"
        "s_add_u32 s58, s57, 0x1000\n\t"
        "s_add_u32 s59, s57, 0x8000\n\t"
        "s_add_u32 s60, s57, 0x9000\n\t"
        "s_and_b32 s80, s40, 3\n\t"
        "s_lshl_b32 s80, s80, 17\n\t"
        "s_add_u32 s63, s80, %[nC]\n\t"
        "s_add_u32 s66, s80, %[nS]\n\t"
        "s_add_u32 s81, s80, 0x20000\n\t"
        "s_and_b32 s81, s81, 0x60000\n\t"
        "s_add_u32 s67, s81, %[nW]\n\t"
        "s_add_u32 s79, s81, %[nT]\n\t"
        "s_cmp_eq_u32 %[w], 2\n\t"
        "s_cselect_b32 s81, s80, s81\n\t"
        "s_xor_b32 s81, s81, 0x40000\n\t"
        "s_add_u32 s68, s81, %[pob]\n\t"
        "s_lshl_b32 s72, s40, 17\n\t"
        "s_add_u32 s72, s72, %[kvo]\n\t"
        "v_lshlrev_b32_e32 v51, 6, v120\n\t"
        "s_cmp_eq_u32 %[w], 0\n\t"
        "s_cselect_b32 s73, s85, s36\n\t"
        "s_cselect_b32 s74, %[pra], s36\n\t"
        "v_mov_b32_e32 v50, s40\n\t"
        "s_cmp_ge_u32 s40, 60\n\t"
        "s_cselect_b32 s65, s74, s36\n\t"
        "s_mov_b32 s80, s61\n\t"
        "s_mov_b32 s61, s62\n\t"
        "s_mov_b32 s62, s80\n\t"
        "s_waitcnt vmcnt(7)\n\t"
        "s_cmp_eq_u32 s64, s36\n\t"
        "s_cbranch_scc1 .Lk11%=\n\t"
        "s_mov_b32 s97, 0\n\t"
        ".Lk01%=:\n\t"
        "v_readfirstlane_b32 s82, v48\n\t"
        "v_readfirstlane_b32 s83, v49\n\t"
        "s_xor_b32 s82, s82, %[tag]\n\t"
        "s_xor_b32 s83, s83, %[tag]\n\t"
        "s_or_b32 s82, s82, s83\n\t"
        "s_cbranch_scc0 .Lk11%=\n\t"
        "s_sleep 3\n\t"
        "buffer_load_dwordx2 v[48:49], off, %[wsd], s64 sc1\n\t"
        "s_add_u32 s97, s97, 1\n\t"
        "s_cmp_lt_u32 s97, 0x200000\n\t"
        "s_cbranch_scc0 .Lgu%=\n\t"
        "s_waitcnt vmcnt(0)\n\t"
        "s_branch .Lk01%=\n\t"
        ".Lk11%=:\n\t"
        "v_mov_b32_e32 v48, s76\n\t"
        "v_mov_b32_e32 v49, s84\n\t"
        "s_branch .Lloop%=\n\t"
        ".Lmv%=:\n\t"
        ".Lhk0%=:\n\t"
        "s_cbranch_execz .Lhk1%=\n\t"
        "s_mov_b32 s48, 1\n\t"
        "s_cmp_eq_u32 s54, %c[iTOL]\n\t"
        "s_cbranch_scc1 .Lm1%=\n\t"
        "s_max_i32 s80, s85, s56\n\t"
        "s_add_u32 s43, s41, s80\n\t"
        "s_add_u32 s43, s43, %c[iPEN0]\n\t"
        ".Lhk1%=:\n\t"
        "s_cbranch_execz .Lhk2%=\n\t"
        "s_mov_b32 s54, %c[iTOL]\n\t"
        "s_add_u32 s46, s46, %c[iX0C]\n\t"
        "s_mov_b32 s95, 0\n\t"
        "s_add_u32 s51, s51, %c[iX0A]\n\t"
        "v_and_b32_e32 v0, 3, v84\n\t"
        "v_and_b32_e32 v1, 3, v86\n\t"
        ".Lhk2%=:\n\t"
        "s_cbranch_execz .Lhk3%=\n\t"
        "v_lshlrev_b32_e64 v0, v0, 1\n\t"
        "v_lshlrev_b32_e64 v1, v1, 1\n\t"
        "v_or_b32_e32 v0, v0, v1\n\t"
        "s_mov_b32 s80, 0\n\t"
        "v_and_b32_e32 v1, 1, v0\n\t"
        "v_cmp_ne_u32_e32 vcc, 0, v1\n\t"
        ".Lhk3%=:\n\t"
        "s_cbranch_execz .Lhk4%=\n\t"
        "s_nop 1\n\t"
        "s_cmp_lg_u64 vcc, 0\n\t"
        "s_cselect_b32 s81, 2, 0\n\t"
        "s_or_b32 s80, s80, s81\n\t"
        "v_and_b32_e32 v1, 2, v0\n\t"
        "v_cmp_ne_u32_e32 vcc, 0, v1\n\t"
        ".Lhk4%=:\n\t"
        "s_cbranch_execz .Lhk5%=\n\t"
        "s_nop 1\n\t"
        "s_cmp_lg_u64 vcc, 0\n\t"
        "s_cselect_b32 s81, 4, 0\n\t"
        "s_or_b32 s80, s80, s81\n\t"
        "v_and_b32_e32 v1, 4, v0\n\t"
        "v_cmp_ne_u32_e32 vcc, 0, v1\n\t"
        ".Lhk5%=:\n\t"
        "s_cbranch_execz .Lhk6%=\n\t"
        "s_nop 1\n\t"
        "s_cmp_lg_u64 vcc, 0\n\t"
        "s_cselect_b32 s81, 8, 0\n\t"
        "s_or_b32 s80, s80, s81\n\t"
        "v_and_b32_e32 v1, 8, v0\n\t"
        "v_cmp_ne_u32_e32 vcc, 0, v1\n\t"
        ".Lhk6%=:\n\t"
        "s_cbranch_execz .Lhk7%=\n\t"
        "s_nop 1\n\t"
        "s_cmp_lg_u64 vcc, 0\n\t"
        "s_cselect_b32 s81, 16, 0\n\t"
        "s_or_b32 s80, s80, s81\n\t"
        "s_lshr_b32 s80, 0x1adf1210, s80\n\t"
        "s_and_b32 s55, s80, 3\n\t"
        ".Lhk7%=:\n\t"
        "s_cbranch_execz .Lhk8%=\n\t"
        "s_branch .Lal%=\n\t"
        ".Lm1%=:\n\t"
        "s_add_u32 s80, s37, %c[iPEN2]\n\t"
        "s_min_i32 s80, s80, %c[iPENM]\n\t"
        "s_cmp_lg_u32 s37, 0\n\t"
        "s_cselect_b32 s37, s80, %c[iPEN]\n\t"
        "s_add_u32 s80, s56, s37\n\t"
        ".Lhk8%=:\n\t"
        "s_cbranch_execz .Lhk9%=\n\t"
        "s_max_i32 s80, s80, %c[iPMIN]\n\t"
        "s_add_u32 s43, s41, s80\n\t"
        "s_add_u32 s95, s93, %c[LA]\n\t"
        "s_add_u32 s46, s46, %c[iXR]\n\t"
        ".Lal%=:\n\t"
        "s_sub_u32 s80, s55, s43\n\t"
        "s_and_b32 s80, s80, %c[iPHM]\n\t"
        ".Lhk9%=:\n\t"
        "s_cbranch_execz .Lhk10%=\n\t"
        "s_add_u32 s43, s43, s80\n\t"
        "s_branch .Lsd%=\n\t"
        ".Lsf%=:\n\t"
        "s_mov_b32 exec_hi, -1\n\t"
        "s_sleep 2\n\t"
        "buffer_load_dwordx4 v[84:87], v120, %[srd], s66 offen sc1\n\t"
        "s_add_u32 s97, s97, 1\n\t"
        ".Lhk10%=:\n\t"
        "s_cbranch_execz .Lhk11%=\n\t"
        "s_cmp_lt_u32 s97, 0x100000\n\t"
        "s_cbranch_scc0 .Lgu%=\n\t"
        "s_waitcnt vmcnt(0)\n\t"
        "s_branch .Lsr%=\n\t"
        ".Lf1%=:\n\t"
        "v_max_u32_e32 v54, v0, v2\n\t"
        "v_max3_u32 v54, v54, v1, v3\n\t"
        ".Lhk11%=:\n\t"
        "s_cbranch_execz .Lhk12%=\n\t"
        "v_max3_u32 v54, v54, v4, v6\n\t"
        "v_max3_u32 v54, v54, v5, v7\n\t"
        "v_max3_u32 v54, v54, v8, v10\n\t"
        "v_max3_u32 v54, v54, v9, v11\n\t"
        "s_sleep %c[RSLP]\n\t"
        "buffer_load_dwordx4 v[0:3], v120, %[srd], s57 offen sc1\n\t"
        ".Lhk12%=:\n\t"
        "s_cbranch_execz .Lhk13%=\n\t"
        "buffer_load_dwordx4 v[4:7], v120, %[srd], s58 offen sc1\n\t"
        "buffer_load_dwordx4 v[8:11], v120, %[srd], s59 offen sc1\n\t"
        "buffer_load_dwordx4 v[12:15], v120, %[srd], s60 offen sc1\n\t"
        "buffer_store_dword v0, off, %[wsd], s36\n\t"
        "buffer_store_dword v0, off, %[wsd], s36\n\t"
        "buffer_store_dword v0, off, %[wsd], s36\n\t"
        ".Lhk13%=:\n\t"
        "s_cbranch_execz .Lhk14%=\n\t"
        "ds_read_b128 v[92:95], v120 offset:0\n\t"
        "ds_read_b128 v[68:71], v120 offset:4096\n\t"
        "v_max3_u32 v54, v54, v16, v18\n\t"
        "v_max3_u32 v54, v54, v17, v19\n\t"
        "v_max3_u32 v54, v54, v20, v22\n\t"
        "v_max3_u32 v54, v54, v21, v23\n\t"
        ".Lhk14%=:\n\t"
        "s_cbranch_execz .Lhk15%=\n\t"
        "v_max3_u32 v54, v54, v24, v26\n\t"
        "v_max3_u32 v54, v54, v25, v27\n\t"
        "v_max3_u32 v54, v54, v28, v30\n\t"
        "v_max3_u32 v54, v54, v29, v31\n\t"
        "v_max3_u32 v54, v54, v32, v34\n\t"
        "v_max3_u32 v54, v54, v33, v35\n\t"
        ".Lhk15%=:\n\t"
        "s_cbranch_execz .Lhk16%=\n\t"
        "v_max3_u32 v54, v54, v36, v38\n\t"
        "v_max3_u32 v54, v54, v37, v39\n\t"
        "v_max3_u32 v54, v54, v40, v42\n\t"
        "v_max3_u32 v54, v54, v41, v43\n\t"
        "v_max3_u32 v54, v54, v44, v46\n\t"
        "v_max3_u32 v54, v54, v45, v47\n\t"
        ".Lhk16%=:\n\t"
        "s_cbranch_execz .Lhk17%=\n\t"
        "v_max3_u32 v54, v54, v48, v50\n\t"
        "v_max3_u32 v54, v54, v49, v51\n\t"
        "s_waitcnt lgkmcnt(0)\n\t"
        "v_max3_u32 v54, v54, v92, v94\n\t"
        "v_max3_u32 v54, v54, v93, v95\n\t"
        "v_max3_u32 v54, v54, v68, v70\n\t"
        ".Lhk17%=:\n\t"
        "s_cbranch_execz .Lhk18%=\n\t"
        "v_max3_u32 v54, v54, v69, v71\n\t"
        "ds_read_b128 v[92:95], v120 offset:8192\n\t"
        "ds_read_b128 v[68:71], v120 offset:12288\n\t"
        "s_waitcnt lgkmcnt(0)\n\t"
        "v_max3_u32 v54, v54, v92, v94\n\t"
        "v_max3_u32 v54, v54, v93, v95\n\t"
        ".Lhk18%=:\n\t"
        "s_cbranch_execz .Lhk19%=\n\t"
        "v_max3_u32 v54, v54, v68, v70\n\t"
        "v_max3_u32 v54, v54, v69, v71\n\t"
        "v_cmp_eq_u32_e32 vcc, -1, v54\n\t"
        "s_nop 1\n\t"
        "v_mov_b32_e32 v55, v55\n\t"
        "s_nop 0\n\t"
        ".Lhk19%=:\n\t"
        "s_cbranch_execz .Lhk20%=\n\t"
        "s_cbranch_vccz .Lf2%=\n\t"
        "ds_read_b128 v[16:19], v121 offset:0\n\t"
        "ds_read_b128 v[20:23], v121 offset:1024\n\t"
        "ds_read_b128 v[24:27], v121 offset:2048\n\t"
        "ds_read_b128 v[28:31], v121 offset:3072\n\t"
        "ds_read_b128 v[32:35], v121 offset:4096\n\t"
        ".Lhk20%=:\n\t"
        "s_cbranch_execz .Lhk21%=\n\t"
        "ds_read_b128 v[36:39], v121 offset:5120\n\t"
        "ds_read_b128 v[40:43], v121 offset:6144\n\t"
        "ds_read_b128 v[44:47], v121 offset:7168\n\t"
        "ds_read_b128 v[56:59], v121 offset:8192\n\t"
        "ds_read_b128 v[60:63], v121 offset:9216\n\t"
        "ds_read_b128 v[64:67], v121 offset:10240\n\t"
        ".Lhk21%=:\n\t"
        "s_cbranch_execz .Lhk22%=\n\t"
        "ds_read_b128 v[68:71], v121 offset:11264\n\t"
        "ds_read_b128 v[72:75], v121 offset:12288\n\t"
        "ds_read_b128 v[76:79], v121 offset:13312\n\t"
        "ds_read_b128 v[80:83], v121 offset:14336\n\t"
        "ds_read_b128 v[84:87], v121 offset:15360\n\t"
        "s_add_u32 s80, s40, 1\n\t"
        ".Lhk22%=:\n\t"
        "s_cbranch_execz .Lhk23%=\n\t"
        "v_mov_b32_e32 v54, 30720\n\t"
        "v_mov_b32_e32 v55, s80\n\t"
        "ds_write_b32 v54, v55\n\t"
        "s_add_u32 s97, s97, 1\n\t"
        "s_cmp_lt_u32 s97, 0x100000\n\t"
        "s_cbranch_scc0 .Lgu%=\n\t"
        ".Lhk23%=:\n\t"
        "s_cbranch_execz .Lhk24%=\n\t"
        "s_mov_b32 s65, s36\n\t"
        "v_mov_b32_e32 v88, 0\n\t"
        "v_mov_b32_e32 v89, 0\n\t"
        "v_mov_b32_e32 v90, 0\n\t"
        "v_mov_b32_e32 v91, 0\n\t"
        "v_mov_b32_e32 v92, 0\n\t"
        ".Lhk24%=:\n\t"
        "s_cbranch_execz .Lhk25%=\n\t"
        "v_mov_b32_e32 v93, 0\n\t"
        "v_mov_b32_e32 v94, 0\n\t"
        "v_mov_b32_e32 v95, 0\n\t"
        "v_mov_b32_e32 v96, 0\n\t"
        "v_mov_b32_e32 v97, 0\n\t"
        "v_mov_b32_e32 v98, 0\n\t"
        ".Lhk25%=:\n\t"
        "s_cbranch_execz .Lhk26%=\n\t"
        "v_mov_b32_e32 v99, 0\n\t"
        "v_mov_b32_e32 v100, 0\n\t"
        "v_mov_b32_e32 v101, 0\n\t"
        "v_mov_b32_e32 v102, 0\n\t"
        "v_mov_b32_e32 v103, 0\n\t"
        "s_waitcnt lgkmcnt(0)\n\t"
        ".Lhk26%=:\n\t"
        "s_cbranch_execz .Lhk27%=\n\t"
        "s_barrier\n\t"
        "s_branch .Lgo%=\n\t"
        ".Lf2%=:\n\t"
        "s_waitcnt vmcnt(0)\n\t"
        "v_mov_b32_e32 v12, 0x3f803f80\n\t"
        "v_mov_b32_e32 v13, 0x3f803f80\n\t"
        "v_mov_b32_e32 v14, 0x3f803f80\n\t"
        ".Lhk27%=:\n\t"
        "s_cbranch_execz .Lhk28%=\n\t"
        "v_mov_b32_e32 v15, 0x3f803f80\n\t"
        "ds_read_b128 v[0:3], v124 offset:12288\n\t"
        "ds_read_b128 v[4:7], v125 offset:12288\n\t"
        "ds_read_b128 v[8:11], v126 offset:12288\n\t"
        "s_waitcnt lgkmcnt(0)\n\t"
        "s_branch .Lr1%=\n\t"
        ".Lfb%=:\n\t"
        ".Lhk28%=:\n\t"
        "s_cbranch_execz .Lhk29%=\n\t"
        "s_nop 7\n\t"
        "s_mov_b32 s97, 0\n\t"
        "s_add_u32 s80, s40, 1\n\t"
        "v_mov_b32_e32 v54, 30724\n\t"
        "v_mov_b32_e32 v55, s80\n\t"
        "ds_write_b32 v54, v55\n\t"
        ".Lfp%=:\n\t"
        ".Lhk29%=:\n\t"
        "s_cbranch_execz .Lhk30%=\n\t"
        "s_sleep %c[ASLP]\n\t"
        "buffer_load_dword v88, v127, %[srd], s61 offen offset:0 sc1\n\t"
        "buffer_load_dword v89, v127, %[srd], s61 offen offset:256 sc1\n\t"
        "s_add_u32 s97, s97, 1\n\t"
        "s_cmp_lt_u32 s97, 0x100000\n\t"
        "s_cbranch_scc0 .Lgu%=\n\t"
        ".Lhk30%=:\n\t"
        "s_cbranch_execz .Lhk31%=\n\t"
        "s_waitcnt vmcnt(0)\n\t"
        "v_max_u32_e32 v54, v88, v89\n\t"
        "v_cmp_eq_u32_e32 vcc, -1, v54\n\t"
        "s_nop 1\n\t"
        "v_mov_b32_e32 v55, v55\n\t"
        "s_nop 0\n\t"
        ".Lhk31%=:\n\t"
        "s_cbranch_execz .Lhk32%=\n\t"
        "s_cbranch_vccnz .Lfp%=\n\t"
        "v_mfma_f32_16x16x32_bf16 v[40:43], v[112:115], v[36:39], 0\n\t"
        "v_mfma_f32_16x16x32_bf16 v[44:47], v[116:119], v[36:39], 0\n\t"
        "s_nop 1\n\t"
        "v_mov_b32_e32 v55, v55\n\t"
        "s_nop 1\n\t"
        ".Lhk32%=:\n\t"
        "s_cbranch_execz .Lhk33%=\n\t"
        "v_mov_b32_e32 v55, v55\n\t"
        "s_nop 0\n\t"
        "v_pk_mul_f32 v[40:41], v[40:41], v[10:11] op_sel_hi:[1,0]\n\t"
        "v_pk_mul_f32 v[42:43], v[42:43], v[10:11] op_sel_hi:[1,0]\n\t"
        "v_pk_mul_f32 v[44:45], v[44:45], v[10:11] op_sel_hi:[1,0]\n\t"
        "v_pk_mul_f32 v[46:47], v[46:47], v[10:11] op_sel_hi:[1,0]\n\t"
        ".Lhk33%=:\n\t"
        "s_cbranch_execz .Lhk34%=\n\t"
        "v_mfma_f32_16x16x16_bf16 v[40:43], v[88:89], v[90:91], v[40:43]\n\t"
        "s_nop 0\n\t"
        "v_mfma_f32_16x16x16_bf16 v[44:47], v[88:89], v[94:95], v[44:47]\n\t"
        "s_nop 7\n\t"
        "s_branch .Lrb%=\n\t"
        ".Lfm%=:\n\t"
        "v_max3_f32 v3, v0, v64, v65\n\t"
        ".Lhk34%=:\n\t"
        "s_cbranch_execz .Lhk35%=\n\t"
        "v_max3_f32 v1, v0, v64, v65\n\t"
        "s_nop 1\n\t"
        "v_permlane16_swap_b32_e32 v3, v1\n\t"
        "v_max_f32_e32 v2, v3, v1\n\t"
        "v_max_f32_e32 v3, v3, v1\n\t"
        "s_nop 1\n\t"
        ".Lhk35%=:\n\t"
        "s_cbranch_execz .Lhk36%=\n\t"
        "v_permlane32_swap_b32_e32 v2, v3\n\t"
        "v_max_f32_e32 v2, v2, v3\n\t"
        "v_mul_f32_e32 v2, 0xbe0293ee, v2\n\t"
        "v_fmamk_f32 v10, v0, 0x3e0293ee, v2\n\t"
        "v_fmamk_f32 v66, v64, 0x3e0293ee, v2\n\t"
        "v_exp_f32_e32 v10, v10\n\t"
        ".Lhk36%=:\n\t"
        "s_cbranch_execz .Lhk37%=\n\t"
        "v_fmamk_f32 v67, v65, 0x3e0293ee, v2\n\t"
        "v_exp_f32_e32 v66, v66\n\t"
        "v_exp_f32_e32 v67, v67\n\t"
        "v_mul_f32_e32 v96, v96, v10\n\t"
        "v_pk_mul_f32 v[40:41], v[40:41], v[10:11] op_sel_hi:[1,0]\n\t"
        "v_pk_mul_f32 v[42:43], v[42:43], v[10:11] op_sel_hi:[1,0]\n\t"
        ".Lhk37%=:\n\t"
        "s_cbranch_execz .Lhk38%=\n\t"
        "v_pk_mul_f32 v[44:45], v[44:45], v[10:11] op_sel_hi:[1,0]\n\t"
        "v_pk_mul_f32 v[46:47], v[46:47], v[10:11] op_sel_hi:[1,0]\n\t"
        "v_cvt_pk_bf16_f32 v90, v66, v67\n\t"
        "v_cvt_pk_bf16_f32 v95, v66, v67\n\t"
        "s_nop 1\n\t"
        "s_branch .Lrm%=\n\t"
        ".Lta%=:\n\t"
        ".Lhk38%=:\n\t"
        "s_cbranch_execz .Lhk39%=\n\t"
        "s_waitcnt lgkmcnt(0)\n\t"
        "s_branch .Lxa%=\n\t"
        ".Lgu%=:\n\t"
        "s_mov_b64 exec, -1\n\t"
        "v_mov_b32_e32 v54, %c[ferr]\n\t"
        "v_mov_b32_e32 v55, 1\n\t"
        "buffer_store_dword v55, v54, %[wsd], 0 offen sc1\n\t"
        ".Lhk39%=:\n\t"
        "s_cbranch_execz .Lhk40%=\n\t"
        "s_endpgm\n\t"
        ".Lend%=:\n\t"
        "s_nop 0\n\t"
        "v_permlane16_swap_b32_e32 v50, v51\n\t"
        "v_mov_b32_e32 v0, 64\n\t"
        "buffer_store_dwordx2 v[50:51], v34, %[wsd], s72 offen sc1\n\t"
        "s_cmp_lg_u32 %[w], 0\n\t"
        ".Lhk40%=:\n\t"
        "s_cbranch_execz .Lhk41%=\n\t"
        "s_waitcnt vmcnt(0)\n\t"
        "s_barrier\n\t"
        "s_cbranch_scc1 .Le2%=\n\t"
        "s_mov_b64 exec, 1\n\t"
        "buffer_store_dword v0, off, %[wsd], %[pra] sc1\n\t"
        "s_mov_b64 exec, -1\n\t"
        ".Le2%=:\n\t"
        ".Lhk41%=:\n\t"
        "s_cbranch_execz .Lhk42%=\n\t"
        "s_endpgm\n\t"
        ".Lhk42%=:\n\t"
        "s_branch .Lhr%="
        : [nA] "+s"(nA), [nS] "+s"(nS), [nT] "+s"(nT), [nW] "+s"(nW), [nC] "+s"(nC), [b0] "+s"(b0), [pob] "+s"(pob)
        : [srd] "s"(rhd), [wsd] "s"(wsd), [w] "s"(w), [tag] "s"(tag), [kx] "s"(kx), [vx] "s"(vx), [kf] "s"(kf), [kvo] "s"(kvo), [cal] "s"(cal), [sq] "s"(sq), [sk] "s"(sk), [m0b] "s"(m0b),
          [pra] "s"(pr_a), [ferr] "i"(F_ERR), [iD1] "i"(D1), [iD2] "i"(D2), [LC] "i"(LC0), [LA] "i"(LA0), [LC24] "i"(LC0 + 24), [LA24] "i"(LA0 + 24), [E1] "i"(2 * (D1 - LC0)), [E2] "i"(2 * (D2 - LA0)),
          [iPEN] "i"(PEN), [iPEN2] "i"(PEN2), [iPENM] "i"(PEN + 2 * PEN2), [iPEN0] "i"(PEN0), [iTOL] "i"(TOL), [iCT0] "i"(CT0), [iX0C] "i"(X0C), [iX0A] "i"(X0A), [iPHM] "i"(PH_M),
          [iFT] "i"(-3), [iFN] "i"(FINE_N), [AUP] "i"(A_UP), [ASLP] "i"(A_SLP), [LDEC] "i"(L_DEC), [UPSH] "i"(UP_SH), [TRUP] "i"(TR_UP), [TRDN] "i"(TR_DN), [HSLP] "i"(H_SLP), [iPMIN] "i"(PMIN), [VSLP] "i"(V_SLP), [RSLP] "i"(R_SLP), [iVD] "i"(V_DEL), [iXR] "i"(XR), [VASL] "i"(VA_SLP), [CSLP] "i"(C_SLP), [DNSH] "i"(DN_SH), [NX0] "i"(NEARX), [NX1] "i"(8192 - NEARX), [iCS1] "i"(CS1), [iCS2] "i"(CS2), [NY0] "i"(NEARY), [NY1] "i"(16384 - NEARY),
          "{v[120:127]}"(pc), "{v[112:113]}"(pd), "{v96}"(dv), "{v[98:105]}"(pa)
        : "memory", "vcc", "scc", "m0", "s36", "s37", "s38", "s39", "s40", "s41", "s42", "s43", "s44", "s45", "s46", "s47", "s48", "s49", "s50", "s51", "s52", "s53", "s54", "s55", "s56", "s57", "s58", "s59", "s60", "s61", "s62", "s63", "s64", "s65", "s66", "s67", "s68", "s69", "s70", "s71", "s72", "s73", "s74", "s75", "s76", "s77", "s78", "s79", "s80", "s81", "s82", "s83", "s84", "s85", "s86", "s87", "s88", "s89", "s90", "s91", "s92", "s93", "s94", "s95", "s96", "s97", "s98", "s99");
    } else {
        const int cb = i32;
        const v4u sv = mk_srd(p.Wv, 1024 * 4096 * 2), so = mk_srd(p.Wo, 1024 * 4096 * 2);
        const unsigned key = KEYM & ((cb & 1) * 2 + G), tu = cb >> 2, m0b = w * 32768, f = (g & 1) | ((c >> 1) << 1);
        const unsigned t0 = m0b + (16 * (g >> 1) + 4 * (g & 1) + (c & 1) + 8 * (c >> 1)) * 128 + (f << 5) + (n16 & 3) * 8, t1 = m0b + (16 * g + 4 * (c >> 1) + (c & 1)) * 64 + ((c >> 1) << 5) + (n16 & 3) * 8;
        v8u pa;
        pa[0] = (64 * w + (l >> 3)) * 8192 + vi * 128 + (((l & 7) ^ ((l >> 5) << 1)) << 4); pa[1] = pa[0] ^ 64u;
        pa[2] = (1024 * w + (l >> 2)) * 2048 + cb * 64 + (((l & 3) ^ ((g & 1) << 1)) << 4);
#pragma unroll
        for (int q = 0; q < 4; ++q) pa[3 + q] = t0 ^ (((w + q) & 3) << 5);
        pa[7] = t1;
        const v2u pb = {t1 ^ 32u, (unsigned)((w * 1024 + cb * 32) * 2048 + l * 1024) + (((3u - (so.x >> 7)) & 7u) << 7)};
        const v16u pc = {(unsigned)tid * 16, (n16 & 2) ? 0xEEEEu : 0x4444u, (unsigned)((vi & 1) * 1024 + w * 256 + n16 * 16 + g * 4), (unsigned)(4096 + tid * 16),
                   w < 3 ? (unsigned)((cb >> 4) * 32768 + ((cb >> 1) & 7) * 1024 + ((cb & 1) * 2 + (l >> 5)) * 256 + G * 128 + (l & 31) * 4) : OOB,
                   tid < 3 ? (unsigned)(l * 1024 + (32 + vi) * 8) : OOB, tid == 192 ? pr_a : OOB, (unsigned)(L_V + 8192 + w * 512 + (g & 1) * 256 + (g >> 1) * 64 + n16 * 4),
                   (unsigned)(L_V + (((l >> 2) & 1) + ((l >> 3) & 2)) * 128 + (l >> 5) * 64 + (l & 8) * 4 + (l & 3) * 8), 0u,
                   (unsigned)(L_V + w * 2048 + l * 8), (unsigned)(L_V + ((w + 1) & 3) * 2048 + 512 + l * 8), (unsigned)(L_V + ((w + 2) & 3) * 2048 + 1024 + l * 8), (unsigned)(L_V + ((w + 3) & 3) * 2048 + 1536 + l * 8), 0u, 0u};
        if (tid < 2) ((lu32*)(size_t)(L_V + L_FL))[tid] = ~0u;
    asm volatile(
        "s_mov_b32 s82, 3\n\t"
        ".Ld0%=:\n\t"
        "s_xor_b32 s80, s82, %[key]\n\t"
        "s_mul_i32 s81, s80, 131072\n\t"
        "s_lshl_b32 s80, s80, 11\n\t"
        "s_add_u32 s80, s80, %[m0b]\n\t"
        "s_add_u32 m0, s80, 0\n\t"
        "s_add_u32 s81, s81, 0\n\t"
        "buffer_load_dwordx4 v96, %[sv], s81 offen lds\n\t"
        "s_add_u32 m0, m0, 0x400\n\t"
        "s_add_u32 s81, s81, 65536\n\t"
        "buffer_load_dwordx4 v97, %[sv], s81 offen lds\n\t"
        "s_sub_u32 s82, s82, 1\n\t"
        "s_cbranch_scc0 .Ld0%=\n\t"
        "s_mov_b32 s82, 3\n\t"
        ".Ld1%=:\n\t"
        "s_xor_b32 s80, s82, %[key]\n\t"
        "s_mul_i32 s81, s80, 131072\n\t"
        "s_lshl_b32 s80, s80, 11\n\t"
        "s_add_u32 s80, s80, %[m0b]\n\t"
        "s_add_u32 m0, s80, 8192\n\t"
        "s_add_u32 s81, s81, 2097152\n\t"
        "buffer_load_dwordx4 v96, %[sv], s81 offen lds\n\t"
        "s_add_u32 m0, m0, 0x400\n\t"
        "s_add_u32 s81, s81, 65536\n\t"
        "buffer_load_dwordx4 v97, %[sv], s81 offen lds\n\t"
        "s_sub_u32 s82, s82, 1\n\t"
        "s_cbranch_scc0 .Ld1%=\n\t"
        "s_cmp_eq_u32 %[tu], 0\n\t"
        "s_cselect_b32 s81, 0, 0x7fff0000\n\t"
        "buffer_load_dword v110, v105, %[so], s81 offen\n\t"
        "s_mov_b32 s82, 3\n\t"
        ".Ld2%=:\n\t"
        "s_xor_b32 s80, s82, %[key]\n\t"
        "s_mul_i32 s81, s80, 131072\n\t"
        "s_lshl_b32 s80, s80, 11\n\t"
        "s_add_u32 s80, s80, %[m0b]\n\t"
        "s_add_u32 m0, s80, 16384\n\t"
        "s_add_u32 s81, s81, 4194304\n\t"
        "buffer_load_dwordx4 v96, %[sv], s81 offen lds\n\t"
        "s_add_u32 m0, m0, 0x400\n\t"
        "s_add_u32 s81, s81, 65536\n\t"
        "buffer_load_dwordx4 v97, %[sv], s81 offen lds\n\t"
        "s_sub_u32 s82, s82, 1\n\t"
        "s_cbranch_scc0 .Ld2%=\n\t"
        "s_cmp_eq_u32 %[tu], 1\n\t"
        "s_cselect_b32 s81, 0, 0x7fff0000\n\t"
        "buffer_load_dword v110, v105, %[so], s81 offen\n\t"
        "s_mov_b32 s82, 3\n\t"
        ".Ld3%=:\n\t"
        "s_xor_b32 s80, s82, %[key]\n\t"
        "s_mul_i32 s81, s80, 131072\n\t"
        "s_lshl_b32 s80, s80, 11\n\t"
        "s_add_u32 s80, s80, %[m0b]\n\t"
        "s_add_u32 m0, s80, 24576\n\t"
        "s_add_u32 s81, s81, 6291456\n\t"
        "buffer_load_dwordx4 v96, %[sv], s81 offen lds\n\t"
        "s_add_u32 m0, m0, 0x400\n\t"
        "s_add_u32 s81, s81, 65536\n\t"
        "buffer_load_dwordx4 v97, %[sv], s81 offen lds\n\t"
        "s_sub_u32 s82, s82, 1\n\t"
        "s_cbranch_scc0 .Ld3%=\n\t"
        "s_waitcnt vmcnt(26)\n\t"
        "ds_read_b64_tr_b16 v[128:129], v99 offset:0\n\t"
        "ds_read_b64_tr_b16 v[130:131], v99 offset:256\n\t"
        "ds_read_b64_tr_b16 v[132:133], v99 offset:4096\n\t"
        "ds_read_b64_tr_b16 v[134:135], v99 offset:4352\n\t"
        "ds_read_b64_tr_b16 v[136:137], v100 offset:0\n\t"
        "ds_read_b64_tr_b16 v[138:139], v100 offset:256\n\t"
        "ds_read_b64_tr_b16 v[140:141], v100 offset:4096\n\t"
        "ds_read_b64_tr_b16 v[142:143], v100 offset:4352\n\t"
        "ds_read_b64_tr_b16 v[144:145], v101 offset:0\n\t"
        "ds_read_b64_tr_b16 v[146:147], v101 offset:256\n\t"
        "ds_read_b64_tr_b16 v[148:149], v101 offset:4096\n\t"
        "ds_read_b64_tr_b16 v[150:151], v101 offset:4352\n\t"
        "ds_read_b64_tr_b16 v[152:153], v102 offset:0\n\t"
        "ds_read_b64_tr_b16 v[154:155], v102 offset:256\n\t"
        "ds_read_b64_tr_b16 v[156:157], v102 offset:4096\n\t"
        "ds_read_b64_tr_b16 v[158:159], v102 offset:4352\n\t"
        "s_waitcnt lgkmcnt(0)\n\t"
        "s_cmp_eq_u32 %[tu], 2\n\t"
        "s_cselect_b32 s81, 0, 0x7fff0000\n\t"
        "buffer_load_dword v110, v105, %[so], s81 offen\n\t"
        "s_mov_b32 s82, 3\n\t"
        ".Ld4%=:\n\t"
        "s_xor_b32 s80, s82, %[key]\n\t"
        "s_mul_i32 s81, s80, 65536\n\t"
        "s_lshl_b32 s80, s80, 11\n\t"
        "s_add_u32 s80, s80, %[m0b]\n\t"
        "s_add_u32 m0, s80, 0\n\t"
        "s_add_u32 s81, s81, 0\n\t"
        "buffer_load_dwordx4 v98, %[so], s81 offen lds\n\t"
        "s_add_u32 m0, m0, 0x400\n\t"
        "s_add_u32 s81, s81, 32768\n\t"
        "buffer_load_dwordx4 v98, %[so], s81 offen lds\n\t"
        "s_sub_u32 s82, s82, 1\n\t"
        "s_cbranch_scc0 .Ld4%=\n\t"
        "s_waitcnt vmcnt(27)\n\t"
        "ds_read_b64_tr_b16 v[160:161], v99 offset:8192\n\t"
        "ds_read_b64_tr_b16 v[162:163], v99 offset:8448\n\t"
        "ds_read_b64_tr_b16 v[164:165], v99 offset:12288\n\t"
        "ds_read_b64_tr_b16 v[166:167], v99 offset:12544\n\t"
        "ds_read_b64_tr_b16 v[168:169], v100 offset:8192\n\t"
        "ds_read_b64_tr_b16 v[170:171], v100 offset:8448\n\t"
        "ds_read_b64_tr_b16 v[172:173], v100 offset:12288\n\t"
        "ds_read_b64_tr_b16 v[174:175], v100 offset:12544\n\t"
        "ds_read_b64_tr_b16 v[176:177], v101 offset:8192\n\t"
        "ds_read_b64_tr_b16 v[178:179], v101 offset:8448\n\t"
        "ds_read_b64_tr_b16 v[180:181], v101 offset:12288\n\t"
        "ds_read_b64_tr_b16 v[182:183], v101 offset:12544\n\t"
        "ds_read_b64_tr_b16 v[184:185], v102 offset:8192\n\t"
        "ds_read_b64_tr_b16 v[186:187], v102 offset:8448\n\t"
        "ds_read_b64_tr_b16 v[188:189], v102 offset:12288\n\t"
        "ds_read_b64_tr_b16 v[190:191], v102 offset:12544\n\t"
        "s_waitcnt lgkmcnt(0)\n\t"
        "s_cmp_eq_u32 %[tu], 3\n\t"
        "s_cselect_b32 s81, 0, 0x7fff0000\n\t"
        "buffer_load_dword v110, v105, %[so], s81 offen\n\t"
        "s_mov_b32 s82, 3\n\t"
        ".Ld5%=:\n\t"
        "s_xor_b32 s80, s82, %[key]\n\t"
        "s_mul_i32 s81, s80, 65536\n\t"
        "s_lshl_b32 s80, s80, 11\n\t"
        "s_add_u32 s80, s80, %[m0b]\n\t"
        "s_add_u32 m0, s80, 8192\n\t"
        "s_add_u32 s81, s81, 262144\n\t"
        "buffer_load_dwordx4 v98, %[so], s81 offen lds\n\t"
        "s_add_u32 m0, m0, 0x400\n\t"
        "s_add_u32 s81, s81, 32768\n\t"
        "buffer_load_dwordx4 v98, %[so], s81 offen lds\n\t"
        "s_sub_u32 s82, s82, 1\n\t"
        "s_cbranch_scc0 .Ld5%=\n\t"
        "s_waitcnt vmcnt(27)\n\t"
        "ds_read_b64_tr_b16 v[192:193], v99 offset:16384\n\t"
        "ds_read_b64_tr_b16 v[194:195], v99 offset:16640\n\t"
        "ds_read_b64_tr_b16 v[196:197], v99 offset:20480\n\t"
        "ds_read_b64_tr_b16 v[198:199], v99 offset:20736\n\t"
        "ds_read_b64_tr_b16 v[200:201], v100 offset:16384\n\t"
        "ds_read_b64_tr_b16 v[202:203], v100 offset:16640\n\t"
        "ds_read_b64_tr_b16 v[204:205], v100 offset:20480\n\t"
        "ds_read_b64_tr_b16 v[206:207], v100 offset:20736\n\t"
        "ds_read_b64_tr_b16 v[208:209], v101 offset:16384\n\t"
        "ds_read_b64_tr_b16 v[210:211], v101 offset:16640\n\t"
        "ds_read_b64_tr_b16 v[212:213], v101 offset:20480\n\t"
        "ds_read_b64_tr_b16 v[214:215], v101 offset:20736\n\t"
        "ds_read_b64_tr_b16 v[216:217], v102 offset:16384\n\t"
        "ds_read_b64_tr_b16 v[218:219], v102 offset:16640\n\t"
        "ds_read_b64_tr_b16 v[220:221], v102 offset:20480\n\t"
        "ds_read_b64_tr_b16 v[222:223], v102 offset:20736\n\t"
        "s_waitcnt lgkmcnt(0)\n\t"
        "s_cmp_eq_u32 %[tu], 4\n\t"
        "s_cselect_b32 s81, 0, 0x7fff0000\n\t"
        "buffer_load_dword v110, v105, %[so], s81 offen\n\t"
        "s_mov_b32 s82, 3\n\t"
        ".Ld6%=:\n\t"
        "s_xor_b32 s80, s82, %[key]\n\t"
        "s_mul_i32 s81, s80, 65536\n\t"
        "s_lshl_b32 s80, s80, 11\n\t"
        "s_add_u32 s80, s80, %[m0b]\n\t"
        "s_add_u32 m0, s80, 16384\n\t"
        "s_add_u32 s81, s81, 524288\n\t"
        "buffer_load_dwordx4 v98, %[so], s81 offen lds\n\t"
        "s_add_u32 m0, m0, 0x400\n\t"
        "s_add_u32 s81, s81, 32768\n\t"
        "buffer_load_dwordx4 v98, %[so], s81 offen lds\n\t"
        "s_sub_u32 s82, s82, 1\n\t"
        "s_cbranch_scc0 .Ld6%=\n\t"
        "s_waitcnt vmcnt(27)\n\t"
        "ds_read_b64_tr_b16 v[224:225], v99 offset:24576\n\t"
        "ds_read_b64_tr_b16 v[226:227], v99 offset:24832\n\t"
        "ds_read_b64_tr_b16 v[228:229], v99 offset:28672\n\t"
        "ds_read_b64_tr_b16 v[230:231], v99 offset:28928\n\t"
        "ds_read_b64_tr_b16 v[232:233], v100 offset:24576\n\t"
        "ds_read_b64_tr_b16 v[234:235], v100 offset:24832\n\t"
        "ds_read_b64_tr_b16 v[236:237], v100 offset:28672\n\t"
        "ds_read_b64_tr_b16 v[238:239], v100 offset:28928\n\t"
        "ds_read_b64_tr_b16 v[240:241], v101 offset:24576\n\t"
        "ds_read_b64_tr_b16 v[242:243], v101 offset:24832\n\t"
        "ds_read_b64_tr_b16 v[244:245], v101 offset:28672\n\t"
        "ds_read_b64_tr_b16 v[246:247], v101 offset:28928\n\t"
        "ds_read_b64_tr_b16 v[248:249], v102 offset:24576\n\t"
        "ds_read_b64_tr_b16 v[250:251], v102 offset:24832\n\t"
        "ds_read_b64_tr_b16 v[252:253], v102 offset:28672\n\t"
        "ds_read_b64_tr_b16 v[254:255], v102 offset:28928\n\t"
        "s_waitcnt lgkmcnt(0)\n\t"
        "s_cmp_eq_u32 %[tu], 5\n\t"
        "s_cselect_b32 s81, 0, 0x7fff0000\n\t"
        "buffer_load_dword v110, v105, %[so], s81 offen\n\t"
        "s_mov_b32 s82, 3\n\t"
        ".Ld7%=:\n\t"
        "s_xor_b32 s80, s82, %[key]\n\t"
        "s_mul_i32 s81, s80, 65536\n\t"
        "s_lshl_b32 s80, s80, 11\n\t"
        "s_add_u32 s80, s80, %[m0b]\n\t"
        "s_add_u32 m0, s80, 24576\n\t"
        "s_add_u32 s81, s81, 786432\n\t"
        "buffer_load_dwordx4 v98, %[so], s81 offen lds\n\t"
        "s_add_u32 m0, m0, 0x400\n\t"
        "s_add_u32 s81, s81, 32768\n\t"
        "buffer_load_dwordx4 v98, %[so], s81 offen lds\n\t"
        "s_sub_u32 s82, s82, 1\n\t"
        "s_cbranch_scc0 .Ld7%=\n\t"
        "s_mov_b64 exec, 0\n\t"
        "s_branch .Lhk0%=\n\t"
        ".Lhr%=:\n\t"
        "s_mov_b64 exec, -1\n\t"
        "s_waitcnt vmcnt(27)\n\t"
        "ds_read_b64_tr_b16 a[0:1], v103 offset:0\n\t"
        "ds_read_b64_tr_b16 a[2:3], v103 offset:128\n\t"
        "ds_read_b64_tr_b16 a[4:5], v103 offset:4096\n\t"
        "ds_read_b64_tr_b16 a[6:7], v103 offset:4224\n\t"
        "ds_read_b64_tr_b16 a[8:9], v104 offset:0\n\t"
        "ds_read_b64_tr_b16 a[10:11], v104 offset:128\n\t"
        "ds_read_b64_tr_b16 a[12:13], v104 offset:4096\n\t"
        "ds_read_b64_tr_b16 a[14:15], v104 offset:4224\n\t"
        "ds_read_b64_tr_b16 a[16:17], v103 offset:512\n\t"
        "ds_read_b64_tr_b16 a[18:19], v103 offset:640\n\t"
        "ds_read_b64_tr_b16 a[20:21], v103 offset:4608\n\t"
        "ds_read_b64_tr_b16 a[22:23], v103 offset:4736\n\t"
        "ds_read_b64_tr_b16 a[24:25], v104 offset:512\n\t"
        "ds_read_b64_tr_b16 a[26:27], v104 offset:640\n\t"
        "ds_read_b64_tr_b16 a[28:29], v104 offset:4608\n\t"
        "ds_read_b64_tr_b16 a[30:31], v104 offset:4736\n\t"
        "s_waitcnt lgkmcnt(0)\n\t"
        "s_cmp_eq_u32 %[tu], 6\n\t"
        "s_cselect_b32 s81, 0, 0x7fff0000\n\t"
        "buffer_load_dword v110, v105, %[so], s81 offen\n\t"
        "s_mov_b32 s82, 3\n\t"
        ".Ld8%=:\n\t"
        "s_xor_b32 s80, s82, %[key]\n\t"
        "s_mul_i32 s81, s80, 65536\n\t"
        "s_lshl_b32 s80, s80, 11\n\t"
        "s_add_u32 s80, s80, %[m0b]\n\t"
        "s_add_u32 m0, s80, 0\n\t"
        "s_add_u32 s81, s81, 1048576\n\t"
        "buffer_load_dwordx4 v98, %[so], s81 offen lds\n\t"
        "s_add_u32 m0, m0, 0x400\n\t"
        "s_add_u32 s81, s81, 32768\n\t"
        "buffer_load_dwordx4 v98, %[so], s81 offen lds\n\t"
        "s_sub_u32 s82, s82, 1\n\t"
        "s_cbranch_scc0 .Ld8%=\n\t"
        "s_waitcnt vmcnt(27)\n\t"
        "ds_read_b64_tr_b16 a[32:33], v103 offset:8192\n\t"
        "ds_read_b64_tr_b16 a[34:35], v103 offset:8320\n\t"
        "ds_read_b64_tr_b16 a[36:37], v103 offset:12288\n\t"
        "ds_read_b64_tr_b16 a[38:39], v103 offset:12416\n\t"
        "ds_read_b64_tr_b16 a[40:41], v104 offset:8192\n\t"
        "ds_read_b64_tr_b16 a[42:43], v104 offset:8320\n\t"
        "ds_read_b64_tr_b16 a[44:45], v104 offset:12288\n\t"
        "ds_read_b64_tr_b16 a[46:47], v104 offset:12416\n\t"
        "ds_read_b64_tr_b16 a[48:49], v103 offset:8704\n\t"
        "ds_read_b64_tr_b16 a[50:51], v103 offset:8832\n\t"
        "ds_read_b64_tr_b16 a[52:53], v103 offset:12800\n\t"
        "ds_read_b64_tr_b16 a[54:55], v103 offset:12928\n\t"
        "ds_read_b64_tr_b16 a[56:57], v104 offset:8704\n\t"
        "ds_read_b64_tr_b16 a[58:59], v104 offset:8832\n\t"
        "ds_read_b64_tr_b16 a[60:61], v104 offset:12800\n\t"
        "ds_read_b64_tr_b16 a[62:63], v104 offset:12928\n\t"
        "s_waitcnt lgkmcnt(0)\n\t"
        "s_cmp_eq_u32 %[tu], 7\n\t"
        "s_cselect_b32 s81, 0, 0x7fff0000\n\t"
        "buffer_load_dword v110, v105, %[so], s81 offen\n\t"
        "s_mov_b32 s82, 3\n\t"
        ".Ld9%=:\n\t"
        "s_xor_b32 s80, s82, %[key]\n\t"
        "s_mul_i32 s81, s80, 65536\n\t"
        "s_lshl_b32 s80, s80, 11\n\t"
        "s_add_u32 s80, s80, %[m0b]\n\t"
        "s_add_u32 m0, s80, 8192\n\t"
        "s_add_u32 s81, s81, 1310720\n\t"
        "buffer_load_dwordx4 v98, %[so], s81 offen lds\n\t"
        "s_add_u32 m0, m0, 0x400\n\t"
        "s_add_u32 s81, s81, 32768\n\t"
        "buffer_load_dwordx4 v98, %[so], s81 offen lds\n\t"
        "s_sub_u32 s82, s82, 1\n\t"
        "s_cbranch_scc0 .Ld9%=\n\t"
        "s_waitcnt vmcnt(27)\n\t"
        "ds_read_b64_tr_b16 a[64:65], v103 offset:16384\n\t"
        "ds_read_b64_tr_b16 a[66:67], v103 offset:16512\n\t"
        "ds_read_b64_tr_b16 a[68:69], v103 offset:20480\n\t"
        "ds_read_b64_tr_b16 a[70:71], v103 offset:20608\n\t"
        "ds_read_b64_tr_b16 a[72:73], v104 offset:16384\n\t"
        "ds_read_b64_tr_b16 a[74:75], v104 offset:16512\n\t"
        "ds_read_b64_tr_b16 a[76:77], v104 offset:20480\n\t"
        "ds_read_b64_tr_b16 a[78:79], v104 offset:20608\n\t"
        "ds_read_b64_tr_b16 a[80:81], v103 offset:16896\n\t"
        "ds_read_b64_tr_b16 a[82:83], v103 offset:17024\n\t"
        "ds_read_b64_tr_b16 a[84:85], v103 offset:20992\n\t"
        "ds_read_b64_tr_b16 a[86:87], v103 offset:21120\n\t"
        "ds_read_b64_tr_b16 a[88:89], v104 offset:16896\n\t"
        "ds_read_b64_tr_b16 a[90:91], v104 offset:17024\n\t"
        "ds_read_b64_tr_b16 a[92:93], v104 offset:20992\n\t"
        "ds_read_b64_tr_b16 a[94:95], v104 offset:21120\n\t"
        "buffer_load_dwordx4 v[0:3], v112, %[wsd], %[cal] offen sc1\n\t"
        "s_waitcnt lgkmcnt(0)\n\t"
        "s_mov_b32 s82, 3\n\t"
        ".Ld10%=:\n\t"
        "s_xor_b32 s80, s82, %[key]\n\t"
        "s_mul_i32 s81, s80, 65536\n\t"
        "s_lshl_b32 s80, s80, 11\n\t"
        "s_add_u32 s80, s80, %[m0b]\n\t"
        "s_add_u32 m0, s80, 16384\n\t"
        "s_add_u32 s81, s81, 1572864\n\t"
        "buffer_load_dwordx4 v98, %[so], s81 offen lds\n\t"
        "s_add_u32 m0, m0, 0x400\n\t"
        "s_add_u32 s81, s81, 32768\n\t"
        "buffer_load_dwordx4 v98, %[so], s81 offen lds\n\t"
        "s_sub_u32 s82, s82, 1\n\t"
        "s_cbranch_scc0 .Ld10%=\n\t"
        "s_waitcnt vmcnt(27)\n\t"
        "ds_read_b64_tr_b16 a[96:97], v103 offset:24576\n\t"
        "ds_read_b64_tr_b16 a[98:99], v103 offset:24704\n\t"
        "ds_read_b64_tr_b16 a[100:101], v103 offset:28672\n\t"
        "ds_read_b64_tr_b16 a[102:103], v103 offset:28800\n\t"
        "ds_read_b64_tr_b16 a[104:105], v104 offset:24576\n\t"
        "ds_read_b64_tr_b16 a[106:107], v104 offset:24704\n\t"
        "ds_read_b64_tr_b16 a[108:109], v104 offset:28672\n\t"
        "ds_read_b64_tr_b16 a[110:111], v104 offset:28800\n\t"
        "ds_read_b64_tr_b16 a[112:113], v103 offset:25088\n\t"
        "ds_read_b64_tr_b16 a[114:115], v103 offset:25216\n\t"
        "ds_read_b64_tr_b16 a[116:117], v103 offset:29184\n\t"
        "ds_read_b64_tr_b16 a[118:119], v103 offset:29312\n\t"
        "ds_read_b64_tr_b16 a[120:121], v104 offset:25088\n\t"
        "ds_read_b64_tr_b16 a[122:123], v104 offset:25216\n\t"
        "ds_read_b64_tr_b16 a[124:125], v104 offset:29184\n\t"
        "ds_read_b64_tr_b16 a[126:127], v104 offset:29312\n\t"
        "s_waitcnt lgkmcnt(0)\n\t"
        "s_mov_b32 s82, 3\n\t"
        ".Ld11%=:\n\t"
        "s_xor_b32 s80, s82, %[key]\n\t"
        "s_mul_i32 s81, s80, 65536\n\t"
        "s_lshl_b32 s80, s80, 11\n\t"
        "s_add_u32 s80, s80, %[m0b]\n\t"
        "s_add_u32 m0, s80, 24576\n\t"
        "s_add_u32 s81, s81, 1835008\n\t"
        "buffer_load_dwordx4 v98, %[so], s81 offen lds\n\t"
        "s_add_u32 m0, m0, 0x400\n\t"
        "s_add_u32 s81, s81, 32768\n\t"
        "buffer_load_dwordx4 v98, %[so], s81 offen lds\n\t"
        "s_sub_u32 s82, s82, 1\n\t"
        "s_cbranch_scc0 .Ld11%=\n\t"
        "s_waitcnt vmcnt(26)\n\t"
        "ds_read_b64_tr_b16 a[128:129], v103 offset:0\n\t"
        "ds_read_b64_tr_b16 a[130:131], v103 offset:128\n\t"
        "ds_read_b64_tr_b16 a[132:133], v103 offset:4096\n\t"
        "ds_read_b64_tr_b16 a[134:135], v103 offset:4224\n\t"
        "ds_read_b64_tr_b16 a[136:137], v104 offset:0\n\t"
        "ds_read_b64_tr_b16 a[138:139], v104 offset:128\n\t"
        "ds_read_b64_tr_b16 a[140:141], v104 offset:4096\n\t"
        "ds_read_b64_tr_b16 a[142:143], v104 offset:4224\n\t"
        "ds_read_b64_tr_b16 a[144:145], v103 offset:512\n\t"
        "ds_read_b64_tr_b16 a[146:147], v103 offset:640\n\t"
        "ds_read_b64_tr_b16 a[148:149], v103 offset:4608\n\t"
        "ds_read_b64_tr_b16 a[150:151], v103 offset:4736\n\t"
        "ds_read_b64_tr_b16 a[152:153], v104 offset:512\n\t"
        "ds_read_b64_tr_b16 a[154:155], v104 offset:640\n\t"
        "ds_read_b64_tr_b16 a[156:157], v104 offset:4608\n\t"
        "ds_read_b64_tr_b16 a[158:159], v104 offset:4736\n\t"
        "s_waitcnt lgkmcnt(0)\n\t"
        "s_waitcnt vmcnt(17)\n\t"
        "ds_read_b64_tr_b16 a[160:161], v103 offset:8192\n\t"
        "ds_read_b64_tr_b16 a[162:163], v103 offset:8320\n\t"
        "ds_read_b64_tr_b16 a[164:165], v103 offset:12288\n\t"
        "ds_read_b64_tr_b16 a[166:167], v103 offset:12416\n\t"
        "ds_read_b64_tr_b16 a[168:169], v104 offset:8192\n\t"
        "ds_read_b64_tr_b16 a[170:171], v104 offset:8320\n\t"
        "ds_read_b64_tr_b16 a[172:173], v104 offset:12288\n\t"
        "ds_read_b64_tr_b16 a[174:175], v104 offset:12416\n\t"
        "ds_read_b64_tr_b16 a[176:177], v103 offset:8704\n\t"
        "ds_read_b64_tr_b16 a[178:179], v103 offset:8832\n\t"
        "ds_read_b64_tr_b16 a[180:181], v103 offset:12800\n\t"
        "ds_read_b64_tr_b16 a[182:183], v103 offset:12928\n\t"
        "ds_read_b64_tr_b16 a[184:185], v104 offset:8704\n\t"
        "ds_read_b64_tr_b16 a[186:187], v104 offset:8832\n\t"
        "ds_read_b64_tr_b16 a[188:189], v104 offset:12800\n\t"
        "ds_read_b64_tr_b16 a[190:191], v104 offset:12928\n\t"
        "s_waitcnt lgkmcnt(0)\n\t"
        "s_waitcnt vmcnt(8)\n\t"
        "ds_read_b64_tr_b16 a[192:193], v103 offset:16384\n\t"
        "ds_read_b64_tr_b16 a[194:195], v103 offset:16512\n\t"
        "ds_read_b64_tr_b16 a[196:197], v103 offset:20480\n\t"
        "ds_read_b64_tr_b16 a[198:199], v103 offset:20608\n\t"
        "ds_read_b64_tr_b16 a[200:201], v104 offset:16384\n\t"
        "ds_read_b64_tr_b16 a[202:203], v104 offset:16512\n\t"
        "ds_read_b64_tr_b16 a[204:205], v104 offset:20480\n\t"
        "ds_read_b64_tr_b16 a[206:207], v104 offset:20608\n\t"
        "ds_read_b64_tr_b16 a[208:209], v103 offset:16896\n\t"
        "ds_read_b64_tr_b16 a[210:211], v103 offset:17024\n\t"
        "ds_read_b64_tr_b16 a[212:213], v103 offset:20992\n\t"
        "ds_read_b64_tr_b16 a[214:215], v103 offset:21120\n\t"
        "ds_read_b64_tr_b16 a[216:217], v104 offset:16896\n\t"
        "ds_read_b64_tr_b16 a[218:219], v104 offset:17024\n\t"
        "ds_read_b64_tr_b16 a[220:221], v104 offset:20992\n\t"
        "ds_read_b64_tr_b16 a[222:223], v104 offset:21120\n\t"
        "s_mov_b32 s97, 0\n\t"
        ".Lg0%=:\n\t"
        "v_cmp_ne_u32_e32 vcc, %[tag], v0\n\t"
        "v_cmp_ne_u32_e64 s[82:83], %[tag], v2\n\t"
        "s_nop 1\n\t"
        "s_or_b64 vcc, vcc, s[82:83]\n\t"
        "s_cbranch_scc0 .Lg1%=\n\t"
        "s_sleep 4\n\t"
        "buffer_load_dwordx4 v[0:3], v112, %[wsd], %[cal] offen sc1\n\t"
        "s_add_u32 s97, s97, 1\n\t"
        "s_cmp_lt_u32 s97, 0x100000\n\t"
        "s_cbranch_scc0 .Lgu%=\n\t"
        "s_waitcnt vmcnt(0)\n\t"
        "s_branch .Lg0%=\n\t"
        ".Lg1%=:\n\t"
        "s_mov_b32 s84, 0x55555555\n\t"
        "s_mov_b32 s85, s84\n\t"
        "v_and_b32_e32 v0, 1, v1\n\t"
        "v_and_b32_e32 v2, 1, v3\n\t"
        "v_cmp_ne_u32_e32 vcc, 0, v0\n\t"
        "v_cmp_ne_u32_e64 s[82:83], 0, v2\n\t"
        "s_nop 0\n\t"
        "s_and_b64 s[82:83], s[82:83], s[84:85]\n\t"
        "s_bcnt1_i32_b64 s87, s[82:83]\n\t"
        "s_and_b64 s[82:83], vcc, s[84:85]\n\t"
        "s_bcnt1_i32_b64 s86, s[82:83]\n\t"
        "s_andn2_b64 s[82:83], vcc, s[84:85]\n\t"
        "s_bcnt1_i32_b64 s88, s[82:83]\n\t"
        "v_and_b32_e32 v0, 2, v1\n\t"
        "v_and_b32_e32 v2, 2, v3\n\t"
        "v_cmp_ne_u32_e32 vcc, 0, v0\n\t"
        "v_cmp_ne_u32_e64 s[82:83], 0, v2\n\t"
        "s_nop 0\n\t"
        "s_and_b64 s[82:83], s[82:83], s[84:85]\n\t"
        "s_bcnt1_i32_b64 s90, s[82:83]\n\t"
        "s_and_b64 s[82:83], vcc, s[84:85]\n\t"
        "s_bcnt1_i32_b64 s89, s[82:83]\n\t"
        "s_andn2_b64 s[82:83], vcc, s[84:85]\n\t"
        "s_bcnt1_i32_b64 s91, s[82:83]\n\t"
        "s_add_u32 s80, s86, s87\n\t"
        "s_add_u32 s80, s80, s88\n\t"
        "s_cmp_ge_u32 s80, 48\n\t"
        "s_cselect_b32 s80, 8, 24\n\t"
        "s_cmp_gt_u32 s86, s80\n\t"
        "s_cselect_b32 s86, %c[NX1], %c[NX0]\n\t"
        "s_cmp_gt_u32 s89, 16\n\t"
        "s_cselect_b32 s81, %c[NY1], %c[NY0]\n\t"
        "s_or_b32 s86, s86, s81\n\t"
        "s_cmp_gt_u32 s87, s80\n\t"
        "s_cselect_b32 s87, %c[NX1], %c[NX0]\n\t"
        "s_cmp_gt_u32 s90, 16\n\t"
        "s_cselect_b32 s81, %c[NY1], %c[NY0]\n\t"
        "s_or_b32 s87, s87, s81\n\t"
        "s_cmp_gt_u32 s88, s80\n\t"
        "s_cselect_b32 s88, %c[NX1], %c[NX0]\n\t"
        "s_cmp_gt_u32 s91, 16\n\t"
        "s_cselect_b32 s81, %c[NY1], %c[NY0]\n\t"
        "s_or_b32 s88, s88, s81\n\t"
        "s_xor_b32 %[nA], %[nA], s86\n\t"
        "s_xor_b32 %[nW], %[nW], s86\n\t"
        "s_xor_b32 %[nC], %[nC], s87\n\t"
        "s_xor_b32 %[nS], %[nS], s88\n\t"
        "s_xor_b32 %[nT], %[nT], s88\n\t"
        "s_xor_b32 %[b0], %[b0], s88\n\t"
        "s_cmp_eq_u32 %[w], 1\n\t"
        "s_cselect_b32 s80, s87, s86\n\t"
        "s_cmp_eq_u32 %[w], 2\n\t"
        "s_cselect_b32 s80, s88, s80\n\t"
        "s_xor_b32 %[pob], %[pob], s80\n\t"
        "s_lshl_b32 s80, %[w], 10\n\t"
        "s_sub_u32 %[nS], %[nS], s80\n\t"
        "s_sub_u32 %[pob], %[pob], s80\n\t"
        "s_mov_b32 s36, 0xf0000000\n\t"
        "s_mov_b32 s38, 0xff00ff00\n\t"
        "s_mov_b32 s39, 0xff00ff00\n\t"
        "s_mov_b32 s40, 0\n\t"
        "s_mov_b32 s37, 0\n\t"
        "s_brev_b32 s54, 1\n\t"
        "s_mov_b32 s46, %c[iD1]\n\t"
        "s_mov_b32 s45, %c[iD1]\n\t"
        "s_mov_b32 s47, %c[iD2]\n\t"
        "s_mov_b32 s48, 1\n\t"
        "s_mov_b32 s50, %c[LC]\n\t"
        "s_mov_b32 s51, %c[LA]\n\t"
        "s_mov_b32 s52, %c[E1]\n\t"
        "s_mov_b32 s53, %c[E2]\n\t"
        "s_mov_b32 s43, 0\n\t"
        "s_mov_b32 s61, %[b0]\n\t"
        "s_add_u32 s62, %[b0], 0x40000\n\t"
        "s_mov_b32 s75, 162816\n\t"
        "s_waitcnt lgkmcnt(0)\n\t"
        "s_waitcnt vmcnt(0)\n\t"
        "ds_read_b64_tr_b16 a[224:225], v103 offset:24576\n\t"
        "ds_read_b64_tr_b16 a[226:227], v103 offset:24704\n\t"
        "ds_read_b64_tr_b16 a[228:229], v103 offset:28672\n\t"
        "ds_read_b64_tr_b16 a[230:231], v103 offset:28800\n\t"
        "ds_read_b64_tr_b16 a[232:233], v104 offset:24576\n\t"
        "ds_read_b64_tr_b16 a[234:235], v104 offset:24704\n\t"
        "ds_read_b64_tr_b16 a[236:237], v104 offset:28672\n\t"
        "ds_read_b64_tr_b16 a[238:239], v104 offset:28800\n\t"
        "ds_read_b64_tr_b16 a[240:241], v103 offset:25088\n\t"
        "ds_read_b64_tr_b16 a[242:243], v103 offset:25216\n\t"
        "ds_read_b64_tr_b16 a[244:245], v103 offset:29184\n\t"
        "ds_read_b64_tr_b16 a[246:247], v103 offset:29312\n\t"
        "ds_read_b64_tr_b16 a[248:249], v104 offset:25088\n\t"
        "ds_read_b64_tr_b16 a[250:251], v104 offset:25216\n\t"
        "ds_read_b64_tr_b16 a[252:253], v104 offset:29184\n\t"
        "ds_read_b64_tr_b16 a[254:255], v104 offset:29312\n\t"
        "s_waitcnt lgkmcnt(0)\n\t"
        "rec_v_code:\n\t"
        "s_and_b32 s80, s40, 3\n\t"
        "s_lshl_b32 s80, s80, 17\n\t"
        "s_add_u32 s57, s80, %[nA]\n\t"
        "s_add_u32 s58, s57, 0x1000\n\t"
        "s_add_u32 s59, s57, 0x8000\n\t"
        "s_add_u32 s60, s57, 0x9000\n\t"
        "v_mov_b32_e32 v92, -1\n\t"
        "v_mov_b32_e32 v93, -1\n\t"
        "v_mov_b32_e32 v94, -1\n\t"
        "v_mov_b32_e32 v95, -1\n\t"
        "v_mov_b32_e32 v48, 0\n\t"
        "v_mov_b32_e32 v49, 0\n\t"
        "v_mov_b32_e32 v50, 0\n\t"
        "v_mov_b32_e32 v51, 0\n\t"
        "v_mov_b32_e32 v52, 0\n\t"
        "v_mov_b32_e32 v53, 0\n\t"
        "v_mov_b32_e32 v54, 0\n\t"
        "v_mov_b32_e32 v55, 0\n\t"
        "v_mov_b32_e32 v56, 0\n\t"
        "v_mov_b32_e32 v57, 0\n\t"
        "v_mov_b32_e32 v58, 0\n\t"
        "v_mov_b32_e32 v59, 0\n\t"
        "v_mov_b32_e32 v60, 0\n\t"
        "v_mov_b32_e32 v61, 0\n\t"
        "v_mov_b32_e32 v62, 0\n\t"
        "v_mov_b32_e32 v63, 0\n\t"
        "s_barrier\n\t"
        "s_memrealtime s[76:77]\n\t"
        "s_mov_b32 s79, %[nT]\n\t"
        "v_mov_b32_e32 v86, 64\n\t"
        "s_add_u32 s61, %[b0], 0x40000\n\t"
        "s_mov_b32 s62, %[b0]\n\t"
        "s_sub_u32 s75, s75, 16\n\t"
        "s_waitcnt lgkmcnt(0)\n\t"
        "s_mov_b32 s41, s76\n\t"
        "s_mov_b32 s69, s76\n\t"
        ".p2align 6\n\t"
        ".Lloop%=:\n\t"
        "s_mov_b32 s97, 0\n\t"
        "s_movk_i32 s96, 0x1000\n\t"
        ".Lwa%=:\n\t"
        "s_memrealtime s[98:99]\n\t"
        "s_addk_i32 s96, -1\n\t"
        "s_cmp_lt_i32 s96, 0\n\t"
        "s_cbranch_scc1 .Lta%=\n\t"
        "s_waitcnt lgkmcnt(0)\n\t"
        "s_sub_i32 s98, s98, s69\n\t"
        "s_cmp_lt_i32 s98, %c[iFT]\n\t"
        "s_cbranch_scc1 .Lwa%=\n\t"
        "s_cmp_ge_i32 s98, 0\n\t"
        "s_cbranch_scc1 .Lxa%=\n\t"
        ".Lqa%=:\n\t"
        "s_nop %c[iFN]\n\t"
        "s_addk_i32 s98, 1\n\t"
        "s_cmp_lt_i32 s98, 0\n\t"
        "s_cbranch_scc1 .Lqa%=\n\t"
        ".Lxa%=:\n\t"
        "buffer_load_dwordx4 v[0:3], v112, %[srd], s57 offen sc1\n\t"
        "s_nop 7\n\t"
        "buffer_load_dwordx4 v[4:7], v112, %[srd], s58 offen sc1\n\t"
        "s_nop 7\n\t"
        "buffer_load_dwordx4 v[8:11], v112, %[srd], s59 offen sc1\n\t"
        "s_nop 7\n\t"
        "buffer_load_dwordx4 v[12:15], v112, %[srd], s60 offen sc1\n\t"
        "v_mov_b32_e32 v80, 161792\n\t"
        "ds_read_b64 v[80:81], v80\n\t"
        "s_waitcnt lgkmcnt(0)\n\t"
        "v_readfirstlane_b32 s80, v80\n\t"
        "v_readfirstlane_b32 s81, v81\n\t"
        "s_cmp_eq_u32 s80, s40\n\t"
        "s_cselect_b32 s84, 1, 0\n\t"
        "s_cmp_eq_u32 s81, s40\n\t"
        "s_cselect_b32 s81, 4, 0\n\t"
        "s_or_b32 s84, s84, s81\n\t"
        "v_mov_b32_e32 v106, s76\n\t"
        "v_mov_b32_e32 v107, s84\n\t"
        "buffer_store_dwordx2 v[106:107], v117, %[srd], s79 offen sc1\n\t"
        "s_and_b32 s80, s40, 3\n\t"
        "s_lshl_b32 s80, s80, 17\n\t"
        "s_add_u32 s63, s80, %[nC]\n\t"
        "s_add_u32 s66, s80, %[nS]\n\t"
        "s_add_u32 s81, s80, 0x20000\n\t"
        "s_and_b32 s81, s81, 0x60000\n\t"
        "s_add_u32 s67, s81, %[nW]\n\t"
        "s_add_u32 s79, s81, %[nT]\n\t"
        "s_cmp_eq_u32 %[w], 2\n\t"
        "s_cselect_b32 s81, s80, s81\n\t"
        "s_xor_b32 s81, s81, 0x40000\n\t"
        "s_add_u32 s68, s81, %[pob]\n\t"
        "s_lshl_b32 s72, s40, 17\n\t"
        "s_add_u32 s72, s72, %[kvo]\n\t"
        "s_mov_b32 s80, s61\n\t"
        "s_mov_b32 s61, s62\n\t"
        "s_mov_b32 s62, s80\n\t"
        ".Lgo%=:\n\t"
        "s_waitcnt vmcnt(4)\n\t"
        "v_smfmac_f32_16x16x64_bf16 v[52:55], v[0:3], v[136:143], v113\n\t"
        "v_smfmac_f32_16x16x64_bf16 v[56:59], v[0:3], v[144:151], v113\n\t"
        "v_smfmac_f32_16x16x64_bf16 v[60:63], v[0:3], v[152:159], v113\n\t"
        "v_smfmac_f32_16x16x64_bf16 v[48:51], v[0:3], v[128:135], v113\n\t"
        "s_waitcnt vmcnt(3)\n\t"
        "v_smfmac_f32_16x16x64_bf16 v[52:55], v[4:7], v[168:175], v113\n\t"
        "v_smfmac_f32_16x16x64_bf16 v[56:59], v[4:7], v[176:183], v113\n\t"
        "v_smfmac_f32_16x16x64_bf16 v[60:63], v[4:7], v[184:191], v113\n\t"
        "v_smfmac_f32_16x16x64_bf16 v[48:51], v[4:7], v[160:167], v113\n\t"
        "s_waitcnt vmcnt(2)\n\t"
        "v_smfmac_f32_16x16x64_bf16 v[52:55], v[8:11], v[200:207], v113\n\t"
        "v_smfmac_f32_16x16x64_bf16 v[56:59], v[8:11], v[208:215], v113\n\t"
        "v_smfmac_f32_16x16x64_bf16 v[60:63], v[8:11], v[216:223], v113\n\t"
        "v_smfmac_f32_16x16x64_bf16 v[48:51], v[8:11], v[192:199], v113\n\t"
        "s_waitcnt vmcnt(1)\n\t"
        "v_smfmac_f32_16x16x64_bf16 v[52:55], v[12:15], v[232:239], v113\n\t"
        "v_smfmac_f32_16x16x64_bf16 v[56:59], v[12:15], v[240:247], v113\n\t"
        "v_smfmac_f32_16x16x64_bf16 v[60:63], v[12:15], v[248:255], v113\n\t"
        "v_smfmac_f32_16x16x64_bf16 v[48:51], v[12:15], v[224:231], v113\n\t"
        "s_nop 1\n\t"
        "v_mov_b32_e32 v104, v104\n\t"
        "s_nop 1\n\t"
        "v_add_f32_e32 v98, v52, v54\n\t"
        "v_add_f32_e32 v99, v53, v55\n\t"
        "ds_write_b64 v123, v[98:99]\n\t"
        "v_add_f32_e32 v100, v56, v58\n\t"
        "v_add_f32_e32 v101, v57, v59\n\t"
        "ds_write_b64 v124, v[100:101]\n\t"
        "v_add_f32_e32 v102, v60, v62\n\t"
        "v_add_f32_e32 v103, v61, v63\n\t"
        "ds_write_b64 v125, v[102:103]\n\t"
        "v_pk_add_f32 v[96:97], v[48:49], v[50:51]\n\t"
        "v_cmp_u_f32_e32 vcc, v96, v97\n\t"
        "s_nop 2\n\t"
        "v_mov_b32_e32 v104, v104\n\t"
        "s_cbranch_vccnz .Lfa%=\n\t"
        ".Lra%=:\n\t"
        "v_mov_b32_e32 v104, v104\n\t"
        "s_nop 2\n\t"
        "v_mov_b32_e32 v104, v104\n\t"
        "s_nop 2\n\t"
        "v_mov_b32_e32 v104, v104\n\t"
        "s_nop 2\n\t"
        "v_mov_b32_e32 v104, v104\n\t"
        "s_waitcnt lgkmcnt(0)\n\t"
        "s_barrier\n\t"
        "v_mov_b32_e32 v104, v104\n\t"
        "ds_read_b64 v[98:99], v122 offset:512\n\t"
        "ds_read_b64 v[100:101], v122 offset:1024\n\t"
        "ds_read_b64 v[102:103], v122 offset:1536\n\t"
        "v_mov_b32_e32 v104, v104\n\t"
        "s_nop 2\n\t"
        "v_mov_b32_e32 v104, v104\n\t"
        "s_nop 2\n\t"
        "v_mov_b32_e32 v104, v104\n\t"
        "s_waitcnt lgkmcnt(2)\n\t"
        "v_pk_add_f32 v[96:97], v[96:97], v[98:99]\n\t"
        "s_waitcnt lgkmcnt(0)\n\t"
        "v_pk_add_f32 v[100:101], v[100:101], v[102:103]\n\t"
        "v_pk_add_f32 v[96:97], v[96:97], v[100:101]\n\t"
        "v_cvt_pk_bf16_f32 v80, v96, v97\n\t"
        "v_min_u32_e32 v80, -2, v80\n\t"
        "buffer_store_dword v80, v114, %[srd], s61 offen sc1\n\t"
        "s_cmp_eq_u32 s40, 63\n\t"
        "s_cbranch_scc1 .Lend%=\n\t"
        "v_mov_b32_e32 v64, 0\n\t"
        "v_mov_b32_e32 v65, 0\n\t"
        "v_mov_b32_e32 v66, 0\n\t"
        "v_mov_b32_e32 v67, 0\n\t"
        "v_mov_b32_e32 v68, 0\n\t"
        "v_mov_b32_e32 v69, 0\n\t"
        "v_mov_b32_e32 v70, 0\n\t"
        "v_mov_b32_e32 v71, 0\n\t"
        "v_mov_b32_e32 v72, 0\n\t"
        "v_mov_b32_e32 v73, 0\n\t"
        "v_mov_b32_e32 v74, 0\n\t"
        "v_mov_b32_e32 v75, 0\n\t"
        "v_mov_b32_e32 v76, 0\n\t"
        "v_mov_b32_e32 v77, 0\n\t"
        "v_mov_b32_e32 v78, 0\n\t"
        "v_mov_b32_e32 v79, 0\n\t"
        "s_xor_b32 s64, s63, %c[iCS1]\n\t"
        "s_xor_b32 s65, s63, %c[iCS2]\n\t"
        "s_xor_b32 s73, s64, %c[iCS2]\n\t"
        "s_sleep %c[VSLP]\n\t"
        "buffer_load_dwordx4 v[88:91], v112, %[srd], s66 offen sc1\n\t"
        "s_cmp_eq_u32 %[w], 0\n\t"
        "s_cselect_b32 exec_hi, 0, -1\n\t"
        "buffer_store_dwordx4 v[92:95], v112, %[srd], s68 offen sc1\n\t"
        "s_mov_b32 exec_hi, -1\n\t"
        "s_waitcnt lgkmcnt(0)\n\t"
        "v_mov_b32_e32 v48, 0\n\t"
        "v_mov_b32_e32 v49, 0\n\t"
        "v_mov_b32_e32 v50, 0\n\t"
        "v_mov_b32_e32 v51, 0\n\t"
        "v_mov_b32_e32 v52, 0\n\t"
        "v_mov_b32_e32 v53, 0\n\t"
        "v_mov_b32_e32 v54, 0\n\t"
        "v_mov_b32_e32 v55, 0\n\t"
        "v_mov_b32_e32 v56, 0\n\t"
        "v_mov_b32_e32 v57, 0\n\t"
        "v_mov_b32_e32 v58, 0\n\t"
        "v_mov_b32_e32 v59, 0\n\t"
        "v_mov_b32_e32 v60, 0\n\t"
        "v_mov_b32_e32 v61, 0\n\t"
        "v_mov_b32_e32 v62, 0\n\t"
        "v_mov_b32_e32 v63, 0\n\t"
        "s_add_u32 s81, s40, 1\n\t"
        "s_and_b32 s80, s81, 3\n\t"
        "s_lshl_b32 s80, s80, 17\n\t"
        "s_add_u32 s57, s80, %[nA]\n\t"
        "s_add_u32 s58, s57, 0x1000\n\t"
        "s_add_u32 s59, s57, 0x8000\n\t"
        "s_add_u32 s60, s57, 0x9000\n\t"
        "s_waitcnt vmcnt(1)\n\t"
        "s_mov_b32 s97, 0\n\t"
        ".Lsr%=:\n\t"
        "s_mov_b32 exec_hi, 0xffff\n\t"
        "v_subrev_u32_e32 v4, s41, v88\n\t"
        "v_subrev_u32_e32 v5, s41, v90\n\t"
        "v_max_i32_e32 v4, v4, v5\n\t"
        "v_max_u32_e32 v5, v89, v91\n\t"
        "v_cmp_eq_u32_e32 vcc, -1, v5\n\t"
        "v_max_i32_dpp v4, v4, v4 quad_perm:[1,0,3,2] row_mask:0xf bank_mask:0xf\n\t"
        "v_or_b32_e32 v6, v89, v91\n\t"
        "s_nop 0\n\t"
        "v_max_i32_dpp v4, v4, v4 quad_perm:[2,3,0,1] row_mask:0xf bank_mask:0xf\n\t"
        "s_nop 1\n\t"
        "v_max_i32_dpp v4, v4, v4 row_ror:4 row_mask:0xf bank_mask:0xf\n\t"
        "s_nop 1\n\t"
        "v_max_i32_dpp v4, v4, v4 row_ror:8 row_mask:0xf bank_mask:0xf\n\t"
        "s_cbranch_vccnz .Lsf%=\n\t"
        "v_readlane_b32 s85, v4, 0\n\t"
        "v_readlane_b32 s81, v4, 16\n\t"
        "v_readlane_b32 s56, v4, 32\n\t"
        "v_and_b32_e32 v5, 1, v6\n\t"
        "v_cmp_ne_u32_e64 s[86:87], 0, v5\n\t"
        "v_and_b32_e32 v5, 2, v6\n\t"
        "v_cmp_ne_u32_e64 s[88:89], 0, v5\n\t"
        "v_and_b32_e32 v5, 4, v6\n\t"
        "v_cmp_ne_u32_e64 s[90:91], 0, v5\n\t"
        "s_max_i32 s56, s56, s81\n\t"
        "s_add_u32 s92, s85, s41\n\t"
        "s_sub_u32 s92, s92, s43\n\t"
        "s_add_i32 s93, s56, s47\n\t"
        "s_mov_b32 s44, s46\n\t"
        "s_mov_b32 s46, s45\n\t"
        "s_mov_b32 s94, s48\n\t"
        "s_mov_b32 s43, s41\n\t"
        "s_cmp_gt_i32 s56, s54\n\t"
        "s_cbranch_scc1 .Lmv%=\n\t"
        "s_mov_b32 s37, 0\n\t"
        "s_mov_b32 s48, 0\n\t"
        "s_mov_b32 s95, 0\n\t"
        ".Lsd%=:\n\t"
        "s_mov_b32 exec_hi, -1\n\t"
        "s_add_u32 s42, s43, s46\n\t"
        "s_mov_b32 s97, 0\n\t"
        "s_movk_i32 s96, 0x1000\n\t"
        ".Lwc%=:\n\t"
        "s_memrealtime s[98:99]\n\t"
        "s_addk_i32 s96, -1\n\t"
        "s_cmp_lt_i32 s96, 0\n\t"
        "s_cbranch_scc1 .Ltc%=\n\t"
        "s_waitcnt lgkmcnt(0)\n\t"
        "s_sub_i32 s98, s98, s42\n\t"
        "s_cmp_lt_i32 s98, %c[iFT]\n\t"
        "s_cbranch_scc1 .Lwc%=\n\t"
        "s_cmp_ge_i32 s98, 0\n\t"
        "s_cbranch_scc1 .Lxc%=\n\t"
        ".Lqc%=:\n\t"
        "s_nop %c[iFN]\n\t"
        "s_addk_i32 s98, 1\n\t"
        "s_cmp_lt_i32 s98, 0\n\t"
        "s_cbranch_scc1 .Lqc%=\n\t"
        ".Lxc%=:\n\t"
        "buffer_load_dwordx4 v[16:19], v112, %[srd], s63 offen sc1\n\t"
        "s_cmp_ge_u32 s40, %c[iCT0]\n\t"
        "s_cselect_b32 s85, 1, 0\n\t"
        "s_cmp_lg_u64 s[86:87], 0\n\t"
        "s_cselect_b32 s86, s85, 0\n\t"
        "s_cmp_lg_u64 s[88:89], 0\n\t"
        "s_cselect_b32 s87, s85, 0\n\t"
        "s_cmp_lg_u64 s[90:91], 0\n\t"
        "s_cselect_b32 s88, s85, 0\n\t"
        "buffer_load_dwordx4 v[20:23], v115, %[srd], s63 offen sc1\n\t"
        "s_or_b32 s80, s94, s86\n\t"
        "s_or_b32 s80, s80, s87\n\t"
        "s_andn2_b32 s90, s85, s80\n\t"
        "s_cmp_le_i32 s92, s44\n\t"
        "s_cselect_b32 s80, s85, 0\n\t"
        "s_andn2_b32 s91, s80, s88\n\t"
        "s_and_b32 s80, s40, %c[LDEC]\n\t"
        "s_cselect_b32 s89, 0, 1\n\t"
        "buffer_load_dwordx4 v[24:27], v112, %[srd], s64 offen sc1\n\t"
        "s_andn2_b32 s80, s86, s94\n\t"
        "s_cmp_lt_i32 s51, %c[LA24]\n\t"
        "s_cselect_b32 s80, s80, 0\n\t"
        "s_lshl2_add_u32 s51, s80, s51\n\t"
        "s_sub_i32 s51, s51, s89\n\t"
        "s_max_i32 s51, s51, %c[LA]\n\t"
        "s_lshl_b32 s80, s93, 1\n\t"
        "s_sub_i32 s81, s80, s53\n\t"
        "buffer_load_dwordx4 v[28:31], v115, %[srd], s64 offen sc1\n\t"
        "s_ashr_i32 s82, s81, %c[UPSH]\n\t"
        "s_max_i32 s82, s82, %c[TRUP]\n\t"
        "s_add_i32 s82, s82, s53\n\t"
        "s_min_i32 s82, s82, s80\n\t"
        "s_ashr_i32 s83, s81, %c[DNSH]\n\t"
        "s_min_i32 s83, s83, -%c[TRDN]\n\t"
        "s_add_i32 s83, s83, s53\n\t"
        "s_max_i32 s83, s83, s80\n\t"
        "buffer_load_dwordx4 v[32:35], v112, %[srd], s65 offen sc1\n\t"
        "s_cmp_gt_i32 s81, 0\n\t"
        "s_cselect_b32 s82, s82, s83\n\t"
        "s_cmp_lg_u32 s91, 0\n\t"
        "s_cselect_b32 s53, s82, s53\n\t"
        "s_add_i32 s80, s53, 1\n\t"
        "s_ashr_i32 s80, s80, 1\n\t"
        "s_add_i32 s47, s80, s51\n\t"
        "s_add_u32 s81, s47, %c[AUP]\n\t"
        "buffer_load_dwordx4 v[36:39], v115, %[srd], s65 offen sc1\n\t"
        "s_min_i32 s80, s95, s81\n\t"
        "s_max_i32 s47, s47, s80\n\t"
        "s_add_u32 s41, s42, s47\n\t"
        "s_cmp_eq_u32 s40, 0\n\t"
        "s_cselect_b32 s51, %c[LA], s51\n\t"
        "s_mov_b32 s80, s88\n\t"
        "s_add_i32 s81, s92, s50\n\t"
        "s_sub_i32 s81, s81, 4\n\t"
        "buffer_load_dwordx4 v[40:43], v112, %[srd], s73 offen sc1\n\t"
        "s_cmp_le_i32 s81, s44\n\t"
        "s_cselect_b32 s80, s80, 0\n\t"
        "s_cmp_lt_i32 s50, %c[LC24]\n\t"
        "s_cselect_b32 s80, s80, 0\n\t"
        "s_lshl2_add_u32 s50, s80, s50\n\t"
        "s_sub_i32 s50, s50, s89\n\t"
        "s_max_i32 s50, s50, %c[LC]\n\t"
        "s_lshl_b32 s80, s92, 1\n\t"
        "buffer_load_dwordx4 v[44:47], v115, %[srd], s73 offen sc1\n\t"
        "s_sub_i32 s81, s80, s52\n\t"
        "s_ashr_i32 s82, s81, %c[UPSH]\n\t"
        "s_max_i32 s82, s82, %c[TRUP]\n\t"
        "s_add_i32 s82, s82, s52\n\t"
        "s_min_i32 s82, s82, s80\n\t"
        "s_ashr_i32 s83, s81, %c[DNSH]\n\t"
        "s_min_i32 s83, s83, -%c[TRDN]\n\t"
        "s_add_i32 s83, s83, s52\n\t"
        "s_max_i32 s83, s83, s80\n\t"
        "s_cmp_gt_i32 s81, 0\n\t"
        "s_cselect_b32 s82, s82, s83\n\t"
        "s_cmp_lg_u32 s90, 0\n\t"
        "s_cselect_b32 s52, s82, s52\n\t"
        "s_add_i32 s80, s52, 1\n\t"
        "s_ashr_i32 s80, s80, 1\n\t"
        "s_add_i32 s45, s80, s50\n\t"
        ".p2align 6\n\t"
        ".Lrc%=:\n\t"
        "s_waitcnt vmcnt(7)\n\t"
        "v_smfmac_f32_16x16x64_bf16 v[64:67], v[16:19], a[0:7], v113\n\t"
        "v_smfmac_f32_16x16x64_bf16 v[68:71], v[16:19], a[8:15], v113\n\t"
        "v_smfmac_f32_16x16x64_bf16 v[72:75], v[16:19], a[16:23], v113\n\t"
        "v_smfmac_f32_16x16x64_bf16 v[76:79], v[16:19], a[24:31], v113\n\t"
        "s_waitcnt vmcnt(6)\n\t"
        "v_smfmac_f32_16x16x64_bf16 v[64:67], v[20:23], a[32:39], v113\n\t"
        "v_smfmac_f32_16x16x64_bf16 v[68:71], v[20:23], a[40:47], v113\n\t"
        "v_smfmac_f32_16x16x64_bf16 v[72:75], v[20:23], a[48:55], v113\n\t"
        "v_smfmac_f32_16x16x64_bf16 v[76:79], v[20:23], a[56:63], v113\n\t"
        "s_waitcnt vmcnt(5)\n\t"
        "v_smfmac_f32_16x16x64_bf16 v[64:67], v[24:27], a[64:71], v113\n\t"
        "v_smfmac_f32_16x16x64_bf16 v[68:71], v[24:27], a[72:79], v113\n\t"
        "v_smfmac_f32_16x16x64_bf16 v[72:75], v[24:27], a[80:87], v113\n\t"
        "v_smfmac_f32_16x16x64_bf16 v[76:79], v[24:27], a[88:95], v113\n\t"
        "s_waitcnt vmcnt(4)\n\t"
        "v_smfmac_f32_16x16x64_bf16 v[64:67], v[28:31], a[96:103], v113\n\t"
        "v_smfmac_f32_16x16x64_bf16 v[68:71], v[28:31], a[104:111], v113\n\t"
        "v_smfmac_f32_16x16x64_bf16 v[72:75], v[28:31], a[112:119], v113\n\t"
        "v_smfmac_f32_16x16x64_bf16 v[76:79], v[28:31], a[120:127], v113\n\t"
        "s_waitcnt vmcnt(3)\n\t"
        "v_smfmac_f32_16x16x64_bf16 v[64:67], v[32:35], a[128:135], v113\n\t"
        "v_smfmac_f32_16x16x64_bf16 v[68:71], v[32:35], a[136:143], v113\n\t"
        "v_smfmac_f32_16x16x64_bf16 v[72:75], v[32:35], a[144:151], v113\n\t"
        "v_smfmac_f32_16x16x64_bf16 v[76:79], v[32:35], a[152:159], v113\n\t"
        "s_waitcnt vmcnt(2)\n\t"
        "v_smfmac_f32_16x16x64_bf16 v[64:67], v[36:39], a[160:167], v113\n\t"
        "v_smfmac_f32_16x16x64_bf16 v[68:71], v[36:39], a[168:175], v113\n\t"
        "v_smfmac_f32_16x16x64_bf16 v[72:75], v[36:39], a[176:183], v113\n\t"
        "v_smfmac_f32_16x16x64_bf16 v[76:79], v[36:39], a[184:191], v113\n\t"
        "s_waitcnt vmcnt(1)\n\t"
        "v_smfmac_f32_16x16x64_bf16 v[64:67], v[40:43], a[192:199], v113\n\t"
        "v_smfmac_f32_16x16x64_bf16 v[68:71], v[40:43], a[200:207], v113\n\t"
        "v_smfmac_f32_16x16x64_bf16 v[72:75], v[40:43], a[208:215], v113\n\t"
        "v_smfmac_f32_16x16x64_bf16 v[76:79], v[40:43], a[216:223], v113\n\t"
        "s_waitcnt vmcnt(0)\n\t"
        "v_smfmac_f32_16x16x64_bf16 v[64:67], v[44:47], a[224:231], v113\n\t"
        "s_nop 2\n\t"
        "v_smfmac_f32_16x16x64_bf16 v[68:71], v[44:47], a[232:239], v113\n\t"
        "s_cmp_gt_u32 %[w], 2\n\t"
        "s_nop 1\n\t"
        "v_smfmac_f32_16x16x64_bf16 v[72:75], v[44:47], a[240:247], v113\n\t"
        "v_add_f32_e32 v80, v64, v66\n\t"
        "s_nop 0\n\t"
        "v_smfmac_f32_16x16x64_bf16 v[76:79], v[44:47], a[248:255], v113\n\t"
        "v_add_f32_e32 v81, v65, v67\n\t"
        "v_add_f32_e32 v82, v68, v70\n\t"
        "v_add_f32_e32 v83, v69, v71\n\t"
        "s_mov_b32 exec_lo, 0\n\t"
        "s_nop 0\n\t"
        "v_add_f32_e32 v80, v72, v74\n\t"
        "v_add_f32_e32 v81, v73, v75\n\t"
        "s_nop 0\n\t"
        "v_pk_add_f32 v[82:83], v[76:77], v[78:79]\n\t"
        "s_mov_b32 exec_lo, -1\n\t"
        "s_nop 0\n\t"
        "v_permlane32_swap_b32_e32 v80, v82\n\t"
        "v_add_f32_e32 v80, v80, v82\n\t"
        "ds_write_b32 v119, v80\n\t"
        "v_permlane32_swap_b32_e32 v81, v83\n\t"
        "v_add_f32_e32 v81, v81, v83\n\t"
        "ds_write_b32 v119, v81 offset:128\n\t"
        "v_cmp_u_f32_e32 vcc, v80, v81\n\t"
        "s_nop 2\n\t"
        "v_mov_b32_e32 v104, v104\n\t"
        "s_cbranch_vccnz .Lfc%=\n\t"
        ".Lrd%=:\n\t"
        "v_mov_b32_e32 v104, v104\n\t"
        "s_cbranch_scc1 .Lno%=\n\t"
        "v_mov_b32_e32 v104, v104\n\t"
        "s_nop 2\n\t"
        "v_mov_b32_e32 v104, v104\n\t"
        "s_waitcnt lgkmcnt(0)\n\t"
        "s_barrier\n\t"
        "v_mov_b32_e32 v104, v104\n\t"
        "ds_read_b64 v[0:1], v120 offset:8192\n\t"
        "ds_read_b64 v[2:3], v120 offset:8704\n\t"
        "ds_read_b64 v[4:5], v120 offset:9216\n\t"
        "ds_read_b64 v[6:7], v120 offset:9728\n\t"
        "v_mov_b32_e32 v104, v104\n\t"
        "s_nop 2\n\t"
        "v_mov_b32_e32 v104, v104\n\t"
        "s_nop 2\n\t"
        "v_mov_b32_e32 v104, v104\n\t"
        "s_waitcnt lgkmcnt(2)\n\t"
        "v_pk_add_f32 v[0:1], v[0:1], v[2:3]\n\t"
        "s_waitcnt lgkmcnt(0)\n\t"
        "v_pk_add_f32 v[4:5], v[4:5], v[6:7]\n\t"
        "v_pk_add_f32 v[0:1], v[0:1], v[4:5]\n\t"
        "v_cvt_pk_bf16_f32 v0, v0, v1\n\t"
        "v_min_u32_e32 v0, -2, v0\n\t"
        "buffer_store_dword v0, v116, %[srd], s67 offen sc1\n\t"
        ".Lnp%=:\n\t"
        "s_memrealtime s[76:77]\n\t"
        "s_add_u32 s40, s40, 1\n\t"
        "s_waitcnt lgkmcnt(0)\n\t"
        "s_add_u32 s81, s76, %c[iVD]\n\t"
        "s_sub_u32 s80, s76, s41\n\t"
        "s_cmp_gt_i32 s80, %c[iTOL]\n\t"
        "s_cselect_b32 s69, s81, s41\n\t"
        "s_branch .Lloop%=\n\t"
        ".Lmv%=:\n\t"
        ".Lhk0%=:\n\t"
        "s_cbranch_execz .Lhk1%=\n\t"
        "s_mov_b32 s48, 1\n\t"
        "s_cmp_eq_u32 s54, %c[iTOL]\n\t"
        "s_cbranch_scc1 .Lm1%=\n\t"
        "s_max_i32 s80, s85, s56\n\t"
        "s_add_u32 s43, s41, s80\n\t"
        "s_add_u32 s43, s43, %c[iPEN0]\n\t"
        ".Lhk1%=:\n\t"
        "s_cbranch_execz .Lhk2%=\n\t"
        "s_mov_b32 s54, %c[iTOL]\n\t"
        "s_add_u32 s46, s46, %c[iX0C]\n\t"
        "s_mov_b32 s95, 0\n\t"
        "s_add_u32 s51, s51, %c[iX0A]\n\t"
        "v_and_b32_e32 v4, 3, v88\n\t"
        "v_and_b32_e32 v5, 3, v90\n\t"
        ".Lhk2%=:\n\t"
        "s_cbranch_execz .Lhk3%=\n\t"
        "v_lshlrev_b32_e64 v4, v4, 1\n\t"
        "v_lshlrev_b32_e64 v5, v5, 1\n\t"
        "v_or_b32_e32 v4, v4, v5\n\t"
        "s_mov_b32 s80, 0\n\t"
        "v_and_b32_e32 v5, 1, v4\n\t"
        "v_cmp_ne_u32_e32 vcc, 0, v5\n\t"
        ".Lhk3%=:\n\t"
        "s_cbranch_execz .Lhk4%=\n\t"
        "s_nop 1\n\t"
        "s_cmp_lg_u64 vcc, 0\n\t"
        "s_cselect_b32 s81, 2, 0\n\t"
        "s_or_b32 s80, s80, s81\n\t"
        "v_and_b32_e32 v5, 2, v4\n\t"
        "v_cmp_ne_u32_e32 vcc, 0, v5\n\t"
        ".Lhk4%=:\n\t"
        "s_cbranch_execz .Lhk5%=\n\t"
        "s_nop 1\n\t"
        "s_cmp_lg_u64 vcc, 0\n\t"
        "s_cselect_b32 s81, 4, 0\n\t"
        "s_or_b32 s80, s80, s81\n\t"
        "v_and_b32_e32 v5, 4, v4\n\t"
        "v_cmp_ne_u32_e32 vcc, 0, v5\n\t"
        ".Lhk5%=:\n\t"
        "s_cbranch_execz .Lhk6%=\n\t"
        "s_nop 1\n\t"
        "s_cmp_lg_u64 vcc, 0\n\t"
        "s_cselect_b32 s81, 8, 0\n\t"
        "s_or_b32 s80, s80, s81\n\t"
        "v_and_b32_e32 v5, 8, v4\n\t"
        "v_cmp_ne_u32_e32 vcc, 0, v5\n\t"
        ".Lhk6%=:\n\t"
        "s_cbranch_execz .Lhk7%=\n\t"
        "s_nop 1\n\t"
        "s_cmp_lg_u64 vcc, 0\n\t"
        "s_cselect_b32 s81, 16, 0\n\t"
        "s_or_b32 s80, s80, s81\n\t"
        "s_lshr_b32 s80, 0x1adf1210, s80\n\t"
        "s_and_b32 s55, s80, 3\n\t"
        ".Lhk7%=:\n\t"
        "s_cbranch_execz .Lhk8%=\n\t"
        "s_branch .Lal%=\n\t"
        ".Lm1%=:\n\t"
        "s_add_u32 s80, s37, %c[iPEN2]\n\t"
        "s_min_i32 s80, s80, %c[iPENM]\n\t"
        "s_cmp_lg_u32 s37, 0\n\t"
        "s_cselect_b32 s37, s80, %c[iPEN]\n\t"
        "s_add_u32 s80, s56, s37\n\t"
        ".Lhk8%=:\n\t"
        "s_cbranch_execz .Lhk9%=\n\t"
        "s_max_i32 s80, s80, %c[iPMIN]\n\t"
        "s_add_u32 s43, s41, s80\n\t"
        "s_add_u32 s95, s93, %c[LA]\n\t"
        "s_add_u32 s46, s46, %c[iXR]\n\t"
        ".Lal%=:\n\t"
        "s_sub_u32 s80, s55, s43\n\t"
        "s_and_b32 s80, s80, %c[iPHM]\n\t"
        ".Lhk9%=:\n\t"
        "s_cbranch_execz .Lhk10%=\n\t"
        "s_add_u32 s43, s43, s80\n\t"
        "s_branch .Lsd%=\n\t"
        ".Lsf%=:\n\t"
        "s_mov_b32 exec_hi, -1\n\t"
        "s_sleep 2\n\t"
        "buffer_load_dwordx4 v[88:91], v112, %[srd], s66 offen sc1\n\t"
        "s_add_u32 s97, s97, 1\n\t"
        ".Lhk10%=:\n\t"
        "s_cbranch_execz .Lhk11%=\n\t"
        "s_cmp_lt_u32 s97, 0x100000\n\t"
        "s_cbranch_scc0 .Lgu%=\n\t"
        "s_waitcnt vmcnt(0)\n\t"
        "s_branch .Lsr%=\n\t"
        ".Lfa%=:\n\t"
        "v_max_u32_e32 v84, v0, v2\n\t"
        "v_max3_u32 v84, v84, v1, v3\n\t"
        ".Lhk11%=:\n\t"
        "s_cbranch_execz .Lhk12%=\n\t"
        "v_max3_u32 v84, v84, v4, v6\n\t"
        "v_max3_u32 v84, v84, v5, v7\n\t"
        "v_max3_u32 v84, v84, v8, v10\n\t"
        "v_max3_u32 v84, v84, v9, v11\n\t"
        "v_max3_u32 v84, v84, v12, v14\n\t"
        "v_max3_u32 v84, v84, v13, v15\n\t"
        ".Lhk12%=:\n\t"
        "s_cbranch_execz .Lhk13%=\n\t"
        "v_cmp_eq_u32_e32 vcc, -1, v84\n\t"
        "s_nop 1\n\t"
        "v_mov_b32_e32 v104, v104\n\t"
        "s_nop 0\n\t"
        "s_cbranch_vccz .Lra%=\n\t"
        "s_add_u32 s80, s40, 1\n\t"
        ".Lhk13%=:\n\t"
        "s_cbranch_execz .Lhk14%=\n\t"
        "v_mov_b32_e32 v80, 161792\n\t"
        "v_mov_b32_e32 v81, s80\n\t"
        "ds_write_b32 v80, v81\n\t"
        "v_mov_b32_e32 v48, 0\n\t"
        "v_mov_b32_e32 v49, 0\n\t"
        "v_mov_b32_e32 v50, 0\n\t"
        ".Lhk14%=:\n\t"
        "s_cbranch_execz .Lhk15%=\n\t"
        "v_mov_b32_e32 v51, 0\n\t"
        "v_mov_b32_e32 v52, 0\n\t"
        "v_mov_b32_e32 v53, 0\n\t"
        "v_mov_b32_e32 v54, 0\n\t"
        "v_mov_b32_e32 v55, 0\n\t"
        "v_mov_b32_e32 v56, 0\n\t"
        ".Lhk15%=:\n\t"
        "s_cbranch_execz .Lhk16%=\n\t"
        "v_mov_b32_e32 v57, 0\n\t"
        "v_mov_b32_e32 v58, 0\n\t"
        "v_mov_b32_e32 v59, 0\n\t"
        "v_mov_b32_e32 v60, 0\n\t"
        "v_mov_b32_e32 v61, 0\n\t"
        "v_mov_b32_e32 v62, 0\n\t"
        ".Lhk16%=:\n\t"
        "s_cbranch_execz .Lhk17%=\n\t"
        "v_mov_b32_e32 v63, 0\n\t"
        "s_sleep %c[VASL]\n\t"
        "buffer_load_dwordx4 v[0:3], v112, %[srd], s57 offen sc1\n\t"
        "buffer_load_dwordx4 v[4:7], v112, %[srd], s58 offen sc1\n\t"
        "buffer_load_dwordx4 v[8:11], v112, %[srd], s59 offen sc1\n\t"
        "buffer_load_dwordx4 v[12:15], v112, %[srd], s60 offen sc1\n\t"
        ".Lhk17%=:\n\t"
        "s_cbranch_execz .Lhk18%=\n\t"
        "buffer_store_dword v0, off, %[wsd], s36\n\t"
        "s_add_u32 s97, s97, 1\n\t"
        "s_cmp_lt_u32 s97, 0x100000\n\t"
        "s_cbranch_scc0 .Lgu%=\n\t"
        "s_branch .Lgo%=\n\t"
        ".Lfc%=:\n\t"
        "v_max_u32_e32 v84, v16, v18\n\t"
        ".Lhk18%=:\n\t"
        "s_cbranch_execz .Lhk19%=\n\t"
        "v_max3_u32 v84, v84, v20, v22\n\t"
        "v_max3_u32 v84, v84, v24, v26\n\t"
        "v_max3_u32 v84, v84, v28, v30\n\t"
        "v_max3_u32 v84, v84, v32, v34\n\t"
        "v_max3_u32 v84, v84, v36, v38\n\t"
        "v_max3_u32 v84, v84, v40, v42\n\t"
        ".Lhk19%=:\n\t"
        "s_cbranch_execz .Lhk20%=\n\t"
        "v_max3_u32 v84, v84, v44, v46\n\t"
        "v_cmp_eq_u32_e32 vcc, -1, v84\n\t"
        "s_nop 1\n\t"
        "v_mov_b32_e32 v104, v104\n\t"
        "s_nop 0\n\t"
        "s_cbranch_vccz .Lrd%=\n\t"
        ".Lhk20%=:\n\t"
        "s_cbranch_execz .Lhk21%=\n\t"
        "s_sleep %c[CSLP]\n\t"
        "buffer_load_dwordx4 v[16:19], v112, %[srd], s63 offen sc1\n\t"
        "v_mov_b32_e32 v64, 0\n\t"
        "v_mov_b32_e32 v65, 0\n\t"
        "buffer_load_dwordx4 v[20:23], v115, %[srd], s63 offen sc1\n\t"
        "v_mov_b32_e32 v66, 0\n\t"
        ".Lhk21%=:\n\t"
        "s_cbranch_execz .Lhk22%=\n\t"
        "v_mov_b32_e32 v67, 0\n\t"
        "buffer_load_dwordx4 v[24:27], v112, %[srd], s64 offen sc1\n\t"
        "v_mov_b32_e32 v68, 0\n\t"
        "v_mov_b32_e32 v69, 0\n\t"
        "buffer_load_dwordx4 v[28:31], v115, %[srd], s64 offen sc1\n\t"
        "v_mov_b32_e32 v70, 0\n\t"
        ".Lhk22%=:\n\t"
        "s_cbranch_execz .Lhk23%=\n\t"
        "v_mov_b32_e32 v71, 0\n\t"
        "buffer_load_dwordx4 v[32:35], v112, %[srd], s65 offen sc1\n\t"
        "v_mov_b32_e32 v72, 0\n\t"
        "v_mov_b32_e32 v73, 0\n\t"
        "buffer_load_dwordx4 v[36:39], v115, %[srd], s65 offen sc1\n\t"
        "v_mov_b32_e32 v74, 0\n\t"
        ".Lhk23%=:\n\t"
        "s_cbranch_execz .Lhk24%=\n\t"
        "v_mov_b32_e32 v75, 0\n\t"
        "buffer_load_dwordx4 v[40:43], v112, %[srd], s73 offen sc1\n\t"
        "v_mov_b32_e32 v76, 0\n\t"
        "v_mov_b32_e32 v77, 0\n\t"
        "buffer_load_dwordx4 v[44:47], v115, %[srd], s73 offen sc1\n\t"
        "v_mov_b32_e32 v78, 0\n\t"
        ".Lhk24%=:\n\t"
        "s_cbranch_execz .Lhk25%=\n\t"
        "v_mov_b32_e32 v79, 0\n\t"
        "s_add_u32 s80, s40, 1\n\t"
        "v_mov_b32_e32 v80, 161796\n\t"
        "v_mov_b32_e32 v81, s80\n\t"
        "ds_write_b32 v80, v81\n\t"
        "s_add_u32 s97, s97, 1\n\t"
        ".Lhk25%=:\n\t"
        "s_cbranch_execz .Lhk26%=\n\t"
        "s_cmp_lt_u32 s97, 0x100000\n\t"
        "s_cbranch_scc0 .Lgu%=\n\t"
        "s_branch .Lrc%=\n\t"
        ".Lno%=:\n\t"
        "s_waitcnt lgkmcnt(0)\n\t"
        "s_barrier\n\t"
        "s_branch .Lnp%=\n\t"
        ".Lta%=:\n\t"
        ".Lhk26%=:\n\t"
        "s_cbranch_execz .Lhk27%=\n\t"
        "s_waitcnt lgkmcnt(0)\n\t"
        "s_branch .Lxa%=\n\t"
        ".Ltc%=:\n\t"
        "s_waitcnt lgkmcnt(0)\n\t"
        "s_branch .Lxc%=\n\t"
        ".Lgu%=:\n\t"
        "s_mov_b64 exec, -1\n\t"
        "v_mov_b32_e32 v80, %c[ferr]\n\t"
        ".Lhk27%=:\n\t"
        "s_cbranch_execz .Lhk28%=\n\t"
        "v_mov_b32_e32 v81, 2\n\t"
        "buffer_store_dword v81, v80, %[wsd], 0 offen sc1\n\t"
        "s_endpgm\n\t"
        ".Lend%=:\n\t"
        "buffer_store_dword v80, v114, %[wsd], s72 offen sc1\n\t"
        "s_waitcnt vmcnt(0)\n\t"
        "s_barrier\n\t"
        ".Lhk28%=:\n\t"
        "s_cbranch_execz .Lhk29%=\n\t"
        "buffer_store_dword v86, v118, %[wsd], 0 offen sc1\n\t"
        "s_endpgm\n\t"
        ".Lhk29%=:\n\t"
        "s_branch .Lhr%="
        : [nA] "+s"(nA), [nS] "+s"(nS), [nT] "+s"(nT), [nW] "+s"(nW), [nC] "+s"(nC), [b0] "+s"(b0), [pob] "+s"(pob)
        : [srd] "s"(rhd), [wsd] "s"(wsd), [w] "s"(w), [tag] "s"(tag), [kvo] "s"(kvo), [cal] "s"(cal), [sv] "s"(sv), [so] "s"(so), [m0b] "s"(m0b), [key] "s"(key), [tu] "s"(tu),
          [ferr] "i"(F_ERR), [iD1] "i"(D1), [iD2] "i"(D2), [LC] "i"(LC0), [LA] "i"(LA0), [LC24] "i"(LC0 + 24), [LA24] "i"(LA0 + 24), [E1] "i"(2 * (D1 - LC0)), [E2] "i"(2 * (D2 - LA0)),
          [iPEN] "i"(PEN), [iPEN2] "i"(PEN2), [iPENM] "i"(PEN + 2 * PEN2), [iPEN0] "i"(PEN0), [iTOL] "i"(TOL), [iCT0] "i"(CT0), [iX0C] "i"(X0C), [iX0A] "i"(X0A), [iPHM] "i"(PH_M),
          [iFT] "i"(-3), [iFN] "i"(FINE_N), [AUP] "i"(A_UP), [ASLP] "i"(A_SLP), [LDEC] "i"(L_DEC), [UPSH] "i"(UP_SH), [TRUP] "i"(TR_UP), [TRDN] "i"(TR_DN), [HSLP] "i"(H_SLP), [iPMIN] "i"(PMIN), [VSLP] "i"(V_SLP), [RSLP] "i"(R_SLP), [iVD] "i"(V_DEL), [iXR] "i"(XR), [VASL] "i"(VA_SLP), [CSLP] "i"(C_SLP), [DNSH] "i"(DN_SH), [NX0] "i"(NEARX), [NX1] "i"(8192 - NEARX), [iCS1] "i"(CS1), [iCS2] "i"(CS2), [NY0] "i"(NEARY), [NY1] "i"(16384 - NEARY),
          "{v[112:127]}"(pc), "{v[96:103]}"(pa), "{v[104:105]}"(pb)
        : "memory", "vcc", "scc", "m0", "s36", "s37", "s38", "s39", "s40", "s41", "s42", "s43", "s44", "s45", "s46", "s47", "s48", "s49", "s50", "s51", "s52", "s53", "s54", "s55", "s56", "s57", "s58", "s59", "s60", "s61", "s62", "s63", "s64", "s65", "s66", "s67", "s68", "s69", "s70", "s71", "s72", "s73", "s74", "s75", "s76", "s77", "s78", "s79", "s80", "s81", "s82", "s83", "s84", "s85", "s86", "s87", "s88", "s89", "s90", "s91", "s92", "s93", "s94", "s95", "s96", "s97", "s98", "s99");
    }
}

using f32x16 = __attribute__((ext_vector_type(16))) float;
constexpr int CV_RP = 80;
constexpr int CV_U = 96 * CV_RP;
constexpr int CV_KP = 4416;
constexpr int CV_K0 = 4 * CV_U;
constexpr int CV_Y = CV_K0 + 16 * CV_KP;
__device__ __forceinline__ float lo16(unsigned x) { return __uint_as_float(x << 16); }
__device__ __forceinline__ float hi16(unsigned x) { return __uint_as_float(x & 0xFFFF0000u); }
__device__ __forceinline__ void prep_block(const Params& p, rsrc_t r, int k4, const int wv, const unsigned tag) {
    const int tid = otid(), w = wv, l = tid & 63;
    const int cu = (k4 & 3) * 32 + w * 8 + (l >> 3), oc0 = (k4 >> 2) * 8 + (l & 7);
    const rsrc_t rk = __builtin_amdgcn_make_buffer_rsrc((void*)p.Ak, 0, S * D * 2, 0x00020000), rv = __builtin_amdgcn_make_buffer_rsrc((void*)p.Av, 0, S * D * 2, 0x00020000);
    v4u sk[4], kn[11], vn[8], gw[2];
    auto fetch = [&](int oc) {
#pragma unroll
        for (int j = 0; j < 11; ++j) kn[j] = __builtin_amdgcn_raw_buffer_load_b128(rk, (unsigned)((oc * 8 + j - 3) * 2048 + cu * 16), 0, 2);
#pragma unroll
        for (int i = 0; i < 8; ++i) vn[i] = __builtin_amdgcn_raw_buffer_load_b128(rv, (unsigned)((oc * 8 + i) * 2048 + cu * 16), 0, 2); };
    fetch(oc0);
#pragma unroll
    for (int q = 0; q < 4; ++q) sk[q] = *(const v4u*)(p.shift_k + cu * 32 + q * 8);
#pragma unroll
    for (int q = 0; q < 2; ++q) gw[q] = *(const v4u*)(p.gate_w + q * 512 + l * 8);
    st4(r, tid < 128 ? F_RDY + k4 * 512 + tid * 4 : tid < 132 ? F_ATT + (k4 * 4 + tid - 128) * 64 : (tid == 192 && k4 == 0) ? F_CONV : 0xF0000000u, 0u);
#pragma unroll 1
    for (int it = 0; it < 4; ++it) {
        const int oc = oc0 + 64 * it, tk = (it * 32 + k4) * 16 + w;
        v4u kr[11], vr[8], xg[4][2];
#pragma unroll
        for (int j = 0; j < 11; ++j) kr[j] = kn[j];
#pragma unroll
        for (int i = 0; i < 8; ++i) vr[i] = vn[i];
        __builtin_amdgcn_sched_barrier(0);
#pragma unroll
        for (int i = 0; i < 4; ++i)
#pragma unroll
            for (int q = 0; q < 2; ++q) xg[i][q] = *(const v4u*)(p.Aq + (size_t)(tk + 4 * i) * D + q * 512 + l * 8);
        fetch(it < 3 ? oc + 64 : 4096);
        __builtin_amdgcn_sched_barrier(0);
#pragma unroll
        for (int c8 = 0; c8 < 8; ++c8) {
            const unsigned s01 = sk[c8 >> 1][(c8 & 1) * 2], s23 = sk[c8 >> 1][(c8 & 1) * 2 + 1];
            const float sv[4] = {hi16(s23), lo16(s23), hi16(s01), lo16(s01)};
            float kk[11], u[8];
#pragma unroll
            for (int j = 0; j < 11; ++j) kk[j] = (c8 & 1) ? hi16(kr[j][c8 >> 1]) : lo16(kr[j][c8 >> 1]);
            f32x2 acc[4];
#pragma unroll
            for (int j = 0; j < 4; ++j)
#pragma unroll
                for (int i = 0; i < 4; ++i) { if (FASTP) { const f32x2 t = (f32x2){kk[i + j], kk[i + j + 4]} * sv[j]; acc[i] = j == 0 ? t : j == 3 ? rbf2(acc[i] + t) : acc[i] + t; }
                    else { const f32x2 t = rbf2((f32x2){kk[i + j], kk[i + j + 4]} * sv[j]); acc[i] = j == 0 ? t : rbf2(acc[i] + t); } }
#pragma unroll
            for (int i = 0; i < 8; ++i) u[i] = acc[i & 3][i >> 2] * ((c8 & 1) ? hi16(vr[i][c8 >> 1]) : lo16(vr[i][c8 >> 1]));
            st16(r, W_U + (cu * 8 + c8) * 4096 + oc * 16, (v4u){pack2(u[7], u[6]), pack2(u[5], u[4]), pack2(u[3], u[2]), pack2(u[1], u[0])});
        }
        float ga[4];
#pragma unroll
        for (int i = 0; i < 4; ++i) { float acc = 0.f;
#pragma unroll
            for (int q = 0; q < 2; ++q)
#pragma unroll
                for (int e = 0; e < 4; ++e) { const unsigned xa = xg[i][q][e], gb = gw[q][e]; acc += rbfh(lo16(xa) * lo16(gb)); acc += rbfh(hi16(xa) * hi16(gb)); }
            ga[i] = acc; }
        for (int o = 32; o > 0; o >>= 1)
#pragma unroll
            for (int i = 0; i < 4; ++i) ga[i] += __shfl_xor(ga[i], o);
        const float z = rbfh(l == 0 ? ga[0] : l == 1 ? ga[1] : l == 2 ? ga[2] : ga[3]);
        st4(r, l < 4 ? W_GATE + (tk + 4 * l) * 4 : 0xF0000000u, __float_as_uint(rbfh(1.f / (1.f + expf(-z)))));
    }
    DRAIN(); __syncthreads();
    if (tid == 0) st4(r, F_PREP + k4 * 4, tag);
}
__device__ __forceinline__ bool conv_block(const Params& p, rsrc_t r, const int g0, const int n, unsigned char* lds, const int wv, const int j, const unsigned tag, const unsigned skew) {
    (void)j;
    const int tid = otid(), w = wv, l = tid & 63;
    const rsrc_t rs = __builtin_amdgcn_make_buffer_rsrc((void*)p.ssm_k, 0, S * D * 2, 0x00020000);
    v4u kr[4], uv[4]; v2u kx[4]; unsigned dn;
    auto fetch_k = [&](int ch) {
#pragma unroll
        for (int i = 0; i < 4; ++i) { const unsigned o = (unsigned)(ch * 4096 + (i * 64 + l) * 16);
            kr[i] = __builtin_amdgcn_raw_buffer_load_b128(rs, o, 0, 2); kx[i] = __builtin_amdgcn_raw_buffer_load_b64(rs, (i == 3 && l == 63) ? 0xF0000000u : o + 16, 0, 2); }
        dn = p.ssm_D[ch & 1023]; };
    auto fetch_u = [&](unsigned o) {
#pragma unroll
        for (int i = 0; i < 4; ++i) uv[i] = ld16(r, o + (i * 64 + l) * 16); };
    fetch_k(g0 * 4 + w);
    unsigned char* const uc = lds + w * CV_U; unsigned char* const kc = lds + CV_K0 + w * 4 * CV_KP;
    {
        const v4u z = {0u, 0u, 0u, 0u};
#pragma unroll
        for (int i = 0; i < 3; ++i) *(v4u*)(uc + min(i * 64 + l, 159) * 16) = z;
#pragma unroll
        for (int i = 0; i < 2; ++i) { const int q = min(i * 64 + l, 79), e = q % 20; *(v4u*)(kc + (q / 20) * CV_KP + e * 16 + (e < 8 ? 0 : 4096)) = z; }
    }
    if (!wait_prep(r, l, tag)) return false;
    wait_long(rtnow() + skew);
    fetch_u(W_U + (g0 * 4 + w) * 4096);
    const int b = l & 31, kg = l >> 5, cc = (b + 1) & 3;
#pragma unroll 1
    for (int it = 0; it < n; ++it) {
        const unsigned dv = dn;
#pragma unroll
        for (int i = 0; i < 4; ++i) { const v4u d = kr[i]; const v2u e = kx[i]; unsigned char* q = kc + 128 + (i * 64 + l) * 16;
            const unsigned s0 = __builtin_amdgcn_alignbit(d.y, d.x, 16), s1 = __builtin_amdgcn_alignbit(d.z, d.y, 16), s2 = __builtin_amdgcn_alignbit(d.w, d.z, 16), s3 = __builtin_amdgcn_alignbit(e.x, d.w, 16), s4 = __builtin_amdgcn_alignbit(e.y, e.x, 16);
            *(v4u*)q = d; *(v4u*)(q + CV_KP) = (v4u){s0, s1, s2, s3}; *(v4u*)(q + 2 * CV_KP) = (v4u){d.y, d.z, d.w, e.x}; *(v4u*)(q + 3 * CV_KP) = (v4u){s1, s2, s3, s4};
            if (i == 0 && l == 0) { *(unsigned*)(q + CV_KP - 4) = d.x << 16; *(unsigned*)(q + 2 * CV_KP - 4) = d.x; *(v2u*)(q + 3 * CV_KP - 8) = (v2u){d.x << 16, s0}; } }
#pragma unroll
        for (int i = 0; i < 4; ++i) *(v4u*)(uc + (32 + i * 16 + (l >> 2)) * CV_RP + (l & 3) * 16) = uv[i];
        __builtin_amdgcn_wave_barrier();
        __builtin_amdgcn_sched_barrier(0);
        fetch_k(it + 1 < n ? (g0 + it + 1) * 4 + w : 1024); fetch_u(it + 1 < n ? W_U + ((g0 + it + 1) * 4 + w) * 4096 : 0xF0000000u);
        __builtin_amdgcn_sched_barrier(0);
        f32x16 acc0, acc1, ad0, ad1;
#pragma unroll
        for (int i = 0; i < 16; ++i) { acc0[i] = 0.f; acc1[i] = 0.f; ad0[i] = 0.f; ad1[i] = 0.f; }
        typedef const __attribute__((address_space(3))) v2u* l2p;
        unsigned kb = (unsigned)(size_t)(kc + cc * CV_KP + (b - 8 * kg - 7 + 64 - cc) * 2) - 32, kb8 = kb + 8;
        asm volatile("" : "+v"(kb), "+v"(kb8));
        const unsigned char* ub = uc + (32 + b) * CV_RP + kg * 16;
        v2u ka[4][2], kz[4][2]; bf16x8 fa[4], fz[4];
        auto ld_k = [&](int set, int un, int hi) { const int dl = un >> 1, kh = un & 1; const unsigned o = (unsigned)((32 * dl - 16 * kh + 16) * 2 + hi * 2048);
            if (hi) { kz[set][0] = *(l2p)(size_t)(kb + o); kz[set][1] = *(l2p)(size_t)(kb8 + o); } else { ka[set][0] = *(l2p)(size_t)(kb + o); ka[set][1] = *(l2p)(size_t)(kb8 + o); } };
        auto ld_f = [&](int set, int un) { const int dl = un >> 1, kh = un & 1;
            fa[set] = *(const bf16x8*)(ub + (32 - dl) * CV_RP + kh * 32); fz[set] = *(const bf16x8*)(ub - dl * CV_RP + kh * 32); };
#pragma unroll
        for (int q = 0; q < 2; ++q) { ld_k(q, q, 0); ld_f(q, q); ld_k(q, q, 1); }
#pragma unroll
        for (int kh = 0; kh < 2; ++kh) { const int e = 7 - (b - 16 * kh - 8 * kg); const unsigned x = dv << ((e & 1) * 16); const int q = e >> 1;
            const v4u di = {q == 0 ? x : 0u, q == 1 ? x : 0u, q == 2 ? x : 0u, q == 3 ? x : 0u};
            asm volatile("s_nop 1\n\tv_mfma_f32_32x32x16_bf16 %0, %2, %4, %0\n\tv_mfma_f32_32x32x16_bf16 %1, %3, %4, %1" : "+v"(ad1), "+v"(ad0) : "v"(fa[kh]), "v"(fz[kh]), "v"(di)); }
#pragma unroll 1
        for (int u0 = 0; u0 < 64; u0 += 4) {
#pragma unroll
            for (int q = 0; q < 4; ++q) {
                const int un = u0 + q + 2, ns = (q + 2) & 3;
                const v4u kav = {ka[q][0].x, ka[q][0].y, ka[q][1].x, ka[q][1].y}, kzv = {kz[q][0].x, kz[q][0].y, kz[q][1].x, kz[q][1].y};
                __builtin_amdgcn_sched_barrier(0);
                asm volatile("s_nop 1\n\tv_mfma_f32_32x32x16_bf16 %0, %1, %2, %0" : "+v"(acc1) : "v"(fa[q]), "v"(kav));
                ld_k(ns, un, 0);
                __builtin_amdgcn_sched_barrier(0);
                asm volatile("v_mfma_f32_32x32x16_bf16 %0, %1, %2, %0" : "+v"(acc0) : "v"(fz[q]), "v"(kav));
                ld_f(ns, un);
                __builtin_amdgcn_sched_barrier(0);
                asm volatile("v_mfma_f32_32x32x16_bf16 %0, %1, %2, %0" : "+v"(acc1) : "v"(fz[q]), "v"(kzv));
                ld_k(ns, un, 1);
            }
        }
        asm volatile("s_nop 7\n\ts_nop 7" : "+v"(acc0), "+v"(acc1), "+v"(ad0), "+v"(ad1));
        unsigned char* const yb = lds + CV_Y + (it & 1) * 16384;
#pragma unroll
        for (int i = 0; i < 32; i += 2) { const int row = (i >> 4) * 32 + (i & 3) + 8 * ((i & 15) >> 2) + 4 * kg;
            const unsigned y = pack2(rbfh(i < 16 ? acc0[i] : acc1[i - 16]) + rbfh(i < 16 ? ad0[i] : ad1[i - 16]), rbfh(i < 16 ? acc0[i + 1] : acc1[i - 15]) + rbfh(i < 16 ? ad0[i + 1] : ad1[i - 15]));
            *(unsigned short*)(yb + (row * 32 + b) * 8 + w * 2) = (unsigned short)y; *(unsigned short*)(yb + (row * 32 + 32 + b) * 8 + w * 2) = (unsigned short)(y >> 16); }
        asm volatile("s_waitcnt lgkmcnt(0)\n\ts_barrier" ::: "memory");
#pragma unroll
        for (int i = 0; i < 4; ++i) { const int t = tid + 256 * i; st16(r, W_U + (g0 + it) * 16384 + t * 16, *(const v4u*)(yb + t * 16)); }
    }
    DRAIN(); __syncthreads();
    if (tid == 0) __hip_atomic_fetch_add((unsigned*)(p.ws + F_CONV), (unsigned)n, __ATOMIC_RELAXED, __HIP_MEMORY_SCOPE_AGENT);
    return true;
}

__device__ __forceinline__ void dma16(unsigned ldsdst, unsigned voff, const v4u& srd, unsigned soff) {
    asm volatile("s_mov_b32 m0, %0\n\ts_nop 0\n\tbuffer_load_dwordx4 %1, %2, %3 offen lds" :: "s"(ldsdst), "v"(voff), "s"(srd), "s"(soff) : "m0", "memory"); }
__device__ __forceinline__ void dma16n(unsigned ldsdst, unsigned voff, const v4u& srd, unsigned soff) {
    asm volatile("s_mov_b32 m0, %0\n\ts_nop 0\n\tbuffer_load_dwordx4 %1, %2, %3 offen nt lds" :: "s"(ldsdst), "v"(voff), "s"(srd), "s"(soff) : "m0", "memory"); }
__device__ __forceinline__ v4u pack_acc(const f32x4& a, const f32x4& b) {
    float x0, x1, x2, x3, y0, y1, y2, y3;
    asm volatile("v_accvgpr_read_b32 %0, %8\n\tv_accvgpr_read_b32 %1, %9\n\tv_accvgpr_read_b32 %2, %10\n\tv_accvgpr_read_b32 %3, %11\n\t"
                 "v_accvgpr_read_b32 %4, %12\n\tv_accvgpr_read_b32 %5, %13\n\tv_accvgpr_read_b32 %6, %14\n\tv_accvgpr_read_b32 %7, %15"
                 : "=v"(x0), "=v"(x1), "=v"(x2), "=v"(x3), "=v"(y0), "=v"(y1), "=v"(y2), "=v"(y3)
                 : "a"(a[0]), "a"(a[1]), "a"(a[2]), "a"(a[3]), "a"(b[0]), "a"(b[1]), "a"(b[2]), "a"(b[3]));
    return (v4u){pack2(x0, x1), pack2(x2, x3), pack2(y0, y1), pack2(y2, y3)}; }
#define SB() __builtin_amdgcn_sched_barrier(0)
#define WO_REGS "a128", "a129", "a130", "a131", "a132", "a133", "a134", "a135", "a136", "a137", "a138", "a139", "a140", "a141", "a142", "a143", "a144", "a145", "a146", "a147", "a148", "a149", "a150", "a151", "a152", "a153", "a154", "a155", "a156", "a157", "a158", "a159", "a160", "a161", "a162", "a163", "a164", "a165", "a166", "a167", "a168", "a169", "a170", "a171", "a172", "a173", "a174", "a175", "a176", "a177", "a178", "a179", "a180", "a181", "a182", "a183", "a184", "a185", "a186", "a187", "a188", "a189", "a190", "a191", "a192", "a193", "a194", "a195", "a196", "a197", "a198", "a199", "a200", "a201", "a202", "a203", "a204", "a205", "a206", "a207", "a208", "a209", "a210", "a211", "a212", "a213", "a214", "a215", "a216", "a217", "a218", "a219", "a220", "a221", "a222", "a223", "a224", "a225", "a226", "a227", "a228", "a229", "a230", "a231", "a232", "a233", "a234", "a235", "a236", "a237", "a238", "a239", "a240", "a241", "a242", "a243", "a244", "a245", "a246", "a247", "a248", "a249", "a250", "a251", "a252", "a253", "a254", "a255", "v192", "v193", "v194", "v195", "v196", "v197", "v198", "v199", "v200", "v201", "v202", "v203", "v204", "v205", "v206", "v207", "v208", "v209", "v210", "v211", "v212", "v213", "v214", "v215", "v216", "v217", "v218", "v219", "v220", "v221", "v222", "v223", "v224", "v225", "v226", "v227", "v228", "v229", "v230", "v231", "v232", "v233", "v234", "v235", "v236", "v237", "v238", "v239", "v240", "v241", "v242", "v243", "v244", "v245", "v246", "v247", "v248", "v249", "v250", "v251", "v252", "v253", "v254", "v255"
struct NoHook { __device__ void operator()(int) const {} };
template <int MODE, int MI, int NJ, int NS, int HX = 0, class HOOK = NoHook, int MN = MI>
__device__ __forceinline__ void gemm(rsrc_t r, const bf16_t* __restrict__ A, const bf16_t* __restrict__ B, int m0, int n0, unsigned char* lds, unsigned coff, const int wv,
                                     const int rot, const void* tk = nullptr,
                                     const bool have = false, const bf16_t* Bn = nullptr, int m0n = 0, int n0n = 0, HOOK&& hook = NoHook()) {
    constexpr int AB = MN * 4096, BB = NJ * 2048, B0 = (NS / 2) * AB, NB = NJ / 2, NM = MI * NJ, NR = MI + NJ, BP = NJ * 64;
    static_assert(NS % 2 == 0 && 32 % NS == 0 && B0 <= 65536 && NS * BB <= 65536 && (NJ == 4 || NJ == 8), "ring");
    const int tid = otid();
    const int w = wv, l = tid & 63, n16 = l & 15, g = l >> 4;
    const int wm = w >> 1, wn = w & 1;
    const unsigned lb = __builtin_amdgcn_readfirstlane((unsigned)(size_t)lds);
    const v4u sa = mk_srd(A, 2048 * 1024 * 2); v4u sb = mk_srd(B, 1024 * 4096 * 2);
    f32x4 acc[MI][NJ];
#pragma unroll
    for (int i = 0; i < MI; ++i)
#pragma unroll
        for (int j = 0; j < NJ; ++j) acc[i][j] = (f32x4){0.f, 0.f, 0.f, 0.f};
    unsigned va = (unsigned)((m0 + 8 * MI * w + (l >> 3)) * 2048 + (((l & 7) ^ (l >> 3)) << 4));
    unsigned vb[2];
    if (NJ == 8) { vb[0] = (unsigned)((8 * w + (l >> 5)) * 8192 + n0 * 2) + (unsigned)(((l & 31) << 4) ^ ((l >> 5) << 5) ^ (wn << 7)); vb[1] = vb[0] ^ 64u; }
    else { vb[0] = (unsigned)((8 * w + g) * 8192 + n0 * 2) + (unsigned)((n16 << 4) ^ (g << 5) ^ (wn << 7)); vb[1] = vb[0]; }
    auto rk = [&](int kt) { return (kt & ~31) | ((kt + rot) & 31); };
    unsigned la = lb + w * MI * 1024;
    auto st_a = [&](int buf, int kt, int q) { dma16(la + buf * AB + q * 1024, va, sa, (unsigned)(rk(kt) * 64 + q * 16384)); };
    auto st_b = [&](int buf, int kt, int x) { if ((B_NT >> (NJ == 4 ? 0 : MI == 4 ? 1 : 2)) & 1) dma16n(lb + B0 + buf * BB + (w * NB + x) * 1024, vb[x & 1], sb, (unsigned)((rk(kt) * 32 + x * (8 / NB)) * 8192));
        else dma16(lb + B0 + buf * BB + (w * NB + x) * 1024, vb[x & 1], sb, (unsigned)((rk(kt) * 32 + x * (8 / NB)) * 8192)); };
    const unsigned f = (unsigned)((n16 >> 2) | ((g & 1) << 2));
    constexpr int NQ = MODE == 1 ? NJ : NJ / 2;
    unsigned ra[2], rb[NQ];
    ra[0] = lb + (16 * MI * wm + n16) * 128 + ((g ^ (n16 & 7)) << 4); ra[1] = ra[0] ^ 64u;
    {   const unsigned row = lb + B0 + (8 * g + (n16 >> 2)) * BP + (NJ == 8 ? 256 * wn : 0), g0 = NJ == 8 ? 0u : 4u * wn;
#pragma unroll
        for (int j = 0; j < NQ; ++j) {
            if (MODE == 1) rb[j] = row + (((g0 + j) ^ f) << 5) + 8 * (n16 & 3);
            else rb[j] = row + (((g0 + 2 * j + ((n16 & 3) >> 1)) ^ f) << 5) + (n16 & 1) * 16;
            asm volatile("" : "+v"(rb[j])); } }
    bf16x8 af[2][MI], bfr[2][NJ];
    auto rd = [&](int set, int buf, int x) {
        if (x < NJ) { const unsigned a = rb[MODE == 1 ? x : (x >> 1)] + (MODE == 1 ? 0 : (x & 1) * 8) + buf * BB;
            const v2u lo = trb(a), hi = trb(a + 4 * BP); v4u v = {lo.x, lo.y, hi.x, hi.y}; bfr[set][x] = as_frag(v); }
        else af[set][x - NJ] = ldsa(ra[buf & 1] + (buf >> 1) * AB + (x - NJ) * 2048); };
    auto mf = [&](int set, int i, int j) {
        if (HX) asm volatile("v_mfma_f32_16x16x32_bf16 %0, %1, %2, %0" : "+a"(acc[i][j]) : "v"(MODE == 1 ? af[set][i] : bfr[set][j]), "v"(MODE == 1 ? bfr[set][j] : af[set][i]) : WO_REGS);
        else if (MODE == 1) asm volatile("v_mfma_f32_16x16x32_bf16 %0, %1, %2, %0" : "+a"(acc[i][j]) : "v"(af[set][i]), "v"(bfr[set][j]));
        else asm volatile("v_mfma_f32_16x16x32_bf16 %0, %1, %2, %0" : "+a"(acc[i][j]) : "v"(bfr[set][j]), "v"(af[set][i])); };
    if (!have) {
#pragma unroll
        for (int t = 0; t < NS; ++t) {
            if (t & 1) {
#pragma unroll
                for (int q = 0; q < MI; ++q) st_a(t >> 1, t - 1, q); }
#pragma unroll
            for (int q = 0; q < NB; ++q) st_b(t, t, q);
            if (HX) hook(t); }
    }
#pragma unroll
    for (int i = 0; i < MI; ++i)
#pragma unroll
        for (int j = 0; j < NJ; ++j) asm volatile("" : "+a"(acc[i][j]));
    asm volatile("s_nop 1" ::: "memory");
    if (!have) {
        asm volatile("s_waitcnt vmcnt(%c0)\n\ts_barrier" :: "i"((NS - 1) * (NB + HX) + (NS / 2 - 1) * MI) : "memory");
    }
#pragma unroll
    for (int x = 0; x < NR; ++x) rd(0, 0, x);
    constexpr int HEAD = NM / 8;
    const v4u st = mk_srd(tk, tk ? 32768 : 0); unsigned tdum = 0u;
#pragma unroll 1
    for (int k0 = 0; k0 < 32; k0 += NS) {
        int kb = k0 + NS;
        if (kb == 32) {
            kb = Bn ? 0 : 0x10100;
            if (Bn) { va += (unsigned)((m0n - m0 + 8 * (MN - MI) * w) * 2048); la = lb + w * MN * 1024; vb[0] += (unsigned)((n0n - n0) * 2); vb[1] += (unsigned)((n0n - n0) * 2); sb = mk_srd(Bn, 1024 * 4096 * 2); } }
        if (NS == 4) asm volatile("buffer_load_dword %0, %1, %2, %3 offen" : "+v"(tdum) : "v"(l < 32 ? (unsigned)l * 128u : 0xF0000000u), "s"(st), "s"((unsigned)k0 * 1024u) : "memory");
#pragma unroll
        for (int u = 0; u < NS; ++u) {
            SB();
#pragma unroll
            for (int q = 0; q < HEAD; ++q) mf(u & 1, q / NJ, q % NJ);
            SB();
            asm volatile("s_waitcnt vmcnt(%c0) lgkmcnt(0)\n\ts_barrier" :: "i"((NS - 2) * (NB + HX) + (NS / 2 - 1 - (u & 1)) * MI) : "memory");
            SB();
            constexpr int span = NM - HEAD;
#pragma unroll
            for (int q = HEAD; q < NM; ++q) {
                mf(u & 1, q / NJ, q % NJ);
                const int x = q - HEAD, nd = NB + ((u & 1) ? MI : 0);
#pragma unroll
                for (int y = 0; y < NR; ++y) if ((y * (span - HEAD)) / NR == x) rd((u & 1) ^ 1, (u + 1) % NS, y);
#pragma unroll
                for (int y = 0; y < nd; ++y) if ((y * span + span / 2) / nd == x) { if ((u & 1) && y < MI) st_a(u >> 1, kb + u - 1, y); else st_b(u, kb + u, y - ((u & 1) ? MI : 0)); }
                SB(); }
            if (MN > MI && (u & 1) && k0 == 32 - NS) {
#pragma unroll
                for (int y = MI; y < MN; ++y) st_a(u >> 1, kb + u - 1, y); }
            if (HX) hook(NS + k0 + u);
        }
    }
    asm volatile("s_nop 7\n\ts_nop 3" : "+v"(tdum) :: "memory");
    SB();
    const int col = n0 + 16 * NJ * wn, head = col >> 7, sub = (col & 127) >> 4, seg0 = (m0 >> 5) + (MI / 2) * wm;
#pragma unroll
    for (int sg = 0; sg < MI / 2; ++sg) {
        const unsigned base = coff + (unsigned)(((seg0 + sg) * 32 + head) * 8192 + l * 16);
#pragma unroll
        for (int q = 0; q < NJ; ++q) {
            if (HX) asm volatile("" ::: WO_REGS);
            if (EPI_SLP && NJ == 8) { SB(); __builtin_amdgcn_s_sleep(EPI_SLP); SB(); }
            if (MODE == 0) { const int tt = q / (NJ / 2), kk = q % (NJ / 2); st16(r, base + (tt * 4 + (sub >> 1) + kk) * 1024, pack_acc(acc[2 * sg + tt][2 * kk], acc[2 * sg + tt][2 * kk + 1])); }
            else st16(r, base + (sub + q) * 1024, pack_acc(acc[2 * sg][q], acc[2 * sg + 1][q])); }
    }
}

__device__ __forceinline__ void attn_block(const Params& p, const int wv, const int h, const unsigned tag) {
    const int l = otid() & 63, n16 = l & 15, g = l >> 4;
    const unsigned OOB = 0x80000000u;
    const v4u rd = mk_srd(p.ws, W_TOTAL);
    const unsigned kx = W_KX + h * 8192, qx = W_QX + h * 8192, vx = W_VF + h * 8192, km = HO_KVM + h * 4096;
    const v8u pc = {(unsigned)l * 16, (unsigned)(n16 * 16 + (g & 1) * 8), F_KVQ + ((l >> 2) * 32 + h) * 64 + min(l & 3, 2) * 4, F_PROG + h * 128 + min(l, 2) * 8, (unsigned)(wv * 2048 + l * 16),
                    (unsigned)(wv * 2048 + (8 * g + (n16 >> 2)) * 16 + (n16 & 1) * 8), (l & 31) ? OOB : F_ATT + (l >> 5) * 64, l < 32 ? F_RDY + l * 128 : OOB};
    const int dq = n16 - 4 * g;
    asm volatile(
        "s_mov_b32 s36, 0xf0000000\n\t"
        "s_mov_b32 s37, %[w]\n\t"
        "s_mov_b32 s38, s36\n\t"
        "s_mov_b32 s40, 0\n\t"
        "s_memrealtime s[98:99]\n\t"
        "s_waitcnt lgkmcnt(0)\n\t"
        "s_sub_u32 s41, s98, 0x100000\n\t"
        "s_mov_b64 s[42:43], 0\n\t"
        "s_mov_b32 s44, -1\n\t"
        "s_mov_b32 s45, -1\n\t"
        "s_mov_b32 s56, 0\n\t"
        "s_mov_b32 s57, -1\n\t"
        "s_lshl_b32 s58, %[w], 10\n\t"
        "s_add_u32 s58, s58, 16384\n\t"
        "v_cmp_le_i32_e64 s[48:49], 0, v252\n\t"
        "v_cmp_le_i32_e64 s[50:51], 1, v252\n\t"
        "v_cmp_le_i32_e64 s[52:53], 2, v252\n\t"
        "v_cmp_le_i32_e64 s[54:55], 3, v252\n\t"
        "v_mov_b32_e32 v253, 0xce6e6b28\n\t"
        "v_mov_b32_e32 v254, 0xff800000\n\t"
        ".Ltop%=:\n\t"
        "s_cmp_eq_u32 s38, 0\n\t"
        "s_cbranch_scc1 .La2%=\n\t"
        "s_mov_b32 s97, 0\n\t"
        ".La1%=:\n\t"
        "buffer_load_dword v240, v246, %[rd], 0 offen sc1\n\t"
        "buffer_load_dword v241, v247, %[rd], 0 offen sc1\n\t"
        "s_add_u32 s97, s97, 1\n\t"
        "s_cmp_lt_u32 s97, 0x200000\n\t"
        "s_cbranch_scc0 .Lgu%=\n\t"
        "s_memrealtime s[98:99]\n\t"
        "s_waitcnt vmcnt(0) lgkmcnt(0)\n\t"
        "v_cmp_eq_u32_e32 vcc, %[tag], v240\n\t"
        "s_nop 1\n\t"
        "s_or_b64 s[42:43], s[42:43], vcc\n\t"
        "v_readlane_b32 s80, v241, 0\n\t"
        "v_readlane_b32 s81, v241, 1\n\t"
        "v_readlane_b32 s82, v241, 2\n\t"
        "s_min_u32 s80, s80, s81\n\t"
        "s_min_u32 s80, s80, s82\n\t"
        "s_cmp_gt_u32 s80, s40\n\t"
        "s_cselect_b32 s41, s98, s41\n\t"
        "s_max_u32 s40, s40, s80\n\t"
        "s_and_b32 s80, s37, 60\n\t"
        "s_lshr_b64 s[80:81], s[42:43], s80\n\t"
        "s_andn2_b32 s80, 15, s80\n\t"
        "s_cbranch_scc0 .La1%=x\n\t"
        "s_sleep 16\n\t"
        "s_branch .La1%=\n\t"
        ".La1%=x:\n\t"
        "s_cmp_gt_u32 s40, s37\n\t"
        "s_cselect_b32 s39, 0, s36\n\t"
        "s_lshl_b32 s80, s37, 18\n\t"
        "s_or_b32 s80, s80, 0\n\t"
        "s_lshl_b32 s81, s37, 17\n\t"
        "s_add_u32 s81, s81, %[km]\n\t"
        "s_or_b32 s81, s81, s39\n\t"
        "s_add_u32 s82, s80, %[kx]\n\t"
        "s_add_u32 s83, s82, 0x1000\n\t"
        "buffer_load_dwordx4 v[0:3], v244, %[rd], s82 offen offset:0 sc1\n\t"
        "buffer_load_dwordx4 v[4:7], v244, %[rd], s82 offen offset:1024 sc1\n\t"
        "buffer_load_dwordx4 v[8:11], v244, %[rd], s82 offen offset:2048 sc1\n\t"
        "buffer_load_dwordx4 v[12:15], v244, %[rd], s82 offen offset:3072 sc1\n\t"
        "buffer_load_dwordx4 v[16:19], v244, %[rd], s83 offen offset:0 sc1\n\t"
        "buffer_load_dwordx4 v[20:23], v244, %[rd], s83 offen offset:1024 sc1\n\t"
        "buffer_load_dwordx4 v[24:27], v244, %[rd], s83 offen offset:2048 sc1\n\t"
        "buffer_load_dwordx4 v[28:31], v244, %[rd], s83 offen offset:3072 sc1\n\t"
        "s_cmp_eq_u64 s[42:43], -1\n\t"
        "s_cbranch_scc1 .La3%=\n\t"
        "s_sleep %c[aslp]\n\t"
        ".La3%=:\n\t"
        "s_sleep %c[bslp]\n\t"
        "s_add_u32 s82, s80, %[qx]\n\t"
        "s_add_u32 s83, s82, 0x1000\n\t"
        "buffer_load_dwordx4 v[32:35], v244, %[rd], s82 offen offset:0 sc1\n\t"
        "buffer_load_dwordx4 v[36:39], v244, %[rd], s82 offen offset:1024 sc1\n\t"
        "buffer_load_dwordx4 v[40:43], v244, %[rd], s82 offen offset:2048 sc1\n\t"
        "buffer_load_dwordx4 v[44:47], v244, %[rd], s82 offen offset:3072 sc1\n\t"
        "buffer_load_dwordx4 v[48:51], v244, %[rd], s83 offen offset:0 sc1\n\t"
        "buffer_load_dwordx4 v[52:55], v244, %[rd], s83 offen offset:1024 sc1\n\t"
        "buffer_load_dwordx4 v[56:59], v244, %[rd], s83 offen offset:2048 sc1\n\t"
        "buffer_load_dwordx4 v[60:63], v244, %[rd], s83 offen offset:3072 sc1\n\t"
        "s_cmp_eq_u64 s[42:43], -1\n\t"
        "s_cbranch_scc1 .La4%=\n\t"
        "s_sleep %c[aslp]\n\t"
        ".La4%=:\n\t"
        "s_sleep %c[bslp]\n\t"
        "buffer_load_dwordx4 v[96:99], v244, %[rd], s81 offen offset:0 sc1\n\t"
        "buffer_load_dwordx4 v[100:103], v244, %[rd], s81 offen offset:1024 sc1\n\t"
        "buffer_load_dwordx2 v[104:105], v245, %[rd], s81 offen offset:2048 sc1\n\t"
        "buffer_load_dwordx2 v[106:107], v245, %[rd], s81 offen offset:2304 sc1\n\t"
        "buffer_load_dwordx2 v[108:109], v245, %[rd], s81 offen offset:2560 sc1\n\t"
        "buffer_load_dwordx2 v[110:111], v245, %[rd], s81 offen offset:2816 sc1\n\t"
        "buffer_load_dwordx2 v[112:113], v245, %[rd], s81 offen offset:3072 sc1\n\t"
        "buffer_load_dwordx2 v[114:115], v245, %[rd], s81 offen offset:3328 sc1\n\t"
        "buffer_load_dwordx2 v[116:117], v245, %[rd], s81 offen offset:3584 sc1\n\t"
        "buffer_load_dwordx2 v[118:119], v245, %[rd], s81 offen offset:3840 sc1\n\t"
        "s_add_u32 s82, s80, %[vx]\n\t"
        "s_add_u32 s83, s82, 0x1000\n\t"
        "buffer_load_dwordx4 v[64:67], v244, %[rd], s82 offen offset:0 sc1\n\t"
        "buffer_load_dwordx4 v[68:71], v244, %[rd], s82 offen offset:1024 sc1\n\t"
        "buffer_load_dwordx4 v[72:75], v244, %[rd], s82 offen offset:2048 sc1\n\t"
        "buffer_load_dwordx4 v[76:79], v244, %[rd], s82 offen offset:3072 sc1\n\t"
        "buffer_load_dwordx4 v[80:83], v244, %[rd], s83 offen offset:0 sc1\n\t"
        "buffer_load_dwordx4 v[84:87], v244, %[rd], s83 offen offset:1024 sc1\n\t"
        "buffer_load_dwordx4 v[88:91], v244, %[rd], s83 offen offset:2048 sc1\n\t"
        "buffer_load_dwordx4 v[92:95], v244, %[rd], s83 offen offset:3072 sc1\n\t"
        "s_cmp_eq_u64 s[42:43], -1\n\t"
        "s_cbranch_scc1 .La5%=\n\t"
        "s_sleep %c[aslp]\n\t"
        ".La5%=:\n\t"
        "s_sleep %c[bslp]\n\t"
        "buffer_store_dword v0, off, %[rd], s36\n\t"
        "buffer_store_dword v0, off, %[rd], s36\n\t"
        "buffer_store_dword v0, off, %[rd], s36\n\t"
        "buffer_store_dword v0, off, %[rd], s36\n\t"
        "buffer_store_dword v0, off, %[rd], s36\n\t"
        "buffer_store_dword v0, off, %[rd], s36\n\t"
        "buffer_store_dword v0, off, %[rd], s36\n\t"
        "buffer_store_dword v0, off, %[rd], s36\n\t"
        "buffer_store_dword v0, off, %[rd], s36\n\t"
        ".La2%=:\n\t"
        "s_cmp_gt_u32 s40, s37\n\t"
        "s_cselect_b32 s80, s39, 0\n\t"
        "s_cmp_eq_u32 s80, 0\n\t"
        "s_cbranch_scc1 .La6%=\n\t"
        "s_mov_b32 s39, 0\n\t"
        "s_lshl_b32 s81, s37, 17\n\t"
        "s_add_u32 s81, s81, %[km]\n\t"
        "buffer_load_dwordx4 v[96:99], v244, %[rd], s81 offen offset:0 sc1\n\t"
        "buffer_load_dwordx4 v[100:103], v244, %[rd], s81 offen offset:1024 sc1\n\t"
        "buffer_load_dwordx2 v[104:105], v245, %[rd], s81 offen offset:2048 sc1\n\t"
        "buffer_load_dwordx2 v[106:107], v245, %[rd], s81 offen offset:2304 sc1\n\t"
        "buffer_load_dwordx2 v[108:109], v245, %[rd], s81 offen offset:2560 sc1\n\t"
        "buffer_load_dwordx2 v[110:111], v245, %[rd], s81 offen offset:2816 sc1\n\t"
        "buffer_load_dwordx2 v[112:113], v245, %[rd], s81 offen offset:3072 sc1\n\t"
        "buffer_load_dwordx2 v[114:115], v245, %[rd], s81 offen offset:3328 sc1\n\t"
        "buffer_load_dwordx2 v[116:117], v245, %[rd], s81 offen offset:3584 sc1\n\t"
        "buffer_load_dwordx2 v[118:119], v245, %[rd], s81 offen offset:3840 sc1\n\t"
        "buffer_store_dword v0, off, %[rd], s36\n\t"
        "buffer_store_dword v0, off, %[rd], s36\n\t"
        "buffer_store_dword v0, off, %[rd], s36\n\t"
        "buffer_store_dword v0, off, %[rd], s36\n\t"
        "buffer_store_dword v0, off, %[rd], s36\n\t"
        "buffer_store_dword v0, off, %[rd], s36\n\t"
        "buffer_store_dword v0, off, %[rd], s36\n\t"
        "buffer_store_dword v0, off, %[rd], s36\n\t"
        ".La6%=:\n\t"
        "s_add_u32 s86, s37, 4\n\t"
        "s_and_b32 s80, s86, 60\n\t"
        "s_lshr_b64 s[80:81], s[42:43], s80\n\t"
        "s_andn2_b32 s80, 15, s80\n\t"
        "s_cselect_b32 s46, s36, 0\n\t"
        "s_cmp_lt_u32 s86, 64\n\t"
        "s_cselect_b32 s46, s46, s36\n\t"
        "s_cmp_gt_u32 s40, s86\n\t"
        "s_cselect_b32 s47, s46, s36\n\t"
        "s_lshl_b32 s80, s86, 18\n\t"
        "s_or_b32 s80, s80, s46\n\t"
        "s_lshl_b32 s81, s86, 17\n\t"
        "s_add_u32 s81, s81, %[km]\n\t"
        "s_or_b32 s81, s81, s47\n\t"
        "s_add_u32 s82, s80, %[kx]\n\t"
        "s_add_u32 s83, s82, 0x1000\n\t"
        "buffer_load_dwordx4 a[0:3], v244, %[rd], s82 offen offset:0 sc1\n\t"
        "buffer_load_dwordx4 a[4:7], v244, %[rd], s82 offen offset:1024 sc1\n\t"
        "buffer_load_dwordx4 a[8:11], v244, %[rd], s82 offen offset:2048 sc1\n\t"
        "buffer_load_dwordx4 a[12:15], v244, %[rd], s82 offen offset:3072 sc1\n\t"
        "buffer_load_dwordx4 a[16:19], v244, %[rd], s83 offen offset:0 sc1\n\t"
        "buffer_load_dwordx4 a[20:23], v244, %[rd], s83 offen offset:1024 sc1\n\t"
        "buffer_load_dwordx4 a[24:27], v244, %[rd], s83 offen offset:2048 sc1\n\t"
        "buffer_load_dwordx4 a[28:31], v244, %[rd], s83 offen offset:3072 sc1\n\t"
        "s_cmp_eq_u64 s[42:43], -1\n\t"
        "s_cbranch_scc1 .La7%=\n\t"
        "s_sleep %c[aslp]\n\t"
        ".La7%=:\n\t"
        "s_sleep %c[bslp]\n\t"
        "s_add_u32 s82, s80, %[qx]\n\t"
        "s_add_u32 s83, s82, 0x1000\n\t"
        "buffer_load_dwordx4 a[32:35], v244, %[rd], s82 offen offset:0 sc1\n\t"
        "buffer_load_dwordx4 a[36:39], v244, %[rd], s82 offen offset:1024 sc1\n\t"
        "buffer_load_dwordx4 a[40:43], v244, %[rd], s82 offen offset:2048 sc1\n\t"
        "buffer_load_dwordx4 a[44:47], v244, %[rd], s82 offen offset:3072 sc1\n\t"
        "buffer_load_dwordx4 a[48:51], v244, %[rd], s83 offen offset:0 sc1\n\t"
        "buffer_load_dwordx4 a[52:55], v244, %[rd], s83 offen offset:1024 sc1\n\t"
        "buffer_load_dwordx4 a[56:59], v244, %[rd], s83 offen offset:2048 sc1\n\t"
        "buffer_load_dwordx4 a[60:63], v244, %[rd], s83 offen offset:3072 sc1\n\t"
        "s_cmp_eq_u64 s[42:43], -1\n\t"
        "s_cbranch_scc1 .La8%=\n\t"
        "s_sleep %c[aslp]\n\t"
        ".La8%=:\n\t"
        "s_sleep %c[bslp]\n\t"
        "buffer_load_dwordx4 a[96:99], v244, %[rd], s81 offen offset:0 sc1\n\t"
        "buffer_load_dwordx4 a[100:103], v244, %[rd], s81 offen offset:1024 sc1\n\t"
        "buffer_load_dwordx2 a[104:105], v245, %[rd], s81 offen offset:2048 sc1\n\t"
        "buffer_load_dwordx2 a[106:107], v245, %[rd], s81 offen offset:2304 sc1\n\t"
        "buffer_load_dwordx2 a[108:109], v245, %[rd], s81 offen offset:2560 sc1\n\t"
        "buffer_load_dwordx2 a[110:111], v245, %[rd], s81 offen offset:2816 sc1\n\t"
        "buffer_load_dwordx2 a[112:113], v245, %[rd], s81 offen offset:3072 sc1\n\t"
        "buffer_load_dwordx2 a[114:115], v245, %[rd], s81 offen offset:3328 sc1\n\t"
        "buffer_load_dwordx2 a[116:117], v245, %[rd], s81 offen offset:3584 sc1\n\t"
        "buffer_load_dwordx2 a[118:119], v245, %[rd], s81 offen offset:3840 sc1\n\t"
        "s_add_u32 s82, s80, %[vx]\n\t"
        "s_add_u32 s83, s82, 0x1000\n\t"
        "buffer_load_dwordx4 a[64:67], v244, %[rd], s82 offen offset:0 sc1\n\t"
        "buffer_load_dwordx4 a[68:71], v244, %[rd], s82 offen offset:1024 sc1\n\t"
        "buffer_load_dwordx4 a[72:75], v244, %[rd], s82 offen offset:2048 sc1\n\t"
        "buffer_load_dwordx4 a[76:79], v244, %[rd], s82 offen offset:3072 sc1\n\t"
        "buffer_load_dwordx4 a[80:83], v244, %[rd], s83 offen offset:0 sc1\n\t"
        "buffer_load_dwordx4 a[84:87], v244, %[rd], s83 offen offset:1024 sc1\n\t"
        "buffer_load_dwordx4 a[88:91], v244, %[rd], s83 offen offset:2048 sc1\n\t"
        "buffer_load_dwordx4 a[92:95], v244, %[rd], s83 offen offset:3072 sc1\n\t"
        "s_cmp_eq_u64 s[42:43], -1\n\t"
        "s_cbranch_scc1 .La9%=\n\t"
        "s_sleep %c[aslp]\n\t"
        ".La9%=:\n\t"
        "s_sleep %c[bslp]\n\t"
        "s_waitcnt vmcnt(42)\n\t"
        "s_memrealtime s[98:99]\n\t"
        "v_mfma_f32_16x16x32_bf16 v[136:139], v[0:3], v[32:35], 0\n\t"
        "v_mfma_f32_16x16x32_bf16 v[148:151], v[0:3], v[48:51], 0\n\t"
        "v_mfma_f32_16x16x32_bf16 v[152:155], v[16:19], v[48:51], 0\n\t"
        "v_mfma_f32_16x16x32_bf16 v[136:139], v[4:7], v[36:39], v[136:139]\n\t"
        "v_mfma_f32_16x16x32_bf16 v[148:151], v[4:7], v[52:55], v[148:151]\n\t"
        "v_mfma_f32_16x16x32_bf16 v[152:155], v[20:23], v[52:55], v[152:155]\n\t"
        "v_mfma_f32_16x16x32_bf16 v[136:139], v[8:11], v[40:43], v[136:139]\n\t"
        "v_mfma_f32_16x16x32_bf16 v[148:151], v[8:11], v[56:59], v[148:151]\n\t"
        "v_mfma_f32_16x16x32_bf16 v[152:155], v[24:27], v[56:59], v[152:155]\n\t"
        "v_mfma_f32_16x16x32_bf16 v[136:139], v[12:15], v[44:47], v[136:139]\n\t"
        "v_mfma_f32_16x16x32_bf16 v[148:151], v[12:15], v[60:63], v[148:151]\n\t"
        "v_mfma_f32_16x16x32_bf16 v[152:155], v[28:31], v[60:63], v[152:155]\n\t"
        "s_waitcnt lgkmcnt(0)\n\t"
        "v_cmp_eq_u32_e32 vcc, %[tag], v240\n\t"
        "s_nop 1\n\t"
        "s_or_b64 s[42:43], s[42:43], vcc\n\t"
        "v_readlane_b32 s80, v241, 0\n\t"
        "v_readlane_b32 s81, v241, 1\n\t"
        "v_readlane_b32 s82, v241, 2\n\t"
        "s_min_u32 s80, s80, s81\n\t"
        "s_min_u32 s80, s80, s82\n\t"
        "s_cmp_gt_u32 s80, s40\n\t"
        "s_cselect_b32 s41, s98, s41\n\t"
        "s_max_u32 s40, s40, s80\n\t"
        "v_readlane_b32 s84, v242, 0\n\t"
        "v_readlane_b32 s85, v242, 32\n\t"
        "v_mov_b32_e32 v243, 32\n\t"
        "s_and_b32 s80, s45, 3\n\t"
        "s_lshl_b32 s80, s80, 12\n\t"
        "s_lshr_b32 s81, s45, 2\n\t"
        "s_lshl_b32 s81, s81, 3\n\t"
        "s_add_u32 s80, s80, s81\n\t"
        "s_and_b32 s81, s45, 1\n\t"
        "s_lshl_b32 s81, s81, 13\n\t"
        "s_lshr_b32 s82, s45, 1\n\t"
        "s_lshl_b32 s82, s82, 2\n\t"
        "s_add_u32 s81, s81, s82\n\t"
        "s_cmp_lt_i32 s45, 60\n\t"
        "s_cselect_b32 s80, s80, s81\n\t"
        "s_cselect_b32 s81, 4, 0x1000\n\t"
        "s_add_u32 s81, s81, s80\n\t"
        "s_cmp_eq_u32 s84, 31\n\t"
        "s_cselect_b32 s80, s80, s36\n\t"
        "s_cmp_eq_u32 s85, 31\n\t"
        "s_cselect_b32 s81, s81, s36\n\t"
        "s_cmp_lt_i32 s45, 0\n\t"
        "s_cselect_b32 s80, s36, s80\n\t"
        "s_cselect_b32 s81, s36, s81\n\t"
        "buffer_store_dword v243, v251, %[rd], s80 offen sc1\n\t"
        "buffer_store_dword v243, v251, %[rd], s81 offen sc1\n\t"
        "s_add_u32 s86, s37, 8\n\t"
        "s_cmp_lg_u64 s[42:43], -1\n\t"
        "s_cselect_b32 s84, 0, s36\n\t"
        "s_cmp_gt_u32 s40, s86\n\t"
        "s_cselect_b32 s85, s36, s39\n\t"
        "buffer_load_dword v240, v246, %[rd], s84 offen sc1\n\t"
        "buffer_load_dword v241, v247, %[rd], s85 offen sc1\n\t"
        "v_cvt_pk_bf16_f32 v136, 0, v136\n\t"
        "v_cvt_pk_bf16_f32 v137, 0, v137\n\t"
        "v_cvt_pk_bf16_f32 v138, 0, v138\n\t"
        "v_cvt_pk_bf16_f32 v139, 0, v139\n\t"
        "v_mul_f32_e32 v136, 0x3db504f3, v136\n\t"
        "v_mul_f32_e32 v137, 0x3db504f3, v137\n\t"
        "v_mul_f32_e32 v138, 0x3db504f3, v138\n\t"
        "v_mul_f32_e32 v139, 0x3db504f3, v139\n\t"
        "v_cvt_pk_bf16_f32 v136, 0, v136\n\t"
        "v_cvt_pk_bf16_f32 v137, 0, v137\n\t"
        "v_cvt_pk_bf16_f32 v138, 0, v138\n\t"
        "v_cvt_pk_bf16_f32 v139, 0, v139\n\t"
        "v_cndmask_b32_e64 v136, v253, v136, s[48:49]\n\t"
        "v_cndmask_b32_e64 v137, v253, v137, s[50:51]\n\t"
        "v_cndmask_b32_e64 v138, v253, v138, s[52:53]\n\t"
        "v_cndmask_b32_e64 v139, v253, v139, s[54:55]\n\t"
        "v_cvt_pk_bf16_f32 v148, 0, v148\n\t"
        "v_cvt_pk_bf16_f32 v149, 0, v149\n\t"
        "v_cvt_pk_bf16_f32 v150, 0, v150\n\t"
        "v_cvt_pk_bf16_f32 v151, 0, v151\n\t"
        "v_mul_f32_e32 v148, 0x3db504f3, v148\n\t"
        "v_mul_f32_e32 v149, 0x3db504f3, v149\n\t"
        "v_mul_f32_e32 v150, 0x3db504f3, v150\n\t"
        "v_mul_f32_e32 v151, 0x3db504f3, v151\n\t"
        "v_cvt_pk_bf16_f32 v148, 0, v148\n\t"
        "v_cvt_pk_bf16_f32 v149, 0, v149\n\t"
        "v_cvt_pk_bf16_f32 v150, 0, v150\n\t"
        "v_cvt_pk_bf16_f32 v151, 0, v151\n\t"
        "v_cvt_pk_bf16_f32 v152, 0, v152\n\t"
        "v_cvt_pk_bf16_f32 v153, 0, v153\n\t"
        "v_cvt_pk_bf16_f32 v154, 0, v154\n\t"
        "v_cvt_pk_bf16_f32 v155, 0, v155\n\t"
        "v_mul_f32_e32 v152, 0x3db504f3, v152\n\t"
        "v_mul_f32_e32 v153, 0x3db504f3, v153\n\t"
        "v_mul_f32_e32 v154, 0x3db504f3, v154\n\t"
        "v_mul_f32_e32 v155, 0x3db504f3, v155\n\t"
        "v_cvt_pk_bf16_f32 v152, 0, v152\n\t"
        "v_cvt_pk_bf16_f32 v153, 0, v153\n\t"
        "v_cvt_pk_bf16_f32 v154, 0, v154\n\t"
        "v_cvt_pk_bf16_f32 v155, 0, v155\n\t"
        "v_cndmask_b32_e64 v152, v253, v152, s[48:49]\n\t"
        "v_cndmask_b32_e64 v153, v253, v153, s[50:51]\n\t"
        "v_cndmask_b32_e64 v154, v253, v154, s[52:53]\n\t"
        "v_cndmask_b32_e64 v155, v253, v155, s[54:55]\n\t"
        "v_max3_f32 v172, v136, v137, v253\n\t"
        "v_max3_f32 v172, v172, v138, v139\n\t"
        "v_mov_b32_e32 v140, v253\n\t"
        "v_max_f32_e32 v174, v148, v149\n\t"
        "v_max3_f32 v174, v174, v150, v151\n\t"
        "v_max3_f32 v174, v174, v152, v153\n\t"
        "v_max3_f32 v174, v174, v154, v155\n\t"
        "s_cmp_eq_u32 s39, 0\n\t"
        "s_cbranch_scc1 .La11%=\n\t"
        "s_waitcnt vmcnt(0)\n\t"
        "s_lshl_b32 s80, s44, 7\n\t"
        "s_cmp_lt_i32 s44, 0\n\t"
        "s_cselect_b32 s80, s36, s80\n\t"
        "v_mov_b32_e32 v242, 1\n\t"
        "buffer_atomic_add v242, v250, %[rd], s80 offen sc0 sc1\n\t"
        "s_waitcnt vmcnt(0)\n\t"
        "v_readlane_b32 s84, v242, 0\n\t"
        "v_readlane_b32 s85, v242, 32\n\t"
        "v_mov_b32_e32 v243, 32\n\t"
        "s_and_b32 s80, s44, 3\n\t"
        "s_lshl_b32 s80, s80, 12\n\t"
        "s_lshr_b32 s81, s44, 2\n\t"
        "s_lshl_b32 s81, s81, 3\n\t"
        "s_add_u32 s80, s80, s81\n\t"
        "s_and_b32 s81, s44, 1\n\t"
        "s_lshl_b32 s81, s81, 13\n\t"
        "s_lshr_b32 s82, s44, 1\n\t"
        "s_lshl_b32 s82, s82, 2\n\t"
        "s_add_u32 s81, s81, s82\n\t"
        "s_cmp_lt_i32 s44, 60\n\t"
        "s_cselect_b32 s80, s80, s81\n\t"
        "s_cselect_b32 s81, 4, 0x1000\n\t"
        "s_add_u32 s81, s81, s80\n\t"
        "s_cmp_eq_u32 s84, 31\n\t"
        "s_cselect_b32 s80, s80, s36\n\t"
        "s_cmp_eq_u32 s85, 31\n\t"
        "s_cselect_b32 s81, s81, s36\n\t"
        "s_cmp_lt_i32 s44, 0\n\t"
        "s_cselect_b32 s80, s36, s80\n\t"
        "s_cselect_b32 s81, s36, s81\n\t"
        "buffer_store_dword v243, v251, %[rd], s80 offen sc1\n\t"
        "buffer_store_dword v243, v251, %[rd], s81 offen sc1\n\t"
        "s_mov_b32 s44, -1\n\t"
        "s_mov_b32 s97, 0\n\t"
        ".La10%=:\n\t"
        "s_cmp_gt_u32 s40, s37\n\t"
        "s_cbranch_scc1 .La10%=x\n\t"
        "s_memrealtime s[98:99]\n\t"
        "s_add_u32 s97, s97, 1\n\t"
        "s_cmp_lt_u32 s97, 0x400000\n\t"
        "s_cbranch_scc0 .Lgu%=\n\t"
        "s_sub_u32 s80, s37, s40\n\t"
        "s_cmp_ge_u32 s37, %c[efrom]\n\t"
        "s_subb_u32 s80, s80, 0\n\t"
        "s_mul_i32 s80, s80, %c[smin]\n\t"
        "s_waitcnt lgkmcnt(0)\n\t"
        "s_sub_u32 s81, s98, s41\n\t"
        "s_cmp_ge_i32 s81, s80\n\t"
        "s_cbranch_scc1 .La10%=p\n\t"
        "s_sleep 16\n\t"
        "s_branch .La10%=\n\t"
        ".La10%=p:\n\t"
        "buffer_load_dword v241, v247, %[rd], 0 offen sc1\n\t"
        "s_waitcnt vmcnt(0)\n\t"
        "v_readlane_b32 s80, v241, 0\n\t"
        "v_readlane_b32 s81, v241, 1\n\t"
        "v_readlane_b32 s82, v241, 2\n\t"
        "s_min_u32 s80, s80, s81\n\t"
        "s_min_u32 s80, s80, s82\n\t"
        "s_cmp_gt_u32 s80, s40\n\t"
        "s_cselect_b32 s41, s98, s41\n\t"
        "s_max_u32 s40, s40, s80\n\t"
        "s_cmp_gt_u32 s40, s37\n\t"
        "s_cbranch_scc1 .La10%=x\n\t"
        "s_sleep %c[pslp]\n\t"
        "s_branch .La10%=\n\t"
        ".La10%=x:\n\t"
        "s_lshl_b32 s81, s37, 17\n\t"
        "s_add_u32 s81, s81, %[km]\n\t"
        "buffer_load_dwordx4 v[96:99], v244, %[rd], s81 offen offset:0 sc1\n\t"
        "buffer_load_dwordx4 v[100:103], v244, %[rd], s81 offen offset:1024 sc1\n\t"
        "buffer_load_dwordx2 v[104:105], v245, %[rd], s81 offen offset:2048 sc1\n\t"
        "buffer_load_dwordx2 v[106:107], v245, %[rd], s81 offen offset:2304 sc1\n\t"
        "buffer_load_dwordx2 v[108:109], v245, %[rd], s81 offen offset:2560 sc1\n\t"
        "buffer_load_dwordx2 v[110:111], v245, %[rd], s81 offen offset:2816 sc1\n\t"
        "buffer_load_dwordx2 v[112:113], v245, %[rd], s81 offen offset:3072 sc1\n\t"
        "buffer_load_dwordx2 v[114:115], v245, %[rd], s81 offen offset:3328 sc1\n\t"
        "buffer_load_dwordx2 v[116:117], v245, %[rd], s81 offen offset:3584 sc1\n\t"
        "buffer_load_dwordx2 v[118:119], v245, %[rd], s81 offen offset:3840 sc1\n\t"
        "s_waitcnt vmcnt(0)\n\t"
        ".La11%=:\n\t"
        "ds_write_b128 v248, v[96:99] offset:0\n\t"
        "ds_write_b128 v248, v[100:103] offset:1024\n\t"
        "ds_read_b64_tr_b16 v[120:121], v249 offset:0\n\t"
        "ds_read_b64_tr_b16 v[122:123], v249 offset:64\n\t"
        "ds_read_b64_tr_b16 v[124:125], v249 offset:512\n\t"
        "ds_read_b64_tr_b16 v[126:127], v249 offset:576\n\t"
        "ds_read_b64_tr_b16 v[128:129], v249 offset:1024\n\t"
        "ds_read_b64_tr_b16 v[130:131], v249 offset:1088\n\t"
        "ds_read_b64_tr_b16 v[132:133], v249 offset:1536\n\t"
        "ds_read_b64_tr_b16 v[134:135], v249 offset:1600\n\t"
        "s_waitcnt lgkmcnt(6)\n\t"
        "v_mfma_f32_16x16x32_bf16 v[144:147], v[120:123], v[32:35], 0\n\t"
        "v_mfma_f32_16x16x32_bf16 v[156:159], v[120:123], v[48:51], 0\n\t"
        "s_waitcnt lgkmcnt(4)\n\t"
        "v_mfma_f32_16x16x32_bf16 v[144:147], v[124:127], v[36:39], v[144:147]\n\t"
        "v_mfma_f32_16x16x32_bf16 v[156:159], v[124:127], v[52:55], v[156:159]\n\t"
        "s_waitcnt lgkmcnt(2)\n\t"
        "v_mfma_f32_16x16x32_bf16 v[144:147], v[128:131], v[40:43], v[144:147]\n\t"
        "v_mfma_f32_16x16x32_bf16 v[156:159], v[128:131], v[56:59], v[156:159]\n\t"
        "s_waitcnt lgkmcnt(0)\n\t"
        "v_mfma_f32_16x16x32_bf16 v[144:147], v[132:135], v[44:47], v[144:147]\n\t"
        "v_mfma_f32_16x16x32_bf16 v[156:159], v[132:135], v[60:63], v[156:159]\n\t"
        "s_nop 1\n\t"
        "v_mov_b32_e32 v252, v252\n\t"
        "s_nop 1\n\t"
        "v_mov_b32_e32 v252, v252\n\t"
        "s_nop 0\n\t"
        "v_cvt_pk_bf16_f32 v144, 0, v144\n\t"
        "v_cvt_pk_bf16_f32 v145, 0, v145\n\t"
        "v_cvt_pk_bf16_f32 v146, 0, v146\n\t"
        "v_cvt_pk_bf16_f32 v147, 0, v147\n\t"
        "v_mul_f32_e32 v144, 0x3db504f3, v144\n\t"
        "v_mul_f32_e32 v145, 0x3db504f3, v145\n\t"
        "v_mul_f32_e32 v146, 0x3db504f3, v146\n\t"
        "v_mul_f32_e32 v147, 0x3db504f3, v147\n\t"
        "v_cvt_pk_bf16_f32 v144, 0, v144\n\t"
        "v_cvt_pk_bf16_f32 v145, 0, v145\n\t"
        "v_cvt_pk_bf16_f32 v146, 0, v146\n\t"
        "v_cvt_pk_bf16_f32 v147, 0, v147\n\t"
        "v_cndmask_b32_e64 v144, v144, v254, s[56:57]\n\t"
        "v_cndmask_b32_e64 v145, v145, v254, s[56:57]\n\t"
        "v_cndmask_b32_e64 v146, v146, v254, s[56:57]\n\t"
        "v_cndmask_b32_e64 v147, v147, v254, s[56:57]\n\t"
        "v_max3_f32 v172, v172, v144, v145\n\t"
        "v_max3_f32 v173, v172, v146, v147\n\t"
        "v_max3_f32 v172, v172, v146, v147\n\t"
        "s_nop 1\n\t"
        "v_permlane16_swap_b32_e32 v172, v173\n\t"
        "v_max_f32_e32 v172, v172, v173\n\t"
        "v_mov_b32_e32 v173, v172\n\t"
        "s_nop 1\n\t"
        "v_permlane32_swap_b32_e32 v172, v173\n\t"
        "v_max_f32_e32 v172, v172, v173\n\t"
        "v_sub_f32_e32 v136, v136, v172\n\t"
        "v_sub_f32_e32 v137, v137, v172\n\t"
        "v_sub_f32_e32 v138, v138, v172\n\t"
        "v_sub_f32_e32 v139, v139, v172\n\t"
        "v_sub_f32_e32 v140, v140, v172\n\t"
        "v_sub_f32_e32 v144, v144, v172\n\t"
        "v_sub_f32_e32 v145, v145, v172\n\t"
        "v_sub_f32_e32 v146, v146, v172\n\t"
        "v_sub_f32_e32 v147, v147, v172\n\t"
        "v_mul_f32_e32 v136, 0x3fb8aa3b, v136\n\t"
        "v_mul_f32_e32 v137, 0x3fb8aa3b, v137\n\t"
        "v_mul_f32_e32 v138, 0x3fb8aa3b, v138\n\t"
        "v_mul_f32_e32 v139, 0x3fb8aa3b, v139\n\t"
        "v_mul_f32_e32 v140, 0x3fb8aa3b, v140\n\t"
        "v_mul_f32_e32 v144, 0x3fb8aa3b, v144\n\t"
        "v_mul_f32_e32 v145, 0x3fb8aa3b, v145\n\t"
        "v_mul_f32_e32 v146, 0x3fb8aa3b, v146\n\t"
        "v_mul_f32_e32 v147, 0x3fb8aa3b, v147\n\t"
        "v_exp_f32_e32 v136, v136\n\t"
        "v_exp_f32_e32 v137, v137\n\t"
        "v_exp_f32_e32 v138, v138\n\t"
        "v_exp_f32_e32 v139, v139\n\t"
        "v_exp_f32_e32 v140, v140\n\t"
        "v_exp_f32_e32 v144, v144\n\t"
        "v_exp_f32_e32 v145, v145\n\t"
        "v_exp_f32_e32 v146, v146\n\t"
        "v_exp_f32_e32 v147, v147\n\t"
        "v_mul_f32_e32 v172, 4.0, v140\n\t"
        "v_add_f32_e32 v172, v172, v136\n\t"
        "v_add_f32_e32 v172, v172, v137\n\t"
        "v_add_f32_e32 v172, v172, v138\n\t"
        "v_add_f32_e32 v172, v172, v139\n\t"
        "v_add_f32_e32 v172, v172, v144\n\t"
        "v_add_f32_e32 v172, v172, v145\n\t"
        "v_add_f32_e32 v172, v172, v146\n\t"
        "v_add_f32_e32 v172, v172, v147\n\t"
        "v_mov_b32_e32 v173, v172\n\t"
        "s_nop 1\n\t"
        "v_permlane16_swap_b32_e32 v172, v173\n\t"
        "v_add_f32_e32 v172, v172, v173\n\t"
        "v_mov_b32_e32 v173, v172\n\t"
        "s_nop 1\n\t"
        "v_permlane32_swap_b32_e32 v172, v173\n\t"
        "v_add_f32_e32 v172, v172, v173\n\t"
        "v_rcp_f32_e32 v172, v172\n\t"
        "s_nop 0\n\t"
        "v_mul_f32_e32 v136, v136, v172\n\t"
        "v_mul_f32_e32 v137, v137, v172\n\t"
        "v_mul_f32_e32 v138, v138, v172\n\t"
        "v_mul_f32_e32 v139, v139, v172\n\t"
        "v_mul_f32_e32 v140, v140, v172\n\t"
        "v_mul_f32_e32 v144, v144, v172\n\t"
        "v_mul_f32_e32 v145, v145, v172\n\t"
        "v_mul_f32_e32 v146, v146, v172\n\t"
        "v_mul_f32_e32 v147, v147, v172\n\t"
        "v_cvt_pk_bf16_f32 v160, v136, v137\n\t"
        "v_cvt_pk_bf16_f32 v161, v138, v139\n\t"
        "v_cvt_pk_bf16_f32 v162, v140, v140\n\t"
        "v_cvt_pk_bf16_f32 v163, v140, v140\n\t"
        "v_cvt_pk_bf16_f32 v164, v144, v145\n\t"
        "v_cvt_pk_bf16_f32 v165, v146, v147\n\t"
        "v_mfma_f32_16x16x32_bf16 v[176:179], v[64:67], v[160:163], 0\n\t"
        "v_cvt_pk_bf16_f32 v156, 0, v156\n\t"
        "v_cvt_pk_bf16_f32 v157, 0, v157\n\t"
        "v_cvt_pk_bf16_f32 v158, 0, v158\n\t"
        "v_mfma_f32_16x16x32_bf16 v[180:183], v[68:71], v[160:163], 0\n\t"
        "v_cvt_pk_bf16_f32 v159, 0, v159\n\t"
        "v_mul_f32_e32 v156, 0x3db504f3, v156\n\t"
        "v_mul_f32_e32 v157, 0x3db504f3, v157\n\t"
        "v_mfma_f32_16x16x32_bf16 v[184:187], v[72:75], v[160:163], 0\n\t"
        "v_mul_f32_e32 v158, 0x3db504f3, v158\n\t"
        "v_mul_f32_e32 v159, 0x3db504f3, v159\n\t"
        "v_cvt_pk_bf16_f32 v156, 0, v156\n\t"
        "v_mfma_f32_16x16x32_bf16 v[188:191], v[76:79], v[160:163], 0\n\t"
        "v_cvt_pk_bf16_f32 v157, 0, v157\n\t"
        "v_cvt_pk_bf16_f32 v158, 0, v158\n\t"
        "v_cvt_pk_bf16_f32 v159, 0, v159\n\t"
        "v_mfma_f32_16x16x32_bf16 v[192:195], v[80:83], v[160:163], 0\n\t"
        "v_cndmask_b32_e64 v156, v156, v254, s[56:57]\n\t"
        "v_cndmask_b32_e64 v157, v157, v254, s[56:57]\n\t"
        "v_cndmask_b32_e64 v158, v158, v254, s[56:57]\n\t"
        "v_mfma_f32_16x16x32_bf16 v[196:199], v[84:87], v[160:163], 0\n\t"
        "v_cndmask_b32_e64 v159, v159, v254, s[56:57]\n\t"
        "v_max3_f32 v174, v174, v156, v157\n\t"
        "v_max3_f32 v175, v174, v158, v159\n\t"
        "v_mfma_f32_16x16x32_bf16 v[200:203], v[88:91], v[160:163], 0\n\t"
        "v_max3_f32 v174, v174, v158, v159\n\t"
        "s_nop 1\n\t"
        "v_permlane16_swap_b32_e32 v174, v175\n\t"
        "v_max_f32_e32 v174, v174, v175\n\t"
        "v_mfma_f32_16x16x32_bf16 v[204:207], v[92:95], v[160:163], 0\n\t"
        "v_mov_b32_e32 v175, v174\n\t"
        "s_nop 1\n\t"
        "v_permlane32_swap_b32_e32 v174, v175\n\t"
        "v_max_f32_e32 v174, v174, v175\n\t"
        "v_mfma_f32_16x16x16_bf16 v[176:179], v[104:105], v[164:165], v[176:179]\n\t"
        "v_sub_f32_e32 v148, v148, v174\n\t"
        "v_sub_f32_e32 v149, v149, v174\n\t"
        "v_sub_f32_e32 v150, v150, v174\n\t"
        "v_mfma_f32_16x16x16_bf16 v[180:183], v[106:107], v[164:165], v[180:183]\n\t"
        "v_sub_f32_e32 v151, v151, v174\n\t"
        "v_sub_f32_e32 v152, v152, v174\n\t"
        "v_sub_f32_e32 v153, v153, v174\n\t"
        "v_mfma_f32_16x16x16_bf16 v[184:187], v[108:109], v[164:165], v[184:187]\n\t"
        "v_sub_f32_e32 v154, v154, v174\n\t"
        "v_sub_f32_e32 v155, v155, v174\n\t"
        "v_sub_f32_e32 v156, v156, v174\n\t"
        "v_mfma_f32_16x16x16_bf16 v[188:191], v[110:111], v[164:165], v[188:191]\n\t"
        "v_sub_f32_e32 v157, v157, v174\n\t"
        "v_sub_f32_e32 v158, v158, v174\n\t"
        "v_sub_f32_e32 v159, v159, v174\n\t"
        "v_mfma_f32_16x16x16_bf16 v[192:195], v[112:113], v[164:165], v[192:195]\n\t"
        "v_mul_f32_e32 v148, 0x3fb8aa3b, v148\n\t"
        "v_mul_f32_e32 v149, 0x3fb8aa3b, v149\n\t"
        "v_mul_f32_e32 v150, 0x3fb8aa3b, v150\n\t"
        "v_mfma_f32_16x16x16_bf16 v[196:199], v[114:115], v[164:165], v[196:199]\n\t"
        "v_mul_f32_e32 v151, 0x3fb8aa3b, v151\n\t"
        "v_mul_f32_e32 v152, 0x3fb8aa3b, v152\n\t"
        "v_mul_f32_e32 v153, 0x3fb8aa3b, v153\n\t"
        "v_mfma_f32_16x16x16_bf16 v[200:203], v[116:117], v[164:165], v[200:203]\n\t"
        "v_mul_f32_e32 v154, 0x3fb8aa3b, v154\n\t"
        "v_mul_f32_e32 v155, 0x3fb8aa3b, v155\n\t"
        "v_mul_f32_e32 v156, 0x3fb8aa3b, v156\n\t"
        "v_mfma_f32_16x16x16_bf16 v[204:207], v[118:119], v[164:165], v[204:207]\n\t"
        "v_mul_f32_e32 v157, 0x3fb8aa3b, v157\n\t"
        "v_mul_f32_e32 v158, 0x3fb8aa3b, v158\n\t"
        "v_mul_f32_e32 v159, 0x3fb8aa3b, v159\n\t"
        "v_exp_f32_e32 v148, v148\n\t"
        "v_exp_f32_e32 v149, v149\n\t"
        "v_exp_f32_e32 v150, v150\n\t"
        "v_exp_f32_e32 v151, v151\n\t"
        "v_exp_f32_e32 v152, v152\n\t"
        "v_exp_f32_e32 v153, v153\n\t"
        "v_exp_f32_e32 v154, v154\n\t"
        "v_exp_f32_e32 v155, v155\n\t"
        "v_exp_f32_e32 v156, v156\n\t"
        "v_exp_f32_e32 v157, v157\n\t"
        "v_exp_f32_e32 v158, v158\n\t"
        "v_exp_f32_e32 v159, v159\n\t"
        "v_add_f32_e32 v174, v148, v149\n\t"
        "v_add_f32_e32 v174, v174, v150\n\t"
        "v_add_f32_e32 v174, v174, v151\n\t"
        "v_add_f32_e32 v174, v174, v152\n\t"
        "v_add_f32_e32 v174, v174, v153\n\t"
        "v_add_f32_e32 v174, v174, v154\n\t"
        "v_add_f32_e32 v174, v174, v155\n\t"
        "v_add_f32_e32 v174, v174, v156\n\t"
        "v_add_f32_e32 v174, v174, v157\n\t"
        "v_add_f32_e32 v174, v174, v158\n\t"
        "v_add_f32_e32 v174, v174, v159\n\t"
        "v_mov_b32_e32 v175, v174\n\t"
        "s_nop 1\n\t"
        "v_permlane16_swap_b32_e32 v174, v175\n\t"
        "v_add_f32_e32 v174, v174, v175\n\t"
        "v_mov_b32_e32 v175, v174\n\t"
        "s_nop 1\n\t"
        "v_permlane32_swap_b32_e32 v174, v175\n\t"
        "v_add_f32_e32 v174, v174, v175\n\t"
        "v_rcp_f32_e32 v174, v174\n\t"
        "s_nop 0\n\t"
        "v_mul_f32_e32 v148, v148, v174\n\t"
        "v_mul_f32_e32 v149, v149, v174\n\t"
        "v_mul_f32_e32 v150, v150, v174\n\t"
        "v_mul_f32_e32 v151, v151, v174\n\t"
        "v_mul_f32_e32 v152, v152, v174\n\t"
        "v_mul_f32_e32 v153, v153, v174\n\t"
        "v_mul_f32_e32 v154, v154, v174\n\t"
        "v_mul_f32_e32 v155, v155, v174\n\t"
        "v_mul_f32_e32 v156, v156, v174\n\t"
        "v_mul_f32_e32 v157, v157, v174\n\t"
        "v_mul_f32_e32 v158, v158, v174\n\t"
        "v_mul_f32_e32 v159, v159, v174\n\t"
        "v_cvt_pk_bf16_f32 v166, v148, v149\n\t"
        "v_cvt_pk_bf16_f32 v167, v150, v151\n\t"
        "v_cvt_pk_bf16_f32 v168, v152, v153\n\t"
        "v_cvt_pk_bf16_f32 v169, v154, v155\n\t"
        "v_cvt_pk_bf16_f32 v170, v156, v157\n\t"
        "v_cvt_pk_bf16_f32 v171, v158, v159\n\t"
        "s_waitcnt vmcnt(38)\n\t"
        "s_lshl_b32 s80, s44, 7\n\t"
        "s_cmp_lt_i32 s44, 0\n\t"
        "s_cselect_b32 s80, s36, s80\n\t"
        "v_mov_b32_e32 v242, 1\n\t"
        "buffer_atomic_add v242, v250, %[rd], s80 offen sc0 sc1\n\t"
        "s_lshl_b32 s86, s37, 18\n\t"
        "s_add_u32 s86, s86, %[qx]\n\t"
        "s_add_u32 s87, s86, 0x1000\n\t"
        "v_mfma_f32_16x16x32_bf16 v[208:211], v[64:67], v[166:169], 0\n\t"
        "v_cvt_pk_bf16_f32 v176, v176, v177\n\t"
        "v_cvt_pk_bf16_f32 v177, v178, v179\n\t"
        "v_mfma_f32_16x16x32_bf16 v[212:215], v[68:71], v[166:169], 0\n\t"
        "v_cvt_pk_bf16_f32 v178, v180, v181\n\t"
        "v_cvt_pk_bf16_f32 v179, v182, v183\n\t"
        "v_mfma_f32_16x16x32_bf16 v[216:219], v[72:75], v[166:169], 0\n\t"
        "buffer_store_dwordx4 v[176:179], v244, %[rd], s86 offen offset:0 sc1\n\t"
        "v_cvt_pk_bf16_f32 v184, v184, v185\n\t"
        "v_mfma_f32_16x16x32_bf16 v[220:223], v[76:79], v[166:169], 0\n\t"
        "v_cvt_pk_bf16_f32 v185, v186, v187\n\t"
        "v_cvt_pk_bf16_f32 v186, v188, v189\n\t"
        "v_mfma_f32_16x16x32_bf16 v[224:227], v[80:83], v[166:169], 0\n\t"
        "v_cvt_pk_bf16_f32 v187, v190, v191\n\t"
        "buffer_store_dwordx4 v[184:187], v244, %[rd], s86 offen offset:1024 sc1\n\t"
        "v_mfma_f32_16x16x32_bf16 v[228:231], v[84:87], v[166:169], 0\n\t"
        "v_cvt_pk_bf16_f32 v192, v192, v193\n\t"
        "v_cvt_pk_bf16_f32 v193, v194, v195\n\t"
        "v_mfma_f32_16x16x32_bf16 v[232:235], v[88:91], v[166:169], 0\n\t"
        "v_cvt_pk_bf16_f32 v194, v196, v197\n\t"
        "v_cvt_pk_bf16_f32 v195, v198, v199\n\t"
        "v_mfma_f32_16x16x32_bf16 v[236:239], v[92:95], v[166:169], 0\n\t"
        "buffer_store_dwordx4 v[192:195], v244, %[rd], s86 offen offset:2048 sc1\n\t"
        "v_cvt_pk_bf16_f32 v200, v200, v201\n\t"
        "v_mfma_f32_16x16x16_bf16 v[208:211], v[104:105], v[170:171], v[208:211]\n\t"
        "v_cvt_pk_bf16_f32 v201, v202, v203\n\t"
        "v_cvt_pk_bf16_f32 v202, v204, v205\n\t"
        "v_mfma_f32_16x16x16_bf16 v[212:215], v[106:107], v[170:171], v[212:215]\n\t"
        "v_cvt_pk_bf16_f32 v203, v206, v207\n\t"
        "buffer_store_dwordx4 v[200:203], v244, %[rd], s86 offen offset:3072 sc1\n\t"
        "v_mfma_f32_16x16x16_bf16 v[216:219], v[108:109], v[170:171], v[216:219]\n\t"
        "v_mfma_f32_16x16x16_bf16 v[220:223], v[110:111], v[170:171], v[220:223]\n\t"
        "v_mfma_f32_16x16x16_bf16 v[224:227], v[112:113], v[170:171], v[224:227]\n\t"
        "v_mfma_f32_16x16x16_bf16 v[228:231], v[114:115], v[170:171], v[228:231]\n\t"
        "v_mfma_f32_16x16x16_bf16 v[232:235], v[116:117], v[170:171], v[232:235]\n\t"
        "v_mfma_f32_16x16x16_bf16 v[236:239], v[118:119], v[170:171], v[236:239]\n\t"
        "v_cvt_pk_bf16_f32 v208, v208, v209\n\t"
        "v_cvt_pk_bf16_f32 v209, v210, v211\n\t"
        "v_cvt_pk_bf16_f32 v210, v212, v213\n\t"
        "v_cvt_pk_bf16_f32 v211, v214, v215\n\t"
        "buffer_store_dwordx4 v[208:211], v244, %[rd], s87 offen offset:0 sc1\n\t"
        "v_cvt_pk_bf16_f32 v216, v216, v217\n\t"
        "v_cvt_pk_bf16_f32 v217, v218, v219\n\t"
        "v_cvt_pk_bf16_f32 v218, v220, v221\n\t"
        "v_cvt_pk_bf16_f32 v219, v222, v223\n\t"
        "buffer_store_dwordx4 v[216:219], v244, %[rd], s87 offen offset:1024 sc1\n\t"
        "v_cvt_pk_bf16_f32 v224, v224, v225\n\t"
        "v_cvt_pk_bf16_f32 v225, v226, v227\n\t"
        "v_cvt_pk_bf16_f32 v226, v228, v229\n\t"
        "v_cvt_pk_bf16_f32 v227, v230, v231\n\t"
        "buffer_store_dwordx4 v[224:227], v244, %[rd], s87 offen offset:2048 sc1\n\t"
        "v_cvt_pk_bf16_f32 v232, v232, v233\n\t"
        "v_cvt_pk_bf16_f32 v233, v234, v235\n\t"
        "v_cvt_pk_bf16_f32 v234, v236, v237\n\t"
        "v_cvt_pk_bf16_f32 v235, v238, v239\n\t"
        "buffer_store_dwordx4 v[232:235], v244, %[rd], s87 offen offset:3072 sc1\n\t"
        "s_mov_b32 s45, s44\n\t"
        "s_mov_b32 s44, s37\n\t"
        "s_add_u32 s80, s37, 4\n\t"
        "s_cmp_gt_u32 s40, s80\n\t"
        "s_cselect_b32 s80, s46, 1\n\t"
        "s_cmp_eq_u32 s80, 0\n\t"
        "s_cbranch_scc1 .La12%=\n\t"
        "s_waitcnt vmcnt(0)\n\t"
        "v_readlane_b32 s84, v242, 0\n\t"
        "v_readlane_b32 s85, v242, 32\n\t"
        "v_mov_b32_e32 v243, 32\n\t"
        "s_and_b32 s80, s45, 3\n\t"
        "s_lshl_b32 s80, s80, 12\n\t"
        "s_lshr_b32 s81, s45, 2\n\t"
        "s_lshl_b32 s81, s81, 3\n\t"
        "s_add_u32 s80, s80, s81\n\t"
        "s_and_b32 s81, s45, 1\n\t"
        "s_lshl_b32 s81, s81, 13\n\t"
        "s_lshr_b32 s82, s45, 1\n\t"
        "s_lshl_b32 s82, s82, 2\n\t"
        "s_add_u32 s81, s81, s82\n\t"
        "s_cmp_lt_i32 s45, 60\n\t"
        "s_cselect_b32 s80, s80, s81\n\t"
        "s_cselect_b32 s81, 4, 0x1000\n\t"
        "s_add_u32 s81, s81, s80\n\t"
        "s_cmp_eq_u32 s84, 31\n\t"
        "s_cselect_b32 s80, s80, s36\n\t"
        "s_cmp_eq_u32 s85, 31\n\t"
        "s_cselect_b32 s81, s81, s36\n\t"
        "s_cmp_lt_i32 s45, 0\n\t"
        "s_cselect_b32 s80, s36, s80\n\t"
        "s_cselect_b32 s81, s36, s81\n\t"
        "buffer_store_dword v243, v251, %[rd], s80 offen sc1\n\t"
        "buffer_store_dword v243, v251, %[rd], s81 offen sc1\n\t"
        "s_lshl_b32 s80, s44, 7\n\t"
        "s_cmp_lt_i32 s44, 0\n\t"
        "s_cselect_b32 s80, s36, s80\n\t"
        "v_mov_b32_e32 v242, 1\n\t"
        "buffer_atomic_add v242, v250, %[rd], s80 offen sc0 sc1\n\t"
        "s_waitcnt vmcnt(0)\n\t"
        "v_readlane_b32 s84, v242, 0\n\t"
        "v_readlane_b32 s85, v242, 32\n\t"
        "v_mov_b32_e32 v243, 32\n\t"
        "s_and_b32 s80, s44, 3\n\t"
        "s_lshl_b32 s80, s80, 12\n\t"
        "s_lshr_b32 s81, s44, 2\n\t"
        "s_lshl_b32 s81, s81, 3\n\t"
        "s_add_u32 s80, s80, s81\n\t"
        "s_and_b32 s81, s44, 1\n\t"
        "s_lshl_b32 s81, s81, 13\n\t"
        "s_lshr_b32 s82, s44, 1\n\t"
        "s_lshl_b32 s82, s82, 2\n\t"
        "s_add_u32 s81, s81, s82\n\t"
        "s_cmp_lt_i32 s44, 60\n\t"
        "s_cselect_b32 s80, s80, s81\n\t"
        "s_cselect_b32 s81, 4, 0x1000\n\t"
        "s_add_u32 s81, s81, s80\n\t"
        "s_cmp_eq_u32 s84, 31\n\t"
        "s_cselect_b32 s80, s80, s36\n\t"
        "s_cmp_eq_u32 s85, 31\n\t"
        "s_cselect_b32 s81, s81, s36\n\t"
        "s_cmp_lt_i32 s44, 0\n\t"
        "s_cselect_b32 s80, s36, s80\n\t"
        "s_cselect_b32 s81, s36, s81\n\t"
        "buffer_store_dword v243, v251, %[rd], s80 offen sc1\n\t"
        "buffer_store_dword v243, v251, %[rd], s81 offen sc1\n\t"
        "s_mov_b32 s44, -1\n\t"
        "s_mov_b32 s45, -1\n\t"
        ".La12%=:\n\t"
        "s_mov_b32 s38, s46\n\t"
        "s_mov_b32 s39, s47\n\t"
        "s_add_u32 s37, s37, 4\n\t"
        "s_cmp_eq_u32 s38, 0\n\t"
        "s_cbranch_scc1 .La14%=\n\t"
        "s_mov_b32 s97, 0\n\t"
        ".La13%=:\n\t"
        "buffer_load_dword v240, v246, %[rd], 0 offen sc1\n\t"
        "buffer_load_dword v241, v247, %[rd], 0 offen sc1\n\t"
        "s_add_u32 s97, s97, 1\n\t"
        "s_cmp_lt_u32 s97, 0x200000\n\t"
        "s_cbranch_scc0 .Lgu%=\n\t"
        "s_memrealtime s[98:99]\n\t"
        "s_waitcnt vmcnt(0) lgkmcnt(0)\n\t"
        "v_cmp_eq_u32_e32 vcc, %[tag], v240\n\t"
        "s_nop 1\n\t"
        "s_or_b64 s[42:43], s[42:43], vcc\n\t"
        "v_readlane_b32 s80, v241, 0\n\t"
        "v_readlane_b32 s81, v241, 1\n\t"
        "v_readlane_b32 s82, v241, 2\n\t"
        "s_min_u32 s80, s80, s81\n\t"
        "s_min_u32 s80, s80, s82\n\t"
        "s_cmp_gt_u32 s80, s40\n\t"
        "s_cselect_b32 s41, s98, s41\n\t"
        "s_max_u32 s40, s40, s80\n\t"
        "s_and_b32 s80, s37, 60\n\t"
        "s_lshr_b64 s[80:81], s[42:43], s80\n\t"
        "s_andn2_b32 s80, 15, s80\n\t"
        "s_cbranch_scc0 .La13%=x\n\t"
        "s_sleep 16\n\t"
        "s_branch .La13%=\n\t"
        ".La13%=x:\n\t"
        "s_cmp_gt_u32 s40, s37\n\t"
        "s_cselect_b32 s39, 0, s36\n\t"
        "s_lshl_b32 s80, s37, 18\n\t"
        "s_or_b32 s80, s80, 0\n\t"
        "s_lshl_b32 s81, s37, 17\n\t"
        "s_add_u32 s81, s81, %[km]\n\t"
        "s_or_b32 s81, s81, s39\n\t"
        "s_add_u32 s82, s80, %[kx]\n\t"
        "s_add_u32 s83, s82, 0x1000\n\t"
        "buffer_load_dwordx4 a[0:3], v244, %[rd], s82 offen offset:0 sc1\n\t"
        "buffer_load_dwordx4 a[4:7], v244, %[rd], s82 offen offset:1024 sc1\n\t"
        "buffer_load_dwordx4 a[8:11], v244, %[rd], s82 offen offset:2048 sc1\n\t"
        "buffer_load_dwordx4 a[12:15], v244, %[rd], s82 offen offset:3072 sc1\n\t"
        "buffer_load_dwordx4 a[16:19], v244, %[rd], s83 offen offset:0 sc1\n\t"
        "buffer_load_dwordx4 a[20:23], v244, %[rd], s83 offen offset:1024 sc1\n\t"
        "buffer_load_dwordx4 a[24:27], v244, %[rd], s83 offen offset:2048 sc1\n\t"
        "buffer_load_dwordx4 a[28:31], v244, %[rd], s83 offen offset:3072 sc1\n\t"
        "s_cmp_eq_u64 s[42:43], -1\n\t"
        "s_cbranch_scc1 .La15%=\n\t"
        "s_sleep %c[aslp]\n\t"
        ".La15%=:\n\t"
        "s_sleep %c[bslp]\n\t"
        "s_add_u32 s82, s80, %[qx]\n\t"
        "s_add_u32 s83, s82, 0x1000\n\t"
        "buffer_load_dwordx4 a[32:35], v244, %[rd], s82 offen offset:0 sc1\n\t"
        "buffer_load_dwordx4 a[36:39], v244, %[rd], s82 offen offset:1024 sc1\n\t"
        "buffer_load_dwordx4 a[40:43], v244, %[rd], s82 offen offset:2048 sc1\n\t"
        "buffer_load_dwordx4 a[44:47], v244, %[rd], s82 offen offset:3072 sc1\n\t"
        "buffer_load_dwordx4 a[48:51], v244, %[rd], s83 offen offset:0 sc1\n\t"
        "buffer_load_dwordx4 a[52:55], v244, %[rd], s83 offen offset:1024 sc1\n\t"
        "buffer_load_dwordx4 a[56:59], v244, %[rd], s83 offen offset:2048 sc1\n\t"
        "buffer_load_dwordx4 a[60:63], v244, %[rd], s83 offen offset:3072 sc1\n\t"
        "s_cmp_eq_u64 s[42:43], -1\n\t"
        "s_cbranch_scc1 .La16%=\n\t"
        "s_sleep %c[aslp]\n\t"
        ".La16%=:\n\t"
        "s_sleep %c[bslp]\n\t"
        "buffer_load_dwordx4 a[96:99], v244, %[rd], s81 offen offset:0 sc1\n\t"
        "buffer_load_dwordx4 a[100:103], v244, %[rd], s81 offen offset:1024 sc1\n\t"
        "buffer_load_dwordx2 a[104:105], v245, %[rd], s81 offen offset:2048 sc1\n\t"
        "buffer_load_dwordx2 a[106:107], v245, %[rd], s81 offen offset:2304 sc1\n\t"
        "buffer_load_dwordx2 a[108:109], v245, %[rd], s81 offen offset:2560 sc1\n\t"
        "buffer_load_dwordx2 a[110:111], v245, %[rd], s81 offen offset:2816 sc1\n\t"
        "buffer_load_dwordx2 a[112:113], v245, %[rd], s81 offen offset:3072 sc1\n\t"
        "buffer_load_dwordx2 a[114:115], v245, %[rd], s81 offen offset:3328 sc1\n\t"
        "buffer_load_dwordx2 a[116:117], v245, %[rd], s81 offen offset:3584 sc1\n\t"
        "buffer_load_dwordx2 a[118:119], v245, %[rd], s81 offen offset:3840 sc1\n\t"
        "s_add_u32 s82, s80, %[vx]\n\t"
        "s_add_u32 s83, s82, 0x1000\n\t"
        "buffer_load_dwordx4 a[64:67], v244, %[rd], s82 offen offset:0 sc1\n\t"
        "buffer_load_dwordx4 a[68:71], v244, %[rd], s82 offen offset:1024 sc1\n\t"
        "buffer_load_dwordx4 a[72:75], v244, %[rd], s82 offen offset:2048 sc1\n\t"
        "buffer_load_dwordx4 a[76:79], v244, %[rd], s82 offen offset:3072 sc1\n\t"
        "buffer_load_dwordx4 a[80:83], v244, %[rd], s83 offen offset:0 sc1\n\t"
        "buffer_load_dwordx4 a[84:87], v244, %[rd], s83 offen offset:1024 sc1\n\t"
        "buffer_load_dwordx4 a[88:91], v244, %[rd], s83 offen offset:2048 sc1\n\t"
        "buffer_load_dwordx4 a[92:95], v244, %[rd], s83 offen offset:3072 sc1\n\t"
        "s_cmp_eq_u64 s[42:43], -1\n\t"
        "s_cbranch_scc1 .La17%=\n\t"
        "s_sleep %c[aslp]\n\t"
        ".La17%=:\n\t"
        "s_sleep %c[bslp]\n\t"
        "buffer_store_dword v0, off, %[rd], s36\n\t"
        "buffer_store_dword v0, off, %[rd], s36\n\t"
        "buffer_store_dword v0, off, %[rd], s36\n\t"
        "buffer_store_dword v0, off, %[rd], s36\n\t"
        "buffer_store_dword v0, off, %[rd], s36\n\t"
        "buffer_store_dword v0, off, %[rd], s36\n\t"
        "buffer_store_dword v0, off, %[rd], s36\n\t"
        "buffer_store_dword v0, off, %[rd], s36\n\t"
        "buffer_store_dword v0, off, %[rd], s36\n\t"
        ".La14%=:\n\t"
        "s_cmp_gt_u32 s40, s37\n\t"
        "s_cselect_b32 s80, s39, 0\n\t"
        "s_cmp_eq_u32 s80, 0\n\t"
        "s_cbranch_scc1 .La18%=\n\t"
        "s_mov_b32 s39, 0\n\t"
        "s_lshl_b32 s81, s37, 17\n\t"
        "s_add_u32 s81, s81, %[km]\n\t"
        "buffer_load_dwordx4 a[96:99], v244, %[rd], s81 offen offset:0 sc1\n\t"
        "buffer_load_dwordx4 a[100:103], v244, %[rd], s81 offen offset:1024 sc1\n\t"
        "buffer_load_dwordx2 a[104:105], v245, %[rd], s81 offen offset:2048 sc1\n\t"
        "buffer_load_dwordx2 a[106:107], v245, %[rd], s81 offen offset:2304 sc1\n\t"
        "buffer_load_dwordx2 a[108:109], v245, %[rd], s81 offen offset:2560 sc1\n\t"
        "buffer_load_dwordx2 a[110:111], v245, %[rd], s81 offen offset:2816 sc1\n\t"
        "buffer_load_dwordx2 a[112:113], v245, %[rd], s81 offen offset:3072 sc1\n\t"
        "buffer_load_dwordx2 a[114:115], v245, %[rd], s81 offen offset:3328 sc1\n\t"
        "buffer_load_dwordx2 a[116:117], v245, %[rd], s81 offen offset:3584 sc1\n\t"
        "buffer_load_dwordx2 a[118:119], v245, %[rd], s81 offen offset:3840 sc1\n\t"
        "buffer_store_dword v0, off, %[rd], s36\n\t"
        "buffer_store_dword v0, off, %[rd], s36\n\t"
        "buffer_store_dword v0, off, %[rd], s36\n\t"
        "buffer_store_dword v0, off, %[rd], s36\n\t"
        "buffer_store_dword v0, off, %[rd], s36\n\t"
        "buffer_store_dword v0, off, %[rd], s36\n\t"
        "buffer_store_dword v0, off, %[rd], s36\n\t"
        "buffer_store_dword v0, off, %[rd], s36\n\t"
        ".La18%=:\n\t"
        "s_add_u32 s86, s37, 4\n\t"
        "s_and_b32 s80, s86, 60\n\t"
        "s_lshr_b64 s[80:81], s[42:43], s80\n\t"
        "s_andn2_b32 s80, 15, s80\n\t"
        "s_cselect_b32 s46, s36, 0\n\t"
        "s_cmp_lt_u32 s86, 64\n\t"
        "s_cselect_b32 s46, s46, s36\n\t"
        "s_cmp_gt_u32 s40, s86\n\t"
        "s_cselect_b32 s47, s46, s36\n\t"
        "s_lshl_b32 s80, s86, 18\n\t"
        "s_or_b32 s80, s80, s46\n\t"
        "s_lshl_b32 s81, s86, 17\n\t"
        "s_add_u32 s81, s81, %[km]\n\t"
        "s_or_b32 s81, s81, s47\n\t"
        "s_add_u32 s82, s80, %[kx]\n\t"
        "s_add_u32 s83, s82, 0x1000\n\t"
        "buffer_load_dwordx4 v[0:3], v244, %[rd], s82 offen offset:0 sc1\n\t"
        "buffer_load_dwordx4 v[4:7], v244, %[rd], s82 offen offset:1024 sc1\n\t"
        "buffer_load_dwordx4 v[8:11], v244, %[rd], s82 offen offset:2048 sc1\n\t"
        "buffer_load_dwordx4 v[12:15], v244, %[rd], s82 offen offset:3072 sc1\n\t"
        "buffer_load_dwordx4 v[16:19], v244, %[rd], s83 offen offset:0 sc1\n\t"
        "buffer_load_dwordx4 v[20:23], v244, %[rd], s83 offen offset:1024 sc1\n\t"
        "buffer_load_dwordx4 v[24:27], v244, %[rd], s83 offen offset:2048 sc1\n\t"
        "buffer_load_dwordx4 v[28:31], v244, %[rd], s83 offen offset:3072 sc1\n\t"
        "s_cmp_eq_u64 s[42:43], -1\n\t"
        "s_cbranch_scc1 .La19%=\n\t"
        "s_sleep %c[aslp]\n\t"
        ".La19%=:\n\t"
        "s_sleep %c[bslp]\n\t"
        "s_add_u32 s82, s80, %[qx]\n\t"
        "s_add_u32 s83, s82, 0x1000\n\t"
        "buffer_load_dwordx4 v[32:35], v244, %[rd], s82 offen offset:0 sc1\n\t"
        "buffer_load_dwordx4 v[36:39], v244, %[rd], s82 offen offset:1024 sc1\n\t"
        "buffer_load_dwordx4 v[40:43], v244, %[rd], s82 offen offset:2048 sc1\n\t"
        "buffer_load_dwordx4 v[44:47], v244, %[rd], s82 offen offset:3072 sc1\n\t"
        "buffer_load_dwordx4 v[48:51], v244, %[rd], s83 offen offset:0 sc1\n\t"
        "buffer_load_dwordx4 v[52:55], v244, %[rd], s83 offen offset:1024 sc1\n\t"
        "buffer_load_dwordx4 v[56:59], v244, %[rd], s83 offen offset:2048 sc1\n\t"
        "buffer_load_dwordx4 v[60:63], v244, %[rd], s83 offen offset:3072 sc1\n\t"
        "s_cmp_eq_u64 s[42:43], -1\n\t"
        "s_cbranch_scc1 .La20%=\n\t"
        "s_sleep %c[aslp]\n\t"
        ".La20%=:\n\t"
        "s_sleep %c[bslp]\n\t"
        "buffer_load_dwordx4 v[96:99], v244, %[rd], s81 offen offset:0 sc1\n\t"
        "buffer_load_dwordx4 v[100:103], v244, %[rd], s81 offen offset:1024 sc1\n\t"
        "buffer_load_dwordx2 v[104:105], v245, %[rd], s81 offen offset:2048 sc1\n\t"
        "buffer_load_dwordx2 v[106:107], v245, %[rd], s81 offen offset:2304 sc1\n\t"
        "buffer_load_dwordx2 v[108:109], v245, %[rd], s81 offen offset:2560 sc1\n\t"
        "buffer_load_dwordx2 v[110:111], v245, %[rd], s81 offen offset:2816 sc1\n\t"
        "buffer_load_dwordx2 v[112:113], v245, %[rd], s81 offen offset:3072 sc1\n\t"
        "buffer_load_dwordx2 v[114:115], v245, %[rd], s81 offen offset:3328 sc1\n\t"
        "buffer_load_dwordx2 v[116:117], v245, %[rd], s81 offen offset:3584 sc1\n\t"
        "buffer_load_dwordx2 v[118:119], v245, %[rd], s81 offen offset:3840 sc1\n\t"
        "s_add_u32 s82, s80, %[vx]\n\t"
        "s_add_u32 s83, s82, 0x1000\n\t"
        "buffer_load_dwordx4 v[64:67], v244, %[rd], s82 offen offset:0 sc1\n\t"
        "buffer_load_dwordx4 v[68:71], v244, %[rd], s82 offen offset:1024 sc1\n\t"
        "buffer_load_dwordx4 v[72:75], v244, %[rd], s82 offen offset:2048 sc1\n\t"
        "buffer_load_dwordx4 v[76:79], v244, %[rd], s82 offen offset:3072 sc1\n\t"
        "buffer_load_dwordx4 v[80:83], v244, %[rd], s83 offen offset:0 sc1\n\t"
        "buffer_load_dwordx4 v[84:87], v244, %[rd], s83 offen offset:1024 sc1\n\t"
        "buffer_load_dwordx4 v[88:91], v244, %[rd], s83 offen offset:2048 sc1\n\t"
        "buffer_load_dwordx4 v[92:95], v244, %[rd], s83 offen offset:3072 sc1\n\t"
        "s_cmp_eq_u64 s[42:43], -1\n\t"
        "s_cbranch_scc1 .La21%=\n\t"
        "s_sleep %c[aslp]\n\t"
        ".La21%=:\n\t"
        "s_sleep %c[bslp]\n\t"
        "s_waitcnt vmcnt(42)\n\t"
        "s_memrealtime s[98:99]\n\t"
        "v_mfma_f32_16x16x32_bf16 v[136:139], a[0:3], a[32:35], 0\n\t"
        "v_mfma_f32_16x16x32_bf16 v[148:151], a[0:3], a[48:51], 0\n\t"
        "v_mfma_f32_16x16x32_bf16 v[152:155], a[16:19], a[48:51], 0\n\t"
        "v_mfma_f32_16x16x32_bf16 v[136:139], a[4:7], a[36:39], v[136:139]\n\t"
        "v_mfma_f32_16x16x32_bf16 v[148:151], a[4:7], a[52:55], v[148:151]\n\t"
        "v_mfma_f32_16x16x32_bf16 v[152:155], a[20:23], a[52:55], v[152:155]\n\t"
        "v_mfma_f32_16x16x32_bf16 v[136:139], a[8:11], a[40:43], v[136:139]\n\t"
        "v_mfma_f32_16x16x32_bf16 v[148:151], a[8:11], a[56:59], v[148:151]\n\t"
        "v_mfma_f32_16x16x32_bf16 v[152:155], a[24:27], a[56:59], v[152:155]\n\t"
        "v_mfma_f32_16x16x32_bf16 v[136:139], a[12:15], a[44:47], v[136:139]\n\t"
        "v_mfma_f32_16x16x32_bf16 v[148:151], a[12:15], a[60:63], v[148:151]\n\t"
        "v_mfma_f32_16x16x32_bf16 v[152:155], a[28:31], a[60:63], v[152:155]\n\t"
        "s_waitcnt lgkmcnt(0)\n\t"
        "v_cmp_eq_u32_e32 vcc, %[tag], v240\n\t"
        "s_nop 1\n\t"
        "s_or_b64 s[42:43], s[42:43], vcc\n\t"
        "v_readlane_b32 s80, v241, 0\n\t"
        "v_readlane_b32 s81, v241, 1\n\t"
        "v_readlane_b32 s82, v241, 2\n\t"
        "s_min_u32 s80, s80, s81\n\t"
        "s_min_u32 s80, s80, s82\n\t"
        "s_cmp_gt_u32 s80, s40\n\t"
        "s_cselect_b32 s41, s98, s41\n\t"
        "s_max_u32 s40, s40, s80\n\t"
        "v_readlane_b32 s84, v242, 0\n\t"
        "v_readlane_b32 s85, v242, 32\n\t"
        "v_mov_b32_e32 v243, 32\n\t"
        "s_and_b32 s80, s45, 3\n\t"
        "s_lshl_b32 s80, s80, 12\n\t"
        "s_lshr_b32 s81, s45, 2\n\t"
        "s_lshl_b32 s81, s81, 3\n\t"
        "s_add_u32 s80, s80, s81\n\t"
        "s_and_b32 s81, s45, 1\n\t"
        "s_lshl_b32 s81, s81, 13\n\t"
        "s_lshr_b32 s82, s45, 1\n\t"
        "s_lshl_b32 s82, s82, 2\n\t"
        "s_add_u32 s81, s81, s82\n\t"
        "s_cmp_lt_i32 s45, 60\n\t"
        "s_cselect_b32 s80, s80, s81\n\t"
        "s_cselect_b32 s81, 4, 0x1000\n\t"
        "s_add_u32 s81, s81, s80\n\t"
        "s_cmp_eq_u32 s84, 31\n\t"
        "s_cselect_b32 s80, s80, s36\n\t"
        "s_cmp_eq_u32 s85, 31\n\t"
        "s_cselect_b32 s81, s81, s36\n\t"
        "s_cmp_lt_i32 s45, 0\n\t"
        "s_cselect_b32 s80, s36, s80\n\t"
        "s_cselect_b32 s81, s36, s81\n\t"
        "buffer_store_dword v243, v251, %[rd], s80 offen sc1\n\t"
        "buffer_store_dword v243, v251, %[rd], s81 offen sc1\n\t"
        "s_add_u32 s86, s37, 8\n\t"
        "s_cmp_lg_u64 s[42:43], -1\n\t"
        "s_cselect_b32 s84, 0, s36\n\t"
        "s_cmp_gt_u32 s40, s86\n\t"
        "s_cselect_b32 s85, s36, s39\n\t"
        "buffer_load_dword v240, v246, %[rd], s84 offen sc1\n\t"
        "buffer_load_dword v241, v247, %[rd], s85 offen sc1\n\t"
        "v_cvt_pk_bf16_f32 v136, 0, v136\n\t"
        "v_cvt_pk_bf16_f32 v137, 0, v137\n\t"
        "v_cvt_pk_bf16_f32 v138, 0, v138\n\t"
        "v_cvt_pk_bf16_f32 v139, 0, v139\n\t"
        "v_mul_f32_e32 v136, 0x3db504f3, v136\n\t"
        "v_mul_f32_e32 v137, 0x3db504f3, v137\n\t"
        "v_mul_f32_e32 v138, 0x3db504f3, v138\n\t"
        "v_mul_f32_e32 v139, 0x3db504f3, v139\n\t"
        "v_cvt_pk_bf16_f32 v136, 0, v136\n\t"
        "v_cvt_pk_bf16_f32 v137, 0, v137\n\t"
        "v_cvt_pk_bf16_f32 v138, 0, v138\n\t"
        "v_cvt_pk_bf16_f32 v139, 0, v139\n\t"
        "v_cndmask_b32_e64 v136, v253, v136, s[48:49]\n\t"
        "v_cndmask_b32_e64 v137, v253, v137, s[50:51]\n\t"
        "v_cndmask_b32_e64 v138, v253, v138, s[52:53]\n\t"
        "v_cndmask_b32_e64 v139, v253, v139, s[54:55]\n\t"
        "v_cvt_pk_bf16_f32 v148, 0, v148\n\t"
        "v_cvt_pk_bf16_f32 v149, 0, v149\n\t"
        "v_cvt_pk_bf16_f32 v150, 0, v150\n\t"
        "v_cvt_pk_bf16_f32 v151, 0, v151\n\t"
        "v_mul_f32_e32 v148, 0x3db504f3, v148\n\t"
        "v_mul_f32_e32 v149, 0x3db504f3, v149\n\t"
        "v_mul_f32_e32 v150, 0x3db504f3, v150\n\t"
        "v_mul_f32_e32 v151, 0x3db504f3, v151\n\t"
        "v_cvt_pk_bf16_f32 v148, 0, v148\n\t"
        "v_cvt_pk_bf16_f32 v149, 0, v149\n\t"
        "v_cvt_pk_bf16_f32 v150, 0, v150\n\t"
        "v_cvt_pk_bf16_f32 v151, 0, v151\n\t"
        "v_cvt_pk_bf16_f32 v152, 0, v152\n\t"
        "v_cvt_pk_bf16_f32 v153, 0, v153\n\t"
        "v_cvt_pk_bf16_f32 v154, 0, v154\n\t"
        "v_cvt_pk_bf16_f32 v155, 0, v155\n\t"
        "v_mul_f32_e32 v152, 0x3db504f3, v152\n\t"
        "v_mul_f32_e32 v153, 0x3db504f3, v153\n\t"
        "v_mul_f32_e32 v154, 0x3db504f3, v154\n\t"
        "v_mul_f32_e32 v155, 0x3db504f3, v155\n\t"
        "v_cvt_pk_bf16_f32 v152, 0, v152\n\t"
        "v_cvt_pk_bf16_f32 v153, 0, v153\n\t"
        "v_cvt_pk_bf16_f32 v154, 0, v154\n\t"
        "v_cvt_pk_bf16_f32 v155, 0, v155\n\t"
        "v_cndmask_b32_e64 v152, v253, v152, s[48:49]\n\t"
        "v_cndmask_b32_e64 v153, v253, v153, s[50:51]\n\t"
        "v_cndmask_b32_e64 v154, v253, v154, s[52:53]\n\t"
        "v_cndmask_b32_e64 v155, v253, v155, s[54:55]\n\t"
        "v_max3_f32 v172, v136, v137, v253\n\t"
        "v_max3_f32 v172, v172, v138, v139\n\t"
        "v_mov_b32_e32 v140, v253\n\t"
        "v_max_f32_e32 v174, v148, v149\n\t"
        "v_max3_f32 v174, v174, v150, v151\n\t"
        "v_max3_f32 v174, v174, v152, v153\n\t"
        "v_max3_f32 v174, v174, v154, v155\n\t"
        "s_cmp_eq_u32 s39, 0\n\t"
        "s_cbranch_scc1 .La23%=\n\t"
        "s_waitcnt vmcnt(0)\n\t"
        "s_lshl_b32 s80, s44, 7\n\t"
        "s_cmp_lt_i32 s44, 0\n\t"
        "s_cselect_b32 s80, s36, s80\n\t"
        "v_mov_b32_e32 v242, 1\n\t"
        "buffer_atomic_add v242, v250, %[rd], s80 offen sc0 sc1\n\t"
        "s_waitcnt vmcnt(0)\n\t"
        "v_readlane_b32 s84, v242, 0\n\t"
        "v_readlane_b32 s85, v242, 32\n\t"
        "v_mov_b32_e32 v243, 32\n\t"
        "s_and_b32 s80, s44, 3\n\t"
        "s_lshl_b32 s80, s80, 12\n\t"
        "s_lshr_b32 s81, s44, 2\n\t"
        "s_lshl_b32 s81, s81, 3\n\t"
        "s_add_u32 s80, s80, s81\n\t"
        "s_and_b32 s81, s44, 1\n\t"
        "s_lshl_b32 s81, s81, 13\n\t"
        "s_lshr_b32 s82, s44, 1\n\t"
        "s_lshl_b32 s82, s82, 2\n\t"
        "s_add_u32 s81, s81, s82\n\t"
        "s_cmp_lt_i32 s44, 60\n\t"
        "s_cselect_b32 s80, s80, s81\n\t"
        "s_cselect_b32 s81, 4, 0x1000\n\t"
        "s_add_u32 s81, s81, s80\n\t"
        "s_cmp_eq_u32 s84, 31\n\t"
        "s_cselect_b32 s80, s80, s36\n\t"
        "s_cmp_eq_u32 s85, 31\n\t"
        "s_cselect_b32 s81, s81, s36\n\t"
        "s_cmp_lt_i32 s44, 0\n\t"
        "s_cselect_b32 s80, s36, s80\n\t"
        "s_cselect_b32 s81, s36, s81\n\t"
        "buffer_store_dword v243, v251, %[rd], s80 offen sc1\n\t"
        "buffer_store_dword v243, v251, %[rd], s81 offen sc1\n\t"
        "s_mov_b32 s44, -1\n\t"
        "s_mov_b32 s97, 0\n\t"
        ".La22%=:\n\t"
        "s_cmp_gt_u32 s40, s37\n\t"
        "s_cbranch_scc1 .La22%=x\n\t"
        "s_memrealtime s[98:99]\n\t"
        "s_add_u32 s97, s97, 1\n\t"
        "s_cmp_lt_u32 s97, 0x400000\n\t"
        "s_cbranch_scc0 .Lgu%=\n\t"
        "s_sub_u32 s80, s37, s40\n\t"
        "s_cmp_ge_u32 s37, %c[efrom]\n\t"
        "s_subb_u32 s80, s80, 0\n\t"
        "s_mul_i32 s80, s80, %c[smin]\n\t"
        "s_waitcnt lgkmcnt(0)\n\t"
        "s_sub_u32 s81, s98, s41\n\t"
        "s_cmp_ge_i32 s81, s80\n\t"
        "s_cbranch_scc1 .La22%=p\n\t"
        "s_sleep 16\n\t"
        "s_branch .La22%=\n\t"
        ".La22%=p:\n\t"
        "buffer_load_dword v241, v247, %[rd], 0 offen sc1\n\t"
        "s_waitcnt vmcnt(0)\n\t"
        "v_readlane_b32 s80, v241, 0\n\t"
        "v_readlane_b32 s81, v241, 1\n\t"
        "v_readlane_b32 s82, v241, 2\n\t"
        "s_min_u32 s80, s80, s81\n\t"
        "s_min_u32 s80, s80, s82\n\t"
        "s_cmp_gt_u32 s80, s40\n\t"
        "s_cselect_b32 s41, s98, s41\n\t"
        "s_max_u32 s40, s40, s80\n\t"
        "s_cmp_gt_u32 s40, s37\n\t"
        "s_cbranch_scc1 .La22%=x\n\t"
        "s_sleep %c[pslp]\n\t"
        "s_branch .La22%=\n\t"
        ".La22%=x:\n\t"
        "s_lshl_b32 s81, s37, 17\n\t"
        "s_add_u32 s81, s81, %[km]\n\t"
        "buffer_load_dwordx4 a[96:99], v244, %[rd], s81 offen offset:0 sc1\n\t"
        "buffer_load_dwordx4 a[100:103], v244, %[rd], s81 offen offset:1024 sc1\n\t"
        "buffer_load_dwordx2 a[104:105], v245, %[rd], s81 offen offset:2048 sc1\n\t"
        "buffer_load_dwordx2 a[106:107], v245, %[rd], s81 offen offset:2304 sc1\n\t"
        "buffer_load_dwordx2 a[108:109], v245, %[rd], s81 offen offset:2560 sc1\n\t"
        "buffer_load_dwordx2 a[110:111], v245, %[rd], s81 offen offset:2816 sc1\n\t"
        "buffer_load_dwordx2 a[112:113], v245, %[rd], s81 offen offset:3072 sc1\n\t"
        "buffer_load_dwordx2 a[114:115], v245, %[rd], s81 offen offset:3328 sc1\n\t"
        "buffer_load_dwordx2 a[116:117], v245, %[rd], s81 offen offset:3584 sc1\n\t"
        "buffer_load_dwordx2 a[118:119], v245, %[rd], s81 offen offset:3840 sc1\n\t"
        "s_waitcnt vmcnt(0)\n\t"
        ".La23%=:\n\t"
        "ds_write_b128 v248, a[96:99] offset:0\n\t"
        "ds_write_b128 v248, a[100:103] offset:1024\n\t"
        "ds_read_b64_tr_b16 v[120:121], v249 offset:0\n\t"
        "ds_read_b64_tr_b16 v[122:123], v249 offset:64\n\t"
        "ds_read_b64_tr_b16 v[124:125], v249 offset:512\n\t"
        "ds_read_b64_tr_b16 v[126:127], v249 offset:576\n\t"
        "ds_read_b64_tr_b16 v[128:129], v249 offset:1024\n\t"
        "ds_read_b64_tr_b16 v[130:131], v249 offset:1088\n\t"
        "ds_read_b64_tr_b16 v[132:133], v249 offset:1536\n\t"
        "ds_read_b64_tr_b16 v[134:135], v249 offset:1600\n\t"
        "s_waitcnt lgkmcnt(6)\n\t"
        "v_mfma_f32_16x16x32_bf16 v[144:147], v[120:123], a[32:35], 0\n\t"
        "v_mfma_f32_16x16x32_bf16 v[156:159], v[120:123], a[48:51], 0\n\t"
        "s_waitcnt lgkmcnt(4)\n\t"
        "v_mfma_f32_16x16x32_bf16 v[144:147], v[124:127], a[36:39], v[144:147]\n\t"
        "v_mfma_f32_16x16x32_bf16 v[156:159], v[124:127], a[52:55], v[156:159]\n\t"
        "s_waitcnt lgkmcnt(2)\n\t"
        "v_mfma_f32_16x16x32_bf16 v[144:147], v[128:131], a[40:43], v[144:147]\n\t"
        "v_mfma_f32_16x16x32_bf16 v[156:159], v[128:131], a[56:59], v[156:159]\n\t"
        "s_waitcnt lgkmcnt(0)\n\t"
        "v_mfma_f32_16x16x32_bf16 v[144:147], v[132:135], a[44:47], v[144:147]\n\t"
        "v_mfma_f32_16x16x32_bf16 v[156:159], v[132:135], a[60:63], v[156:159]\n\t"
        "s_nop 1\n\t"
        "v_mov_b32_e32 v252, v252\n\t"
        "s_nop 1\n\t"
        "v_mov_b32_e32 v252, v252\n\t"
        "s_nop 0\n\t"
        "v_cvt_pk_bf16_f32 v144, 0, v144\n\t"
        "v_cvt_pk_bf16_f32 v145, 0, v145\n\t"
        "v_cvt_pk_bf16_f32 v146, 0, v146\n\t"
        "v_cvt_pk_bf16_f32 v147, 0, v147\n\t"
        "v_mul_f32_e32 v144, 0x3db504f3, v144\n\t"
        "v_mul_f32_e32 v145, 0x3db504f3, v145\n\t"
        "v_mul_f32_e32 v146, 0x3db504f3, v146\n\t"
        "v_mul_f32_e32 v147, 0x3db504f3, v147\n\t"
        "v_cvt_pk_bf16_f32 v144, 0, v144\n\t"
        "v_cvt_pk_bf16_f32 v145, 0, v145\n\t"
        "v_cvt_pk_bf16_f32 v146, 0, v146\n\t"
        "v_cvt_pk_bf16_f32 v147, 0, v147\n\t"
        "v_cndmask_b32_e64 v144, v144, v254, s[56:57]\n\t"
        "v_cndmask_b32_e64 v145, v145, v254, s[56:57]\n\t"
        "v_cndmask_b32_e64 v146, v146, v254, s[56:57]\n\t"
        "v_cndmask_b32_e64 v147, v147, v254, s[56:57]\n\t"
        "v_max3_f32 v172, v172, v144, v145\n\t"
        "v_max3_f32 v173, v172, v146, v147\n\t"
        "v_max3_f32 v172, v172, v146, v147\n\t"
        "s_nop 1\n\t"
        "v_permlane16_swap_b32_e32 v172, v173\n\t"
        "v_max_f32_e32 v172, v172, v173\n\t"
        "v_mov_b32_e32 v173, v172\n\t"
        "s_nop 1\n\t"
        "v_permlane32_swap_b32_e32 v172, v173\n\t"
        "v_max_f32_e32 v172, v172, v173\n\t"
        "v_sub_f32_e32 v136, v136, v172\n\t"
        "v_sub_f32_e32 v137, v137, v172\n\t"
        "v_sub_f32_e32 v138, v138, v172\n\t"
        "v_sub_f32_e32 v139, v139, v172\n\t"
        "v_sub_f32_e32 v140, v140, v172\n\t"
        "v_sub_f32_e32 v144, v144, v172\n\t"
        "v_sub_f32_e32 v145, v145, v172\n\t"
        "v_sub_f32_e32 v146, v146, v172\n\t"
        "v_sub_f32_e32 v147, v147, v172\n\t"
        "v_mul_f32_e32 v136, 0x3fb8aa3b, v136\n\t"
        "v_mul_f32_e32 v137, 0x3fb8aa3b, v137\n\t"
        "v_mul_f32_e32 v138, 0x3fb8aa3b, v138\n\t"
        "v_mul_f32_e32 v139, 0x3fb8aa3b, v139\n\t"
        "v_mul_f32_e32 v140, 0x3fb8aa3b, v140\n\t"
        "v_mul_f32_e32 v144, 0x3fb8aa3b, v144\n\t"
        "v_mul_f32_e32 v145, 0x3fb8aa3b, v145\n\t"
        "v_mul_f32_e32 v146, 0x3fb8aa3b, v146\n\t"
        "v_mul_f32_e32 v147, 0x3fb8aa3b, v147\n\t"
        "v_exp_f32_e32 v136, v136\n\t"
        "v_exp_f32_e32 v137, v137\n\t"
        "v_exp_f32_e32 v138, v138\n\t"
        "v_exp_f32_e32 v139, v139\n\t"
        "v_exp_f32_e32 v140, v140\n\t"
        "v_exp_f32_e32 v144, v144\n\t"
        "v_exp_f32_e32 v145, v145\n\t"
        "v_exp_f32_e32 v146, v146\n\t"
        "v_exp_f32_e32 v147, v147\n\t"
        "v_mul_f32_e32 v172, 4.0, v140\n\t"
        "v_add_f32_e32 v172, v172, v136\n\t"
        "v_add_f32_e32 v172, v172, v137\n\t"
        "v_add_f32_e32 v172, v172, v138\n\t"
        "v_add_f32_e32 v172, v172, v139\n\t"
        "v_add_f32_e32 v172, v172, v144\n\t"
        "v_add_f32_e32 v172, v172, v145\n\t"
        "v_add_f32_e32 v172, v172, v146\n\t"
        "v_add_f32_e32 v172, v172, v147\n\t"
        "v_mov_b32_e32 v173, v172\n\t"
        "s_nop 1\n\t"
        "v_permlane16_swap_b32_e32 v172, v173\n\t"
        "v_add_f32_e32 v172, v172, v173\n\t"
        "v_mov_b32_e32 v173, v172\n\t"
        "s_nop 1\n\t"
        "v_permlane32_swap_b32_e32 v172, v173\n\t"
        "v_add_f32_e32 v172, v172, v173\n\t"
        "v_rcp_f32_e32 v172, v172\n\t"
        "s_nop 0\n\t"
        "v_mul_f32_e32 v136, v136, v172\n\t"
        "v_mul_f32_e32 v137, v137, v172\n\t"
        "v_mul_f32_e32 v138, v138, v172\n\t"
        "v_mul_f32_e32 v139, v139, v172\n\t"
        "v_mul_f32_e32 v140, v140, v172\n\t"
        "v_mul_f32_e32 v144, v144, v172\n\t"
        "v_mul_f32_e32 v145, v145, v172\n\t"
        "v_mul_f32_e32 v146, v146, v172\n\t"
        "v_mul_f32_e32 v147, v147, v172\n\t"
        "v_cvt_pk_bf16_f32 v160, v136, v137\n\t"
        "v_cvt_pk_bf16_f32 v161, v138, v139\n\t"
        "v_cvt_pk_bf16_f32 v162, v140, v140\n\t"
        "v_cvt_pk_bf16_f32 v163, v140, v140\n\t"
        "v_cvt_pk_bf16_f32 v164, v144, v145\n\t"
        "v_cvt_pk_bf16_f32 v165, v146, v147\n\t"
        "v_mfma_f32_16x16x32_bf16 v[176:179], a[64:67], v[160:163], 0\n\t"
        "v_cvt_pk_bf16_f32 v156, 0, v156\n\t"
        "v_cvt_pk_bf16_f32 v157, 0, v157\n\t"
        "v_cvt_pk_bf16_f32 v158, 0, v158\n\t"
        "v_mfma_f32_16x16x32_bf16 v[180:183], a[68:71], v[160:163], 0\n\t"
        "v_cvt_pk_bf16_f32 v159, 0, v159\n\t"
        "v_mul_f32_e32 v156, 0x3db504f3, v156\n\t"
        "v_mul_f32_e32 v157, 0x3db504f3, v157\n\t"
        "v_mfma_f32_16x16x32_bf16 v[184:187], a[72:75], v[160:163], 0\n\t"
        "v_mul_f32_e32 v158, 0x3db504f3, v158\n\t"
        "v_mul_f32_e32 v159, 0x3db504f3, v159\n\t"
        "v_cvt_pk_bf16_f32 v156, 0, v156\n\t"
        "v_mfma_f32_16x16x32_bf16 v[188:191], a[76:79], v[160:163], 0\n\t"
        "v_cvt_pk_bf16_f32 v157, 0, v157\n\t"
        "v_cvt_pk_bf16_f32 v158, 0, v158\n\t"
        "v_cvt_pk_bf16_f32 v159, 0, v159\n\t"
        "v_mfma_f32_16x16x32_bf16 v[192:195], a[80:83], v[160:163], 0\n\t"
        "v_cndmask_b32_e64 v156, v156, v254, s[56:57]\n\t"
        "v_cndmask_b32_e64 v157, v157, v254, s[56:57]\n\t"
        "v_cndmask_b32_e64 v158, v158, v254, s[56:57]\n\t"
        "v_mfma_f32_16x16x32_bf16 v[196:199], a[84:87], v[160:163], 0\n\t"
        "v_cndmask_b32_e64 v159, v159, v254, s[56:57]\n\t"
        "v_max3_f32 v174, v174, v156, v157\n\t"
        "v_max3_f32 v175, v174, v158, v159\n\t"
        "v_mfma_f32_16x16x32_bf16 v[200:203], a[88:91], v[160:163], 0\n\t"
        "v_max3_f32 v174, v174, v158, v159\n\t"
        "s_nop 1\n\t"
        "v_permlane16_swap_b32_e32 v174, v175\n\t"
        "v_max_f32_e32 v174, v174, v175\n\t"
        "v_mfma_f32_16x16x32_bf16 v[204:207], a[92:95], v[160:163], 0\n\t"
        "v_mov_b32_e32 v175, v174\n\t"
        "s_nop 1\n\t"
        "v_permlane32_swap_b32_e32 v174, v175\n\t"
        "v_max_f32_e32 v174, v174, v175\n\t"
        "v_mfma_f32_16x16x16_bf16 v[176:179], a[104:105], v[164:165], v[176:179]\n\t"
        "v_sub_f32_e32 v148, v148, v174\n\t"
        "v_sub_f32_e32 v149, v149, v174\n\t"
        "v_sub_f32_e32 v150, v150, v174\n\t"
        "v_mfma_f32_16x16x16_bf16 v[180:183], a[106:107], v[164:165], v[180:183]\n\t"
        "v_sub_f32_e32 v151, v151, v174\n\t"
        "v_sub_f32_e32 v152, v152, v174\n\t"
        "v_sub_f32_e32 v153, v153, v174\n\t"
        "v_mfma_f32_16x16x16_bf16 v[184:187], a[108:109], v[164:165], v[184:187]\n\t"
        "v_sub_f32_e32 v154, v154, v174\n\t"
        "v_sub_f32_e32 v155, v155, v174\n\t"
        "v_sub_f32_e32 v156, v156, v174\n\t"
        "v_mfma_f32_16x16x16_bf16 v[188:191], a[110:111], v[164:165], v[188:191]\n\t"
        "v_sub_f32_e32 v157, v157, v174\n\t"
        "v_sub_f32_e32 v158, v158, v174\n\t"
        "v_sub_f32_e32 v159, v159, v174\n\t"
        "v_mfma_f32_16x16x16_bf16 v[192:195], a[112:113], v[164:165], v[192:195]\n\t"
        "v_mul_f32_e32 v148, 0x3fb8aa3b, v148\n\t"
        "v_mul_f32_e32 v149, 0x3fb8aa3b, v149\n\t"
        "v_mul_f32_e32 v150, 0x3fb8aa3b, v150\n\t"
        "v_mfma_f32_16x16x16_bf16 v[196:199], a[114:115], v[164:165], v[196:199]\n\t"
        "v_mul_f32_e32 v151, 0x3fb8aa3b, v151\n\t"
        "v_mul_f32_e32 v152, 0x3fb8aa3b, v152\n\t"
        "v_mul_f32_e32 v153, 0x3fb8aa3b, v153\n\t"
        "v_mfma_f32_16x16x16_bf16 v[200:203], a[116:117], v[164:165], v[200:203]\n\t"
        "v_mul_f32_e32 v154, 0x3fb8aa3b, v154\n\t"
        "v_mul_f32_e32 v155, 0x3fb8aa3b, v155\n\t"
        "v_mul_f32_e32 v156, 0x3fb8aa3b, v156\n\t"
        "v_mfma_f32_16x16x16_bf16 v[204:207], a[118:119], v[164:165], v[204:207]\n\t"
        "v_mul_f32_e32 v157, 0x3fb8aa3b, v157\n\t"
        "v_mul_f32_e32 v158, 0x3fb8aa3b, v158\n\t"
        "v_mul_f32_e32 v159, 0x3fb8aa3b, v159\n\t"
        "v_exp_f32_e32 v148, v148\n\t"
        "v_exp_f32_e32 v149, v149\n\t"
        "v_exp_f32_e32 v150, v150\n\t"
        "v_exp_f32_e32 v151, v151\n\t"
        "v_exp_f32_e32 v152, v152\n\t"
        "v_exp_f32_e32 v153, v153\n\t"
        "v_exp_f32_e32 v154, v154\n\t"
        "v_exp_f32_e32 v155, v155\n\t"
        "v_exp_f32_e32 v156, v156\n\t"
        "v_exp_f32_e32 v157, v157\n\t"
        "v_exp_f32_e32 v158, v158\n\t"
        "v_exp_f32_e32 v159, v159\n\t"
        "v_add_f32_e32 v174, v148, v149\n\t"
        "v_add_f32_e32 v174, v174, v150\n\t"
        "v_add_f32_e32 v174, v174, v151\n\t"
        "v_add_f32_e32 v174, v174, v152\n\t"
        "v_add_f32_e32 v174, v174, v153\n\t"
        "v_add_f32_e32 v174, v174, v154\n\t"
        "v_add_f32_e32 v174, v174, v155\n\t"
        "v_add_f32_e32 v174, v174, v156\n\t"
        "v_add_f32_e32 v174, v174, v157\n\t"
        "v_add_f32_e32 v174, v174, v158\n\t"
        "v_add_f32_e32 v174, v174, v159\n\t"
        "v_mov_b32_e32 v175, v174\n\t"
        "s_nop 1\n\t"
        "v_permlane16_swap_b32_e32 v174, v175\n\t"
        "v_add_f32_e32 v174, v174, v175\n\t"
        "v_mov_b32_e32 v175, v174\n\t"
        "s_nop 1\n\t"
        "v_permlane32_swap_b32_e32 v174, v175\n\t"
        "v_add_f32_e32 v174, v174, v175\n\t"
        "v_rcp_f32_e32 v174, v174\n\t"
        "s_nop 0\n\t"
        "v_mul_f32_e32 v148, v148, v174\n\t"
        "v_mul_f32_e32 v149, v149, v174\n\t"
        "v_mul_f32_e32 v150, v150, v174\n\t"
        "v_mul_f32_e32 v151, v151, v174\n\t"
        "v_mul_f32_e32 v152, v152, v174\n\t"
        "v_mul_f32_e32 v153, v153, v174\n\t"
        "v_mul_f32_e32 v154, v154, v174\n\t"
        "v_mul_f32_e32 v155, v155, v174\n\t"
        "v_mul_f32_e32 v156, v156, v174\n\t"
        "v_mul_f32_e32 v157, v157, v174\n\t"
        "v_mul_f32_e32 v158, v158, v174\n\t"
        "v_mul_f32_e32 v159, v159, v174\n\t"
        "v_cvt_pk_bf16_f32 v166, v148, v149\n\t"
        "v_cvt_pk_bf16_f32 v167, v150, v151\n\t"
        "v_cvt_pk_bf16_f32 v168, v152, v153\n\t"
        "v_cvt_pk_bf16_f32 v169, v154, v155\n\t"
        "v_cvt_pk_bf16_f32 v170, v156, v157\n\t"
        "v_cvt_pk_bf16_f32 v171, v158, v159\n\t"
        "s_waitcnt vmcnt(38)\n\t"
        "s_lshl_b32 s80, s44, 7\n\t"
        "s_cmp_lt_i32 s44, 0\n\t"
        "s_cselect_b32 s80, s36, s80\n\t"
        "v_mov_b32_e32 v242, 1\n\t"
        "buffer_atomic_add v242, v250, %[rd], s80 offen sc0 sc1\n\t"
        "s_lshl_b32 s86, s37, 18\n\t"
        "s_add_u32 s86, s86, %[qx]\n\t"
        "s_add_u32 s87, s86, 0x1000\n\t"
        "v_mfma_f32_16x16x32_bf16 v[208:211], a[64:67], v[166:169], 0\n\t"
        "v_cvt_pk_bf16_f32 v176, v176, v177\n\t"
        "v_cvt_pk_bf16_f32 v177, v178, v179\n\t"
        "v_mfma_f32_16x16x32_bf16 v[212:215], a[68:71], v[166:169], 0\n\t"
        "v_cvt_pk_bf16_f32 v178, v180, v181\n\t"
        "v_cvt_pk_bf16_f32 v179, v182, v183\n\t"
        "v_mfma_f32_16x16x32_bf16 v[216:219], a[72:75], v[166:169], 0\n\t"
        "buffer_store_dwordx4 v[176:179], v244, %[rd], s86 offen offset:0 sc1\n\t"
        "v_cvt_pk_bf16_f32 v184, v184, v185\n\t"
        "v_mfma_f32_16x16x32_bf16 v[220:223], a[76:79], v[166:169], 0\n\t"
        "v_cvt_pk_bf16_f32 v185, v186, v187\n\t"
        "v_cvt_pk_bf16_f32 v186, v188, v189\n\t"
        "v_mfma_f32_16x16x32_bf16 v[224:227], a[80:83], v[166:169], 0\n\t"
        "v_cvt_pk_bf16_f32 v187, v190, v191\n\t"
        "buffer_store_dwordx4 v[184:187], v244, %[rd], s86 offen offset:1024 sc1\n\t"
        "v_mfma_f32_16x16x32_bf16 v[228:231], a[84:87], v[166:169], 0\n\t"
        "v_cvt_pk_bf16_f32 v192, v192, v193\n\t"
        "v_cvt_pk_bf16_f32 v193, v194, v195\n\t"
        "v_mfma_f32_16x16x32_bf16 v[232:235], a[88:91], v[166:169], 0\n\t"
        "v_cvt_pk_bf16_f32 v194, v196, v197\n\t"
        "v_cvt_pk_bf16_f32 v195, v198, v199\n\t"
        "v_mfma_f32_16x16x32_bf16 v[236:239], a[92:95], v[166:169], 0\n\t"
        "buffer_store_dwordx4 v[192:195], v244, %[rd], s86 offen offset:2048 sc1\n\t"
        "v_cvt_pk_bf16_f32 v200, v200, v201\n\t"
        "v_mfma_f32_16x16x16_bf16 v[208:211], a[104:105], v[170:171], v[208:211]\n\t"
        "v_cvt_pk_bf16_f32 v201, v202, v203\n\t"
        "v_cvt_pk_bf16_f32 v202, v204, v205\n\t"
        "v_mfma_f32_16x16x16_bf16 v[212:215], a[106:107], v[170:171], v[212:215]\n\t"
        "v_cvt_pk_bf16_f32 v203, v206, v207\n\t"
        "buffer_store_dwordx4 v[200:203], v244, %[rd], s86 offen offset:3072 sc1\n\t"
        "v_mfma_f32_16x16x16_bf16 v[216:219], a[108:109], v[170:171], v[216:219]\n\t"
        "v_mfma_f32_16x16x16_bf16 v[220:223], a[110:111], v[170:171], v[220:223]\n\t"
        "v_mfma_f32_16x16x16_bf16 v[224:227], a[112:113], v[170:171], v[224:227]\n\t"
        "v_mfma_f32_16x16x16_bf16 v[228:231], a[114:115], v[170:171], v[228:231]\n\t"
        "v_mfma_f32_16x16x16_bf16 v[232:235], a[116:117], v[170:171], v[232:235]\n\t"
        "v_mfma_f32_16x16x16_bf16 v[236:239], a[118:119], v[170:171], v[236:239]\n\t"
        "v_cvt_pk_bf16_f32 v208, v208, v209\n\t"
        "v_cvt_pk_bf16_f32 v209, v210, v211\n\t"
        "v_cvt_pk_bf16_f32 v210, v212, v213\n\t"
        "v_cvt_pk_bf16_f32 v211, v214, v215\n\t"
        "buffer_store_dwordx4 v[208:211], v244, %[rd], s87 offen offset:0 sc1\n\t"
        "v_cvt_pk_bf16_f32 v216, v216, v217\n\t"
        "v_cvt_pk_bf16_f32 v217, v218, v219\n\t"
        "v_cvt_pk_bf16_f32 v218, v220, v221\n\t"
        "v_cvt_pk_bf16_f32 v219, v222, v223\n\t"
        "buffer_store_dwordx4 v[216:219], v244, %[rd], s87 offen offset:1024 sc1\n\t"
        "v_cvt_pk_bf16_f32 v224, v224, v225\n\t"
        "v_cvt_pk_bf16_f32 v225, v226, v227\n\t"
        "v_cvt_pk_bf16_f32 v226, v228, v229\n\t"
        "v_cvt_pk_bf16_f32 v227, v230, v231\n\t"
        "buffer_store_dwordx4 v[224:227], v244, %[rd], s87 offen offset:2048 sc1\n\t"
        "v_cvt_pk_bf16_f32 v232, v232, v233\n\t"
        "v_cvt_pk_bf16_f32 v233, v234, v235\n\t"
        "v_cvt_pk_bf16_f32 v234, v236, v237\n\t"
        "v_cvt_pk_bf16_f32 v235, v238, v239\n\t"
        "buffer_store_dwordx4 v[232:235], v244, %[rd], s87 offen offset:3072 sc1\n\t"
        "s_mov_b32 s45, s44\n\t"
        "s_mov_b32 s44, s37\n\t"
        "s_add_u32 s80, s37, 4\n\t"
        "s_cmp_gt_u32 s40, s80\n\t"
        "s_cselect_b32 s80, s46, 1\n\t"
        "s_cmp_eq_u32 s80, 0\n\t"
        "s_cbranch_scc1 .La24%=\n\t"
        "s_waitcnt vmcnt(0)\n\t"
        "v_readlane_b32 s84, v242, 0\n\t"
        "v_readlane_b32 s85, v242, 32\n\t"
        "v_mov_b32_e32 v243, 32\n\t"
        "s_and_b32 s80, s45, 3\n\t"
        "s_lshl_b32 s80, s80, 12\n\t"
        "s_lshr_b32 s81, s45, 2\n\t"
        "s_lshl_b32 s81, s81, 3\n\t"
        "s_add_u32 s80, s80, s81\n\t"
        "s_and_b32 s81, s45, 1\n\t"
        "s_lshl_b32 s81, s81, 13\n\t"
        "s_lshr_b32 s82, s45, 1\n\t"
        "s_lshl_b32 s82, s82, 2\n\t"
        "s_add_u32 s81, s81, s82\n\t"
        "s_cmp_lt_i32 s45, 60\n\t"
        "s_cselect_b32 s80, s80, s81\n\t"
        "s_cselect_b32 s81, 4, 0x1000\n\t"
        "s_add_u32 s81, s81, s80\n\t"
        "s_cmp_eq_u32 s84, 31\n\t"
        "s_cselect_b32 s80, s80, s36\n\t"
        "s_cmp_eq_u32 s85, 31\n\t"
        "s_cselect_b32 s81, s81, s36\n\t"
        "s_cmp_lt_i32 s45, 0\n\t"
        "s_cselect_b32 s80, s36, s80\n\t"
        "s_cselect_b32 s81, s36, s81\n\t"
        "buffer_store_dword v243, v251, %[rd], s80 offen sc1\n\t"
        "buffer_store_dword v243, v251, %[rd], s81 offen sc1\n\t"
        "s_lshl_b32 s80, s44, 7\n\t"
        "s_cmp_lt_i32 s44, 0\n\t"
        "s_cselect_b32 s80, s36, s80\n\t"
        "v_mov_b32_e32 v242, 1\n\t"
        "buffer_atomic_add v242, v250, %[rd], s80 offen sc0 sc1\n\t"
        "s_waitcnt vmcnt(0)\n\t"
        "v_readlane_b32 s84, v242, 0\n\t"
        "v_readlane_b32 s85, v242, 32\n\t"
        "v_mov_b32_e32 v243, 32\n\t"
        "s_and_b32 s80, s44, 3\n\t"
        "s_lshl_b32 s80, s80, 12\n\t"
        "s_lshr_b32 s81, s44, 2\n\t"
        "s_lshl_b32 s81, s81, 3\n\t"
        "s_add_u32 s80, s80, s81\n\t"
        "s_and_b32 s81, s44, 1\n\t"
        "s_lshl_b32 s81, s81, 13\n\t"
        "s_lshr_b32 s82, s44, 1\n\t"
        "s_lshl_b32 s82, s82, 2\n\t"
        "s_add_u32 s81, s81, s82\n\t"
        "s_cmp_lt_i32 s44, 60\n\t"
        "s_cselect_b32 s80, s80, s81\n\t"
        "s_cselect_b32 s81, 4, 0x1000\n\t"
        "s_add_u32 s81, s81, s80\n\t"
        "s_cmp_eq_u32 s84, 31\n\t"
        "s_cselect_b32 s80, s80, s36\n\t"
        "s_cmp_eq_u32 s85, 31\n\t"
        "s_cselect_b32 s81, s81, s36\n\t"
        "s_cmp_lt_i32 s44, 0\n\t"
        "s_cselect_b32 s80, s36, s80\n\t"
        "s_cselect_b32 s81, s36, s81\n\t"
        "buffer_store_dword v243, v251, %[rd], s80 offen sc1\n\t"
        "buffer_store_dword v243, v251, %[rd], s81 offen sc1\n\t"
        "s_mov_b32 s44, -1\n\t"
        "s_mov_b32 s45, -1\n\t"
        ".La24%=:\n\t"
        "s_mov_b32 s38, s46\n\t"
        "s_mov_b32 s39, s47\n\t"
        "s_add_u32 s37, s37, 4\n\t"
        "s_cmp_lt_u32 s37, 64\n\t"
        "s_cbranch_scc1 .Ltop%=\n\t"
        "s_endpgm\n\t"
        ".Lgu%=:\n\t"
        "v_mov_b32_e32 v0, 64\n\t"
        "s_mov_b32 s80, %c[ferr]\n\t"
        "buffer_store_dword v0, off, %[rd], s80 sc1\n\t"
        "s_endpgm"
        :: [rd] "s"(rd), [w] "s"(wv), [tag] "s"(tag), [kx] "s"(kx), [qx] "s"(qx), [vx] "s"(vx), [km] "s"(km), [blk] "s"(h),
          [ferr] "i"(F_ERR), [dbg] "i"(W_DBG), [pslp] "i"(P_SLP), [aslp] "i"(AT_SLP), [bslp] "i"(BT_SLP), [efrom] "i"(EARLY_FROM), [smin] "i"(STEP_MIN), "{v[244:251]}"(pc), "{v252}"(dq)
        : "memory", "vcc", "scc", "s36", "s37", "s38", "s39", "s40", "s41", "s42", "s43", "s44", "s45", "s46", "s47", "s48", "s49", "s50", "s51", "s52", "s53", "s54", "s55", "s56", "s57", "s58", "s59", "s80", "s81", "s82", "s83", "s84", "s85", "s86", "s87", "s88", "s89", "s90", "s91", "s92", "s93", "s94", "s95", "s96", "s97", "s98", "s99");
}

constexpr int OP_P = 144;
__device__ __forceinline__ void oproj_block(const Params& p, const int wv, const int nt, const int rho, const int j, const unsigned tag, const unsigned g_t0) {
    const int w = wv, l = otid() & 63, n16 = l & 15, g = l >> 4;
    const v4u rd = mk_srd(p.ws, W_TOTAL), ro = mk_srd(p.out, S * D * 2), rx = mk_srd(p.Aq, S * D * 2);
    const unsigned snd = (unsigned)((w >> 1) ^ nt) << 31, own = snd ^ 0x80000000u;
    const unsigned ep = (nt == 0 && rho == 0 && w == 0) ? F_EP : 0xF0000000u;
    const int n0 = (nt >> 1) * 64 + 4 * n16, row = 4 * w + g;
    const unsigned a_b = W_QX + ((nt & 1) * 16 + w * 4) * 8192 + l * 16, tr = (4 * g + (n16 >> 2)), tc = (n16 & 3) * 8;
    const v16u pc = {a_b, a_b + 4096, F_RDY + (rho * 32 + nt) * 128 + (l & 31) * 4, W_U + (n0 >> 2) * 16384 + row * 8, W_GATE + row * 4, (unsigned)((row * 1024 + n0) * 2),
                     W_KX + ((nt >> 1) * 4 + w) * 2048 + l * 16,
                     (unsigned)(w * 8320 + g * 256 + ((n16 + 4 * g) & 15) * 16), (unsigned)((n16 >> 2) * 1040 + (n16 & 3) * 256 + ((row + 4 * n16) & 15) * 16),
                     (unsigned)(w * 10240 + (l >> 3) * OP_P + (l & 7) * 16), w * 10240 + tr * OP_P + tc, WO_L + w * 16384 + tr * 128 + tc, 0u, 0u, 0u, 0u};
    asm volatile(
        "ds_write_b128 v185, v[192:195] offset:0\n\t"
        "ds_write_b128 v185, v[196:199] offset:1152\n\t"
        "ds_write_b128 v185, v[200:203] offset:2304\n\t"
        "ds_write_b128 v185, v[204:207] offset:3456\n\t"
        "ds_read_b64_tr_b16 a[0:1], v186 offset:0\n\t"
        "ds_read_b64_tr_b16 a[2:3], v186 offset:2304\n\t"
        "ds_read_b64_tr_b16 a[4:5], v186 offset:32\n\t"
        "ds_read_b64_tr_b16 a[6:7], v186 offset:2336\n\t"
        "ds_read_b64_tr_b16 a[8:9], v186 offset:64\n\t"
        "ds_read_b64_tr_b16 a[10:11], v186 offset:2368\n\t"
        "ds_read_b64_tr_b16 a[12:13], v186 offset:96\n\t"
        "ds_read_b64_tr_b16 a[14:15], v186 offset:2400\n\t"
        "ds_write_b128 v185, v[208:211] offset:5120\n\t"
        "ds_write_b128 v185, v[212:215] offset:6272\n\t"
        "ds_write_b128 v185, v[216:219] offset:7424\n\t"
        "ds_write_b128 v185, v[220:223] offset:8576\n\t"
        "ds_read_b64_tr_b16 a[16:17], v186 offset:5120\n\t"
        "ds_read_b64_tr_b16 a[18:19], v186 offset:7424\n\t"
        "ds_read_b64_tr_b16 a[20:21], v186 offset:5152\n\t"
        "ds_read_b64_tr_b16 a[22:23], v186 offset:7456\n\t"
        "ds_read_b64_tr_b16 a[24:25], v186 offset:5184\n\t"
        "ds_read_b64_tr_b16 a[26:27], v186 offset:7488\n\t"
        "ds_read_b64_tr_b16 a[28:29], v186 offset:5216\n\t"
        "ds_read_b64_tr_b16 a[30:31], v186 offset:7520\n\t"
        "ds_write_b128 v185, v[224:227] offset:0\n\t"
        "ds_write_b128 v185, v[228:231] offset:1152\n\t"
        "ds_write_b128 v185, v[232:235] offset:2304\n\t"
        "ds_write_b128 v185, v[236:239] offset:3456\n\t"
        "ds_read_b64_tr_b16 a[32:33], v186 offset:0\n\t"
        "ds_read_b64_tr_b16 a[34:35], v186 offset:2304\n\t"
        "ds_read_b64_tr_b16 a[36:37], v186 offset:32\n\t"
        "ds_read_b64_tr_b16 a[38:39], v186 offset:2336\n\t"
        "ds_read_b64_tr_b16 a[40:41], v186 offset:64\n\t"
        "ds_read_b64_tr_b16 a[42:43], v186 offset:2368\n\t"
        "ds_read_b64_tr_b16 a[44:45], v186 offset:96\n\t"
        "ds_read_b64_tr_b16 a[46:47], v186 offset:2400\n\t"
        "ds_write_b128 v185, v[240:243] offset:5120\n\t"
        "ds_write_b128 v185, v[244:247] offset:6272\n\t"
        "ds_write_b128 v185, v[248:251] offset:7424\n\t"
        "ds_write_b128 v185, v[252:255] offset:8576\n\t"
        "ds_read_b64_tr_b16 a[48:49], v186 offset:5120\n\t"
        "ds_read_b64_tr_b16 a[50:51], v186 offset:7424\n\t"
        "ds_read_b64_tr_b16 a[52:53], v186 offset:5152\n\t"
        "ds_read_b64_tr_b16 a[54:55], v186 offset:7456\n\t"
        "ds_read_b64_tr_b16 a[56:57], v186 offset:5184\n\t"
        "ds_read_b64_tr_b16 a[58:59], v186 offset:7488\n\t"
        "ds_read_b64_tr_b16 a[60:61], v186 offset:5216\n\t"
        "ds_read_b64_tr_b16 a[62:63], v186 offset:7520\n\t"
        "ds_read_b64_tr_b16 a[64:65], v187 offset:0\n\t"
        "ds_read_b64_tr_b16 a[66:67], v187 offset:2048\n\t"
        "ds_read_b64_tr_b16 a[68:69], v187 offset:32\n\t"
        "ds_read_b64_tr_b16 a[70:71], v187 offset:2080\n\t"
        "ds_read_b64_tr_b16 a[72:73], v187 offset:64\n\t"
        "ds_read_b64_tr_b16 a[74:75], v187 offset:2112\n\t"
        "ds_read_b64_tr_b16 a[76:77], v187 offset:96\n\t"
        "ds_read_b64_tr_b16 a[78:79], v187 offset:2144\n\t"
        "ds_read_b64_tr_b16 a[80:81], v187 offset:4096\n\t"
        "ds_read_b64_tr_b16 a[82:83], v187 offset:6144\n\t"
        "ds_read_b64_tr_b16 a[84:85], v187 offset:4128\n\t"
        "ds_read_b64_tr_b16 a[86:87], v187 offset:6176\n\t"
        "ds_read_b64_tr_b16 a[88:89], v187 offset:4160\n\t"
        "ds_read_b64_tr_b16 a[90:91], v187 offset:6208\n\t"
        "ds_read_b64_tr_b16 a[92:93], v187 offset:4192\n\t"
        "ds_read_b64_tr_b16 a[94:95], v187 offset:6240\n\t"
        "ds_read_b64_tr_b16 a[96:97], v187 offset:8192\n\t"
        "ds_read_b64_tr_b16 a[98:99], v187 offset:10240\n\t"
        "ds_read_b64_tr_b16 a[100:101], v187 offset:8224\n\t"
        "ds_read_b64_tr_b16 a[102:103], v187 offset:10272\n\t"
        "ds_read_b64_tr_b16 a[104:105], v187 offset:8256\n\t"
        "ds_read_b64_tr_b16 a[106:107], v187 offset:10304\n\t"
        "ds_read_b64_tr_b16 a[108:109], v187 offset:8288\n\t"
        "ds_read_b64_tr_b16 a[110:111], v187 offset:10336\n\t"
        "ds_read_b64_tr_b16 a[112:113], v187 offset:12288\n\t"
        "ds_read_b64_tr_b16 a[114:115], v187 offset:14336\n\t"
        "ds_read_b64_tr_b16 a[116:117], v187 offset:12320\n\t"
        "ds_read_b64_tr_b16 a[118:119], v187 offset:14368\n\t"
        "ds_read_b64_tr_b16 a[120:121], v187 offset:12352\n\t"
        "ds_read_b64_tr_b16 a[122:123], v187 offset:14400\n\t"
        "ds_read_b64_tr_b16 a[124:125], v187 offset:12384\n\t"
        "ds_read_b64_tr_b16 a[126:127], v187 offset:14432\n\t"
        "ds_write_b128 v185, a[128:131] offset:0\n\t"
        "ds_write_b128 v185, a[132:135] offset:1152\n\t"
        "ds_write_b128 v185, a[136:139] offset:2304\n\t"
        "ds_write_b128 v185, a[140:143] offset:3456\n\t"
        "s_waitcnt lgkmcnt(0)\n\t"
        "ds_read_b64_tr_b16 a[128:129], v186 offset:0\n\t"
        "ds_read_b64_tr_b16 a[130:131], v186 offset:2304\n\t"
        "ds_read_b64_tr_b16 a[132:133], v186 offset:32\n\t"
        "ds_read_b64_tr_b16 a[134:135], v186 offset:2336\n\t"
        "ds_read_b64_tr_b16 a[136:137], v186 offset:64\n\t"
        "ds_read_b64_tr_b16 a[138:139], v186 offset:2368\n\t"
        "ds_read_b64_tr_b16 a[140:141], v186 offset:96\n\t"
        "ds_read_b64_tr_b16 a[142:143], v186 offset:2400\n\t"
        "ds_write_b128 v185, a[144:147] offset:5120\n\t"
        "ds_write_b128 v185, a[148:151] offset:6272\n\t"
        "ds_write_b128 v185, a[152:155] offset:7424\n\t"
        "ds_write_b128 v185, a[156:159] offset:8576\n\t"
        "s_waitcnt lgkmcnt(0)\n\t"
        "ds_read_b64_tr_b16 a[144:145], v186 offset:5120\n\t"
        "ds_read_b64_tr_b16 a[146:147], v186 offset:7424\n\t"
        "ds_read_b64_tr_b16 a[148:149], v186 offset:5152\n\t"
        "ds_read_b64_tr_b16 a[150:151], v186 offset:7456\n\t"
        "ds_read_b64_tr_b16 a[152:153], v186 offset:5184\n\t"
        "ds_read_b64_tr_b16 a[154:155], v186 offset:7488\n\t"
        "ds_read_b64_tr_b16 a[156:157], v186 offset:5216\n\t"
        "ds_read_b64_tr_b16 a[158:159], v186 offset:7520\n\t"
        "ds_write_b128 v185, a[160:163] offset:0\n\t"
        "ds_write_b128 v185, a[164:167] offset:1152\n\t"
        "ds_write_b128 v185, a[168:171] offset:2304\n\t"
        "ds_write_b128 v185, a[172:175] offset:3456\n\t"
        "s_waitcnt lgkmcnt(0)\n\t"
        "ds_read_b64_tr_b16 a[160:161], v186 offset:0\n\t"
        "ds_read_b64_tr_b16 a[162:163], v186 offset:2304\n\t"
        "ds_read_b64_tr_b16 a[164:165], v186 offset:32\n\t"
        "ds_read_b64_tr_b16 a[166:167], v186 offset:2336\n\t"
        "ds_read_b64_tr_b16 a[168:169], v186 offset:64\n\t"
        "ds_read_b64_tr_b16 a[170:171], v186 offset:2368\n\t"
        "ds_read_b64_tr_b16 a[172:173], v186 offset:96\n\t"
        "ds_read_b64_tr_b16 a[174:175], v186 offset:2400\n\t"
        "ds_write_b128 v185, a[176:179] offset:5120\n\t"
        "ds_write_b128 v185, a[180:183] offset:6272\n\t"
        "ds_write_b128 v185, a[184:187] offset:7424\n\t"
        "ds_write_b128 v185, a[188:191] offset:8576\n\t"
        "s_waitcnt lgkmcnt(0)\n\t"
        "ds_read_b64_tr_b16 a[176:177], v186 offset:5120\n\t"
        "ds_read_b64_tr_b16 a[178:179], v186 offset:7424\n\t"
        "ds_read_b64_tr_b16 a[180:181], v186 offset:5152\n\t"
        "ds_read_b64_tr_b16 a[182:183], v186 offset:7456\n\t"
        "ds_read_b64_tr_b16 a[184:185], v186 offset:5184\n\t"
        "ds_read_b64_tr_b16 a[186:187], v186 offset:7488\n\t"
        "ds_read_b64_tr_b16 a[188:189], v186 offset:5216\n\t"
        "ds_read_b64_tr_b16 a[190:191], v186 offset:7520\n\t"
        "ds_write_b128 v185, a[192:195] offset:0\n\t"
        "ds_write_b128 v185, a[196:199] offset:1152\n\t"
        "ds_write_b128 v185, a[200:203] offset:2304\n\t"
        "ds_write_b128 v185, a[204:207] offset:3456\n\t"
        "s_waitcnt lgkmcnt(0)\n\t"
        "ds_read_b64_tr_b16 a[192:193], v186 offset:0\n\t"
        "ds_read_b64_tr_b16 a[194:195], v186 offset:2304\n\t"
        "ds_read_b64_tr_b16 a[196:197], v186 offset:32\n\t"
        "ds_read_b64_tr_b16 a[198:199], v186 offset:2336\n\t"
        "ds_read_b64_tr_b16 a[200:201], v186 offset:64\n\t"
        "ds_read_b64_tr_b16 a[202:203], v186 offset:2368\n\t"
        "ds_read_b64_tr_b16 a[204:205], v186 offset:96\n\t"
        "ds_read_b64_tr_b16 a[206:207], v186 offset:2400\n\t"
        "ds_write_b128 v185, a[208:211] offset:5120\n\t"
        "ds_write_b128 v185, a[212:215] offset:6272\n\t"
        "ds_write_b128 v185, a[216:219] offset:7424\n\t"
        "ds_write_b128 v185, a[220:223] offset:8576\n\t"
        "s_waitcnt lgkmcnt(0)\n\t"
        "ds_read_b64_tr_b16 a[208:209], v186 offset:5120\n\t"
        "ds_read_b64_tr_b16 a[210:211], v186 offset:7424\n\t"
        "ds_read_b64_tr_b16 a[212:213], v186 offset:5152\n\t"
        "ds_read_b64_tr_b16 a[214:215], v186 offset:7456\n\t"
        "ds_read_b64_tr_b16 a[216:217], v186 offset:5184\n\t"
        "ds_read_b64_tr_b16 a[218:219], v186 offset:7488\n\t"
        "ds_read_b64_tr_b16 a[220:221], v186 offset:5216\n\t"
        "ds_read_b64_tr_b16 a[222:223], v186 offset:7520\n\t"
        "ds_write_b128 v185, a[224:227] offset:0\n\t"
        "ds_write_b128 v185, a[228:231] offset:1152\n\t"
        "ds_write_b128 v185, a[232:235] offset:2304\n\t"
        "ds_write_b128 v185, a[236:239] offset:3456\n\t"
        "s_waitcnt lgkmcnt(0)\n\t"
        "ds_read_b64_tr_b16 a[224:225], v186 offset:0\n\t"
        "ds_read_b64_tr_b16 a[226:227], v186 offset:2304\n\t"
        "ds_read_b64_tr_b16 a[228:229], v186 offset:32\n\t"
        "ds_read_b64_tr_b16 a[230:231], v186 offset:2336\n\t"
        "ds_read_b64_tr_b16 a[232:233], v186 offset:64\n\t"
        "ds_read_b64_tr_b16 a[234:235], v186 offset:2368\n\t"
        "ds_read_b64_tr_b16 a[236:237], v186 offset:96\n\t"
        "ds_read_b64_tr_b16 a[238:239], v186 offset:2400\n\t"
        "ds_write_b128 v185, a[240:243] offset:5120\n\t"
        "ds_write_b128 v185, a[244:247] offset:6272\n\t"
        "ds_write_b128 v185, a[248:251] offset:7424\n\t"
        "ds_write_b128 v185, a[252:255] offset:8576\n\t"
        "s_waitcnt lgkmcnt(0)\n\t"
        "ds_read_b64_tr_b16 a[240:241], v186 offset:5120\n\t"
        "ds_read_b64_tr_b16 a[242:243], v186 offset:7424\n\t"
        "ds_read_b64_tr_b16 a[244:245], v186 offset:5152\n\t"
        "ds_read_b64_tr_b16 a[246:247], v186 offset:7456\n\t"
        "ds_read_b64_tr_b16 a[248:249], v186 offset:5184\n\t"
        "ds_read_b64_tr_b16 a[250:251], v186 offset:7488\n\t"
        "ds_read_b64_tr_b16 a[252:253], v186 offset:5216\n\t"
        "ds_read_b64_tr_b16 a[254:255], v186 offset:7520\n\t"
        "s_waitcnt lgkmcnt(0)\n\t"
        "v_mov_b32_e32 v240, v176\n\t"
        "v_mov_b32_e32 v241, v177\n\t"
        "v_mov_b32_e32 v242, v178\n\t"
        "v_mov_b32_e32 v243, v179\n\t"
        "v_mov_b32_e32 v244, v180\n\t"
        "v_mov_b32_e32 v245, v181\n\t"
        "v_mov_b32_e32 v246, v182\n\t"
        "v_mov_b32_e32 v247, v183\n\t"
        "v_mov_b32_e32 v248, v184\n\t"
        "v_mov_b32_e32 v205, %[tag]\n\t"
        "v_mov_b32_e32 v207, %[tag]\n\t"
        "v_mov_b32_e32 v209, %[tag]\n\t"
        "v_mov_b32_e32 v211, %[tag]\n\t"
        "v_mov_b32_e32 v213, %[tag]\n\t"
        "v_mov_b32_e32 v215, %[tag]\n\t"
        "v_mov_b32_e32 v217, %[tag]\n\t"
        "v_mov_b32_e32 v219, %[tag]\n\t"
        "s_mov_b32 s36, 0xf0000000\n\t"
        "s_mov_b32 s37, 0\n\t"
        "s_mov_b32 s38, -1\n\t"
        "s_mov_b32 s39, -1\n\t"
        "s_mov_b32 s40, 0\n\t"
        "s_mov_b32 s43, 0\n\t"
        "s_lshl_b32 s45, %[w], 10\n\t"
        "s_add_u32 s45, s45, 69632\n\t"
        "s_lshl_b32 s44, %[rho], 1\n\t"
        "s_barrier\n\t"
        "s_mov_b32 s97, 0\n\t"
        "v_and_b32_e32 v186, 0x7c, v242\n\t"
        "s_mov_b32 s80, %c[fprep]\n\t"
        "s_mov_b32 s81, %c[fconv]\n\t"
        ".Lo1%=:\n\t"
        "buffer_load_dword v187, v186, %[rd], s80 offen sc1\n\t"
        "s_add_u32 s97, s97, 1\n\t"
        "s_cmp_lt_u32 s97, 0x200000\n\t"
        "s_cbranch_scc0 .Lgu%=\n\t"
        "s_waitcnt vmcnt(0)\n\t"
        "v_cmp_ne_u32_e32 vcc, %[tag], v187\n\t"
        "s_nop 3\n\t"
        "s_cbranch_vccz .Lo2%=\n\t"
        "s_sleep 8\n\t"
        "s_branch .Lo1%=\n\t"
        ".Lo2%=:\n\t"
        "buffer_load_dword v187, off, %[rd], s81 sc1\n\t"
        "s_add_u32 s97, s97, 1\n\t"
        "s_cmp_lt_u32 s97, 0x200000\n\t"
        "s_cbranch_scc0 .Lgu%=\n\t"
        "s_waitcnt vmcnt(0)\n\t"
        "v_cmp_gt_u32_e32 vcc, 0x100, v187\n\t"
        "s_nop 3\n\t"
        "s_cbranch_vccz .Lo3%=\n\t"
        "s_sleep 8\n\t"
        "s_branch .Lo2%=\n\t"
        ".Lo3%=:\n\t"
        ".Lnew%=:\n\t"
        "s_lshl_b32 s85, s37, 1\n\t"
        "s_and_b32 s80, s40, 0x3fffffff\n\t"
        "s_lshr_b32 s80, s80, s85\n\t"
        "s_andn2_b32 s80, 3, s80\n\t"
        "s_cbranch_scc0 .Lgo%=\n\t"
        "s_cmp_lt_i32 s39, 0\n\t"
        "s_cbranch_scc1 .Lo4%=\n\t"
        "s_waitcnt vmcnt(0)\n\t"
        "s_add_i32 s80, s39, 0\n\t"
        "s_lshl_b32 s80, s80, 15\n\t"
        "s_or_b32 s81, s39, %[snd]\n\t"
        "s_cmp_lt_i32 s81, 0\n\t"
        "s_cselect_b32 s83, s36, s80\n\t"
        "s_cbranch_scc1 .Lo7%=\n\t"
        "s_mov_b32 s97, 0\n\t"
        ".Lo5%=:\n\t"
        "v_xor_b32_e32 v186, %[tag], v161\n\t"
        "v_xor_b32_e32 v187, %[tag], v163\n\t"
        "v_xor_b32_e32 v188, %[tag], v165\n\t"
        "v_or3_b32 v186, v186, v187, v188\n\t"
        "v_xor_b32_e32 v187, %[tag], v167\n\t"
        "v_or_b32_e32 v186, v186, v187\n\t"
        "v_cmp_ne_u32_e32 vcc, 0, v186\n\t"
        "v_sub_f32_e32 v198, 1.0, v184\n\t"
        "v_add_f32_e32 v190, v152, v160\n\t"
        "v_add_f32_e32 v191, v153, v162\n\t"
        "v_add_f32_e32 v192, v154, v164\n\t"
        "v_add_f32_e32 v193, v155, v166\n\t"
        "s_cbranch_vccz .Lo6%=\n\t"
        "s_sleep %c[xslp]\n\t"
        "s_lshl_b32 s82, s83, 2\n\t"
        "buffer_load_dwordx4 v[160:163], v246, %[rd], s82 offen offset:0 sc1\n\t"
        "buffer_load_dwordx4 v[164:167], v246, %[rd], s82 offen offset:1024 sc1\n\t"
        "s_add_u32 s97, s97, 1\n\t"
        "s_cmp_lt_u32 s97, 0x200000\n\t"
        "s_cbranch_scc0 .Lgu%=\n\t"
        "s_waitcnt vmcnt(0)\n\t"
        "s_branch .Lo5%=\n\t"
        ".Lo6%=:\n\t"
        "v_cvt_pk_bf16_f32 v198, 0, v198\n\t"
        "v_cvt_pk_bf16_f32 v190, 0, v190\n\t"
        "v_cvt_pk_bf16_f32 v191, 0, v191\n\t"
        "v_cvt_pk_bf16_f32 v192, 0, v192\n\t"
        "v_cvt_pk_bf16_f32 v193, 0, v193\n\t"
        "v_lshlrev_b32_e32 v186, 16, v180\n\t"
        "v_lshlrev_b32_e32 v194, 16, v176\n\t"
        "v_and_b32_e32 v187, 0xffff0000, v180\n\t"
        "v_and_b32_e32 v195, 0xffff0000, v176\n\t"
        "v_lshlrev_b32_e32 v188, 16, v181\n\t"
        "v_lshlrev_b32_e32 v196, 16, v177\n\t"
        "v_and_b32_e32 v189, 0xffff0000, v181\n\t"
        "v_and_b32_e32 v197, 0xffff0000, v177\n\t"
        "v_mul_f32_e32 v186, v186, v194\n\t"
        "v_mul_f32_e32 v187, v187, v195\n\t"
        "v_mul_f32_e32 v188, v188, v196\n\t"
        "v_mul_f32_e32 v189, v189, v197\n\t"
        "v_mul_f32_e32 v190, v198, v190\n\t"
        "v_mul_f32_e32 v191, v198, v191\n\t"
        "v_mul_f32_e32 v192, v198, v192\n\t"
        "v_mul_f32_e32 v193, v198, v193\n\t"
        "v_cvt_pk_bf16_f32 v186, 0, v186\n\t"
        "v_cvt_pk_bf16_f32 v187, 0, v187\n\t"
        "v_cvt_pk_bf16_f32 v188, 0, v188\n\t"
        "v_cvt_pk_bf16_f32 v189, 0, v189\n\t"
        "v_cvt_pk_bf16_f32 v190, 0, v190\n\t"
        "v_cvt_pk_bf16_f32 v191, 0, v191\n\t"
        "v_cvt_pk_bf16_f32 v192, 0, v192\n\t"
        "v_cvt_pk_bf16_f32 v193, 0, v193\n\t"
        "v_mul_f32_e32 v186, v184, v186\n\t"
        "v_mul_f32_e32 v187, v184, v187\n\t"
        "v_mul_f32_e32 v188, v184, v188\n\t"
        "v_mul_f32_e32 v189, v184, v189\n\t"
        "v_cvt_pk_bf16_f32 v186, 0, v186\n\t"
        "v_cvt_pk_bf16_f32 v187, 0, v187\n\t"
        "v_cvt_pk_bf16_f32 v188, 0, v188\n\t"
        "v_cvt_pk_bf16_f32 v189, 0, v189\n\t"
        "v_add_f32_e32 v186, v186, v190\n\t"
        "v_add_f32_e32 v187, v187, v191\n\t"
        "v_add_f32_e32 v188, v188, v192\n\t"
        "v_add_f32_e32 v189, v189, v193\n\t"
        "v_cvt_pk_bf16_f32 v200, v186, v187\n\t"
        "v_cvt_pk_bf16_f32 v201, v188, v189\n\t"
        ".Lo7%=:\n\t"
        "buffer_store_dwordx2 v[200:201], v245, %[ro], s83 offen\n\t"
        "s_add_i32 s80, s39, 1\n\t"
        "s_lshl_b32 s80, s80, 15\n\t"
        "s_or_b32 s81, s39, %[snd]\n\t"
        "s_cmp_lt_i32 s81, 0\n\t"
        "s_cselect_b32 s83, s36, s80\n\t"
        "s_cbranch_scc1 .Lo10%=\n\t"
        "s_mov_b32 s97, 0\n\t"
        ".Lo8%=:\n\t"
        "v_xor_b32_e32 v186, %[tag], v169\n\t"
        "v_xor_b32_e32 v187, %[tag], v171\n\t"
        "v_xor_b32_e32 v188, %[tag], v173\n\t"
        "v_or3_b32 v186, v186, v187, v188\n\t"
        "v_xor_b32_e32 v187, %[tag], v175\n\t"
        "v_or_b32_e32 v186, v186, v187\n\t"
        "v_cmp_ne_u32_e32 vcc, 0, v186\n\t"
        "v_sub_f32_e32 v198, 1.0, v185\n\t"
        "v_add_f32_e32 v190, v156, v168\n\t"
        "v_add_f32_e32 v191, v157, v170\n\t"
        "v_add_f32_e32 v192, v158, v172\n\t"
        "v_add_f32_e32 v193, v159, v174\n\t"
        "s_cbranch_vccz .Lo9%=\n\t"
        "s_sleep %c[xslp]\n\t"
        "s_lshl_b32 s82, s83, 2\n\t"
        "buffer_load_dwordx4 v[168:171], v246, %[rd], s82 offen offset:0 sc1\n\t"
        "buffer_load_dwordx4 v[172:175], v246, %[rd], s82 offen offset:1024 sc1\n\t"
        "s_add_u32 s97, s97, 1\n\t"
        "s_cmp_lt_u32 s97, 0x200000\n\t"
        "s_cbranch_scc0 .Lgu%=\n\t"
        "s_waitcnt vmcnt(0)\n\t"
        "s_branch .Lo8%=\n\t"
        ".Lo9%=:\n\t"
        "v_cvt_pk_bf16_f32 v198, 0, v198\n\t"
        "v_cvt_pk_bf16_f32 v190, 0, v190\n\t"
        "v_cvt_pk_bf16_f32 v191, 0, v191\n\t"
        "v_cvt_pk_bf16_f32 v192, 0, v192\n\t"
        "v_cvt_pk_bf16_f32 v193, 0, v193\n\t"
        "v_lshlrev_b32_e32 v186, 16, v182\n\t"
        "v_lshlrev_b32_e32 v194, 16, v178\n\t"
        "v_and_b32_e32 v187, 0xffff0000, v182\n\t"
        "v_and_b32_e32 v195, 0xffff0000, v178\n\t"
        "v_lshlrev_b32_e32 v188, 16, v183\n\t"
        "v_lshlrev_b32_e32 v196, 16, v179\n\t"
        "v_and_b32_e32 v189, 0xffff0000, v183\n\t"
        "v_and_b32_e32 v197, 0xffff0000, v179\n\t"
        "v_mul_f32_e32 v186, v186, v194\n\t"
        "v_mul_f32_e32 v187, v187, v195\n\t"
        "v_mul_f32_e32 v188, v188, v196\n\t"
        "v_mul_f32_e32 v189, v189, v197\n\t"
        "v_mul_f32_e32 v190, v198, v190\n\t"
        "v_mul_f32_e32 v191, v198, v191\n\t"
        "v_mul_f32_e32 v192, v198, v192\n\t"
        "v_mul_f32_e32 v193, v198, v193\n\t"
        "v_cvt_pk_bf16_f32 v186, 0, v186\n\t"
        "v_cvt_pk_bf16_f32 v187, 0, v187\n\t"
        "v_cvt_pk_bf16_f32 v188, 0, v188\n\t"
        "v_cvt_pk_bf16_f32 v189, 0, v189\n\t"
        "v_cvt_pk_bf16_f32 v190, 0, v190\n\t"
        "v_cvt_pk_bf16_f32 v191, 0, v191\n\t"
        "v_cvt_pk_bf16_f32 v192, 0, v192\n\t"
        "v_cvt_pk_bf16_f32 v193, 0, v193\n\t"
        "v_mul_f32_e32 v186, v185, v186\n\t"
        "v_mul_f32_e32 v187, v185, v187\n\t"
        "v_mul_f32_e32 v188, v185, v188\n\t"
        "v_mul_f32_e32 v189, v185, v189\n\t"
        "v_cvt_pk_bf16_f32 v186, 0, v186\n\t"
        "v_cvt_pk_bf16_f32 v187, 0, v187\n\t"
        "v_cvt_pk_bf16_f32 v188, 0, v188\n\t"
        "v_cvt_pk_bf16_f32 v189, 0, v189\n\t"
        "v_add_f32_e32 v186, v186, v190\n\t"
        "v_add_f32_e32 v187, v187, v191\n\t"
        "v_add_f32_e32 v188, v188, v192\n\t"
        "v_add_f32_e32 v189, v189, v193\n\t"
        "v_cvt_pk_bf16_f32 v200, v186, v187\n\t"
        "v_cvt_pk_bf16_f32 v201, v188, v189\n\t"
        ".Lo10%=:\n\t"
        "buffer_store_dwordx2 v[200:201], v245, %[ro], s83 offen\n\t"
        "s_mov_b32 s39, -1\n\t"
        ".Lo4%=:\n\t"
        "s_cmp_eq_u32 s37, 15\n\t"
        "s_cbranch_scc1 .Lsol%=\n\t"
        "s_mov_b32 s97, 0\n\t"
        ".Lo11%=:\n\t"
        "s_lshr_b32 s80, s40, s85\n\t"
        "s_andn2_b32 s80, 3, s80\n\t"
        "s_cbranch_scc0 .Lo12%=\n\t"
        "s_cmp_eq_u32 s97, 0\n\t"
        "s_cbranch_scc1 .Lo11%=x\n\t"
        "s_sleep %c[oslp]\n\t"
        ".Lo11%=x:\n\t"
        "buffer_load_dword v220, v242, %[rd], 0 offen sc1\n\t"
        "s_add_u32 s97, s97, 1\n\t"
        "s_cmp_lt_u32 s97, 0x200000\n\t"
        "s_cbranch_scc0 .Lgu%=\n\t"
        "s_waitcnt vmcnt(0)\n\t"
        "v_cmp_eq_u32_e32 vcc, 32, v220\n\t"
        "s_nop 1\n\t"
        "s_or_b32 s40, s40, vcc_lo\n\t"
        "s_branch .Lo11%=\n\t"
        ".Lo12%=:\n\t"
        ".Lgo%=:\n\t"
        "s_lshl_b32 s84, s44, 17\n\t"
        "buffer_load_dwordx4 v[0:3], v240, %[rd], s84 offen offset:0 sc1\n\t"
        "buffer_load_dwordx4 v[4:7], v240, %[rd], s84 offen offset:1024 sc1\n\t"
        "buffer_load_dwordx4 v[8:11], v240, %[rd], s84 offen offset:2048 sc1\n\t"
        "buffer_load_dwordx4 v[12:15], v240, %[rd], s84 offen offset:3072 sc1\n\t"
        "s_add_u32 s84, s84, 8192\n\t"
        "buffer_load_dwordx4 v[16:19], v240, %[rd], s84 offen offset:0 sc1\n\t"
        "buffer_load_dwordx4 v[20:23], v240, %[rd], s84 offen offset:1024 sc1\n\t"
        "buffer_load_dwordx4 v[24:27], v240, %[rd], s84 offen offset:2048 sc1\n\t"
        "buffer_load_dwordx4 v[28:31], v240, %[rd], s84 offen offset:3072 sc1\n\t"
        "s_add_u32 s84, s84, 8192\n\t"
        "buffer_load_dwordx4 v[32:35], v240, %[rd], s84 offen offset:0 sc1\n\t"
        "buffer_load_dwordx4 v[36:39], v240, %[rd], s84 offen offset:1024 sc1\n\t"
        "buffer_load_dwordx4 v[40:43], v240, %[rd], s84 offen offset:2048 sc1\n\t"
        "buffer_load_dwordx4 v[44:47], v240, %[rd], s84 offen offset:3072 sc1\n\t"
        "s_add_u32 s84, s84, 8192\n\t"
        "buffer_load_dwordx4 v[48:51], v240, %[rd], s84 offen offset:0 sc1\n\t"
        "buffer_load_dwordx4 v[52:55], v240, %[rd], s84 offen offset:1024 sc1\n\t"
        "buffer_load_dwordx4 v[56:59], v240, %[rd], s84 offen offset:2048 sc1\n\t"
        "buffer_load_dwordx4 v[60:63], v240, %[rd], s84 offen offset:3072 sc1\n\t"
        "s_sub_u32 s84, s84, 20480\n\t"
        "buffer_load_dwordx4 v[64:67], v240, %[rd], s84 offen offset:0 sc1\n\t"
        "buffer_load_dwordx4 v[68:71], v240, %[rd], s84 offen offset:1024 sc1\n\t"
        "buffer_load_dwordx4 v[72:75], v240, %[rd], s84 offen offset:2048 sc1\n\t"
        "buffer_load_dwordx4 v[76:79], v240, %[rd], s84 offen offset:3072 sc1\n\t"
        "s_add_u32 s84, s84, 8192\n\t"
        "buffer_load_dwordx4 v[80:83], v240, %[rd], s84 offen offset:0 sc1\n\t"
        "buffer_load_dwordx4 v[84:87], v240, %[rd], s84 offen offset:1024 sc1\n\t"
        "buffer_load_dwordx4 v[88:91], v240, %[rd], s84 offen offset:2048 sc1\n\t"
        "buffer_load_dwordx4 v[92:95], v240, %[rd], s84 offen offset:3072 sc1\n\t"
        "s_add_u32 s84, s84, 8192\n\t"
        "buffer_load_dwordx4 v[96:99], v240, %[rd], s84 offen offset:0 sc1\n\t"
        "buffer_load_dwordx4 v[100:103], v240, %[rd], s84 offen offset:1024 sc1\n\t"
        "buffer_load_dwordx4 v[104:107], v240, %[rd], s84 offen offset:2048 sc1\n\t"
        "buffer_load_dwordx4 v[108:111], v240, %[rd], s84 offen offset:3072 sc1\n\t"
        "s_add_u32 s84, s84, 8192\n\t"
        "buffer_load_dwordx4 v[112:115], v240, %[rd], s84 offen offset:0 sc1\n\t"
        "buffer_load_dwordx4 v[116:119], v240, %[rd], s84 offen offset:1024 sc1\n\t"
        "buffer_load_dwordx4 v[120:123], v240, %[rd], s84 offen offset:2048 sc1\n\t"
        "buffer_load_dwordx4 v[124:127], v240, %[rd], s84 offen offset:3072 sc1\n\t"
        "s_add_u32 s84, s84, 8192\n\t"
        "buffer_store_dword v0, off, %[ro], s36\n\t"
        "buffer_store_dword v0, off, %[ro], s36\n\t"
        "buffer_store_dword v0, off, %[ro], s36\n\t"
        "buffer_store_dword v0, off, %[ro], s36\n\t"
        "buffer_store_dword v0, off, %[ro], s36\n\t"
        "buffer_store_dword v0, off, %[ro], s36\n\t"
        "buffer_store_dword v0, off, %[ro], s36\n\t"
        "buffer_store_dword v0, off, %[ro], s36\n\t"
        "buffer_store_dword v0, off, %[ro], s36\n\t"
        "buffer_store_dword v0, off, %[ro], s36\n\t"
        "buffer_store_dword v0, off, %[ro], s36\n\t"
        "buffer_store_dword v0, off, %[ro], s36\n\t"
        "buffer_store_dword v0, off, %[ro], s36\n\t"
        "buffer_store_dword v0, off, %[ro], s36\n\t"
        "buffer_store_dword v0, off, %[ro], s36\n\t"
        "buffer_store_dword v0, off, %[ro], s36\n\t"
        "buffer_store_dword v0, off, %[ro], s36\n\t"
        ".Lrnd%=:\n\t"
        "s_lshl_b32 s85, s37, 1\n\t"
        "s_and_b32 s86, s40, 0x3fffffff\n\t"
        "s_lshr_b32 s86, s86, s85\n\t"
        "s_andn2_b32 s80, 12, s86\n\t"
        "s_cselect_b32 s41, 0, 1\n\t"
        "s_add_u32 s84, s44, 8\n\t"
        "s_lshl_b32 s84, s84, 17\n\t"
        "s_cmp_eq_u32 s41, 0\n\t"
        "s_cselect_b32 s84, s36, s84\n\t"
        "s_cselect_b32 s87, 0, s36\n\t"
        "s_andn2_b32 s80, 48, s86\n\t"
        "s_cselect_b32 s80, 0, s36\n\t"
        "v_add_u32_e32 v222, s43, v247\n\t"
        "s_sub_u32 s81, 33280, s43\n\t"
        "v_add_u32_e32 v223, s81, v248\n\t"
        "buffer_load_dword v220, v242, %[rd], s80 offen sc1\n\t"
        "s_waitcnt vmcnt(49)\n\t"
        "v_mfma_f32_16x16x32_bf16 v[128:131], a[128:131], v[0:3], 0\n\t"
        "ds_read_b128 v[186:189], v223 offset:0\n\t"
        "ds_read_b128 v[190:193], v223 offset:8320\n\t"
        "v_mfma_f32_16x16x32_bf16 v[132:135], a[132:135], v[0:3], 0\n\t"
        "ds_read_b128 v[194:197], v223 offset:16640\n\t"
        "ds_read_b128 v[198:201], v223 offset:24960\n\t"
        "v_mfma_f32_16x16x32_bf16 v[136:139], a[136:139], v[0:3], 0\n\t"
        "s_nop 0\n\t"
        "s_nop 0\n\t"
        "v_mfma_f32_16x16x32_bf16 v[140:143], a[140:143], v[0:3], 0\n\t"
        "s_nop 0\n\t"
        "s_nop 0\n\t"
        "buffer_load_dwordx4 v[0:3], v240, %[rd], s84 offen offset:0 sc1\n\t"
        "s_waitcnt vmcnt(49)\n\t"
        "v_mfma_f32_16x16x32_bf16 v[128:131], a[144:147], v[4:7], v[128:131]\n\t"
        "s_nop 0\n\t"
        "s_nop 0\n\t"
        "v_mfma_f32_16x16x32_bf16 v[132:135], a[148:151], v[4:7], v[132:135]\n\t"
        "s_waitcnt lgkmcnt(0)\n\t"
        "v_add_f32_e32 v186, v186, v190\n\t"
        "v_mfma_f32_16x16x32_bf16 v[136:139], a[152:155], v[4:7], v[136:139]\n\t"
        "v_add_f32_e32 v187, v187, v191\n\t"
        "v_add_f32_e32 v188, v188, v192\n\t"
        "v_mfma_f32_16x16x32_bf16 v[140:143], a[156:159], v[4:7], v[140:143]\n\t"
        "v_add_f32_e32 v189, v189, v193\n\t"
        "v_add_f32_e32 v194, v194, v198\n\t"
        "buffer_load_dwordx4 v[4:7], v240, %[rd], s84 offen offset:1024 sc1\n\t"
        "s_waitcnt vmcnt(49)\n\t"
        "v_mfma_f32_16x16x32_bf16 v[128:131], a[160:163], v[8:11], v[128:131]\n\t"
        "v_add_f32_e32 v195, v195, v199\n\t"
        "v_add_f32_e32 v196, v196, v200\n\t"
        "v_mfma_f32_16x16x32_bf16 v[132:135], a[164:167], v[8:11], v[132:135]\n\t"
        "v_add_f32_e32 v197, v197, v201\n\t"
        "v_add_f32_e32 v204, v186, v194\n\t"
        "v_mfma_f32_16x16x32_bf16 v[136:139], a[168:171], v[8:11], v[136:139]\n\t"
        "v_add_f32_e32 v206, v187, v195\n\t"
        "v_add_f32_e32 v208, v188, v196\n\t"
        "v_mfma_f32_16x16x32_bf16 v[140:143], a[172:175], v[8:11], v[140:143]\n\t"
        "v_add_f32_e32 v210, v189, v197\n\t"
        "s_add_i32 s80, s38, 0\n\t"
        "buffer_load_dwordx4 v[8:11], v240, %[rd], s84 offen offset:2048 sc1\n\t"
        "s_waitcnt vmcnt(47)\n\t"
        "v_mfma_f32_16x16x32_bf16 v[128:131], a[176:179], v[12:15], v[128:131]\n\t"
        "s_lshl_b32 s80, s80, 17\n\t"
        "s_or_b32 s81, s38, %[own]\n\t"
        "v_mfma_f32_16x16x32_bf16 v[132:135], a[180:183], v[12:15], v[132:135]\n\t"
        "s_cmp_lt_i32 s81, 0\n\t"
        "s_cselect_b32 s82, s36, s80\n\t"
        "buffer_store_dwordx4 v[204:207], v246, %[rd], s82 offen offset:0 sc1\n\t"
        "v_mfma_f32_16x16x32_bf16 v[136:139], a[184:187], v[12:15], v[136:139]\n\t"
        "buffer_store_dwordx4 v[208:211], v246, %[rd], s82 offen offset:1024 sc1\n\t"
        "v_mov_b32_e32 v144, v204\n\t"
        "v_mfma_f32_16x16x32_bf16 v[140:143], a[188:191], v[12:15], v[140:143]\n\t"
        "v_mov_b32_e32 v145, v206\n\t"
        "v_mov_b32_e32 v146, v208\n\t"
        "buffer_load_dwordx4 v[12:15], v240, %[rd], s84 offen offset:3072 sc1\n\t"
        "s_add_u32 s84, s84, 8192\n\t"
        "s_waitcnt vmcnt(49)\n\t"
        "v_mfma_f32_16x16x32_bf16 v[128:131], a[192:195], v[16:19], v[128:131]\n\t"
        "v_mov_b32_e32 v147, v210\n\t"
        "ds_read_b128 v[186:189], v223 offset:4160\n\t"
        "v_mfma_f32_16x16x32_bf16 v[132:135], a[196:199], v[16:19], v[132:135]\n\t"
        "ds_read_b128 v[190:193], v223 offset:12480\n\t"
        "ds_read_b128 v[194:197], v223 offset:20800\n\t"
        "v_mfma_f32_16x16x32_bf16 v[136:139], a[200:203], v[16:19], v[136:139]\n\t"
        "ds_read_b128 v[198:201], v223 offset:29120\n\t"
        "s_nop 0\n\t"
        "v_mfma_f32_16x16x32_bf16 v[140:143], a[204:207], v[16:19], v[140:143]\n\t"
        "s_nop 0\n\t"
        "s_nop 0\n\t"
        "buffer_load_dwordx4 v[16:19], v240, %[rd], s84 offen offset:0 sc1\n\t"
        "s_waitcnt vmcnt(49)\n\t"
        "v_mfma_f32_16x16x32_bf16 v[128:131], a[208:211], v[20:23], v[128:131]\n\t"
        "s_nop 0\n\t"
        "s_nop 0\n\t"
        "v_mfma_f32_16x16x32_bf16 v[132:135], a[212:215], v[20:23], v[132:135]\n\t"
        "s_nop 0\n\t"
        "s_waitcnt lgkmcnt(0)\n\t"
        "v_mfma_f32_16x16x32_bf16 v[136:139], a[216:219], v[20:23], v[136:139]\n\t"
        "v_add_f32_e32 v186, v186, v190\n\t"
        "v_add_f32_e32 v187, v187, v191\n\t"
        "v_mfma_f32_16x16x32_bf16 v[140:143], a[220:223], v[20:23], v[140:143]\n\t"
        "v_add_f32_e32 v188, v188, v192\n\t"
        "v_add_f32_e32 v189, v189, v193\n\t"
        "buffer_load_dwordx4 v[20:23], v240, %[rd], s84 offen offset:1024 sc1\n\t"
        "s_waitcnt vmcnt(49)\n\t"
        "v_mfma_f32_16x16x32_bf16 v[128:131], a[224:227], v[24:27], v[128:131]\n\t"
        "v_add_f32_e32 v194, v194, v198\n\t"
        "v_add_f32_e32 v195, v195, v199\n\t"
        "v_mfma_f32_16x16x32_bf16 v[132:135], a[228:231], v[24:27], v[132:135]\n\t"
        "v_add_f32_e32 v196, v196, v200\n\t"
        "v_add_f32_e32 v197, v197, v201\n\t"
        "v_mfma_f32_16x16x32_bf16 v[136:139], a[232:235], v[24:27], v[136:139]\n\t"
        "v_add_f32_e32 v212, v186, v194\n\t"
        "v_add_f32_e32 v214, v187, v195\n\t"
        "v_mfma_f32_16x16x32_bf16 v[140:143], a[236:239], v[24:27], v[140:143]\n\t"
        "v_add_f32_e32 v216, v188, v196\n\t"
        "v_add_f32_e32 v218, v189, v197\n\t"
        "buffer_load_dwordx4 v[24:27], v240, %[rd], s84 offen offset:2048 sc1\n\t"
        "s_waitcnt vmcnt(47)\n\t"
        "v_mfma_f32_16x16x32_bf16 v[128:131], a[240:243], v[28:31], v[128:131]\n\t"
        "s_add_i32 s80, s38, 1\n\t"
        "s_lshl_b32 s80, s80, 17\n\t"
        "v_mfma_f32_16x16x32_bf16 v[132:135], a[244:247], v[28:31], v[132:135]\n\t"
        "s_or_b32 s81, s38, %[own]\n\t"
        "s_cmp_lt_i32 s81, 0\n\t"
        "s_cselect_b32 s82, s36, s80\n\t"
        "v_mfma_f32_16x16x32_bf16 v[136:139], a[248:251], v[28:31], v[136:139]\n\t"
        "buffer_store_dwordx4 v[212:215], v246, %[rd], s82 offen offset:0 sc1\n\t"
        "buffer_store_dwordx4 v[216:219], v246, %[rd], s82 offen offset:1024 sc1\n\t"
        "v_mfma_f32_16x16x32_bf16 v[140:143], a[252:255], v[28:31], v[140:143]\n\t"
        "v_mov_b32_e32 v148, v212\n\t"
        "v_mov_b32_e32 v149, v214\n\t"
        "buffer_load_dwordx4 v[28:31], v240, %[rd], s84 offen offset:3072 sc1\n\t"
        "s_add_u32 s84, s84, 8192\n\t"
        "s_waitcnt vmcnt(49)\n\t"
        "v_mfma_f32_16x16x32_bf16 v[128:131], a[0:3], v[32:35], v[128:131]\n\t"
        "v_mov_b32_e32 v150, v216\n\t"
        "v_mov_b32_e32 v151, v218\n\t"
        "v_mfma_f32_16x16x32_bf16 v[132:135], a[4:7], v[32:35], v[132:135]\n\t"
        "s_waitcnt vmcnt(34)\n\t"
        "s_add_i32 s80, s39, 0\n\t"
        "v_mfma_f32_16x16x32_bf16 v[136:139], a[8:11], v[32:35], v[136:139]\n\t"
        "s_lshl_b32 s80, s80, 15\n\t"
        "s_or_b32 s81, s39, %[snd]\n\t"
        "v_mfma_f32_16x16x32_bf16 v[140:143], a[12:15], v[32:35], v[140:143]\n\t"
        "s_cmp_lt_i32 s81, 0\n\t"
        "s_cselect_b32 s83, s36, s80\n\t"
        "s_cselect_b64 s[88:89], 0, -1\n\t"
        "v_xor_b32_e32 v224, %[tag], v161\n\t"
        "buffer_load_dwordx4 v[32:35], v240, %[rd], s84 offen offset:0 sc1\n\t"
        "s_waitcnt vmcnt(49)\n\t"
        "v_mfma_f32_16x16x32_bf16 v[128:131], a[16:19], v[36:39], v[128:131]\n\t"
        "v_xor_b32_e32 v225, %[tag], v163\n\t"
        "v_xor_b32_e32 v226, %[tag], v165\n\t"
        "v_mfma_f32_16x16x32_bf16 v[132:135], a[20:23], v[36:39], v[132:135]\n\t"
        "v_or3_b32 v224, v224, v225, v226\n\t"
        "v_xor_b32_e32 v225, %[tag], v167\n\t"
        "v_mfma_f32_16x16x32_bf16 v[136:139], a[24:27], v[36:39], v[136:139]\n\t"
        "v_or_b32_e32 v224, v224, v225\n\t"
        "v_cmp_ne_u32_e32 vcc, 0, v224\n\t"
        "v_mfma_f32_16x16x32_bf16 v[140:143], a[28:31], v[36:39], v[140:143]\n\t"
        "v_sub_f32_e32 v236, 1.0, v184\n\t"
        "v_cvt_pk_bf16_f32 v236, 0, v236\n\t"
        "s_and_b64 vcc, vcc, s[88:89]\n\t"
        "s_cbranch_scc1 .Lo13%=\n\t"
        ".Lo14%=:\n\t"
        "buffer_load_dwordx4 v[36:39], v240, %[rd], s84 offen offset:1024 sc1\n\t"
        "s_waitcnt vmcnt(49)\n\t"
        "v_mfma_f32_16x16x32_bf16 v[128:131], a[32:35], v[40:43], v[128:131]\n\t"
        "v_add_f32_e32 v228, v152, v160\n\t"
        "v_add_f32_e32 v229, v153, v162\n\t"
        "v_mfma_f32_16x16x32_bf16 v[132:135], a[36:39], v[40:43], v[132:135]\n\t"
        "v_add_f32_e32 v230, v154, v164\n\t"
        "v_add_f32_e32 v231, v155, v166\n\t"
        "v_mfma_f32_16x16x32_bf16 v[136:139], a[40:43], v[40:43], v[136:139]\n\t"
        "v_cvt_pk_bf16_f32 v228, 0, v228\n\t"
        "v_cvt_pk_bf16_f32 v229, 0, v229\n\t"
        "v_mfma_f32_16x16x32_bf16 v[140:143], a[44:47], v[40:43], v[140:143]\n\t"
        "v_cvt_pk_bf16_f32 v230, 0, v230\n\t"
        "v_cvt_pk_bf16_f32 v231, 0, v231\n\t"
        "buffer_load_dwordx4 v[40:43], v240, %[rd], s84 offen offset:2048 sc1\n\t"
        "s_waitcnt vmcnt(49)\n\t"
        "v_mfma_f32_16x16x32_bf16 v[128:131], a[48:51], v[44:47], v[128:131]\n\t"
        "v_lshlrev_b32_e32 v224, 16, v180\n\t"
        "v_lshlrev_b32_e32 v232, 16, v176\n\t"
        "v_mfma_f32_16x16x32_bf16 v[132:135], a[52:55], v[44:47], v[132:135]\n\t"
        "v_and_b32_e32 v225, 0xffff0000, v180\n\t"
        "v_and_b32_e32 v233, 0xffff0000, v176\n\t"
        "v_mfma_f32_16x16x32_bf16 v[136:139], a[56:59], v[44:47], v[136:139]\n\t"
        "v_lshlrev_b32_e32 v226, 16, v181\n\t"
        "v_lshlrev_b32_e32 v234, 16, v177\n\t"
        "v_mfma_f32_16x16x32_bf16 v[140:143], a[60:63], v[44:47], v[140:143]\n\t"
        "v_and_b32_e32 v227, 0xffff0000, v181\n\t"
        "v_and_b32_e32 v235, 0xffff0000, v177\n\t"
        "buffer_load_dwordx4 v[44:47], v240, %[rd], s84 offen offset:3072 sc1\n\t"
        "s_add_u32 s84, s84, 8192\n\t"
        "s_waitcnt vmcnt(49)\n\t"
        "v_mfma_f32_16x16x32_bf16 v[128:131], a[64:67], v[48:51], v[128:131]\n\t"
        "v_mul_f32_e32 v224, v224, v232\n\t"
        "v_mul_f32_e32 v225, v225, v233\n\t"
        "v_mfma_f32_16x16x32_bf16 v[132:135], a[68:71], v[48:51], v[132:135]\n\t"
        "v_mul_f32_e32 v226, v226, v234\n\t"
        "v_mul_f32_e32 v227, v227, v235\n\t"
        "v_mfma_f32_16x16x32_bf16 v[136:139], a[72:75], v[48:51], v[136:139]\n\t"
        "v_mul_f32_e32 v228, v236, v228\n\t"
        "v_mul_f32_e32 v229, v236, v229\n\t"
        "v_mfma_f32_16x16x32_bf16 v[140:143], a[76:79], v[48:51], v[140:143]\n\t"
        "v_mul_f32_e32 v230, v236, v230\n\t"
        "v_mul_f32_e32 v231, v236, v231\n\t"
        "buffer_load_dwordx4 v[48:51], v240, %[rd], s84 offen offset:0 sc1\n\t"
        "s_waitcnt vmcnt(49)\n\t"
        "v_mfma_f32_16x16x32_bf16 v[128:131], a[80:83], v[52:55], v[128:131]\n\t"
        "v_cvt_pk_bf16_f32 v224, 0, v224\n\t"
        "v_cvt_pk_bf16_f32 v225, 0, v225\n\t"
        "v_mfma_f32_16x16x32_bf16 v[132:135], a[84:87], v[52:55], v[132:135]\n\t"
        "v_cvt_pk_bf16_f32 v226, 0, v226\n\t"
        "v_cvt_pk_bf16_f32 v227, 0, v227\n\t"
        "v_mfma_f32_16x16x32_bf16 v[136:139], a[88:91], v[52:55], v[136:139]\n\t"
        "v_cvt_pk_bf16_f32 v228, 0, v228\n\t"
        "v_cvt_pk_bf16_f32 v229, 0, v229\n\t"
        "v_mfma_f32_16x16x32_bf16 v[140:143], a[92:95], v[52:55], v[140:143]\n\t"
        "v_cvt_pk_bf16_f32 v230, 0, v230\n\t"
        "v_cvt_pk_bf16_f32 v231, 0, v231\n\t"
        "buffer_load_dwordx4 v[52:55], v240, %[rd], s84 offen offset:1024 sc1\n\t"
        "s_waitcnt vmcnt(49)\n\t"
        "v_mfma_f32_16x16x32_bf16 v[128:131], a[96:99], v[56:59], v[128:131]\n\t"
        "v_mul_f32_e32 v224, v184, v224\n\t"
        "v_mul_f32_e32 v225, v184, v225\n\t"
        "v_mfma_f32_16x16x32_bf16 v[132:135], a[100:103], v[56:59], v[132:135]\n\t"
        "v_mul_f32_e32 v226, v184, v226\n\t"
        "v_mul_f32_e32 v227, v184, v227\n\t"
        "v_mfma_f32_16x16x32_bf16 v[136:139], a[104:107], v[56:59], v[136:139]\n\t"
        "v_cvt_pk_bf16_f32 v224, 0, v224\n\t"
        "v_cvt_pk_bf16_f32 v225, 0, v225\n\t"
        "v_mfma_f32_16x16x32_bf16 v[140:143], a[108:111], v[56:59], v[140:143]\n\t"
        "v_cvt_pk_bf16_f32 v226, 0, v226\n\t"
        "v_cvt_pk_bf16_f32 v227, 0, v227\n\t"
        "buffer_load_dwordx4 v[56:59], v240, %[rd], s84 offen offset:2048 sc1\n\t"
        "s_waitcnt vmcnt(48)\n\t"
        "v_mfma_f32_16x16x32_bf16 v[128:131], a[112:115], v[60:63], v[128:131]\n\t"
        "v_add_f32_e32 v224, v224, v228\n\t"
        "v_add_f32_e32 v225, v225, v229\n\t"
        "v_mfma_f32_16x16x32_bf16 v[132:135], a[116:119], v[60:63], v[132:135]\n\t"
        "v_add_f32_e32 v226, v226, v230\n\t"
        "v_add_f32_e32 v227, v227, v231\n\t"
        "v_mfma_f32_16x16x32_bf16 v[136:139], a[120:123], v[60:63], v[136:139]\n\t"
        "v_cvt_pk_bf16_f32 v238, v224, v225\n\t"
        "v_cvt_pk_bf16_f32 v239, v226, v227\n\t"
        "v_mfma_f32_16x16x32_bf16 v[140:143], a[124:127], v[60:63], v[140:143]\n\t"
        "buffer_store_dwordx2 v[238:239], v245, %[ro], s83 offen\n\t"
        "s_add_i32 s80, s38, 0\n\t"
        "buffer_load_dwordx4 v[60:63], v240, %[rd], s84 offen offset:3072 sc1\n\t"
        "s_sub_u32 s84, s84, 20480\n\t"
        "ds_write_b128 v222, v[128:131] offset:0\n\t"
        "ds_write_b128 v222, v[132:135] offset:1040\n\t"
        "ds_write_b128 v222, v[136:139] offset:2080\n\t"
        "s_nop 0\n\t"
        "ds_write_b128 v222, v[140:143] offset:3120\n\t"
        "s_waitcnt vmcnt(47)\n\t"
        "v_mfma_f32_16x16x32_bf16 v[128:131], a[128:131], v[64:67], 0\n\t"
        "s_lshl_b32 s80, s80, 6\n\t"
        "s_or_b32 s81, s38, %[snd]\n\t"
        "v_mfma_f32_16x16x32_bf16 v[132:135], a[132:135], v[64:67], 0\n\t"
        "s_cmp_lt_i32 s81, 0\n\t"
        "s_cselect_b32 s82, s36, s80\n\t"
        "buffer_load_dword v184, v244, %[rd], s82 offen sc1\n\t"
        "v_mfma_f32_16x16x32_bf16 v[136:139], a[136:139], v[64:67], 0\n\t"
        "s_lshl_b32 s80, s80, 1\n\t"
        "s_cmp_lt_i32 s81, 0\n\t"
        "s_cselect_b32 s82, s36, s80\n\t"
        "v_mfma_f32_16x16x32_bf16 v[140:143], a[140:143], v[64:67], 0\n\t"
        "buffer_load_dwordx2 v[180:181], v243, %[rd], s82 offen sc1\n\t"
        "s_lshl_b32 s80, s80, 8\n\t"
        "buffer_load_dwordx4 v[64:67], v240, %[rd], s84 offen offset:0 sc1\n\t"
        "s_waitcnt vmcnt(46)\n\t"
        "v_mfma_f32_16x16x32_bf16 v[128:131], a[144:147], v[68:71], v[128:131]\n\t"
        "s_cmp_lt_i32 s81, 0\n\t"
        "s_cselect_b32 s82, s36, s80\n\t"
        "buffer_load_dwordx2 v[176:177], v245, %[rx], s82 offen\n\t"
        "v_mfma_f32_16x16x32_bf16 v[132:135], a[148:151], v[68:71], v[132:135]\n\t"
        "s_add_i32 s80, s38, 0\n\t"
        "s_lshl_b32 s80, s80, 17\n\t"
        "v_mfma_f32_16x16x32_bf16 v[136:139], a[152:155], v[68:71], v[136:139]\n\t"
        "s_or_b32 s81, s38, %[snd]\n\t"
        "s_cmp_lt_i32 s81, 0\n\t"
        "s_cselect_b32 s82, s36, s80\n\t"
        "v_mfma_f32_16x16x32_bf16 v[140:143], a[156:159], v[68:71], v[140:143]\n\t"
        "buffer_load_dwordx4 v[160:163], v246, %[rd], s82 offen offset:0 sc1\n\t"
        "buffer_load_dwordx4 v[164:167], v246, %[rd], s82 offen offset:1024 sc1\n\t"
        "buffer_load_dwordx4 v[68:71], v240, %[rd], s84 offen offset:1024 sc1\n\t"
        "s_waitcnt vmcnt(49)\n\t"
        "v_mfma_f32_16x16x32_bf16 v[128:131], a[160:163], v[72:75], v[128:131]\n\t"
        "s_waitcnt vmcnt(34)\n\t"
        "v_mfma_f32_16x16x32_bf16 v[132:135], a[164:167], v[72:75], v[132:135]\n\t"
        "s_add_i32 s80, s39, 1\n\t"
        "s_lshl_b32 s80, s80, 15\n\t"
        "v_mfma_f32_16x16x32_bf16 v[136:139], a[168:171], v[72:75], v[136:139]\n\t"
        "s_or_b32 s81, s39, %[snd]\n\t"
        "s_cmp_lt_i32 s81, 0\n\t"
        "s_cselect_b32 s83, s36, s80\n\t"
        "s_cselect_b64 s[88:89], 0, -1\n\t"
        "v_mfma_f32_16x16x32_bf16 v[140:143], a[172:175], v[72:75], v[140:143]\n\t"
        "v_xor_b32_e32 v224, %[tag], v169\n\t"
        "v_xor_b32_e32 v225, %[tag], v171\n\t"
        "buffer_load_dwordx4 v[72:75], v240, %[rd], s84 offen offset:2048 sc1\n\t"
        "s_waitcnt vmcnt(49)\n\t"
        "v_mfma_f32_16x16x32_bf16 v[128:131], a[176:179], v[76:79], v[128:131]\n\t"
        "v_xor_b32_e32 v226, %[tag], v173\n\t"
        "v_or3_b32 v224, v224, v225, v226\n\t"
        "v_mfma_f32_16x16x32_bf16 v[132:135], a[180:183], v[76:79], v[132:135]\n\t"
        "v_xor_b32_e32 v225, %[tag], v175\n\t"
        "v_or_b32_e32 v224, v224, v225\n\t"
        "v_mfma_f32_16x16x32_bf16 v[136:139], a[184:187], v[76:79], v[136:139]\n\t"
        "v_cmp_ne_u32_e32 vcc, 0, v224\n\t"
        "v_sub_f32_e32 v236, 1.0, v185\n\t"
        "v_mfma_f32_16x16x32_bf16 v[140:143], a[188:191], v[76:79], v[140:143]\n\t"
        "v_cvt_pk_bf16_f32 v236, 0, v236\n\t"
        "s_and_b64 vcc, vcc, s[88:89]\n\t"
        "s_cbranch_scc1 .Lo15%=\n\t"
        ".Lo16%=:\n\t"
        "v_add_f32_e32 v228, v156, v168\n\t"
        "buffer_load_dwordx4 v[76:79], v240, %[rd], s84 offen offset:3072 sc1\n\t"
        "s_add_u32 s84, s84, 8192\n\t"
        "s_waitcnt vmcnt(49)\n\t"
        "v_mfma_f32_16x16x32_bf16 v[128:131], a[192:195], v[80:83], v[128:131]\n\t"
        "v_add_f32_e32 v229, v157, v170\n\t"
        "v_add_f32_e32 v230, v158, v172\n\t"
        "v_mfma_f32_16x16x32_bf16 v[132:135], a[196:199], v[80:83], v[132:135]\n\t"
        "v_add_f32_e32 v231, v159, v174\n\t"
        "v_cvt_pk_bf16_f32 v228, 0, v228\n\t"
        "v_mfma_f32_16x16x32_bf16 v[136:139], a[200:203], v[80:83], v[136:139]\n\t"
        "v_cvt_pk_bf16_f32 v229, 0, v229\n\t"
        "v_cvt_pk_bf16_f32 v230, 0, v230\n\t"
        "v_mfma_f32_16x16x32_bf16 v[140:143], a[204:207], v[80:83], v[140:143]\n\t"
        "v_cvt_pk_bf16_f32 v231, 0, v231\n\t"
        "v_lshlrev_b32_e32 v224, 16, v182\n\t"
        "buffer_load_dwordx4 v[80:83], v240, %[rd], s84 offen offset:0 sc1\n\t"
        "s_waitcnt vmcnt(49)\n\t"
        "v_mfma_f32_16x16x32_bf16 v[128:131], a[208:211], v[84:87], v[128:131]\n\t"
        "v_lshlrev_b32_e32 v232, 16, v178\n\t"
        "v_and_b32_e32 v225, 0xffff0000, v182\n\t"
        "v_mfma_f32_16x16x32_bf16 v[132:135], a[212:215], v[84:87], v[132:135]\n\t"
        "v_and_b32_e32 v233, 0xffff0000, v178\n\t"
        "v_lshlrev_b32_e32 v226, 16, v183\n\t"
        "v_mfma_f32_16x16x32_bf16 v[136:139], a[216:219], v[84:87], v[136:139]\n\t"
        "v_lshlrev_b32_e32 v234, 16, v179\n\t"
        "v_and_b32_e32 v227, 0xffff0000, v183\n\t"
        "v_mfma_f32_16x16x32_bf16 v[140:143], a[220:223], v[84:87], v[140:143]\n\t"
        "v_and_b32_e32 v235, 0xffff0000, v179\n\t"
        "v_mul_f32_e32 v224, v224, v232\n\t"
        "buffer_load_dwordx4 v[84:87], v240, %[rd], s84 offen offset:1024 sc1\n\t"
        "s_waitcnt vmcnt(49)\n\t"
        "v_mfma_f32_16x16x32_bf16 v[128:131], a[224:227], v[88:91], v[128:131]\n\t"
        "v_mul_f32_e32 v225, v225, v233\n\t"
        "v_mul_f32_e32 v226, v226, v234\n\t"
        "v_mfma_f32_16x16x32_bf16 v[132:135], a[228:231], v[88:91], v[132:135]\n\t"
        "v_mul_f32_e32 v227, v227, v235\n\t"
        "v_mul_f32_e32 v228, v236, v228\n\t"
        "v_mfma_f32_16x16x32_bf16 v[136:139], a[232:235], v[88:91], v[136:139]\n\t"
        "v_mul_f32_e32 v229, v236, v229\n\t"
        "v_mul_f32_e32 v230, v236, v230\n\t"
        "v_mfma_f32_16x16x32_bf16 v[140:143], a[236:239], v[88:91], v[140:143]\n\t"
        "v_mul_f32_e32 v231, v236, v231\n\t"
        "v_cvt_pk_bf16_f32 v224, 0, v224\n\t"
        "buffer_load_dwordx4 v[88:91], v240, %[rd], s84 offen offset:2048 sc1\n\t"
        "s_waitcnt vmcnt(49)\n\t"
        "v_mfma_f32_16x16x32_bf16 v[128:131], a[240:243], v[92:95], v[128:131]\n\t"
        "v_cvt_pk_bf16_f32 v225, 0, v225\n\t"
        "v_cvt_pk_bf16_f32 v226, 0, v226\n\t"
        "v_mfma_f32_16x16x32_bf16 v[132:135], a[244:247], v[92:95], v[132:135]\n\t"
        "v_cvt_pk_bf16_f32 v227, 0, v227\n\t"
        "v_cvt_pk_bf16_f32 v228, 0, v228\n\t"
        "v_mfma_f32_16x16x32_bf16 v[136:139], a[248:251], v[92:95], v[136:139]\n\t"
        "v_cvt_pk_bf16_f32 v229, 0, v229\n\t"
        "v_cvt_pk_bf16_f32 v230, 0, v230\n\t"
        "v_mfma_f32_16x16x32_bf16 v[140:143], a[252:255], v[92:95], v[140:143]\n\t"
        "v_cvt_pk_bf16_f32 v231, 0, v231\n\t"
        "v_mul_f32_e32 v224, v185, v224\n\t"
        "buffer_load_dwordx4 v[92:95], v240, %[rd], s84 offen offset:3072 sc1\n\t"
        "s_add_u32 s84, s84, 8192\n\t"
        "buffer_load_dword v221, v242, %[rd], s87 offen sc1\n\t"
        "s_waitcnt vmcnt(49)\n\t"
        "v_mfma_f32_16x16x32_bf16 v[128:131], a[0:3], v[96:99], v[128:131]\n\t"
        "v_mul_f32_e32 v225, v185, v225\n\t"
        "v_mul_f32_e32 v226, v185, v226\n\t"
        "v_mfma_f32_16x16x32_bf16 v[132:135], a[4:7], v[96:99], v[132:135]\n\t"
        "v_mul_f32_e32 v227, v185, v227\n\t"
        "v_cvt_pk_bf16_f32 v224, 0, v224\n\t"
        "v_mfma_f32_16x16x32_bf16 v[136:139], a[8:11], v[96:99], v[136:139]\n\t"
        "v_cvt_pk_bf16_f32 v225, 0, v225\n\t"
        "v_cvt_pk_bf16_f32 v226, 0, v226\n\t"
        "v_mfma_f32_16x16x32_bf16 v[140:143], a[12:15], v[96:99], v[140:143]\n\t"
        "v_cvt_pk_bf16_f32 v227, 0, v227\n\t"
        "v_add_f32_e32 v224, v224, v228\n\t"
        "buffer_load_dwordx4 v[96:99], v240, %[rd], s84 offen offset:0 sc1\n\t"
        "s_waitcnt vmcnt(48)\n\t"
        "v_mfma_f32_16x16x32_bf16 v[128:131], a[16:19], v[100:103], v[128:131]\n\t"
        "v_add_f32_e32 v225, v225, v229\n\t"
        "v_add_f32_e32 v226, v226, v230\n\t"
        "v_mfma_f32_16x16x32_bf16 v[132:135], a[20:23], v[100:103], v[132:135]\n\t"
        "v_add_f32_e32 v227, v227, v231\n\t"
        "v_cvt_pk_bf16_f32 v238, v224, v225\n\t"
        "v_mfma_f32_16x16x32_bf16 v[136:139], a[24:27], v[100:103], v[136:139]\n\t"
        "v_cvt_pk_bf16_f32 v239, v226, v227\n\t"
        "buffer_store_dwordx2 v[238:239], v245, %[ro], s83 offen\n\t"
        "v_mfma_f32_16x16x32_bf16 v[140:143], a[28:31], v[100:103], v[140:143]\n\t"
        "s_add_i32 s80, s38, 1\n\t"
        "s_lshl_b32 s80, s80, 6\n\t"
        "buffer_load_dwordx4 v[100:103], v240, %[rd], s84 offen offset:1024 sc1\n\t"
        "s_waitcnt vmcnt(47)\n\t"
        "v_mfma_f32_16x16x32_bf16 v[128:131], a[32:35], v[104:107], v[128:131]\n\t"
        "s_or_b32 s81, s38, %[snd]\n\t"
        "s_cmp_lt_i32 s81, 0\n\t"
        "s_cselect_b32 s82, s36, s80\n\t"
        "v_mfma_f32_16x16x32_bf16 v[132:135], a[36:39], v[104:107], v[132:135]\n\t"
        "buffer_load_dword v185, v244, %[rd], s82 offen sc1\n\t"
        "s_lshl_b32 s80, s80, 1\n\t"
        "v_mfma_f32_16x16x32_bf16 v[136:139], a[40:43], v[104:107], v[136:139]\n\t"
        "s_cmp_lt_i32 s81, 0\n\t"
        "s_cselect_b32 s82, s36, s80\n\t"
        "buffer_load_dwordx2 v[182:183], v243, %[rd], s82 offen sc1\n\t"
        "v_mfma_f32_16x16x32_bf16 v[140:143], a[44:47], v[104:107], v[140:143]\n\t"
        "s_lshl_b32 s80, s80, 8\n\t"
        "s_cmp_lt_i32 s81, 0\n\t"
        "s_cselect_b32 s82, s36, s80\n\t"
        "buffer_load_dwordx4 v[104:107], v240, %[rd], s84 offen offset:2048 sc1\n\t"
        "s_waitcnt vmcnt(46)\n\t"
        "v_mfma_f32_16x16x32_bf16 v[128:131], a[48:51], v[108:111], v[128:131]\n\t"
        "buffer_load_dwordx2 v[178:179], v245, %[rx], s82 offen\n\t"
        "s_add_i32 s80, s38, 1\n\t"
        "v_mfma_f32_16x16x32_bf16 v[132:135], a[52:55], v[108:111], v[132:135]\n\t"
        "s_lshl_b32 s80, s80, 17\n\t"
        "s_or_b32 s81, s38, %[snd]\n\t"
        "v_mfma_f32_16x16x32_bf16 v[136:139], a[56:59], v[108:111], v[136:139]\n\t"
        "s_cmp_lt_i32 s81, 0\n\t"
        "s_cselect_b32 s82, s36, s80\n\t"
        "buffer_load_dwordx4 v[168:171], v246, %[rd], s82 offen offset:0 sc1\n\t"
        "v_mfma_f32_16x16x32_bf16 v[140:143], a[60:63], v[108:111], v[140:143]\n\t"
        "buffer_load_dwordx4 v[172:175], v246, %[rd], s82 offen offset:1024 sc1\n\t"
        "buffer_load_dwordx4 v[108:111], v240, %[rd], s84 offen offset:3072 sc1\n\t"
        "s_add_u32 s84, s84, 8192\n\t"
        "s_waitcnt vmcnt(49)\n\t"
        "v_mfma_f32_16x16x32_bf16 v[128:131], a[64:67], v[112:115], v[128:131]\n\t"
        "v_mfma_f32_16x16x32_bf16 v[132:135], a[68:71], v[112:115], v[132:135]\n\t"
        "v_mfma_f32_16x16x32_bf16 v[136:139], a[72:75], v[112:115], v[136:139]\n\t"
        "v_mfma_f32_16x16x32_bf16 v[140:143], a[76:79], v[112:115], v[140:143]\n\t"
        "buffer_load_dwordx4 v[112:115], v240, %[rd], s84 offen offset:0 sc1\n\t"
        "s_waitcnt vmcnt(49)\n\t"
        "v_mfma_f32_16x16x32_bf16 v[128:131], a[80:83], v[116:119], v[128:131]\n\t"
        "v_mfma_f32_16x16x32_bf16 v[132:135], a[84:87], v[116:119], v[132:135]\n\t"
        "v_mfma_f32_16x16x32_bf16 v[136:139], a[88:91], v[116:119], v[136:139]\n\t"
        "v_mfma_f32_16x16x32_bf16 v[140:143], a[92:95], v[116:119], v[140:143]\n\t"
        "buffer_load_dwordx4 v[116:119], v240, %[rd], s84 offen offset:1024 sc1\n\t"
        "s_waitcnt vmcnt(49)\n\t"
        "v_mfma_f32_16x16x32_bf16 v[128:131], a[96:99], v[120:123], v[128:131]\n\t"
        "v_mfma_f32_16x16x32_bf16 v[132:135], a[100:103], v[120:123], v[132:135]\n\t"
        "v_mfma_f32_16x16x32_bf16 v[136:139], a[104:107], v[120:123], v[136:139]\n\t"
        "v_mfma_f32_16x16x32_bf16 v[140:143], a[108:111], v[120:123], v[140:143]\n\t"
        "buffer_load_dwordx4 v[120:123], v240, %[rd], s84 offen offset:2048 sc1\n\t"
        "s_waitcnt vmcnt(49)\n\t"
        "v_mfma_f32_16x16x32_bf16 v[128:131], a[112:115], v[124:127], v[128:131]\n\t"
        "v_mfma_f32_16x16x32_bf16 v[132:135], a[116:119], v[124:127], v[132:135]\n\t"
        "v_mfma_f32_16x16x32_bf16 v[136:139], a[120:123], v[124:127], v[136:139]\n\t"
        "v_mfma_f32_16x16x32_bf16 v[140:143], a[124:127], v[124:127], v[140:143]\n\t"
        "buffer_load_dwordx4 v[124:127], v240, %[rd], s84 offen offset:3072 sc1\n\t"
        "s_add_u32 s84, s84, 8192\n\t"
        "s_nop 2\n\t"
        "ds_write_b128 v222, v[128:131] offset:4160\n\t"
        "ds_write_b128 v222, v[132:135] offset:5200\n\t"
        "ds_write_b128 v222, v[136:139] offset:6240\n\t"
        "ds_write_b128 v222, v[140:143] offset:7280\n\t"
        "s_waitcnt vmcnt(49)\n\t"
        "v_cmp_eq_u32_e32 vcc, 32, v220\n\t"
        "s_nop 1\n\t"
        "s_or_b32 s40, s40, vcc_lo\n\t"
        "s_waitcnt lgkmcnt(0)\n\t"
        "s_barrier\n\t"
        "s_cmp_eq_u32 s41, 0\n\t"
        "s_cbranch_scc1 .Lfl%=\n\t"
        "v_mov_b32_e32 v152, v144\n\t"
        "v_mov_b32_e32 v153, v145\n\t"
        "v_mov_b32_e32 v154, v146\n\t"
        "v_mov_b32_e32 v155, v147\n\t"
        "v_mov_b32_e32 v156, v148\n\t"
        "v_mov_b32_e32 v157, v149\n\t"
        "v_mov_b32_e32 v158, v150\n\t"
        "v_mov_b32_e32 v159, v151\n\t"
        "s_mov_b32 s39, s38\n\t"
        "s_mov_b32 s38, s44\n\t"
        "s_add_u32 s37, s37, 1\n\t"
        "s_add_u32 s44, s44, 8\n\t"
        "s_sub_u32 s43, 33280, s43\n\t"
        "s_branch .Lrnd%=\n\t"
        ".Lfl%=:\n\t"
        "v_add_u32_e32 v223, s43, v248\n\t"
        "buffer_load_dword v220, v242, %[rd], 0 offen sc1\n\t"
        "ds_read_b128 v[186:189], v223 offset:0\n\t"
        "ds_read_b128 v[190:193], v223 offset:8320\n\t"
        "ds_read_b128 v[194:197], v223 offset:16640\n\t"
        "ds_read_b128 v[198:201], v223 offset:24960\n\t"
        "s_waitcnt lgkmcnt(0)\n\t"
        "v_add_f32_e32 v186, v186, v190\n\t"
        "v_add_f32_e32 v187, v187, v191\n\t"
        "v_add_f32_e32 v188, v188, v192\n\t"
        "v_add_f32_e32 v189, v189, v193\n\t"
        "v_add_f32_e32 v194, v194, v198\n\t"
        "v_add_f32_e32 v195, v195, v199\n\t"
        "v_add_f32_e32 v196, v196, v200\n\t"
        "v_add_f32_e32 v197, v197, v201\n\t"
        "v_add_f32_e32 v204, v186, v194\n\t"
        "v_add_f32_e32 v206, v187, v195\n\t"
        "v_add_f32_e32 v208, v188, v196\n\t"
        "v_add_f32_e32 v210, v189, v197\n\t"
        "s_add_i32 s80, s44, 0\n\t"
        "s_lshl_b32 s80, s80, 17\n\t"
        "s_or_b32 s81, s44, %[own]\n\t"
        "s_cmp_lt_i32 s81, 0\n\t"
        "s_cselect_b32 s82, s36, s80\n\t"
        "buffer_store_dwordx4 v[204:207], v246, %[rd], s82 offen offset:0 sc1\n\t"
        "buffer_store_dwordx4 v[208:211], v246, %[rd], s82 offen offset:1024 sc1\n\t"
        "v_mov_b32_e32 v128, v204\n\t"
        "v_mov_b32_e32 v129, v206\n\t"
        "v_mov_b32_e32 v130, v208\n\t"
        "v_mov_b32_e32 v131, v210\n\t"
        "ds_read_b128 v[186:189], v223 offset:4160\n\t"
        "ds_read_b128 v[190:193], v223 offset:12480\n\t"
        "ds_read_b128 v[194:197], v223 offset:20800\n\t"
        "ds_read_b128 v[198:201], v223 offset:29120\n\t"
        "s_waitcnt lgkmcnt(0)\n\t"
        "v_add_f32_e32 v186, v186, v190\n\t"
        "v_add_f32_e32 v187, v187, v191\n\t"
        "v_add_f32_e32 v188, v188, v192\n\t"
        "v_add_f32_e32 v189, v189, v193\n\t"
        "v_add_f32_e32 v194, v194, v198\n\t"
        "v_add_f32_e32 v195, v195, v199\n\t"
        "v_add_f32_e32 v196, v196, v200\n\t"
        "v_add_f32_e32 v197, v197, v201\n\t"
        "v_add_f32_e32 v212, v186, v194\n\t"
        "v_add_f32_e32 v214, v187, v195\n\t"
        "v_add_f32_e32 v216, v188, v196\n\t"
        "v_add_f32_e32 v218, v189, v197\n\t"
        "s_add_i32 s80, s44, 1\n\t"
        "s_lshl_b32 s80, s80, 17\n\t"
        "s_or_b32 s81, s44, %[own]\n\t"
        "s_cmp_lt_i32 s81, 0\n\t"
        "s_cselect_b32 s82, s36, s80\n\t"
        "buffer_store_dwordx4 v[212:215], v246, %[rd], s82 offen offset:0 sc1\n\t"
        "buffer_store_dwordx4 v[216:219], v246, %[rd], s82 offen offset:1024 sc1\n\t"
        "v_mov_b32_e32 v132, v212\n\t"
        "v_mov_b32_e32 v133, v214\n\t"
        "v_mov_b32_e32 v134, v216\n\t"
        "v_mov_b32_e32 v135, v218\n\t"
        "s_waitcnt vmcnt(4)\n\t"
        "v_cmp_eq_u32_e32 vcc, 32, v220\n\t"
        "s_nop 1\n\t"
        "s_or_b32 s40, s40, vcc_lo\n\t"
        "s_add_i32 s80, s38, 0\n\t"
        "s_lshl_b32 s80, s80, 15\n\t"
        "s_or_b32 s81, s38, %[snd]\n\t"
        "s_cmp_lt_i32 s81, 0\n\t"
        "s_cselect_b32 s83, s36, s80\n\t"
        "s_cbranch_scc1 .Lo19%=\n\t"
        "s_mov_b32 s97, 0\n\t"
        ".Lo17%=:\n\t"
        "v_xor_b32_e32 v186, %[tag], v161\n\t"
        "v_xor_b32_e32 v187, %[tag], v163\n\t"
        "v_xor_b32_e32 v188, %[tag], v165\n\t"
        "v_or3_b32 v186, v186, v187, v188\n\t"
        "v_xor_b32_e32 v187, %[tag], v167\n\t"
        "v_or_b32_e32 v186, v186, v187\n\t"
        "v_cmp_ne_u32_e32 vcc, 0, v186\n\t"
        "v_sub_f32_e32 v198, 1.0, v184\n\t"
        "v_add_f32_e32 v190, v144, v160\n\t"
        "v_add_f32_e32 v191, v145, v162\n\t"
        "v_add_f32_e32 v192, v146, v164\n\t"
        "v_add_f32_e32 v193, v147, v166\n\t"
        "s_cbranch_vccz .Lo18%=\n\t"
        "s_sleep %c[xslp]\n\t"
        "s_lshl_b32 s82, s83, 2\n\t"
        "buffer_load_dwordx4 v[160:163], v246, %[rd], s82 offen offset:0 sc1\n\t"
        "buffer_load_dwordx4 v[164:167], v246, %[rd], s82 offen offset:1024 sc1\n\t"
        "s_add_u32 s97, s97, 1\n\t"
        "s_cmp_lt_u32 s97, 0x200000\n\t"
        "s_cbranch_scc0 .Lgu%=\n\t"
        "s_waitcnt vmcnt(0)\n\t"
        "s_branch .Lo17%=\n\t"
        ".Lo18%=:\n\t"
        "v_cvt_pk_bf16_f32 v198, 0, v198\n\t"
        "v_cvt_pk_bf16_f32 v190, 0, v190\n\t"
        "v_cvt_pk_bf16_f32 v191, 0, v191\n\t"
        "v_cvt_pk_bf16_f32 v192, 0, v192\n\t"
        "v_cvt_pk_bf16_f32 v193, 0, v193\n\t"
        "v_lshlrev_b32_e32 v186, 16, v180\n\t"
        "v_lshlrev_b32_e32 v194, 16, v176\n\t"
        "v_and_b32_e32 v187, 0xffff0000, v180\n\t"
        "v_and_b32_e32 v195, 0xffff0000, v176\n\t"
        "v_lshlrev_b32_e32 v188, 16, v181\n\t"
        "v_lshlrev_b32_e32 v196, 16, v177\n\t"
        "v_and_b32_e32 v189, 0xffff0000, v181\n\t"
        "v_and_b32_e32 v197, 0xffff0000, v177\n\t"
        "v_mul_f32_e32 v186, v186, v194\n\t"
        "v_mul_f32_e32 v187, v187, v195\n\t"
        "v_mul_f32_e32 v188, v188, v196\n\t"
        "v_mul_f32_e32 v189, v189, v197\n\t"
        "v_mul_f32_e32 v190, v198, v190\n\t"
        "v_mul_f32_e32 v191, v198, v191\n\t"
        "v_mul_f32_e32 v192, v198, v192\n\t"
        "v_mul_f32_e32 v193, v198, v193\n\t"
        "v_cvt_pk_bf16_f32 v186, 0, v186\n\t"
        "v_cvt_pk_bf16_f32 v187, 0, v187\n\t"
        "v_cvt_pk_bf16_f32 v188, 0, v188\n\t"
        "v_cvt_pk_bf16_f32 v189, 0, v189\n\t"
        "v_cvt_pk_bf16_f32 v190, 0, v190\n\t"
        "v_cvt_pk_bf16_f32 v191, 0, v191\n\t"
        "v_cvt_pk_bf16_f32 v192, 0, v192\n\t"
        "v_cvt_pk_bf16_f32 v193, 0, v193\n\t"
        "v_mul_f32_e32 v186, v184, v186\n\t"
        "v_mul_f32_e32 v187, v184, v187\n\t"
        "v_mul_f32_e32 v188, v184, v188\n\t"
        "v_mul_f32_e32 v189, v184, v189\n\t"
        "v_cvt_pk_bf16_f32 v186, 0, v186\n\t"
        "v_cvt_pk_bf16_f32 v187, 0, v187\n\t"
        "v_cvt_pk_bf16_f32 v188, 0, v188\n\t"
        "v_cvt_pk_bf16_f32 v189, 0, v189\n\t"
        "v_add_f32_e32 v186, v186, v190\n\t"
        "v_add_f32_e32 v187, v187, v191\n\t"
        "v_add_f32_e32 v188, v188, v192\n\t"
        "v_add_f32_e32 v189, v189, v193\n\t"
        "v_cvt_pk_bf16_f32 v200, v186, v187\n\t"
        "v_cvt_pk_bf16_f32 v201, v188, v189\n\t"
        ".Lo19%=:\n\t"
        "buffer_store_dwordx2 v[200:201], v245, %[ro], s83 offen\n\t"
        "s_add_i32 s80, s38, 1\n\t"
        "s_lshl_b32 s80, s80, 15\n\t"
        "s_or_b32 s81, s38, %[snd]\n\t"
        "s_cmp_lt_i32 s81, 0\n\t"
        "s_cselect_b32 s83, s36, s80\n\t"
        "s_cbranch_scc1 .Lo22%=\n\t"
        "s_mov_b32 s97, 0\n\t"
        ".Lo20%=:\n\t"
        "v_xor_b32_e32 v186, %[tag], v169\n\t"
        "v_xor_b32_e32 v187, %[tag], v171\n\t"
        "v_xor_b32_e32 v188, %[tag], v173\n\t"
        "v_or3_b32 v186, v186, v187, v188\n\t"
        "v_xor_b32_e32 v187, %[tag], v175\n\t"
        "v_or_b32_e32 v186, v186, v187\n\t"
        "v_cmp_ne_u32_e32 vcc, 0, v186\n\t"
        "v_sub_f32_e32 v198, 1.0, v185\n\t"
        "v_add_f32_e32 v190, v148, v168\n\t"
        "v_add_f32_e32 v191, v149, v170\n\t"
        "v_add_f32_e32 v192, v150, v172\n\t"
        "v_add_f32_e32 v193, v151, v174\n\t"
        "s_cbranch_vccz .Lo21%=\n\t"
        "s_sleep %c[xslp]\n\t"
        "s_lshl_b32 s82, s83, 2\n\t"
        "buffer_load_dwordx4 v[168:171], v246, %[rd], s82 offen offset:0 sc1\n\t"
        "buffer_load_dwordx4 v[172:175], v246, %[rd], s82 offen offset:1024 sc1\n\t"
        "s_add_u32 s97, s97, 1\n\t"
        "s_cmp_lt_u32 s97, 0x200000\n\t"
        "s_cbranch_scc0 .Lgu%=\n\t"
        "s_waitcnt vmcnt(0)\n\t"
        "s_branch .Lo20%=\n\t"
        ".Lo21%=:\n\t"
        "v_cvt_pk_bf16_f32 v198, 0, v198\n\t"
        "v_cvt_pk_bf16_f32 v190, 0, v190\n\t"
        "v_cvt_pk_bf16_f32 v191, 0, v191\n\t"
        "v_cvt_pk_bf16_f32 v192, 0, v192\n\t"
        "v_cvt_pk_bf16_f32 v193, 0, v193\n\t"
        "v_lshlrev_b32_e32 v186, 16, v182\n\t"
        "v_lshlrev_b32_e32 v194, 16, v178\n\t"
        "v_and_b32_e32 v187, 0xffff0000, v182\n\t"
        "v_and_b32_e32 v195, 0xffff0000, v178\n\t"
        "v_lshlrev_b32_e32 v188, 16, v183\n\t"
        "v_lshlrev_b32_e32 v196, 16, v179\n\t"
        "v_and_b32_e32 v189, 0xffff0000, v183\n\t"
        "v_and_b32_e32 v197, 0xffff0000, v179\n\t"
        "v_mul_f32_e32 v186, v186, v194\n\t"
        "v_mul_f32_e32 v187, v187, v195\n\t"
        "v_mul_f32_e32 v188, v188, v196\n\t"
        "v_mul_f32_e32 v189, v189, v197\n\t"
        "v_mul_f32_e32 v190, v198, v190\n\t"
        "v_mul_f32_e32 v191, v198, v191\n\t"
        "v_mul_f32_e32 v192, v198, v192\n\t"
        "v_mul_f32_e32 v193, v198, v193\n\t"
        "v_cvt_pk_bf16_f32 v186, 0, v186\n\t"
        "v_cvt_pk_bf16_f32 v187, 0, v187\n\t"
        "v_cvt_pk_bf16_f32 v188, 0, v188\n\t"
        "v_cvt_pk_bf16_f32 v189, 0, v189\n\t"
        "v_cvt_pk_bf16_f32 v190, 0, v190\n\t"
        "v_cvt_pk_bf16_f32 v191, 0, v191\n\t"
        "v_cvt_pk_bf16_f32 v192, 0, v192\n\t"
        "v_cvt_pk_bf16_f32 v193, 0, v193\n\t"
        "v_mul_f32_e32 v186, v185, v186\n\t"
        "v_mul_f32_e32 v187, v185, v187\n\t"
        "v_mul_f32_e32 v188, v185, v188\n\t"
        "v_mul_f32_e32 v189, v185, v189\n\t"
        "v_cvt_pk_bf16_f32 v186, 0, v186\n\t"
        "v_cvt_pk_bf16_f32 v187, 0, v187\n\t"
        "v_cvt_pk_bf16_f32 v188, 0, v188\n\t"
        "v_cvt_pk_bf16_f32 v189, 0, v189\n\t"
        "v_add_f32_e32 v186, v186, v190\n\t"
        "v_add_f32_e32 v187, v187, v191\n\t"
        "v_add_f32_e32 v188, v188, v192\n\t"
        "v_add_f32_e32 v189, v189, v193\n\t"
        "v_cvt_pk_bf16_f32 v200, v186, v187\n\t"
        "v_cvt_pk_bf16_f32 v201, v188, v189\n\t"
        ".Lo22%=:\n\t"
        "buffer_store_dwordx2 v[200:201], v245, %[ro], s83 offen\n\t"
        "s_add_i32 s80, s44, 0\n\t"
        "s_lshl_b32 s80, s80, 6\n\t"
        "s_or_b32 s81, s44, %[snd]\n\t"
        "s_cmp_lt_i32 s81, 0\n\t"
        "s_cselect_b32 s82, s36, s80\n\t"
        "buffer_load_dword v184, v244, %[rd], s82 offen sc1\n\t"
        "s_lshl_b32 s80, s80, 1\n\t"
        "s_cmp_lt_i32 s81, 0\n\t"
        "s_cselect_b32 s82, s36, s80\n\t"
        "buffer_load_dwordx2 v[180:181], v243, %[rd], s82 offen sc1\n\t"
        "s_lshl_b32 s80, s80, 8\n\t"
        "s_cmp_lt_i32 s81, 0\n\t"
        "s_cselect_b32 s82, s36, s80\n\t"
        "buffer_load_dwordx2 v[176:177], v245, %[rx], s82 offen\n\t"
        "s_add_i32 s80, s44, 1\n\t"
        "s_lshl_b32 s80, s80, 6\n\t"
        "s_or_b32 s81, s44, %[snd]\n\t"
        "s_cmp_lt_i32 s81, 0\n\t"
        "s_cselect_b32 s82, s36, s80\n\t"
        "buffer_load_dword v185, v244, %[rd], s82 offen sc1\n\t"
        "s_lshl_b32 s80, s80, 1\n\t"
        "s_cmp_lt_i32 s81, 0\n\t"
        "s_cselect_b32 s82, s36, s80\n\t"
        "buffer_load_dwordx2 v[182:183], v243, %[rd], s82 offen sc1\n\t"
        "s_lshl_b32 s80, s80, 8\n\t"
        "s_cmp_lt_i32 s81, 0\n\t"
        "s_cselect_b32 s82, s36, s80\n\t"
        "buffer_load_dwordx2 v[178:179], v245, %[rx], s82 offen\n\t"
        "s_add_i32 s80, s44, 0\n\t"
        "s_lshl_b32 s80, s80, 17\n\t"
        "s_or_b32 s81, s44, %[snd]\n\t"
        "s_cmp_lt_i32 s81, 0\n\t"
        "s_cselect_b32 s82, s36, s80\n\t"
        "buffer_load_dwordx4 v[160:163], v246, %[rd], s82 offen offset:0 sc1\n\t"
        "buffer_load_dwordx4 v[164:167], v246, %[rd], s82 offen offset:1024 sc1\n\t"
        "s_add_i32 s80, s44, 1\n\t"
        "s_lshl_b32 s80, s80, 17\n\t"
        "s_or_b32 s81, s44, %[snd]\n\t"
        "s_cmp_lt_i32 s81, 0\n\t"
        "s_cselect_b32 s82, s36, s80\n\t"
        "buffer_load_dwordx4 v[168:171], v246, %[rd], s82 offen offset:0 sc1\n\t"
        "buffer_load_dwordx4 v[172:175], v246, %[rd], s82 offen offset:1024 sc1\n\t"
        "s_mov_b32 s38, -1\n\t"
        "v_mov_b32_e32 v152, v128\n\t"
        "v_mov_b32_e32 v153, v129\n\t"
        "v_mov_b32_e32 v154, v130\n\t"
        "v_mov_b32_e32 v155, v131\n\t"
        "v_mov_b32_e32 v156, v132\n\t"
        "v_mov_b32_e32 v157, v133\n\t"
        "v_mov_b32_e32 v158, v134\n\t"
        "v_mov_b32_e32 v159, v135\n\t"
        "s_mov_b32 s39, s44\n\t"
        "s_add_u32 s37, s37, 1\n\t"
        "s_add_u32 s44, s44, 8\n\t"
        "s_sub_u32 s43, 33280, s43\n\t"
        "s_branch .Lnew%=\n\t"
        ".Lsol%=:\n\t"
        "s_add_u32 s44, %[rho], 120\n\t"
        "s_mov_b32 s37, 30\n\t"
        ".Lone%=:\n\t"
        "s_add_i32 s80, s44, 0\n\t"
        "s_lshl_b32 s80, s80, 6\n\t"
        "s_or_b32 s81, s44, %[snd]\n\t"
        "s_cmp_lt_i32 s81, 0\n\t"
        "s_cselect_b32 s82, s36, s80\n\t"
        "buffer_load_dword v184, v244, %[rd], s82 offen sc1\n\t"
        "s_lshl_b32 s80, s80, 1\n\t"
        "s_cmp_lt_i32 s81, 0\n\t"
        "s_cselect_b32 s82, s36, s80\n\t"
        "buffer_load_dwordx2 v[180:181], v243, %[rd], s82 offen sc1\n\t"
        "s_lshl_b32 s80, s80, 8\n\t"
        "s_cmp_lt_i32 s81, 0\n\t"
        "s_cselect_b32 s82, s36, s80\n\t"
        "buffer_load_dwordx2 v[176:177], v245, %[rx], s82 offen\n\t"
        "v_add_u32_e32 v222, s43, v247\n\t"
        "v_add_u32_e32 v223, s43, v248\n\t"
        "s_mov_b32 s97, 0\n\t"
        ".Lo23%=:\n\t"
        "s_lshr_b32 s80, s40, s37\n\t"
        "s_andn2_b32 s80, 1, s80\n\t"
        "s_cbranch_scc0 .Lo24%=\n\t"
        "s_cmp_eq_u32 s97, 0\n\t"
        "s_cbranch_scc1 .Lo23%=x\n\t"
        "s_sleep %c[oslp]\n\t"
        ".Lo23%=x:\n\t"
        "buffer_load_dword v220, v242, %[rd], 0 offen sc1\n\t"
        "s_add_u32 s97, s97, 1\n\t"
        "s_cmp_lt_u32 s97, 0x200000\n\t"
        "s_cbranch_scc0 .Lgu%=\n\t"
        "s_waitcnt vmcnt(0)\n\t"
        "v_cmp_eq_u32_e32 vcc, 32, v220\n\t"
        "s_nop 1\n\t"
        "s_or_b32 s40, s40, vcc_lo\n\t"
        "s_branch .Lo23%=\n\t"
        ".Lo24%=:\n\t"
        "s_lshr_b32 s84, s44, 1\n\t"
        "s_lshl_b32 s84, s84, 18\n\t"
        "s_and_b32 s80, s44, 1\n\t"
        "s_lshl_b32 s80, s80, 12\n\t"
        "s_or_b32 s84, s84, s80\n\t"
        "buffer_load_dwordx4 v[0:3], v240, %[rd], s84 offen offset:0 sc1\n\t"
        "buffer_load_dwordx4 v[4:7], v240, %[rd], s84 offen offset:1024 sc1\n\t"
        "buffer_load_dwordx4 v[8:11], v240, %[rd], s84 offen offset:2048 sc1\n\t"
        "buffer_load_dwordx4 v[12:15], v240, %[rd], s84 offen offset:3072 sc1\n\t"
        "s_add_u32 s84, s84, 8192\n\t"
        "buffer_load_dwordx4 v[16:19], v240, %[rd], s84 offen offset:0 sc1\n\t"
        "buffer_load_dwordx4 v[20:23], v240, %[rd], s84 offen offset:1024 sc1\n\t"
        "buffer_load_dwordx4 v[24:27], v240, %[rd], s84 offen offset:2048 sc1\n\t"
        "buffer_load_dwordx4 v[28:31], v240, %[rd], s84 offen offset:3072 sc1\n\t"
        "s_add_u32 s84, s84, 8192\n\t"
        "buffer_load_dwordx4 v[32:35], v240, %[rd], s84 offen offset:0 sc1\n\t"
        "buffer_load_dwordx4 v[36:39], v240, %[rd], s84 offen offset:1024 sc1\n\t"
        "buffer_load_dwordx4 v[40:43], v240, %[rd], s84 offen offset:2048 sc1\n\t"
        "buffer_load_dwordx4 v[44:47], v240, %[rd], s84 offen offset:3072 sc1\n\t"
        "s_add_u32 s84, s84, 8192\n\t"
        "buffer_load_dwordx4 v[48:51], v240, %[rd], s84 offen offset:0 sc1\n\t"
        "buffer_load_dwordx4 v[52:55], v240, %[rd], s84 offen offset:1024 sc1\n\t"
        "buffer_load_dwordx4 v[56:59], v240, %[rd], s84 offen offset:2048 sc1\n\t"
        "buffer_load_dwordx4 v[60:63], v240, %[rd], s84 offen offset:3072 sc1\n\t"
        "s_sub_u32 s84, s84, 20480\n\t"
        "s_waitcnt vmcnt(15)\n\t"
        "v_mfma_f32_16x16x32_bf16 v[128:131], a[128:131], v[0:3], 0\n\t"
        "v_mfma_f32_16x16x32_bf16 v[132:135], a[132:135], v[0:3], 0\n\t"
        "v_mfma_f32_16x16x32_bf16 v[136:139], a[136:139], v[0:3], 0\n\t"
        "v_mfma_f32_16x16x32_bf16 v[140:143], a[140:143], v[0:3], 0\n\t"
        "s_waitcnt vmcnt(14)\n\t"
        "v_mfma_f32_16x16x32_bf16 v[128:131], a[144:147], v[4:7], v[128:131]\n\t"
        "v_mfma_f32_16x16x32_bf16 v[132:135], a[148:151], v[4:7], v[132:135]\n\t"
        "v_mfma_f32_16x16x32_bf16 v[136:139], a[152:155], v[4:7], v[136:139]\n\t"
        "v_mfma_f32_16x16x32_bf16 v[140:143], a[156:159], v[4:7], v[140:143]\n\t"
        "s_waitcnt vmcnt(13)\n\t"
        "v_mfma_f32_16x16x32_bf16 v[128:131], a[160:163], v[8:11], v[128:131]\n\t"
        "v_mfma_f32_16x16x32_bf16 v[132:135], a[164:167], v[8:11], v[132:135]\n\t"
        "v_mfma_f32_16x16x32_bf16 v[136:139], a[168:171], v[8:11], v[136:139]\n\t"
        "v_mfma_f32_16x16x32_bf16 v[140:143], a[172:175], v[8:11], v[140:143]\n\t"
        "s_waitcnt vmcnt(12)\n\t"
        "v_mfma_f32_16x16x32_bf16 v[128:131], a[176:179], v[12:15], v[128:131]\n\t"
        "v_mfma_f32_16x16x32_bf16 v[132:135], a[180:183], v[12:15], v[132:135]\n\t"
        "v_mfma_f32_16x16x32_bf16 v[136:139], a[184:187], v[12:15], v[136:139]\n\t"
        "v_mfma_f32_16x16x32_bf16 v[140:143], a[188:191], v[12:15], v[140:143]\n\t"
        "s_waitcnt vmcnt(11)\n\t"
        "v_mfma_f32_16x16x32_bf16 v[128:131], a[192:195], v[16:19], v[128:131]\n\t"
        "v_mfma_f32_16x16x32_bf16 v[132:135], a[196:199], v[16:19], v[132:135]\n\t"
        "v_mfma_f32_16x16x32_bf16 v[136:139], a[200:203], v[16:19], v[136:139]\n\t"
        "v_mfma_f32_16x16x32_bf16 v[140:143], a[204:207], v[16:19], v[140:143]\n\t"
        "s_waitcnt vmcnt(10)\n\t"
        "v_mfma_f32_16x16x32_bf16 v[128:131], a[208:211], v[20:23], v[128:131]\n\t"
        "v_mfma_f32_16x16x32_bf16 v[132:135], a[212:215], v[20:23], v[132:135]\n\t"
        "v_mfma_f32_16x16x32_bf16 v[136:139], a[216:219], v[20:23], v[136:139]\n\t"
        "v_mfma_f32_16x16x32_bf16 v[140:143], a[220:223], v[20:23], v[140:143]\n\t"
        "s_waitcnt vmcnt(9)\n\t"
        "v_mfma_f32_16x16x32_bf16 v[128:131], a[224:227], v[24:27], v[128:131]\n\t"
        "v_mfma_f32_16x16x32_bf16 v[132:135], a[228:231], v[24:27], v[132:135]\n\t"
        "v_mfma_f32_16x16x32_bf16 v[136:139], a[232:235], v[24:27], v[136:139]\n\t"
        "v_mfma_f32_16x16x32_bf16 v[140:143], a[236:239], v[24:27], v[140:143]\n\t"
        "s_waitcnt vmcnt(8)\n\t"
        "v_mfma_f32_16x16x32_bf16 v[128:131], a[240:243], v[28:31], v[128:131]\n\t"
        "v_mfma_f32_16x16x32_bf16 v[132:135], a[244:247], v[28:31], v[132:135]\n\t"
        "v_mfma_f32_16x16x32_bf16 v[136:139], a[248:251], v[28:31], v[136:139]\n\t"
        "v_mfma_f32_16x16x32_bf16 v[140:143], a[252:255], v[28:31], v[140:143]\n\t"
        "s_waitcnt vmcnt(7)\n\t"
        "v_mfma_f32_16x16x32_bf16 v[128:131], a[0:3], v[32:35], v[128:131]\n\t"
        "v_mfma_f32_16x16x32_bf16 v[132:135], a[4:7], v[32:35], v[132:135]\n\t"
        "v_mfma_f32_16x16x32_bf16 v[136:139], a[8:11], v[32:35], v[136:139]\n\t"
        "v_mfma_f32_16x16x32_bf16 v[140:143], a[12:15], v[32:35], v[140:143]\n\t"
        "s_waitcnt vmcnt(6)\n\t"
        "v_mfma_f32_16x16x32_bf16 v[128:131], a[16:19], v[36:39], v[128:131]\n\t"
        "v_mfma_f32_16x16x32_bf16 v[132:135], a[20:23], v[36:39], v[132:135]\n\t"
        "v_mfma_f32_16x16x32_bf16 v[136:139], a[24:27], v[36:39], v[136:139]\n\t"
        "v_mfma_f32_16x16x32_bf16 v[140:143], a[28:31], v[36:39], v[140:143]\n\t"
        "s_waitcnt vmcnt(5)\n\t"
        "v_mfma_f32_16x16x32_bf16 v[128:131], a[32:35], v[40:43], v[128:131]\n\t"
        "v_mfma_f32_16x16x32_bf16 v[132:135], a[36:39], v[40:43], v[132:135]\n\t"
        "v_mfma_f32_16x16x32_bf16 v[136:139], a[40:43], v[40:43], v[136:139]\n\t"
        "v_mfma_f32_16x16x32_bf16 v[140:143], a[44:47], v[40:43], v[140:143]\n\t"
        "s_waitcnt vmcnt(4)\n\t"
        "v_mfma_f32_16x16x32_bf16 v[128:131], a[48:51], v[44:47], v[128:131]\n\t"
        "v_mfma_f32_16x16x32_bf16 v[132:135], a[52:55], v[44:47], v[132:135]\n\t"
        "v_mfma_f32_16x16x32_bf16 v[136:139], a[56:59], v[44:47], v[136:139]\n\t"
        "v_mfma_f32_16x16x32_bf16 v[140:143], a[60:63], v[44:47], v[140:143]\n\t"
        "s_waitcnt vmcnt(3)\n\t"
        "v_mfma_f32_16x16x32_bf16 v[128:131], a[64:67], v[48:51], v[128:131]\n\t"
        "v_mfma_f32_16x16x32_bf16 v[132:135], a[68:71], v[48:51], v[132:135]\n\t"
        "v_mfma_f32_16x16x32_bf16 v[136:139], a[72:75], v[48:51], v[136:139]\n\t"
        "v_mfma_f32_16x16x32_bf16 v[140:143], a[76:79], v[48:51], v[140:143]\n\t"
        "s_waitcnt vmcnt(2)\n\t"
        "v_mfma_f32_16x16x32_bf16 v[128:131], a[80:83], v[52:55], v[128:131]\n\t"
        "v_mfma_f32_16x16x32_bf16 v[132:135], a[84:87], v[52:55], v[132:135]\n\t"
        "v_mfma_f32_16x16x32_bf16 v[136:139], a[88:91], v[52:55], v[136:139]\n\t"
        "v_mfma_f32_16x16x32_bf16 v[140:143], a[92:95], v[52:55], v[140:143]\n\t"
        "s_waitcnt vmcnt(1)\n\t"
        "v_mfma_f32_16x16x32_bf16 v[128:131], a[96:99], v[56:59], v[128:131]\n\t"
        "v_mfma_f32_16x16x32_bf16 v[132:135], a[100:103], v[56:59], v[132:135]\n\t"
        "v_mfma_f32_16x16x32_bf16 v[136:139], a[104:107], v[56:59], v[136:139]\n\t"
        "v_mfma_f32_16x16x32_bf16 v[140:143], a[108:111], v[56:59], v[140:143]\n\t"
        "s_waitcnt vmcnt(0)\n\t"
        "v_mfma_f32_16x16x32_bf16 v[128:131], a[112:115], v[60:63], v[128:131]\n\t"
        "v_mfma_f32_16x16x32_bf16 v[132:135], a[116:119], v[60:63], v[132:135]\n\t"
        "v_mfma_f32_16x16x32_bf16 v[136:139], a[120:123], v[60:63], v[136:139]\n\t"
        "v_mfma_f32_16x16x32_bf16 v[140:143], a[124:127], v[60:63], v[140:143]\n\t"
        "s_nop 4\n\t"
        "ds_write_b128 v222, v[128:131] offset:0\n\t"
        "ds_write_b128 v222, v[132:135] offset:1040\n\t"
        "ds_write_b128 v222, v[136:139] offset:2080\n\t"
        "ds_write_b128 v222, v[140:143] offset:3120\n\t"
        "s_waitcnt lgkmcnt(0)\n\t"
        "s_barrier\n\t"
        "ds_read_b128 v[186:189], v223 offset:0\n\t"
        "ds_read_b128 v[190:193], v223 offset:8320\n\t"
        "ds_read_b128 v[194:197], v223 offset:16640\n\t"
        "ds_read_b128 v[198:201], v223 offset:24960\n\t"
        "s_waitcnt lgkmcnt(0)\n\t"
        "v_add_f32_e32 v186, v186, v190\n\t"
        "v_add_f32_e32 v187, v187, v191\n\t"
        "v_add_f32_e32 v188, v188, v192\n\t"
        "v_add_f32_e32 v189, v189, v193\n\t"
        "v_add_f32_e32 v194, v194, v198\n\t"
        "v_add_f32_e32 v195, v195, v199\n\t"
        "v_add_f32_e32 v196, v196, v200\n\t"
        "v_add_f32_e32 v197, v197, v201\n\t"
        "v_add_f32_e32 v204, v186, v194\n\t"
        "v_add_f32_e32 v206, v187, v195\n\t"
        "v_add_f32_e32 v208, v188, v196\n\t"
        "v_add_f32_e32 v210, v189, v197\n\t"
        "s_add_i32 s80, s44, 0\n\t"
        "s_lshl_b32 s80, s80, 17\n\t"
        "s_or_b32 s81, s44, %[own]\n\t"
        "s_cmp_lt_i32 s81, 0\n\t"
        "s_cselect_b32 s82, s36, s80\n\t"
        "buffer_store_dwordx4 v[204:207], v246, %[rd], s82 offen offset:0 sc1\n\t"
        "buffer_store_dwordx4 v[208:211], v246, %[rd], s82 offen offset:1024 sc1\n\t"
        "v_mov_b32_e32 v144, v204\n\t"
        "v_mov_b32_e32 v145, v206\n\t"
        "v_mov_b32_e32 v146, v208\n\t"
        "v_mov_b32_e32 v147, v210\n\t"
        "s_sleep %c[xdel]\n\t"
        "s_add_i32 s80, s44, 0\n\t"
        "s_lshl_b32 s80, s80, 17\n\t"
        "s_or_b32 s81, s44, %[snd]\n\t"
        "s_cmp_lt_i32 s81, 0\n\t"
        "s_cselect_b32 s82, s36, s80\n\t"
        "buffer_load_dwordx4 v[160:163], v246, %[rd], s82 offen offset:0 sc1\n\t"
        "buffer_load_dwordx4 v[164:167], v246, %[rd], s82 offen offset:1024 sc1\n\t"
        "s_waitcnt vmcnt(0)\n\t"
        "s_add_i32 s80, s44, 0\n\t"
        "s_lshl_b32 s80, s80, 15\n\t"
        "s_or_b32 s81, s44, %[snd]\n\t"
        "s_cmp_lt_i32 s81, 0\n\t"
        "s_cselect_b32 s83, s36, s80\n\t"
        "s_cbranch_scc1 .Lo27%=\n\t"
        "s_mov_b32 s97, 0\n\t"
        ".Lo25%=:\n\t"
        "v_xor_b32_e32 v186, %[tag], v161\n\t"
        "v_xor_b32_e32 v187, %[tag], v163\n\t"
        "v_xor_b32_e32 v188, %[tag], v165\n\t"
        "v_or3_b32 v186, v186, v187, v188\n\t"
        "v_xor_b32_e32 v187, %[tag], v167\n\t"
        "v_or_b32_e32 v186, v186, v187\n\t"
        "v_cmp_ne_u32_e32 vcc, 0, v186\n\t"
        "v_sub_f32_e32 v198, 1.0, v184\n\t"
        "v_add_f32_e32 v190, v144, v160\n\t"
        "v_add_f32_e32 v191, v145, v162\n\t"
        "v_add_f32_e32 v192, v146, v164\n\t"
        "v_add_f32_e32 v193, v147, v166\n\t"
        "s_cbranch_vccz .Lo26%=\n\t"
        "s_sleep %c[xslp]\n\t"
        "s_lshl_b32 s82, s83, 2\n\t"
        "buffer_load_dwordx4 v[160:163], v246, %[rd], s82 offen offset:0 sc1\n\t"
        "buffer_load_dwordx4 v[164:167], v246, %[rd], s82 offen offset:1024 sc1\n\t"
        "s_add_u32 s97, s97, 1\n\t"
        "s_cmp_lt_u32 s97, 0x200000\n\t"
        "s_cbranch_scc0 .Lgu%=\n\t"
        "s_waitcnt vmcnt(0)\n\t"
        "s_branch .Lo25%=\n\t"
        ".Lo26%=:\n\t"
        "v_cvt_pk_bf16_f32 v198, 0, v198\n\t"
        "v_cvt_pk_bf16_f32 v190, 0, v190\n\t"
        "v_cvt_pk_bf16_f32 v191, 0, v191\n\t"
        "v_cvt_pk_bf16_f32 v192, 0, v192\n\t"
        "v_cvt_pk_bf16_f32 v193, 0, v193\n\t"
        "v_lshlrev_b32_e32 v186, 16, v180\n\t"
        "v_lshlrev_b32_e32 v194, 16, v176\n\t"
        "v_and_b32_e32 v187, 0xffff0000, v180\n\t"
        "v_and_b32_e32 v195, 0xffff0000, v176\n\t"
        "v_lshlrev_b32_e32 v188, 16, v181\n\t"
        "v_lshlrev_b32_e32 v196, 16, v177\n\t"
        "v_and_b32_e32 v189, 0xffff0000, v181\n\t"
        "v_and_b32_e32 v197, 0xffff0000, v177\n\t"
        "v_mul_f32_e32 v186, v186, v194\n\t"
        "v_mul_f32_e32 v187, v187, v195\n\t"
        "v_mul_f32_e32 v188, v188, v196\n\t"
        "v_mul_f32_e32 v189, v189, v197\n\t"
        "v_mul_f32_e32 v190, v198, v190\n\t"
        "v_mul_f32_e32 v191, v198, v191\n\t"
        "v_mul_f32_e32 v192, v198, v192\n\t"
        "v_mul_f32_e32 v193, v198, v193\n\t"
        "v_cvt_pk_bf16_f32 v186, 0, v186\n\t"
        "v_cvt_pk_bf16_f32 v187, 0, v187\n\t"
        "v_cvt_pk_bf16_f32 v188, 0, v188\n\t"
        "v_cvt_pk_bf16_f32 v189, 0, v189\n\t"
        "v_cvt_pk_bf16_f32 v190, 0, v190\n\t"
        "v_cvt_pk_bf16_f32 v191, 0, v191\n\t"
        "v_cvt_pk_bf16_f32 v192, 0, v192\n\t"
        "v_cvt_pk_bf16_f32 v193, 0, v193\n\t"
        "v_mul_f32_e32 v186, v184, v186\n\t"
        "v_mul_f32_e32 v187, v184, v187\n\t"
        "v_mul_f32_e32 v188, v184, v188\n\t"
        "v_mul_f32_e32 v189, v184, v189\n\t"
        "v_cvt_pk_bf16_f32 v186, 0, v186\n\t"
        "v_cvt_pk_bf16_f32 v187, 0, v187\n\t"
        "v_cvt_pk_bf16_f32 v188, 0, v188\n\t"
        "v_cvt_pk_bf16_f32 v189, 0, v189\n\t"
        "v_add_f32_e32 v186, v186, v190\n\t"
        "v_add_f32_e32 v187, v187, v191\n\t"
        "v_add_f32_e32 v188, v188, v192\n\t"
        "v_add_f32_e32 v189, v189, v193\n\t"
        "v_cvt_pk_bf16_f32 v200, v186, v187\n\t"
        "v_cvt_pk_bf16_f32 v201, v188, v189\n\t"
        ".Lo27%=:\n\t"
        "buffer_store_dwordx2 v[200:201], v245, %[ro], s83 offen\n\t"
        "s_add_u32 s37, s37, 1\n\t"
        "s_add_u32 s44, s44, 4\n\t"
        "s_sub_u32 s43, 33280, s43\n\t"
        "s_cmp_lt_u32 s37, 32\n\t"
        "s_cbranch_scc1 .Lone%=\n\t"
        "buffer_store_dword v205, off, %[rd], %[ep] sc1\n\t"
        "s_endpgm\n\t"
        ".Lo13%=:\n\t"
        "s_mov_b32 s97, 0\n\t"
        "s_lshl_b32 s82, s83, 2\n\t"
        ".Lo13%=r:\n\t"
        "s_sleep %c[xslp]\n\t"
        "buffer_load_dwordx4 v[160:163], v246, %[rd], s82 offen offset:0 sc1\n\t"
        "buffer_load_dwordx4 v[164:167], v246, %[rd], s82 offen offset:1024 sc1\n\t"
        "s_add_u32 s97, s97, 1\n\t"
        "s_cmp_lt_u32 s97, 0x200000\n\t"
        "s_cbranch_scc0 .Lgu%=\n\t"
        "s_waitcnt vmcnt(0)\n\t"
        "v_xor_b32_e32 v224, %[tag], v161\n\t"
        "v_xor_b32_e32 v225, %[tag], v163\n\t"
        "v_xor_b32_e32 v226, %[tag], v165\n\t"
        "v_or3_b32 v224, v224, v225, v226\n\t"
        "v_xor_b32_e32 v225, %[tag], v167\n\t"
        "v_or_b32_e32 v224, v224, v225\n\t"
        "v_cmp_ne_u32_e32 vcc, 0, v224\n\t"
        "s_nop 3\n\t"
        "s_cbranch_vccnz .Lo13%=r\n\t"
        "s_branch .Lo14%=\n\t"
        ".Lo15%=:\n\t"
        "s_mov_b32 s97, 0\n\t"
        "s_lshl_b32 s82, s83, 2\n\t"
        ".Lo15%=r:\n\t"
        "s_sleep %c[xslp]\n\t"
        "buffer_load_dwordx4 v[168:171], v246, %[rd], s82 offen offset:0 sc1\n\t"
        "buffer_load_dwordx4 v[172:175], v246, %[rd], s82 offen offset:1024 sc1\n\t"
        "s_add_u32 s97, s97, 1\n\t"
        "s_cmp_lt_u32 s97, 0x200000\n\t"
        "s_cbranch_scc0 .Lgu%=\n\t"
        "s_waitcnt vmcnt(0)\n\t"
        "v_xor_b32_e32 v224, %[tag], v169\n\t"
        "v_xor_b32_e32 v225, %[tag], v171\n\t"
        "v_xor_b32_e32 v226, %[tag], v173\n\t"
        "v_or3_b32 v224, v224, v225, v226\n\t"
        "v_xor_b32_e32 v225, %[tag], v175\n\t"
        "v_or_b32_e32 v224, v224, v225\n\t"
        "v_cmp_ne_u32_e32 vcc, 0, v224\n\t"
        "s_nop 3\n\t"
        "s_cbranch_vccnz .Lo15%=r\n\t"
        "s_branch .Lo16%=\n\t"
        ".Lgu%=:\n\t"
        "v_mov_b32_e32 v0, 128\n\t"
        "s_mov_b32 s80, %c[ferr]\n\t"
        "buffer_store_dword v0, off, %[rd], s80 sc1\n\t"
        "s_endpgm"
        :: [rd] "s"(rd), [ro] "s"(ro), [rx] "s"(rx), [w] "s"(w), [rho] "s"(rho), [tag] "s"(tag), [own] "s"(own), [snd] "s"(snd), [ep] "s"(ep), [blk] "s"(j), [t0] "s"(g_t0),
          [ferr] "i"(F_ERR), [fprep] "i"(F_PREP), [fconv] "i"(F_CONV), [fprog] "i"(F_PROG), [dbg] "i"(W_DBG), [oslp] "i"(O_SLP), [xslp] "i"(X_SLP), [xdel] "i"(X_DEL), "{v[176:191]}"(pc)
        : "memory", "vcc", "scc", "s36", "s37", "s38", "s39", "s40", "s41", "s42", "s43", "s44", "s45", "s46", "s47", "s80", "s81", "s82", "s83", "s84", "s85", "s86", "s87", "s88", "s89", "s90", "s91", "s92", "s93", "s94", "s95", "s96", "s97", "s98", "s99", WO_REGS);
}

__device__ __forceinline__ void bulk_block(const Params& p, rsrc_t r, unsigned char* lds, const int wv, const int j, const unsigned tag, const unsigned g_t0) {
    const int x = (j & 7) >> 1, i32 = (j >> 3) * 2 + (j & 1);
    auto flags = [&](int mt, int nmt, int h0, int nh, int mat) {
        DRAIN(); __syncthreads();
        const int tid = otid(); if (tid < nmt * nh) st4(r, F_KVQ + ((mt + tid / nh) * 32 + h0 + tid % nh) * 64 + mat * 4, tag); };
#pragma unroll 1
    for (int t = 0; t < 2; ++t) {
        const int mt = 2 * t + (i32 & 1), mat = (i32 >> 1) & 1, hd = 8 * x + (i32 >> 2), rot = ((i32 >> 1) & 7) * 2;
        const bf16_t* Bm = mat ? p.Wv : p.Wk;
        if (mat) gemm<1, 4, 4, 8>(r, p.Aq, Bm, mt * 128, hd * 128, lds, W_VF, wv, rot, nullptr, t == 1, t == 0 ? Bm : nullptr, (mt + 2) * 128, hd * 128);
        else gemm<0, 4, 4, 8>(r, p.Aq, Bm, mt * 128, hd * 128, lds, W_KX, wv, rot, nullptr, t == 1, t == 0 ? Bm : nullptr, (mt + 2) * 128, hd * 128);
        flags(mt, 1, hd, 1, mat);
    }
    const int hp = 4 * x + (i32 >> 3), q8 = i32 & 7, rot = ROT2 * (i32 >> 2);
    if (SKEW) wait_long(rtnow() + (unsigned)((q8 & 3) * 4 + x) * SKEW);
    {
        const int m = q8 < 4 ? 4 + (q8 & 1) : q8 - 4, mat = q8 < 4 ? q8 >> 1 : 2;
        const bf16_t* Bn = mat == 2 ? p.Wq : mat == 1 ? p.Wv : p.Wk;
        const int mt = 4 + (q8 & 3), mv = q8 >> 2;
        const bf16_t* tk = (wv == 1 && j < 32) ? p.Aq + (64 + j) * 16384 : nullptr;
        if (mv) gemm<1, 4, 8, 4, 0, NoHook, 8>(r, p.Aq, p.Wv, mt * 128, hp * 256, lds, W_VF, wv, rot, tk, false, Bn, m * 256, hp * 256);
        else gemm<0, 4, 8, 4, 0, NoHook, 8>(r, p.Aq, p.Wk, mt * 128, hp * 256, lds, W_KX, wv, rot, tk, false, Bn, m * 256, hp * 256);
        flags(mt, 1, 2 * hp, 2, mv);
        const bf16_t* tl = (wv == 1 && j < 32) ? p.Aq + (96 + j) * 16384 : nullptr;
        if (mat == 1) gemm<1, 8, 8, 4>(r, p.Aq, Bn, m * 256, hp * 256, lds, W_VF, wv, rot, tl, true);
        else gemm<0, 8, 8, 4>(r, p.Aq, Bn, m * 256, hp * 256, lds, mat == 2 ? W_QX : W_KX, wv, rot, tl, true);
        flags(2 * m, 2, 2 * hp, 2, mat);
    }
    if (CVH == 4 && !conv_block(p, r, 128 + j, 1, lds, wv, j, tag, 0u)) return;
    {
        unsigned wi = 0u;
        const int l = otid() & 63; const v4u sw = mk_srd(p.Wo, 4096 * 1024 * 2); const unsigned wa = (unsigned)(((i32 & 1) * 2048 + wv * 512 + (l >> 3)) * 2048 + (i32 >> 1) * 128 + (l & 7) * 16);
        unsigned long long tab;
        asm volatile("s_getpc_b64 %0\n\ts_branch .Lwo%=\n\t"
                     "buffer_load_dwordx4 a[128:131], v191, s[92:95], s91 offen\n\ts_setpc_b64 s[96:97]\n\t" "buffer_load_dwordx4 a[132:135], v191, s[92:95], s91 offen\n\ts_setpc_b64 s[96:97]\n\t"
                     "buffer_load_dwordx4 a[136:139], v191, s[92:95], s91 offen\n\ts_setpc_b64 s[96:97]\n\t" "buffer_load_dwordx4 a[140:143], v191, s[92:95], s91 offen\n\ts_setpc_b64 s[96:97]\n\t"
                     "buffer_load_dwordx4 a[144:147], v191, s[92:95], s91 offen\n\ts_setpc_b64 s[96:97]\n\t" "buffer_load_dwordx4 a[148:151], v191, s[92:95], s91 offen\n\ts_setpc_b64 s[96:97]\n\t"
                     "buffer_load_dwordx4 a[152:155], v191, s[92:95], s91 offen\n\ts_setpc_b64 s[96:97]\n\t" "buffer_load_dwordx4 a[156:159], v191, s[92:95], s91 offen\n\ts_setpc_b64 s[96:97]\n\t"
                     "buffer_load_dwordx4 a[160:163], v191, s[92:95], s91 offen\n\ts_setpc_b64 s[96:97]\n\t" "buffer_load_dwordx4 a[164:167], v191, s[92:95], s91 offen\n\ts_setpc_b64 s[96:97]\n\t"
                     "buffer_load_dwordx4 a[168:171], v191, s[92:95], s91 offen\n\ts_setpc_b64 s[96:97]\n\t" "buffer_load_dwordx4 a[172:175], v191, s[92:95], s91 offen\n\ts_setpc_b64 s[96:97]\n\t"
                     "buffer_load_dwordx4 a[176:179], v191, s[92:95], s91 offen\n\ts_setpc_b64 s[96:97]\n\t" "buffer_load_dwordx4 a[180:183], v191, s[92:95], s91 offen\n\ts_setpc_b64 s[96:97]\n\t"
                     "buffer_load_dwordx4 a[184:187], v191, s[92:95], s91 offen\n\ts_setpc_b64 s[96:97]\n\t" "buffer_load_dwordx4 a[188:191], v191, s[92:95], s91 offen\n\ts_setpc_b64 s[96:97]\n\t"
                     "buffer_load_dwordx4 a[192:195], v191, s[92:95], s91 offen\n\ts_setpc_b64 s[96:97]\n\t" "buffer_load_dwordx4 a[196:199], v191, s[92:95], s91 offen\n\ts_setpc_b64 s[96:97]\n\t"
                     "buffer_load_dwordx4 a[200:203], v191, s[92:95], s91 offen\n\ts_setpc_b64 s[96:97]\n\t" "buffer_load_dwordx4 a[204:207], v191, s[92:95], s91 offen\n\ts_setpc_b64 s[96:97]\n\t"
                     "buffer_load_dwordx4 a[208:211], v191, s[92:95], s91 offen\n\ts_setpc_b64 s[96:97]\n\t" "buffer_load_dwordx4 a[212:215], v191, s[92:95], s91 offen\n\ts_setpc_b64 s[96:97]\n\t"
                     "buffer_load_dwordx4 a[216:219], v191, s[92:95], s91 offen\n\ts_setpc_b64 s[96:97]\n\t" "buffer_load_dwordx4 a[220:223], v191, s[92:95], s91 offen\n\ts_setpc_b64 s[96:97]\n\t"
                     "buffer_load_dwordx4 a[224:227], v191, s[92:95], s91 offen\n\ts_setpc_b64 s[96:97]\n\t" "buffer_load_dwordx4 a[228:231], v191, s[92:95], s91 offen\n\ts_setpc_b64 s[96:97]\n\t"
                     "buffer_load_dwordx4 a[232:235], v191, s[92:95], s91 offen\n\ts_setpc_b64 s[96:97]\n\t" "buffer_load_dwordx4 a[236:239], v191, s[92:95], s91 offen\n\ts_setpc_b64 s[96:97]\n\t"
                     "buffer_load_dwordx4 a[240:243], v191, s[92:95], s91 offen\n\ts_setpc_b64 s[96:97]\n\t" "buffer_load_dwordx4 a[244:247], v191, s[92:95], s91 offen\n\ts_setpc_b64 s[96:97]\n\t"
                     "buffer_load_dwordx4 a[248:251], v191, s[92:95], s91 offen\n\ts_setpc_b64 s[96:97]\n\t" "buffer_load_dwordx4 a[252:255], v191, s[92:95], s91 offen\n\ts_setpc_b64 s[96:97]\n\t"
                     "buffer_load_dwordx4 v[192:195], v191, s[92:95], s91 offen\n\ts_setpc_b64 s[96:97]\n\t" "buffer_load_dwordx4 v[196:199], v191, s[92:95], s91 offen\n\ts_setpc_b64 s[96:97]\n\t"
                     "buffer_load_dwordx4 v[200:203], v191, s[92:95], s91 offen\n\ts_setpc_b64 s[96:97]\n\t" "buffer_load_dwordx4 v[204:207], v191, s[92:95], s91 offen\n\ts_setpc_b64 s[96:97]\n\t"
                     "buffer_load_dwordx4 v[208:211], v191, s[92:95], s91 offen\n\ts_setpc_b64 s[96:97]\n\t" "buffer_load_dwordx4 v[212:215], v191, s[92:95], s91 offen\n\ts_setpc_b64 s[96:97]\n\t"
                     "buffer_load_dwordx4 v[216:219], v191, s[92:95], s91 offen\n\ts_setpc_b64 s[96:97]\n\t" "buffer_load_dwordx4 v[220:223], v191, s[92:95], s91 offen\n\ts_setpc_b64 s[96:97]\n\t"
                     "buffer_load_dwordx4 v[224:227], v191, s[92:95], s91 offen\n\ts_setpc_b64 s[96:97]\n\t" "buffer_load_dwordx4 v[228:231], v191, s[92:95], s91 offen\n\ts_setpc_b64 s[96:97]\n\t"
                     "buffer_load_dwordx4 v[232:235], v191, s[92:95], s91 offen\n\ts_setpc_b64 s[96:97]\n\t" "buffer_load_dwordx4 v[236:239], v191, s[92:95], s91 offen\n\ts_setpc_b64 s[96:97]\n\t"
                     "buffer_load_dwordx4 v[240:243], v191, s[92:95], s91 offen\n\ts_setpc_b64 s[96:97]\n\t" "buffer_load_dwordx4 v[244:247], v191, s[92:95], s91 offen\n\ts_setpc_b64 s[96:97]\n\t"
                     "buffer_load_dwordx4 v[248:251], v191, s[92:95], s91 offen\n\ts_setpc_b64 s[96:97]\n\t" "buffer_load_dwordx4 v[252:255], v191, s[92:95], s91 offen\n\ts_setpc_b64 s[96:97]\n\t"
                     "buffer_load_dwordx4 v191, s[92:95], s91 offen lds\n\ts_setpc_b64 s[96:97]\n.Lwo%=:" : "=s"(tab));
        auto hook = [&](int) {
            const unsigned q = min(wi, 63u);
            asm volatile("s_mov_b32 m0, %1\n\ts_swappc_b64 s[96:97], %0"
                         :: "s"(tab + (min(q, 48u) * 12u + 4u)), "s"(WO_L + wv * 16384 + (q - 48u) * 1024u), "{v191}"(wa), "{s[92:95]}"(sw), "{s91}"(q * 16384u) : "memory", "m0", "s96", "s97", WO_REGS);
            ++wi; };
#pragma unroll 1
        for (int t = 0; t < 2; ++t) {
            const int mt = t ? 8 + q8 : 12 + (q8 & 3), mv = t ? 2 : q8 >> 2;
            if (mv == 1) gemm<1, 4, 8, 4, 1>(r, p.Aq, p.Wv, mt * 128, hp * 256, lds, W_VF, wv, rot, nullptr, false, p.Wq, (8 + q8) * 128, hp * 256, hook);
            else gemm<0, 4, 8, 4, 1>(r, p.Aq, t ? p.Wq : p.Wk, mt * 128, hp * 256, lds, t ? W_QX : W_KX, wv, rot, nullptr, t == 1, t ? nullptr : p.Wq, (8 + q8) * 128, hp * 256, hook);
            flags(mt, 1, 2 * hp, 2, mv);
        }
        oproj_block(p, wv, i32, x, j, tag, g_t0);
    }
}

__device__ __forceinline__ void help_block(const Params& p, rsrc_t r, unsigned char* lds, const int wv, const int k, const unsigned tag, const unsigned far) {
    const int j = -32 + k; (void)j;
    {
        const unsigned th = rtnow() + HELP_D + (unsigned)k * SKEWP, OOB = 0xF0000000u;
        const int l = otid() & 63, n16 = l & 15;
        const rsrc_t rh = __builtin_amdgcn_make_buffer_rsrc(p.ws + hx_base(p.ws), 0, U_END * 16384, 0x00020000);
        const unsigned un = (unsigned)(wv * 32 + (k & 30)) * 16384u + (k & 1) * 4096u, ca = l == 0 ? un : OOB;
        v4u z[4]; unsigned best[4] = {~0u, ~0u, ~0u, ~0u};
#pragma unroll
        for (int i = 0; i < 4; ++i) z[i] = (v4u){~0u, ~0u, ~0u, ~0u};
        for (int i = 0; i < CAL_N; ++i)
#pragma unroll
            for (int q = 0; q < 4; ++q) {
                const unsigned t0 = (unsigned)__builtin_amdgcn_s_memtime(); __builtin_amdgcn_s_waitcnt(0);
                st16(rh, ca + q * 8192, z[0]); DRAIN();
                const unsigned t1 = (unsigned)__builtin_amdgcn_s_memtime(); __builtin_amdgcn_s_waitcnt(0);
                best[q] = min(best[q], t1 - t0); }
        const unsigned up = min(best[1], best[3]) < min(best[0], best[2]) ? 1u : 0u, vote = (up ^ far) | ((up ? best[2] < best[0] : best[3] < best[1]) ? 2u : 0u);
        if (wv == 0 && k < 8) {
#pragma unroll
            for (int i = 0; i < 4; ++i) z[i] = __builtin_elementwise_min(*(const v4u*)(p.mem0 + ((n16 & 1) + 2 * (n16 >> 2)) * 1024 + ((k & 3) * 4 + i) * 64 + (l >> 4) * 16 + (n16 & 2) * 4), (v4u){~1u, ~1u, ~1u, ~1u}); }
#pragma unroll
        for (int i = 0; i < 16; ++i) st16(rh, un + (i >> 2) * 8192 + (i & 3) * 1024 + l * 16, z[i & 3]);
        st4(r, (wv == 3 && l < 3) ? F_PROG + k * 128 + l * 8 : OOB, l ? 63u : 0u);
        DRAIN();
        st8(r, l == 0 ? F_CAL + k * 32 + wv * 8 : OOB, (v2u){tag, vote});
        wait_long(th);
    }
    prep_block(p, r, k, wv, tag);
    if (!conv_block(p, r, k * CVH, CVH, lds, wv, j, tag, (unsigned)k * SKEWH)) return;
    if (ATD) wait_long(rtnow() + ATD);
    attn_block(p, wv, k, tag);
}

__global__ void __launch_bounds__(256, 1) k_main(Params p) {
    extern __shared__ __attribute__((aligned(16))) unsigned char lds[];
    const unsigned g_t0 = DIAG ? rtnow() : 0u;
    rsrc_t r = __builtin_amdgcn_make_buffer_rsrc(p.ws, 0, W_TOTAL, 0x00020000);
    const int wv = __builtin_amdgcn_readfirstlane(threadIdx.x >> 6);
    int role = blockIdx.x;
    unsigned xcc; asm volatile("s_getreg_b32 %0, hwreg(HW_REG_XCC_ID)" : "=s"(xcc)); xcc &= 7u;
    unsigned old = 0u, xr = xcc, rk = blockIdx.x >> 3;
    if (MIXH) { if (xcc == 3u) { xr = 4u + (rk >> 3); rk = 24u + (rk & 7u); } else if (xcc > 3u && rk >= 24u) { xr = 3u; rk = (xcc - 4u) * 8u + (rk & 7u); } }
    unsigned slot = (xr >> 2) * 128u + (rk >> 1) * 8u + (xr & 3u) * 2u + (rk & 1u);
    const unsigned tag = (__builtin_amdgcn_readfirstlane(ld4(r, (threadIdx.x & 63) == 1 ? F_CLM + slot * 64 + 4 : F_EP)) * 2654435761u + 0x9E3779B9u) | 1u;
    if (threadIdx.x == 0) old = __hip_atomic_exchange((unsigned*)(p.ws + F_CLM + slot * 64), tag, __ATOMIC_RELAXED, __HIP_MEMORY_SCOPE_AGENT);
    if (threadIdx.x == 0) {
        for (int i = 0; i < 256 && old == tag; ++i) { slot = (slot + 1u) & 255u; old = __hip_atomic_exchange((unsigned*)(p.ws + F_CLM + slot * 64), tag, __ATOMIC_RELAXED, __HIP_MEMORY_SCOPE_AGENT); }
        *(volatile __attribute__((address_space(3))) int*)(size_t)(unsigned)(size_t)lds = (int)slot;
    }
    __syncthreads();
    role = __builtin_amdgcn_readfirstlane(*(volatile __attribute__((address_space(3))) int*)(size_t)(unsigned)(size_t)lds) & 255;
    __syncthreads();
    if (DIAG >= 10 && blockIdx.x < 8 && threadIdx.x == 0) st4(r, F_PROG + 72 + xcc * 128, g_t0);
    if (role >= 128) bulk_block(p, r, lds, wv, role - 128, tag, g_t0);
    else if ((role & 6) == 6) help_block(p, r, lds, wv, (role >> 3) * 2 + (role & 1), tag, xcc >> 2);
    else rec_block(p, wv, role, tag);
}

extern "C" void kernel_launch(void* const* d_in, const int* in_sizes, int n_in, void* d_out, int out_size, void* d_ws, size_t ws_size, hipStream_t stream) {
    Params p;
    p.Aq = (const bf16_t*)d_in[0]; p.Ak = (const bf16_t*)d_in[1]; p.Av = (const bf16_t*)d_in[2];
    p.shift_k = (const bf16_t*)d_in[3]; p.ssm_k = (const bf16_t*)d_in[4]; p.ssm_D = (const bf16_t*)d_in[5];
    p.mem0 = (const bf16_t*)d_in[7]; p.Wq = (const bf16_t*)d_in[8]; p.Wk = (const bf16_t*)d_in[9];
    p.Wv = (const bf16_t*)d_in[10]; p.Wo = (const bf16_t*)d_in[11]; p.gate_w = (const bf16_t*)d_in[13];
    p.out = (bf16_t*)d_out; p.ws = (unsigned char*)d_ws;
    (void)hipFuncSetAttribute((const void*)k_main, hipFuncAttributeMaxDynamicSharedMemorySize, L_TOTAL);
    k_main<<<256, 256, L_TOTAL, stream>>>(p);
}
```

```cpp
#include <hip/hip_runtime.h>
#include <stdint.h>
#include <type_traits>
typedef unsigned short bf16_t;
using bf16x8 = __attribute__((ext_vector_type(8))) short;
using f32x4 = __attribute__((ext_vector_type(4))) float;
typedef unsigned v4u __attribute__((ext_vector_type(4)));
typedef unsigned v2u __attribute__((ext_vector_type(2)));
typedef __amdgpu_buffer_rsrc_t rsrc_t;

typedef __bf16 hbf2 __attribute__((ext_vector_type(2)));
typedef float f32x2 __attribute__((ext_vector_type(2)));
__device__ __forceinline__ unsigned pack2(float a, float b) { f32x2 v = {a, b}; return __builtin_bit_cast(unsigned, __builtin_convertvector(v, hbf2)); }
__device__ __forceinline__ float rbfh(float f) { return __uint_as_float(pack2(0.f, f)); }
__device__ __forceinline__ f32x2 rbf2(f32x2 f) { return (f32x2){rbfh(f.x), rbfh(f.y)}; }
__device__ __forceinline__ unsigned nosent(unsigned x) {
    typedef unsigned short us2 __attribute__((ext_vector_type(2)));
    const us2 a = __builtin_bit_cast(us2, x), c = {(unsigned short)0xFFFE, (unsigned short)0xFFFE}; return __builtin_bit_cast(unsigned, __builtin_elementwise_min(a, c)); }

constexpr int S = 2048, D = 1024, H = 32, DH = 128, M = 8, SEG = 32, NSEG = 64;

constexpr unsigned W_FLG = 0;
constexpr unsigned F_KVQ = W_FLG;
constexpr unsigned F_EP = W_FLG + 32768;
constexpr unsigned F_ERR = W_FLG + 32768 + 256;
constexpr unsigned F_PREP = W_FLG + 32768 + 512;
constexpr unsigned F_CAL = W_FLG + 73728;
constexpr unsigned F_CLM = W_FLG + 77824;
constexpr unsigned F_CONV = W_FLG + 33792;
constexpr unsigned F_PROG = W_FLG + 36864;
constexpr unsigned F_ATT = W_FLG + 40960;
constexpr unsigned F_RDY = W_FLG + 49152;
constexpr unsigned FLG_BYTES = 94208;
constexpr unsigned HO_KVM = W_FLG + FLG_BYTES;
constexpr unsigned W_GATE = HO_KVM + 64 * 32 * 4096;
constexpr unsigned W_QX = W_GATE + 8192;
constexpr unsigned W_KX = W_QX + S * 4096 * 2;
constexpr unsigned W_VF = W_KX + S * 4096 * 2;
constexpr unsigned DBG_BYTES = 0;
constexpr unsigned W_DBG = W_VF + 64 * 32 * 8192;
constexpr unsigned W_U = W_DBG + DBG_BYTES;
constexpr unsigned W_TOTAL = W_U + S * D * 2;

__device__ __forceinline__ v4u ld16(rsrc_t r, unsigned off) { return __builtin_amdgcn_raw_buffer_load_b128(r, off, 0, 16); }
__device__ __forceinline__ v2u ld8(rsrc_t r, unsigned off) { return __builtin_amdgcn_raw_buffer_load_b64(r, off, 0, 16); }
__device__ __forceinline__ unsigned ld4(rsrc_t r, unsigned off) { return __builtin_amdgcn_raw_buffer_load_b32(r, off, 0, 16); }
__device__ __forceinline__ void st16(rsrc_t r, unsigned off, v4u v) { __builtin_amdgcn_raw_buffer_store_b128(v, r, off, 0, 16); }
__device__ __forceinline__ void st8(rsrc_t r, unsigned off, v2u v) { __builtin_amdgcn_raw_buffer_store_b64(v, r, off, 0, 16); }
__device__ __forceinline__ void st4(rsrc_t r, unsigned off, unsigned v) { __builtin_amdgcn_raw_buffer_store_b32(v, r, off, 0, 16); }
__device__ __forceinline__ bf16x8 as_frag(v4u v) { return __builtin_bit_cast(bf16x8, v); }
#define D1 128
#define D2 82
#define D3 64
#define LB0 16
#define LC0 7
#define LA0 8
#define TR_UP 4
#define TR_DN 2
#define L_DEC 15
#define STAG 1
#define UP_SH 2
#define EARLY_FROM 58
#define OUT_WARM 0
#define AROT 0
#define P_SLP 2
#define O_SLP 1
#define W_NT 0
#define TOL 2
#define A_SLP 0
#define PEN2 20
#define A_UP 6
#define CT0 2
#define CPF 1
#define PH_M 3
#define SKEW 20
#define SKEW1 0
#define XT 1
#define TRK 3
#define WO_NT 0
#define WO_TRK 1
#define WIN 3
#define ROT0 3
#define ROT1 2
#define ROT2 2
#define B_NT 0
#define CALM 15
#define R4M 1
#define HX_SH 0u
#define FINE_T 1
#define FINE_N 0
#define FINE_A 0
#define O_ND 12
#define PEN0 32
#define X0C 24
#define X0A 20
#define X1A 8
#define PEN 40
#define MEMBAR() asm volatile("" ::: "memory")
#define ENDPGM() asm volatile("s_endpgm" ::: "memory")
#define DRAIN() asm volatile("s_waitcnt vmcnt(0)" ::: "memory")

__device__ __forceinline__ float xor16_max(float x) { auto r = __builtin_amdgcn_permlane16_swap(__float_as_uint(x), __float_as_uint(x), false, false); return fmaxf(__uint_as_float(r[0]), __uint_as_float(r[1])); }
__device__ __forceinline__ float xor32_max(float x) { auto r = __builtin_amdgcn_permlane32_swap(__float_as_uint(x), __float_as_uint(x), false, false); return fmaxf(__uint_as_float(r[0]), __uint_as_float(r[1])); }
__device__ __forceinline__ float xor16_add(float x) { auto r = __builtin_amdgcn_permlane16_swap(__float_as_uint(x), __float_as_uint(x), false, false); return __uint_as_float(r[0]) + __uint_as_float(r[1]); }
__device__ __forceinline__ float xor32_add(float x) { auto r = __builtin_amdgcn_permlane32_swap(__float_as_uint(x), __float_as_uint(x), false, false); return __uint_as_float(r[0]) + __uint_as_float(r[1]); }

typedef short v4i16_t __attribute__((ext_vector_type(4)));
__device__ __forceinline__ v2u trb(unsigned lds_addr) { return __builtin_bit_cast(v2u, __builtin_amdgcn_ds_read_tr16_b64_v4i16((__attribute__((address_space(3))) v4i16_t*)(size_t)lds_addr)); }
__device__ __forceinline__ int otid_(int wv) { int t; asm volatile("v_mbcnt_lo_u32_b32 %0, -1, 0\n\tv_mbcnt_hi_u32_b32 %0, -1, %0" : "=v"(t)); return wv * 64 + t; }
#define otid() otid_(wv)
__device__ __forceinline__ bool wait_word(rsrc_t r, unsigned off, unsigned want, unsigned code) {
    for (unsigned spin = 0;; ++spin) { MEMBAR();
        const unsigned v = __builtin_amdgcn_readfirstlane(ld4(r, off));
        if (v >= want) return true;
        if (spin > (1u << 20)) { st4(r, F_ERR, code); return false; }
        __builtin_amdgcn_s_sleep(8); }
}

__device__ __forceinline__ bool wait_prep(rsrc_t r, int l, unsigned tag) {
    for (unsigned spin = 0;; ++spin) { MEMBAR();
        const unsigned v = ld4(r, F_PREP + (l & 31) * 4);
        if (__all(v == tag)) return true;
        if (spin > (1u << 20)) { st4(r, F_ERR, 512u); return false; }
        __builtin_amdgcn_s_sleep(8); }
}
__device__ __forceinline__ unsigned rtnow() { return (unsigned)__builtin_amdgcn_s_memrealtime(); }
__device__ __forceinline__ void wait_long(unsigned target) { for (int i = 0; i < 4096; ++i) { if ((int)(rtnow() - target) >= 0) break; __builtin_amdgcn_s_sleep(8); } }
constexpr int L_TAB = 20480;
constexpr int L_QK = 24576;
__device__ __forceinline__ unsigned img(int f) { return (unsigned)(L_QK + f * 16 + (f >> 4) * 128); }
constexpr int L_FL = 30720, L_Z = L_FL + 128, L_LOG = 31744, L_W = 32768, L_V = 131072;
constexpr int L_TOTAL = 163840;
constexpr unsigned U_END = 96;
#define PMIN 82
#define V_SLP 2
#define R_SLP 0
constexpr unsigned WO_L = 98304;
#define HELP_D 200
#define V_DEL 24
#define XR 8
#define PFW 0
#define KEYM 3
#define DIAG 0
#define NEARX 0
#define MIXH 1
#define SKEWH 10
#define SKEWP 8
#define NEARY 0
#define CS1 0x8000u
#define CS2 0x10000u
#define X_SLP 2
#define X_DEL 10
#define FASTP 0
#define CV1 3
#define AT1 7
#define CVH 8
#define DN_SH 1
#define EPI_SLP 0
#define AT_SLP 18
#define VA_SLP 0
#define C_SLP 0
#define ATD 0
#define S0B 40
#define BT_SLP 0
#define L_SLP 10
#define Y_DEL 6
#ifndef M_T
#define M_T 16
#endif
#ifndef M_0
#define M_0 184
#endif
#define CAL_O 0u
#define H_SLP 0
#define CAL_N 3
#define STEP_MIN 180u

struct Params {
    const bf16_t *Aq, *Ak, *Av, *shift_k, *ssm_k, *ssm_D, *mem0, *Wq, *Wk, *Wv, *Wo, *gate_w;
    bf16_t* out; unsigned char* ws;
};
__host__ __device__ __forceinline__ unsigned hx_base(const void* ws) { const size_t a = (size_t)ws; return (unsigned)((((a + W_TOTAL + 0x7FFFFFu) >> 23) << 23) - a) + HX_SH; }

typedef short v4s __attribute__((ext_vector_type(4)));
typedef unsigned v32u __attribute__((ext_vector_type(32)));
typedef unsigned v16u __attribute__((ext_vector_type(16)));
typedef unsigned v8u __attribute__((ext_vector_type(8)));
typedef unsigned v12u __attribute__((ext_vector_type(12)));
__device__ __forceinline__ v4u mk_srd(const void* p, unsigned bytes) { const size_t a = (size_t)p; v4u d;
    d.x = __builtin_amdgcn_readfirstlane((unsigned)a); d.y = __builtin_amdgcn_readfirstlane((unsigned)(a >> 32) & 0xFFFFu); d.z = __builtin_amdgcn_readfirstlane(bytes); d.w = 0x00020000u; return d; }
__device__ __forceinline__ bf16x8 ldsa(unsigned addr) { return *(const __attribute__((address_space(3))) bf16x8*)(size_t)addr; }
__device__ __forceinline__ void rec_block(const Params& p, const int wv, const int b, const unsigned tag) {
    const int w = wv, tid = otid(), l = tid & 63, n16 = l & 15, g = l >> 4, c = n16 >> 2;
    const int x = (b & 7) >> 1, i32 = (b >> 3) * 2 + (b & 1);
    const unsigned OOB = 0xF0000000u;
    const v4u rhd = mk_srd(p.ws + hx_base(p.ws), U_END * 16384), wsd = mk_srd(p.ws, W_TOTAL);
    const int G = x - 1, vi = G * 32 + i32, h = x == 0 ? i32 : vi >> 1;
    const unsigned pr_a = F_PROG + h * 128, cal = F_CAL - w * 1024, kvo = HO_KVM + h * 4096 + (x ? 2048 : 0);
    if (CPF) {
        unsigned lo, hi, l2; asm volatile("s_getpc_b64 s[98:99]\n.Lpc%=:\n\ts_add_u32 %2, s98, rec_v_code-.Lpc%=\n\ts_add_u32 %0, s98, rec_s_code-.Lpc%=\n\ts_addc_u32 %1, s99, 0" : "=s"(lo), "=s"(hi), "=s"(l2) :: "s98", "s99", "scc");
        const size_t a = (((size_t)hi << 32) | lo) + (size_t)(long long)((x ? (int)(l2 - lo) : 0) + ((i32 * 4 + w) % 12) * 1024 - 4096 + l * 16);
        asm volatile("s_mov_b32 m0, %1\n\ts_nop 0\n\tglobal_load_lds_dwordx4 %0, off" :: "v"(a & ~(size_t)15), "s"(x ? L_V + 16384 : 0) : "memory", "m0");
    }
    typedef __attribute__((address_space(3))) volatile unsigned lu32;
    auto cpy = [](int k) -> unsigned { return k == 1 ? 0u : k * 32768u + 16384u; };
    auto cun = [](int u) -> unsigned { return ((u & 1) ? CS1 : 0u) ^ ((u & 2) ? CS2 : 0u); };
    unsigned nA = cpy(x), nW = cpy(w), nT = 64 * 16384 + (x ? 0 : h * 8), nS = 64 * 16384 + x * 1024, nC = 32 * 16384 + (x == 2 ? 16384 : 0), b0 = 65 * 16384 + (h >> 2) * 32768 + (h & 3) * 2048;
    unsigned pob = w == 0 ? nA + (i32 >> 4) * 32768 + (i32 & 15) * 512 : (w == 1 && x) ? (nC ^ cun(i32 >> 3)) + (i32 & 7) * 1024 : (w == 2 && i32 == 0) ? nS : OOB;
    if (x == 0) {
        const v4u sq = mk_srd(p.Wq, 1024 * 4096 * 2), sk = mk_srd(p.Wk, 1024 * 4096 * 2);
        const unsigned m0b = L_W + w * 1024, f = (c & 1) | ((g & 1) << 1) | ((c >> 1) << 2);
        const unsigned t0 = L_W + (16 * (g >> 1) + 4 * (g & 1) + (c & 1) + 8 * (c >> 1)) * 256 + (((2 * w) ^ f) << 5) + (n16 & 3) * 8;
        const unsigned dv = (4 * w + g) * 8192 + h * 256 + ((n16 ^ (((g & 1) | (w << 1)) << 1)) << 4);
        v8u pa;
#pragma unroll
        for (int q = 0; q < 8; ++q) pa[q] = (t0 + ((w + (q >> 1)) & 3) * 32768) ^ ((q & 1) << 5);
        const unsigned kx = W_KX + h * 8192, vx = W_VF + h * 8192 + w * 1024, kf = F_KVQ + h * 64;
        const unsigned aq = img(8 * g + (n16 >> 2)) + (n16 & 1) * 8;
        const v8u pc = {(unsigned)tid * 16, (unsigned)(L_W + w * 32768 + l * 16), (n16 & 2) ? 0xEEEEu : 0x4444u, aq,
                  (unsigned)(((w + 1) & 3) * 1024 + l * 16), (unsigned)(((w + 2) & 3) * 1024 + l * 16), (unsigned)(((w + 3) & 3) * 1024 + l * 16), img(32 * w + n16) + g * 4};
        const v2u pd = {(n16 & 2) ? (unsigned)L_Z : aq + w * 768, (n16 & 2) ? aq + w * 768 : (unsigned)L_Z};
        *(__attribute__((address_space(3))) v4u*)(size_t)(L_TAB + tid * 16) = (v4u){(unsigned)(L_QK + 3072 + g * 384 + n16 * 8),
                  n16 < 8 ? (unsigned)((((n16 >> 2) * 16384) ^ cun((h >> 1) & 3)) + ((h & 1) * 4 + (h >> 3)) * 1024 + w * 256 + (g >> 1) * 128 + (n16 & 2) * 32 + (g & 1) * 32 + (n16 & 1) * 16) : OOB,
                  (unsigned)((32 * w + 16 * (g & 1) + n16) * 16 + (g >> 1) * 8), (unsigned)(L_QK + 3072 + w * 768 + ((n16 & 1) + 2 * (n16 >> 2)) * 32 + g * 8 + (n16 & 2) * 2)};
        if (tid < 64) ((lu32*)(size_t)L_FL)[tid] = 0u;
    asm volatile(
        "s_add_u32 s80, %[m0b], 0\n\t"
        "s_mov_b32 s81, 0\n\t"
        "s_mov_b32 s82, 4\n\t"
        ".Ld0%=:\n\t"
        "s_mov_b32 m0, s80\n\t"
        "s_add_u32 s80, s80, 0x8000\n\t"
        "buffer_load_dwordx4 v96, %[sq], s81 offen lds\n\t"
        "s_add_u32 m0, m0, 0x1000\n\t"
        "s_add_u32 s81, s81, 0x20000\n\t"
        "buffer_load_dwordx4 v96, %[sq], s81 offen lds\n\t"
        "s_add_u32 m0, m0, 0x1000\n\t"
        "s_add_u32 s81, s81, 0x20000\n\t"
        "buffer_load_dwordx4 v96, %[sq], s81 offen lds\n\t"
        "s_add_u32 m0, m0, 0x1000\n\t"
        "s_add_u32 s81, s81, 0x20000\n\t"
        "buffer_load_dwordx4 v96, %[sq], s81 offen lds\n\t"
        "s_add_u32 m0, m0, 0x1000\n\t"
        "s_add_u32 s81, s81, 0x20000\n\t"
        "s_sub_u32 s82, s82, 1\n\t"
        "s_cmp_lg_u32 s82, 0\n\t"
        "s_cbranch_scc1 .Ld0%=\n\t"
        "s_add_u32 s80, %[m0b], 16384\n\t"
        "s_mov_b32 s81, 0\n\t"
        "s_mov_b32 s82, 4\n\t"
        ".Ld1%=:\n\t"
        "s_mov_b32 m0, s80\n\t"
        "s_add_u32 s80, s80, 0x8000\n\t"
        "buffer_load_dwordx4 v96, %[sk], s81 offen lds\n\t"
        "s_add_u32 m0, m0, 0x1000\n\t"
        "s_add_u32 s81, s81, 0x20000\n\t"
        "buffer_load_dwordx4 v96, %[sk], s81 offen lds\n\t"
        "s_add_u32 m0, m0, 0x1000\n\t"
        "s_add_u32 s81, s81, 0x20000\n\t"
        "buffer_load_dwordx4 v96, %[sk], s81 offen lds\n\t"
        "s_add_u32 m0, m0, 0x1000\n\t"
        "s_add_u32 s81, s81, 0x20000\n\t"
        "buffer_load_dwordx4 v96, %[sk], s81 offen lds\n\t"
        "s_add_u32 m0, m0, 0x1000\n\t"
        "s_add_u32 s81, s81, 0x20000\n\t"
        "s_sub_u32 s82, s82, 1\n\t"
        "s_cmp_lg_u32 s82, 0\n\t"
        "s_cbranch_scc1 .Ld1%=\n\t"
        "s_waitcnt vmcnt(16)\n\t"
        "s_barrier\n\t"
        "ds_read_b64_tr_b16 v[0:1], v98 offset:0\n\t"
        "ds_read_b64_tr_b16 v[2:3], v98 offset:512\n\t"
        "ds_read_b64_tr_b16 v[4:5], v98 offset:8192\n\t"
        "ds_read_b64_tr_b16 v[6:7], v98 offset:8704\n\t"
        "ds_read_b64_tr_b16 v[8:9], v99 offset:0\n\t"
        "ds_read_b64_tr_b16 v[10:11], v99 offset:512\n\t"
        "ds_read_b64_tr_b16 v[12:13], v99 offset:8192\n\t"
        "ds_read_b64_tr_b16 v[14:15], v99 offset:8704\n\t"
        "ds_read_b64_tr_b16 a[16:17], v100 offset:0\n\t"
        "ds_read_b64_tr_b16 a[18:19], v100 offset:512\n\t"
        "ds_read_b64_tr_b16 a[20:21], v100 offset:8192\n\t"
        "ds_read_b64_tr_b16 a[22:23], v100 offset:8704\n\t"
        "ds_read_b64_tr_b16 a[24:25], v101 offset:0\n\t"
        "ds_read_b64_tr_b16 a[26:27], v101 offset:512\n\t"
        "ds_read_b64_tr_b16 a[28:29], v101 offset:8192\n\t"
        "ds_read_b64_tr_b16 a[30:31], v101 offset:8704\n\t"
        "ds_read_b64_tr_b16 a[32:33], v102 offset:0\n\t"
        "ds_read_b64_tr_b16 a[34:35], v102 offset:512\n\t"
        "ds_read_b64_tr_b16 a[36:37], v102 offset:8192\n\t"
        "ds_read_b64_tr_b16 a[38:39], v102 offset:8704\n\t"
        "ds_read_b64_tr_b16 a[40:41], v103 offset:0\n\t"
        "ds_read_b64_tr_b16 a[42:43], v103 offset:512\n\t"
        "ds_read_b64_tr_b16 a[44:45], v103 offset:8192\n\t"
        "ds_read_b64_tr_b16 a[46:47], v103 offset:8704\n\t"
        "ds_read_b64_tr_b16 a[48:49], v104 offset:0\n\t"
        "ds_read_b64_tr_b16 a[50:51], v104 offset:512\n\t"
        "ds_read_b64_tr_b16 a[52:53], v104 offset:8192\n\t"
        "ds_read_b64_tr_b16 a[54:55], v104 offset:8704\n\t"
        "ds_read_b64_tr_b16 a[56:57], v105 offset:0\n\t"
        "ds_read_b64_tr_b16 a[58:59], v105 offset:512\n\t"
        "ds_read_b64_tr_b16 a[60:61], v105 offset:8192\n\t"
        "ds_read_b64_tr_b16 a[62:63], v105 offset:8704\n\t"
        "s_waitcnt lgkmcnt(0)\n\t"
        "s_barrier\n\t"
        "s_add_u32 s80, %[m0b], 0\n\t"
        "s_mov_b32 s81, 2097152\n\t"
        "s_mov_b32 s82, 4\n\t"
        ".Ld2%=:\n\t"
        "s_mov_b32 m0, s80\n\t"
        "s_add_u32 s80, s80, 0x8000\n\t"
        "buffer_load_dwordx4 v96, %[sq], s81 offen lds\n\t"
        "s_add_u32 m0, m0, 0x1000\n\t"
        "s_add_u32 s81, s81, 0x20000\n\t"
        "buffer_load_dwordx4 v96, %[sq], s81 offen lds\n\t"
        "s_add_u32 m0, m0, 0x1000\n\t"
        "s_add_u32 s81, s81, 0x20000\n\t"
        "buffer_load_dwordx4 v96, %[sq], s81 offen lds\n\t"
        "s_add_u32 m0, m0, 0x1000\n\t"
        "s_add_u32 s81, s81, 0x20000\n\t"
        "buffer_load_dwordx4 v96, %[sq], s81 offen lds\n\t"
        "s_add_u32 m0, m0, 0x1000\n\t"
        "s_add_u32 s81, s81, 0x20000\n\t"
        "s_sub_u32 s82, s82, 1\n\t"
        "s_cmp_lg_u32 s82, 0\n\t"
        "s_cbranch_scc1 .Ld2%=\n\t"
        "s_waitcnt vmcnt(16)\n\t"
        "s_barrier\n\t"
        "ds_read_b64_tr_b16 v[16:17], v98 offset:16384\n\t"
        "ds_read_b64_tr_b16 v[18:19], v98 offset:16896\n\t"
        "ds_read_b64_tr_b16 v[20:21], v98 offset:24576\n\t"
        "ds_read_b64_tr_b16 v[22:23], v98 offset:25088\n\t"
        "ds_read_b64_tr_b16 v[24:25], v99 offset:16384\n\t"
        "ds_read_b64_tr_b16 v[26:27], v99 offset:16896\n\t"
        "ds_read_b64_tr_b16 v[28:29], v99 offset:24576\n\t"
        "ds_read_b64_tr_b16 v[30:31], v99 offset:25088\n\t"
        "ds_read_b64_tr_b16 a[80:81], v100 offset:16384\n\t"
        "ds_read_b64_tr_b16 a[82:83], v100 offset:16896\n\t"
        "ds_read_b64_tr_b16 a[84:85], v100 offset:24576\n\t"
        "ds_read_b64_tr_b16 a[86:87], v100 offset:25088\n\t"
        "ds_read_b64_tr_b16 a[88:89], v101 offset:16384\n\t"
        "ds_read_b64_tr_b16 a[90:91], v101 offset:16896\n\t"
        "ds_read_b64_tr_b16 a[92:93], v101 offset:24576\n\t"
        "ds_read_b64_tr_b16 a[94:95], v101 offset:25088\n\t"
        "ds_read_b64_tr_b16 a[96:97], v102 offset:16384\n\t"
        "ds_read_b64_tr_b16 a[98:99], v102 offset:16896\n\t"
        "ds_read_b64_tr_b16 a[100:101], v102 offset:24576\n\t"
        "ds_read_b64_tr_b16 a[102:103], v102 offset:25088\n\t"
        "ds_read_b64_tr_b16 a[104:105], v103 offset:16384\n\t"
        "ds_read_b64_tr_b16 a[106:107], v103 offset:16896\n\t"
        "ds_read_b64_tr_b16 a[108:109], v103 offset:24576\n\t"
        "ds_read_b64_tr_b16 a[110:111], v103 offset:25088\n\t"
        "ds_read_b64_tr_b16 a[112:113], v104 offset:16384\n\t"
        "ds_read_b64_tr_b16 a[114:115], v104 offset:16896\n\t"
        "ds_read_b64_tr_b16 a[116:117], v104 offset:24576\n\t"
        "ds_read_b64_tr_b16 a[118:119], v104 offset:25088\n\t"
        "ds_read_b64_tr_b16 a[120:121], v105 offset:16384\n\t"
        "ds_read_b64_tr_b16 a[122:123], v105 offset:16896\n\t"
        "ds_read_b64_tr_b16 a[124:125], v105 offset:24576\n\t"
        "ds_read_b64_tr_b16 a[126:127], v105 offset:25088\n\t"
        "s_waitcnt lgkmcnt(0)\n\t"
        "s_barrier\n\t"
        "s_add_u32 s80, %[m0b], 16384\n\t"
        "s_mov_b32 s81, 2097152\n\t"
        "s_mov_b32 s82, 4\n\t"
        ".Ld3%=:\n\t"
        "s_mov_b32 m0, s80\n\t"
        "s_add_u32 s80, s80, 0x8000\n\t"
        "buffer_load_dwordx4 v96, %[sk], s81 offen lds\n\t"
        "s_add_u32 m0, m0, 0x1000\n\t"
        "s_add_u32 s81, s81, 0x20000\n\t"
        "buffer_load_dwordx4 v96, %[sk], s81 offen lds\n\t"
        "s_add_u32 m0, m0, 0x1000\n\t"
        "s_add_u32 s81, s81, 0x20000\n\t"
        "buffer_load_dwordx4 v96, %[sk], s81 offen lds\n\t"
        "s_add_u32 m0, m0, 0x1000\n\t"
        "s_add_u32 s81, s81, 0x20000\n\t"
        "buffer_load_dwordx4 v96, %[sk], s81 offen lds\n\t"
        "s_add_u32 m0, m0, 0x1000\n\t"
        "s_add_u32 s81, s81, 0x20000\n\t"
        "s_sub_u32 s82, s82, 1\n\t"
        "s_cmp_lg_u32 s82, 0\n\t"
        "s_cbranch_scc1 .Ld3%=\n\t"
        "s_waitcnt vmcnt(16)\n\t"
        "s_barrier\n\t"
        "ds_read_b64_tr_b16 v[32:33], v98 offset:0\n\t"
        "ds_read_b64_tr_b16 v[34:35], v98 offset:512\n\t"
        "ds_read_b64_tr_b16 v[36:37], v98 offset:8192\n\t"
        "ds_read_b64_tr_b16 v[38:39], v98 offset:8704\n\t"
        "ds_read_b64_tr_b16 v[40:41], v99 offset:0\n\t"
        "ds_read_b64_tr_b16 v[42:43], v99 offset:512\n\t"
        "ds_read_b64_tr_b16 v[44:45], v99 offset:8192\n\t"
        "ds_read_b64_tr_b16 v[46:47], v99 offset:8704\n\t"
        "ds_read_b64_tr_b16 a[144:145], v100 offset:0\n\t"
        "ds_read_b64_tr_b16 a[146:147], v100 offset:512\n\t"
        "ds_read_b64_tr_b16 a[148:149], v100 offset:8192\n\t"
        "ds_read_b64_tr_b16 a[150:151], v100 offset:8704\n\t"
        "ds_read_b64_tr_b16 a[152:153], v101 offset:0\n\t"
        "ds_read_b64_tr_b16 a[154:155], v101 offset:512\n\t"
        "ds_read_b64_tr_b16 a[156:157], v101 offset:8192\n\t"
        "ds_read_b64_tr_b16 a[158:159], v101 offset:8704\n\t"
        "ds_read_b64_tr_b16 a[160:161], v102 offset:0\n\t"
        "ds_read_b64_tr_b16 a[162:163], v102 offset:512\n\t"
        "ds_read_b64_tr_b16 a[164:165], v102 offset:8192\n\t"
        "ds_read_b64_tr_b16 a[166:167], v102 offset:8704\n\t"
        "ds_read_b64_tr_b16 a[168:169], v103 offset:0\n\t"
        "ds_read_b64_tr_b16 a[170:171], v103 offset:512\n\t"
        "ds_read_b64_tr_b16 a[172:173], v103 offset:8192\n\t"
        "ds_read_b64_tr_b16 a[174:175], v103 offset:8704\n\t"
        "ds_read_b64_tr_b16 a[176:177], v104 offset:0\n\t"
        "ds_read_b64_tr_b16 a[178:179], v104 offset:512\n\t"
        "ds_read_b64_tr_b16 a[180:181], v104 offset:8192\n\t"
        "ds_read_b64_tr_b16 a[182:183], v104 offset:8704\n\t"
        "ds_read_b64_tr_b16 a[184:185], v105 offset:0\n\t"
        "ds_read_b64_tr_b16 a[186:187], v105 offset:512\n\t"
        "ds_read_b64_tr_b16 a[188:189], v105 offset:8192\n\t"
        "ds_read_b64_tr_b16 a[190:191], v105 offset:8704\n\t"
        "s_waitcnt lgkmcnt(0)\n\t"
        "s_barrier\n\t"
        "s_add_u32 s80, %[m0b], 0\n\t"
        "s_mov_b32 s81, 4194304\n\t"
        "s_mov_b32 s82, 4\n\t"
        ".Ld4%=:\n\t"
        "s_mov_b32 m0, s80\n\t"
        "s_add_u32 s80, s80, 0x8000\n\t"
        "buffer_load_dwordx4 v96, %[sq], s81 offen lds\n\t"
        "s_add_u32 m0, m0, 0x1000\n\t"
        "s_add_u32 s81, s81, 0x20000\n\t"
        "buffer_load_dwordx4 v96, %[sq], s81 offen lds\n\t"
        "s_add_u32 m0, m0, 0x1000\n\t"
        "s_add_u32 s81, s81, 0x20000\n\t"
        "buffer_load_dwordx4 v96, %[sq], s81 offen lds\n\t"
        "s_add_u32 m0, m0, 0x1000\n\t"
        "s_add_u32 s81, s81, 0x20000\n\t"
        "buffer_load_dwordx4 v96, %[sq], s81 offen lds\n\t"
        "s_add_u32 m0, m0, 0x1000\n\t"
        "s_add_u32 s81, s81, 0x20000\n\t"
        "s_sub_u32 s82, s82, 1\n\t"
        "s_cmp_lg_u32 s82, 0\n\t"
        "s_cbranch_scc1 .Ld4%=\n\t"
        "s_mov_b64 exec, 0\n\t"
        "s_branch .Lhk0%=\n\t"
        ".Lhr%=:\n\t"
        "s_mov_b64 exec, -1\n\t"
        "s_waitcnt vmcnt(16)\n\t"
        "s_barrier\n\t"
        "ds_read_b64_tr_b16 v[48:49], v98 offset:16384\n\t"
        "ds_read_b64_tr_b16 v[50:51], v98 offset:16896\n\t"
        "ds_read_b64_tr_b16 v[52:53], v98 offset:24576\n\t"
        "ds_read_b64_tr_b16 v[54:55], v98 offset:25088\n\t"
        "ds_read_b64_tr_b16 v[56:57], v99 offset:16384\n\t"
        "ds_read_b64_tr_b16 v[58:59], v99 offset:16896\n\t"
        "ds_read_b64_tr_b16 v[60:61], v99 offset:24576\n\t"
        "ds_read_b64_tr_b16 v[62:63], v99 offset:25088\n\t"
        "ds_read_b64_tr_b16 a[208:209], v100 offset:16384\n\t"
        "ds_read_b64_tr_b16 a[210:211], v100 offset:16896\n\t"
        "ds_read_b64_tr_b16 a[212:213], v100 offset:24576\n\t"
        "ds_read_b64_tr_b16 a[214:215], v100 offset:25088\n\t"
        "ds_read_b64_tr_b16 a[216:217], v101 offset:16384\n\t"
        "ds_read_b64_tr_b16 a[218:219], v101 offset:16896\n\t"
        "ds_read_b64_tr_b16 a[220:221], v101 offset:24576\n\t"
        "ds_read_b64_tr_b16 a[222:223], v101 offset:25088\n\t"
        "ds_read_b64_tr_b16 a[224:225], v102 offset:16384\n\t"
        "ds_read_b64_tr_b16 a[226:227], v102 offset:16896\n\t"
        "ds_read_b64_tr_b16 a[228:229], v102 offset:24576\n\t"
        "ds_read_b64_tr_b16 a[230:231], v102 offset:25088\n\t"
        "ds_read_b64_tr_b16 a[232:233], v103 offset:16384\n\t"
        "ds_read_b64_tr_b16 a[234:235], v103 offset:16896\n\t"
        "ds_read_b64_tr_b16 a[236:237], v103 offset:24576\n\t"
        "ds_read_b64_tr_b16 a[238:239], v103 offset:25088\n\t"
        "ds_read_b64_tr_b16 a[240:241], v104 offset:16384\n\t"
        "ds_read_b64_tr_b16 a[242:243], v104 offset:16896\n\t"
        "ds_read_b64_tr_b16 a[244:245], v104 offset:24576\n\t"
        "ds_read_b64_tr_b16 a[246:247], v104 offset:25088\n\t"
        "ds_read_b64_tr_b16 a[248:249], v105 offset:16384\n\t"
        "ds_read_b64_tr_b16 a[250:251], v105 offset:16896\n\t"
        "ds_read_b64_tr_b16 a[252:253], v105 offset:24576\n\t"
        "ds_read_b64_tr_b16 a[254:255], v105 offset:25088\n\t"
        "s_waitcnt lgkmcnt(0)\n\t"
        "s_barrier\n\t"
        "s_add_u32 s80, %[m0b], 16384\n\t"
        "s_mov_b32 s81, 4194304\n\t"
        "s_mov_b32 s82, 4\n\t"
        ".Ld5%=:\n\t"
        "s_mov_b32 m0, s80\n\t"
        "s_add_u32 s80, s80, 0x8000\n\t"
        "buffer_load_dwordx4 v96, %[sk], s81 offen lds\n\t"
        "s_add_u32 m0, m0, 0x1000\n\t"
        "s_add_u32 s81, s81, 0x20000\n\t"
        "buffer_load_dwordx4 v96, %[sk], s81 offen lds\n\t"
        "s_add_u32 m0, m0, 0x1000\n\t"
        "s_add_u32 s81, s81, 0x20000\n\t"
        "buffer_load_dwordx4 v96, %[sk], s81 offen lds\n\t"
        "s_add_u32 m0, m0, 0x1000\n\t"
        "s_add_u32 s81, s81, 0x20000\n\t"
        "buffer_load_dwordx4 v96, %[sk], s81 offen lds\n\t"
        "s_add_u32 m0, m0, 0x1000\n\t"
        "s_add_u32 s81, s81, 0x20000\n\t"
        "s_sub_u32 s82, s82, 1\n\t"
        "s_cmp_lg_u32 s82, 0\n\t"
        "s_cbranch_scc1 .Ld5%=\n\t"
        "s_waitcnt vmcnt(16)\n\t"
        "s_barrier\n\t"
        "ds_read_b64_tr_b16 v[128:129], v98 offset:0\n\t"
        "ds_read_b64_tr_b16 v[130:131], v98 offset:512\n\t"
        "ds_read_b64_tr_b16 v[132:133], v98 offset:8192\n\t"
        "ds_read_b64_tr_b16 v[134:135], v98 offset:8704\n\t"
        "ds_read_b64_tr_b16 v[136:137], v99 offset:0\n\t"
        "ds_read_b64_tr_b16 v[138:139], v99 offset:512\n\t"
        "ds_read_b64_tr_b16 v[140:141], v99 offset:8192\n\t"
        "ds_read_b64_tr_b16 v[142:143], v99 offset:8704\n\t"
        "ds_read_b64_tr_b16 v[144:145], v100 offset:0\n\t"
        "ds_read_b64_tr_b16 v[146:147], v100 offset:512\n\t"
        "ds_read_b64_tr_b16 v[148:149], v100 offset:8192\n\t"
        "ds_read_b64_tr_b16 v[150:151], v100 offset:8704\n\t"
        "ds_read_b64_tr_b16 v[152:153], v101 offset:0\n\t"
        "ds_read_b64_tr_b16 v[154:155], v101 offset:512\n\t"
        "ds_read_b64_tr_b16 v[156:157], v101 offset:8192\n\t"
        "ds_read_b64_tr_b16 v[158:159], v101 offset:8704\n\t"
        "ds_read_b64_tr_b16 v[160:161], v102 offset:0\n\t"
        "ds_read_b64_tr_b16 v[162:163], v102 offset:512\n\t"
        "ds_read_b64_tr_b16 v[164:165], v102 offset:8192\n\t"
        "ds_read_b64_tr_b16 v[166:167], v102 offset:8704\n\t"
        "ds_read_b64_tr_b16 v[168:169], v103 offset:0\n\t"
        "ds_read_b64_tr_b16 v[170:171], v103 offset:512\n\t"
        "ds_read_b64_tr_b16 v[172:173], v103 offset:8192\n\t"
        "ds_read_b64_tr_b16 v[174:175], v103 offset:8704\n\t"
        "ds_read_b64_tr_b16 v[176:177], v104 offset:0\n\t"
        "ds_read_b64_tr_b16 v[178:179], v104 offset:512\n\t"
        "ds_read_b64_tr_b16 v[180:181], v104 offset:8192\n\t"
        "ds_read_b64_tr_b16 v[182:183], v104 offset:8704\n\t"
        "ds_read_b64_tr_b16 v[184:185], v105 offset:0\n\t"
        "ds_read_b64_tr_b16 v[186:187], v105 offset:512\n\t"
        "ds_read_b64_tr_b16 v[188:189], v105 offset:8192\n\t"
        "ds_read_b64_tr_b16 v[190:191], v105 offset:8704\n\t"
        "s_waitcnt lgkmcnt(0)\n\t"
        "s_barrier\n\t"
        "s_add_u32 s80, %[m0b], 0\n\t"
        "s_mov_b32 s81, 6291456\n\t"
        "s_mov_b32 s82, 4\n\t"
        ".Ld6%=:\n\t"
        "s_mov_b32 m0, s80\n\t"
        "s_add_u32 s80, s80, 0x8000\n\t"
        "buffer_load_dwordx4 v96, %[sq], s81 offen lds\n\t"
        "s_add_u32 m0, m0, 0x1000\n\t"
        "s_add_u32 s81, s81, 0x20000\n\t"
        "buffer_load_dwordx4 v96, %[sq], s81 offen lds\n\t"
        "s_add_u32 m0, m0, 0x1000\n\t"
        "s_add_u32 s81, s81, 0x20000\n\t"
        "buffer_load_dwordx4 v96, %[sq], s81 offen lds\n\t"
        "s_add_u32 m0, m0, 0x1000\n\t"
        "s_add_u32 s81, s81, 0x20000\n\t"
        "buffer_load_dwordx4 v96, %[sq], s81 offen lds\n\t"
        "s_add_u32 m0, m0, 0x1000\n\t"
        "s_add_u32 s81, s81, 0x20000\n\t"
        "s_sub_u32 s82, s82, 1\n\t"
        "s_cmp_lg_u32 s82, 0\n\t"
        "s_cbranch_scc1 .Ld6%=\n\t"
        "s_waitcnt vmcnt(16)\n\t"
        "s_barrier\n\t"
        "ds_read_b64_tr_b16 v[192:193], v98 offset:16384\n\t"
        "ds_read_b64_tr_b16 v[194:195], v98 offset:16896\n\t"
        "ds_read_b64_tr_b16 v[196:197], v98 offset:24576\n\t"
        "ds_read_b64_tr_b16 v[198:199], v98 offset:25088\n\t"
        "ds_read_b64_tr_b16 v[200:201], v99 offset:16384\n\t"
        "ds_read_b64_tr_b16 v[202:203], v99 offset:16896\n\t"
        "ds_read_b64_tr_b16 v[204:205], v99 offset:24576\n\t"
        "ds_read_b64_tr_b16 v[206:207], v99 offset:25088\n\t"
        "ds_read_b64_tr_b16 v[208:209], v100 offset:16384\n\t"
        "ds_read_b64_tr_b16 v[210:211], v100 offset:16896\n\t"
        "ds_read_b64_tr_b16 v[212:213], v100 offset:24576\n\t"
        "ds_read_b64_tr_b16 v[214:215], v100 offset:25088\n\t"
        "ds_read_b64_tr_b16 v[216:217], v101 offset:16384\n\t"
        "ds_read_b64_tr_b16 v[218:219], v101 offset:16896\n\t"
        "ds_read_b64_tr_b16 v[220:221], v101 offset:24576\n\t"
        "ds_read_b64_tr_b16 v[222:223], v101 offset:25088\n\t"
        "ds_read_b64_tr_b16 v[224:225], v102 offset:16384\n\t"
        "ds_read_b64_tr_b16 v[226:227], v102 offset:16896\n\t"
        "ds_read_b64_tr_b16 v[228:229], v102 offset:24576\n\t"
        "ds_read_b64_tr_b16 v[230:231], v102 offset:25088\n\t"
        "ds_read_b64_tr_b16 v[232:233], v103 offset:16384\n\t"
        "ds_read_b64_tr_b16 v[234:235], v103 offset:16896\n\t"
        "ds_read_b64_tr_b16 v[236:237], v103 offset:24576\n\t"
        "ds_read_b64_tr_b16 v[238:239], v103 offset:25088\n\t"
        "ds_read_b64_tr_b16 v[240:241], v104 offset:16384\n\t"
        "ds_read_b64_tr_b16 v[242:243], v104 offset:16896\n\t"
        "ds_read_b64_tr_b16 v[244:245], v104 offset:24576\n\t"
        "ds_read_b64_tr_b16 v[246:247], v104 offset:25088\n\t"
        "ds_read_b64_tr_b16 v[248:249], v105 offset:16384\n\t"
        "ds_read_b64_tr_b16 v[250:251], v105 offset:16896\n\t"
        "ds_read_b64_tr_b16 v[252:253], v105 offset:24576\n\t"
        "ds_read_b64_tr_b16 v[254:255], v105 offset:25088\n\t"
        "s_waitcnt lgkmcnt(0)\n\t"
        "s_barrier\n\t"
        "s_add_u32 s80, %[m0b], 16384\n\t"
        "s_mov_b32 s81, 6291456\n\t"
        "s_mov_b32 s82, 4\n\t"
        ".Ld7%=:\n\t"
        "s_mov_b32 m0, s80\n\t"
        "s_add_u32 s80, s80, 0x8000\n\t"
        "buffer_load_dwordx4 v96, %[sk], s81 offen lds\n\t"
        "s_add_u32 m0, m0, 0x1000\n\t"
        "s_add_u32 s81, s81, 0x20000\n\t"
        "buffer_load_dwordx4 v96, %[sk], s81 offen lds\n\t"
        "s_add_u32 m0, m0, 0x1000\n\t"
        "s_add_u32 s81, s81, 0x20000\n\t"
        "buffer_load_dwordx4 v96, %[sk], s81 offen lds\n\t"
        "s_add_u32 m0, m0, 0x1000\n\t"
        "s_add_u32 s81, s81, 0x20000\n\t"
        "buffer_load_dwordx4 v96, %[sk], s81 offen lds\n\t"
        "s_add_u32 m0, m0, 0x1000\n\t"
        "s_add_u32 s81, s81, 0x20000\n\t"
        "s_sub_u32 s82, s82, 1\n\t"
        "s_cmp_lg_u32 s82, 0\n\t"
        "s_cbranch_scc1 .Ld7%=\n\t"
        "s_waitcnt vmcnt(16)\n\t"
        "s_barrier\n\t"
        "ds_read_b64_tr_b16 a[0:1], v98 offset:0\n\t"
        "ds_read_b64_tr_b16 a[2:3], v98 offset:512\n\t"
        "ds_read_b64_tr_b16 a[4:5], v98 offset:8192\n\t"
        "ds_read_b64_tr_b16 a[6:7], v98 offset:8704\n\t"
        "ds_read_b64_tr_b16 a[8:9], v99 offset:0\n\t"
        "ds_read_b64_tr_b16 a[10:11], v99 offset:512\n\t"
        "ds_read_b64_tr_b16 a[12:13], v99 offset:8192\n\t"
        "ds_read_b64_tr_b16 a[14:15], v99 offset:8704\n\t"
        "ds_read_b64_tr_b16 v[64:65], v100 offset:0\n\t"
        "ds_read_b64_tr_b16 v[66:67], v100 offset:512\n\t"
        "ds_read_b64_tr_b16 v[68:69], v100 offset:8192\n\t"
        "ds_read_b64_tr_b16 v[70:71], v100 offset:8704\n\t"
        "ds_read_b64_tr_b16 v[72:73], v101 offset:0\n\t"
        "ds_read_b64_tr_b16 v[74:75], v101 offset:512\n\t"
        "ds_read_b64_tr_b16 v[76:77], v101 offset:8192\n\t"
        "ds_read_b64_tr_b16 v[78:79], v101 offset:8704\n\t"
        "ds_read_b64_tr_b16 v[80:81], v102 offset:0\n\t"
        "ds_read_b64_tr_b16 v[82:83], v102 offset:512\n\t"
        "ds_read_b64_tr_b16 v[84:85], v102 offset:8192\n\t"
        "ds_read_b64_tr_b16 v[86:87], v102 offset:8704\n\t"
        "ds_read_b64_tr_b16 v[88:89], v103 offset:0\n\t"
        "ds_read_b64_tr_b16 v[90:91], v103 offset:512\n\t"
        "ds_read_b64_tr_b16 v[92:93], v103 offset:8192\n\t"
        "ds_read_b64_tr_b16 v[94:95], v103 offset:8704\n\t"
        "ds_read_b64_tr_b16 a[128:129], v104 offset:0\n\t"
        "ds_read_b64_tr_b16 a[130:131], v104 offset:512\n\t"
        "ds_read_b64_tr_b16 a[132:133], v104 offset:8192\n\t"
        "ds_read_b64_tr_b16 a[134:135], v104 offset:8704\n\t"
        "ds_read_b64_tr_b16 a[136:137], v105 offset:0\n\t"
        "ds_read_b64_tr_b16 a[138:139], v105 offset:512\n\t"
        "ds_read_b64_tr_b16 a[140:141], v105 offset:8192\n\t"
        "ds_read_b64_tr_b16 a[142:143], v105 offset:8704\n\t"
        "s_waitcnt lgkmcnt(0)\n\t"
        "s_barrier\n\t"
        "ds_write_b128 v121, v[0:3] offset:0\n\t"
        "ds_write_b128 v121, v[4:7] offset:1024\n\t"
        "ds_write_b128 v121, v[8:11] offset:2048\n\t"
        "ds_write_b128 v121, v[12:15] offset:3072\n\t"
        "ds_write_b128 v121, v[16:19] offset:4096\n\t"
        "ds_write_b128 v121, v[20:23] offset:5120\n\t"
        "ds_write_b128 v121, v[24:27] offset:6144\n\t"
        "ds_write_b128 v121, v[28:31] offset:7168\n\t"
        "ds_write_b128 v121, v[32:35] offset:8192\n\t"
        "ds_write_b128 v121, v[36:39] offset:9216\n\t"
        "ds_write_b128 v121, v[40:43] offset:10240\n\t"
        "ds_write_b128 v121, v[44:47] offset:11264\n\t"
        "ds_write_b128 v121, v[48:51] offset:12288\n\t"
        "ds_write_b128 v121, v[52:55] offset:13312\n\t"
        "ds_write_b128 v121, v[56:59] offset:14336\n\t"
        "ds_write_b128 v121, v[60:63] offset:15360\n\t"
        "s_waitcnt lgkmcnt(0)\n\t"
        "s_mov_b32 s64, %[kf]\n\t"
        "s_mov_b32 s36, 0xf0000000\n\t"
        "buffer_load_dwordx4 v[116:119], v120, %[wsd], %[cal] offen sc1\n\t"
        "buffer_load_dwordx2 v[114:115], off, %[wsd], s64 sc1\n\t"
        "s_waitcnt vmcnt(2)\n\t"
        "s_barrier\n\t"
        "ds_read_b64_tr_b16 a[64:65], v98 offset:16384\n\t"
        "ds_read_b64_tr_b16 a[66:67], v98 offset:16896\n\t"
        "ds_read_b64_tr_b16 a[68:69], v98 offset:24576\n\t"
        "ds_read_b64_tr_b16 a[70:71], v98 offset:25088\n\t"
        "ds_read_b64_tr_b16 a[72:73], v99 offset:16384\n\t"
        "ds_read_b64_tr_b16 a[74:75], v99 offset:16896\n\t"
        "ds_read_b64_tr_b16 a[76:77], v99 offset:24576\n\t"
        "ds_read_b64_tr_b16 a[78:79], v99 offset:25088\n\t"
        "ds_read_b64_tr_b16 v[0:1], v100 offset:16384\n\t"
        "ds_read_b64_tr_b16 v[2:3], v100 offset:16896\n\t"
        "ds_read_b64_tr_b16 v[4:5], v100 offset:24576\n\t"
        "ds_read_b64_tr_b16 v[6:7], v100 offset:25088\n\t"
        "ds_read_b64_tr_b16 v[8:9], v101 offset:16384\n\t"
        "ds_read_b64_tr_b16 v[10:11], v101 offset:16896\n\t"
        "ds_read_b64_tr_b16 v[12:13], v101 offset:24576\n\t"
        "ds_read_b64_tr_b16 v[14:15], v101 offset:25088\n\t"
        "ds_read_b64_tr_b16 v[16:17], v102 offset:16384\n\t"
        "ds_read_b64_tr_b16 v[18:19], v102 offset:16896\n\t"
        "ds_read_b64_tr_b16 v[20:21], v102 offset:24576\n\t"
        "ds_read_b64_tr_b16 v[22:23], v102 offset:25088\n\t"
        "ds_read_b64_tr_b16 v[24:25], v103 offset:16384\n\t"
        "ds_read_b64_tr_b16 v[26:27], v103 offset:16896\n\t"
        "ds_read_b64_tr_b16 v[28:29], v103 offset:24576\n\t"
        "ds_read_b64_tr_b16 v[30:31], v103 offset:25088\n\t"
        "ds_read_b64_tr_b16 a[192:193], v104 offset:16384\n\t"
        "ds_read_b64_tr_b16 a[194:195], v104 offset:16896\n\t"
        "ds_read_b64_tr_b16 a[196:197], v104 offset:24576\n\t"
        "ds_read_b64_tr_b16 a[198:199], v104 offset:25088\n\t"
        "ds_read_b64_tr_b16 a[200:201], v105 offset:16384\n\t"
        "ds_read_b64_tr_b16 a[202:203], v105 offset:16896\n\t"
        "ds_read_b64_tr_b16 a[204:205], v105 offset:24576\n\t"
        "ds_read_b64_tr_b16 a[206:207], v105 offset:25088\n\t"
        "s_waitcnt vmcnt(0)\n\t"
        "s_mov_b32 s97, 0\n\t"
        ".Lg0%=:\n\t"
        "v_cmp_ne_u32_e32 vcc, %[tag], v116\n\t"
        "v_cmp_ne_u32_e64 s[82:83], %[tag], v118\n\t"
        "s_nop 1\n\t"
        "s_or_b64 vcc, vcc, s[82:83]\n\t"
        "s_cbranch_scc0 .Lg1%=\n\t"
        "s_sleep 4\n\t"
        "buffer_load_dwordx4 v[116:119], v120, %[wsd], %[cal] offen sc1\n\t"
        "s_add_u32 s97, s97, 1\n\t"
        "s_cmp_lt_u32 s97, 0x100000\n\t"
        "s_cbranch_scc0 .Lgu%=\n\t"
        "s_waitcnt vmcnt(0)\n\t"
        "s_branch .Lg0%=\n\t"
        ".Lg1%=:\n\t"
        "s_mov_b32 s84, 0x55555555\n\t"
        "s_mov_b32 s85, s84\n\t"
        "v_and_b32_e32 v116, 1, v117\n\t"
        "v_and_b32_e32 v118, 1, v119\n\t"
        "v_cmp_ne_u32_e32 vcc, 0, v116\n\t"
        "v_cmp_ne_u32_e64 s[82:83], 0, v118\n\t"
        "s_nop 0\n\t"
        "s_and_b64 s[82:83], s[82:83], s[84:85]\n\t"
        "s_bcnt1_i32_b64 s87, s[82:83]\n\t"
        "s_and_b64 s[82:83], vcc, s[84:85]\n\t"
        "s_bcnt1_i32_b64 s86, s[82:83]\n\t"
        "s_andn2_b64 s[82:83], vcc, s[84:85]\n\t"
        "s_bcnt1_i32_b64 s88, s[82:83]\n\t"
        "v_and_b32_e32 v116, 2, v117\n\t"
        "v_and_b32_e32 v118, 2, v119\n\t"
        "v_cmp_ne_u32_e32 vcc, 0, v116\n\t"
        "v_cmp_ne_u32_e64 s[82:83], 0, v118\n\t"
        "s_nop 0\n\t"
        "s_and_b64 s[82:83], s[82:83], s[84:85]\n\t"
        "s_bcnt1_i32_b64 s90, s[82:83]\n\t"
        "s_and_b64 s[82:83], vcc, s[84:85]\n\t"
        "s_bcnt1_i32_b64 s89, s[82:83]\n\t"
        "s_andn2_b64 s[82:83], vcc, s[84:85]\n\t"
        "s_bcnt1_i32_b64 s91, s[82:83]\n\t"
        "s_add_u32 s80, s86, s87\n\t"
        "s_add_u32 s80, s80, s88\n\t"
        "s_cmp_ge_u32 s80, 48\n\t"
        "s_cselect_b32 s80, 8, 24\n\t"
        "s_cmp_gt_u32 s86, s80\n\t"
        "s_cselect_b32 s86, %c[NX1], %c[NX0]\n\t"
        "s_cmp_gt_u32 s89, 16\n\t"
        "s_cselect_b32 s81, %c[NY1], %c[NY0]\n\t"
        "s_or_b32 s86, s86, s81\n\t"
        "s_cmp_gt_u32 s87, s80\n\t"
        "s_cselect_b32 s87, %c[NX1], %c[NX0]\n\t"
        "s_cmp_gt_u32 s90, 16\n\t"
        "s_cselect_b32 s81, %c[NY1], %c[NY0]\n\t"
        "s_or_b32 s87, s87, s81\n\t"
        "s_cmp_gt_u32 s88, s80\n\t"
        "s_cselect_b32 s88, %c[NX1], %c[NX0]\n\t"
        "s_cmp_gt_u32 s91, 16\n\t"
        "s_cselect_b32 s81, %c[NY1], %c[NY0]\n\t"
        "s_or_b32 s88, s88, s81\n\t"
        "s_xor_b32 %[nA], %[nA], s86\n\t"
        "s_xor_b32 %[nW], %[nW], s86\n\t"
        "s_xor_b32 %[nC], %[nC], s87\n\t"
        "s_xor_b32 %[nS], %[nS], s88\n\t"
        "s_xor_b32 %[nT], %[nT], s88\n\t"
        "s_xor_b32 %[b0], %[b0], s88\n\t"
        "s_cmp_eq_u32 %[w], 1\n\t"
        "s_cselect_b32 s80, s87, s86\n\t"
        "s_cmp_eq_u32 %[w], 2\n\t"
        "s_cselect_b32 s80, s88, s80\n\t"
        "s_xor_b32 %[pob], %[pob], s80\n\t"
        "s_lshl_b32 s80, %[w], 10\n\t"
        "s_sub_u32 %[nS], %[nS], s80\n\t"
        "s_sub_u32 %[pob], %[pob], s80\n\t"
        "ds_read_b32 v116, v120 offset:20484\n\t"
        "s_and_b32 s80, s87, 0x4000\n\t"
        "s_xor_b32 %[nC], %[nC], s80\n\t"
        "s_lshl_b32 s81, %[w], 8\n\t"
        "s_add_u32 s81, s81, 24576\n\t"
        "s_sub_u32 %[b0], %[b0], s81\n\t"
        "s_waitcnt lgkmcnt(0)\n\t"
        "v_xor_b32_e32 v116, s80, v116\n\t"
        "ds_write_b32 v120, v116 offset:20484\n\t"
        "s_mov_b32 s36, 0xf0000000\n\t"
        "s_mov_b32 s38, 0xff00ff00\n\t"
        "s_mov_b32 s39, 0xff00ff00\n\t"
        "s_mov_b32 s40, 0\n\t"
        "s_mov_b32 s37, 0\n\t"
        "s_brev_b32 s54, 1\n\t"
        "s_mov_b32 s46, %c[iD1]\n\t"
        "s_mov_b32 s45, %c[iD1]\n\t"
        "s_mov_b32 s47, %c[iD2]\n\t"
        "s_mov_b32 s48, 1\n\t"
        "s_mov_b32 s50, %c[LC]\n\t"
        "s_mov_b32 s51, %c[LA]\n\t"
        "s_mov_b32 s52, %c[E1]\n\t"
        "s_mov_b32 s53, %c[E2]\n\t"
        "s_mov_b32 s43, 0\n\t"
        "s_mov_b32 s61, %[b0]\n\t"
        "s_add_u32 s62, %[b0], 0x40000\n\t"
        "s_mov_b32 s75, 31744\n\t"
        "s_cmp_eq_u32 s64, s36\n\t"
        "s_cbranch_scc1 .Lk10%=\n\t"
        "s_mov_b32 s97, 0\n\t"
        ".Lk00%=:\n\t"
        "v_readfirstlane_b32 s82, v114\n\t"
        "v_readfirstlane_b32 s83, v115\n\t"
        "s_xor_b32 s82, s82, %[tag]\n\t"
        "s_xor_b32 s83, s83, %[tag]\n\t"
        "s_or_b32 s82, s82, s83\n\t"
        "s_cbranch_scc0 .Lk10%=\n\t"
        "s_sleep 3\n\t"
        "buffer_load_dwordx2 v[114:115], off, %[wsd], s64 sc1\n\t"
        "s_add_u32 s97, s97, 1\n\t"
        "s_cmp_lt_u32 s97, 0x200000\n\t"
        "s_cbranch_scc0 .Lgu%=\n\t"
        "s_waitcnt vmcnt(0)\n\t"
        "s_branch .Lk00%=\n\t"
        ".Lk10%=:\n\t"
        "s_add_u32 s80, s40, 0\n\t"
        "s_min_u32 s80, s80, 63\n\t"
        "s_lshl_b32 s80, s80, 18\n\t"
        "s_add_u32 s69, s80, %[kx]\n\t"
        "s_add_u32 s70, s69, 0x1000\n\t"
        "s_lshl_b32 s80, s40, 18\n\t"
        "s_add_u32 s71, s80, %[vx]\n\t"
        "s_add_u32 s80, s40, 1\n\t"
        "s_lshl_b32 s81, s80, 9\n\t"
        "s_add_u32 s81, s81, %[kf]\n\t"
        "s_and_b32 s80, s80, 0x43\n\t"
        "s_cselect_b32 s64, s36, s81\n\t"
        "buffer_load_dwordx4 v[104:107], v120, %[wsd], s69 offen sc1\n\t"
        "buffer_load_dwordx4 v[108:111], v120, %[wsd], s70 offen sc1\n\t"
        "s_waitcnt lgkmcnt(0)\n\t"
        "s_barrier\n\t"
        "ds_write_b128 v121, v[64:67] offset:16384\n\t"
        "ds_write_b128 v121, v[68:71] offset:17408\n\t"
        "ds_write_b128 v121, v[72:75] offset:18432\n\t"
        "ds_write_b128 v121, v[76:79] offset:19456\n\t"
        "ds_write_b128 v121, v[80:83] offset:20480\n\t"
        "ds_write_b128 v121, v[84:87] offset:21504\n\t"
        "ds_write_b128 v121, v[88:91] offset:22528\n\t"
        "ds_write_b128 v121, v[92:95] offset:23552\n\t"
        "ds_write_b128 v121, v[0:3] offset:24576\n\t"
        "ds_write_b128 v121, v[4:7] offset:25600\n\t"
        "ds_write_b128 v121, v[8:11] offset:26624\n\t"
        "ds_write_b128 v121, v[12:15] offset:27648\n\t"
        "ds_write_b128 v121, v[16:19] offset:28672\n\t"
        "ds_write_b128 v121, v[20:23] offset:29696\n\t"
        "ds_write_b128 v121, v[24:27] offset:30720\n\t"
        "ds_write_b128 v121, v[28:31] offset:31744\n\t"
        "s_waitcnt lgkmcnt(0)\n\t"
        "rec_s_code:\n\t"
        "ds_read_b128 v[16:19], v121 offset:0\n\t"
        "ds_read_b128 v[20:23], v121 offset:1024\n\t"
        "ds_read_b128 v[24:27], v121 offset:2048\n\t"
        "ds_read_b128 v[28:31], v121 offset:3072\n\t"
        "ds_read_b128 v[32:35], v121 offset:4096\n\t"
        "ds_read_b128 v[36:39], v121 offset:5120\n\t"
        "ds_read_b128 v[40:43], v121 offset:6144\n\t"
        "ds_read_b128 v[44:47], v121 offset:7168\n\t"
        "ds_read_b128 v[56:59], v121 offset:8192\n\t"
        "ds_read_b128 v[60:63], v121 offset:9216\n\t"
        "ds_read_b128 v[64:67], v121 offset:10240\n\t"
        "ds_read_b128 v[68:71], v121 offset:11264\n\t"
        "ds_read_b128 v[72:75], v121 offset:12288\n\t"
        "ds_read_b128 v[76:79], v121 offset:13312\n\t"
        "ds_read_b128 v[80:83], v121 offset:14336\n\t"
        "ds_read_b128 v[84:87], v121 offset:15360\n\t"
        "s_and_b32 s80, s40, 3\n\t"
        "s_lshl_b32 s80, s80, 17\n\t"
        "s_add_u32 s57, s80, %[nA]\n\t"
        "s_add_u32 s58, s57, 0x1000\n\t"
        "s_add_u32 s59, s57, 0x8000\n\t"
        "s_add_u32 s60, s57, 0x9000\n\t"
        "s_and_b32 s80, s40, 3\n\t"
        "s_lshl_b32 s80, s80, 17\n\t"
        "s_add_u32 s63, s80, %[nC]\n\t"
        "s_add_u32 s66, s80, %[nS]\n\t"
        "s_add_u32 s81, s80, 0x20000\n\t"
        "s_and_b32 s81, s81, 0x60000\n\t"
        "s_add_u32 s67, s81, %[nW]\n\t"
        "s_add_u32 s79, s81, %[nT]\n\t"
        "s_cmp_eq_u32 %[w], 2\n\t"
        "s_cselect_b32 s81, s80, s81\n\t"
        "s_xor_b32 s81, s81, 0x40000\n\t"
        "s_add_u32 s68, s81, %[pob]\n\t"
        "s_lshl_b32 s72, s40, 17\n\t"
        "s_add_u32 s72, s72, %[kvo]\n\t"
        "s_mov_b32 s88, 0\n\t"
        "s_mov_b32 s89, 0\n\t"
        "s_mov_b32 s90, 0\n\t"
        "v_mov_b32_e32 v88, 0\n\t"
        "v_mov_b32_e32 v89, 0\n\t"
        "v_mov_b32_e32 v90, 0\n\t"
        "v_mov_b32_e32 v91, 0\n\t"
        "v_mov_b32_e32 v92, 0\n\t"
        "v_mov_b32_e32 v93, 0\n\t"
        "v_mov_b32_e32 v94, 0\n\t"
        "v_mov_b32_e32 v95, 0\n\t"
        "v_mov_b32_e32 v96, 0\n\t"
        "v_mov_b32_e32 v97, 0\n\t"
        "v_mov_b32_e32 v98, 0\n\t"
        "v_mov_b32_e32 v99, 0\n\t"
        "v_mov_b32_e32 v100, 0\n\t"
        "v_mov_b32_e32 v101, 0\n\t"
        "v_mov_b32_e32 v102, 0\n\t"
        "v_mov_b32_e32 v103, 0\n\t"
        "v_mov_b32_e32 v52, v112\n\t"
        "v_mov_b32_e32 v53, v113\n\t"
        "v_mov_b32_e32 v49, 0\n\t"
        "v_lshlrev_b32_e32 v51, 6, v120\n\t"
        "s_cmp_eq_u32 %[w], 0\n\t"
        "s_cselect_b32 s73, %[nT], s36\n\t"
        "s_cselect_b32 s74, %[pra], s36\n\t"
        "v_mov_b32_e32 v50, s40\n\t"
        "s_cmp_ge_u32 s40, 60\n\t"
        "s_cselect_b32 s65, s74, s36\n\t"
        "s_waitcnt lgkmcnt(0)\n\t"
        "s_barrier\n\t"
        "s_memrealtime s[98:99]\n\t"
        "s_waitcnt lgkmcnt(0)\n\t"
        "s_mov_b32 s41, s98\n\t"
        "v_mov_b32_e32 v48, s98\n\t"
        ".p2align 6\n\t"
        ".Lloop%=:\n\t"
        "s_mov_b32 s97, 0\n\t"
        "s_movk_i32 s96, 0x1000\n\t"
        ".Lwa%=:\n\t"
        "s_memrealtime s[98:99]\n\t"
        "s_addk_i32 s96, -1\n\t"
        "s_cmp_lt_i32 s96, 0\n\t"
        "s_cbranch_scc1 .Lta%=\n\t"
        "s_waitcnt lgkmcnt(0)\n\t"
        "s_sub_i32 s98, s98, s41\n\t"
        "s_cmp_lt_i32 s98, %c[iFT]\n\t"
        "s_cbranch_scc1 .Lwa%=\n\t"
        "s_cmp_ge_i32 s98, 0\n\t"
        "s_cbranch_scc1 .Lxa%=\n\t"
        ".Lqa%=:\n\t"
        "s_nop %c[iFN]\n\t"
        "s_addk_i32 s98, 1\n\t"
        "s_cmp_lt_i32 s98, 0\n\t"
        "s_cbranch_scc1 .Lqa%=\n\t"
        ".Lxa%=:\n\t"
        "buffer_load_dwordx4 v[0:3], v120, %[srd], s57 offen sc1\n\t"
        "s_mov_b32 s80, s88\n\t"
        "s_add_i32 s81, s92, s50\n\t"
        "s_sub_i32 s81, s81, 4\n\t"
        "s_cmp_le_i32 s81, s44\n\t"
        "s_cselect_b32 s80, s80, 0\n\t"
        "s_cmp_lt_i32 s50, %c[LC24]\n\t"
        "s_cselect_b32 s80, s80, 0\n\t"
        "s_lshl2_add_u32 s50, s80, s50\n\t"
        "buffer_load_dwordx4 v[4:7], v120, %[srd], s58 offen sc1\n\t"
        "s_sub_i32 s50, s50, s89\n\t"
        "s_max_i32 s50, s50, %c[LC]\n\t"
        "s_lshl_b32 s80, s92, 1\n\t"
        "s_sub_i32 s81, s80, s52\n\t"
        "s_ashr_i32 s82, s81, %c[UPSH]\n\t"
        "s_max_i32 s82, s82, %c[TRUP]\n\t"
        "s_add_i32 s82, s82, s52\n\t"
        "s_min_i32 s82, s82, s80\n\t"
        "buffer_load_dwordx4 v[8:11], v120, %[srd], s59 offen sc1\n\t"
        "s_ashr_i32 s83, s81, %c[DNSH]\n\t"
        "s_min_i32 s83, s83, -%c[TRDN]\n\t"
        "s_add_i32 s83, s83, s52\n\t"
        "s_max_i32 s83, s83, s80\n\t"
        "s_cmp_gt_i32 s81, 0\n\t"
        "s_cselect_b32 s82, s82, s83\n\t"
        "s_cmp_lg_u32 s90, 0\n\t"
        "s_cselect_b32 s52, s82, s52\n\t"
        "buffer_load_dwordx4 v[12:15], v120, %[srd], s60 offen sc1\n\t"
        "s_add_i32 s80, s52, 1\n\t"
        "s_ashr_i32 s80, s80, 1\n\t"
        "s_add_i32 s45, s80, s50\n\t"
        "s_add_u32 s80, s40, 1\n\t"
        "s_min_u32 s80, s80, 63\n\t"
        "s_lshl_b32 s80, s80, 18\n\t"
        "s_add_u32 s69, s80, %[kx]\n\t"
        "s_add_u32 s70, s69, 0x1000\n\t"
        "s_lshl_b32 s80, s40, 18\n\t"
        "s_add_u32 s71, s80, %[vx]\n\t"
        "s_add_u32 s80, s40, 2\n\t"
        "s_lshl_b32 s81, s80, 9\n\t"
        "s_add_u32 s81, s81, %[kf]\n\t"
        "s_and_b32 s80, s80, 0x43\n\t"
        "s_cselect_b32 s64, s36, s81\n\t"
        "s_nop 15\n\t"
        "s_nop 7\n\t"
        "s_mov_b64 exec, 7\n\t"
        "buffer_store_dwordx2 v[48:49], v51, %[srd], s73 offen sc1\n\t"
        "s_mov_b64 exec, -1\n\t"
        "buffer_load_dwordx4 v[112:115], v120, %[wsd], s71 offen offset:0 sc1\n\t"
        "buffer_load_dwordx4 v[116:119], v120, %[wsd], s71 offen offset:1024 sc1\n\t"
        ".Lgo%=:\n\t"
        "s_waitcnt vmcnt(6)\n\t"
        "v_smfmac_f32_16x16x64_bf16 v[88:91], v[0:3], v[16:23], v122\n\t"
        "ds_write_b128 v120, v[0:3] offset:0\n\t"
        "v_smfmac_f32_16x16x64_bf16 v[92:95], v[0:3], v[24:31], v122\n\t"
        "v_smfmac_f32_16x16x64_bf16 v[96:99], v[0:3], v[32:39], v122\n\t"
        "v_smfmac_f32_16x16x64_bf16 v[100:103], v[0:3], v[40:47], v122\n\t"
        "s_waitcnt vmcnt(5)\n\t"
        "v_smfmac_f32_16x16x64_bf16 v[88:91], v[4:7], v[56:63], v122\n\t"
        "ds_write_b128 v120, v[4:7] offset:4096\n\t"
        "v_smfmac_f32_16x16x64_bf16 v[92:95], v[4:7], v[64:71], v122\n\t"
        "v_smfmac_f32_16x16x64_bf16 v[96:99], v[4:7], v[72:79], v122\n\t"
        "v_smfmac_f32_16x16x64_bf16 v[100:103], v[4:7], v[80:87], v122\n\t"
        "s_waitcnt vmcnt(4)\n\t"
        "v_smfmac_f32_16x16x64_bf16 v[88:91], v[8:11], v[128:135], v122\n\t"
        "ds_write_b128 v120, v[8:11] offset:8192\n\t"
        "v_smfmac_f32_16x16x64_bf16 v[92:95], v[8:11], v[136:143], v122\n\t"
        "s_waitcnt lgkmcnt(1)\n\t"
        "s_barrier\n\t"
        "v_smfmac_f32_16x16x64_bf16 v[96:99], v[8:11], v[192:199], v122\n\t"
        "ds_read_b128 v[16:19], v124 offset:0\n\t"
        "v_smfmac_f32_16x16x64_bf16 v[100:103], v[8:11], v[200:207], v122\n\t"
        "ds_read_b128 v[20:23], v125 offset:0\n\t"
        "s_waitcnt vmcnt(3)\n\t"
        "v_smfmac_f32_16x16x64_bf16 v[88:91], v[12:15], a[0:7], v122\n\t"
        "ds_write_b128 v120, v[12:15] offset:12288\n\t"
        "v_smfmac_f32_16x16x64_bf16 v[92:95], v[12:15], a[8:15], v122\n\t"
        "buffer_store_dword v50, off, %[wsd], s65 sc1\n\t"
        "v_smfmac_f32_16x16x64_bf16 v[96:99], v[12:15], a[64:71], v122\n\t"
        "ds_read_b128 v[24:27], v126 offset:0\n\t"
        "v_smfmac_f32_16x16x64_bf16 v[100:103], v[12:15], a[72:79], v122\n\t"
        "ds_read_b128 v[28:31], v124 offset:4096\n\t"
        "s_waitcnt lgkmcnt(4)\n\t"
        "v_smfmac_f32_16x16x64_bf16 v[88:91], v[16:19], a[16:23], v122\n\t"
        "ds_read_b128 v[32:35], v125 offset:4096\n\t"
        "v_smfmac_f32_16x16x64_bf16 v[92:95], v[16:19], a[24:31], v122\n\t"
        "ds_read_b128 v[36:39], v126 offset:4096\n\t"
        "s_waitcnt lgkmcnt(5)\n\t"
        "v_smfmac_f32_16x16x64_bf16 v[88:91], v[20:23], a[32:39], v122\n\t"
        "ds_read_b128 v[72:75], v121 offset:16384\n\t"
        "v_smfmac_f32_16x16x64_bf16 v[92:95], v[20:23], a[40:47], v122\n\t"
        "s_waitcnt lgkmcnt(5)\n\t"
        "s_barrier\n\t"
        "ds_read_b128 v[76:79], v121 offset:17408\n\t"
        "s_waitcnt lgkmcnt(5)\n\t"
        "v_smfmac_f32_16x16x64_bf16 v[88:91], v[24:27], a[48:55], v122\n\t"
        "ds_read_b128 v[0:3], v124 offset:12288\n\t"
        "v_smfmac_f32_16x16x64_bf16 v[92:95], v[24:27], a[56:63], v122\n\t"
        "ds_read_b128 v[80:83], v121 offset:18432\n\t"
        "s_waitcnt lgkmcnt(6)\n\t"
        "v_smfmac_f32_16x16x64_bf16 v[88:91], v[28:31], a[144:151], v122\n\t"
        "ds_read_b128 v[84:87], v121 offset:19456\n\t"
        "v_smfmac_f32_16x16x64_bf16 v[92:95], v[28:31], a[152:159], v122\n\t"
        "ds_read_b128 v[40:43], v124 offset:8192\n\t"
        "s_waitcnt lgkmcnt(7)\n\t"
        "v_smfmac_f32_16x16x64_bf16 v[88:91], v[32:35], a[160:167], v122\n\t"
        "ds_read_b128 v[44:47], v125 offset:8192\n\t"
        "v_mov_b32_e32 v12, 0x3f803f80\n\t"
        "v_smfmac_f32_16x16x64_bf16 v[92:95], v[32:35], a[168:175], v122\n\t"
        "ds_read_b128 v[56:59], v121 offset:20480\n\t"
        "v_mov_b32_e32 v13, 0x3f803f80\n\t"
        "s_waitcnt lgkmcnt(8)\n\t"
        "v_smfmac_f32_16x16x64_bf16 v[88:91], v[36:39], a[176:183], v122\n\t"
        "ds_read_b128 v[60:63], v121 offset:21504\n\t"
        "v_mov_b32_e32 v14, 0x3f803f80\n\t"
        "v_smfmac_f32_16x16x64_bf16 v[92:95], v[36:39], a[184:191], v122\n\t"
        "ds_read_b128 v[4:7], v125 offset:12288\n\t"
        "v_mov_b32_e32 v15, 0x3f803f80\n\t"
        "s_waitcnt lgkmcnt(8)\n\t"
        "s_waitcnt lgkmcnt(7)\n\t"
        "v_smfmac_f32_16x16x64_bf16 v[88:91], v[0:3], v[72:79], v122\n\t"
        "ds_read_b128 v[64:67], v121 offset:22528\n\t"
        "s_waitcnt lgkmcnt(6)\n\t"
        "v_smfmac_f32_16x16x64_bf16 v[92:95], v[0:3], v[80:87], v122\n\t"
        "ds_read_b128 v[68:71], v121 offset:23552\n\t"
        "s_waitcnt lgkmcnt(6)\n\t"
        "v_smfmac_f32_16x16x64_bf16 v[88:91], v[40:43], v[144:151], v122\n\t"
        "ds_read_b128 v[48:51], v126 offset:8192\n\t"
        "v_smfmac_f32_16x16x64_bf16 v[92:95], v[40:43], v[152:159], v122\n\t"
        "ds_read_b128 v[8:11], v126 offset:12288\n\t"
        "s_waitcnt lgkmcnt(7)\n\t"
        "v_smfmac_f32_16x16x64_bf16 v[88:91], v[44:47], v[160:167], v122\n\t"
        "ds_read_b128 v[72:75], v121 offset:24576\n\t"
        "v_smfmac_f32_16x16x64_bf16 v[92:95], v[44:47], v[168:175], v122\n\t"
        "ds_read_b128 v[76:79], v121 offset:25600\n\t"
        "s_waitcnt lgkmcnt(7)\n\t"
        "s_waitcnt lgkmcnt(6)\n\t"
        "v_smfmac_f32_16x16x64_bf16 v[88:91], v[4:7], v[56:63], v122\n\t"
        "ds_read_b128 v[80:83], v121 offset:26624\n\t"
        "s_waitcnt lgkmcnt(5)\n\t"
        "v_smfmac_f32_16x16x64_bf16 v[92:95], v[4:7], v[64:71], v122\n\t"
        "ds_read_b128 v[84:87], v121 offset:27648\n\t"
        "s_waitcnt lgkmcnt(5)\n\t"
        "v_smfmac_f32_16x16x64_bf16 v[88:91], v[48:51], v[176:183], v122\n\t"
        "v_mov_b32_e32 v64, 0\n\t"
        "v_smfmac_f32_16x16x64_bf16 v[92:95], v[48:51], v[184:191], v122\n\t"
        "v_mov_b32_e32 v65, 0\n\t"
        "s_waitcnt lgkmcnt(4)\n\t"
        "v_smfmac_f32_16x16x64_bf16 v[88:91], v[8:11], a[128:135], v122\n\t"
        "v_mov_b32_e32 v66, 0\n\t"
        "v_smfmac_f32_16x16x64_bf16 v[92:95], v[8:11], a[136:143], v122\n\t"
        "v_mov_b32_e32 v67, 0\n\t"
        "s_waitcnt lgkmcnt(2)\n\t"
        "v_smfmac_f32_16x16x64_bf16 v[96:99], v[0:3], v[72:79], v122\n\t"
        "ds_read_b128 v[72:75], v121 offset:28672\n\t"
        "s_waitcnt lgkmcnt(1)\n\t"
        "v_smfmac_f32_16x16x64_bf16 v[100:103], v[0:3], v[80:87], v122\n\t"
        "ds_read_b128 v[76:79], v121 offset:29696\n\t"
        "v_smfmac_f32_16x16x64_bf16 v[96:99], v[16:19], a[80:87], v122\n\t"
        "v_add_f32_e32 v88, v88, v90\n\t"
        "v_add_f32_e32 v89, v89, v91\n\t"
        "v_smfmac_f32_16x16x64_bf16 v[100:103], v[16:19], a[88:95], v122\n\t"
        "v_cvt_pk_bf16_f32 v90, v88, v89\n\t"
        "v_add_f32_e32 v92, v92, v94\n\t"
        "v_smfmac_f32_16x16x64_bf16 v[96:99], v[20:23], a[96:103], v122\n\t"
        "ds_write_b32 v127, v90\n\t"
        "v_add_f32_e32 v93, v93, v95\n\t"
        "v_cvt_pk_bf16_f32 v91, v92, v93\n\t"
        "v_smfmac_f32_16x16x64_bf16 v[100:103], v[20:23], a[104:111], v122\n\t"
        "ds_write_b32 v127, v91 offset:384\n\t"
        "v_cmp_u_f32_e32 vcc, v88, v89\n\t"
        "v_smfmac_f32_16x16x64_bf16 v[96:99], v[24:27], a[112:119], v122\n\t"
        "ds_read_b64_tr_b16 v[56:57], v52 offset:0\n\t"
        "ds_read_b64_tr_b16 v[58:59], v52 offset:64\n\t"
        "v_smfmac_f32_16x16x64_bf16 v[100:103], v[24:27], a[120:127], v122\n\t"
        "ds_read_b64_tr_b16 v[60:61], v53 offset:0\n\t"
        "ds_read_b64_tr_b16 v[62:63], v53 offset:64\n\t"
        "v_smfmac_f32_16x16x64_bf16 v[96:99], v[28:31], a[208:215], v122\n\t"
        "s_cbranch_vccnz .Lf1%=\n\t"
        ".Lr1%=:\n\t"
        "ds_read_b128 v[80:83], v121 offset:30720\n\t"
        "v_smfmac_f32_16x16x64_bf16 v[100:103], v[28:31], a[216:223], v122\n\t"
        "ds_read_b128 v[84:87], v121 offset:31744\n\t"
        "v_mov_b32_e32 v91, 0\n\t"
        "v_smfmac_f32_16x16x64_bf16 v[96:99], v[32:35], a[224:231], v122\n\t"
        "v_mov_b32_e32 v94, 0\n\t"
        "v_smfmac_f32_16x16x64_bf16 v[100:103], v[32:35], a[232:239], v122\n\t"
        "ds_read_b128 v[32:35], v120 offset:20480\n\t"
        "v_smfmac_f32_16x16x64_bf16 v[96:99], v[36:39], a[240:247], v122\n\t"
        "s_waitcnt lgkmcnt(3)\n\t"
        "s_barrier\n\t"
        "v_mfma_f32_16x16x32_bf16 v[68:71], v[104:107], v[56:59], 0\n\t"
        "ds_read_b64_tr_b16 v[16:17], v123 offset:0\n\t"
        "v_mfma_f32_16x16x32_bf16 v[68:71], v[108:111], v[60:63], v[68:71]\n\t"
        "ds_read_b64_tr_b16 v[18:19], v123 offset:64\n\t"
        "v_smfmac_f32_16x16x64_bf16 v[100:103], v[36:39], a[248:255], v122\n\t"
        "buffer_load_dword v88, v127, %[srd], s61 offen offset:0 sc1\n\t"
        "v_smfmac_f32_16x16x64_bf16 v[96:99], v[8:11], a[192:199], v122\n\t"
        "buffer_load_dword v89, v127, %[srd], s61 offen offset:256 sc1\n\t"
        "v_smfmac_f32_16x16x64_bf16 v[100:103], v[8:11], a[200:207], v122\n\t"
        "s_nop 1\n\t"
        "ds_write_b128 v120, v[68:71] offset:16384\n\t"
        "v_smfmac_f32_16x16x64_bf16 v[96:99], v[4:7], v[72:79], v122\n\t"
        "ds_read_b64_tr_b16 v[20:21], v123 offset:768\n\t"
        "s_waitcnt lgkmcnt(5)\n\t"
        "v_smfmac_f32_16x16x64_bf16 v[100:103], v[4:7], v[80:87], v122\n\t"
        "ds_read_b64_tr_b16 v[22:23], v123 offset:832\n\t"
        "v_smfmac_f32_16x16x64_bf16 v[96:99], v[40:43], v[208:215], v122\n\t"
        "ds_read_b64_tr_b16 v[24:25], v123 offset:1536\n\t"
        "ds_read_b64_tr_b16 v[26:27], v123 offset:1600\n\t"
        "v_smfmac_f32_16x16x64_bf16 v[100:103], v[40:43], v[216:223], v122\n\t"
        "buffer_load_dwordx4 v[84:87], v120, %[srd], s66 offen sc1\n\t"
        "v_smfmac_f32_16x16x64_bf16 v[96:99], v[44:47], v[224:231], v122\n\t"
        "s_waitcnt lgkmcnt(4)\n\t"
        "s_barrier\n\t"
        "ds_read_b128 v[36:39], v124 offset:16384\n\t"
        "v_smfmac_f32_16x16x64_bf16 v[100:103], v[44:47], v[232:239], v122\n\t"
        "ds_read_b128 v[56:59], v125 offset:16384\n\t"
        "v_smfmac_f32_16x16x64_bf16 v[96:99], v[48:51], v[240:247], v122\n\t"
        "ds_read_b128 v[60:63], v126 offset:16384\n\t"
        "v_smfmac_f32_16x16x64_bf16 v[100:103], v[48:51], v[248:255], v122\n\t"
        "ds_read_b64_tr_b16 v[28:29], v123 offset:2304\n\t"
        "ds_read_b64_tr_b16 v[30:31], v123 offset:2368\n\t"
        "s_waitcnt lgkmcnt(4)\n\t"
        "s_cmp_eq_u32 s40, 63\n\t"
        "s_nop 1\n\t"
        "v_add_f32_e32 v96, v96, v98\n\t"
        "v_add_f32_e32 v97, v97, v99\n\t"
        "s_waitcnt lgkmcnt(2)\n\t"
        "v_cvt_pk_bf16_f32 v50, v96, v97\n\t"
        "v_pk_add_f32 v[100:101], v[100:101], v[102:103]\n\t"
        "ds_write_b32 v35, v50\n\t"
        "v_cvt_pk_bf16_f32 v51, v100, v101\n\t"
        "ds_write_b32 v35, v51 offset:384\n\t"
        "v_pk_add_f32 v[56:57], v[56:57], v[60:61]\n\t"
        "s_cbranch_scc1 .Lend%=\n\t"
        "v_pk_add_f32 v[58:59], v[58:59], v[62:63]\n\t"
        "v_pk_add_f32 v[68:69], v[68:69], v[36:37]\n\t"
        "v_pk_add_f32 v[70:71], v[70:71], v[38:39]\n\t"
        "v_pk_add_f32 v[68:69], v[68:69], v[56:57]\n\t"
        "v_pk_add_f32 v[70:71], v[70:71], v[58:59]\n\t"
        "v_max3_f32 v2, v68, v69, v70\n\t"
        "v_max_f32_e32 v2, v2, v71\n\t"
        "s_nop 1\n\t"
        "v_max_f32_dpp v3, v2, v2 row_ror:8 row_mask:0xf bank_mask:0xf\n\t"
        "v_max_f32_dpp v1, v2, v2 row_ror:8 row_mask:0xf bank_mask:0xf\n\t"
        "s_nop 1\n\t"
        "v_permlane16_swap_b32_e32 v3, v1\n\t"
        "s_waitcnt lgkmcnt(0)\n\t"
        "s_barrier\n\t"
        "ds_read_b64_tr_b16 v[72:73], v32 offset:0\n\t"
        "v_max_f32_e32 v2, v3, v1\n\t"
        "ds_read_b64_tr_b16 v[74:75], v32 offset:128\n\t"
        "v_max_f32_e32 v3, v3, v1\n\t"
        "ds_read_b64_tr_b16 v[76:77], v32 offset:1536\n\t"
        "s_nop 0\n\t"
        "v_permlane32_swap_b32_e32 v2, v3\n\t"
        "ds_read_b64_tr_b16 v[78:79], v32 offset:1664\n\t"
        "v_max_f32_e32 v0, v2, v3\n\t"
        "v_mul_f32_e32 v2, 0xbe0293ee, v0\n\t"
        "v_fmamk_f32 v68, v68, 0x3e0293ee, v2\n\t"
        "v_fmamk_f32 v69, v69, 0x3e0293ee, v2\n\t"
        "v_fmamk_f32 v70, v70, 0x3e0293ee, v2\n\t"
        "v_fmamk_f32 v71, v71, 0x3e0293ee, v2\n\t"
        "v_exp_f32_e32 v68, v68\n\t"
        "s_waitcnt lgkmcnt(2)\n\t"
        "v_smfmac_f32_16x16x64_bf16 v[64:67], v[72:75], v[16:23], v122\n\t"
        "s_waitcnt lgkmcnt(0)\n\t"
        "v_smfmac_f32_16x16x64_bf16 v[64:67], v[76:79], v[24:31], v122\n\t"
        "s_waitcnt vmcnt(4)\n\t"
        "v_exp_f32_e32 v69, v69\n\t"
        "v_exp_f32_e32 v70, v70\n\t"
        "v_cvt_pk_bf16_f32 v36, v68, v69\n\t"
        "v_exp_f32_e32 v71, v71\n\t"
        "s_nop 0\n\t"
        "v_mov_b32_dpp v38, v36 row_ror:8 row_mask:0xf bank_mask:0xf\n\t"
        "v_cvt_pk_bf16_f32 v37, v70, v71\n\t"
        "s_nop 1\n\t"
        "v_mov_b32_dpp v39, v37 row_ror:8 row_mask:0xf bank_mask:0xf\n\t"
        "v_pk_add_f32 v[64:65], v[64:65], v[66:67]\n\t"
        "s_nop 0\n\t"
        "v_mfma_f32_16x16x32_bf16 v[96:99], v[12:15], v[36:39], 0\n\t"
        "v_fmamk_f32 v66, v64, 0x3e0293ee, v2\n\t"
        "v_fmamk_f32 v67, v65, 0x3e0293ee, v2\n\t"
        "v_mfma_f32_16x16x32_bf16 v[40:43], v[112:115], v[36:39], 0\n\t"
        "v_max_f32_e32 v3, v66, v67\n\t"
        "v_exp_f32_e32 v66, v66\n\t"
        "v_mfma_f32_16x16x32_bf16 v[44:47], v[116:119], v[36:39], 0\n\t"
        "v_cmp_nge_f32_e32 vcc, 0x41a00000, v3\n\t"
        "v_exp_f32_e32 v67, v67\n\t"
        "v_mov_b32_e32 v10, 1.0\n\t"
        "v_cvt_pk_bf16_f32 v90, v66, v67\n\t"
        "v_cvt_pk_bf16_f32 v95, v66, v67\n\t"
        "s_cbranch_vccnz .Lfm%=\n\t"
        ".Lrm%=:\n\t"
        "s_waitcnt vmcnt(1)\n\t"
        "v_mfma_f32_16x16x16_bf16 v[96:99], v[12:13], v[90:91], v[96:99]\n\t"
        "v_max_u32_e32 v54, v88, v89\n\t"
        "v_mfma_f32_16x16x16_bf16 v[40:43], v[88:89], v[90:91], v[40:43]\n\t"
        "v_cmp_eq_u32_e32 vcc, -1, v54\n\t"
        "v_mfma_f32_16x16x16_bf16 v[44:47], v[88:89], v[94:95], v[44:47]\n\t"
        "s_nop 1\n\t"
        "v_mov_b32_e32 v55, v55\n\t"
        "s_nop 0\n\t"
        "v_rcp_f32_e32 v2, v96\n\t"
        "s_cbranch_vccnz .Lfb%=\n\t"
        ".Lrb%=:\n\t"
        "v_pk_mul_f32 v[40:41], v[40:41], v[2:3] op_sel_hi:[1,0]\n\t"
        "v_pk_mul_f32 v[42:43], v[42:43], v[2:3] op_sel_hi:[1,0]\n\t"
        "v_cvt_pk_bf16_f32 v100, v40, v41\n\t"
        "v_pk_mul_f32 v[44:45], v[44:45], v[2:3] op_sel_hi:[1,0]\n\t"
        "v_pk_mul_f32 v[46:47], v[46:47], v[2:3] op_sel_hi:[1,0]\n\t"
        "v_cvt_pk_bf16_f32 v101, v42, v43\n\t"
        "v_cvt_pk_bf16_f32 v102, v44, v45\n\t"
        "v_min_u32_e32 v100, -2, v100\n\t"
        "v_cvt_pk_bf16_f32 v103, v46, v47\n\t"
        "v_min_u32_e32 v102, -2, v102\n\t"
        "buffer_store_dwordx4 v[100:103], v33, %[srd], s63 offen sc1\n\t"
        "s_memrealtime s[76:77]\n\t"
        "v_mov_b32_e32 v8, -1\n\t"
        "v_mov_b32_e32 v9, -1\n\t"
        "v_mov_b32_e32 v10, -1\n\t"
        "v_mov_b32_e32 v11, -1\n\t"
        "v_permlane16_swap_b32_e32 v50, v51\n\t"
        "v_permlane16_swap_b32_e32 v88, v89\n\t"
        "v_mov_b32_e32 v14, s40\n\t"
        "s_waitcnt lgkmcnt(0)\n\t"
        "s_waitcnt vmcnt(1)\n\t"
        "s_mov_b32 s97, 0\n\t"
        ".Lsr%=:\n\t"
        "s_mov_b32 exec_hi, 0xffff\n\t"
        "v_subrev_u32_e32 v0, s41, v84\n\t"
        "v_subrev_u32_e32 v1, s41, v86\n\t"
        "v_max_i32_e32 v0, v0, v1\n\t"
        "v_max_u32_e32 v1, v85, v87\n\t"
        "v_cmp_eq_u32_e32 vcc, -1, v1\n\t"
        "v_max_i32_dpp v0, v0, v0 quad_perm:[1,0,3,2] row_mask:0xf bank_mask:0xf\n\t"
        "v_or_b32_e32 v2, v85, v87\n\t"
        "s_nop 0\n\t"
        "v_max_i32_dpp v0, v0, v0 quad_perm:[2,3,0,1] row_mask:0xf bank_mask:0xf\n\t"
        "s_nop 1\n\t"
        "v_max_i32_dpp v0, v0, v0 row_ror:4 row_mask:0xf bank_mask:0xf\n\t"
        "s_nop 1\n\t"
        "v_max_i32_dpp v0, v0, v0 row_ror:8 row_mask:0xf bank_mask:0xf\n\t"
        "s_cbranch_vccnz .Lsf%=\n\t"
        "v_readlane_b32 s85, v0, 0\n\t"
        "v_readlane_b32 s81, v0, 16\n\t"
        "v_readlane_b32 s56, v0, 32\n\t"
        "v_and_b32_e32 v1, 1, v2\n\t"
        "v_cmp_ne_u32_e64 s[86:87], 0, v1\n\t"
        "v_and_b32_e32 v1, 2, v2\n\t"
        "v_cmp_ne_u32_e64 s[88:89], 0, v1\n\t"
        "v_and_b32_e32 v1, 4, v2\n\t"
        "v_cmp_ne_u32_e64 s[90:91], 0, v1\n\t"
        "s_max_i32 s56, s56, s81\n\t"
        "s_add_u32 s92, s85, s41\n\t"
        "s_sub_u32 s92, s92, s43\n\t"
        "s_add_i32 s93, s56, s47\n\t"
        "s_mov_b32 s44, s46\n\t"
        "s_mov_b32 s46, s45\n\t"
        "s_mov_b32 s94, s48\n\t"
        "s_mov_b32 s43, s41\n\t"
        "s_cmp_gt_i32 s56, s54\n\t"
        "s_cbranch_scc1 .Lmv%=\n\t"
        "s_mov_b32 s37, 0\n\t"
        "s_mov_b32 s48, 0\n\t"
        "s_mov_b32 s95, 0\n\t"
        ".Lsd%=:\n\t"
        "s_mov_b32 exec_hi, -1\n\t"
        "s_add_u32 s42, s43, s46\n\t"
        "s_sleep %c[HSLP]\n\t"
        "buffer_load_dwordx2 v[48:49], off, %[wsd], s64 sc1\n\t"
        "buffer_load_dwordx4 v[104:107], v120, %[wsd], s69 offen sc1\n\t"
        "buffer_load_dwordx4 v[108:111], v120, %[wsd], s70 offen sc1\n\t"
        "buffer_store_dwordx2 v[50:51], v34, %[wsd], s72 offen sc1\n\t"
        "buffer_store_dwordx2 v[88:89], v34, %[wsd], s72 offen offset:2048 sc1\n\t"
        "s_lshl_b32 s80, %[w], 8\n\t"
        "s_add_u32 s80, s80, s61\n\t"
        "s_add_u32 s80, s80, 24576\n\t"
        "buffer_store_dwordx2 v[8:9], v34, %[srd], s80 offen sc1\n\t"
        "s_cmp_eq_u32 %[w], 0\n\t"
        "s_cselect_b32 exec_hi, 0, -1\n\t"
        "buffer_store_dwordx4 v[8:11], v120, %[srd], s68 offen sc1\n\t"
        "s_mov_b32 exec_hi, -1\n\t"
        "s_mov_b64 exec, 1\n\t"
        "buffer_store_dword v14, off, %[wsd], s74 sc1\n\t"
        "s_mov_b64 exec, -1\n\t"
        "ds_read_b128 v[16:19], v121 offset:0\n\t"
        "ds_read_b128 v[20:23], v121 offset:1024\n\t"
        "ds_read_b128 v[24:27], v121 offset:2048\n\t"
        "ds_read_b128 v[28:31], v121 offset:3072\n\t"
        "ds_read_b128 v[32:35], v121 offset:4096\n\t"
        "ds_read_b128 v[36:39], v121 offset:5120\n\t"
        "ds_read_b128 v[40:43], v121 offset:6144\n\t"
        "ds_read_b128 v[44:47], v121 offset:7168\n\t"
        "ds_read_b128 v[56:59], v121 offset:8192\n\t"
        "ds_read_b128 v[60:63], v121 offset:9216\n\t"
        "ds_read_b128 v[64:67], v121 offset:10240\n\t"
        "ds_read_b128 v[68:71], v121 offset:11264\n\t"
        "ds_read_b128 v[72:75], v121 offset:12288\n\t"
        "ds_read_b128 v[76:79], v121 offset:13312\n\t"
        "ds_read_b128 v[80:83], v121 offset:14336\n\t"
        "ds_read_b128 v[84:87], v121 offset:15360\n\t"
        "s_cmp_ge_u32 s40, %c[iCT0]\n\t"
        "s_cselect_b32 s85, 1, 0\n\t"
        "s_cmp_lg_u64 s[86:87], 0\n\t"
        "s_cselect_b32 s86, s85, 0\n\t"
        "s_cmp_lg_u64 s[88:89], 0\n\t"
        "s_cselect_b32 s87, s85, 0\n\t"
        "s_cmp_lg_u64 s[90:91], 0\n\t"
        "s_cselect_b32 s88, s85, 0\n\t"
        "s_or_b32 s80, s94, s86\n\t"
        "s_or_b32 s80, s80, s87\n\t"
        "s_andn2_b32 s90, s85, s80\n\t"
        "s_cmp_le_i32 s92, s44\n\t"
        "s_cselect_b32 s80, s85, 0\n\t"
        "s_andn2_b32 s91, s80, s88\n\t"
        "s_and_b32 s80, s40, %c[LDEC]\n\t"
        "s_cselect_b32 s89, 0, 1\n\t"
        "s_andn2_b32 s80, s86, s94\n\t"
        "s_cmp_lt_i32 s51, %c[LA24]\n\t"
        "s_cselect_b32 s80, s80, 0\n\t"
        "s_lshl2_add_u32 s51, s80, s51\n\t"
        "s_sub_i32 s51, s51, s89\n\t"
        "s_max_i32 s51, s51, %c[LA]\n\t"
        "s_lshl_b32 s80, s93, 1\n\t"
        "s_sub_i32 s81, s80, s53\n\t"
        "s_ashr_i32 s82, s81, %c[UPSH]\n\t"
        "s_max_i32 s82, s82, %c[TRUP]\n\t"
        "s_add_i32 s82, s82, s53\n\t"
        "s_min_i32 s82, s82, s80\n\t"
        "s_ashr_i32 s83, s81, %c[DNSH]\n\t"
        "s_min_i32 s83, s83, -%c[TRDN]\n\t"
        "s_add_i32 s83, s83, s53\n\t"
        "s_max_i32 s83, s83, s80\n\t"
        "s_cmp_gt_i32 s81, 0\n\t"
        "s_cselect_b32 s82, s82, s83\n\t"
        "s_cmp_lg_u32 s91, 0\n\t"
        "s_cselect_b32 s53, s82, s53\n\t"
        "s_add_i32 s80, s53, 1\n\t"
        "s_ashr_i32 s80, s80, 1\n\t"
        "s_add_i32 s47, s80, s51\n\t"
        "s_add_u32 s81, s47, %c[AUP]\n\t"
        "s_min_i32 s80, s95, s81\n\t"
        "s_max_i32 s47, s47, s80\n\t"
        "s_add_u32 s41, s42, s47\n\t"
        "s_cmp_eq_u32 s40, 0\n\t"
        "s_cselect_b32 s51, %c[LA], s51\n\t"
        "v_mov_b32_e32 v88, 0\n\t"
        "v_mov_b32_e32 v89, 0\n\t"
        "v_mov_b32_e32 v90, 0\n\t"
        "v_mov_b32_e32 v91, 0\n\t"
        "v_mov_b32_e32 v92, 0\n\t"
        "v_mov_b32_e32 v93, 0\n\t"
        "v_mov_b32_e32 v94, 0\n\t"
        "v_mov_b32_e32 v95, 0\n\t"
        "v_mov_b32_e32 v96, 0\n\t"
        "v_mov_b32_e32 v97, 0\n\t"
        "v_mov_b32_e32 v98, 0\n\t"
        "v_mov_b32_e32 v99, 0\n\t"
        "v_mov_b32_e32 v100, 0\n\t"
        "v_mov_b32_e32 v101, 0\n\t"
        "v_mov_b32_e32 v102, 0\n\t"
        "v_mov_b32_e32 v103, 0\n\t"
        "s_add_u32 s40, s40, 1\n\t"
        "v_mov_b32_e32 v6, 30720\n\t"
        "ds_read_b64 v[6:7], v6\n\t"
        "s_waitcnt lgkmcnt(0)\n\t"
        "v_readfirstlane_b32 s80, v6\n\t"
        "v_readfirstlane_b32 s81, v7\n\t"
        "s_cmp_eq_u32 s80, s40\n\t"
        "s_cselect_b32 s84, 1, 0\n\t"
        "s_cmp_eq_u32 s81, s40\n\t"
        "s_cselect_b32 s81, 2, 0\n\t"
        "s_or_b32 s84, s84, s81\n\t"
        "s_mov_b32 s85, s79\n\t"
        "s_and_b32 s80, s40, 3\n\t"
        "s_lshl_b32 s80, s80, 17\n\t"
        "s_add_u32 s57, s80, %[nA]\n\t"
        "s_add_u32 s58, s57, 0x1000\n\t"
        "s_add_u32 s59, s57, 0x8000\n\t"
        "s_add_u32 s60, s57, 0x9000\n\t"
        "s_and_b32 s80, s40, 3\n\t"
        "s_lshl_b32 s80, s80, 17\n\t"
        "s_add_u32 s63, s80, %[nC]\n\t"
        "s_add_u32 s66, s80, %[nS]\n\t"
        "s_add_u32 s81, s80, 0x20000\n\t"
        "s_and_b32 s81, s81, 0x60000\n\t"
        "s_add_u32 s67, s81, %[nW]\n\t"
        "s_add_u32 s79, s81, %[nT]\n\t"
        "s_cmp_eq_u32 %[w], 2\n\t"
        "s_cselect_b32 s81, s80, s81\n\t"
        "s_xor_b32 s81, s81, 0x40000\n\t"
        "s_add_u32 s68, s81, %[pob]\n\t"
        "s_lshl_b32 s72, s40, 17\n\t"
        "s_add_u32 s72, s72, %[kvo]\n\t"
        "v_lshlrev_b32_e32 v51, 6, v120\n\t"
        "s_cmp_eq_u32 %[w], 0\n\t"
        "s_cselect_b32 s73, s85, s36\n\t"
        "s_cselect_b32 s74, %[pra], s36\n\t"
        "v_mov_b32_e32 v50, s40\n\t"
        "s_cmp_ge_u32 s40, 60\n\t"
        "s_cselect_b32 s65, s74, s36\n\t"
        "s_mov_b32 s80, s61\n\t"
        "s_mov_b32 s61, s62\n\t"
        "s_mov_b32 s62, s80\n\t"
        "s_waitcnt vmcnt(7)\n\t"
        "s_cmp_eq_u32 s64, s36\n\t"
        "s_cbranch_scc1 .Lk11%=\n\t"
        "s_mov_b32 s97, 0\n\t"
        ".Lk01%=:\n\t"
        "v_readfirstlane_b32 s82, v48\n\t"
        "v_readfirstlane_b32 s83, v49\n\t"
        "s_xor_b32 s82, s82, %[tag]\n\t"
        "s_xor_b32 s83, s83, %[tag]\n\t"
        "s_or_b32 s82, s82, s83\n\t"
        "s_cbranch_scc0 .Lk11%=\n\t"
        "s_sleep 3\n\t"
        "buffer_load_dwordx2 v[48:49], off, %[wsd], s64 sc1\n\t"
        "s_add_u32 s97, s97, 1\n\t"
        "s_cmp_lt_u32 s97, 0x200000\n\t"
        "s_cbranch_scc0 .Lgu%=\n\t"
        "s_waitcnt vmcnt(0)\n\t"
        "s_branch .Lk01%=\n\t"
        ".Lk11%=:\n\t"
        "v_mov_b32_e32 v48, s76\n\t"
        "v_mov_b32_e32 v49, s84\n\t"
        "s_branch .Lloop%=\n\t"
        ".Lmv%=:\n\t"
        ".Lhk0%=:\n\t"
        "s_cbranch_execz .Lhk1%=\n\t"
        "s_mov_b32 s48, 1\n\t"
        "s_cmp_eq_u32 s54, %c[iTOL]\n\t"
        "s_cbranch_scc1 .Lm1%=\n\t"
        "s_max_i32 s80, s85, s56\n\t"
        "s_add_u32 s43, s41, s80\n\t"
        "s_add_u32 s43, s43, %c[iPEN0]\n\t"
        ".Lhk1%=:\n\t"
        "s_cbranch_execz .Lhk2%=\n\t"
        "s_mov_b32 s54, %c[iTOL]\n\t"
        "s_add_u32 s46, s46, %c[iX0C]\n\t"
        "s_mov_b32 s95, 0\n\t"
        "s_add_u32 s51, s51, %c[iX0A]\n\t"
        "v_and_b32_e32 v0, 3, v84\n\t"
        "v_and_b32_e32 v1, 3, v86\n\t"
        ".Lhk2%=:\n\t"
        "s_cbranch_execz .Lhk3%=\n\t"
        "v_lshlrev_b32_e64 v0, v0, 1\n\t"
        "v_lshlrev_b32_e64 v1, v1, 1\n\t"
        "v_or_b32_e32 v0, v0, v1\n\t"
        "s_mov_b32 s80, 0\n\t"
        "v_and_b32_e32 v1, 1, v0\n\t"
        "v_cmp_ne_u32_e32 vcc, 0, v1\n\t"
        ".Lhk3%=:\n\t"
        "s_cbranch_execz .Lhk4%=\n\t"
        "s_nop 1\n\t"
        "s_cmp_lg_u64 vcc, 0\n\t"
        "s_cselect_b32 s81, 2, 0\n\t"
        "s_or_b32 s80, s80, s81\n\t"
        "v_and_b32_e32 v1, 2, v0\n\t"
        "v_cmp_ne_u32_e32 vcc, 0, v1\n\t"
        ".Lhk4%=:\n\t"
        "s_cbranch_execz .Lhk5%=\n\t"
        "s_nop 1\n\t"
        "s_cmp_lg_u64 vcc, 0\n\t"
        "s_cselect_b32 s81, 4, 0\n\t"
        "s_or_b32 s80, s80, s81\n\t"
        "v_and_b32_e32 v1, 4, v0\n\t"
        "v_cmp_ne_u32_e32 vcc, 0, v1\n\t"
        ".Lhk5%=:\n\t"
        "s_cbranch_execz .Lhk6%=\n\t"
        "s_nop 1\n\t"
        "s_cmp_lg_u64 vcc, 0\n\t"
        "s_cselect_b32 s81, 8, 0\n\t"
        "s_or_b32 s80, s80, s81\n\t"
        "v_and_b32_e32 v1, 8, v0\n\t"
        "v_cmp_ne_u32_e32 vcc, 0, v1\n\t"
        ".Lhk6%=:\n\t"
        "s_cbranch_execz .Lhk7%=\n\t"
        "s_nop 1\n\t"
        "s_cmp_lg_u64 vcc, 0\n\t"
        "s_cselect_b32 s81, 16, 0\n\t"
        "s_or_b32 s80, s80, s81\n\t"
        "s_lshr_b32 s80, 0x1adf1210, s80\n\t"
        "s_and_b32 s55, s80, 3\n\t"
        ".Lhk7%=:\n\t"
        "s_cbranch_execz .Lhk8%=\n\t"
        "s_branch .Lal%=\n\t"
        ".Lm1%=:\n\t"
        "s_add_u32 s80, s37, %c[iPEN2]\n\t"
        "s_min_i32 s80, s80, %c[iPENM]\n\t"
        "s_cmp_lg_u32 s37, 0\n\t"
        "s_cselect_b32 s37, s80, %c[iPEN]\n\t"
        "s_add_u32 s80, s56, s37\n\t"
        ".Lhk8%=:\n\t"
        "s_cbranch_execz .Lhk9%=\n\t"
        "s_max_i32 s80, s80, %c[iPMIN]\n\t"
        "s_add_u32 s43, s41, s80\n\t"
        "s_add_u32 s95, s93, %c[LA]\n\t"
        "s_add_u32 s46, s46, %c[iXR]\n\t"
        ".Lal%=:\n\t"
        "s_sub_u32 s80, s55, s43\n\t"
        "s_and_b32 s80, s80, %c[iPHM]\n\t"
        ".Lhk9%=:\n\t"
        "s_cbranch_execz .Lhk10%=\n\t"
        "s_add_u32 s43, s43, s80\n\t"
        "s_branch .Lsd%=\n\t"
        ".Lsf%=:\n\t"
        "s_mov_b32 exec_hi, -1\n\t"
        "s_sleep 2\n\t"
        "buffer_load_dwordx4 v[84:87], v120, %[srd], s66 offen sc1\n\t"
        "s_add_u32 s97, s97, 1\n\t"
        ".Lhk10%=:\n\t"
        "s_cbranch_execz .Lhk11%=\n\t"
        "s_cmp_lt_u32 s97, 0x100000\n\t"
        "s_cbranch_scc0 .Lgu%=\n\t"
        "s_waitcnt vmcnt(0)\n\t"
        "s_branch .Lsr%=\n\t"
        ".Lf1%=:\n\t"
        "v_max_u32_e32 v54, v0, v2\n\t"
        "v_max3_u32 v54, v54, v1, v3\n\t"
        ".Lhk11%=:\n\t"
        "s_cbranch_execz .Lhk12%=\n\t"
        "v_max3_u32 v54, v54, v4, v6\n\t"
        "v_max3_u32 v54, v54, v5, v7\n\t"
        "v_max3_u32 v54, v54, v8, v10\n\t"
        "v_max3_u32 v54, v54, v9, v11\n\t"
        "s_sleep %c[RSLP]\n\t"
        "buffer_load_dwordx4 v[0:3], v120, %[srd], s57 offen sc1\n\t"
        ".Lhk12%=:\n\t"
        "s_cbranch_execz .Lhk13%=\n\t"
        "buffer_load_dwordx4 v[4:7], v120, %[srd], s58 offen sc1\n\t"
        "buffer_load_dwordx4 v[8:11], v120, %[srd], s59 offen sc1\n\t"
        "buffer_load_dwordx4 v[12:15], v120, %[srd], s60 offen sc1\n\t"
        "buffer_store_dword v0, off, %[wsd], s36\n\t"
        "buffer_store_dword v0, off, %[wsd], s36\n\t"
        "buffer_store_dword v0, off, %[wsd], s36\n\t"
        ".Lhk13%=:\n\t"
        "s_cbranch_execz .Lhk14%=\n\t"
        "ds_read_b128 v[92:95], v120 offset:0\n\t"
        "ds_read_b128 v[68:71], v120 offset:4096\n\t"
        "v_max3_u32 v54, v54, v16, v18\n\t"
        "v_max3_u32 v54, v54, v17, v19\n\t"
        "v_max3_u32 v54, v54, v20, v22\n\t"
        "v_max3_u32 v54, v54, v21, v23\n\t"
        ".Lhk14%=:\n\t"
        "s_cbranch_execz .Lhk15%=\n\t"
        "v_max3_u32 v54, v54, v24, v26\n\t"
        "v_max3_u32 v54, v54, v25, v27\n\t"
        "v_max3_u32 v54, v54, v28, v30\n\t"
        "v_max3_u32 v54, v54, v29, v31\n\t"
        "v_max3_u32 v54, v54, v32, v34\n\t"
        "v_max3_u32 v54, v54, v33, v35\n\t"
        ".Lhk15%=:\n\t"
        "s_cbranch_execz .Lhk16%=\n\t"
        "v_max3_u32 v54, v54, v36, v38\n\t"
        "v_max3_u32 v54, v54, v37, v39\n\t"
        "v_max3_u32 v54, v54, v40, v42\n\t"
        "v_max3_u32 v54, v54, v41, v43\n\t"
        "v_max3_u32 v54, v54, v44, v46\n\t"
        "v_max3_u32 v54, v54, v45, v47\n\t"
        ".Lhk16%=:\n\t"
        "s_cbranch_execz .Lhk17%=\n\t"
        "v_max3_u32 v54, v54, v48, v50\n\t"
        "v_max3_u32 v54, v54, v49, v51\n\t"
        "s_waitcnt lgkmcnt(0)\n\t"
        "v_max3_u32 v54, v54, v92, v94\n\t"
        "v_max3_u32 v54, v54, v93, v95\n\t"
        "v_max3_u32 v54, v54, v68, v70\n\t"
        ".Lhk17%=:\n\t"
        "s_cbranch_execz .Lhk18%=\n\t"
        "v_max3_u32 v54, v54, v69, v71\n\t"
        "ds_read_b128 v[92:95], v120 offset:8192\n\t"
        "ds_read_b128 v[68:71], v120 offset:12288\n\t"
        "s_waitcnt lgkmcnt(0)\n\t"
        "v_max3_u32 v54, v54, v92, v94\n\t"
        "v_max3_u32 v54, v54, v93, v95\n\t"
        ".Lhk18%=:\n\t"
        "s_cbranch_execz .Lhk19%=\n\t"
        "v_max3_u32 v54, v54, v68, v70\n\t"
        "v_max3_u32 v54, v54, v69, v71\n\t"
        "v_cmp_eq_u32_e32 vcc, -1, v54\n\t"
        "s_nop 1\n\t"
        "v_mov_b32_e32 v55, v55\n\t"
        "s_nop 0\n\t"
        ".Lhk19%=:\n\t"
        "s_cbranch_execz .Lhk20%=\n\t"
        "s_cbranch_vccz .Lf2%=\n\t"
        "ds_read_b128 v[16:19], v121 offset:0\n\t"
        "ds_read_b128 v[20:23], v121 offset:1024\n\t"
        "ds_read_b128 v[24:27], v121 offset:2048\n\t"
        "ds_read_b128 v[28:31], v121 offset:3072\n\t"
        "ds_read_b128 v[32:35], v121 offset:4096\n\t"
        ".Lhk20%=:\n\t"
        "s_cbranch_execz .Lhk21%=\n\t"
        "ds_read_b128 v[36:39], v121 offset:5120\n\t"
        "ds_read_b128 v[40:43], v121 offset:6144\n\t"
        "ds_read_b128 v[44:47], v121 offset:7168\n\t"
        "ds_read_b128 v[56:59], v121 offset:8192\n\t"
        "ds_read_b128 v[60:63], v121 offset:9216\n\t"
        "ds_read_b128 v[64:67], v121 offset:10240\n\t"
        ".Lhk21%=:\n\t"
        "s_cbranch_execz .Lhk22%=\n\t"
        "ds_read_b128 v[68:71], v121 offset:11264\n\t"
        "ds_read_b128 v[72:75], v121 offset:12288\n\t"
        "ds_read_b128 v[76:79], v121 offset:13312\n\t"
        "ds_read_b128 v[80:83], v121 offset:14336\n\t"
        "ds_read_b128 v[84:87], v121 offset:15360\n\t"
        "s_add_u32 s80, s40, 1\n\t"
        ".Lhk22%=:\n\t"
        "s_cbranch_execz .Lhk23%=\n\t"
        "v_mov_b32_e32 v54, 30720\n\t"
        "v_mov_b32_e32 v55, s80\n\t"
        "ds_write_b32 v54, v55\n\t"
        "s_add_u32 s97, s97, 1\n\t"
        "s_cmp_lt_u32 s97, 0x100000\n\t"
        "s_cbranch_scc0 .Lgu%=\n\t"
        ".Lhk23%=:\n\t"
        "s_cbranch_execz .Lhk24%=\n\t"
        "s_mov_b32 s65, s36\n\t"
        "v_mov_b32_e32 v88, 0\n\t"
        "v_mov_b32_e32 v89, 0\n\t"
        "v_mov_b32_e32 v90, 0\n\t"
        "v_mov_b32_e32 v91, 0\n\t"
        "v_mov_b32_e32 v92, 0\n\t"
        ".Lhk24%=:\n\t"
        "s_cbranch_execz .Lhk25%=\n\t"
        "v_mov_b32_e32 v93, 0\n\t"
        "v_mov_b32_e32 v94, 0\n\t"
        "v_mov_b32_e32 v95, 0\n\t"
        "v_mov_b32_e32 v96, 0\n\t"
        "v_mov_b32_e32 v97, 0\n\t"
        "v_mov_b32_e32 v98, 0\n\t"
        ".Lhk25%=:\n\t"
        "s_cbranch_execz .Lhk26%=\n\t"
        "v_mov_b32_e32 v99, 0\n\t"
        "v_mov_b32_e32 v100, 0\n\t"
        "v_mov_b32_e32 v101, 0\n\t"
        "v_mov_b32_e32 v102, 0\n\t"
        "v_mov_b32_e32 v103, 0\n\t"
        "s_waitcnt lgkmcnt(0)\n\t"
        ".Lhk26%=:\n\t"
        "s_cbranch_execz .Lhk27%=\n\t"
        "s_barrier\n\t"
        "s_branch .Lgo%=\n\t"
        ".Lf2%=:\n\t"
        "s_waitcnt vmcnt(0)\n\t"
        "v_mov_b32_e32 v12, 0x3f803f80\n\t"
        "v_mov_b32_e32 v13, 0x3f803f80\n\t"
        "v_mov_b32_e32 v14, 0x3f803f80\n\t"
        ".Lhk27%=:\n\t"
        "s_cbranch_execz .Lhk28%=\n\t"
        "v_mov_b32_e32 v15, 0x3f803f80\n\t"
        "ds_read_b128 v[0:3], v124 offset:12288\n\t"
        "ds_read_b128 v[4:7], v125 offset:12288\n\t"
        "ds_read_b128 v[8:11], v126 offset:12288\n\t"
        "s_waitcnt lgkmcnt(0)\n\t"
        "s_branch .Lr1%=\n\t"
        ".Lfb%=:\n\t"
        ".Lhk28%=:\n\t"
        "s_cbranch_execz .Lhk29%=\n\t"
        "s_nop 7\n\t"
        "s_mov_b32 s97, 0\n\t"
        "s_add_u32 s80, s40, 1\n\t"
        "v_mov_b32_e32 v54, 30724\n\t"
        "v_mov_b32_e32 v55, s80\n\t"
        "ds_write_b32 v54, v55\n\t"
        ".Lfp%=:\n\t"
        ".Lhk29%=:\n\t"
        "s_cbranch_execz .Lhk30%=\n\t"
        "s_sleep %c[ASLP]\n\t"
        "buffer_load_dword v88, v127, %[srd], s61 offen offset:0 sc1\n\t"
        "buffer_load_dword v89, v127, %[srd], s61 offen offset:256 sc1\n\t"
        "s_add_u32 s97, s97, 1\n\t"
        "s_cmp_lt_u32 s97, 0x100000\n\t"
        "s_cbranch_scc0 .Lgu%=\n\t"
        ".Lhk30%=:\n\t"
        "s_cbranch_execz .Lhk31%=\n\t"
        "s_waitcnt vmcnt(0)\n\t"
        "v_max_u32_e32 v54, v88, v89\n\t"
        "v_cmp_eq_u32_e32 vcc, -1, v54\n\t"
        "s_nop 1\n\t"
        "v_mov_b32_e32 v55, v55\n\t"
        "s_nop 0\n\t"
        ".Lhk31%=:\n\t"
        "s_cbranch_execz .Lhk32%=\n\t"
        "s_cbranch_vccnz .Lfp%=\n\t"
        "v_mfma_f32_16x16x32_bf16 v[40:43], v[112:115], v[36:39], 0\n\t"
        "v_mfma_f32_16x16x32_bf16 v[44:47], v[116:119], v[36:39], 0\n\t"
        "s_nop 1\n\t"
        "v_mov_b32_e32 v55, v55\n\t"
        "s_nop 1\n\t"
        ".Lhk32%=:\n\t"
        "s_cbranch_execz .Lhk33%=\n\t"
        "v_mov_b32_e32 v55, v55\n\t"
        "s_nop 0\n\t"
        "v_pk_mul_f32 v[40:41], v[40:41], v[10:11] op_sel_hi:[1,0]\n\t"
        "v_pk_mul_f32 v[42:43], v[42:43], v[10:11] op_sel_hi:[1,0]\n\t"
        "v_pk_mul_f32 v[44:45], v[44:45], v[10:11] op_sel_hi:[1,0]\n\t"
        "v_pk_mul_f32 v[46:47], v[46:47], v[10:11] op_sel_hi:[1,0]\n\t"
        ".Lhk33%=:\n\t"
        "s_cbranch_execz .Lhk34%=\n\t"
        "v_mfma_f32_16x16x16_bf16 v[40:43], v[88:89], v[90:91], v[40:43]\n\t"
        "s_nop 0\n\t"
        "v_mfma_f32_16x16x16_bf16 v[44:47], v[88:89], v[94:95], v[44:47]\n\t"
        "s_nop 7\n\t"
        "s_branch .Lrb%=\n\t"
        ".Lfm%=:\n\t"
        "v_max3_f32 v3, v0, v64, v65\n\t"
        ".Lhk34%=:\n\t"
        "s_cbranch_execz .Lhk35%=\n\t"
        "v_max3_f32 v1, v0, v64, v65\n\t"
        "s_nop 1\n\t"
        "v_permlane16_swap_b32_e32 v3, v1\n\t"
        "v_max_f32_e32 v2, v3, v1\n\t"
        "v_max_f32_e32 v3, v3, v1\n\t"
        "s_nop 1\n\t"
        ".Lhk35%=:\n\t"
        "s_cbranch_execz .Lhk36%=\n\t"
        "v_permlane32_swap_b32_e32 v2, v3\n\t"
        "v_max_f32_e32 v2, v2, v3\n\t"
        "v_mul_f32_e32 v2, 0xbe0293ee, v2\n\t"
        "v_fmamk_f32 v10, v0, 0x3e0293ee, v2\n\t"
        "v_fmamk_f32 v66, v64, 0x3e0293ee, v2\n\t"
        "v_exp_f32_e32 v10, v10\n\t"
        ".Lhk36%=:\n\t"
        "s_cbranch_execz .Lhk37%=\n\t"
        "v_fmamk_f32 v67, v65, 0x3e0293ee, v2\n\t"
        "v_exp_f32_e32 v66, v66\n\t"
        "v_exp_f32_e32 v67, v67\n\t"
        "v_mul_f32_e32 v96, v96, v10\n\t"
        "v_pk_mul_f32 v[40:41], v[40:41], v[10:11] op_sel_hi:[1,0]\n\t"
        "v_pk_mul_f32 v[42:43], v[42:43], v[10:11] op_sel_hi:[1,0]\n\t"
        ".Lhk37%=:\n\t"
        "s_cbranch_execz .Lhk38%=\n\t"
        "v_pk_mul_f32 v[44:45], v[44:45], v[10:11] op_sel_hi:[1,0]\n\t"
        "v_pk_mul_f32 v[46:47], v[46:47], v[10:11] op_sel_hi:[1,0]\n\t"
        "v_cvt_pk_bf16_f32 v90, v66, v67\n\t"
        "v_cvt_pk_bf16_f32 v95, v66, v67\n\t"
        "s_nop 1\n\t"
        "s_branch .Lrm%=\n\t"
        ".Lta%=:\n\t"
        ".Lhk38%=:\n\t"
        "s_cbranch_execz .Lhk39%=\n\t"
        "s_waitcnt lgkmcnt(0)\n\t"
        "s_branch .Lxa%=\n\t"
        ".Lgu%=:\n\t"
        "s_mov_b64 exec, -1\n\t"
        "v_mov_b32_e32 v54, %c[ferr]\n\t"
        "v_mov_b32_e32 v55, 1\n\t"
        "buffer_store_dword v55, v54, %[wsd], 0 offen sc1\n\t"
        ".Lhk39%=:\n\t"
        "s_cbranch_execz .Lhk40%=\n\t"
        "s_endpgm\n\t"
        ".Lend%=:\n\t"
        "v_min_u32_e32 v50, -2, v50\n\t"
        "v_min_u32_e32 v51, -2, v51\n\t"
        "s_nop 1\n\t"
        "v_permlane16_swap_b32_e32 v50, v51\n\t"
        "buffer_store_dwordx2 v[50:51], v34, %[wsd], s72 offen sc1\n\t"
        ".Lhk40%=:\n\t"
        "s_cbranch_execz .Lhk41%=\n\t"
        "s_endpgm\n\t"
        ".Lhk41%=:\n\t"
        "s_branch .Lhr%="
        : [nA] "+s"(nA), [nS] "+s"(nS), [nT] "+s"(nT), [nW] "+s"(nW), [nC] "+s"(nC), [b0] "+s"(b0), [pob] "+s"(pob)
        : [srd] "s"(rhd), [wsd] "s"(wsd), [w] "s"(w), [tag] "s"(tag), [kx] "s"(kx), [vx] "s"(vx), [kf] "s"(kf), [kvo] "s"(kvo), [cal] "s"(cal), [sq] "s"(sq), [sk] "s"(sk), [m0b] "s"(m0b),
          [pra] "s"(pr_a), [ferr] "i"(F_ERR), [iD1] "i"(D1), [iD2] "i"(D2), [LC] "i"(LC0), [LA] "i"(LA0), [LC24] "i"(LC0 + 24), [LA24] "i"(LA0 + 24), [E1] "i"(2 * (D1 - LC0)), [E2] "i"(2 * (D2 - LA0)),
          [iPEN] "i"(PEN), [iPEN2] "i"(PEN2), [iPENM] "i"(PEN + 2 * PEN2), [iPEN0] "i"(PEN0), [iTOL] "i"(TOL), [iCT0] "i"(CT0), [iX0C] "i"(X0C), [iX0A] "i"(X0A), [iPHM] "i"(PH_M),
          [iFT] "i"(-3), [iFN] "i"(FINE_N), [AUP] "i"(A_UP), [ASLP] "i"(A_SLP), [LDEC] "i"(L_DEC), [UPSH] "i"(UP_SH), [TRUP] "i"(TR_UP), [TRDN] "i"(TR_DN), [HSLP] "i"(H_SLP), [iPMIN] "i"(PMIN), [VSLP] "i"(V_SLP), [RSLP] "i"(R_SLP), [iVD] "i"(V_DEL), [iXR] "i"(XR), [VASL] "i"(VA_SLP), [CSLP] "i"(C_SLP), [DNSH] "i"(DN_SH), [NX0] "i"(NEARX), [NX1] "i"(8192 - NEARX), [iCS1] "i"(CS1), [iCS2] "i"(CS2), [NY0] "i"(NEARY), [NY1] "i"(16384 - NEARY),
          "{v[120:127]}"(pc), "{v[112:113]}"(pd), "{v96}"(dv), "{v[98:105]}"(pa)
        : "memory", "vcc", "scc", "m0", "s36", "s37", "s38", "s39", "s40", "s41", "s42", "s43", "s44", "s45", "s46", "s47", "s48", "s49", "s50", "s51", "s52", "s53", "s54", "s55", "s56", "s57", "s58", "s59", "s60", "s61", "s62", "s63", "s64", "s65", "s66", "s67", "s68", "s69", "s70", "s71", "s72", "s73", "s74", "s75", "s76", "s77", "s78", "s79", "s80", "s81", "s82", "s83", "s84", "s85", "s86", "s87", "s88", "s89", "s90", "s91", "s92", "s93", "s94", "s95", "s96", "s97", "s98", "s99");
    } else {
        const int cb = i32;
        const v4u sv = mk_srd(p.Wv, 1024 * 4096 * 2), so = mk_srd(p.Wo, 1024 * 4096 * 2);
        const unsigned key = KEYM & ((cb & 1) * 2 + G), tu = cb >> 2, m0b = w * 32768, f = (g & 1) | ((c >> 1) << 1);
        const unsigned t0 = m0b + (16 * (g >> 1) + 4 * (g & 1) + (c & 1) + 8 * (c >> 1)) * 128 + (f << 5) + (n16 & 3) * 8, t1 = m0b + (16 * g + 4 * (c >> 1) + (c & 1)) * 64 + ((c >> 1) << 5) + (n16 & 3) * 8;
        v8u pa;
        pa[0] = (64 * w + (l >> 3)) * 8192 + vi * 128 + (((l & 7) ^ ((l >> 5) << 1)) << 4); pa[1] = pa[0] ^ 64u;
        pa[2] = (1024 * w + (l >> 2)) * 2048 + cb * 64 + (((l & 3) ^ ((g & 1) << 1)) << 4);
#pragma unroll
        for (int q = 0; q < 4; ++q) pa[3 + q] = t0 ^ (((w + q) & 3) << 5);
        pa[7] = t1;
        const v2u pb = {t1 ^ 32u, (unsigned)((w * 1024 + cb * 32) * 2048 + l * 1024) + (((3u - (so.x >> 7)) & 7u) << 7)};
        const v16u pc = {(unsigned)tid * 16, (n16 & 2) ? 0xEEEEu : 0x4444u, (unsigned)((vi & 1) * 1024 + w * 256 + n16 * 16 + g * 4), (unsigned)(4096 + tid * 16),
                   w < 3 ? (unsigned)((cb >> 4) * 32768 + ((cb >> 1) & 7) * 1024 + ((cb & 1) * 2 + (l >> 5)) * 256 + G * 128 + (l & 31) * 4) : OOB,
                   tid < 3 ? (unsigned)(l * 1024 + (32 + vi) * 8) : OOB, 0u, (unsigned)(L_V + 8192 + w * 512 + (g & 1) * 256 + (g >> 1) * 64 + n16 * 4),
                   (unsigned)(L_V + (((l >> 2) & 1) + ((l >> 3) & 2)) * 128 + (l >> 5) * 64 + (l & 8) * 4 + (l & 3) * 8), 0u,
                   (unsigned)(L_V + w * 2048 + l * 8), (unsigned)(L_V + ((w + 1) & 3) * 2048 + 512 + l * 8), (unsigned)(L_V + ((w + 2) & 3) * 2048 + 1024 + l * 8), (unsigned)(L_V + ((w + 3) & 3) * 2048 + 1536 + l * 8), 0u, 0u};
        if (tid < 2) ((lu32*)(size_t)(L_V + L_FL))[tid] = ~0u;
    asm volatile(
        "s_mov_b32 s82, 3\n\t"
        ".Ld0%=:\n\t"
        "s_xor_b32 s80, s82, %[key]\n\t"
        "s_mul_i32 s81, s80, 131072\n\t"
        "s_lshl_b32 s80, s80, 11\n\t"
        "s_add_u32 s80, s80, %[m0b]\n\t"
        "s_add_u32 m0, s80, 0\n\t"
        "s_add_u32 s81, s81, 0\n\t"
        "buffer_load_dwordx4 v96, %[sv], s81 offen lds\n\t"
        "s_add_u32 m0, m0, 0x400\n\t"
        "s_add_u32 s81, s81, 65536\n\t"
        "buffer_load_dwordx4 v97, %[sv], s81 offen lds\n\t"
        "s_sub_u32 s82, s82, 1\n\t"
        "s_cbranch_scc0 .Ld0%=\n\t"
        "s_mov_b32 s82, 3\n\t"
        ".Ld1%=:\n\t"
        "s_xor_b32 s80, s82, %[key]\n\t"
        "s_mul_i32 s81, s80, 131072\n\t"
        "s_lshl_b32 s80, s80, 11\n\t"
        "s_add_u32 s80, s80, %[m0b]\n\t"
        "s_add_u32 m0, s80, 8192\n\t"
        "s_add_u32 s81, s81, 2097152\n\t"
        "buffer_load_dwordx4 v96, %[sv], s81 offen lds\n\t"
        "s_add_u32 m0, m0, 0x400\n\t"
        "s_add_u32 s81, s81, 65536\n\t"
        "buffer_load_dwordx4 v97, %[sv], s81 offen lds\n\t"
        "s_sub_u32 s82, s82, 1\n\t"
        "s_cbranch_scc0 .Ld1%=\n\t"
        "s_cmp_eq_u32 %[tu], 0\n\t"
        "s_cselect_b32 s81, 0, 0x7fff0000\n\t"
        "buffer_load_dword v110, v105, %[so], s81 offen\n\t"
        "s_mov_b32 s82, 3\n\t"
        ".Ld2%=:\n\t"
        "s_xor_b32 s80, s82, %[key]\n\t"
        "s_mul_i32 s81, s80, 131072\n\t"
        "s_lshl_b32 s80, s80, 11\n\t"
        "s_add_u32 s80, s80, %[m0b]\n\t"
        "s_add_u32 m0, s80, 16384\n\t"
        "s_add_u32 s81, s81, 4194304\n\t"
        "buffer_load_dwordx4 v96, %[sv], s81 offen lds\n\t"
        "s_add_u32 m0, m0, 0x400\n\t"
        "s_add_u32 s81, s81, 65536\n\t"
        "buffer_load_dwordx4 v97, %[sv], s81 offen lds\n\t"
        "s_sub_u32 s82, s82, 1\n\t"
        "s_cbranch_scc0 .Ld2%=\n\t"
        "s_cmp_eq_u32 %[tu], 1\n\t"
        "s_cselect_b32 s81, 0, 0x7fff0000\n\t"
        "buffer_load_dword v110, v105, %[so], s81 offen\n\t"
        "s_mov_b32 s82, 3\n\t"
        ".Ld3%=:\n\t"
        "s_xor_b32 s80, s82, %[key]\n\t"
        "s_mul_i32 s81, s80, 131072\n\t"
        "s_lshl_b32 s80, s80, 11\n\t"
        "s_add_u32 s80, s80, %[m0b]\n\t"
        "s_add_u32 m0, s80, 24576\n\t"
        "s_add_u32 s81, s81, 6291456\n\t"
        "buffer_load_dwordx4 v96, %[sv], s81 offen lds\n\t"
        "s_add_u32 m0, m0, 0x400\n\t"
        "s_add_u32 s81, s81, 65536\n\t"
        "buffer_load_dwordx4 v97, %[sv], s81 offen lds\n\t"
        "s_sub_u32 s82, s82, 1\n\t"
        "s_cbranch_scc0 .Ld3%=\n\t"
        "s_waitcnt vmcnt(26)\n\t"
        "ds_read_b64_tr_b16 v[128:129], v99 offset:0\n\t"
        "ds_read_b64_tr_b16 v[130:131], v99 offset:256\n\t"
        "ds_read_b64_tr_b16 v[132:133], v99 offset:4096\n\t"
        "ds_read_b64_tr_b16 v[134:135], v99 offset:4352\n\t"
        "ds_read_b64_tr_b16 v[136:137], v100 offset:0\n\t"
        "ds_read_b64_tr_b16 v[138:139], v100 offset:256\n\t"
        "ds_read_b64_tr_b16 v[140:141], v100 offset:4096\n\t"
        "ds_read_b64_tr_b16 v[142:143], v100 offset:4352\n\t"
        "ds_read_b64_tr_b16 v[144:145], v101 offset:0\n\t"
        "ds_read_b64_tr_b16 v[146:147], v101 offset:256\n\t"
        "ds_read_b64_tr_b16 v[148:149], v101 offset:4096\n\t"
        "ds_read_b64_tr_b16 v[150:151], v101 offset:4352\n\t"
        "ds_read_b64_tr_b16 v[152:153], v102 offset:0\n\t"
        "ds_read_b64_tr_b16 v[154:155], v102 offset:256\n\t"
        "ds_read_b64_tr_b16 v[156:157], v102 offset:4096\n\t"
        "ds_read_b64_tr_b16 v[158:159], v102 offset:4352\n\t"
        "s_waitcnt lgkmcnt(0)\n\t"
        "s_cmp_eq_u32 %[tu], 2\n\t"
        "s_cselect_b32 s81, 0, 0x7fff0000\n\t"
        "buffer_load_dword v110, v105, %[so], s81 offen\n\t"
        "s_mov_b32 s82, 3\n\t"
        ".Ld4%=:\n\t"
        "s_xor_b32 s80, s82, %[key]\n\t"
        "s_mul_i32 s81, s80, 65536\n\t"
        "s_lshl_b32 s80, s80, 11\n\t"
        "s_add_u32 s80, s80, %[m0b]\n\t"
        "s_add_u32 m0, s80, 0\n\t"
        "s_add_u32 s81, s81, 0\n\t"
        "buffer_load_dwordx4 v98, %[so], s81 offen lds\n\t"
        "s_add_u32 m0, m0, 0x400\n\t"
        "s_add_u32 s81, s81, 32768\n\t"
        "buffer_load_dwordx4 v98, %[so], s81 offen lds\n\t"
        "s_sub_u32 s82, s82, 1\n\t"
        "s_cbranch_scc0 .Ld4%=\n\t"
        "s_waitcnt vmcnt(27)\n\t"
        "ds_read_b64_tr_b16 v[160:161], v99 offset:8192\n\t"
        "ds_read_b64_tr_b16 v[162:163], v99 offset:8448\n\t"
        "ds_read_b64_tr_b16 v[164:165], v99 offset:12288\n\t"
        "ds_read_b64_tr_b16 v[166:167], v99 offset:12544\n\t"
        "ds_read_b64_tr_b16 v[168:169], v100 offset:8192\n\t"
        "ds_read_b64_tr_b16 v[170:171], v100 offset:8448\n\t"
        "ds_read_b64_tr_b16 v[172:173], v100 offset:12288\n\t"
        "ds_read_b64_tr_b16 v[174:175], v100 offset:12544\n\t"
        "ds_read_b64_tr_b16 v[176:177], v101 offset:8192\n\t"
        "ds_read_b64_tr_b16 v[178:179], v101 offset:8448\n\t"
        "ds_read_b64_tr_b16 v[180:181], v101 offset:12288\n\t"
        "ds_read_b64_tr_b16 v[182:183], v101 offset:12544\n\t"
        "ds_read_b64_tr_b16 v[184:185], v102 offset:8192\n\t"
        "ds_read_b64_tr_b16 v[186:187], v102 offset:8448\n\t"
        "ds_read_b64_tr_b16 v[188:189], v102 offset:12288\n\t"
        "ds_read_b64_tr_b16 v[190:191], v102 offset:12544\n\t"
        "s_waitcnt lgkmcnt(0)\n\t"
        "s_cmp_eq_u32 %[tu], 3\n\t"
        "s_cselect_b32 s81, 0, 0x7fff0000\n\t"
        "buffer_load_dword v110, v105, %[so], s81 offen\n\t"
        "s_mov_b32 s82, 3\n\t"
        ".Ld5%=:\n\t"
        "s_xor_b32 s80, s82, %[key]\n\t"
        "s_mul_i32 s81, s80, 65536\n\t"
        "s_lshl_b32 s80, s80, 11\n\t"
        "s_add_u32 s80, s80, %[m0b]\n\t"
        "s_add_u32 m0, s80, 8192\n\t"
        "s_add_u32 s81, s81, 262144\n\t"
        "buffer_load_dwordx4 v98, %[so], s81 offen lds\n\t"
        "s_add_u32 m0, m0, 0x400\n\t"
        "s_add_u32 s81, s81, 32768\n\t"
        "buffer_load_dwordx4 v98, %[so], s81 offen lds\n\t"
        "s_sub_u32 s82, s82, 1\n\t"
        "s_cbranch_scc0 .Ld5%=\n\t"
        "s_waitcnt vmcnt(27)\n\t"
        "ds_read_b64_tr_b16 v[192:193], v99 offset:16384\n\t"
        "ds_read_b64_tr_b16 v[194:195], v99 offset:16640\n\t"
        "ds_read_b64_tr_b16 v[196:197], v99 offset:20480\n\t"
        "ds_read_b64_tr_b16 v[198:199], v99 offset:20736\n\t"
        "ds_read_b64_tr_b16 v[200:201], v100 offset:16384\n\t"
        "ds_read_b64_tr_b16 v[202:203], v100 offset:16640\n\t"
        "ds_read_b64_tr_b16 v[204:205], v100 offset:20480\n\t"
        "ds_read_b64_tr_b16 v[206:207], v100 offset:20736\n\t"
        "ds_read_b64_tr_b16 v[208:209], v101 offset:16384\n\t"
        "ds_read_b64_tr_b16 v[210:211], v101 offset:16640\n\t"
        "ds_read_b64_tr_b16 v[212:213], v101 offset:20480\n\t"
        "ds_read_b64_tr_b16 v[214:215], v101 offset:20736\n\t"
        "ds_read_b64_tr_b16 v[216:217], v102 offset:16384\n\t"
        "ds_read_b64_tr_b16 v[218:219], v102 offset:16640\n\t"
        "ds_read_b64_tr_b16 v[220:221], v102 offset:20480\n\t"
        "ds_read_b64_tr_b16 v[222:223], v102 offset:20736\n\t"
        "s_waitcnt lgkmcnt(0)\n\t"
        "s_cmp_eq_u32 %[tu], 4\n\t"
        "s_cselect_b32 s81, 0, 0x7fff0000\n\t"
        "buffer_load_dword v110, v105, %[so], s81 offen\n\t"
        "s_mov_b32 s82, 3\n\t"
        ".Ld6%=:\n\t"
        "s_xor_b32 s80, s82, %[key]\n\t"
        "s_mul_i32 s81, s80, 65536\n\t"
        "s_lshl_b32 s80, s80, 11\n\t"
        "s_add_u32 s80, s80, %[m0b]\n\t"
        "s_add_u32 m0, s80, 16384\n\t"
        "s_add_u32 s81, s81, 524288\n\t"
        "buffer_load_dwordx4 v98, %[so], s81 offen lds\n\t"
        "s_add_u32 m0, m0, 0x400\n\t"
        "s_add_u32 s81, s81, 32768\n\t"
        "buffer_load_dwordx4 v98, %[so], s81 offen lds\n\t"
        "s_sub_u32 s82, s82, 1\n\t"
        "s_cbranch_scc0 .Ld6%=\n\t"
        "s_waitcnt vmcnt(27)\n\t"
        "ds_read_b64_tr_b16 v[224:225], v99 offset:24576\n\t"
        "ds_read_b64_tr_b16 v[226:227], v99 offset:24832\n\t"
        "ds_read_b64_tr_b16 v[228:229], v99 offset:28672\n\t"
        "ds_read_b64_tr_b16 v[230:231], v99 offset:28928\n\t"
        "ds_read_b64_tr_b16 v[232:233], v100 offset:24576\n\t"
        "ds_read_b64_tr_b16 v[234:235], v100 offset:24832\n\t"
        "ds_read_b64_tr_b16 v[236:237], v100 offset:28672\n\t"
        "ds_read_b64_tr_b16 v[238:239], v100 offset:28928\n\t"
        "ds_read_b64_tr_b16 v[240:241], v101 offset:24576\n\t"
        "ds_read_b64_tr_b16 v[242:243], v101 offset:24832\n\t"
        "ds_read_b64_tr_b16 v[244:245], v101 offset:28672\n\t"
        "ds_read_b64_tr_b16 v[246:247], v101 offset:28928\n\t"
        "ds_read_b64_tr_b16 v[248:249], v102 offset:24576\n\t"
        "ds_read_b64_tr_b16 v[250:251], v102 offset:24832\n\t"
        "ds_read_b64_tr_b16 v[252:253], v102 offset:28672\n\t"
        "ds_read_b64_tr_b16 v[254:255], v102 offset:28928\n\t"
        "s_waitcnt lgkmcnt(0)\n\t"
        "s_cmp_eq_u32 %[tu], 5\n\t"
        "s_cselect_b32 s81, 0, 0x7fff0000\n\t"
        "buffer_load_dword v110, v105, %[so], s81 offen\n\t"
        "s_mov_b32 s82, 3\n\t"
        ".Ld7%=:\n\t"
        "s_xor_b32 s80, s82, %[key]\n\t"
        "s_mul_i32 s81, s80, 65536\n\t"
        "s_lshl_b32 s80, s80, 11\n\t"
        "s_add_u32 s80, s80, %[m0b]\n\t"
        "s_add_u32 m0, s80, 24576\n\t"
        "s_add_u32 s81, s81, 786432\n\t"
        "buffer_load_dwordx4 v98, %[so], s81 offen lds\n\t"
        "s_add_u32 m0, m0, 0x400\n\t"
        "s_add_u32 s81, s81, 32768\n\t"
        "buffer_load_dwordx4 v98, %[so], s81 offen lds\n\t"
        "s_sub_u32 s82, s82, 1\n\t"
        "s_cbranch_scc0 .Ld7%=\n\t"
        "s_mov_b64 exec, 0\n\t"
        "s_branch .Lhk0%=\n\t"
        ".Lhr%=:\n\t"
        "s_mov_b64 exec, -1\n\t"
        "s_waitcnt vmcnt(27)\n\t"
        "ds_read_b64_tr_b16 a[0:1], v103 offset:0\n\t"
        "ds_read_b64_tr_b16 a[2:3], v103 offset:128\n\t"
        "ds_read_b64_tr_b16 a[4:5], v103 offset:4096\n\t"
        "ds_read_b64_tr_b16 a[6:7], v103 offset:4224\n\t"
        "ds_read_b64_tr_b16 a[8:9], v104 offset:0\n\t"
        "ds_read_b64_tr_b16 a[10:11], v104 offset:128\n\t"
        "ds_read_b64_tr_b16 a[12:13], v104 offset:4096\n\t"
        "ds_read_b64_tr_b16 a[14:15], v104 offset:4224\n\t"
        "ds_read_b64_tr_b16 a[16:17], v103 offset:512\n\t"
        "ds_read_b64_tr_b16 a[18:19], v103 offset:640\n\t"
        "ds_read_b64_tr_b16 a[20:21], v103 offset:4608\n\t"
        "ds_read_b64_tr_b16 a[22:23], v103 offset:4736\n\t"
        "ds_read_b64_tr_b16 a[24:25], v104 offset:512\n\t"
        "ds_read_b64_tr_b16 a[26:27], v104 offset:640\n\t"
        "ds_read_b64_tr_b16 a[28:29], v104 offset:4608\n\t"
        "ds_read_b64_tr_b16 a[30:31], v104 offset:4736\n\t"
        "s_waitcnt lgkmcnt(0)\n\t"
        "s_cmp_eq_u32 %[tu], 6\n\t"
        "s_cselect_b32 s81, 0, 0x7fff0000\n\t"
        "buffer_load_dword v110, v105, %[so], s81 offen\n\t"
        "s_mov_b32 s82, 3\n\t"
        ".Ld8%=:\n\t"
        "s_xor_b32 s80, s82, %[key]\n\t"
        "s_mul_i32 s81, s80, 65536\n\t"
        "s_lshl_b32 s80, s80, 11\n\t"
        "s_add_u32 s80, s80, %[m0b]\n\t"
        "s_add_u32 m0, s80, 0\n\t"
        "s_add_u32 s81, s81, 1048576\n\t"
        "buffer_load_dwordx4 v98, %[so], s81 offen lds\n\t"
        "s_add_u32 m0, m0, 0x400\n\t"
        "s_add_u32 s81, s81, 32768\n\t"
        "buffer_load_dwordx4 v98, %[so], s81 offen lds\n\t"
        "s_sub_u32 s82, s82, 1\n\t"
        "s_cbranch_scc0 .Ld8%=\n\t"
        "s_waitcnt vmcnt(27)\n\t"
        "ds_read_b64_tr_b16 a[32:33], v103 offset:8192\n\t"
        "ds_read_b64_tr_b16 a[34:35], v103 offset:8320\n\t"
        "ds_read_b64_tr_b16 a[36:37], v103 offset:12288\n\t"
        "ds_read_b64_tr_b16 a[38:39], v103 offset:12416\n\t"
        "ds_read_b64_tr_b16 a[40:41], v104 offset:8192\n\t"
        "ds_read_b64_tr_b16 a[42:43], v104 offset:8320\n\t"
        "ds_read_b64_tr_b16 a[44:45], v104 offset:12288\n\t"
        "ds_read_b64_tr_b16 a[46:47], v104 offset:12416\n\t"
        "ds_read_b64_tr_b16 a[48:49], v103 offset:8704\n\t"
        "ds_read_b64_tr_b16 a[50:51], v103 offset:8832\n\t"
        "ds_read_b64_tr_b16 a[52:53], v103 offset:12800\n\t"
        "ds_read_b64_tr_b16 a[54:55], v103 offset:12928\n\t"
        "ds_read_b64_tr_b16 a[56:57], v104 offset:8704\n\t"
        "ds_read_b64_tr_b16 a[58:59], v104 offset:8832\n\t"
        "ds_read_b64_tr_b16 a[60:61], v104 offset:12800\n\t"
        "ds_read_b64_tr_b16 a[62:63], v104 offset:12928\n\t"
        "s_waitcnt lgkmcnt(0)\n\t"
        "s_cmp_eq_u32 %[tu], 7\n\t"
        "s_cselect_b32 s81, 0, 0x7fff0000\n\t"
        "buffer_load_dword v110, v105, %[so], s81 offen\n\t"
        "s_mov_b32 s82, 3\n\t"
        ".Ld9%=:\n\t"
        "s_xor_b32 s80, s82, %[key]\n\t"
        "s_mul_i32 s81, s80, 65536\n\t"
        "s_lshl_b32 s80, s80, 11\n\t"
        "s_add_u32 s80, s80, %[m0b]\n\t"
        "s_add_u32 m0, s80, 8192\n\t"
        "s_add_u32 s81, s81, 1310720\n\t"
        "buffer_load_dwordx4 v98, %[so], s81 offen lds\n\t"
        "s_add_u32 m0, m0, 0x400\n\t"
        "s_add_u32 s81, s81, 32768\n\t"
        "buffer_load_dwordx4 v98, %[so], s81 offen lds\n\t"
        "s_sub_u32 s82, s82, 1\n\t"
        "s_cbranch_scc0 .Ld9%=\n\t"
        "s_waitcnt vmcnt(27)\n\t"
        "ds_read_b64_tr_b16 a[64:65], v103 offset:16384\n\t"
        "ds_read_b64_tr_b16 a[66:67], v103 offset:16512\n\t"
        "ds_read_b64_tr_b16 a[68:69], v103 offset:20480\n\t"
        "ds_read_b64_tr_b16 a[70:71], v103 offset:20608\n\t"
        "ds_read_b64_tr_b16 a[72:73], v104 offset:16384\n\t"
        "ds_read_b64_tr_b16 a[74:75], v104 offset:16512\n\t"
        "ds_read_b64_tr_b16 a[76:77], v104 offset:20480\n\t"
        "ds_read_b64_tr_b16 a[78:79], v104 offset:20608\n\t"
        "ds_read_b64_tr_b16 a[80:81], v103 offset:16896\n\t"
        "ds_read_b64_tr_b16 a[82:83], v103 offset:17024\n\t"
        "ds_read_b64_tr_b16 a[84:85], v103 offset:20992\n\t"
        "ds_read_b64_tr_b16 a[86:87], v103 offset:21120\n\t"
        "ds_read_b64_tr_b16 a[88:89], v104 offset:16896\n\t"
        "ds_read_b64_tr_b16 a[90:91], v104 offset:17024\n\t"
        "ds_read_b64_tr_b16 a[92:93], v104 offset:20992\n\t"
        "ds_read_b64_tr_b16 a[94:95], v104 offset:21120\n\t"
        "buffer_load_dwordx4 v[0:3], v112, %[wsd], %[cal] offen sc1\n\t"
        "s_waitcnt lgkmcnt(0)\n\t"
        "s_mov_b32 s82, 3\n\t"
        ".Ld10%=:\n\t"
        "s_xor_b32 s80, s82, %[key]\n\t"
        "s_mul_i32 s81, s80, 65536\n\t"
        "s_lshl_b32 s80, s80, 11\n\t"
        "s_add_u32 s80, s80, %[m0b]\n\t"
        "s_add_u32 m0, s80, 16384\n\t"
        "s_add_u32 s81, s81, 1572864\n\t"
        "buffer_load_dwordx4 v98, %[so], s81 offen lds\n\t"
        "s_add_u32 m0, m0, 0x400\n\t"
        "s_add_u32 s81, s81, 32768\n\t"
        "buffer_load_dwordx4 v98, %[so], s81 offen lds\n\t"
        "s_sub_u32 s82, s82, 1\n\t"
        "s_cbranch_scc0 .Ld10%=\n\t"
        "s_waitcnt vmcnt(27)\n\t"
        "ds_read_b64_tr_b16 a[96:97], v103 offset:24576\n\t"
        "ds_read_b64_tr_b16 a[98:99], v103 offset:24704\n\t"
        "ds_read_b64_tr_b16 a[100:101], v103 offset:28672\n\t"
        "ds_read_b64_tr_b16 a[102:103], v103 offset:28800\n\t"
        "ds_read_b64_tr_b16 a[104:105], v104 offset:24576\n\t"
        "ds_read_b64_tr_b16 a[106:107], v104 offset:24704\n\t"
        "ds_read_b64_tr_b16 a[108:109], v104 offset:28672\n\t"
        "ds_read_b64_tr_b16 a[110:111], v104 offset:28800\n\t"
        "ds_read_b64_tr_b16 a[112:113], v103 offset:25088\n\t"
        "ds_read_b64_tr_b16 a[114:115], v103 offset:25216\n\t"
        "ds_read_b64_tr_b16 a[116:117], v103 offset:29184\n\t"
        "ds_read_b64_tr_b16 a[118:119], v103 offset:29312\n\t"
        "ds_read_b64_tr_b16 a[120:121], v104 offset:25088\n\t"
        "ds_read_b64_tr_b16 a[122:123], v104 offset:25216\n\t"
        "ds_read_b64_tr_b16 a[124:125], v104 offset:29184\n\t"
        "ds_read_b64_tr_b16 a[126:127], v104 offset:29312\n\t"
        "s_waitcnt lgkmcnt(0)\n\t"
        "s_mov_b32 s82, 3\n\t"
        ".Ld11%=:\n\t"
        "s_xor_b32 s80, s82, %[key]\n\t"
        "s_mul_i32 s81, s80, 65536\n\t"
        "s_lshl_b32 s80, s80, 11\n\t"
        "s_add_u32 s80, s80, %[m0b]\n\t"
        "s_add_u32 m0, s80, 24576\n\t"
        "s_add_u32 s81, s81, 1835008\n\t"
        "buffer_load_dwordx4 v98, %[so], s81 offen lds\n\t"
        "s_add_u32 m0, m0, 0x400\n\t"
        "s_add_u32 s81, s81, 32768\n\t"
        "buffer_load_dwordx4 v98, %[so], s81 offen lds\n\t"
        "s_sub_u32 s82, s82, 1\n\t"
        "s_cbranch_scc0 .Ld11%=\n\t"
        "s_waitcnt vmcnt(26)\n\t"
        "ds_read_b64_tr_b16 a[128:129], v103 offset:0\n\t"
        "ds_read_b64_tr_b16 a[130:131], v103 offset:128\n\t"
        "ds_read_b64_tr_b16 a[132:133], v103 offset:4096\n\t"
        "ds_read_b64_tr_b16 a[134:135], v103 offset:4224\n\t"
        "ds_read_b64_tr_b16 a[136:137], v104 offset:0\n\t"
        "ds_read_b64_tr_b16 a[138:139], v104 offset:128\n\t"
        "ds_read_b64_tr_b16 a[140:141], v104 offset:4096\n\t"
        "ds_read_b64_tr_b16 a[142:143], v104 offset:4224\n\t"
        "ds_read_b64_tr_b16 a[144:145], v103 offset:512\n\t"
        "ds_read_b64_tr_b16 a[146:147], v103 offset:640\n\t"
        "ds_read_b64_tr_b16 a[148:149], v103 offset:4608\n\t"
        "ds_read_b64_tr_b16 a[150:151], v103 offset:4736\n\t"
        "ds_read_b64_tr_b16 a[152:153], v104 offset:512\n\t"
        "ds_read_b64_tr_b16 a[154:155], v104 offset:640\n\t"
        "ds_read_b64_tr_b16 a[156:157], v104 offset:4608\n\t"
        "ds_read_b64_tr_b16 a[158:159], v104 offset:4736\n\t"
        "s_waitcnt lgkmcnt(0)\n\t"
        "s_waitcnt vmcnt(17)\n\t"
        "ds_read_b64_tr_b16 a[160:161], v103 offset:8192\n\t"
        "ds_read_b64_tr_b16 a[162:163], v103 offset:8320\n\t"
        "ds_read_b64_tr_b16 a[164:165], v103 offset:12288\n\t"
        "ds_read_b64_tr_b16 a[166:167], v103 offset:12416\n\t"
        "ds_read_b64_tr_b16 a[168:169], v104 offset:8192\n\t"
        "ds_read_b64_tr_b16 a[170:171], v104 offset:8320\n\t"
        "ds_read_b64_tr_b16 a[172:173], v104 offset:12288\n\t"
        "ds_read_b64_tr_b16 a[174:175], v104 offset:12416\n\t"
        "ds_read_b64_tr_b16 a[176:177], v103 offset:8704\n\t"
        "ds_read_b64_tr_b16 a[178:179], v103 offset:8832\n\t"
        "ds_read_b64_tr_b16 a[180:181], v103 offset:12800\n\t"
        "ds_read_b64_tr_b16 a[182:183], v103 offset:12928\n\t"
        "ds_read_b64_tr_b16 a[184:185], v104 offset:8704\n\t"
        "ds_read_b64_tr_b16 a[186:187], v104 offset:8832\n\t"
        "ds_read_b64_tr_b16 a[188:189], v104 offset:12800\n\t"
        "ds_read_b64_tr_b16 a[190:191], v104 offset:12928\n\t"
        "s_waitcnt lgkmcnt(0)\n\t"
        "s_waitcnt vmcnt(8)\n\t"
        "ds_read_b64_tr_b16 a[192:193], v103 offset:16384\n\t"
        "ds_read_b64_tr_b16 a[194:195], v103 offset:16512\n\t"
        "ds_read_b64_tr_b16 a[196:197], v103 offset:20480\n\t"
        "ds_read_b64_tr_b16 a[198:199], v103 offset:20608\n\t"
        "ds_read_b64_tr_b16 a[200:201], v104 offset:16384\n\t"
        "ds_read_b64_tr_b16 a[202:203], v104 offset:16512\n\t"
        "ds_read_b64_tr_b16 a[204:205], v104 offset:20480\n\t"
        "ds_read_b64_tr_b16 a[206:207], v104 offset:20608\n\t"
        "ds_read_b64_tr_b16 a[208:209], v103 offset:16896\n\t"
        "ds_read_b64_tr_b16 a[210:211], v103 offset:17024\n\t"
        "ds_read_b64_tr_b16 a[212:213], v103 offset:20992\n\t"
        "ds_read_b64_tr_b16 a[214:215], v103 offset:21120\n\t"
        "ds_read_b64_tr_b16 a[216:217], v104 offset:16896\n\t"
        "ds_read_b64_tr_b16 a[218:219], v104 offset:17024\n\t"
        "ds_read_b64_tr_b16 a[220:221], v104 offset:20992\n\t"
        "ds_read_b64_tr_b16 a[222:223], v104 offset:21120\n\t"
        "s_mov_b32 s97, 0\n\t"
        ".Lg0%=:\n\t"
        "v_cmp_ne_u32_e32 vcc, %[tag], v0\n\t"
        "v_cmp_ne_u32_e64 s[82:83], %[tag], v2\n\t"
        "s_nop 1\n\t"
        "s_or_b64 vcc, vcc, s[82:83]\n\t"
        "s_cbranch_scc0 .Lg1%=\n\t"
        "s_sleep 4\n\t"
        "buffer_load_dwordx4 v[0:3], v112, %[wsd], %[cal] offen sc1\n\t"
        "s_add_u32 s97, s97, 1\n\t"
        "s_cmp_lt_u32 s97, 0x100000\n\t"
        "s_cbranch_scc0 .Lgu%=\n\t"
        "s_waitcnt vmcnt(0)\n\t"
        "s_branch .Lg0%=\n\t"
        ".Lg1%=:\n\t"
        "s_mov_b32 s84, 0x55555555\n\t"
        "s_mov_b32 s85, s84\n\t"
        "v_and_b32_e32 v0, 1, v1\n\t"
        "v_and_b32_e32 v2, 1, v3\n\t"
        "v_cmp_ne_u32_e32 vcc, 0, v0\n\t"
        "v_cmp_ne_u32_e64 s[82:83], 0, v2\n\t"
        "s_nop 0\n\t"
        "s_and_b64 s[82:83], s[82:83], s[84:85]\n\t"
        "s_bcnt1_i32_b64 s87, s[82:83]\n\t"
        "s_and_b64 s[82:83], vcc, s[84:85]\n\t"
        "s_bcnt1_i32_b64 s86, s[82:83]\n\t"
        "s_andn2_b64 s[82:83], vcc, s[84:85]\n\t"
        "s_bcnt1_i32_b64 s88, s[82:83]\n\t"
        "v_and_b32_e32 v0, 2, v1\n\t"
        "v_and_b32_e32 v2, 2, v3\n\t"
        "v_cmp_ne_u32_e32 vcc, 0, v0\n\t"
        "v_cmp_ne_u32_e64 s[82:83], 0, v2\n\t"
        "s_nop 0\n\t"
        "s_and_b64 s[82:83], s[82:83], s[84:85]\n\t"
        "s_bcnt1_i32_b64 s90, s[82:83]\n\t"
        "s_and_b64 s[82:83], vcc, s[84:85]\n\t"
        "s_bcnt1_i32_b64 s89, s[82:83]\n\t"
        "s_andn2_b64 s[82:83], vcc, s[84:85]\n\t"
        "s_bcnt1_i32_b64 s91, s[82:83]\n\t"
        "s_add_u32 s80, s86, s87\n\t"
        "s_add_u32 s80, s80, s88\n\t"
        "s_cmp_ge_u32 s80, 48\n\t"
        "s_cselect_b32 s80, 8, 24\n\t"
        "s_cmp_gt_u32 s86, s80\n\t"
        "s_cselect_b32 s86, %c[NX1], %c[NX0]\n\t"
        "s_cmp_gt_u32 s89, 16\n\t"
        "s_cselect_b32 s81, %c[NY1], %c[NY0]\n\t"
        "s_or_b32 s86, s86, s81\n\t"
        "s_cmp_gt_u32 s87, s80\n\t"
        "s_cselect_b32 s87, %c[NX1], %c[NX0]\n\t"
        "s_cmp_gt_u32 s90, 16\n\t"
        "s_cselect_b32 s81, %c[NY1], %c[NY0]\n\t"
        "s_or_b32 s87, s87, s81\n\t"
        "s_cmp_gt_u32 s88, s80\n\t"
        "s_cselect_b32 s88, %c[NX1], %c[NX0]\n\t"
        "s_cmp_gt_u32 s91, 16\n\t"
        "s_cselect_b32 s81, %c[NY1], %c[NY0]\n\t"
        "s_or_b32 s88, s88, s81\n\t"
        "s_xor_b32 %[nA], %[nA], s86\n\t"
        "s_xor_b32 %[nW], %[nW], s86\n\t"
        "s_xor_b32 %[nC], %[nC], s87\n\t"
        "s_xor_b32 %[nS], %[nS], s88\n\t"
        "s_xor_b32 %[nT], %[nT], s88\n\t"
        "s_xor_b32 %[b0], %[b0], s88\n\t"
        "s_cmp_eq_u32 %[w], 1\n\t"
        "s_cselect_b32 s80, s87, s86\n\t"
        "s_cmp_eq_u32 %[w], 2\n\t"
        "s_cselect_b32 s80, s88, s80\n\t"
        "s_xor_b32 %[pob], %[pob], s80\n\t"
        "s_lshl_b32 s80, %[w], 10\n\t"
        "s_sub_u32 %[nS], %[nS], s80\n\t"
        "s_sub_u32 %[pob], %[pob], s80\n\t"
        "s_mov_b32 s36, 0xf0000000\n\t"
        "s_mov_b32 s38, 0xff00ff00\n\t"
        "s_mov_b32 s39, 0xff00ff00\n\t"
        "s_mov_b32 s40, 0\n\t"
        "s_mov_b32 s37, 0\n\t"
        "s_brev_b32 s54, 1\n\t"
        "s_mov_b32 s46, %c[iD1]\n\t"
        "s_mov_b32 s45, %c[iD1]\n\t"
        "s_mov_b32 s47, %c[iD2]\n\t"
        "s_mov_b32 s48, 1\n\t"
        "s_mov_b32 s50, %c[LC]\n\t"
        "s_mov_b32 s51, %c[LA]\n\t"
        "s_mov_b32 s52, %c[E1]\n\t"
        "s_mov_b32 s53, %c[E2]\n\t"
        "s_mov_b32 s43, 0\n\t"
        "s_mov_b32 s61, %[b0]\n\t"
        "s_add_u32 s62, %[b0], 0x40000\n\t"
        "s_mov_b32 s75, 162816\n\t"
        "s_waitcnt lgkmcnt(0)\n\t"
        "s_waitcnt vmcnt(0)\n\t"
        "ds_read_b64_tr_b16 a[224:225], v103 offset:24576\n\t"
        "ds_read_b64_tr_b16 a[226:227], v103 offset:24704\n\t"
        "ds_read_b64_tr_b16 a[228:229], v103 offset:28672\n\t"
        "ds_read_b64_tr_b16 a[230:231], v103 offset:28800\n\t"
        "ds_read_b64_tr_b16 a[232:233], v104 offset:24576\n\t"
        "ds_read_b64_tr_b16 a[234:235], v104 offset:24704\n\t"
        "ds_read_b64_tr_b16 a[236:237], v104 offset:28672\n\t"
        "ds_read_b64_tr_b16 a[238:239], v104 offset:28800\n\t"
        "ds_read_b64_tr_b16 a[240:241], v103 offset:25088\n\t"
        "ds_read_b64_tr_b16 a[242:243], v103 offset:25216\n\t"
        "ds_read_b64_tr_b16 a[244:245], v103 offset:29184\n\t"
        "ds_read_b64_tr_b16 a[246:247], v103 offset:29312\n\t"
        "ds_read_b64_tr_b16 a[248:249], v104 offset:25088\n\t"
        "ds_read_b64_tr_b16 a[250:251], v104 offset:25216\n\t"
        "ds_read_b64_tr_b16 a[252:253], v104 offset:29184\n\t"
        "ds_read_b64_tr_b16 a[254:255], v104 offset:29312\n\t"
        "s_waitcnt lgkmcnt(0)\n\t"
        "rec_v_code:\n\t"
        "s_and_b32 s80, s40, 3\n\t"
        "s_lshl_b32 s80, s80, 17\n\t"
        "s_add_u32 s57, s80, %[nA]\n\t"
        "s_add_u32 s58, s57, 0x1000\n\t"
        "s_add_u32 s59, s57, 0x8000\n\t"
        "s_add_u32 s60, s57, 0x9000\n\t"
        "v_mov_b32_e32 v92, -1\n\t"
        "v_mov_b32_e32 v93, -1\n\t"
        "v_mov_b32_e32 v94, -1\n\t"
        "v_mov_b32_e32 v95, -1\n\t"
        "v_mov_b32_e32 v48, 0\n\t"
        "v_mov_b32_e32 v49, 0\n\t"
        "v_mov_b32_e32 v50, 0\n\t"
        "v_mov_b32_e32 v51, 0\n\t"
        "v_mov_b32_e32 v52, 0\n\t"
        "v_mov_b32_e32 v53, 0\n\t"
        "v_mov_b32_e32 v54, 0\n\t"
        "v_mov_b32_e32 v55, 0\n\t"
        "v_mov_b32_e32 v56, 0\n\t"
        "v_mov_b32_e32 v57, 0\n\t"
        "v_mov_b32_e32 v58, 0\n\t"
        "v_mov_b32_e32 v59, 0\n\t"
        "v_mov_b32_e32 v60, 0\n\t"
        "v_mov_b32_e32 v61, 0\n\t"
        "v_mov_b32_e32 v62, 0\n\t"
        "v_mov_b32_e32 v63, 0\n\t"
        "s_barrier\n\t"
        "s_memrealtime s[76:77]\n\t"
        "s_mov_b32 s79, %[nT]\n\t"
        "s_add_u32 s61, %[b0], 0x40000\n\t"
        "s_mov_b32 s62, %[b0]\n\t"
        "s_sub_u32 s75, s75, 16\n\t"
        "s_waitcnt lgkmcnt(0)\n\t"
        "s_mov_b32 s41, s76\n\t"
        "s_mov_b32 s69, s76\n\t"
        ".p2align 6\n\t"
        ".Lloop%=:\n\t"
        "s_mov_b32 s97, 0\n\t"
        "s_movk_i32 s96, 0x1000\n\t"
        ".Lwa%=:\n\t"
        "s_memrealtime s[98:99]\n\t"
        "s_addk_i32 s96, -1\n\t"
        "s_cmp_lt_i32 s96, 0\n\t"
        "s_cbranch_scc1 .Lta%=\n\t"
        "s_waitcnt lgkmcnt(0)\n\t"
        "s_sub_i32 s98, s98, s69\n\t"
        "s_cmp_lt_i32 s98, %c[iFT]\n\t"
        "s_cbranch_scc1 .Lwa%=\n\t"
        "s_cmp_ge_i32 s98, 0\n\t"
        "s_cbranch_scc1 .Lxa%=\n\t"
        ".Lqa%=:\n\t"
        "s_nop %c[iFN]\n\t"
        "s_addk_i32 s98, 1\n\t"
        "s_cmp_lt_i32 s98, 0\n\t"
        "s_cbranch_scc1 .Lqa%=\n\t"
        ".Lxa%=:\n\t"
        "buffer_load_dwordx4 v[0:3], v112, %[srd], s57 offen sc1\n\t"
        "s_nop 7\n\t"
        "buffer_load_dwordx4 v[4:7], v112, %[srd], s58 offen sc1\n\t"
        "s_nop 7\n\t"
        "buffer_load_dwordx4 v[8:11], v112, %[srd], s59 offen sc1\n\t"
        "s_nop 7\n\t"
        "buffer_load_dwordx4 v[12:15], v112, %[srd], s60 offen sc1\n\t"
        "v_mov_b32_e32 v80, 161792\n\t"
        "ds_read_b64 v[80:81], v80\n\t"
        "s_waitcnt lgkmcnt(0)\n\t"
        "v_readfirstlane_b32 s80, v80\n\t"
        "v_readfirstlane_b32 s81, v81\n\t"
        "s_cmp_eq_u32 s80, s40\n\t"
        "s_cselect_b32 s84, 1, 0\n\t"
        "s_cmp_eq_u32 s81, s40\n\t"
        "s_cselect_b32 s81, 4, 0\n\t"
        "s_or_b32 s84, s84, s81\n\t"
        "v_mov_b32_e32 v106, s76\n\t"
        "v_mov_b32_e32 v107, s84\n\t"
        "buffer_store_dwordx2 v[106:107], v117, %[srd], s79 offen sc1\n\t"
        "s_and_b32 s80, s40, 3\n\t"
        "s_lshl_b32 s80, s80, 17\n\t"
        "s_add_u32 s63, s80, %[nC]\n\t"
        "s_add_u32 s66, s80, %[nS]\n\t"
        "s_add_u32 s81, s80, 0x20000\n\t"
        "s_and_b32 s81, s81, 0x60000\n\t"
        "s_add_u32 s67, s81, %[nW]\n\t"
        "s_add_u32 s79, s81, %[nT]\n\t"
        "s_cmp_eq_u32 %[w], 2\n\t"
        "s_cselect_b32 s81, s80, s81\n\t"
        "s_xor_b32 s81, s81, 0x40000\n\t"
        "s_add_u32 s68, s81, %[pob]\n\t"
        "s_lshl_b32 s72, s40, 17\n\t"
        "s_add_u32 s72, s72, %[kvo]\n\t"
        "s_mov_b32 s80, s61\n\t"
        "s_mov_b32 s61, s62\n\t"
        "s_mov_b32 s62, s80\n\t"
        ".Lgo%=:\n\t"
        "s_waitcnt vmcnt(4)\n\t"
        "v_smfmac_f32_16x16x64_bf16 v[52:55], v[0:3], v[136:143], v113\n\t"
        "v_smfmac_f32_16x16x64_bf16 v[56:59], v[0:3], v[144:151], v113\n\t"
        "v_smfmac_f32_16x16x64_bf16 v[60:63], v[0:3], v[152:159], v113\n\t"
        "v_smfmac_f32_16x16x64_bf16 v[48:51], v[0:3], v[128:135], v113\n\t"
        "s_waitcnt vmcnt(3)\n\t"
        "v_smfmac_f32_16x16x64_bf16 v[52:55], v[4:7], v[168:175], v113\n\t"
        "v_smfmac_f32_16x16x64_bf16 v[56:59], v[4:7], v[176:183], v113\n\t"
        "v_smfmac_f32_16x16x64_bf16 v[60:63], v[4:7], v[184:191], v113\n\t"
        "v_smfmac_f32_16x16x64_bf16 v[48:51], v[4:7], v[160:167], v113\n\t"
        "s_waitcnt vmcnt(2)\n\t"
        "v_smfmac_f32_16x16x64_bf16 v[52:55], v[8:11], v[200:207], v113\n\t"
        "v_smfmac_f32_16x16x64_bf16 v[56:59], v[8:11], v[208:215], v113\n\t"
        "v_smfmac_f32_16x16x64_bf16 v[60:63], v[8:11], v[216:223], v113\n\t"
        "v_smfmac_f32_16x16x64_bf16 v[48:51], v[8:11], v[192:199], v113\n\t"
        "s_waitcnt vmcnt(1)\n\t"
        "v_smfmac_f32_16x16x64_bf16 v[52:55], v[12:15], v[232:239], v113\n\t"
        "v_smfmac_f32_16x16x64_bf16 v[56:59], v[12:15], v[240:247], v113\n\t"
        "v_smfmac_f32_16x16x64_bf16 v[60:63], v[12:15], v[248:255], v113\n\t"
        "v_smfmac_f32_16x16x64_bf16 v[48:51], v[12:15], v[224:231], v113\n\t"
        "s_nop 1\n\t"
        "v_mov_b32_e32 v104, v104\n\t"
        "s_nop 1\n\t"
        "v_add_f32_e32 v98, v52, v54\n\t"
        "v_add_f32_e32 v99, v53, v55\n\t"
        "ds_write_b64 v123, v[98:99]\n\t"
        "v_add_f32_e32 v100, v56, v58\n\t"
        "v_add_f32_e32 v101, v57, v59\n\t"
        "ds_write_b64 v124, v[100:101]\n\t"
        "v_add_f32_e32 v102, v60, v62\n\t"
        "v_add_f32_e32 v103, v61, v63\n\t"
        "ds_write_b64 v125, v[102:103]\n\t"
        "v_pk_add_f32 v[96:97], v[48:49], v[50:51]\n\t"
        "v_cmp_u_f32_e32 vcc, v96, v97\n\t"
        "s_nop 2\n\t"
        "v_mov_b32_e32 v104, v104\n\t"
        "s_cbranch_vccnz .Lfa%=\n\t"
        ".Lra%=:\n\t"
        "v_mov_b32_e32 v104, v104\n\t"
        "s_nop 2\n\t"
        "v_mov_b32_e32 v104, v104\n\t"
        "s_nop 2\n\t"
        "v_mov_b32_e32 v104, v104\n\t"
        "s_nop 2\n\t"
        "v_mov_b32_e32 v104, v104\n\t"
        "s_waitcnt lgkmcnt(0)\n\t"
        "s_barrier\n\t"
        "v_mov_b32_e32 v104, v104\n\t"
        "ds_read_b64 v[98:99], v122 offset:512\n\t"
        "ds_read_b64 v[100:101], v122 offset:1024\n\t"
        "ds_read_b64 v[102:103], v122 offset:1536\n\t"
        "v_mov_b32_e32 v104, v104\n\t"
        "s_nop 2\n\t"
        "v_mov_b32_e32 v104, v104\n\t"
        "s_nop 2\n\t"
        "v_mov_b32_e32 v104, v104\n\t"
        "s_waitcnt lgkmcnt(2)\n\t"
        "v_pk_add_f32 v[96:97], v[96:97], v[98:99]\n\t"
        "s_waitcnt lgkmcnt(0)\n\t"
        "v_pk_add_f32 v[100:101], v[100:101], v[102:103]\n\t"
        "v_pk_add_f32 v[96:97], v[96:97], v[100:101]\n\t"
        "v_cvt_pk_bf16_f32 v80, v96, v97\n\t"
        "v_min_u32_e32 v80, -2, v80\n\t"
        "buffer_store_dword v80, v114, %[srd], s61 offen sc1\n\t"
        "s_cmp_eq_u32 s40, 63\n\t"
        "s_cbranch_scc1 .Lend%=\n\t"
        "v_mov_b32_e32 v64, 0\n\t"
        "v_mov_b32_e32 v65, 0\n\t"
        "v_mov_b32_e32 v66, 0\n\t"
        "v_mov_b32_e32 v67, 0\n\t"
        "v_mov_b32_e32 v68, 0\n\t"
        "v_mov_b32_e32 v69, 0\n\t"
        "v_mov_b32_e32 v70, 0\n\t"
        "v_mov_b32_e32 v71, 0\n\t"
        "v_mov_b32_e32 v72, 0\n\t"
        "v_mov_b32_e32 v73, 0\n\t"
        "v_mov_b32_e32 v74, 0\n\t"
        "v_mov_b32_e32 v75, 0\n\t"
        "v_mov_b32_e32 v76, 0\n\t"
        "v_mov_b32_e32 v77, 0\n\t"
        "v_mov_b32_e32 v78, 0\n\t"
        "v_mov_b32_e32 v79, 0\n\t"
        "s_xor_b32 s64, s63, %c[iCS1]\n\t"
        "s_xor_b32 s65, s63, %c[iCS2]\n\t"
        "s_xor_b32 s73, s64, %c[iCS2]\n\t"
        "s_sleep %c[VSLP]\n\t"
        "buffer_load_dwordx4 v[88:91], v112, %[srd], s66 offen sc1\n\t"
        "s_cmp_eq_u32 %[w], 0\n\t"
        "s_cselect_b32 exec_hi, 0, -1\n\t"
        "buffer_store_dwordx4 v[92:95], v112, %[srd], s68 offen sc1\n\t"
        "s_mov_b32 exec_hi, -1\n\t"
        "s_waitcnt lgkmcnt(0)\n\t"
        "v_mov_b32_e32 v48, 0\n\t"
        "v_mov_b32_e32 v49, 0\n\t"
        "v_mov_b32_e32 v50, 0\n\t"
        "v_mov_b32_e32 v51, 0\n\t"
        "v_mov_b32_e32 v52, 0\n\t"
        "v_mov_b32_e32 v53, 0\n\t"
        "v_mov_b32_e32 v54, 0\n\t"
        "v_mov_b32_e32 v55, 0\n\t"
        "v_mov_b32_e32 v56, 0\n\t"
        "v_mov_b32_e32 v57, 0\n\t"
        "v_mov_b32_e32 v58, 0\n\t"
        "v_mov_b32_e32 v59, 0\n\t"
        "v_mov_b32_e32 v60, 0\n\t"
        "v_mov_b32_e32 v61, 0\n\t"
        "v_mov_b32_e32 v62, 0\n\t"
        "v_mov_b32_e32 v63, 0\n\t"
        "s_add_u32 s81, s40, 1\n\t"
        "s_and_b32 s80, s81, 3\n\t"
        "s_lshl_b32 s80, s80, 17\n\t"
        "s_add_u32 s57, s80, %[nA]\n\t"
        "s_add_u32 s58, s57, 0x1000\n\t"
        "s_add_u32 s59, s57, 0x8000\n\t"
        "s_add_u32 s60, s57, 0x9000\n\t"
        "s_waitcnt vmcnt(1)\n\t"
        "s_mov_b32 s97, 0\n\t"
        ".Lsr%=:\n\t"
        "s_mov_b32 exec_hi, 0xffff\n\t"
        "v_subrev_u32_e32 v4, s41, v88\n\t"
        "v_subrev_u32_e32 v5, s41, v90\n\t"
        "v_max_i32_e32 v4, v4, v5\n\t"
        "v_max_u32_e32 v5, v89, v91\n\t"
        "v_cmp_eq_u32_e32 vcc, -1, v5\n\t"
        "v_max_i32_dpp v4, v4, v4 quad_perm:[1,0,3,2] row_mask:0xf bank_mask:0xf\n\t"
        "v_or_b32_e32 v6, v89, v91\n\t"
        "s_nop 0\n\t"
        "v_max_i32_dpp v4, v4, v4 quad_perm:[2,3,0,1] row_mask:0xf bank_mask:0xf\n\t"
        "s_nop 1\n\t"
        "v_max_i32_dpp v4, v4, v4 row_ror:4 row_mask:0xf bank_mask:0xf\n\t"
        "s_nop 1\n\t"
        "v_max_i32_dpp v4, v4, v4 row_ror:8 row_mask:0xf bank_mask:0xf\n\t"
        "s_cbranch_vccnz .Lsf%=\n\t"
        "v_readlane_b32 s85, v4, 0\n\t"
        "v_readlane_b32 s81, v4, 16\n\t"
        "v_readlane_b32 s56, v4, 32\n\t"
        "v_and_b32_e32 v5, 1, v6\n\t"
        "v_cmp_ne_u32_e64 s[86:87], 0, v5\n\t"
        "v_and_b32_e32 v5, 2, v6\n\t"
        "v_cmp_ne_u32_e64 s[88:89], 0, v5\n\t"
        "v_and_b32_e32 v5, 4, v6\n\t"
        "v_cmp_ne_u32_e64 s[90:91], 0, v5\n\t"
        "s_max_i32 s56, s56, s81\n\t"
        "s_add_u32 s92, s85, s41\n\t"
        "s_sub_u32 s92, s92, s43\n\t"
        "s_add_i32 s93, s56, s47\n\t"
        "s_mov_b32 s44, s46\n\t"
        "s_mov_b32 s46, s45\n\t"
        "s_mov_b32 s94, s48\n\t"
        "s_mov_b32 s43, s41\n\t"
        "s_cmp_gt_i32 s56, s54\n\t"
        "s_cbranch_scc1 .Lmv%=\n\t"
        "s_mov_b32 s37, 0\n\t"
        "s_mov_b32 s48, 0\n\t"
        "s_mov_b32 s95, 0\n\t"
        ".Lsd%=:\n\t"
        "s_mov_b32 exec_hi, -1\n\t"
        "s_add_u32 s42, s43, s46\n\t"
        "s_mov_b32 s97, 0\n\t"
        "s_movk_i32 s96, 0x1000\n\t"
        ".Lwc%=:\n\t"
        "s_memrealtime s[98:99]\n\t"
        "s_addk_i32 s96, -1\n\t"
        "s_cmp_lt_i32 s96, 0\n\t"
        "s_cbranch_scc1 .Ltc%=\n\t"
        "s_waitcnt lgkmcnt(0)\n\t"
        "s_sub_i32 s98, s98, s42\n\t"
        "s_cmp_lt_i32 s98, %c[iFT]\n\t"
        "s_cbranch_scc1 .Lwc%=\n\t"
        "s_cmp_ge_i32 s98, 0\n\t"
        "s_cbranch_scc1 .Lxc%=\n\t"
        ".Lqc%=:\n\t"
        "s_nop %c[iFN]\n\t"
        "s_addk_i32 s98, 1\n\t"
        "s_cmp_lt_i32 s98, 0\n\t"
        "s_cbranch_scc1 .Lqc%=\n\t"
        ".Lxc%=:\n\t"
        "buffer_load_dwordx4 v[16:19], v112, %[srd], s63 offen sc1\n\t"
        "s_cmp_ge_u32 s40, %c[iCT0]\n\t"
        "s_cselect_b32 s85, 1, 0\n\t"
        "s_cmp_lg_u64 s[86:87], 0\n\t"
        "s_cselect_b32 s86, s85, 0\n\t"
        "s_cmp_lg_u64 s[88:89], 0\n\t"
        "s_cselect_b32 s87, s85, 0\n\t"
        "s_cmp_lg_u64 s[90:91], 0\n\t"
        "s_cselect_b32 s88, s85, 0\n\t"
        "buffer_load_dwordx4 v[20:23], v115, %[srd], s63 offen sc1\n\t"
        "s_or_b32 s80, s94, s86\n\t"
        "s_or_b32 s80, s80, s87\n\t"
        "s_andn2_b32 s90, s85, s80\n\t"
        "s_cmp_le_i32 s92, s44\n\t"
        "s_cselect_b32 s80, s85, 0\n\t"
        "s_andn2_b32 s91, s80, s88\n\t"
        "s_and_b32 s80, s40, %c[LDEC]\n\t"
        "s_cselect_b32 s89, 0, 1\n\t"
        "buffer_load_dwordx4 v[24:27], v112, %[srd], s64 offen sc1\n\t"
        "s_andn2_b32 s80, s86, s94\n\t"
        "s_cmp_lt_i32 s51, %c[LA24]\n\t"
        "s_cselect_b32 s80, s80, 0\n\t"
        "s_lshl2_add_u32 s51, s80, s51\n\t"
        "s_sub_i32 s51, s51, s89\n\t"
        "s_max_i32 s51, s51, %c[LA]\n\t"
        "s_lshl_b32 s80, s93, 1\n\t"
        "s_sub_i32 s81, s80, s53\n\t"
        "buffer_load_dwordx4 v[28:31], v115, %[srd], s64 offen sc1\n\t"
        "s_ashr_i32 s82, s81, %c[UPSH]\n\t"
        "s_max_i32 s82, s82, %c[TRUP]\n\t"
        "s_add_i32 s82, s82, s53\n\t"
        "s_min_i32 s82, s82, s80\n\t"
        "s_ashr_i32 s83, s81, %c[DNSH]\n\t"
        "s_min_i32 s83, s83, -%c[TRDN]\n\t"
        "s_add_i32 s83, s83, s53\n\t"
        "s_max_i32 s83, s83, s80\n\t"
        "buffer_load_dwordx4 v[32:35], v112, %[srd], s65 offen sc1\n\t"
        "s_cmp_gt_i32 s81, 0\n\t"
        "s_cselect_b32 s82, s82, s83\n\t"
        "s_cmp_lg_u32 s91, 0\n\t"
        "s_cselect_b32 s53, s82, s53\n\t"
        "s_add_i32 s80, s53, 1\n\t"
        "s_ashr_i32 s80, s80, 1\n\t"
        "s_add_i32 s47, s80, s51\n\t"
        "s_add_u32 s81, s47, %c[AUP]\n\t"
        "buffer_load_dwordx4 v[36:39], v115, %[srd], s65 offen sc1\n\t"
        "s_min_i32 s80, s95, s81\n\t"
        "s_max_i32 s47, s47, s80\n\t"
        "s_add_u32 s41, s42, s47\n\t"
        "s_cmp_eq_u32 s40, 0\n\t"
        "s_cselect_b32 s51, %c[LA], s51\n\t"
        "s_mov_b32 s80, s88\n\t"
        "s_add_i32 s81, s92, s50\n\t"
        "s_sub_i32 s81, s81, 4\n\t"
        "buffer_load_dwordx4 v[40:43], v112, %[srd], s73 offen sc1\n\t"
        "s_cmp_le_i32 s81, s44\n\t"
        "s_cselect_b32 s80, s80, 0\n\t"
        "s_cmp_lt_i32 s50, %c[LC24]\n\t"
        "s_cselect_b32 s80, s80, 0\n\t"
        "s_lshl2_add_u32 s50, s80, s50\n\t"
        "s_sub_i32 s50, s50, s89\n\t"
        "s_max_i32 s50, s50, %c[LC]\n\t"
        "s_lshl_b32 s80, s92, 1\n\t"
        "buffer_load_dwordx4 v[44:47], v115, %[srd], s73 offen sc1\n\t"
        "s_sub_i32 s81, s80, s52\n\t"
        "s_ashr_i32 s82, s81, %c[UPSH]\n\t"
        "s_max_i32 s82, s82, %c[TRUP]\n\t"
        "s_add_i32 s82, s82, s52\n\t"
        "s_min_i32 s82, s82, s80\n\t"
        "s_ashr_i32 s83, s81, %c[DNSH]\n\t"
        "s_min_i32 s83, s83, -%c[TRDN]\n\t"
        "s_add_i32 s83, s83, s52\n\t"
        "s_max_i32 s83, s83, s80\n\t"
        "s_cmp_gt_i32 s81, 0\n\t"
        "s_cselect_b32 s82, s82, s83\n\t"
        "s_cmp_lg_u32 s90, 0\n\t"
        "s_cselect_b32 s52, s82, s52\n\t"
        "s_add_i32 s80, s52, 1\n\t"
        "s_ashr_i32 s80, s80, 1\n\t"
        "s_add_i32 s45, s80, s50\n\t"
        ".p2align 6\n\t"
        ".Lrc%=:\n\t"
        "s_waitcnt vmcnt(7)\n\t"
        "v_smfmac_f32_16x16x64_bf16 v[64:67], v[16:19], a[0:7], v113\n\t"
        "v_smfmac_f32_16x16x64_bf16 v[68:71], v[16:19], a[8:15], v113\n\t"
        "v_smfmac_f32_16x16x64_bf16 v[72:75], v[16:19], a[16:23], v113\n\t"
        "v_smfmac_f32_16x16x64_bf16 v[76:79], v[16:19], a[24:31], v113\n\t"
        "s_waitcnt vmcnt(6)\n\t"
        "v_smfmac_f32_16x16x64_bf16 v[64:67], v[20:23], a[32:39], v113\n\t"
        "v_smfmac_f32_16x16x64_bf16 v[68:71], v[20:23], a[40:47], v113\n\t"
        "v_smfmac_f32_16x16x64_bf16 v[72:75], v[20:23], a[48:55], v113\n\t"
        "v_smfmac_f32_16x16x64_bf16 v[76:79], v[20:23], a[56:63], v113\n\t"
        "s_waitcnt vmcnt(5)\n\t"
        "v_smfmac_f32_16x16x64_bf16 v[64:67], v[24:27], a[64:71], v113\n\t"
        "v_smfmac_f32_16x16x64_bf16 v[68:71], v[24:27], a[72:79], v113\n\t"
        "v_smfmac_f32_16x16x64_bf16 v[72:75], v[24:27], a[80:87], v113\n\t"
        "v_smfmac_f32_16x16x64_bf16 v[76:79], v[24:27], a[88:95], v113\n\t"
        "s_waitcnt vmcnt(4)\n\t"
        "v_smfmac_f32_16x16x64_bf16 v[64:67], v[28:31], a[96:103], v113\n\t"
        "v_smfmac_f32_16x16x64_bf16 v[68:71], v[28:31], a[104:111], v113\n\t"
        "v_smfmac_f32_16x16x64_bf16 v[72:75], v[28:31], a[112:119], v113\n\t"
        "v_smfmac_f32_16x16x64_bf16 v[76:79], v[28:31], a[120:127], v113\n\t"
        "s_waitcnt vmcnt(3)\n\t"
        "v_smfmac_f32_16x16x64_bf16 v[64:67], v[32:35], a[128:135], v113\n\t"
        "v_smfmac_f32_16x16x64_bf16 v[68:71], v[32:35], a[136:143], v113\n\t"
        "v_smfmac_f32_16x16x64_bf16 v[72:75], v[32:35], a[144:151], v113\n\t"
        "v_smfmac_f32_16x16x64_bf16 v[76:79], v[32:35], a[152:159], v113\n\t"
        "s_waitcnt vmcnt(2)\n\t"
        "v_smfmac_f32_16x16x64_bf16 v[64:67], v[36:39], a[160:167], v113\n\t"
        "v_smfmac_f32_16x16x64_bf16 v[68:71], v[36:39], a[168:175], v113\n\t"
        "v_smfmac_f32_16x16x64_bf16 v[72:75], v[36:39], a[176:183], v113\n\t"
        "v_smfmac_f32_16x16x64_bf16 v[76:79], v[36:39], a[184:191], v113\n\t"
        "s_waitcnt vmcnt(1)\n\t"
        "v_smfmac_f32_16x16x64_bf16 v[64:67], v[40:43], a[192:199], v113\n\t"
        "v_smfmac_f32_16x16x64_bf16 v[68:71], v[40:43], a[200:207], v113\n\t"
        "v_smfmac_f32_16x16x64_bf16 v[72:75], v[40:43], a[208:215], v113\n\t"
        "v_smfmac_f32_16x16x64_bf16 v[76:79], v[40:43], a[216:223], v113\n\t"
        "s_waitcnt vmcnt(0)\n\t"
        "v_smfmac_f32_16x16x64_bf16 v[64:67], v[44:47], a[224:231], v113\n\t"
        "s_nop 2\n\t"
        "v_smfmac_f32_16x16x64_bf16 v[68:71], v[44:47], a[232:239], v113\n\t"
        "s_cmp_gt_u32 %[w], 2\n\t"
        "s_nop 1\n\t"
        "v_smfmac_f32_16x16x64_bf16 v[72:75], v[44:47], a[240:247], v113\n\t"
        "v_add_f32_e32 v80, v64, v66\n\t"
        "s_nop 0\n\t"
        "v_smfmac_f32_16x16x64_bf16 v[76:79], v[44:47], a[248:255], v113\n\t"
        "v_add_f32_e32 v81, v65, v67\n\t"
        "v_add_f32_e32 v82, v68, v70\n\t"
        "v_add_f32_e32 v83, v69, v71\n\t"
        "s_mov_b32 exec_lo, 0\n\t"
        "s_nop 0\n\t"
        "v_add_f32_e32 v80, v72, v74\n\t"
        "v_add_f32_e32 v81, v73, v75\n\t"
        "s_nop 0\n\t"
        "v_pk_add_f32 v[82:83], v[76:77], v[78:79]\n\t"
        "s_mov_b32 exec_lo, -1\n\t"
        "s_nop 0\n\t"
        "v_permlane32_swap_b32_e32 v80, v82\n\t"
        "v_add_f32_e32 v80, v80, v82\n\t"
        "ds_write_b32 v119, v80\n\t"
        "v_permlane32_swap_b32_e32 v81, v83\n\t"
        "v_add_f32_e32 v81, v81, v83\n\t"
        "ds_write_b32 v119, v81 offset:128\n\t"
        "v_cmp_u_f32_e32 vcc, v80, v81\n\t"
        "s_nop 2\n\t"
        "v_mov_b32_e32 v104, v104\n\t"
        "s_cbranch_vccnz .Lfc%=\n\t"
        ".Lrd%=:\n\t"
        "v_mov_b32_e32 v104, v104\n\t"
        "s_cbranch_scc1 .Lno%=\n\t"
        "v_mov_b32_e32 v104, v104\n\t"
        "s_nop 2\n\t"
        "v_mov_b32_e32 v104, v104\n\t"
        "s_waitcnt lgkmcnt(0)\n\t"
        "s_barrier\n\t"
        "v_mov_b32_e32 v104, v104\n\t"
        "ds_read_b64 v[0:1], v120 offset:8192\n\t"
        "ds_read_b64 v[2:3], v120 offset:8704\n\t"
        "ds_read_b64 v[4:5], v120 offset:9216\n\t"
        "ds_read_b64 v[6:7], v120 offset:9728\n\t"
        "v_mov_b32_e32 v104, v104\n\t"
        "s_nop 2\n\t"
        "v_mov_b32_e32 v104, v104\n\t"
        "s_nop 2\n\t"
        "v_mov_b32_e32 v104, v104\n\t"
        "s_waitcnt lgkmcnt(2)\n\t"
        "v_pk_add_f32 v[0:1], v[0:1], v[2:3]\n\t"
        "s_waitcnt lgkmcnt(0)\n\t"
        "v_pk_add_f32 v[4:5], v[4:5], v[6:7]\n\t"
        "v_pk_add_f32 v[0:1], v[0:1], v[4:5]\n\t"
        "v_cvt_pk_bf16_f32 v0, v0, v1\n\t"
        "v_min_u32_e32 v0, -2, v0\n\t"
        "buffer_store_dword v0, v116, %[srd], s67 offen sc1\n\t"
        ".Lnp%=:\n\t"
        "s_memrealtime s[76:77]\n\t"
        "s_add_u32 s40, s40, 1\n\t"
        "s_waitcnt lgkmcnt(0)\n\t"
        "s_add_u32 s81, s76, %c[iVD]\n\t"
        "s_sub_u32 s80, s76, s41\n\t"
        "s_cmp_gt_i32 s80, %c[iTOL]\n\t"
        "s_cselect_b32 s69, s81, s41\n\t"
        "s_branch .Lloop%=\n\t"
        ".Lmv%=:\n\t"
        ".Lhk0%=:\n\t"
        "s_cbranch_execz .Lhk1%=\n\t"
        "s_mov_b32 s48, 1\n\t"
        "s_cmp_eq_u32 s54, %c[iTOL]\n\t"
        "s_cbranch_scc1 .Lm1%=\n\t"
        "s_max_i32 s80, s85, s56\n\t"
        "s_add_u32 s43, s41, s80\n\t"
        "s_add_u32 s43, s43, %c[iPEN0]\n\t"
        ".Lhk1%=:\n\t"
        "s_cbranch_execz .Lhk2%=\n\t"
        "s_mov_b32 s54, %c[iTOL]\n\t"
        "s_add_u32 s46, s46, %c[iX0C]\n\t"
        "s_mov_b32 s95, 0\n\t"
        "s_add_u32 s51, s51, %c[iX0A]\n\t"
        "v_and_b32_e32 v4, 3, v88\n\t"
        "v_and_b32_e32 v5, 3, v90\n\t"
        ".Lhk2%=:\n\t"
        "s_cbranch_execz .Lhk3%=\n\t"
        "v_lshlrev_b32_e64 v4, v4, 1\n\t"
        "v_lshlrev_b32_e64 v5, v5, 1\n\t"
        "v_or_b32_e32 v4, v4, v5\n\t"
        "s_mov_b32 s80, 0\n\t"
        "v_and_b32_e32 v5, 1, v4\n\t"
        "v_cmp_ne_u32_e32 vcc, 0, v5\n\t"
        ".Lhk3%=:\n\t"
        "s_cbranch_execz .Lhk4%=\n\t"
        "s_nop 1\n\t"
        "s_cmp_lg_u64 vcc, 0\n\t"
        "s_cselect_b32 s81, 2, 0\n\t"
        "s_or_b32 s80, s80, s81\n\t"
        "v_and_b32_e32 v5, 2, v4\n\t"
        "v_cmp_ne_u32_e32 vcc, 0, v5\n\t"
        ".Lhk4%=:\n\t"
        "s_cbranch_execz .Lhk5%=\n\t"
        "s_nop 1\n\t"
        "s_cmp_lg_u64 vcc, 0\n\t"
        "s_cselect_b32 s81, 4, 0\n\t"
        "s_or_b32 s80, s80, s81\n\t"
        "v_and_b32_e32 v5, 4, v4\n\t"
        "v_cmp_ne_u32_e32 vcc, 0, v5\n\t"
        ".Lhk5%=:\n\t"
        "s_cbranch_execz .Lhk6%=\n\t"
        "s_nop 1\n\t"
        "s_cmp_lg_u64 vcc, 0\n\t"
        "s_cselect_b32 s81, 8, 0\n\t"
        "s_or_b32 s80, s80, s81\n\t"
        "v_and_b32_e32 v5, 8, v4\n\t"
        "v_cmp_ne_u32_e32 vcc, 0, v5\n\t"
        ".Lhk6%=:\n\t"
        "s_cbranch_execz .Lhk7%=\n\t"
        "s_nop 1\n\t"
        "s_cmp_lg_u64 vcc, 0\n\t"
        "s_cselect_b32 s81, 16, 0\n\t"
        "s_or_b32 s80, s80, s81\n\t"
        "s_lshr_b32 s80, 0x1adf1210, s80\n\t"
        "s_and_b32 s55, s80, 3\n\t"
        ".Lhk7%=:\n\t"
        "s_cbranch_execz .Lhk8%=\n\t"
        "s_branch .Lal%=\n\t"
        ".Lm1%=:\n\t"
        "s_add_u32 s80, s37, %c[iPEN2]\n\t"
        "s_min_i32 s80, s80, %c[iPENM]\n\t"
        "s_cmp_lg_u32 s37, 0\n\t"
        "s_cselect_b32 s37, s80, %c[iPEN]\n\t"
        "s_add_u32 s80, s56, s37\n\t"
        ".Lhk8%=:\n\t"
        "s_cbranch_execz .Lhk9%=\n\t"
        "s_max_i32 s80, s80, %c[iPMIN]\n\t"
        "s_add_u32 s43, s41, s80\n\t"
        "s_add_u32 s95, s93, %c[LA]\n\t"
        "s_add_u32 s46, s46, %c[iXR]\n\t"
        ".Lal%=:\n\t"
        "s_sub_u32 s80, s55, s43\n\t"
        "s_and_b32 s80, s80, %c[iPHM]\n\t"
        ".Lhk9%=:\n\t"
        "s_cbranch_execz .Lhk10%=\n\t"
        "s_add_u32 s43, s43, s80\n\t"
        "s_branch .Lsd%=\n\t"
        ".Lsf%=:\n\t"
        "s_mov_b32 exec_hi, -1\n\t"
        "s_sleep 2\n\t"
        "buffer_load_dwordx4 v[88:91], v112, %[srd], s66 offen sc1\n\t"
        "s_add_u32 s97, s97, 1\n\t"
        ".Lhk10%=:\n\t"
        "s_cbranch_execz .Lhk11%=\n\t"
        "s_cmp_lt_u32 s97, 0x100000\n\t"
        "s_cbranch_scc0 .Lgu%=\n\t"
        "s_waitcnt vmcnt(0)\n\t"
        "s_branch .Lsr%=\n\t"
        ".Lfa%=:\n\t"
        "v_max_u32_e32 v84, v0, v2\n\t"
        "v_max3_u32 v84, v84, v1, v3\n\t"
        ".Lhk11%=:\n\t"
        "s_cbranch_execz .Lhk12%=\n\t"
        "v_max3_u32 v84, v84, v4, v6\n\t"
        "v_max3_u32 v84, v84, v5, v7\n\t"
        "v_max3_u32 v84, v84, v8, v10\n\t"
        "v_max3_u32 v84, v84, v9, v11\n\t"
        "v_max3_u32 v84, v84, v12, v14\n\t"
        "v_max3_u32 v84, v84, v13, v15\n\t"
        ".Lhk12%=:\n\t"
        "s_cbranch_execz .Lhk13%=\n\t"
        "v_cmp_eq_u32_e32 vcc, -1, v84\n\t"
        "s_nop 1\n\t"
        "v_mov_b32_e32 v104, v104\n\t"
        "s_nop 0\n\t"
        "s_cbranch_vccz .Lra%=\n\t"
        "s_add_u32 s80, s40, 1\n\t"
        ".Lhk13%=:\n\t"
        "s_cbranch_execz .Lhk14%=\n\t"
        "v_mov_b32_e32 v80, 161792\n\t"
        "v_mov_b32_e32 v81, s80\n\t"
        "ds_write_b32 v80, v81\n\t"
        "v_mov_b32_e32 v48, 0\n\t"
        "v_mov_b32_e32 v49, 0\n\t"
        "v_mov_b32_e32 v50, 0\n\t"
        ".Lhk14%=:\n\t"
        "s_cbranch_execz .Lhk15%=\n\t"
        "v_mov_b32_e32 v51, 0\n\t"
        "v_mov_b32_e32 v52, 0\n\t"
        "v_mov_b32_e32 v53, 0\n\t"
        "v_mov_b32_e32 v54, 0\n\t"
        "v_mov_b32_e32 v55, 0\n\t"
        "v_mov_b32_e32 v56, 0\n\t"
        ".Lhk15%=:\n\t"
        "s_cbranch_execz .Lhk16%=\n\t"
        "v_mov_b32_e32 v57, 0\n\t"
        "v_mov_b32_e32 v58, 0\n\t"
        "v_mov_b32_e32 v59, 0\n\t"
        "v_mov_b32_e32 v60, 0\n\t"
        "v_mov_b32_e32 v61, 0\n\t"
        "v_mov_b32_e32 v62, 0\n\t"
        ".Lhk16%=:\n\t"
        "s_cbranch_execz .Lhk17%=\n\t"
        "v_mov_b32_e32 v63, 0\n\t"
        "s_sleep %c[VASL]\n\t"
        "buffer_load_dwordx4 v[0:3], v112, %[srd], s57 offen sc1\n\t"
        "buffer_load_dwordx4 v[4:7], v112, %[srd], s58 offen sc1\n\t"
        "buffer_load_dwordx4 v[8:11], v112, %[srd], s59 offen sc1\n\t"
        "buffer_load_dwordx4 v[12:15], v112, %[srd], s60 offen sc1\n\t"
        ".Lhk17%=:\n\t"
        "s_cbranch_execz .Lhk18%=\n\t"
        "buffer_store_dword v0, off, %[wsd], s36\n\t"
        "s_add_u32 s97, s97, 1\n\t"
        "s_cmp_lt_u32 s97, 0x100000\n\t"
        "s_cbranch_scc0 .Lgu%=\n\t"
        "s_branch .Lgo%=\n\t"
        ".Lfc%=:\n\t"
        "v_max_u32_e32 v84, v16, v18\n\t"
        ".Lhk18%=:\n\t"
        "s_cbranch_execz .Lhk19%=\n\t"
        "v_max3_u32 v84, v84, v20, v22\n\t"
        "v_max3_u32 v84, v84, v24, v26\n\t"
        "v_max3_u32 v84, v84, v28, v30\n\t"
        "v_max3_u32 v84, v84, v32, v34\n\t"
        "v_max3_u32 v84, v84, v36, v38\n\t"
        "v_max3_u32 v84, v84, v40, v42\n\t"
        ".Lhk19%=:\n\t"
        "s_cbranch_execz .Lhk20%=\n\t"
        "v_max3_u32 v84, v84, v44, v46\n\t"
        "v_cmp_eq_u32_e32 vcc, -1, v84\n\t"
        "s_nop 1\n\t"
        "v_mov_b32_e32 v104, v104\n\t"
        "s_nop 0\n\t"
        "s_cbranch_vccz .Lrd%=\n\t"
        ".Lhk20%=:\n\t"
        "s_cbranch_execz .Lhk21%=\n\t"
        "s_sleep %c[CSLP]\n\t"
        "buffer_load_dwordx4 v[16:19], v112, %[srd], s63 offen sc1\n\t"
        "v_mov_b32_e32 v64, 0\n\t"
        "v_mov_b32_e32 v65, 0\n\t"
        "buffer_load_dwordx4 v[20:23], v115, %[srd], s63 offen sc1\n\t"
        "v_mov_b32_e32 v66, 0\n\t"
        ".Lhk21%=:\n\t"
        "s_cbranch_execz .Lhk22%=\n\t"
        "v_mov_b32_e32 v67, 0\n\t"
        "buffer_load_dwordx4 v[24:27], v112, %[srd], s64 offen sc1\n\t"
        "v_mov_b32_e32 v68, 0\n\t"
        "v_mov_b32_e32 v69, 0\n\t"
        "buffer_load_dwordx4 v[28:31], v115, %[srd], s64 offen sc1\n\t"
        "v_mov_b32_e32 v70, 0\n\t"
        ".Lhk22%=:\n\t"
        "s_cbranch_execz .Lhk23%=\n\t"
        "v_mov_b32_e32 v71, 0\n\t"
        "buffer_load_dwordx4 v[32:35], v112, %[srd], s65 offen sc1\n\t"
        "v_mov_b32_e32 v72, 0\n\t"
        "v_mov_b32_e32 v73, 0\n\t"
        "buffer_load_dwordx4 v[36:39], v115, %[srd], s65 offen sc1\n\t"
        "v_mov_b32_e32 v74, 0\n\t"
        ".Lhk23%=:\n\t"
        "s_cbranch_execz .Lhk24%=\n\t"
        "v_mov_b32_e32 v75, 0\n\t"
        "buffer_load_dwordx4 v[40:43], v112, %[srd], s73 offen sc1\n\t"
        "v_mov_b32_e32 v76, 0\n\t"
        "v_mov_b32_e32 v77, 0\n\t"
        "buffer_load_dwordx4 v[44:47], v115, %[srd], s73 offen sc1\n\t"
        "v_mov_b32_e32 v78, 0\n\t"
        ".Lhk24%=:\n\t"
        "s_cbranch_execz .Lhk25%=\n\t"
        "v_mov_b32_e32 v79, 0\n\t"
        "s_add_u32 s80, s40, 1\n\t"
        "v_mov_b32_e32 v80, 161796\n\t"
        "v_mov_b32_e32 v81, s80\n\t"
        "ds_write_b32 v80, v81\n\t"
        "s_add_u32 s97, s97, 1\n\t"
        ".Lhk25%=:\n\t"
        "s_cbranch_execz .Lhk26%=\n\t"
        "s_cmp_lt_u32 s97, 0x100000\n\t"
        "s_cbranch_scc0 .Lgu%=\n\t"
        "s_branch .Lrc%=\n\t"
        ".Lno%=:\n\t"
        "s_waitcnt lgkmcnt(0)\n\t"
        "s_barrier\n\t"
        "s_branch .Lnp%=\n\t"
        ".Lta%=:\n\t"
        ".Lhk26%=:\n\t"
        "s_cbranch_execz .Lhk27%=\n\t"
        "s_waitcnt lgkmcnt(0)\n\t"
        "s_branch .Lxa%=\n\t"
        ".Ltc%=:\n\t"
        "s_waitcnt lgkmcnt(0)\n\t"
        "s_branch .Lxc%=\n\t"
        ".Lgu%=:\n\t"
        "s_mov_b64 exec, -1\n\t"
        "v_mov_b32_e32 v80, %c[ferr]\n\t"
        ".Lhk27%=:\n\t"
        "s_cbranch_execz .Lhk28%=\n\t"
        "v_mov_b32_e32 v81, 2\n\t"
        "buffer_store_dword v81, v80, %[wsd], 0 offen sc1\n\t"
        "s_endpgm\n\t"
        ".Lend%=:\n\t"
        "buffer_store_dword v80, v114, %[wsd], s72 offen sc1\n\t"
        "s_endpgm\n\t"
        ".Lhk28%=:\n\t"
        "s_branch .Lhr%="
        : [nA] "+s"(nA), [nS] "+s"(nS), [nT] "+s"(nT), [nW] "+s"(nW), [nC] "+s"(nC), [b0] "+s"(b0), [pob] "+s"(pob)
        : [srd] "s"(rhd), [wsd] "s"(wsd), [w] "s"(w), [tag] "s"(tag), [kvo] "s"(kvo), [cal] "s"(cal), [sv] "s"(sv), [so] "s"(so), [m0b] "s"(m0b), [key] "s"(key), [tu] "s"(tu),
          [ferr] "i"(F_ERR), [iD1] "i"(D1), [iD2] "i"(D2), [LC] "i"(LC0), [LA] "i"(LA0), [LC24] "i"(LC0 + 24), [LA24] "i"(LA0 + 24), [E1] "i"(2 * (D1 - LC0)), [E2] "i"(2 * (D2 - LA0)),
          [iPEN] "i"(PEN), [iPEN2] "i"(PEN2), [iPENM] "i"(PEN + 2 * PEN2), [iPEN0] "i"(PEN0), [iTOL] "i"(TOL), [iCT0] "i"(CT0), [iX0C] "i"(X0C), [iX0A] "i"(X0A), [iPHM] "i"(PH_M),
          [iFT] "i"(-3), [iFN] "i"(FINE_N), [AUP] "i"(A_UP), [ASLP] "i"(A_SLP), [LDEC] "i"(L_DEC), [UPSH] "i"(UP_SH), [TRUP] "i"(TR_UP), [TRDN] "i"(TR_DN), [HSLP] "i"(H_SLP), [iPMIN] "i"(PMIN), [VSLP] "i"(V_SLP), [RSLP] "i"(R_SLP), [iVD] "i"(V_DEL), [iXR] "i"(XR), [VASL] "i"(VA_SLP), [CSLP] "i"(C_SLP), [DNSH] "i"(DN_SH), [NX0] "i"(NEARX), [NX1] "i"(8192 - NEARX), [iCS1] "i"(CS1), [iCS2] "i"(CS2), [NY0] "i"(NEARY), [NY1] "i"(16384 - NEARY),
          "{v[112:127]}"(pc), "{v[96:103]}"(pa), "{v[104:105]}"(pb)
        : "memory", "vcc", "scc", "m0", "s36", "s37", "s38", "s39", "s40", "s41", "s42", "s43", "s44", "s45", "s46", "s47", "s48", "s49", "s50", "s51", "s52", "s53", "s54", "s55", "s56", "s57", "s58", "s59", "s60", "s61", "s62", "s63", "s64", "s65", "s66", "s67", "s68", "s69", "s70", "s71", "s72", "s73", "s74", "s75", "s76", "s77", "s78", "s79", "s80", "s81", "s82", "s83", "s84", "s85", "s86", "s87", "s88", "s89", "s90", "s91", "s92", "s93", "s94", "s95", "s96", "s97", "s98", "s99");
    }
}

using f32x16 = __attribute__((ext_vector_type(16))) float;
constexpr int CV_RP = 80;
constexpr int CV_U = 96 * CV_RP;
constexpr int CV_KP = 4416;
constexpr int CV_K0 = 4 * CV_U;
constexpr int CV_Y = CV_K0 + 16 * CV_KP;
__device__ __forceinline__ float lo16(unsigned x) { return __uint_as_float(x << 16); }
__device__ __forceinline__ float hi16(unsigned x) { return __uint_as_float(x & 0xFFFF0000u); }
__device__ __forceinline__ void prep_block(const Params& p, rsrc_t r, int k4, const int wv, const unsigned tag) {
    const int tid = otid(), w = wv, l = tid & 63;
    const int cu = (k4 & 3) * 32 + w * 8 + (l >> 3), oc0 = (k4 >> 2) * 8 + (l & 7);
    const rsrc_t rk = __builtin_amdgcn_make_buffer_rsrc((void*)p.Ak, 0, S * D * 2, 0x00020000), rv = __builtin_amdgcn_make_buffer_rsrc((void*)p.Av, 0, S * D * 2, 0x00020000);
    v4u sk[4], kn[11], vn[8], gw[2];
    auto fetch = [&](int oc) {
#pragma unroll
        for (int j = 0; j < 11; ++j) kn[j] = __builtin_amdgcn_raw_buffer_load_b128(rk, (unsigned)((oc * 8 + j - 3) * 2048 + cu * 16), 0, 2);
#pragma unroll
        for (int i = 0; i < 8; ++i) vn[i] = __builtin_amdgcn_raw_buffer_load_b128(rv, (unsigned)((oc * 8 + i) * 2048 + cu * 16), 0, 2); };
    fetch(oc0);
#pragma unroll
    for (int q = 0; q < 4; ++q) sk[q] = *(const v4u*)(p.shift_k + cu * 32 + q * 8);
#pragma unroll
    for (int q = 0; q < 2; ++q) gw[q] = *(const v4u*)(p.gate_w + q * 512 + l * 8);
    st4(r, tid < 128 ? F_RDY + k4 * 512 + tid * 4 : tid < 132 ? F_ATT + (k4 * 4 + tid - 128) * 64 : (tid == 192 && k4 == 0) ? F_CONV : 0xF0000000u, 0u);
#pragma unroll 1
    for (int it = 0; it < 4; ++it) {
        const int oc = oc0 + 64 * it, tk = (it * 32 + k4) * 16 + w;
        v4u kr[11], vr[8], xg[4][2];
#pragma unroll
        for (int j = 0; j < 11; ++j) kr[j] = kn[j];
#pragma unroll
        for (int i = 0; i < 8; ++i) vr[i] = vn[i];
        __builtin_amdgcn_sched_barrier(0);
#pragma unroll
        for (int i = 0; i < 4; ++i)
#pragma unroll
            for (int q = 0; q < 2; ++q) xg[i][q] = *(const v4u*)(p.Aq + (size_t)(tk + 4 * i) * D + q * 512 + l * 8);
        fetch(it < 3 ? oc + 64 : 4096);
        __builtin_amdgcn_sched_barrier(0);
#pragma unroll
        for (int c8 = 0; c8 < 8; ++c8) {
            const unsigned s01 = sk[c8 >> 1][(c8 & 1) * 2], s23 = sk[c8 >> 1][(c8 & 1) * 2 + 1];
            const float sv[4] = {hi16(s23), lo16(s23), hi16(s01), lo16(s01)};
            float kk[11], u[8];
#pragma unroll
            for (int j = 0; j < 11; ++j) kk[j] = (c8 & 1) ? hi16(kr[j][c8 >> 1]) : lo16(kr[j][c8 >> 1]);
            f32x2 acc[4];
#pragma unroll
            for (int j = 0; j < 4; ++j)
#pragma unroll
                for (int i = 0; i < 4; ++i) { if (FASTP) { const f32x2 t = (f32x2){kk[i + j], kk[i + j + 4]} * sv[j]; acc[i] = j == 0 ? t : j == 3 ? rbf2(acc[i] + t) : acc[i] + t; }
                    else { const f32x2 t = rbf2((f32x2){kk[i + j], kk[i + j + 4]} * sv[j]); acc[i] = j == 0 ? t : rbf2(acc[i] + t); } }
#pragma unroll
            for (int i = 0; i < 8; ++i) u[i] = acc[i & 3][i >> 2] * ((c8 & 1) ? hi16(vr[i][c8 >> 1]) : lo16(vr[i][c8 >> 1]));
            st16(r, W_U + (cu * 8 + c8) * 4096 + oc * 16, (v4u){pack2(u[7], u[6]), pack2(u[5], u[4]), pack2(u[3], u[2]), pack2(u[1], u[0])});
        }
        float ga[4];
#pragma unroll
        for (int i = 0; i < 4; ++i) { float acc = 0.f;
#pragma unroll
            for (int q = 0; q < 2; ++q)
#pragma unroll
                for (int e = 0; e < 4; ++e) { const unsigned xa = xg[i][q][e], gb = gw[q][e]; acc += rbfh(lo16(xa) * lo16(gb)); acc += rbfh(hi16(xa) * hi16(gb)); }
            ga[i] = acc; }
        for (int o = 32; o > 0; o >>= 1)
#pragma unroll
            for (int i = 0; i < 4; ++i) ga[i] += __shfl_xor(ga[i], o);
        const float z = rbfh(l == 0 ? ga[0] : l == 1 ? ga[1] : l == 2 ? ga[2] : ga[3]);
        st4(r, l < 4 ? W_GATE + (tk + 4 * l) * 4 : 0xF0000000u, __float_as_uint(rbfh(1.f / (1.f + expf(-z)))));
    }
    DRAIN(); __syncthreads();
    if (tid == 0) st4(r, F_PREP + k4 * 4, tag);
}
__device__ __forceinline__ bool conv_block(const Params& p, rsrc_t r, const int g0, const int n, unsigned char* lds, const int wv, const int j, const unsigned tag, const unsigned skew) {
    (void)j;
    const int tid = otid(), w = wv, l = tid & 63;
    const rsrc_t rs = __builtin_amdgcn_make_buffer_rsrc((void*)p.ssm_k, 0, S * D * 2, 0x00020000);
    v4u kr[4], uv[4]; v2u kx[4]; unsigned dn;
    auto fetch_k = [&](int ch) {
#pragma unroll
        for (int i = 0; i < 4; ++i) { const unsigned o = (unsigned)(ch * 4096 + (i * 64 + l) * 16);
            kr[i] = __builtin_amdgcn_raw_buffer_load_b128(rs, o, 0, 2); kx[i] = __builtin_amdgcn_raw_buffer_load_b64(rs, (i == 3 && l == 63) ? 0xF0000000u : o + 16, 0, 2); }
        dn = p.ssm_D[ch & 1023]; };
    auto fetch_u = [&](unsigned o) {
#pragma unroll
        for (int i = 0; i < 4; ++i) uv[i] = ld16(r, o + (i * 64 + l) * 16); };
    fetch_k(g0 * 4 + w);
    unsigned char* const uc = lds + w * CV_U; unsigned char* const kc = lds + CV_K0 + w * 4 * CV_KP;
    {
        const v4u z = {0u, 0u, 0u, 0u};
#pragma unroll
        for (int i = 0; i < 3; ++i) *(v4u*)(uc + min(i * 64 + l, 159) * 16) = z;
#pragma unroll
        for (int i = 0; i < 2; ++i) { const int q = min(i * 64 + l, 79), e = q % 20; *(v4u*)(kc + (q / 20) * CV_KP + e * 16 + (e < 8 ? 0 : 4096)) = z; }
    }
    if (!wait_prep(r, l, tag)) return false;
    wait_long(rtnow() + skew);
    fetch_u(W_U + (g0 * 4 + w) * 4096);
    const int b = l & 31, kg = l >> 5, cc = (b + 1) & 3;
#pragma unroll 1
    for (int it = 0; it < n; ++it) {
        const unsigned dv = dn;
#pragma unroll
        for (int i = 0; i < 4; ++i) { const v4u d = kr[i]; const v2u e = kx[i]; unsigned char* q = kc + 128 + (i * 64 + l) * 16;
            const unsigned s0 = __builtin_amdgcn_alignbit(d.y, d.x, 16), s1 = __builtin_amdgcn_alignbit(d.z, d.y, 16), s2 = __builtin_amdgcn_alignbit(d.w, d.z, 16), s3 = __builtin_amdgcn_alignbit(e.x, d.w, 16), s4 = __builtin_amdgcn_alignbit(e.y, e.x, 16);
            *(v4u*)q = d; *(v4u*)(q + CV_KP) = (v4u){s0, s1, s2, s3}; *(v4u*)(q + 2 * CV_KP) = (v4u){d.y, d.z, d.w, e.x}; *(v4u*)(q + 3 * CV_KP) = (v4u){s1, s2, s3, s4};
            if (i == 0 && l == 0) { *(unsigned*)(q + CV_KP - 4) = d.x << 16; *(unsigned*)(q + 2 * CV_KP - 4) = d.x; *(v2u*)(q + 3 * CV_KP - 8) = (v2u){d.x << 16, s0}; } }
#pragma unroll
        for (int i = 0; i < 4; ++i) *(v4u*)(uc + (32 + i * 16 + (l >> 2)) * CV_RP + (l & 3) * 16) = uv[i];
        __builtin_amdgcn_wave_barrier();
        __builtin_amdgcn_sched_barrier(0);
        fetch_k(it + 1 < n ? (g0 + it + 1) * 4 + w : 1024); fetch_u(it + 1 < n ? W_U + ((g0 + it + 1) * 4 + w) * 4096 : 0xF0000000u);
        __builtin_amdgcn_sched_barrier(0);
        f32x16 acc0, acc1, ad0, ad1;
#pragma unroll
        for (int i = 0; i < 16; ++i) { acc0[i] = 0.f; acc1[i] = 0.f; ad0[i] = 0.f; ad1[i] = 0.f; }
        typedef const __attribute__((address_space(3))) v2u* l2p;
        unsigned kb = (unsigned)(size_t)(kc + cc * CV_KP + (b - 8 * kg - 7 + 64 - cc) * 2) - 32, kb8 = kb + 8;
        asm volatile("" : "+v"(kb), "+v"(kb8));
        const unsigned char* ub = uc + (32 + b) * CV_RP + kg * 16;
        v2u ka[4][2], kz[4][2]; bf16x8 fa[4], fz[4];
        auto ld_k = [&](int set, int un, int hi) { const int dl = un >> 1, kh = un & 1; const unsigned o = (unsigned)((32 * dl - 16 * kh + 16) * 2 + hi * 2048);
            if (hi) { kz[set][0] = *(l2p)(size_t)(kb + o); kz[set][1] = *(l2p)(size_t)(kb8 + o); } else { ka[set][0] = *(l2p)(size_t)(kb + o); ka[set][1] = *(l2p)(size_t)(kb8 + o); } };
        auto ld_f = [&](int set, int un) { const int dl = un >> 1, kh = un & 1;
            fa[set] = *(const bf16x8*)(ub + (32 - dl) * CV_RP + kh * 32); fz[set] = *(const bf16x8*)(ub - dl * CV_RP + kh * 32); };
#pragma unroll
        for (int q = 0; q < 2; ++q) { ld_k(q, q, 0); ld_f(q, q); ld_k(q, q, 1); }
#pragma unroll
        for (int kh = 0; kh < 2; ++kh) { const int e = 7 - (b - 16 * kh - 8 * kg); const unsigned x = dv << ((e & 1) * 16); const int q = e >> 1;
            const v4u di = {q == 0 ? x : 0u, q == 1 ? x : 0u, q == 2 ? x : 0u, q == 3 ? x : 0u};
            asm volatile("s_nop 1\n\tv_mfma_f32_32x32x16_bf16 %0, %2, %4, %0\n\tv_mfma_f32_32x32x16_bf16 %1, %3, %4, %1" : "+v"(ad1), "+v"(ad0) : "v"(fa[kh]), "v"(fz[kh]), "v"(di)); }
#pragma unroll 1
        for (int u0 = 0; u0 < 64; u0 += 4) {
#pragma unroll
            for (int q = 0; q < 4; ++q) {
                const int un = u0 + q + 2, ns = (q + 2) & 3;
                const v4u kav = {ka[q][0].x, ka[q][0].y, ka[q][1].x, ka[q][1].y}, kzv = {kz[q][0].x, kz[q][0].y, kz[q][1].x, kz[q][1].y};
                __builtin_amdgcn_sched_barrier(0);
                asm volatile("s_nop 1\n\tv_mfma_f32_32x32x16_bf16 %0, %1, %2, %0" : "+v"(acc1) : "v"(fa[q]), "v"(kav));
                ld_k(ns, un, 0);
                __builtin_amdgcn_sched_barrier(0);
                asm volatile("v_mfma_f32_32x32x16_bf16 %0, %1, %2, %0" : "+v"(acc0) : "v"(fz[q]), "v"(kav));
                ld_f(ns, un);
                __builtin_amdgcn_sched_barrier(0);
                asm volatile("v_mfma_f32_32x32x16_bf16 %0, %1, %2, %0" : "+v"(acc1) : "v"(fz[q]), "v"(kzv));
                ld_k(ns, un, 1);
            }
        }
        asm volatile("s_nop 7\n\ts_nop 7" : "+v"(acc0), "+v"(acc1), "+v"(ad0), "+v"(ad1));
        unsigned char* const yb = lds + CV_Y + (it & 1) * 16384;
#pragma unroll
        for (int i = 0; i < 32; i += 2) { const int row = (i >> 4) * 32 + (i & 3) + 8 * ((i & 15) >> 2) + 4 * kg;
            const unsigned y = pack2(rbfh(i < 16 ? acc0[i] : acc1[i - 16]) + rbfh(i < 16 ? ad0[i] : ad1[i - 16]), rbfh(i < 16 ? acc0[i + 1] : acc1[i - 15]) + rbfh(i < 16 ? ad0[i + 1] : ad1[i - 15]));
            *(unsigned short*)(yb + (row * 32 + b) * 8 + w * 2) = (unsigned short)y; *(unsigned short*)(yb + (row * 32 + 32 + b) * 8 + w * 2) = (unsigned short)(y >> 16); }
        asm volatile("s_waitcnt lgkmcnt(0)\n\ts_barrier" ::: "memory");
#pragma unroll
        for (int i = 0; i < 4; ++i) { const int t = tid + 256 * i; st16(r, W_U + (g0 + it) * 16384 + t * 16, *(const v4u*)(yb + t * 16)); }
    }
    DRAIN(); __syncthreads();
    if (tid == 0) __hip_atomic_fetch_add((unsigned*)(p.ws + F_CONV), (unsigned)n, __ATOMIC_RELAXED, __HIP_MEMORY_SCOPE_AGENT);
    return true;
}

__device__ __forceinline__ void dma16(unsigned ldsdst, unsigned voff, const v4u& srd, unsigned soff) {
    asm volatile("s_mov_b32 m0, %0\n\ts_nop 0\n\tbuffer_load_dwordx4 %1, %2, %3 offen lds" :: "s"(ldsdst), "v"(voff), "s"(srd), "s"(soff) : "m0", "memory"); }
__device__ __forceinline__ void dma16n(unsigned ldsdst, unsigned voff, const v4u& srd, unsigned soff) {
    asm volatile("s_mov_b32 m0, %0\n\ts_nop 0\n\tbuffer_load_dwordx4 %1, %2, %3 offen nt lds" :: "s"(ldsdst), "v"(voff), "s"(srd), "s"(soff) : "m0", "memory"); }
__device__ __forceinline__ v4u pack_acc(const f32x4& a, const f32x4& b) {
    float x0, x1, x2, x3, y0, y1, y2, y3;
    asm volatile("v_accvgpr_read_b32 %0, %8\n\tv_accvgpr_read_b32 %1, %9\n\tv_accvgpr_read_b32 %2, %10\n\tv_accvgpr_read_b32 %3, %11\n\t"
                 "v_accvgpr_read_b32 %4, %12\n\tv_accvgpr_read_b32 %5, %13\n\tv_accvgpr_read_b32 %6, %14\n\tv_accvgpr_read_b32 %7, %15"
                 : "=v"(x0), "=v"(x1), "=v"(x2), "=v"(x3), "=v"(y0), "=v"(y1), "=v"(y2), "=v"(y3)
                 : "a"(a[0]), "a"(a[1]), "a"(a[2]), "a"(a[3]), "a"(b[0]), "a"(b[1]), "a"(b[2]), "a"(b[3]));
    return (v4u){pack2(x0, x1), pack2(x2, x3), pack2(y0, y1), pack2(y2, y3)}; }
#define SB() __builtin_amdgcn_sched_barrier(0)
#define WO_REGS "a128", "a129", "a130", "a131", "a132", "a133", "a134", "a135", "a136", "a137", "a138", "a139", "a140", "a141", "a142", "a143", "a144", "a145", "a146", "a147", "a148", "a149", "a150", "a151", "a152", "a153", "a154", "a155", "a156", "a157", "a158", "a159", "a160", "a161", "a162", "a163", "a164", "a165", "a166", "a167", "a168", "a169", "a170", "a171", "a172", "a173", "a174", "a175", "a176", "a177", "a178", "a179", "a180", "a181", "a182", "a183", "a184", "a185", "a186", "a187", "a188", "a189", "a190", "a191", "a192", "a193", "a194", "a195", "a196", "a197", "a198", "a199", "a200", "a201", "a202", "a203", "a204", "a205", "a206", "a207", "a208", "a209", "a210", "a211", "a212", "a213", "a214", "a215", "a216", "a217", "a218", "a219", "a220", "a221", "a222", "a223", "a224", "a225", "a226", "a227", "a228", "a229", "a230", "a231", "a232", "a233", "a234", "a235", "a236", "a237", "a238", "a239", "a240", "a241", "a242", "a243", "a244", "a245", "a246", "a247", "a248", "a249", "a250", "a251", "a252", "a253", "a254", "a255", "v192", "v193", "v194", "v195", "v196", "v197", "v198", "v199", "v200", "v201", "v202", "v203", "v204", "v205", "v206", "v207", "v208", "v209", "v210", "v211", "v212", "v213", "v214", "v215", "v216", "v217", "v218", "v219", "v220", "v221", "v222", "v223", "v224", "v225", "v226", "v227", "v228", "v229", "v230", "v231", "v232", "v233", "v234", "v235", "v236", "v237", "v238", "v239", "v240", "v241", "v242", "v243", "v244", "v245", "v246", "v247", "v248", "v249", "v250", "v251", "v252", "v253", "v254", "v255"
struct NoHook { __device__ void operator()(int) const {} };
template <int MODE, int MI, int NJ, int NS, int HX = 0, class HOOK = NoHook, int MN = MI>
__device__ __forceinline__ void gemm(rsrc_t r, const bf16_t* __restrict__ A, const bf16_t* __restrict__ B, int m0, int n0, unsigned char* lds, unsigned coff, const int wv,
                                     const int rot, const void* tk = nullptr,
                                     const bool have = false, const bf16_t* Bn = nullptr, int m0n = 0, int n0n = 0, HOOK&& hook = NoHook()) {
    constexpr int AB = MN * 4096, BB = NJ * 2048, B0 = (NS / 2) * AB, NB = NJ / 2, NM = MI * NJ, NR = MI + NJ, BP = NJ * 64;
    static_assert(NS % 2 == 0 && 32 % NS == 0 && B0 <= 65536 && NS * BB <= 65536 && (NJ == 4 || NJ == 8), "ring");
    const int tid = otid();
    const int w = wv, l = tid & 63, n16 = l & 15, g = l >> 4;
    const int wm = w >> 1, wn = w & 1;
    const unsigned lb = __builtin_amdgcn_readfirstlane((unsigned)(size_t)lds);
    const v4u sa = mk_srd(A, 2048 * 1024 * 2); v4u sb = mk_srd(B, 1024 * 4096 * 2);
    f32x4 acc[MI][NJ];
#pragma unroll
    for (int i = 0; i < MI; ++i)
#pragma unroll
        for (int j = 0; j < NJ; ++j) acc[i][j] = (f32x4){0.f, 0.f, 0.f, 0.f};
    unsigned va = (unsigned)((m0 + 8 * MI * w + (l >> 3)) * 2048 + (((l & 7) ^ (l >> 3)) << 4));
    unsigned vb[2];
    if (NJ == 8) { vb[0] = (unsigned)((8 * w + (l >> 5)) * 8192 + n0 * 2) + (unsigned)(((l & 31) << 4) ^ ((l >> 5) << 5) ^ (wn << 7)); vb[1] = vb[0] ^ 64u; }
    else { vb[0] = (unsigned)((8 * w + g) * 8192 + n0 * 2) + (unsigned)((n16 << 4) ^ (g << 5) ^ (wn << 7)); vb[1] = vb[0]; }
    auto rk = [&](int kt) { return (kt & ~31) | ((kt + rot) & 31); };
    unsigned la = lb + w * MI * 1024;
    auto st_a = [&](int buf, int kt, int q) { dma16(la + buf * AB + q * 1024, va, sa, (unsigned)(rk(kt) * 64 + q * 16384)); };
    auto st_b = [&](int buf, int kt, int x) { if ((B_NT >> (NJ == 4 ? 0 : MI == 4 ? 1 : 2)) & 1) dma16n(lb + B0 + buf * BB + (w * NB + x) * 1024, vb[x & 1], sb, (unsigned)((rk(kt) * 32 + x * (8 / NB)) * 8192));
        else dma16(lb + B0 + buf * BB + (w * NB + x) * 1024, vb[x & 1], sb, (unsigned)((rk(kt) * 32 + x * (8 / NB)) * 8192)); };
    const unsigned f = (unsigned)((n16 >> 2) | ((g & 1) << 2));
    constexpr int NQ = MODE == 1 ? NJ : NJ / 2;
    unsigned ra[2], rb[NQ];
    ra[0] = lb + (16 * MI * wm + n16) * 128 + ((g ^ (n16 & 7)) << 4); ra[1] = ra[0] ^ 64u;
    {   const unsigned row = lb + B0 + (8 * g + (n16 >> 2)) * BP + (NJ == 8 ? 256 * wn : 0), g0 = NJ == 8 ? 0u : 4u * wn;
#pragma unroll
        for (int j = 0; j < NQ; ++j) {
            if (MODE == 1) rb[j] = row + (((g0 + j) ^ f) << 5) + 8 * (n16 & 3);
            else rb[j] = row + (((g0 + 2 * j + ((n16 & 3) >> 1)) ^ f) << 5) + (n16 & 1) * 16;
            asm volatile("" : "+v"(rb[j])); } }
    bf16x8 af[2][MI], bfr[2][NJ];
    auto rd = [&](int set, int buf, int x) {
        if (x < NJ) { const unsigned a = rb[MODE == 1 ? x : (x >> 1)] + (MODE == 1 ? 0 : (x & 1) * 8) + buf * BB;
            const v2u lo = trb(a), hi = trb(a + 4 * BP); v4u v = {lo.x, lo.y, hi.x, hi.y}; bfr[set][x] = as_frag(v); }
        else af[set][x - NJ] = ldsa(ra[buf & 1] + (buf >> 1) * AB + (x - NJ) * 2048); };
    auto mf = [&](int set, int i, int j) {
        if (HX) asm volatile("v_mfma_f32_16x16x32_bf16 %0, %1, %2, %0" : "+a"(acc[i][j]) : "v"(MODE == 1 ? af[set][i] : bfr[set][j]), "v"(MODE == 1 ? bfr[set][j] : af[set][i]) : WO_REGS);
        else if (MODE == 1) asm volatile("v_mfma_f32_16x16x32_bf16 %0, %1, %2, %0" : "+a"(acc[i][j]) : "v"(af[set][i]), "v"(bfr[set][j]));
        else asm volatile("v_mfma_f32_16x16x32_bf16 %0, %1, %2, %0" : "+a"(acc[i][j]) : "v"(bfr[set][j]), "v"(af[set][i])); };
    if (!have) {
#pragma unroll
        for (int t = 0; t < NS; ++t) {
            if (t & 1) {
#pragma unroll
                for (int q = 0; q < MI; ++q) st_a(t >> 1, t - 1, q); }
#pragma unroll
            for (int q = 0; q < NB; ++q) st_b(t, t, q);
            if (HX) hook(t); }
    }
#pragma unroll
    for (int i = 0; i < MI; ++i)
#pragma unroll
        for (int j = 0; j < NJ; ++j) asm volatile("" : "+a"(acc[i][j]));
    asm volatile("s_nop 1" ::: "memory");
    if (!have) {
        asm volatile("s_waitcnt vmcnt(%c0)\n\ts_barrier" :: "i"((NS - 1) * (NB + HX) + (NS / 2 - 1) * MI) : "memory");
    }
#pragma unroll
    for (int x = 0; x < NR; ++x) rd(0, 0, x);
    constexpr int HEAD = NM / 8;
    const v4u st = mk_srd(tk, tk ? 32768 : 0); unsigned tdum = 0u;
#pragma unroll 1
    for (int k0 = 0; k0 < 32; k0 += NS) {
        int kb = k0 + NS;
        if (kb == 32) {
            kb = Bn ? 0 : 0x10100;
            if (Bn) { va += (unsigned)((m0n - m0 + 8 * (MN - MI) * w) * 2048); la = lb + w * MN * 1024; vb[0] += (unsigned)((n0n - n0) * 2); vb[1] += (unsigned)((n0n - n0) * 2); sb = mk_srd(Bn, 1024 * 4096 * 2); } }
        if (NS == 4) asm volatile("buffer_load_dword %0, %1, %2, %3 offen" : "+v"(tdum) : "v"(l < 32 ? (unsigned)l * 128u : 0xF0000000u), "s"(st), "s"((unsigned)k0 * 1024u) : "memory");
#pragma unroll
        for (int u = 0; u < NS; ++u) {
            SB();
#pragma unroll
            for (int q = 0; q < HEAD; ++q) mf(u & 1, q / NJ, q % NJ);
            SB();
            asm volatile("s_waitcnt vmcnt(%c0) lgkmcnt(0)\n\ts_barrier" :: "i"((NS - 2) * (NB + HX) + (NS / 2 - 1 - (u & 1)) * MI) : "memory");
            SB();
            constexpr int span = NM - HEAD;
#pragma unroll
            for (int q = HEAD; q < NM; ++q) {
                mf(u & 1, q / NJ, q % NJ);
                const int x = q - HEAD, nd = NB + ((u & 1) ? MI : 0);
#pragma unroll
                for (int y = 0; y < NR; ++y) if ((y * (span - HEAD)) / NR == x) rd((u & 1) ^ 1, (u + 1) % NS, y);
#pragma unroll
                for (int y = 0; y < nd; ++y) if ((y * span + span / 2) / nd == x) { if ((u & 1) && y < MI) st_a(u >> 1, kb + u - 1, y); else st_b(u, kb + u, y - ((u & 1) ? MI : 0)); }
                SB(); }
            if (MN > MI && (u & 1) && k0 == 32 - NS) {
#pragma unroll
                for (int y = MI; y < MN; ++y) st_a(u >> 1, kb + u - 1, y); }
            if (HX) hook(NS + k0 + u);
        }
    }
    asm volatile("s_nop 7\n\ts_nop 3" : "+v"(tdum) :: "memory");
    SB();
    const int col = n0 + 16 * NJ * wn, head = col >> 7, sub = (col & 127) >> 4, seg0 = (m0 >> 5) + (MI / 2) * wm;
#pragma unroll
    for (int sg = 0; sg < MI / 2; ++sg) {
        const unsigned base = coff + (unsigned)(((seg0 + sg) * 32 + head) * 8192 + l * 16);
#pragma unroll
        for (int q = 0; q < NJ; ++q) {
            if (HX) asm volatile("" ::: WO_REGS);
            if (EPI_SLP && NJ == 8) { SB(); __builtin_amdgcn_s_sleep(EPI_SLP); SB(); }
            if (MODE == 0) { const int tt = q / (NJ / 2), kk = q % (NJ / 2); st16(r, base + (tt * 4 + (sub >> 1) + kk) * 1024, pack_acc(acc[2 * sg + tt][2 * kk], acc[2 * sg + tt][2 * kk + 1])); }
            else st16(r, base + (sub + q) * 1024, pack_acc(acc[2 * sg][q], acc[2 * sg + 1][q])); }
    }
}

__device__ __forceinline__ void attn_block(const Params& p, const int wv, const int h, const unsigned tag) {
    const int l = otid() & 63, n16 = l & 15, g = l >> 4;
    const unsigned OOB = 0x80000000u;
    const v4u rd = mk_srd(p.ws, W_TOTAL);
    const unsigned kx = W_KX + h * 8192, qx = W_QX + h * 8192, vx = W_VF + h * 8192, km = HO_KVM + h * 4096;
    const v8u pc = {(unsigned)l * 16, (unsigned)(n16 * 16 + (g & 1) * 8), F_KVQ + ((l >> 2) * 32 + h) * 64 + min(l & 3, 2) * 4, F_PROG + h * 128, (unsigned)(wv * 2048 + l * 16),
                    (unsigned)(wv * 2048 + (8 * g + (n16 >> 2)) * 16 + (n16 & 1) * 8), (l & 31) ? OOB : F_ATT + (l >> 5) * 64, l < 32 ? F_RDY + l * 128 : OOB};
    const int dq = n16 - 4 * g;
    asm volatile(
        "s_mov_b32 s36, 0xf0000000\n\t"
        "s_mov_b32 s37, %[w]\n\t"
        "s_mov_b32 s38, s36\n\t"
        "s_mov_b32 s40, 0\n\t"
        "s_memrealtime s[98:99]\n\t"
        "s_waitcnt lgkmcnt(0)\n\t"
        "s_sub_u32 s41, s98, 0x100000\n\t"
        "s_mov_b64 s[42:43], 0\n\t"
        "s_mov_b32 s44, -1\n\t"
        "s_mov_b32 s45, -1\n\t"
        "s_mov_b32 s56, 0\n\t"
        "s_mov_b32 s57, -1\n\t"
        "s_lshl_b32 s58, %[w], 10\n\t"
        "s_add_u32 s58, s58, 16384\n\t"
        "v_cmp_le_i32_e64 s[48:49], 0, v252\n\t"
        "v_cmp_le_i32_e64 s[50:51], 1, v252\n\t"
        "v_cmp_le_i32_e64 s[52:53], 2, v252\n\t"
        "v_cmp_le_i32_e64 s[54:55], 3, v252\n\t"
        "v_mov_b32_e32 v253, 0xce6e6b28\n\t"
        "v_mov_b32_e32 v254, 0xff800000\n\t"
        ".Ltop%=:\n\t"
        "s_cmp_eq_u32 s38, 0\n\t"
        "s_cbranch_scc1 .La2%=\n\t"
        "s_mov_b32 s97, 0\n\t"
        ".La1%=:\n\t"
        "buffer_load_dword v240, v246, %[rd], 0 offen sc1\n\t"
        "buffer_load_dword v241, v247, %[rd], 0 offen sc1\n\t"
        "s_add_u32 s97, s97, 1\n\t"
        "s_cmp_lt_u32 s97, 0x200000\n\t"
        "s_cbranch_scc0 .Lgu%=\n\t"
        "s_memrealtime s[98:99]\n\t"
        "s_waitcnt vmcnt(0) lgkmcnt(0)\n\t"
        "v_cmp_eq_u32_e32 vcc, %[tag], v240\n\t"
        "s_nop 1\n\t"
        "s_or_b64 s[42:43], s[42:43], vcc\n\t"
        "v_readfirstlane_b32 s80, v241\n\t"
        "s_nop 0\n\t"
        "s_cmp_gt_u32 s80, s40\n\t"
        "s_cselect_b32 s41, s98, s41\n\t"
        "s_max_u32 s40, s40, s80\n\t"
        "s_and_b32 s80, s37, 60\n\t"
        "s_lshr_b64 s[80:81], s[42:43], s80\n\t"
        "s_andn2_b32 s80, 15, s80\n\t"
        "s_cbranch_scc0 .La1%=x\n\t"
        "s_sleep 16\n\t"
        "s_branch .La1%=\n\t"
        ".La1%=x:\n\t"
        "s_cmp_gt_u32 s40, s37\n\t"
        "s_cselect_b32 s39, 0, s36\n\t"
        "s_lshl_b32 s80, s37, 18\n\t"
        "s_or_b32 s80, s80, 0\n\t"
        "s_lshl_b32 s81, s37, 17\n\t"
        "s_add_u32 s81, s81, %[km]\n\t"
        "s_or_b32 s81, s81, s39\n\t"
        "s_add_u32 s82, s80, %[kx]\n\t"
        "s_add_u32 s83, s82, 0x1000\n\t"
        "buffer_load_dwordx4 a[0:3], v244, %[rd], s82 offen offset:0 sc1\n\t"
        "buffer_load_dwordx4 a[4:7], v244, %[rd], s82 offen offset:1024 sc1\n\t"
        "buffer_load_dwordx4 a[8:11], v244, %[rd], s82 offen offset:2048 sc1\n\t"
        "buffer_load_dwordx4 a[12:15], v244, %[rd], s82 offen offset:3072 sc1\n\t"
        "buffer_load_dwordx4 a[16:19], v244, %[rd], s83 offen offset:0 sc1\n\t"
        "buffer_load_dwordx4 a[20:23], v244, %[rd], s83 offen offset:1024 sc1\n\t"
        "buffer_load_dwordx4 a[24:27], v244, %[rd], s83 offen offset:2048 sc1\n\t"
        "buffer_load_dwordx4 a[28:31], v244, %[rd], s83 offen offset:3072 sc1\n\t"
        "s_cmp_eq_u64 s[42:43], -1\n\t"
        "s_cbranch_scc1 .La3%=\n\t"
        "s_sleep %c[aslp]\n\t"
        ".La3%=:\n\t"
        "s_sleep %c[bslp]\n\t"
        "s_add_u32 s82, s80, %[qx]\n\t"
        "s_add_u32 s83, s82, 0x1000\n\t"
        "buffer_load_dwordx4 a[32:35], v244, %[rd], s82 offen offset:0 sc1\n\t"
        "buffer_load_dwordx4 a[36:39], v244, %[rd], s82 offen offset:1024 sc1\n\t"
        "buffer_load_dwordx4 a[40:43], v244, %[rd], s82 offen offset:2048 sc1\n\t"
        "buffer_load_dwordx4 a[44:47], v244, %[rd], s82 offen offset:3072 sc1\n\t"
        "buffer_load_dwordx4 a[48:51], v244, %[rd], s83 offen offset:0 sc1\n\t"
        "buffer_load_dwordx4 a[52:55], v244, %[rd], s83 offen offset:1024 sc1\n\t"
        "buffer_load_dwordx4 a[56:59], v244, %[rd], s83 offen offset:2048 sc1\n\t"
        "buffer_load_dwordx4 a[60:63], v244, %[rd], s83 offen offset:3072 sc1\n\t"
        "s_cmp_eq_u64 s[42:43], -1\n\t"
        "s_cbranch_scc1 .La4%=\n\t"
        "s_sleep %c[aslp]\n\t"
        ".La4%=:\n\t"
        "s_sleep %c[bslp]\n\t"
        "buffer_load_dwordx4 a[96:99], v244, %[rd], s81 offen offset:0 sc1\n\t"
        "buffer_load_dwordx4 a[100:103], v244, %[rd], s81 offen offset:1024 sc1\n\t"
        "buffer_load_dwordx2 a[104:105], v245, %[rd], s81 offen offset:2048 sc1\n\t"
        "buffer_load_dwordx2 a[106:107], v245, %[rd], s81 offen offset:2304 sc1\n\t"
        "buffer_load_dwordx2 a[108:109], v245, %[rd], s81 offen offset:2560 sc1\n\t"
        "buffer_load_dwordx2 a[110:111], v245, %[rd], s81 offen offset:2816 sc1\n\t"
        "buffer_load_dwordx2 a[112:113], v245, %[rd], s81 offen offset:3072 sc1\n\t"
        "buffer_load_dwordx2 a[114:115], v245, %[rd], s81 offen offset:3328 sc1\n\t"
        "buffer_load_dwordx2 a[116:117], v245, %[rd], s81 offen offset:3584 sc1\n\t"
        "buffer_load_dwordx2 a[118:119], v245, %[rd], s81 offen offset:3840 sc1\n\t"
        "s_add_u32 s82, s80, %[vx]\n\t"
        "s_add_u32 s83, s82, 0x1000\n\t"
        "buffer_load_dwordx4 a[64:67], v244, %[rd], s82 offen offset:0 sc1\n\t"
        "buffer_load_dwordx4 a[68:71], v244, %[rd], s82 offen offset:1024 sc1\n\t"
        "buffer_load_dwordx4 a[72:75], v244, %[rd], s82 offen offset:2048 sc1\n\t"
        "buffer_load_dwordx4 a[76:79], v244, %[rd], s82 offen offset:3072 sc1\n\t"
        "buffer_load_dwordx4 a[80:83], v244, %[rd], s83 offen offset:0 sc1\n\t"
        "buffer_load_dwordx4 a[84:87], v244, %[rd], s83 offen offset:1024 sc1\n\t"
        "buffer_load_dwordx4 a[88:91], v244, %[rd], s83 offen offset:2048 sc1\n\t"
        "buffer_load_dwordx4 a[92:95], v244, %[rd], s83 offen offset:3072 sc1\n\t"
        "s_cmp_eq_u64 s[42:43], -1\n\t"
        "s_cbranch_scc1 .La5%=\n\t"
        "s_sleep %c[aslp]\n\t"
        ".La5%=:\n\t"
        "s_sleep %c[bslp]\n\t"
        "buffer_store_dword v0, off, %[rd], s36\n\t"
        "buffer_store_dword v0, off, %[rd], s36\n\t"
        "buffer_store_dword v0, off, %[rd], s36\n\t"
        "buffer_store_dword v0, off, %[rd], s36\n\t"
        "buffer_store_dword v0, off, %[rd], s36\n\t"
        "buffer_store_dword v0, off, %[rd], s36\n\t"
        "buffer_store_dword v0, off, %[rd], s36\n\t"
        "buffer_store_dword v0, off, %[rd], s36\n\t"
        "buffer_store_dword v0, off, %[rd], s36\n\t"
        ".La2%=:\n\t"
        "s_cmp_gt_u32 s40, s37\n\t"
        "s_cselect_b32 s80, s39, 0\n\t"
        "s_cmp_eq_u32 s80, 0\n\t"
        "s_cbranch_scc1 .La6%=\n\t"
        "s_mov_b32 s39, 0\n\t"
        "s_lshl_b32 s81, s37, 17\n\t"
        "s_add_u32 s81, s81, %[km]\n\t"
        "buffer_load_dwordx4 a[96:99], v244, %[rd], s81 offen offset:0 sc1\n\t"
        "buffer_load_dwordx4 a[100:103], v244, %[rd], s81 offen offset:1024 sc1\n\t"
        "buffer_load_dwordx2 a[104:105], v245, %[rd], s81 offen offset:2048 sc1\n\t"
        "buffer_load_dwordx2 a[106:107], v245, %[rd], s81 offen offset:2304 sc1\n\t"
        "buffer_load_dwordx2 a[108:109], v245, %[rd], s81 offen offset:2560 sc1\n\t"
        "buffer_load_dwordx2 a[110:111], v245, %[rd], s81 offen offset:2816 sc1\n\t"
        "buffer_load_dwordx2 a[112:113], v245, %[rd], s81 offen offset:3072 sc1\n\t"
        "buffer_load_dwordx2 a[114:115], v245, %[rd], s81 offen offset:3328 sc1\n\t"
        "buffer_load_dwordx2 a[116:117], v245, %[rd], s81 offen offset:3584 sc1\n\t"
        "buffer_load_dwordx2 a[118:119], v245, %[rd], s81 offen offset:3840 sc1\n\t"
        "buffer_store_dword v0, off, %[rd], s36\n\t"
        "buffer_store_dword v0, off, %[rd], s36\n\t"
        "buffer_store_dword v0, off, %[rd], s36\n\t"
        "buffer_store_dword v0, off, %[rd], s36\n\t"
        "buffer_store_dword v0, off, %[rd], s36\n\t"
        "buffer_store_dword v0, off, %[rd], s36\n\t"
        "buffer_store_dword v0, off, %[rd], s36\n\t"
        "buffer_store_dword v0, off, %[rd], s36\n\t"
        ".La6%=:\n\t"
        "s_add_u32 s86, s37, 4\n\t"
        "s_and_b32 s80, s86, 60\n\t"
        "s_lshr_b64 s[80:81], s[42:43], s80\n\t"
        "s_andn2_b32 s80, 15, s80\n\t"
        "s_cselect_b32 s46, s36, 0\n\t"
        "s_cmp_lt_u32 s86, 64\n\t"
        "s_cselect_b32 s46, s46, s36\n\t"
        "s_cmp_gt_u32 s40, s86\n\t"
        "s_cselect_b32 s47, s46, s36\n\t"
        "s_lshl_b32 s80, s86, 18\n\t"
        "s_or_b32 s80, s80, s46\n\t"
        "s_lshl_b32 s81, s86, 17\n\t"
        "s_add_u32 s81, s81, %[km]\n\t"
        "s_or_b32 s81, s81, s47\n\t"
        "s_add_u32 s82, s80, %[kx]\n\t"
        "s_add_u32 s83, s82, 0x1000\n\t"
        "buffer_load_dwordx4 v[0:3], v244, %[rd], s82 offen offset:0 sc1\n\t"
        "buffer_load_dwordx4 v[4:7], v244, %[rd], s82 offen offset:1024 sc1\n\t"
        "buffer_load_dwordx4 v[8:11], v244, %[rd], s82 offen offset:2048 sc1\n\t"
        "buffer_load_dwordx4 v[12:15], v244, %[rd], s82 offen offset:3072 sc1\n\t"
        "buffer_load_dwordx4 v[16:19], v244, %[rd], s83 offen offset:0 sc1\n\t"
        "buffer_load_dwordx4 v[20:23], v244, %[rd], s83 offen offset:1024 sc1\n\t"
        "buffer_load_dwordx4 v[24:27], v244, %[rd], s83 offen offset:2048 sc1\n\t"
        "buffer_load_dwordx4 v[28:31], v244, %[rd], s83 offen offset:3072 sc1\n\t"
        "s_cmp_eq_u64 s[42:43], -1\n\t"
        "s_cbranch_scc1 .La7%=\n\t"
        "s_sleep %c[aslp]\n\t"
        ".La7%=:\n\t"
        "s_sleep %c[bslp]\n\t"
        "s_add_u32 s82, s80, %[qx]\n\t"
        "s_add_u32 s83, s82, 0x1000\n\t"
        "buffer_load_dwordx4 v[32:35], v244, %[rd], s82 offen offset:0 sc1\n\t"
        "buffer_load_dwordx4 v[36:39], v244, %[rd], s82 offen offset:1024 sc1\n\t"
        "buffer_load_dwordx4 v[40:43], v244, %[rd], s82 offen offset:2048 sc1\n\t"
        "buffer_load_dwordx4 v[44:47], v244, %[rd], s82 offen offset:3072 sc1\n\t"
        "buffer_load_dwordx4 v[48:51], v244, %[rd], s83 offen offset:0 sc1\n\t"
        "buffer_load_dwordx4 v[52:55], v244, %[rd], s83 offen offset:1024 sc1\n\t"
        "buffer_load_dwordx4 v[56:59], v244, %[rd], s83 offen offset:2048 sc1\n\t"
        "buffer_load_dwordx4 v[60:63], v244, %[rd], s83 offen offset:3072 sc1\n\t"
        "s_cmp_eq_u64 s[42:43], -1\n\t"
        "s_cbranch_scc1 .La8%=\n\t"
        "s_sleep %c[aslp]\n\t"
        ".La8%=:\n\t"
        "s_sleep %c[bslp]\n\t"
        "buffer_load_dwordx4 v[96:99], v244, %[rd], s81 offen offset:0 sc1\n\t"
        "buffer_load_dwordx4 v[100:103], v244, %[rd], s81 offen offset:1024 sc1\n\t"
        "buffer_load_dwordx2 v[104:105], v245, %[rd], s81 offen offset:2048 sc1\n\t"
        "buffer_load_dwordx2 v[106:107], v245, %[rd], s81 offen offset:2304 sc1\n\t"
        "buffer_load_dwordx2 v[108:109], v245, %[rd], s81 offen offset:2560 sc1\n\t"
        "buffer_load_dwordx2 v[110:111], v245, %[rd], s81 offen offset:2816 sc1\n\t"
        "buffer_load_dwordx2 v[112:113], v245, %[rd], s81 offen offset:3072 sc1\n\t"
        "buffer_load_dwordx2 v[114:115], v245, %[rd], s81 offen offset:3328 sc1\n\t"
        "buffer_load_dwordx2 v[116:117], v245, %[rd], s81 offen offset:3584 sc1\n\t"
        "buffer_load_dwordx2 v[118:119], v245, %[rd], s81 offen offset:3840 sc1\n\t"
        "s_add_u32 s82, s80, %[vx]\n\t"
        "s_add_u32 s83, s82, 0x1000\n\t"
        "buffer_load_dwordx4 v[64:67], v244, %[rd], s82 offen offset:0 sc1\n\t"
        "buffer_load_dwordx4 v[68:71], v244, %[rd], s82 offen offset:1024 sc1\n\t"
        "buffer_load_dwordx4 v[72:75], v244, %[rd], s82 offen offset:2048 sc1\n\t"
        "buffer_load_dwordx4 v[76:79], v244, %[rd], s82 offen offset:3072 sc1\n\t"
        "buffer_load_dwordx4 v[80:83], v244, %[rd], s83 offen offset:0 sc1\n\t"
        "buffer_load_dwordx4 v[84:87], v244, %[rd], s83 offen offset:1024 sc1\n\t"
        "buffer_load_dwordx4 v[88:91], v244, %[rd], s83 offen offset:2048 sc1\n\t"
        "buffer_load_dwordx4 v[92:95], v244, %[rd], s83 offen offset:3072 sc1\n\t"
        "s_cmp_eq_u64 s[42:43], -1\n\t"
        "s_cbranch_scc1 .La9%=\n\t"
        "s_sleep %c[aslp]\n\t"
        ".La9%=:\n\t"
        "s_sleep %c[bslp]\n\t"
        "s_waitcnt vmcnt(42)\n\t"
        "s_memrealtime s[98:99]\n\t"
        "v_mfma_f32_16x16x32_bf16 v[136:139], a[0:3], a[32:35], 0\n\t"
        "v_mfma_f32_16x16x32_bf16 v[148:151], a[0:3], a[48:51], 0\n\t"
        "v_mfma_f32_16x16x32_bf16 v[152:155], a[16:19], a[48:51], 0\n\t"
        "v_mfma_f32_16x16x32_bf16 v[136:139], a[4:7], a[36:39], v[136:139]\n\t"
        "v_mfma_f32_16x16x32_bf16 v[148:151], a[4:7], a[52:55], v[148:151]\n\t"
        "v_mfma_f32_16x16x32_bf16 v[152:155], a[20:23], a[52:55], v[152:155]\n\t"
        "v_mfma_f32_16x16x32_bf16 v[136:139], a[8:11], a[40:43], v[136:139]\n\t"
        "v_mfma_f32_16x16x32_bf16 v[148:151], a[8:11], a[56:59], v[148:151]\n\t"
        "v_mfma_f32_16x16x32_bf16 v[152:155], a[24:27], a[56:59], v[152:155]\n\t"
        "v_mfma_f32_16x16x32_bf16 v[136:139], a[12:15], a[44:47], v[136:139]\n\t"
        "v_mfma_f32_16x16x32_bf16 v[148:151], a[12:15], a[60:63], v[148:151]\n\t"
        "v_mfma_f32_16x16x32_bf16 v[152:155], a[28:31], a[60:63], v[152:155]\n\t"
        "s_waitcnt lgkmcnt(0)\n\t"
        "v_cmp_eq_u32_e32 vcc, %[tag], v240\n\t"
        "s_nop 1\n\t"
        "s_or_b64 s[42:43], s[42:43], vcc\n\t"
        "v_readfirstlane_b32 s80, v241\n\t"
        "s_nop 0\n\t"
        "s_cmp_gt_u32 s80, s40\n\t"
        "s_cselect_b32 s41, s98, s41\n\t"
        "s_max_u32 s40, s40, s80\n\t"
        "v_readlane_b32 s84, v242, 0\n\t"
        "v_readlane_b32 s85, v242, 32\n\t"
        "v_mov_b32_e32 v243, 32\n\t"
        "s_and_b32 s80, s45, 3\n\t"
        "s_lshl_b32 s80, s80, 12\n\t"
        "s_lshr_b32 s81, s45, 2\n\t"
        "s_lshl_b32 s81, s81, 3\n\t"
        "s_add_u32 s80, s80, s81\n\t"
        "s_lshr_b32 s82, s45, 1\n\t"
        "s_xnor_b32 s81, s45, s82\n\t"
        "s_and_b32 s81, s81, 1\n\t"
        "s_lshl_b32 s81, s81, 13\n\t"
        "s_lshl_b32 s82, s82, 2\n\t"
        "s_add_u32 s81, s81, s82\n\t"
        "s_cmp_lt_i32 s45, 60\n\t"
        "s_cselect_b32 s80, s80, s81\n\t"
        "s_cselect_b32 s81, 4, 0x1000\n\t"
        "s_add_u32 s81, s81, s80\n\t"
        "s_cmp_eq_u32 s84, 31\n\t"
        "s_cselect_b32 s80, s80, s36\n\t"
        "s_cmp_eq_u32 s85, 31\n\t"
        "s_cselect_b32 s81, s81, s36\n\t"
        "s_cmp_lt_i32 s45, 0\n\t"
        "s_cselect_b32 s80, s36, s80\n\t"
        "s_cselect_b32 s81, s36, s81\n\t"
        "buffer_store_dword v243, v251, %[rd], s80 offen sc1\n\t"
        "buffer_store_dword v243, v251, %[rd], s81 offen sc1\n\t"
        "s_add_u32 s86, s37, 8\n\t"
        "s_cmp_lg_u64 s[42:43], -1\n\t"
        "s_cselect_b32 s84, 0, s36\n\t"
        "s_cmp_gt_u32 s40, s86\n\t"
        "s_cselect_b32 s85, s36, s39\n\t"
        "buffer_load_dword v240, v246, %[rd], s84 offen sc1\n\t"
        "buffer_load_dword v241, v247, %[rd], s85 offen sc1\n\t"
        "v_cvt_pk_bf16_f32 v136, 0, v136\n\t"
        "v_cvt_pk_bf16_f32 v137, 0, v137\n\t"
        "v_cvt_pk_bf16_f32 v138, 0, v138\n\t"
        "v_cvt_pk_bf16_f32 v139, 0, v139\n\t"
        "v_mul_f32_e32 v136, 0x3db504f3, v136\n\t"
        "v_mul_f32_e32 v137, 0x3db504f3, v137\n\t"
        "v_mul_f32_e32 v138, 0x3db504f3, v138\n\t"
        "v_mul_f32_e32 v139, 0x3db504f3, v139\n\t"
        "v_cvt_pk_bf16_f32 v136, 0, v136\n\t"
        "v_cvt_pk_bf16_f32 v137, 0, v137\n\t"
        "v_cvt_pk_bf16_f32 v138, 0, v138\n\t"
        "v_cvt_pk_bf16_f32 v139, 0, v139\n\t"
        "v_cndmask_b32_e64 v136, v253, v136, s[48:49]\n\t"
        "v_cndmask_b32_e64 v137, v253, v137, s[50:51]\n\t"
        "v_cndmask_b32_e64 v138, v253, v138, s[52:53]\n\t"
        "v_cndmask_b32_e64 v139, v253, v139, s[54:55]\n\t"
        "v_cvt_pk_bf16_f32 v148, 0, v148\n\t"
        "v_cvt_pk_bf16_f32 v149, 0, v149\n\t"
        "v_cvt_pk_bf16_f32 v150, 0, v150\n\t"
        "v_cvt_pk_bf16_f32 v151, 0, v151\n\t"
        "v_mul_f32_e32 v148, 0x3db504f3, v148\n\t"
        "v_mul_f32_e32 v149, 0x3db504f3, v149\n\t"
        "v_mul_f32_e32 v150, 0x3db504f3, v150\n\t"
        "v_mul_f32_e32 v151, 0x3db504f3, v151\n\t"
        "v_cvt_pk_bf16_f32 v148, 0, v148\n\t"
        "v_cvt_pk_bf16_f32 v149, 0, v149\n\t"
        "v_cvt_pk_bf16_f32 v150, 0, v150\n\t"
        "v_cvt_pk_bf16_f32 v151, 0, v151\n\t"
        "v_cvt_pk_bf16_f32 v152, 0, v152\n\t"
        "v_cvt_pk_bf16_f32 v153, 0, v153\n\t"
        "v_cvt_pk_bf16_f32 v154, 0, v154\n\t"
        "v_cvt_pk_bf16_f32 v155, 0, v155\n\t"
        "v_mul_f32_e32 v152, 0x3db504f3, v152\n\t"
        "v_mul_f32_e32 v153, 0x3db504f3, v153\n\t"
        "v_mul_f32_e32 v154, 0x3db504f3, v154\n\t"
        "v_mul_f32_e32 v155, 0x3db504f3, v155\n\t"
        "v_cvt_pk_bf16_f32 v152, 0, v152\n\t"
        "v_cvt_pk_bf16_f32 v153, 0, v153\n\t"
        "v_cvt_pk_bf16_f32 v154, 0, v154\n\t"
        "v_cvt_pk_bf16_f32 v155, 0, v155\n\t"
        "v_cndmask_b32_e64 v152, v253, v152, s[48:49]\n\t"
        "v_cndmask_b32_e64 v153, v253, v153, s[50:51]\n\t"
        "v_cndmask_b32_e64 v154, v253, v154, s[52:53]\n\t"
        "v_cndmask_b32_e64 v155, v253, v155, s[54:55]\n\t"
        "v_max3_f32 v172, v136, v137, v253\n\t"
        "v_max3_f32 v172, v172, v138, v139\n\t"
        "v_mov_b32_e32 v140, v253\n\t"
        "v_max_f32_e32 v174, v148, v149\n\t"
        "v_max3_f32 v174, v174, v150, v151\n\t"
        "v_max3_f32 v174, v174, v152, v153\n\t"
        "v_max3_f32 v174, v174, v154, v155\n\t"
        "s_cmp_eq_u32 s39, 0\n\t"
        "s_cbranch_scc1 .La11%=\n\t"
        "s_waitcnt vmcnt(0)\n\t"
        "s_lshl_b32 s80, s44, 7\n\t"
        "s_cmp_lt_i32 s44, 0\n\t"
        "s_cselect_b32 s80, s36, s80\n\t"
        "v_mov_b32_e32 v242, 1\n\t"
        "buffer_atomic_add v242, v250, %[rd], s80 offen sc0 sc1\n\t"
        "s_waitcnt vmcnt(0)\n\t"
        "v_readlane_b32 s84, v242, 0\n\t"
        "v_readlane_b32 s85, v242, 32\n\t"
        "v_mov_b32_e32 v243, 32\n\t"
        "s_and_b32 s80, s44, 3\n\t"
        "s_lshl_b32 s80, s80, 12\n\t"
        "s_lshr_b32 s81, s44, 2\n\t"
        "s_lshl_b32 s81, s81, 3\n\t"
        "s_add_u32 s80, s80, s81\n\t"
        "s_lshr_b32 s82, s44, 1\n\t"
        "s_xnor_b32 s81, s44, s82\n\t"
        "s_and_b32 s81, s81, 1\n\t"
        "s_lshl_b32 s81, s81, 13\n\t"
        "s_lshl_b32 s82, s82, 2\n\t"
        "s_add_u32 s81, s81, s82\n\t"
        "s_cmp_lt_i32 s44, 60\n\t"
        "s_cselect_b32 s80, s80, s81\n\t"
        "s_cselect_b32 s81, 4, 0x1000\n\t"
        "s_add_u32 s81, s81, s80\n\t"
        "s_cmp_eq_u32 s84, 31\n\t"
        "s_cselect_b32 s80, s80, s36\n\t"
        "s_cmp_eq_u32 s85, 31\n\t"
        "s_cselect_b32 s81, s81, s36\n\t"
        "s_cmp_lt_i32 s44, 0\n\t"
        "s_cselect_b32 s80, s36, s80\n\t"
        "s_cselect_b32 s81, s36, s81\n\t"
        "buffer_store_dword v243, v251, %[rd], s80 offen sc1\n\t"
        "buffer_store_dword v243, v251, %[rd], s81 offen sc1\n\t"
        "s_mov_b32 s44, -1\n\t"
        "s_mov_b32 s97, 0\n\t"
        "s_min_u32 s87, s37, 62\n\t"
        ".La10%=:\n\t"
        "s_cmp_gt_u32 s40, s87\n\t"
        "s_cbranch_scc1 .La10%=x\n\t"
        "s_memrealtime s[98:99]\n\t"
        "s_add_u32 s97, s97, 1\n\t"
        "s_cmp_lt_u32 s97, 0x400000\n\t"
        "s_cbranch_scc0 .Lgu%=\n\t"
        "s_sub_u32 s80, s87, s40\n\t"
        "s_cmp_ge_u32 s87, %c[efrom]\n\t"
        "s_subb_u32 s80, s80, 0\n\t"
        "s_mul_i32 s80, s80, %c[smin]\n\t"
        "s_waitcnt lgkmcnt(0)\n\t"
        "s_sub_u32 s81, s98, s41\n\t"
        "s_cmp_ge_i32 s81, s80\n\t"
        "s_cbranch_scc1 .La10%=p\n\t"
        "s_sleep 16\n\t"
        "s_branch .La10%=\n\t"
        ".La10%=p:\n\t"
        "buffer_load_dword v241, v247, %[rd], 0 offen sc1\n\t"
        "s_waitcnt vmcnt(0)\n\t"
        "v_readfirstlane_b32 s80, v241\n\t"
        "s_nop 0\n\t"
        "s_cmp_gt_u32 s80, s40\n\t"
        "s_cselect_b32 s41, s98, s41\n\t"
        "s_max_u32 s40, s40, s80\n\t"
        "s_cmp_gt_u32 s40, s87\n\t"
        "s_cbranch_scc1 .La10%=x\n\t"
        "s_sleep %c[pslp]\n\t"
        "s_branch .La10%=\n\t"
        ".La10%=x:\n\t"
        "s_lshl_b32 s81, s37, 17\n\t"
        "s_add_u32 s81, s81, %[km]\n\t"
        "buffer_load_dwordx4 a[96:99], v244, %[rd], s81 offen offset:0 sc1\n\t"
        "buffer_load_dwordx4 a[100:103], v244, %[rd], s81 offen offset:1024 sc1\n\t"
        "buffer_load_dwordx2 a[104:105], v245, %[rd], s81 offen offset:2048 sc1\n\t"
        "buffer_load_dwordx2 a[106:107], v245, %[rd], s81 offen offset:2304 sc1\n\t"
        "buffer_load_dwordx2 a[108:109], v245, %[rd], s81 offen offset:2560 sc1\n\t"
        "buffer_load_dwordx2 a[110:111], v245, %[rd], s81 offen offset:2816 sc1\n\t"
        "buffer_load_dwordx2 a[112:113], v245, %[rd], s81 offen offset:3072 sc1\n\t"
        "buffer_load_dwordx2 a[114:115], v245, %[rd], s81 offen offset:3328 sc1\n\t"
        "buffer_load_dwordx2 a[116:117], v245, %[rd], s81 offen offset:3584 sc1\n\t"
        "buffer_load_dwordx2 a[118:119], v245, %[rd], s81 offen offset:3840 sc1\n\t"
        "s_waitcnt vmcnt(0)\n\t"
        ".La11%=:\n\t"
        "ds_write_b128 v248, a[96:99] offset:0\n\t"
        "ds_write_b128 v248, a[100:103] offset:1024\n\t"
        "ds_read_b64_tr_b16 v[120:121], v249 offset:0\n\t"
        "ds_read_b64_tr_b16 v[122:123], v249 offset:64\n\t"
        "ds_read_b64_tr_b16 v[124:125], v249 offset:512\n\t"
        "ds_read_b64_tr_b16 v[126:127], v249 offset:576\n\t"
        "ds_read_b64_tr_b16 v[128:129], v249 offset:1024\n\t"
        "ds_read_b64_tr_b16 v[130:131], v249 offset:1088\n\t"
        "ds_read_b64_tr_b16 v[132:133], v249 offset:1536\n\t"
        "ds_read_b64_tr_b16 v[134:135], v249 offset:1600\n\t"
        "s_waitcnt lgkmcnt(6)\n\t"
        "v_mfma_f32_16x16x32_bf16 v[144:147], v[120:123], a[32:35], 0\n\t"
        "v_mfma_f32_16x16x32_bf16 v[156:159], v[120:123], a[48:51], 0\n\t"
        "s_waitcnt lgkmcnt(4)\n\t"
        "v_mfma_f32_16x16x32_bf16 v[144:147], v[124:127], a[36:39], v[144:147]\n\t"
        "v_mfma_f32_16x16x32_bf16 v[156:159], v[124:127], a[52:55], v[156:159]\n\t"
        "s_waitcnt lgkmcnt(2)\n\t"
        "v_mfma_f32_16x16x32_bf16 v[144:147], v[128:131], a[40:43], v[144:147]\n\t"
        "v_mfma_f32_16x16x32_bf16 v[156:159], v[128:131], a[56:59], v[156:159]\n\t"
        "s_waitcnt lgkmcnt(0)\n\t"
        "v_mfma_f32_16x16x32_bf16 v[144:147], v[132:135], a[44:47], v[144:147]\n\t"
        "v_mfma_f32_16x16x32_bf16 v[156:159], v[132:135], a[60:63], v[156:159]\n\t"
        "s_nop 1\n\t"
        "v_mov_b32_e32 v252, v252\n\t"
        "s_nop 1\n\t"
        "v_mov_b32_e32 v252, v252\n\t"
        "s_nop 0\n\t"
        "v_cvt_pk_bf16_f32 v144, 0, v144\n\t"
        "v_cvt_pk_bf16_f32 v145, 0, v145\n\t"
        "v_cvt_pk_bf16_f32 v146, 0, v146\n\t"
        "v_cvt_pk_bf16_f32 v147, 0, v147\n\t"
        "v_mul_f32_e32 v144, 0x3db504f3, v144\n\t"
        "v_mul_f32_e32 v145, 0x3db504f3, v145\n\t"
        "v_mul_f32_e32 v146, 0x3db504f3, v146\n\t"
        "v_mul_f32_e32 v147, 0x3db504f3, v147\n\t"
        "v_cvt_pk_bf16_f32 v144, 0, v144\n\t"
        "v_cvt_pk_bf16_f32 v145, 0, v145\n\t"
        "v_cvt_pk_bf16_f32 v146, 0, v146\n\t"
        "v_cvt_pk_bf16_f32 v147, 0, v147\n\t"
        "v_cndmask_b32_e64 v144, v144, v254, s[56:57]\n\t"
        "v_cndmask_b32_e64 v145, v145, v254, s[56:57]\n\t"
        "v_cndmask_b32_e64 v146, v146, v254, s[56:57]\n\t"
        "v_cndmask_b32_e64 v147, v147, v254, s[56:57]\n\t"
        "v_max3_f32 v172, v172, v144, v145\n\t"
        "v_max3_f32 v173, v172, v146, v147\n\t"
        "v_max3_f32 v172, v172, v146, v147\n\t"
        "s_nop 1\n\t"
        "v_permlane16_swap_b32_e32 v172, v173\n\t"
        "v_max_f32_e32 v172, v172, v173\n\t"
        "v_mov_b32_e32 v173, v172\n\t"
        "s_nop 1\n\t"
        "v_permlane32_swap_b32_e32 v172, v173\n\t"
        "v_max_f32_e32 v172, v172, v173\n\t"
        "v_sub_f32_e32 v136, v136, v172\n\t"
        "v_sub_f32_e32 v137, v137, v172\n\t"
        "v_sub_f32_e32 v138, v138, v172\n\t"
        "v_sub_f32_e32 v139, v139, v172\n\t"
        "v_sub_f32_e32 v140, v140, v172\n\t"
        "v_sub_f32_e32 v144, v144, v172\n\t"
        "v_sub_f32_e32 v145, v145, v172\n\t"
        "v_sub_f32_e32 v146, v146, v172\n\t"
        "v_sub_f32_e32 v147, v147, v172\n\t"
        "v_mul_f32_e32 v136, 0x3fb8aa3b, v136\n\t"
        "v_mul_f32_e32 v137, 0x3fb8aa3b, v137\n\t"
        "v_mul_f32_e32 v138, 0x3fb8aa3b, v138\n\t"
        "v_mul_f32_e32 v139, 0x3fb8aa3b, v139\n\t"
        "v_mul_f32_e32 v140, 0x3fb8aa3b, v140\n\t"
        "v_mul_f32_e32 v144, 0x3fb8aa3b, v144\n\t"
        "v_mul_f32_e32 v145, 0x3fb8aa3b, v145\n\t"
        "v_mul_f32_e32 v146, 0x3fb8aa3b, v146\n\t"
        "v_mul_f32_e32 v147, 0x3fb8aa3b, v147\n\t"
        "v_exp_f32_e32 v136, v136\n\t"
        "v_exp_f32_e32 v137, v137\n\t"
        "v_exp_f32_e32 v138, v138\n\t"
        "v_exp_f32_e32 v139, v139\n\t"
        "v_exp_f32_e32 v140, v140\n\t"
        "v_exp_f32_e32 v144, v144\n\t"
        "v_exp_f32_e32 v145, v145\n\t"
        "v_exp_f32_e32 v146, v146\n\t"
        "v_exp_f32_e32 v147, v147\n\t"
        "v_mul_f32_e32 v172, 4.0, v140\n\t"
        "v_add_f32_e32 v172, v172, v136\n\t"
        "v_add_f32_e32 v172, v172, v137\n\t"
        "v_add_f32_e32 v172, v172, v138\n\t"
        "v_add_f32_e32 v172, v172, v139\n\t"
        "v_add_f32_e32 v172, v172, v144\n\t"
        "v_add_f32_e32 v172, v172, v145\n\t"
        "v_add_f32_e32 v172, v172, v146\n\t"
        "v_add_f32_e32 v172, v172, v147\n\t"
        "v_mov_b32_e32 v173, v172\n\t"
        "s_nop 1\n\t"
        "v_permlane16_swap_b32_e32 v172, v173\n\t"
        "v_add_f32_e32 v172, v172, v173\n\t"
        "v_mov_b32_e32 v173, v172\n\t"
        "s_nop 1\n\t"
        "v_permlane32_swap_b32_e32 v172, v173\n\t"
        "v_add_f32_e32 v172, v172, v173\n\t"
        "v_rcp_f32_e32 v172, v172\n\t"
        "s_nop 0\n\t"
        "v_mul_f32_e32 v136, v136, v172\n\t"
        "v_mul_f32_e32 v137, v137, v172\n\t"
        "v_mul_f32_e32 v138, v138, v172\n\t"
        "v_mul_f32_e32 v139, v139, v172\n\t"
        "v_mul_f32_e32 v140, v140, v172\n\t"
        "v_mul_f32_e32 v144, v144, v172\n\t"
        "v_mul_f32_e32 v145, v145, v172\n\t"
        "v_mul_f32_e32 v146, v146, v172\n\t"
        "v_mul_f32_e32 v147, v147, v172\n\t"
        "v_cvt_pk_bf16_f32 v160, v136, v137\n\t"
        "v_cvt_pk_bf16_f32 v161, v138, v139\n\t"
        "v_cvt_pk_bf16_f32 v162, v140, v140\n\t"
        "v_cvt_pk_bf16_f32 v163, v140, v140\n\t"
        "v_cvt_pk_bf16_f32 v164, v144, v145\n\t"
        "v_cvt_pk_bf16_f32 v165, v146, v147\n\t"
        "v_mfma_f32_16x16x32_bf16 v[176:179], a[64:67], v[160:163], 0\n\t"
        "v_cvt_pk_bf16_f32 v156, 0, v156\n\t"
        "v_cvt_pk_bf16_f32 v157, 0, v157\n\t"
        "v_cvt_pk_bf16_f32 v158, 0, v158\n\t"
        "v_mfma_f32_16x16x32_bf16 v[180:183], a[68:71], v[160:163], 0\n\t"
        "v_cvt_pk_bf16_f32 v159, 0, v159\n\t"
        "v_mul_f32_e32 v156, 0x3db504f3, v156\n\t"
        "v_mul_f32_e32 v157, 0x3db504f3, v157\n\t"
        "v_mfma_f32_16x16x32_bf16 v[184:187], a[72:75], v[160:163], 0\n\t"
        "v_mul_f32_e32 v158, 0x3db504f3, v158\n\t"
        "v_mul_f32_e32 v159, 0x3db504f3, v159\n\t"
        "v_cvt_pk_bf16_f32 v156, 0, v156\n\t"
        "v_mfma_f32_16x16x32_bf16 v[188:191], a[76:79], v[160:163], 0\n\t"
        "v_cvt_pk_bf16_f32 v157, 0, v157\n\t"
        "v_cvt_pk_bf16_f32 v158, 0, v158\n\t"
        "v_cvt_pk_bf16_f32 v159, 0, v159\n\t"
        "v_mfma_f32_16x16x32_bf16 v[192:195], a[80:83], v[160:163], 0\n\t"
        "v_cndmask_b32_e64 v156, v156, v254, s[56:57]\n\t"
        "v_cndmask_b32_e64 v157, v157, v254, s[56:57]\n\t"
        "v_cndmask_b32_e64 v158, v158, v254, s[56:57]\n\t"
        "v_mfma_f32_16x16x32_bf16 v[196:199], a[84:87], v[160:163], 0\n\t"
        "v_cndmask_b32_e64 v159, v159, v254, s[56:57]\n\t"
        "v_max3_f32 v174, v174, v156, v157\n\t"
        "v_max3_f32 v175, v174, v158, v159\n\t"
        "v_mfma_f32_16x16x32_bf16 v[200:203], a[88:91], v[160:163], 0\n\t"
        "v_max3_f32 v174, v174, v158, v159\n\t"
        "s_nop 1\n\t"
        "v_permlane16_swap_b32_e32 v174, v175\n\t"
        "v_max_f32_e32 v174, v174, v175\n\t"
        "v_mfma_f32_16x16x32_bf16 v[204:207], a[92:95], v[160:163], 0\n\t"
        "v_mov_b32_e32 v175, v174\n\t"
        "s_nop 1\n\t"
        "v_permlane32_swap_b32_e32 v174, v175\n\t"
        "v_max_f32_e32 v174, v174, v175\n\t"
        "v_mfma_f32_16x16x16_bf16 v[176:179], a[104:105], v[164:165], v[176:179]\n\t"
        "v_sub_f32_e32 v148, v148, v174\n\t"
        "v_sub_f32_e32 v149, v149, v174\n\t"
        "v_sub_f32_e32 v150, v150, v174\n\t"
        "v_mfma_f32_16x16x16_bf16 v[180:183], a[106:107], v[164:165], v[180:183]\n\t"
        "v_sub_f32_e32 v151, v151, v174\n\t"
        "v_sub_f32_e32 v152, v152, v174\n\t"
        "v_sub_f32_e32 v153, v153, v174\n\t"
        "v_mfma_f32_16x16x16_bf16 v[184:187], a[108:109], v[164:165], v[184:187]\n\t"
        "v_sub_f32_e32 v154, v154, v174\n\t"
        "v_sub_f32_e32 v155, v155, v174\n\t"
        "v_sub_f32_e32 v156, v156, v174\n\t"
        "v_mfma_f32_16x16x16_bf16 v[188:191], a[110:111], v[164:165], v[188:191]\n\t"
        "v_sub_f32_e32 v157, v157, v174\n\t"
        "v_sub_f32_e32 v158, v158, v174\n\t"
        "v_sub_f32_e32 v159, v159, v174\n\t"
        "v_mfma_f32_16x16x16_bf16 v[192:195], a[112:113], v[164:165], v[192:195]\n\t"
        "v_mul_f32_e32 v148, 0x3fb8aa3b, v148\n\t"
        "v_mul_f32_e32 v149, 0x3fb8aa3b, v149\n\t"
        "v_mul_f32_e32 v150, 0x3fb8aa3b, v150\n\t"
        "v_mfma_f32_16x16x16_bf16 v[196:199], a[114:115], v[164:165], v[196:199]\n\t"
        "v_mul_f32_e32 v151, 0x3fb8aa3b, v151\n\t"
        "v_mul_f32_e32 v152, 0x3fb8aa3b, v152\n\t"
        "v_mul_f32_e32 v153, 0x3fb8aa3b, v153\n\t"
        "v_mfma_f32_16x16x16_bf16 v[200:203], a[116:117], v[164:165], v[200:203]\n\t"
        "v_mul_f32_e32 v154, 0x3fb8aa3b, v154\n\t"
        "v_mul_f32_e32 v155, 0x3fb8aa3b, v155\n\t"
        "v_mul_f32_e32 v156, 0x3fb8aa3b, v156\n\t"
        "v_mfma_f32_16x16x16_bf16 v[204:207], a[118:119], v[164:165], v[204:207]\n\t"
        "v_mul_f32_e32 v157, 0x3fb8aa3b, v157\n\t"
        "v_mul_f32_e32 v158, 0x3fb8aa3b, v158\n\t"
        "v_mul_f32_e32 v159, 0x3fb8aa3b, v159\n\t"
        "v_exp_f32_e32 v148, v148\n\t"
        "v_exp_f32_e32 v149, v149\n\t"
        "v_exp_f32_e32 v150, v150\n\t"
        "v_exp_f32_e32 v151, v151\n\t"
        "v_exp_f32_e32 v152, v152\n\t"
        "v_exp_f32_e32 v153, v153\n\t"
        "v_exp_f32_e32 v154, v154\n\t"
        "v_exp_f32_e32 v155, v155\n\t"
        "v_exp_f32_e32 v156, v156\n\t"
        "v_exp_f32_e32 v157, v157\n\t"
        "v_exp_f32_e32 v158, v158\n\t"
        "v_exp_f32_e32 v159, v159\n\t"
        "v_add_f32_e32 v174, v148, v149\n\t"
        "v_add_f32_e32 v174, v174, v150\n\t"
        "v_add_f32_e32 v174, v174, v151\n\t"
        "v_add_f32_e32 v174, v174, v152\n\t"
        "v_add_f32_e32 v174, v174, v153\n\t"
        "v_add_f32_e32 v174, v174, v154\n\t"
        "v_add_f32_e32 v174, v174, v155\n\t"
        "v_add_f32_e32 v174, v174, v156\n\t"
        "v_add_f32_e32 v174, v174, v157\n\t"
        "v_add_f32_e32 v174, v174, v158\n\t"
        "v_add_f32_e32 v174, v174, v159\n\t"
        "v_mov_b32_e32 v175, v174\n\t"
        "s_nop 1\n\t"
        "v_permlane16_swap_b32_e32 v174, v175\n\t"
        "v_add_f32_e32 v174, v174, v175\n\t"
        "v_mov_b32_e32 v175, v174\n\t"
        "s_nop 1\n\t"
        "v_permlane32_swap_b32_e32 v174, v175\n\t"
        "v_add_f32_e32 v174, v174, v175\n\t"
        "v_rcp_f32_e32 v174, v174\n\t"
        "s_nop 0\n\t"
        "v_mul_f32_e32 v148, v148, v174\n\t"
        "v_mul_f32_e32 v149, v149, v174\n\t"
        "v_mul_f32_e32 v150, v150, v174\n\t"
        "v_mul_f32_e32 v151, v151, v174\n\t"
        "v_mul_f32_e32 v152, v152, v174\n\t"
        "v_mul_f32_e32 v153, v153, v174\n\t"
        "v_mul_f32_e32 v154, v154, v174\n\t"
        "v_mul_f32_e32 v155, v155, v174\n\t"
        "v_mul_f32_e32 v156, v156, v174\n\t"
        "v_mul_f32_e32 v157, v157, v174\n\t"
        "v_mul_f32_e32 v158, v158, v174\n\t"
        "v_mul_f32_e32 v159, v159, v174\n\t"
        "v_cvt_pk_bf16_f32 v166, v148, v149\n\t"
        "v_cvt_pk_bf16_f32 v167, v150, v151\n\t"
        "v_cvt_pk_bf16_f32 v168, v152, v153\n\t"
        "v_cvt_pk_bf16_f32 v169, v154, v155\n\t"
        "v_cvt_pk_bf16_f32 v170, v156, v157\n\t"
        "v_cvt_pk_bf16_f32 v171, v158, v159\n\t"
        "s_waitcnt vmcnt(38)\n\t"
        "s_lshl_b32 s80, s44, 7\n\t"
        "s_cmp_lt_i32 s44, 0\n\t"
        "s_cselect_b32 s80, s36, s80\n\t"
        "v_mov_b32_e32 v242, 1\n\t"
        "buffer_atomic_add v242, v250, %[rd], s80 offen sc0 sc1\n\t"
        "s_lshl_b32 s86, s37, 18\n\t"
        "s_add_u32 s86, s86, %[qx]\n\t"
        "s_add_u32 s87, s86, 0x1000\n\t"
        "v_mfma_f32_16x16x32_bf16 v[208:211], a[64:67], v[166:169], 0\n\t"
        "v_cvt_pk_bf16_f32 v176, v176, v177\n\t"
        "v_cvt_pk_bf16_f32 v177, v178, v179\n\t"
        "v_mfma_f32_16x16x32_bf16 v[212:215], a[68:71], v[166:169], 0\n\t"
        "v_cvt_pk_bf16_f32 v178, v180, v181\n\t"
        "v_cvt_pk_bf16_f32 v179, v182, v183\n\t"
        "v_mfma_f32_16x16x32_bf16 v[216:219], a[72:75], v[166:169], 0\n\t"
        "buffer_store_dwordx4 v[176:179], v244, %[rd], s86 offen offset:0 sc1\n\t"
        "v_cvt_pk_bf16_f32 v184, v184, v185\n\t"
        "v_mfma_f32_16x16x32_bf16 v[220:223], a[76:79], v[166:169], 0\n\t"
        "v_cvt_pk_bf16_f32 v185, v186, v187\n\t"
        "v_cvt_pk_bf16_f32 v186, v188, v189\n\t"
        "v_mfma_f32_16x16x32_bf16 v[224:227], a[80:83], v[166:169], 0\n\t"
        "v_cvt_pk_bf16_f32 v187, v190, v191\n\t"
        "buffer_store_dwordx4 v[184:187], v244, %[rd], s86 offen offset:1024 sc1\n\t"
        "v_mfma_f32_16x16x32_bf16 v[228:231], a[84:87], v[166:169], 0\n\t"
        "v_cvt_pk_bf16_f32 v192, v192, v193\n\t"
        "v_cvt_pk_bf16_f32 v193, v194, v195\n\t"
        "v_mfma_f32_16x16x32_bf16 v[232:235], a[88:91], v[166:169], 0\n\t"
        "v_cvt_pk_bf16_f32 v194, v196, v197\n\t"
        "v_cvt_pk_bf16_f32 v195, v198, v199\n\t"
        "v_mfma_f32_16x16x32_bf16 v[236:239], a[92:95], v[166:169], 0\n\t"
        "buffer_store_dwordx4 v[192:195], v244, %[rd], s86 offen offset:2048 sc1\n\t"
        "v_cvt_pk_bf16_f32 v200, v200, v201\n\t"
        "v_mfma_f32_16x16x16_bf16 v[208:211], a[104:105], v[170:171], v[208:211]\n\t"
        "v_cvt_pk_bf16_f32 v201, v202, v203\n\t"
        "v_cvt_pk_bf16_f32 v202, v204, v205\n\t"
        "v_mfma_f32_16x16x16_bf16 v[212:215], a[106:107], v[170:171], v[212:215]\n\t"
        "v_cvt_pk_bf16_f32 v203, v206, v207\n\t"
        "buffer_store_dwordx4 v[200:203], v244, %[rd], s86 offen offset:3072 sc1\n\t"
        "v_mfma_f32_16x16x16_bf16 v[216:219], a[108:109], v[170:171], v[216:219]\n\t"
        "v_mfma_f32_16x16x16_bf16 v[220:223], a[110:111], v[170:171], v[220:223]\n\t"
        "v_mfma_f32_16x16x16_bf16 v[224:227], a[112:113], v[170:171], v[224:227]\n\t"
        "v_mfma_f32_16x16x16_bf16 v[228:231], a[114:115], v[170:171], v[228:231]\n\t"
        "v_mfma_f32_16x16x16_bf16 v[232:235], a[116:117], v[170:171], v[232:235]\n\t"
        "v_mfma_f32_16x16x16_bf16 v[236:239], a[118:119], v[170:171], v[236:239]\n\t"
        "v_cvt_pk_bf16_f32 v208, v208, v209\n\t"
        "v_cvt_pk_bf16_f32 v209, v210, v211\n\t"
        "v_cvt_pk_bf16_f32 v210, v212, v213\n\t"
        "v_cvt_pk_bf16_f32 v211, v214, v215\n\t"
        "buffer_store_dwordx4 v[208:211], v244, %[rd], s87 offen offset:0 sc1\n\t"
        "v_cvt_pk_bf16_f32 v216, v216, v217\n\t"
        "v_cvt_pk_bf16_f32 v217, v218, v219\n\t"
        "v_cvt_pk_bf16_f32 v218, v220, v221\n\t"
        "v_cvt_pk_bf16_f32 v219, v222, v223\n\t"
        "buffer_store_dwordx4 v[216:219], v244, %[rd], s87 offen offset:1024 sc1\n\t"
        "v_cvt_pk_bf16_f32 v224, v224, v225\n\t"
        "v_cvt_pk_bf16_f32 v225, v226, v227\n\t"
        "v_cvt_pk_bf16_f32 v226, v228, v229\n\t"
        "v_cvt_pk_bf16_f32 v227, v230, v231\n\t"
        "buffer_store_dwordx4 v[224:227], v244, %[rd], s87 offen offset:2048 sc1\n\t"
        "v_cvt_pk_bf16_f32 v232, v232, v233\n\t"
        "v_cvt_pk_bf16_f32 v233, v234, v235\n\t"
        "v_cvt_pk_bf16_f32 v234, v236, v237\n\t"
        "v_cvt_pk_bf16_f32 v235, v238, v239\n\t"
        "buffer_store_dwordx4 v[232:235], v244, %[rd], s87 offen offset:3072 sc1\n\t"
        "s_mov_b32 s45, s44\n\t"
        "s_mov_b32 s44, s37\n\t"
        "s_add_u32 s80, s37, 4\n\t"
        "s_cmp_gt_u32 s40, s80\n\t"
        "s_cselect_b32 s80, s46, 1\n\t"
        "s_cmp_eq_u32 s80, 0\n\t"
        "s_cbranch_scc1 .La12%=\n\t"
        "s_waitcnt vmcnt(0)\n\t"
        "v_readlane_b32 s84, v242, 0\n\t"
        "v_readlane_b32 s85, v242, 32\n\t"
        "v_mov_b32_e32 v243, 32\n\t"
        "s_and_b32 s80, s45, 3\n\t"
        "s_lshl_b32 s80, s80, 12\n\t"
        "s_lshr_b32 s81, s45, 2\n\t"
        "s_lshl_b32 s81, s81, 3\n\t"
        "s_add_u32 s80, s80, s81\n\t"
        "s_lshr_b32 s82, s45, 1\n\t"
        "s_xnor_b32 s81, s45, s82\n\t"
        "s_and_b32 s81, s81, 1\n\t"
        "s_lshl_b32 s81, s81, 13\n\t"
        "s_lshl_b32 s82, s82, 2\n\t"
        "s_add_u32 s81, s81, s82\n\t"
        "s_cmp_lt_i32 s45, 60\n\t"
        "s_cselect_b32 s80, s80, s81\n\t"
        "s_cselect_b32 s81, 4, 0x1000\n\t"
        "s_add_u32 s81, s81, s80\n\t"
        "s_cmp_eq_u32 s84, 31\n\t"
        "s_cselect_b32 s80, s80, s36\n\t"
        "s_cmp_eq_u32 s85, 31\n\t"
        "s_cselect_b32 s81, s81, s36\n\t"
        "s_cmp_lt_i32 s45, 0\n\t"
        "s_cselect_b32 s80, s36, s80\n\t"
        "s_cselect_b32 s81, s36, s81\n\t"
        "buffer_store_dword v243, v251, %[rd], s80 offen sc1\n\t"
        "buffer_store_dword v243, v251, %[rd], s81 offen sc1\n\t"
        "s_lshl_b32 s80, s44, 7\n\t"
        "s_cmp_lt_i32 s44, 0\n\t"
        "s_cselect_b32 s80, s36, s80\n\t"
        "v_mov_b32_e32 v242, 1\n\t"
        "buffer_atomic_add v242, v250, %[rd], s80 offen sc0 sc1\n\t"
        "s_waitcnt vmcnt(0)\n\t"
        "v_readlane_b32 s84, v242, 0\n\t"
        "v_readlane_b32 s85, v242, 32\n\t"
        "v_mov_b32_e32 v243, 32\n\t"
        "s_and_b32 s80, s44, 3\n\t"
        "s_lshl_b32 s80, s80, 12\n\t"
        "s_lshr_b32 s81, s44, 2\n\t"
        "s_lshl_b32 s81, s81, 3\n\t"
        "s_add_u32 s80, s80, s81\n\t"
        "s_lshr_b32 s82, s44, 1\n\t"
        "s_xnor_b32 s81, s44, s82\n\t"
        "s_and_b32 s81, s81, 1\n\t"
        "s_lshl_b32 s81, s81, 13\n\t"
        "s_lshl_b32 s82, s82, 2\n\t"
        "s_add_u32 s81, s81, s82\n\t"
        "s_cmp_lt_i32 s44, 60\n\t"
        "s_cselect_b32 s80, s80, s81\n\t"
        "s_cselect_b32 s81, 4, 0x1000\n\t"
        "s_add_u32 s81, s81, s80\n\t"
        "s_cmp_eq_u32 s84, 31\n\t"
        "s_cselect_b32 s80, s80, s36\n\t"
        "s_cmp_eq_u32 s85, 31\n\t"
        "s_cselect_b32 s81, s81, s36\n\t"
        "s_cmp_lt_i32 s44, 0\n\t"
        "s_cselect_b32 s80, s36, s80\n\t"
        "s_cselect_b32 s81, s36, s81\n\t"
        "buffer_store_dword v243, v251, %[rd], s80 offen sc1\n\t"
        "buffer_store_dword v243, v251, %[rd], s81 offen sc1\n\t"
        "s_mov_b32 s44, -1\n\t"
        "s_mov_b32 s45, -1\n\t"
        ".La12%=:\n\t"
        "s_mov_b32 s38, s46\n\t"
        "s_mov_b32 s39, s47\n\t"
        "s_add_u32 s37, s37, 4\n\t"
        "s_cmp_eq_u32 s38, 0\n\t"
        "s_cbranch_scc1 .La14%=\n\t"
        "s_mov_b32 s97, 0\n\t"
        ".La13%=:\n\t"
        "buffer_load_dword v240, v246, %[rd], 0 offen sc1\n\t"
        "buffer_load_dword v241, v247, %[rd], 0 offen sc1\n\t"
        "s_add_u32 s97, s97, 1\n\t"
        "s_cmp_lt_u32 s97, 0x200000\n\t"
        "s_cbranch_scc0 .Lgu%=\n\t"
        "s_memrealtime s[98:99]\n\t"
        "s_waitcnt vmcnt(0) lgkmcnt(0)\n\t"
        "v_cmp_eq_u32_e32 vcc, %[tag], v240\n\t"
        "s_nop 1\n\t"
        "s_or_b64 s[42:43], s[42:43], vcc\n\t"
        "v_readfirstlane_b32 s80, v241\n\t"
        "s_nop 0\n\t"
        "s_cmp_gt_u32 s80, s40\n\t"
        "s_cselect_b32 s41, s98, s41\n\t"
        "s_max_u32 s40, s40, s80\n\t"
        "s_and_b32 s80, s37, 60\n\t"
        "s_lshr_b64 s[80:81], s[42:43], s80\n\t"
        "s_andn2_b32 s80, 15, s80\n\t"
        "s_cbranch_scc0 .La13%=x\n\t"
        "s_sleep 16\n\t"
        "s_branch .La13%=\n\t"
        ".La13%=x:\n\t"
        "s_cmp_gt_u32 s40, s37\n\t"
        "s_cselect_b32 s39, 0, s36\n\t"
        "s_lshl_b32 s80, s37, 18\n\t"
        "s_or_b32 s80, s80, 0\n\t"
        "s_lshl_b32 s81, s37, 17\n\t"
        "s_add_u32 s81, s81, %[km]\n\t"
        "s_or_b32 s81, s81, s39\n\t"
        "s_add_u32 s82, s80, %[kx]\n\t"
        "s_add_u32 s83, s82, 0x1000\n\t"
        "buffer_load_dwordx4 v[0:3], v244, %[rd], s82 offen offset:0 sc1\n\t"
        "buffer_load_dwordx4 v[4:7], v244, %[rd], s82 offen offset:1024 sc1\n\t"
        "buffer_load_dwordx4 v[8:11], v244, %[rd], s82 offen offset:2048 sc1\n\t"
        "buffer_load_dwordx4 v[12:15], v244, %[rd], s82 offen offset:3072 sc1\n\t"
        "buffer_load_dwordx4 v[16:19], v244, %[rd], s83 offen offset:0 sc1\n\t"
        "buffer_load_dwordx4 v[20:23], v244, %[rd], s83 offen offset:1024 sc1\n\t"
        "buffer_load_dwordx4 v[24:27], v244, %[rd], s83 offen offset:2048 sc1\n\t"
        "buffer_load_dwordx4 v[28:31], v244, %[rd], s83 offen offset:3072 sc1\n\t"
        "s_cmp_eq_u64 s[42:43], -1\n\t"
        "s_cbranch_scc1 .La15%=\n\t"
        "s_sleep %c[aslp]\n\t"
        ".La15%=:\n\t"
        "s_sleep %c[bslp]\n\t"
        "s_add_u32 s82, s80, %[qx]\n\t"
        "s_add_u32 s83, s82, 0x1000\n\t"
        "buffer_load_dwordx4 v[32:35], v244, %[rd], s82 offen offset:0 sc1\n\t"
        "buffer_load_dwordx4 v[36:39], v244, %[rd], s82 offen offset:1024 sc1\n\t"
        "buffer_load_dwordx4 v[40:43], v244, %[rd], s82 offen offset:2048 sc1\n\t"
        "buffer_load_dwordx4 v[44:47], v244, %[rd], s82 offen offset:3072 sc1\n\t"
        "buffer_load_dwordx4 v[48:51], v244, %[rd], s83 offen offset:0 sc1\n\t"
        "buffer_load_dwordx4 v[52:55], v244, %[rd], s83 offen offset:1024 sc1\n\t"
        "buffer_load_dwordx4 v[56:59], v244, %[rd], s83 offen offset:2048 sc1\n\t"
        "buffer_load_dwordx4 v[60:63], v244, %[rd], s83 offen offset:3072 sc1\n\t"
        "s_cmp_eq_u64 s[42:43], -1\n\t"
        "s_cbranch_scc1 .La16%=\n\t"
        "s_sleep %c[aslp]\n\t"
        ".La16%=:\n\t"
        "s_sleep %c[bslp]\n\t"
        "buffer_load_dwordx4 v[96:99], v244, %[rd], s81 offen offset:0 sc1\n\t"
        "buffer_load_dwordx4 v[100:103], v244, %[rd], s81 offen offset:1024 sc1\n\t"
        "buffer_load_dwordx2 v[104:105], v245, %[rd], s81 offen offset:2048 sc1\n\t"
        "buffer_load_dwordx2 v[106:107], v245, %[rd], s81 offen offset:2304 sc1\n\t"
        "buffer_load_dwordx2 v[108:109], v245, %[rd], s81 offen offset:2560 sc1\n\t"
        "buffer_load_dwordx2 v[110:111], v245, %[rd], s81 offen offset:2816 sc1\n\t"
        "buffer_load_dwordx2 v[112:113], v245, %[rd], s81 offen offset:3072 sc1\n\t"
        "buffer_load_dwordx2 v[114:115], v245, %[rd], s81 offen offset:3328 sc1\n\t"
        "buffer_load_dwordx2 v[116:117], v245, %[rd], s81 offen offset:3584 sc1\n\t"
        "buffer_load_dwordx2 v[118:119], v245, %[rd], s81 offen offset:3840 sc1\n\t"
        "s_add_u32 s82, s80, %[vx]\n\t"
        "s_add_u32 s83, s82, 0x1000\n\t"
        "buffer_load_dwordx4 v[64:67], v244, %[rd], s82 offen offset:0 sc1\n\t"
        "buffer_load_dwordx4 v[68:71], v244, %[rd], s82 offen offset:1024 sc1\n\t"
        "buffer_load_dwordx4 v[72:75], v244, %[rd], s82 offen offset:2048 sc1\n\t"
        "buffer_load_dwordx4 v[76:79], v244, %[rd], s82 offen offset:3072 sc1\n\t"
        "buffer_load_dwordx4 v[80:83], v244, %[rd], s83 offen offset:0 sc1\n\t"
        "buffer_load_dwordx4 v[84:87], v244, %[rd], s83 offen offset:1024 sc1\n\t"
        "buffer_load_dwordx4 v[88:91], v244, %[rd], s83 offen offset:2048 sc1\n\t"
        "buffer_load_dwordx4 v[92:95], v244, %[rd], s83 offen offset:3072 sc1\n\t"
        "s_cmp_eq_u64 s[42:43], -1\n\t"
        "s_cbranch_scc1 .La17%=\n\t"
        "s_sleep %c[aslp]\n\t"
        ".La17%=:\n\t"
        "s_sleep %c[bslp]\n\t"
        "buffer_store_dword v0, off, %[rd], s36\n\t"
        "buffer_store_dword v0, off, %[rd], s36\n\t"
        "buffer_store_dword v0, off, %[rd], s36\n\t"
        "buffer_store_dword v0, off, %[rd], s36\n\t"
        "buffer_store_dword v0, off, %[rd], s36\n\t"
        "buffer_store_dword v0, off, %[rd], s36\n\t"
        "buffer_store_dword v0, off, %[rd], s36\n\t"
        "buffer_store_dword v0, off, %[rd], s36\n\t"
        "buffer_store_dword v0, off, %[rd], s36\n\t"
        ".La14%=:\n\t"
        "s_cmp_gt_u32 s40, s37\n\t"
        "s_cselect_b32 s80, s39, 0\n\t"
        "s_cmp_eq_u32 s80, 0\n\t"
        "s_cbranch_scc1 .La18%=\n\t"
        "s_mov_b32 s39, 0\n\t"
        "s_lshl_b32 s81, s37, 17\n\t"
        "s_add_u32 s81, s81, %[km]\n\t"
        "buffer_load_dwordx4 v[96:99], v244, %[rd], s81 offen offset:0 sc1\n\t"
        "buffer_load_dwordx4 v[100:103], v244, %[rd], s81 offen offset:1024 sc1\n\t"
        "buffer_load_dwordx2 v[104:105], v245, %[rd], s81 offen offset:2048 sc1\n\t"
        "buffer_load_dwordx2 v[106:107], v245, %[rd], s81 offen offset:2304 sc1\n\t"
        "buffer_load_dwordx2 v[108:109], v245, %[rd], s81 offen offset:2560 sc1\n\t"
        "buffer_load_dwordx2 v[110:111], v245, %[rd], s81 offen offset:2816 sc1\n\t"
        "buffer_load_dwordx2 v[112:113], v245, %[rd], s81 offen offset:3072 sc1\n\t"
        "buffer_load_dwordx2 v[114:115], v245, %[rd], s81 offen offset:3328 sc1\n\t"
        "buffer_load_dwordx2 v[116:117], v245, %[rd], s81 offen offset:3584 sc1\n\t"
        "buffer_load_dwordx2 v[118:119], v245, %[rd], s81 offen offset:3840 sc1\n\t"
        "buffer_store_dword v0, off, %[rd], s36\n\t"
        "buffer_store_dword v0, off, %[rd], s36\n\t"
        "buffer_store_dword v0, off, %[rd], s36\n\t"
        "buffer_store_dword v0, off, %[rd], s36\n\t"
        "buffer_store_dword v0, off, %[rd], s36\n\t"
        "buffer_store_dword v0, off, %[rd], s36\n\t"
        "buffer_store_dword v0, off, %[rd], s36\n\t"
        "buffer_store_dword v0, off, %[rd], s36\n\t"
        ".La18%=:\n\t"
        "s_add_u32 s86, s37, 4\n\t"
        "s_and_b32 s80, s86, 60\n\t"
        "s_lshr_b64 s[80:81], s[42:43], s80\n\t"
        "s_andn2_b32 s80, 15, s80\n\t"
        "s_cselect_b32 s46, s36, 0\n\t"
        "s_cmp_lt_u32 s86, 64\n\t"
        "s_cselect_b32 s46, s46, s36\n\t"
        "s_cmp_gt_u32 s40, s86\n\t"
        "s_cselect_b32 s47, s46, s36\n\t"
        "s_lshl_b32 s80, s86, 18\n\t"
        "s_or_b32 s80, s80, s46\n\t"
        "s_lshl_b32 s81, s86, 17\n\t"
        "s_add_u32 s81, s81, %[km]\n\t"
        "s_or_b32 s81, s81, s47\n\t"
        "s_add_u32 s82, s80, %[kx]\n\t"
        "s_add_u32 s83, s82, 0x1000\n\t"
        "buffer_load_dwordx4 a[0:3], v244, %[rd], s82 offen offset:0 sc1\n\t"
        "buffer_load_dwordx4 a[4:7], v244, %[rd], s82 offen offset:1024 sc1\n\t"
        "buffer_load_dwordx4 a[8:11], v244, %[rd], s82 offen offset:2048 sc1\n\t"
        "buffer_load_dwordx4 a[12:15], v244, %[rd], s82 offen offset:3072 sc1\n\t"
        "buffer_load_dwordx4 a[16:19], v244, %[rd], s83 offen offset:0 sc1\n\t"
        "buffer_load_dwordx4 a[20:23], v244, %[rd], s83 offen offset:1024 sc1\n\t"
        "buffer_load_dwordx4 a[24:27], v244, %[rd], s83 offen offset:2048 sc1\n\t"
        "buffer_load_dwordx4 a[28:31], v244, %[rd], s83 offen offset:3072 sc1\n\t"
        "s_cmp_eq_u64 s[42:43], -1\n\t"
        "s_cbranch_scc1 .La19%=\n\t"
        "s_sleep %c[aslp]\n\t"
        ".La19%=:\n\t"
        "s_sleep %c[bslp]\n\t"
        "s_add_u32 s82, s80, %[qx]\n\t"
        "s_add_u32 s83, s82, 0x1000\n\t"
        "buffer_load_dwordx4 a[32:35], v244, %[rd], s82 offen offset:0 sc1\n\t"
        "buffer_load_dwordx4 a[36:39], v244, %[rd], s82 offen offset:1024 sc1\n\t"
        "buffer_load_dwordx4 a[40:43], v244, %[rd], s82 offen offset:2048 sc1\n\t"
        "buffer_load_dwordx4 a[44:47], v244, %[rd], s82 offen offset:3072 sc1\n\t"
        "buffer_load_dwordx4 a[48:51], v244, %[rd], s83 offen offset:0 sc1\n\t"
        "buffer_load_dwordx4 a[52:55], v244, %[rd], s83 offen offset:1024 sc1\n\t"
        "buffer_load_dwordx4 a[56:59], v244, %[rd], s83 offen offset:2048 sc1\n\t"
        "buffer_load_dwordx4 a[60:63], v244, %[rd], s83 offen offset:3072 sc1\n\t"
        "s_cmp_eq_u64 s[42:43], -1\n\t"
        "s_cbranch_scc1 .La20%=\n\t"
        "s_sleep %c[aslp]\n\t"
        ".La20%=:\n\t"
        "s_sleep %c[bslp]\n\t"
        "buffer_load_dwordx4 a[96:99], v244, %[rd], s81 offen offset:0 sc1\n\t"
        "buffer_load_dwordx4 a[100:103], v244, %[rd], s81 offen offset:1024 sc1\n\t"
        "buffer_load_dwordx2 a[104:105], v245, %[rd], s81 offen offset:2048 sc1\n\t"
        "buffer_load_dwordx2 a[106:107], v245, %[rd], s81 offen offset:2304 sc1\n\t"
        "buffer_load_dwordx2 a[108:109], v245, %[rd], s81 offen offset:2560 sc1\n\t"
        "buffer_load_dwordx2 a[110:111], v245, %[rd], s81 offen offset:2816 sc1\n\t"
        "buffer_load_dwordx2 a[112:113], v245, %[rd], s81 offen offset:3072 sc1\n\t"
        "buffer_load_dwordx2 a[114:115], v245, %[rd], s81 offen offset:3328 sc1\n\t"
        "buffer_load_dwordx2 a[116:117], v245, %[rd], s81 offen offset:3584 sc1\n\t"
        "buffer_load_dwordx2 a[118:119], v245, %[rd], s81 offen offset:3840 sc1\n\t"
        "s_add_u32 s82, s80, %[vx]\n\t"
        "s_add_u32 s83, s82, 0x1000\n\t"
        "buffer_load_dwordx4 a[64:67], v244, %[rd], s82 offen offset:0 sc1\n\t"
        "buffer_load_dwordx4 a[68:71], v244, %[rd], s82 offen offset:1024 sc1\n\t"
        "buffer_load_dwordx4 a[72:75], v244, %[rd], s82 offen offset:2048 sc1\n\t"
        "buffer_load_dwordx4 a[76:79], v244, %[rd], s82 offen offset:3072 sc1\n\t"
        "buffer_load_dwordx4 a[80:83], v244, %[rd], s83 offen offset:0 sc1\n\t"
        "buffer_load_dwordx4 a[84:87], v244, %[rd], s83 offen offset:1024 sc1\n\t"
        "buffer_load_dwordx4 a[88:91], v244, %[rd], s83 offen offset:2048 sc1\n\t"
        "buffer_load_dwordx4 a[92:95], v244, %[rd], s83 offen offset:3072 sc1\n\t"
        "s_cmp_eq_u64 s[42:43], -1\n\t"
        "s_cbranch_scc1 .La21%=\n\t"
        "s_sleep %c[aslp]\n\t"
        ".La21%=:\n\t"
        "s_sleep %c[bslp]\n\t"
        "s_waitcnt vmcnt(42)\n\t"
        "s_memrealtime s[98:99]\n\t"
        "v_mfma_f32_16x16x32_bf16 v[136:139], v[0:3], v[32:35], 0\n\t"
        "v_mfma_f32_16x16x32_bf16 v[148:151], v[0:3], v[48:51], 0\n\t"
        "v_mfma_f32_16x16x32_bf16 v[152:155], v[16:19], v[48:51], 0\n\t"
        "v_mfma_f32_16x16x32_bf16 v[136:139], v[4:7], v[36:39], v[136:139]\n\t"
        "v_mfma_f32_16x16x32_bf16 v[148:151], v[4:7], v[52:55], v[148:151]\n\t"
        "v_mfma_f32_16x16x32_bf16 v[152:155], v[20:23], v[52:55], v[152:155]\n\t"
        "v_mfma_f32_16x16x32_bf16 v[136:139], v[8:11], v[40:43], v[136:139]\n\t"
        "v_mfma_f32_16x16x32_bf16 v[148:151], v[8:11], v[56:59], v[148:151]\n\t"
        "v_mfma_f32_16x16x32_bf16 v[152:155], v[24:27], v[56:59], v[152:155]\n\t"
        "v_mfma_f32_16x16x32_bf16 v[136:139], v[12:15], v[44:47], v[136:139]\n\t"
        "v_mfma_f32_16x16x32_bf16 v[148:151], v[12:15], v[60:63], v[148:151]\n\t"
        "v_mfma_f32_16x16x32_bf16 v[152:155], v[28:31], v[60:63], v[152:155]\n\t"
        "s_waitcnt lgkmcnt(0)\n\t"
        "v_cmp_eq_u32_e32 vcc, %[tag], v240\n\t"
        "s_nop 1\n\t"
        "s_or_b64 s[42:43], s[42:43], vcc\n\t"
        "v_readfirstlane_b32 s80, v241\n\t"
        "s_nop 0\n\t"
        "s_cmp_gt_u32 s80, s40\n\t"
        "s_cselect_b32 s41, s98, s41\n\t"
        "s_max_u32 s40, s40, s80\n\t"
        "v_readlane_b32 s84, v242, 0\n\t"
        "v_readlane_b32 s85, v242, 32\n\t"
        "v_mov_b32_e32 v243, 32\n\t"
        "s_and_b32 s80, s45, 3\n\t"
        "s_lshl_b32 s80, s80, 12\n\t"
        "s_lshr_b32 s81, s45, 2\n\t"
        "s_lshl_b32 s81, s81, 3\n\t"
        "s_add_u32 s80, s80, s81\n\t"
        "s_lshr_b32 s82, s45, 1\n\t"
        "s_xnor_b32 s81, s45, s82\n\t"
        "s_and_b32 s81, s81, 1\n\t"
        "s_lshl_b32 s81, s81, 13\n\t"
        "s_lshl_b32 s82, s82, 2\n\t"
        "s_add_u32 s81, s81, s82\n\t"
        "s_cmp_lt_i32 s45, 60\n\t"
        "s_cselect_b32 s80, s80, s81\n\t"
        "s_cselect_b32 s81, 4, 0x1000\n\t"
        "s_add_u32 s81, s81, s80\n\t"
        "s_cmp_eq_u32 s84, 31\n\t"
        "s_cselect_b32 s80, s80, s36\n\t"
        "s_cmp_eq_u32 s85, 31\n\t"
        "s_cselect_b32 s81, s81, s36\n\t"
        "s_cmp_lt_i32 s45, 0\n\t"
        "s_cselect_b32 s80, s36, s80\n\t"
        "s_cselect_b32 s81, s36, s81\n\t"
        "buffer_store_dword v243, v251, %[rd], s80 offen sc1\n\t"
        "buffer_store_dword v243, v251, %[rd], s81 offen sc1\n\t"
        "s_add_u32 s86, s37, 8\n\t"
        "s_cmp_lg_u64 s[42:43], -1\n\t"
        "s_cselect_b32 s84, 0, s36\n\t"
        "s_cmp_gt_u32 s40, s86\n\t"
        "s_cselect_b32 s85, s36, s39\n\t"
        "buffer_load_dword v240, v246, %[rd], s84 offen sc1\n\t"
        "buffer_load_dword v241, v247, %[rd], s85 offen sc1\n\t"
        "v_cvt_pk_bf16_f32 v136, 0, v136\n\t"
        "v_cvt_pk_bf16_f32 v137, 0, v137\n\t"
        "v_cvt_pk_bf16_f32 v138, 0, v138\n\t"
        "v_cvt_pk_bf16_f32 v139, 0, v139\n\t"
        "v_mul_f32_e32 v136, 0x3db504f3, v136\n\t"
        "v_mul_f32_e32 v137, 0x3db504f3, v137\n\t"
        "v_mul_f32_e32 v138, 0x3db504f3, v138\n\t"
        "v_mul_f32_e32 v139, 0x3db504f3, v139\n\t"
        "v_cvt_pk_bf16_f32 v136, 0, v136\n\t"
        "v_cvt_pk_bf16_f32 v137, 0, v137\n\t"
        "v_cvt_pk_bf16_f32 v138, 0, v138\n\t"
        "v_cvt_pk_bf16_f32 v139, 0, v139\n\t"
        "v_cndmask_b32_e64 v136, v253, v136, s[48:49]\n\t"
        "v_cndmask_b32_e64 v137, v253, v137, s[50:51]\n\t"
        "v_cndmask_b32_e64 v138, v253, v138, s[52:53]\n\t"
        "v_cndmask_b32_e64 v139, v253, v139, s[54:55]\n\t"
        "v_cvt_pk_bf16_f32 v148, 0, v148\n\t"
        "v_cvt_pk_bf16_f32 v149, 0, v149\n\t"
        "v_cvt_pk_bf16_f32 v150, 0, v150\n\t"
        "v_cvt_pk_bf16_f32 v151, 0, v151\n\t"
        "v_mul_f32_e32 v148, 0x3db504f3, v148\n\t"
        "v_mul_f32_e32 v149, 0x3db504f3, v149\n\t"
        "v_mul_f32_e32 v150, 0x3db504f3, v150\n\t"
        "v_mul_f32_e32 v151, 0x3db504f3, v151\n\t"
        "v_cvt_pk_bf16_f32 v148, 0, v148\n\t"
        "v_cvt_pk_bf16_f32 v149, 0, v149\n\t"
        "v_cvt_pk_bf16_f32 v150, 0, v150\n\t"
        "v_cvt_pk_bf16_f32 v151, 0, v151\n\t"
        "v_cvt_pk_bf16_f32 v152, 0, v152\n\t"
        "v_cvt_pk_bf16_f32 v153, 0, v153\n\t"
        "v_cvt_pk_bf16_f32 v154, 0, v154\n\t"
        "v_cvt_pk_bf16_f32 v155, 0, v155\n\t"
        "v_mul_f32_e32 v152, 0x3db504f3, v152\n\t"
        "v_mul_f32_e32 v153, 0x3db504f3, v153\n\t"
        "v_mul_f32_e32 v154, 0x3db504f3, v154\n\t"
        "v_mul_f32_e32 v155, 0x3db504f3, v155\n\t"
        "v_cvt_pk_bf16_f32 v152, 0, v152\n\t"
        "v_cvt_pk_bf16_f32 v153, 0, v153\n\t"
        "v_cvt_pk_bf16_f32 v154, 0, v154\n\t"
        "v_cvt_pk_bf16_f32 v155, 0, v155\n\t"
        "v_cndmask_b32_e64 v152, v253, v152, s[48:49]\n\t"
        "v_cndmask_b32_e64 v153, v253, v153, s[50:51]\n\t"
        "v_cndmask_b32_e64 v154, v253, v154, s[52:53]\n\t"
        "v_cndmask_b32_e64 v155, v253, v155, s[54:55]\n\t"
        "v_max3_f32 v172, v136, v137, v253\n\t"
        "v_max3_f32 v172, v172, v138, v139\n\t"
        "v_mov_b32_e32 v140, v253\n\t"
        "v_max_f32_e32 v174, v148, v149\n\t"
        "v_max3_f32 v174, v174, v150, v151\n\t"
        "v_max3_f32 v174, v174, v152, v153\n\t"
        "v_max3_f32 v174, v174, v154, v155\n\t"
        "s_cmp_eq_u32 s39, 0\n\t"
        "s_cbranch_scc1 .La23%=\n\t"
        "s_waitcnt vmcnt(0)\n\t"
        "s_lshl_b32 s80, s44, 7\n\t"
        "s_cmp_lt_i32 s44, 0\n\t"
        "s_cselect_b32 s80, s36, s80\n\t"
        "v_mov_b32_e32 v242, 1\n\t"
        "buffer_atomic_add v242, v250, %[rd], s80 offen sc0 sc1\n\t"
        "s_waitcnt vmcnt(0)\n\t"
        "v_readlane_b32 s84, v242, 0\n\t"
        "v_readlane_b32 s85, v242, 32\n\t"
        "v_mov_b32_e32 v243, 32\n\t"
        "s_and_b32 s80, s44, 3\n\t"
        "s_lshl_b32 s80, s80, 12\n\t"
        "s_lshr_b32 s81, s44, 2\n\t"
        "s_lshl_b32 s81, s81, 3\n\t"
        "s_add_u32 s80, s80, s81\n\t"
        "s_lshr_b32 s82, s44, 1\n\t"
        "s_xnor_b32 s81, s44, s82\n\t"
        "s_and_b32 s81, s81, 1\n\t"
        "s_lshl_b32 s81, s81, 13\n\t"
        "s_lshl_b32 s82, s82, 2\n\t"
        "s_add_u32 s81, s81, s82\n\t"
        "s_cmp_lt_i32 s44, 60\n\t"
        "s_cselect_b32 s80, s80, s81\n\t"
        "s_cselect_b32 s81, 4, 0x1000\n\t"
        "s_add_u32 s81, s81, s80\n\t"
        "s_cmp_eq_u32 s84, 31\n\t"
        "s_cselect_b32 s80, s80, s36\n\t"
        "s_cmp_eq_u32 s85, 31\n\t"
        "s_cselect_b32 s81, s81, s36\n\t"
        "s_cmp_lt_i32 s44, 0\n\t"
        "s_cselect_b32 s80, s36, s80\n\t"
        "s_cselect_b32 s81, s36, s81\n\t"
        "buffer_store_dword v243, v251, %[rd], s80 offen sc1\n\t"
        "buffer_store_dword v243, v251, %[rd], s81 offen sc1\n\t"
        "s_mov_b32 s44, -1\n\t"
        "s_mov_b32 s97, 0\n\t"
        "s_min_u32 s87, s37, 62\n\t"
        ".La22%=:\n\t"
        "s_cmp_gt_u32 s40, s87\n\t"
        "s_cbranch_scc1 .La22%=x\n\t"
        "s_memrealtime s[98:99]\n\t"
        "s_add_u32 s97, s97, 1\n\t"
        "s_cmp_lt_u32 s97, 0x400000\n\t"
        "s_cbranch_scc0 .Lgu%=\n\t"
        "s_sub_u32 s80, s87, s40\n\t"
        "s_cmp_ge_u32 s87, %c[efrom]\n\t"
        "s_subb_u32 s80, s80, 0\n\t"
        "s_mul_i32 s80, s80, %c[smin]\n\t"
        "s_waitcnt lgkmcnt(0)\n\t"
        "s_sub_u32 s81, s98, s41\n\t"
        "s_cmp_ge_i32 s81, s80\n\t"
        "s_cbranch_scc1 .La22%=p\n\t"
        "s_sleep 16\n\t"
        "s_branch .La22%=\n\t"
        ".La22%=p:\n\t"
        "buffer_load_dword v241, v247, %[rd], 0 offen sc1\n\t"
        "s_waitcnt vmcnt(0)\n\t"
        "v_readfirstlane_b32 s80, v241\n\t"
        "s_nop 0\n\t"
        "s_cmp_gt_u32 s80, s40\n\t"
        "s_cselect_b32 s41, s98, s41\n\t"
        "s_max_u32 s40, s40, s80\n\t"
        "s_cmp_gt_u32 s40, s87\n\t"
        "s_cbranch_scc1 .La22%=x\n\t"
        "s_sleep %c[pslp]\n\t"
        "s_branch .La22%=\n\t"
        ".La22%=x:\n\t"
        "s_lshl_b32 s81, s37, 17\n\t"
        "s_add_u32 s81, s81, %[km]\n\t"
        "s_cmp_lg_u32 s37, 63\n\t"
        "s_cbranch_scc1 .La22%=l\n\t"
        "s_sleep %c[lslp]\n\t"
        ".La22%=l:\n\t"
        "buffer_load_dwordx4 v[96:99], v244, %[rd], s81 offen offset:0 sc1\n\t"
        "buffer_load_dwordx4 v[100:103], v244, %[rd], s81 offen offset:1024 sc1\n\t"
        "buffer_load_dwordx2 v[104:105], v245, %[rd], s81 offen offset:2048 sc1\n\t"
        "buffer_load_dwordx2 v[106:107], v245, %[rd], s81 offen offset:2304 sc1\n\t"
        "buffer_load_dwordx2 v[108:109], v245, %[rd], s81 offen offset:2560 sc1\n\t"
        "buffer_load_dwordx2 v[110:111], v245, %[rd], s81 offen offset:2816 sc1\n\t"
        "buffer_load_dwordx2 v[112:113], v245, %[rd], s81 offen offset:3072 sc1\n\t"
        "buffer_load_dwordx2 v[114:115], v245, %[rd], s81 offen offset:3328 sc1\n\t"
        "buffer_load_dwordx2 v[116:117], v245, %[rd], s81 offen offset:3584 sc1\n\t"
        "buffer_load_dwordx2 v[118:119], v245, %[rd], s81 offen offset:3840 sc1\n\t"
        "s_waitcnt vmcnt(0)\n\t"
        "s_cmp_lg_u32 s37, 63\n\t"
        "s_cbranch_scc1 .La23%=\n\t"
        "s_add_u32 s97, s97, 1\n\t"
        "s_cmp_lt_u32 s97, 0x400000\n\t"
        "s_cbranch_scc0 .Lgu%=\n\t"
        "v_max_u32_e32 v120, v96, v97\n\t"
        "v_max3_u32 v120, v120, v98, v99\n\t"
        "v_max3_u32 v120, v120, v100, v101\n\t"
        "v_max3_u32 v120, v120, v102, v103\n\t"
        "v_max3_u32 v120, v120, v104, v105\n\t"
        "v_max3_u32 v120, v120, v106, v107\n\t"
        "v_max3_u32 v120, v120, v108, v109\n\t"
        "v_max3_u32 v120, v120, v110, v111\n\t"
        "v_max3_u32 v120, v120, v112, v113\n\t"
        "v_max3_u32 v120, v120, v114, v115\n\t"
        "v_max3_u32 v120, v120, v116, v117\n\t"
        "v_max3_u32 v120, v120, v118, v119\n\t"
        "v_cmp_eq_u32_e32 vcc, -1, v120\n\t"
        "s_nop 1\n\t"
        "v_mov_b32_e32 v252, v252\n\t"
        "s_nop 0\n\t"
        "s_cbranch_vccnz .La22%=l\n\t"
        ".La23%=:\n\t"
        "ds_write_b128 v248, v[96:99] offset:0\n\t"
        "ds_write_b128 v248, v[100:103] offset:1024\n\t"
        "ds_read_b64_tr_b16 v[120:121], v249 offset:0\n\t"
        "ds_read_b64_tr_b16 v[122:123], v249 offset:64\n\t"
        "ds_read_b64_tr_b16 v[124:125], v249 offset:512\n\t"
        "ds_read_b64_tr_b16 v[126:127], v249 offset:576\n\t"
        "ds_read_b64_tr_b16 v[128:129], v249 offset:1024\n\t"
        "ds_read_b64_tr_b16 v[130:131], v249 offset:1088\n\t"
        "ds_read_b64_tr_b16 v[132:133], v249 offset:1536\n\t"
        "ds_read_b64_tr_b16 v[134:135], v249 offset:1600\n\t"
        "s_waitcnt lgkmcnt(6)\n\t"
        "v_mfma_f32_16x16x32_bf16 v[144:147], v[120:123], v[32:35], 0\n\t"
        "v_mfma_f32_16x16x32_bf16 v[156:159], v[120:123], v[48:51], 0\n\t"
        "s_waitcnt lgkmcnt(4)\n\t"
        "v_mfma_f32_16x16x32_bf16 v[144:147], v[124:127], v[36:39], v[144:147]\n\t"
        "v_mfma_f32_16x16x32_bf16 v[156:159], v[124:127], v[52:55], v[156:159]\n\t"
        "s_waitcnt lgkmcnt(2)\n\t"
        "v_mfma_f32_16x16x32_bf16 v[144:147], v[128:131], v[40:43], v[144:147]\n\t"
        "v_mfma_f32_16x16x32_bf16 v[156:159], v[128:131], v[56:59], v[156:159]\n\t"
        "s_waitcnt lgkmcnt(0)\n\t"
        "v_mfma_f32_16x16x32_bf16 v[144:147], v[132:135], v[44:47], v[144:147]\n\t"
        "v_mfma_f32_16x16x32_bf16 v[156:159], v[132:135], v[60:63], v[156:159]\n\t"
        "s_nop 1\n\t"
        "v_mov_b32_e32 v252, v252\n\t"
        "s_nop 1\n\t"
        "v_mov_b32_e32 v252, v252\n\t"
        "s_nop 0\n\t"
        "v_cvt_pk_bf16_f32 v144, 0, v144\n\t"
        "v_cvt_pk_bf16_f32 v145, 0, v145\n\t"
        "v_cvt_pk_bf16_f32 v146, 0, v146\n\t"
        "v_cvt_pk_bf16_f32 v147, 0, v147\n\t"
        "v_mul_f32_e32 v144, 0x3db504f3, v144\n\t"
        "v_mul_f32_e32 v145, 0x3db504f3, v145\n\t"
        "v_mul_f32_e32 v146, 0x3db504f3, v146\n\t"
        "v_mul_f32_e32 v147, 0x3db504f3, v147\n\t"
        "v_cvt_pk_bf16_f32 v144, 0, v144\n\t"
        "v_cvt_pk_bf16_f32 v145, 0, v145\n\t"
        "v_cvt_pk_bf16_f32 v146, 0, v146\n\t"
        "v_cvt_pk_bf16_f32 v147, 0, v147\n\t"
        "v_cndmask_b32_e64 v144, v144, v254, s[56:57]\n\t"
        "v_cndmask_b32_e64 v145, v145, v254, s[56:57]\n\t"
        "v_cndmask_b32_e64 v146, v146, v254, s[56:57]\n\t"
        "v_cndmask_b32_e64 v147, v147, v254, s[56:57]\n\t"
        "v_max3_f32 v172, v172, v144, v145\n\t"
        "v_max3_f32 v173, v172, v146, v147\n\t"
        "v_max3_f32 v172, v172, v146, v147\n\t"
        "s_nop 1\n\t"
        "v_permlane16_swap_b32_e32 v172, v173\n\t"
        "v_max_f32_e32 v172, v172, v173\n\t"
        "v_mov_b32_e32 v173, v172\n\t"
        "s_nop 1\n\t"
        "v_permlane32_swap_b32_e32 v172, v173\n\t"
        "v_max_f32_e32 v172, v172, v173\n\t"
        "v_sub_f32_e32 v136, v136, v172\n\t"
        "v_sub_f32_e32 v137, v137, v172\n\t"
        "v_sub_f32_e32 v138, v138, v172\n\t"
        "v_sub_f32_e32 v139, v139, v172\n\t"
        "v_sub_f32_e32 v140, v140, v172\n\t"
        "v_sub_f32_e32 v144, v144, v172\n\t"
        "v_sub_f32_e32 v145, v145, v172\n\t"
        "v_sub_f32_e32 v146, v146, v172\n\t"
        "v_sub_f32_e32 v147, v147, v172\n\t"
        "v_mul_f32_e32 v136, 0x3fb8aa3b, v136\n\t"
        "v_mul_f32_e32 v137, 0x3fb8aa3b, v137\n\t"
        "v_mul_f32_e32 v138, 0x3fb8aa3b, v138\n\t"
        "v_mul_f32_e32 v139, 0x3fb8aa3b, v139\n\t"
        "v_mul_f32_e32 v140, 0x3fb8aa3b, v140\n\t"
        "v_mul_f32_e32 v144, 0x3fb8aa3b, v144\n\t"
        "v_mul_f32_e32 v145, 0x3fb8aa3b, v145\n\t"
        "v_mul_f32_e32 v146, 0x3fb8aa3b, v146\n\t"
        "v_mul_f32_e32 v147, 0x3fb8aa3b, v147\n\t"
        "v_exp_f32_e32 v136, v136\n\t"
        "v_exp_f32_e32 v137, v137\n\t"
        "v_exp_f32_e32 v138, v138\n\t"
        "v_exp_f32_e32 v139, v139\n\t"
        "v_exp_f32_e32 v140, v140\n\t"
        "v_exp_f32_e32 v144, v144\n\t"
        "v_exp_f32_e32 v145, v145\n\t"
        "v_exp_f32_e32 v146, v146\n\t"
        "v_exp_f32_e32 v147, v147\n\t"
        "v_mul_f32_e32 v172, 4.0, v140\n\t"
        "v_add_f32_e32 v172, v172, v136\n\t"
        "v_add_f32_e32 v172, v172, v137\n\t"
        "v_add_f32_e32 v172, v172, v138\n\t"
        "v_add_f32_e32 v172, v172, v139\n\t"
        "v_add_f32_e32 v172, v172, v144\n\t"
        "v_add_f32_e32 v172, v172, v145\n\t"
        "v_add_f32_e32 v172, v172, v146\n\t"
        "v_add_f32_e32 v172, v172, v147\n\t"
        "v_mov_b32_e32 v173, v172\n\t"
        "s_nop 1\n\t"
        "v_permlane16_swap_b32_e32 v172, v173\n\t"
        "v_add_f32_e32 v172, v172, v173\n\t"
        "v_mov_b32_e32 v173, v172\n\t"
        "s_nop 1\n\t"
        "v_permlane32_swap_b32_e32 v172, v173\n\t"
        "v_add_f32_e32 v172, v172, v173\n\t"
        "v_rcp_f32_e32 v172, v172\n\t"
        "s_nop 0\n\t"
        "v_mul_f32_e32 v136, v136, v172\n\t"
        "v_mul_f32_e32 v137, v137, v172\n\t"
        "v_mul_f32_e32 v138, v138, v172\n\t"
        "v_mul_f32_e32 v139, v139, v172\n\t"
        "v_mul_f32_e32 v140, v140, v172\n\t"
        "v_mul_f32_e32 v144, v144, v172\n\t"
        "v_mul_f32_e32 v145, v145, v172\n\t"
        "v_mul_f32_e32 v146, v146, v172\n\t"
        "v_mul_f32_e32 v147, v147, v172\n\t"
        "v_cvt_pk_bf16_f32 v160, v136, v137\n\t"
        "v_cvt_pk_bf16_f32 v161, v138, v139\n\t"
        "v_cvt_pk_bf16_f32 v162, v140, v140\n\t"
        "v_cvt_pk_bf16_f32 v163, v140, v140\n\t"
        "v_cvt_pk_bf16_f32 v164, v144, v145\n\t"
        "v_cvt_pk_bf16_f32 v165, v146, v147\n\t"
        "v_mfma_f32_16x16x32_bf16 v[176:179], v[64:67], v[160:163], 0\n\t"
        "v_cvt_pk_bf16_f32 v156, 0, v156\n\t"
        "v_cvt_pk_bf16_f32 v157, 0, v157\n\t"
        "v_cvt_pk_bf16_f32 v158, 0, v158\n\t"
        "v_mfma_f32_16x16x32_bf16 v[180:183], v[68:71], v[160:163], 0\n\t"
        "v_cvt_pk_bf16_f32 v159, 0, v159\n\t"
        "v_mul_f32_e32 v156, 0x3db504f3, v156\n\t"
        "v_mul_f32_e32 v157, 0x3db504f3, v157\n\t"
        "v_mfma_f32_16x16x32_bf16 v[184:187], v[72:75], v[160:163], 0\n\t"
        "v_mul_f32_e32 v158, 0x3db504f3, v158\n\t"
        "v_mul_f32_e32 v159, 0x3db504f3, v159\n\t"
        "v_cvt_pk_bf16_f32 v156, 0, v156\n\t"
        "v_mfma_f32_16x16x32_bf16 v[188:191], v[76:79], v[160:163], 0\n\t"
        "v_cvt_pk_bf16_f32 v157, 0, v157\n\t"
        "v_cvt_pk_bf16_f32 v158, 0, v158\n\t"
        "v_cvt_pk_bf16_f32 v159, 0, v159\n\t"
        "v_mfma_f32_16x16x32_bf16 v[192:195], v[80:83], v[160:163], 0\n\t"
        "v_cndmask_b32_e64 v156, v156, v254, s[56:57]\n\t"
        "v_cndmask_b32_e64 v157, v157, v254, s[56:57]\n\t"
        "v_cndmask_b32_e64 v158, v158, v254, s[56:57]\n\t"
        "v_mfma_f32_16x16x32_bf16 v[196:199], v[84:87], v[160:163], 0\n\t"
        "v_cndmask_b32_e64 v159, v159, v254, s[56:57]\n\t"
        "v_max3_f32 v174, v174, v156, v157\n\t"
        "v_max3_f32 v175, v174, v158, v159\n\t"
        "v_mfma_f32_16x16x32_bf16 v[200:203], v[88:91], v[160:163], 0\n\t"
        "v_max3_f32 v174, v174, v158, v159\n\t"
        "s_nop 1\n\t"
        "v_permlane16_swap_b32_e32 v174, v175\n\t"
        "v_max_f32_e32 v174, v174, v175\n\t"
        "v_mfma_f32_16x16x32_bf16 v[204:207], v[92:95], v[160:163], 0\n\t"
        "v_mov_b32_e32 v175, v174\n\t"
        "s_nop 1\n\t"
        "v_permlane32_swap_b32_e32 v174, v175\n\t"
        "v_max_f32_e32 v174, v174, v175\n\t"
        "v_mfma_f32_16x16x16_bf16 v[176:179], v[104:105], v[164:165], v[176:179]\n\t"
        "v_sub_f32_e32 v148, v148, v174\n\t"
        "v_sub_f32_e32 v149, v149, v174\n\t"
        "v_sub_f32_e32 v150, v150, v174\n\t"
        "v_mfma_f32_16x16x16_bf16 v[180:183], v[106:107], v[164:165], v[180:183]\n\t"
        "v_sub_f32_e32 v151, v151, v174\n\t"
        "v_sub_f32_e32 v152, v152, v174\n\t"
        "v_sub_f32_e32 v153, v153, v174\n\t"
        "v_mfma_f32_16x16x16_bf16 v[184:187], v[108:109], v[164:165], v[184:187]\n\t"
        "v_sub_f32_e32 v154, v154, v174\n\t"
        "v_sub_f32_e32 v155, v155, v174\n\t"
        "v_sub_f32_e32 v156, v156, v174\n\t"
        "v_mfma_f32_16x16x16_bf16 v[188:191], v[110:111], v[164:165], v[188:191]\n\t"
        "v_sub_f32_e32 v157, v157, v174\n\t"
        "v_sub_f32_e32 v158, v158, v174\n\t"
        "v_sub_f32_e32 v159, v159, v174\n\t"
        "v_mfma_f32_16x16x16_bf16 v[192:195], v[112:113], v[164:165], v[192:195]\n\t"
        "v_mul_f32_e32 v148, 0x3fb8aa3b, v148\n\t"
        "v_mul_f32_e32 v149, 0x3fb8aa3b, v149\n\t"
        "v_mul_f32_e32 v150, 0x3fb8aa3b, v150\n\t"
        "v_mfma_f32_16x16x16_bf16 v[196:199], v[114:115], v[164:165], v[196:199]\n\t"
        "v_mul_f32_e32 v151, 0x3fb8aa3b, v151\n\t"
        "v_mul_f32_e32 v152, 0x3fb8aa3b, v152\n\t"
        "v_mul_f32_e32 v153, 0x3fb8aa3b, v153\n\t"
        "v_mfma_f32_16x16x16_bf16 v[200:203], v[116:117], v[164:165], v[200:203]\n\t"
        "v_mul_f32_e32 v154, 0x3fb8aa3b, v154\n\t"
        "v_mul_f32_e32 v155, 0x3fb8aa3b, v155\n\t"
        "v_mul_f32_e32 v156, 0x3fb8aa3b, v156\n\t"
        "v_mfma_f32_16x16x16_bf16 v[204:207], v[118:119], v[164:165], v[204:207]\n\t"
        "v_mul_f32_e32 v157, 0x3fb8aa3b, v157\n\t"
        "v_mul_f32_e32 v158, 0x3fb8aa3b, v158\n\t"
        "v_mul_f32_e32 v159, 0x3fb8aa3b, v159\n\t"
        "v_exp_f32_e32 v148, v148\n\t"
        "v_exp_f32_e32 v149, v149\n\t"
        "v_exp_f32_e32 v150, v150\n\t"
        "v_exp_f32_e32 v151, v151\n\t"
        "v_exp_f32_e32 v152, v152\n\t"
        "v_exp_f32_e32 v153, v153\n\t"
        "v_exp_f32_e32 v154, v154\n\t"
        "v_exp_f32_e32 v155, v155\n\t"
        "v_exp_f32_e32 v156, v156\n\t"
        "v_exp_f32_e32 v157, v157\n\t"
        "v_exp_f32_e32 v158, v158\n\t"
        "v_exp_f32_e32 v159, v159\n\t"
        "v_add_f32_e32 v174, v148, v149\n\t"
        "v_add_f32_e32 v174, v174, v150\n\t"
        "v_add_f32_e32 v174, v174, v151\n\t"
        "v_add_f32_e32 v174, v174, v152\n\t"
        "v_add_f32_e32 v174, v174, v153\n\t"
        "v_add_f32_e32 v174, v174, v154\n\t"
        "v_add_f32_e32 v174, v174, v155\n\t"
        "v_add_f32_e32 v174, v174, v156\n\t"
        "v_add_f32_e32 v174, v174, v157\n\t"
        "v_add_f32_e32 v174, v174, v158\n\t"
        "v_add_f32_e32 v174, v174, v159\n\t"
        "v_mov_b32_e32 v175, v174\n\t"
        "s_nop 1\n\t"
        "v_permlane16_swap_b32_e32 v174, v175\n\t"
        "v_add_f32_e32 v174, v174, v175\n\t"
        "v_mov_b32_e32 v175, v174\n\t"
        "s_nop 1\n\t"
        "v_permlane32_swap_b32_e32 v174, v175\n\t"
        "v_add_f32_e32 v174, v174, v175\n\t"
        "v_rcp_f32_e32 v174, v174\n\t"
        "s_nop 0\n\t"
        "v_mul_f32_e32 v148, v148, v174\n\t"
        "v_mul_f32_e32 v149, v149, v174\n\t"
        "v_mul_f32_e32 v150, v150, v174\n\t"
        "v_mul_f32_e32 v151, v151, v174\n\t"
        "v_mul_f32_e32 v152, v152, v174\n\t"
        "v_mul_f32_e32 v153, v153, v174\n\t"
        "v_mul_f32_e32 v154, v154, v174\n\t"
        "v_mul_f32_e32 v155, v155, v174\n\t"
        "v_mul_f32_e32 v156, v156, v174\n\t"
        "v_mul_f32_e32 v157, v157, v174\n\t"
        "v_mul_f32_e32 v158, v158, v174\n\t"
        "v_mul_f32_e32 v159, v159, v174\n\t"
        "v_cvt_pk_bf16_f32 v166, v148, v149\n\t"
        "v_cvt_pk_bf16_f32 v167, v150, v151\n\t"
        "v_cvt_pk_bf16_f32 v168, v152, v153\n\t"
        "v_cvt_pk_bf16_f32 v169, v154, v155\n\t"
        "v_cvt_pk_bf16_f32 v170, v156, v157\n\t"
        "v_cvt_pk_bf16_f32 v171, v158, v159\n\t"
        "s_waitcnt vmcnt(38)\n\t"
        "s_lshl_b32 s80, s44, 7\n\t"
        "s_cmp_lt_i32 s44, 0\n\t"
        "s_cselect_b32 s80, s36, s80\n\t"
        "v_mov_b32_e32 v242, 1\n\t"
        "buffer_atomic_add v242, v250, %[rd], s80 offen sc0 sc1\n\t"
        "s_lshl_b32 s86, s37, 18\n\t"
        "s_add_u32 s86, s86, %[qx]\n\t"
        "s_add_u32 s87, s86, 0x1000\n\t"
        "v_mfma_f32_16x16x32_bf16 v[208:211], v[64:67], v[166:169], 0\n\t"
        "v_cvt_pk_bf16_f32 v176, v176, v177\n\t"
        "v_cvt_pk_bf16_f32 v177, v178, v179\n\t"
        "v_mfma_f32_16x16x32_bf16 v[212:215], v[68:71], v[166:169], 0\n\t"
        "v_cvt_pk_bf16_f32 v178, v180, v181\n\t"
        "v_cvt_pk_bf16_f32 v179, v182, v183\n\t"
        "v_mfma_f32_16x16x32_bf16 v[216:219], v[72:75], v[166:169], 0\n\t"
        "buffer_store_dwordx4 v[176:179], v244, %[rd], s86 offen offset:0 sc1\n\t"
        "v_cvt_pk_bf16_f32 v184, v184, v185\n\t"
        "v_mfma_f32_16x16x32_bf16 v[220:223], v[76:79], v[166:169], 0\n\t"
        "v_cvt_pk_bf16_f32 v185, v186, v187\n\t"
        "v_cvt_pk_bf16_f32 v186, v188, v189\n\t"
        "v_mfma_f32_16x16x32_bf16 v[224:227], v[80:83], v[166:169], 0\n\t"
        "v_cvt_pk_bf16_f32 v187, v190, v191\n\t"
        "buffer_store_dwordx4 v[184:187], v244, %[rd], s86 offen offset:1024 sc1\n\t"
        "v_mfma_f32_16x16x32_bf16 v[228:231], v[84:87], v[166:169], 0\n\t"
        "v_cvt_pk_bf16_f32 v192, v192, v193\n\t"
        "v_cvt_pk_bf16_f32 v193, v194, v195\n\t"
        "v_mfma_f32_16x16x32_bf16 v[232:235], v[88:91], v[166:169], 0\n\t"
        "v_cvt_pk_bf16_f32 v194, v196, v197\n\t"
        "v_cvt_pk_bf16_f32 v195, v198, v199\n\t"
        "v_mfma_f32_16x16x32_bf16 v[236:239], v[92:95], v[166:169], 0\n\t"
        "buffer_store_dwordx4 v[192:195], v244, %[rd], s86 offen offset:2048 sc1\n\t"
        "v_cvt_pk_bf16_f32 v200, v200, v201\n\t"
        "v_mfma_f32_16x16x16_bf16 v[208:211], v[104:105], v[170:171], v[208:211]\n\t"
        "v_cvt_pk_bf16_f32 v201, v202, v203\n\t"
        "v_cvt_pk_bf16_f32 v202, v204, v205\n\t"
        "v_mfma_f32_16x16x16_bf16 v[212:215], v[106:107], v[170:171], v[212:215]\n\t"
        "v_cvt_pk_bf16_f32 v203, v206, v207\n\t"
        "buffer_store_dwordx4 v[200:203], v244, %[rd], s86 offen offset:3072 sc1\n\t"
        "v_mfma_f32_16x16x16_bf16 v[216:219], v[108:109], v[170:171], v[216:219]\n\t"
        "v_mfma_f32_16x16x16_bf16 v[220:223], v[110:111], v[170:171], v[220:223]\n\t"
        "v_mfma_f32_16x16x16_bf16 v[224:227], v[112:113], v[170:171], v[224:227]\n\t"
        "v_mfma_f32_16x16x16_bf16 v[228:231], v[114:115], v[170:171], v[228:231]\n\t"
        "v_mfma_f32_16x16x16_bf16 v[232:235], v[116:117], v[170:171], v[232:235]\n\t"
        "v_mfma_f32_16x16x16_bf16 v[236:239], v[118:119], v[170:171], v[236:239]\n\t"
        "v_cvt_pk_bf16_f32 v208, v208, v209\n\t"
        "v_cvt_pk_bf16_f32 v209, v210, v211\n\t"
        "v_cvt_pk_bf16_f32 v210, v212, v213\n\t"
        "v_cvt_pk_bf16_f32 v211, v214, v215\n\t"
        "buffer_store_dwordx4 v[208:211], v244, %[rd], s87 offen offset:0 sc1\n\t"
        "v_cvt_pk_bf16_f32 v216, v216, v217\n\t"
        "v_cvt_pk_bf16_f32 v217, v218, v219\n\t"
        "v_cvt_pk_bf16_f32 v218, v220, v221\n\t"
        "v_cvt_pk_bf16_f32 v219, v222, v223\n\t"
        "buffer_store_dwordx4 v[216:219], v244, %[rd], s87 offen offset:1024 sc1\n\t"
        "v_cvt_pk_bf16_f32 v224, v224, v225\n\t"
        "v_cvt_pk_bf16_f32 v225, v226, v227\n\t"
        "v_cvt_pk_bf16_f32 v226, v228, v229\n\t"
        "v_cvt_pk_bf16_f32 v227, v230, v231\n\t"
        "buffer_store_dwordx4 v[224:227], v244, %[rd], s87 offen offset:2048 sc1\n\t"
        "v_cvt_pk_bf16_f32 v232, v232, v233\n\t"
        "v_cvt_pk_bf16_f32 v233, v234, v235\n\t"
        "v_cvt_pk_bf16_f32 v234, v236, v237\n\t"
        "v_cvt_pk_bf16_f32 v235, v238, v239\n\t"
        "buffer_store_dwordx4 v[232:235], v244, %[rd], s87 offen offset:3072 sc1\n\t"
        "s_mov_b32 s45, s44\n\t"
        "s_mov_b32 s44, s37\n\t"
        "s_add_u32 s80, s37, 4\n\t"
        "s_cmp_gt_u32 s40, s80\n\t"
        "s_cselect_b32 s80, s46, 1\n\t"
        "s_cmp_eq_u32 s80, 0\n\t"
        "s_cbranch_scc1 .La24%=\n\t"
        "s_waitcnt vmcnt(0)\n\t"
        "v_readlane_b32 s84, v242, 0\n\t"
        "v_readlane_b32 s85, v242, 32\n\t"
        "v_mov_b32_e32 v243, 32\n\t"
        "s_and_b32 s80, s45, 3\n\t"
        "s_lshl_b32 s80, s80, 12\n\t"
        "s_lshr_b32 s81, s45, 2\n\t"
        "s_lshl_b32 s81, s81, 3\n\t"
        "s_add_u32 s80, s80, s81\n\t"
        "s_lshr_b32 s82, s45, 1\n\t"
        "s_xnor_b32 s81, s45, s82\n\t"
        "s_and_b32 s81, s81, 1\n\t"
        "s_lshl_b32 s81, s81, 13\n\t"
        "s_lshl_b32 s82, s82, 2\n\t"
        "s_add_u32 s81, s81, s82\n\t"
        "s_cmp_lt_i32 s45, 60\n\t"
        "s_cselect_b32 s80, s80, s81\n\t"
        "s_cselect_b32 s81, 4, 0x1000\n\t"
        "s_add_u32 s81, s81, s80\n\t"
        "s_cmp_eq_u32 s84, 31\n\t"
        "s_cselect_b32 s80, s80, s36\n\t"
        "s_cmp_eq_u32 s85, 31\n\t"
        "s_cselect_b32 s81, s81, s36\n\t"
        "s_cmp_lt_i32 s45, 0\n\t"
        "s_cselect_b32 s80, s36, s80\n\t"
        "s_cselect_b32 s81, s36, s81\n\t"
        "buffer_store_dword v243, v251, %[rd], s80 offen sc1\n\t"
        "buffer_store_dword v243, v251, %[rd], s81 offen sc1\n\t"
        "s_lshl_b32 s80, s44, 7\n\t"
        "s_cmp_lt_i32 s44, 0\n\t"
        "s_cselect_b32 s80, s36, s80\n\t"
        "v_mov_b32_e32 v242, 1\n\t"
        "buffer_atomic_add v242, v250, %[rd], s80 offen sc0 sc1\n\t"
        "s_waitcnt vmcnt(0)\n\t"
        "v_readlane_b32 s84, v242, 0\n\t"
        "v_readlane_b32 s85, v242, 32\n\t"
        "v_mov_b32_e32 v243, 32\n\t"
        "s_and_b32 s80, s44, 3\n\t"
        "s_lshl_b32 s80, s80, 12\n\t"
        "s_lshr_b32 s81, s44, 2\n\t"
        "s_lshl_b32 s81, s81, 3\n\t"
        "s_add_u32 s80, s80, s81\n\t"
        "s_lshr_b32 s82, s44, 1\n\t"
        "s_xnor_b32 s81, s44, s82\n\t"
        "s_and_b32 s81, s81, 1\n\t"
        "s_lshl_b32 s81, s81, 13\n\t"
        "s_lshl_b32 s82, s82, 2\n\t"
        "s_add_u32 s81, s81, s82\n\t"
        "s_cmp_lt_i32 s44, 60\n\t"
        "s_cselect_b32 s80, s80, s81\n\t"
        "s_cselect_b32 s81, 4, 0x1000\n\t"
        "s_add_u32 s81, s81, s80\n\t"
        "s_cmp_eq_u32 s84, 31\n\t"
        "s_cselect_b32 s80, s80, s36\n\t"
        "s_cmp_eq_u32 s85, 31\n\t"
        "s_cselect_b32 s81, s81, s36\n\t"
        "s_cmp_lt_i32 s44, 0\n\t"
        "s_cselect_b32 s80, s36, s80\n\t"
        "s_cselect_b32 s81, s36, s81\n\t"
        "buffer_store_dword v243, v251, %[rd], s80 offen sc1\n\t"
        "buffer_store_dword v243, v251, %[rd], s81 offen sc1\n\t"
        "s_mov_b32 s44, -1\n\t"
        "s_mov_b32 s45, -1\n\t"
        ".La24%=:\n\t"
        "s_mov_b32 s38, s46\n\t"
        "s_mov_b32 s39, s47\n\t"
        "s_add_u32 s37, s37, 4\n\t"
        "s_cmp_lt_u32 s37, 64\n\t"
        "s_cbranch_scc1 .Ltop%=\n\t"
        "s_endpgm\n\t"
        ".Lgu%=:\n\t"
        "v_mov_b32_e32 v0, 64\n\t"
        "s_mov_b32 s80, %c[ferr]\n\t"
        "buffer_store_dword v0, off, %[rd], s80 sc1\n\t"
        "s_endpgm"
        :: [rd] "s"(rd), [w] "s"(wv), [tag] "s"(tag), [kx] "s"(kx), [qx] "s"(qx), [vx] "s"(vx), [km] "s"(km), [blk] "s"(h),
          [ferr] "i"(F_ERR), [dbg] "i"(W_DBG), [pslp] "i"(P_SLP), [lslp] "i"(L_SLP), [aslp] "i"(AT_SLP), [bslp] "i"(BT_SLP), [efrom] "i"(EARLY_FROM), [smin] "i"(STEP_MIN), "{v[244:251]}"(pc), "{v252}"(dq)
        : "memory", "vcc", "scc", "s36", "s37", "s38", "s39", "s40", "s41", "s42", "s43", "s44", "s45", "s46", "s47", "s48", "s49", "s50", "s51", "s52", "s53", "s54", "s55", "s56", "s57", "s58", "s59", "s80", "s81", "s82", "s83", "s84", "s85", "s86", "s87", "s88", "s89", "s90", "s91", "s92", "s93", "s94", "s95", "s96", "s97", "s98", "s99");
}

constexpr int OP_P = 144;
__device__ __forceinline__ void oproj_block(const Params& p, const int wv, const int nt, const int rho, const int j, const unsigned tag, const unsigned g_t0) {
    const int w = wv, l = otid() & 63, n16 = l & 15, g = l >> 4;
    const v4u rd = mk_srd(p.ws, W_TOTAL), ro = mk_srd(p.out, S * D * 2), rx = mk_srd(p.Aq, S * D * 2);
    const unsigned snd = (unsigned)((w >> 1) ^ nt) << 31, own = snd ^ 0x80000000u;
    const unsigned ep = (nt == 0 && rho == 0 && w == 0) ? F_EP : 0xF0000000u;
    const int n0 = (nt >> 1) * 64 + 4 * n16, row = 4 * w + g;
    const unsigned a_b = W_QX + ((nt & 1) * 16 + w * 4) * 8192 + l * 16, tr = (4 * g + (n16 >> 2)), tc = (n16 & 3) * 8;
    const v16u pc = {a_b, a_b + 4096, F_RDY + (rho * 32 + nt) * 128 + (l & 31) * 4, W_U + (n0 >> 2) * 16384 + row * 8, W_GATE + row * 4, (unsigned)((row * 1024 + n0) * 2),
                     W_KX + ((nt >> 1) * 4 + w) * 2048 + l * 16,
                     (unsigned)(w * 8320 + g * 256 + ((n16 + 4 * g) & 15) * 16), (unsigned)((n16 >> 2) * 1040 + (n16 & 3) * 256 + ((row + 4 * n16) & 15) * 16),
                     (unsigned)(w * 10240 + (l >> 3) * OP_P + (l & 7) * 16), w * 10240 + tr * OP_P + tc, WO_L + w * 16384 + tr * 128 + tc, 0u, 0u, 0u, 0u};
    asm volatile(
        "ds_write_b128 v185, v[192:195] offset:0\n\t"
        "ds_write_b128 v185, v[196:199] offset:1152\n\t"
        "ds_write_b128 v185, v[200:203] offset:2304\n\t"
        "ds_write_b128 v185, v[204:207] offset:3456\n\t"
        "ds_read_b64_tr_b16 a[0:1], v186 offset:0\n\t"
        "ds_read_b64_tr_b16 a[2:3], v186 offset:2304\n\t"
        "ds_read_b64_tr_b16 a[4:5], v186 offset:32\n\t"
        "ds_read_b64_tr_b16 a[6:7], v186 offset:2336\n\t"
        "ds_read_b64_tr_b16 a[8:9], v186 offset:64\n\t"
        "ds_read_b64_tr_b16 a[10:11], v186 offset:2368\n\t"
        "ds_read_b64_tr_b16 a[12:13], v186 offset:96\n\t"
        "ds_read_b64_tr_b16 a[14:15], v186 offset:2400\n\t"
        "ds_write_b128 v185, v[208:211] offset:5120\n\t"
        "ds_write_b128 v185, v[212:215] offset:6272\n\t"
        "ds_write_b128 v185, v[216:219] offset:7424\n\t"
        "ds_write_b128 v185, v[220:223] offset:8576\n\t"
        "ds_read_b64_tr_b16 a[16:17], v186 offset:5120\n\t"
        "ds_read_b64_tr_b16 a[18:19], v186 offset:7424\n\t"
        "ds_read_b64_tr_b16 a[20:21], v186 offset:5152\n\t"
        "ds_read_b64_tr_b16 a[22:23], v186 offset:7456\n\t"
        "ds_read_b64_tr_b16 a[24:25], v186 offset:5184\n\t"
        "ds_read_b64_tr_b16 a[26:27], v186 offset:7488\n\t"
        "ds_read_b64_tr_b16 a[28:29], v186 offset:5216\n\t"
        "ds_read_b64_tr_b16 a[30:31], v186 offset:7520\n\t"
        "ds_write_b128 v185, v[224:227] offset:0\n\t"
        "ds_write_b128 v185, v[228:231] offset:1152\n\t"
        "ds_write_b128 v185, v[232:235] offset:2304\n\t"
        "ds_write_b128 v185, v[236:239] offset:3456\n\t"
        "ds_read_b64_tr_b16 a[32:33], v186 offset:0\n\t"
        "ds_read_b64_tr_b16 a[34:35], v186 offset:2304\n\t"
        "ds_read_b64_tr_b16 a[36:37], v186 offset:32\n\t"
        "ds_read_b64_tr_b16 a[38:39], v186 offset:2336\n\t"
        "ds_read_b64_tr_b16 a[40:41], v186 offset:64\n\t"
        "ds_read_b64_tr_b16 a[42:43], v186 offset:2368\n\t"
        "ds_read_b64_tr_b16 a[44:45], v186 offset:96\n\t"
        "ds_read_b64_tr_b16 a[46:47], v186 offset:2400\n\t"
        "ds_write_b128 v185, v[240:243] offset:5120\n\t"
        "ds_write_b128 v185, v[244:247] offset:6272\n\t"
        "ds_write_b128 v185, v[248:251] offset:7424\n\t"
        "ds_write_b128 v185, v[252:255] offset:8576\n\t"
        "ds_read_b64_tr_b16 a[48:49], v186 offset:5120\n\t"
        "ds_read_b64_tr_b16 a[50:51], v186 offset:7424\n\t"
        "ds_read_b64_tr_b16 a[52:53], v186 offset:5152\n\t"
        "ds_read_b64_tr_b16 a[54:55], v186 offset:7456\n\t"
        "ds_read_b64_tr_b16 a[56:57], v186 offset:5184\n\t"
        "ds_read_b64_tr_b16 a[58:59], v186 offset:7488\n\t"
        "ds_read_b64_tr_b16 a[60:61], v186 offset:5216\n\t"
        "ds_read_b64_tr_b16 a[62:63], v186 offset:7520\n\t"
        "ds_read_b64_tr_b16 a[64:65], v187 offset:0\n\t"
        "ds_read_b64_tr_b16 a[66:67], v187 offset:2048\n\t"
        "ds_read_b64_tr_b16 a[68:69], v187 offset:32\n\t"
        "ds_read_b64_tr_b16 a[70:71], v187 offset:2080\n\t"
        "ds_read_b64_tr_b16 a[72:73], v187 offset:64\n\t"
        "ds_read_b64_tr_b16 a[74:75], v187 offset:2112\n\t"
        "ds_read_b64_tr_b16 a[76:77], v187 offset:96\n\t"
        "ds_read_b64_tr_b16 a[78:79], v187 offset:2144\n\t"
        "ds_read_b64_tr_b16 a[80:81], v187 offset:4096\n\t"
        "ds_read_b64_tr_b16 a[82:83], v187 offset:6144\n\t"
        "ds_read_b64_tr_b16 a[84:85], v187 offset:4128\n\t"
        "ds_read_b64_tr_b16 a[86:87], v187 offset:6176\n\t"
        "ds_read_b64_tr_b16 a[88:89], v187 offset:4160\n\t"
        "ds_read_b64_tr_b16 a[90:91], v187 offset:6208\n\t"
        "ds_read_b64_tr_b16 a[92:93], v187 offset:4192\n\t"
        "ds_read_b64_tr_b16 a[94:95], v187 offset:6240\n\t"
        "ds_read_b64_tr_b16 a[96:97], v187 offset:8192\n\t"
        "ds_read_b64_tr_b16 a[98:99], v187 offset:10240\n\t"
        "ds_read_b64_tr_b16 a[100:101], v187 offset:8224\n\t"
        "ds_read_b64_tr_b16 a[102:103], v187 offset:10272\n\t"
        "ds_read_b64_tr_b16 a[104:105], v187 offset:8256\n\t"
        "ds_read_b64_tr_b16 a[106:107], v187 offset:10304\n\t"
        "ds_read_b64_tr_b16 a[108:109], v187 offset:8288\n\t"
        "ds_read_b64_tr_b16 a[110:111], v187 offset:10336\n\t"
        "ds_read_b64_tr_b16 a[112:113], v187 offset:12288\n\t"
        "ds_read_b64_tr_b16 a[114:115], v187 offset:14336\n\t"
        "ds_read_b64_tr_b16 a[116:117], v187 offset:12320\n\t"
        "ds_read_b64_tr_b16 a[118:119], v187 offset:14368\n\t"
        "ds_read_b64_tr_b16 a[120:121], v187 offset:12352\n\t"
        "ds_read_b64_tr_b16 a[122:123], v187 offset:14400\n\t"
        "ds_read_b64_tr_b16 a[124:125], v187 offset:12384\n\t"
        "ds_read_b64_tr_b16 a[126:127], v187 offset:14432\n\t"
        "ds_write_b128 v185, a[128:131] offset:0\n\t"
        "ds_write_b128 v185, a[132:135] offset:1152\n\t"
        "ds_write_b128 v185, a[136:139] offset:2304\n\t"
        "ds_write_b128 v185, a[140:143] offset:3456\n\t"
        "s_waitcnt lgkmcnt(0)\n\t"
        "ds_read_b64_tr_b16 a[128:129], v186 offset:0\n\t"
        "ds_read_b64_tr_b16 a[130:131], v186 offset:2304\n\t"
        "ds_read_b64_tr_b16 a[132:133], v186 offset:32\n\t"
        "ds_read_b64_tr_b16 a[134:135], v186 offset:2336\n\t"
        "ds_read_b64_tr_b16 a[136:137], v186 offset:64\n\t"
        "ds_read_b64_tr_b16 a[138:139], v186 offset:2368\n\t"
        "ds_read_b64_tr_b16 a[140:141], v186 offset:96\n\t"
        "ds_read_b64_tr_b16 a[142:143], v186 offset:2400\n\t"
        "ds_write_b128 v185, a[144:147] offset:5120\n\t"
        "ds_write_b128 v185, a[148:151] offset:6272\n\t"
        "ds_write_b128 v185, a[152:155] offset:7424\n\t"
        "ds_write_b128 v185, a[156:159] offset:8576\n\t"
        "s_waitcnt lgkmcnt(0)\n\t"
        "ds_read_b64_tr_b16 a[144:145], v186 offset:5120\n\t"
        "ds_read_b64_tr_b16 a[146:147], v186 offset:7424\n\t"
        "ds_read_b64_tr_b16 a[148:149], v186 offset:5152\n\t"
        "ds_read_b64_tr_b16 a[150:151], v186 offset:7456\n\t"
        "ds_read_b64_tr_b16 a[152:153], v186 offset:5184\n\t"
        "ds_read_b64_tr_b16 a[154:155], v186 offset:7488\n\t"
        "ds_read_b64_tr_b16 a[156:157], v186 offset:5216\n\t"
        "ds_read_b64_tr_b16 a[158:159], v186 offset:7520\n\t"
        "ds_write_b128 v185, a[160:163] offset:0\n\t"
        "ds_write_b128 v185, a[164:167] offset:1152\n\t"
        "ds_write_b128 v185, a[168:171] offset:2304\n\t"
        "ds_write_b128 v185, a[172:175] offset:3456\n\t"
        "s_waitcnt lgkmcnt(0)\n\t"
        "ds_read_b64_tr_b16 a[160:161], v186 offset:0\n\t"
        "ds_read_b64_tr_b16 a[162:163], v186 offset:2304\n\t"
        "ds_read_b64_tr_b16 a[164:165], v186 offset:32\n\t"
        "ds_read_b64_tr_b16 a[166:167], v186 offset:2336\n\t"
        "ds_read_b64_tr_b16 a[168:169], v186 offset:64\n\t"
        "ds_read_b64_tr_b16 a[170:171], v186 offset:2368\n\t"
        "ds_read_b64_tr_b16 a[172:173], v186 offset:96\n\t"
        "ds_read_b64_tr_b16 a[174:175], v186 offset:2400\n\t"
        "ds_write_b128 v185, a[176:179] offset:5120\n\t"
        "ds_write_b128 v185, a[180:183] offset:6272\n\t"
        "ds_write_b128 v185, a[184:187] offset:7424\n\t"
        "ds_write_b128 v185, a[188:191] offset:8576\n\t"
        "s_waitcnt lgkmcnt(0)\n\t"
        "ds_read_b64_tr_b16 a[176:177], v186 offset:5120\n\t"
        "ds_read_b64_tr_b16 a[178:179], v186 offset:7424\n\t"
        "ds_read_b64_tr_b16 a[180:181], v186 offset:5152\n\t"
        "ds_read_b64_tr_b16 a[182:183], v186 offset:7456\n\t"
        "ds_read_b64_tr_b16 a[184:185], v186 offset:5184\n\t"
        "ds_read_b64_tr_b16 a[186:187], v186 offset:7488\n\t"
        "ds_read_b64_tr_b16 a[188:189], v186 offset:5216\n\t"
        "ds_read_b64_tr_b16 a[190:191], v186 offset:7520\n\t"
        "ds_write_b128 v185, a[192:195] offset:0\n\t"
        "ds_write_b128 v185, a[196:199] offset:1152\n\t"
        "ds_write_b128 v185, a[200:203] offset:2304\n\t"
        "ds_write_b128 v185, a[204:207] offset:3456\n\t"
        "s_waitcnt lgkmcnt(0)\n\t"
        "ds_read_b64_tr_b16 a[192:193], v186 offset:0\n\t"
        "ds_read_b64_tr_b16 a[194:195], v186 offset:2304\n\t"
        "ds_read_b64_tr_b16 a[196:197], v186 offset:32\n\t"
        "ds_read_b64_tr_b16 a[198:199], v186 offset:2336\n\t"
        "ds_read_b64_tr_b16 a[200:201], v186 offset:64\n\t"
        "ds_read_b64_tr_b16 a[202:203], v186 offset:2368\n\t"
        "ds_read_b64_tr_b16 a[204:205], v186 offset:96\n\t"
        "ds_read_b64_tr_b16 a[206:207], v186 offset:2400\n\t"
        "ds_write_b128 v185, a[208:211] offset:5120\n\t"
        "ds_write_b128 v185, a[212:215] offset:6272\n\t"
        "ds_write_b128 v185, a[216:219] offset:7424\n\t"
        "ds_write_b128 v185, a[220:223] offset:8576\n\t"
        "s_waitcnt lgkmcnt(0)\n\t"
        "ds_read_b64_tr_b16 a[208:209], v186 offset:5120\n\t"
        "ds_read_b64_tr_b16 a[210:211], v186 offset:7424\n\t"
        "ds_read_b64_tr_b16 a[212:213], v186 offset:5152\n\t"
        "ds_read_b64_tr_b16 a[214:215], v186 offset:7456\n\t"
        "ds_read_b64_tr_b16 a[216:217], v186 offset:5184\n\t"
        "ds_read_b64_tr_b16 a[218:219], v186 offset:7488\n\t"
        "ds_read_b64_tr_b16 a[220:221], v186 offset:5216\n\t"
        "ds_read_b64_tr_b16 a[222:223], v186 offset:7520\n\t"
        "ds_write_b128 v185, a[224:227] offset:0\n\t"
        "ds_write_b128 v185, a[228:231] offset:1152\n\t"
        "ds_write_b128 v185, a[232:235] offset:2304\n\t"
        "ds_write_b128 v185, a[236:239] offset:3456\n\t"
        "s_waitcnt lgkmcnt(0)\n\t"
        "ds_read_b64_tr_b16 a[224:225], v186 offset:0\n\t"
        "ds_read_b64_tr_b16 a[226:227], v186 offset:2304\n\t"
        "ds_read_b64_tr_b16 a[228:229], v186 offset:32\n\t"
        "ds_read_b64_tr_b16 a[230:231], v186 offset:2336\n\t"
        "ds_read_b64_tr_b16 a[232:233], v186 offset:64\n\t"
        "ds_read_b64_tr_b16 a[234:235], v186 offset:2368\n\t"
        "ds_read_b64_tr_b16 a[236:237], v186 offset:96\n\t"
        "ds_read_b64_tr_b16 a[238:239], v186 offset:2400\n\t"
        "ds_write_b128 v185, a[240:243] offset:5120\n\t"
        "ds_write_b128 v185, a[244:247] offset:6272\n\t"
        "ds_write_b128 v185, a[248:251] offset:7424\n\t"
        "ds_write_b128 v185, a[252:255] offset:8576\n\t"
        "s_waitcnt lgkmcnt(0)\n\t"
        "ds_read_b64_tr_b16 a[240:241], v186 offset:5120\n\t"
        "ds_read_b64_tr_b16 a[242:243], v186 offset:7424\n\t"
        "ds_read_b64_tr_b16 a[244:245], v186 offset:5152\n\t"
        "ds_read_b64_tr_b16 a[246:247], v186 offset:7456\n\t"
        "ds_read_b64_tr_b16 a[248:249], v186 offset:5184\n\t"
        "ds_read_b64_tr_b16 a[250:251], v186 offset:7488\n\t"
        "ds_read_b64_tr_b16 a[252:253], v186 offset:5216\n\t"
        "ds_read_b64_tr_b16 a[254:255], v186 offset:7520\n\t"
        "s_waitcnt lgkmcnt(0)\n\t"
        "v_mov_b32_e32 v240, v176\n\t"
        "v_mov_b32_e32 v241, v177\n\t"
        "v_mov_b32_e32 v242, v178\n\t"
        "v_mov_b32_e32 v243, v179\n\t"
        "v_mov_b32_e32 v244, v180\n\t"
        "v_mov_b32_e32 v245, v181\n\t"
        "v_mov_b32_e32 v246, v182\n\t"
        "v_mov_b32_e32 v247, v183\n\t"
        "v_mov_b32_e32 v248, v184\n\t"
        "v_mov_b32_e32 v205, %[tag]\n\t"
        "v_mov_b32_e32 v207, %[tag]\n\t"
        "v_mov_b32_e32 v209, %[tag]\n\t"
        "v_mov_b32_e32 v211, %[tag]\n\t"
        "v_mov_b32_e32 v213, %[tag]\n\t"
        "v_mov_b32_e32 v215, %[tag]\n\t"
        "v_mov_b32_e32 v217, %[tag]\n\t"
        "v_mov_b32_e32 v219, %[tag]\n\t"
        "s_mov_b32 s36, 0xf0000000\n\t"
        "s_mov_b32 s37, 0\n\t"
        "s_mov_b32 s38, -1\n\t"
        "s_mov_b32 s39, -1\n\t"
        "s_mov_b32 s40, 0\n\t"
        "s_mov_b32 s43, 0\n\t"
        "s_lshl_b32 s45, %[w], 10\n\t"
        "s_add_u32 s45, s45, 69632\n\t"
        "s_lshl_b32 s44, %[rho], 1\n\t"
        "s_mov_b64 exec, 0\n\t"
        "s_branch .Lhk0%=\n\t"
        ".Lhr%=:\n\t"
        "s_mov_b64 exec, -1\n\t"
        "s_barrier\n\t"
        "s_mov_b32 s97, 0\n\t"
        "v_and_b32_e32 v186, 0x7c, v242\n\t"
        "s_mov_b32 s80, %c[fprep]\n\t"
        "s_mov_b32 s81, %c[fconv]\n\t"
        ".Lo1%=:\n\t"
        "buffer_load_dword v187, v186, %[rd], s80 offen sc1\n\t"
        "s_add_u32 s97, s97, 1\n\t"
        "s_cmp_lt_u32 s97, 0x200000\n\t"
        "s_cbranch_scc0 .Lgu%=\n\t"
        "s_waitcnt vmcnt(0)\n\t"
        "v_cmp_ne_u32_e32 vcc, %[tag], v187\n\t"
        "s_nop 3\n\t"
        "s_cbranch_vccz .Lo2%=\n\t"
        "s_sleep 8\n\t"
        "s_branch .Lo1%=\n\t"
        ".Lo2%=:\n\t"
        "buffer_load_dword v187, off, %[rd], s81 sc1\n\t"
        "s_add_u32 s97, s97, 1\n\t"
        "s_cmp_lt_u32 s97, 0x200000\n\t"
        "s_cbranch_scc0 .Lgu%=\n\t"
        "s_waitcnt vmcnt(0)\n\t"
        "v_cmp_gt_u32_e32 vcc, 0x100, v187\n\t"
        "s_nop 3\n\t"
        "s_cbranch_vccz .Lo3%=\n\t"
        "s_sleep 8\n\t"
        "s_branch .Lo2%=\n\t"
        ".Lo3%=:\n\t"
        ".Lnew%=:\n\t"
        "s_lshl_b32 s85, s37, 1\n\t"
        "s_and_b32 s80, s40, 0x3fffffff\n\t"
        "s_lshr_b32 s80, s80, s85\n\t"
        "s_andn2_b32 s80, 3, s80\n\t"
        "s_cbranch_scc0 .Lgo%=\n\t"
        "s_cmp_lt_i32 s39, 0\n\t"
        "s_cbranch_scc1 .Lo4%=\n\t"
        "s_waitcnt vmcnt(0)\n\t"
        "s_add_i32 s80, s39, 0\n\t"
        "s_lshl_b32 s80, s80, 15\n\t"
        "s_or_b32 s81, s39, %[snd]\n\t"
        "s_cmp_lt_i32 s81, 0\n\t"
        "s_cselect_b32 s83, s36, s80\n\t"
        "s_cbranch_scc1 .Lo7%=\n\t"
        "s_mov_b32 s97, 0\n\t"
        ".Lo5%=:\n\t"
        "v_xor_b32_e32 v186, %[tag], v161\n\t"
        "v_xor_b32_e32 v187, %[tag], v163\n\t"
        "v_xor_b32_e32 v188, %[tag], v165\n\t"
        "v_or3_b32 v186, v186, v187, v188\n\t"
        "v_xor_b32_e32 v187, %[tag], v167\n\t"
        "v_or_b32_e32 v186, v186, v187\n\t"
        "v_cmp_ne_u32_e32 vcc, 0, v186\n\t"
        "v_sub_f32_e32 v198, 1.0, v184\n\t"
        "v_add_f32_e32 v190, v152, v160\n\t"
        "v_add_f32_e32 v191, v153, v162\n\t"
        "v_add_f32_e32 v192, v154, v164\n\t"
        "v_add_f32_e32 v193, v155, v166\n\t"
        "s_cbranch_vccz .Lo6%=\n\t"
        "s_sleep %c[xslp]\n\t"
        "s_lshl_b32 s82, s83, 2\n\t"
        "buffer_load_dwordx4 v[160:163], v246, %[rd], s82 offen offset:0 sc1\n\t"
        "buffer_load_dwordx4 v[164:167], v246, %[rd], s82 offen offset:1024 sc1\n\t"
        "s_add_u32 s97, s97, 1\n\t"
        "s_cmp_lt_u32 s97, 0x200000\n\t"
        "s_cbranch_scc0 .Lgu%=\n\t"
        "s_waitcnt vmcnt(0)\n\t"
        "s_branch .Lo5%=\n\t"
        ".Lo6%=:\n\t"
        "v_cvt_pk_bf16_f32 v198, 0, v198\n\t"
        "v_cvt_pk_bf16_f32 v190, 0, v190\n\t"
        "v_cvt_pk_bf16_f32 v191, 0, v191\n\t"
        "v_cvt_pk_bf16_f32 v192, 0, v192\n\t"
        "v_cvt_pk_bf16_f32 v193, 0, v193\n\t"
        "v_lshlrev_b32_e32 v186, 16, v180\n\t"
        "v_lshlrev_b32_e32 v194, 16, v176\n\t"
        "v_and_b32_e32 v187, 0xffff0000, v180\n\t"
        "v_and_b32_e32 v195, 0xffff0000, v176\n\t"
        "v_lshlrev_b32_e32 v188, 16, v181\n\t"
        "v_lshlrev_b32_e32 v196, 16, v177\n\t"
        "v_and_b32_e32 v189, 0xffff0000, v181\n\t"
        "v_and_b32_e32 v197, 0xffff0000, v177\n\t"
        "v_mul_f32_e32 v186, v186, v194\n\t"
        "v_mul_f32_e32 v187, v187, v195\n\t"
        "v_mul_f32_e32 v188, v188, v196\n\t"
        "v_mul_f32_e32 v189, v189, v197\n\t"
        "v_mul_f32_e32 v190, v198, v190\n\t"
        "v_mul_f32_e32 v191, v198, v191\n\t"
        "v_mul_f32_e32 v192, v198, v192\n\t"
        "v_mul_f32_e32 v193, v198, v193\n\t"
        "v_cvt_pk_bf16_f32 v186, 0, v186\n\t"
        "v_cvt_pk_bf16_f32 v187, 0, v187\n\t"
        "v_cvt_pk_bf16_f32 v188, 0, v188\n\t"
        "v_cvt_pk_bf16_f32 v189, 0, v189\n\t"
        "v_cvt_pk_bf16_f32 v190, 0, v190\n\t"
        "v_cvt_pk_bf16_f32 v191, 0, v191\n\t"
        "v_cvt_pk_bf16_f32 v192, 0, v192\n\t"
        "v_cvt_pk_bf16_f32 v193, 0, v193\n\t"
        "v_mul_f32_e32 v186, v184, v186\n\t"
        "v_mul_f32_e32 v187, v184, v187\n\t"
        "v_mul_f32_e32 v188, v184, v188\n\t"
        "v_mul_f32_e32 v189, v184, v189\n\t"
        "v_cvt_pk_bf16_f32 v186, 0, v186\n\t"
        "v_cvt_pk_bf16_f32 v187, 0, v187\n\t"
        "v_cvt_pk_bf16_f32 v188, 0, v188\n\t"
        "v_cvt_pk_bf16_f32 v189, 0, v189\n\t"
        "v_add_f32_e32 v186, v186, v190\n\t"
        "v_add_f32_e32 v187, v187, v191\n\t"
        "v_add_f32_e32 v188, v188, v192\n\t"
        "v_add_f32_e32 v189, v189, v193\n\t"
        "v_cvt_pk_bf16_f32 v200, v186, v187\n\t"
        "v_cvt_pk_bf16_f32 v201, v188, v189\n\t"
        ".Lo7%=:\n\t"
        "buffer_store_dwordx2 v[200:201], v245, %[ro], s83 offen\n\t"
        "s_add_i32 s80, s39, 1\n\t"
        "s_lshl_b32 s80, s80, 15\n\t"
        "s_or_b32 s81, s39, %[snd]\n\t"
        "s_cmp_lt_i32 s81, 0\n\t"
        "s_cselect_b32 s83, s36, s80\n\t"
        "s_cbranch_scc1 .Lo10%=\n\t"
        "s_mov_b32 s97, 0\n\t"
        ".Lo8%=:\n\t"
        "v_xor_b32_e32 v186, %[tag], v169\n\t"
        "v_xor_b32_e32 v187, %[tag], v171\n\t"
        "v_xor_b32_e32 v188, %[tag], v173\n\t"
        "v_or3_b32 v186, v186, v187, v188\n\t"
        "v_xor_b32_e32 v187, %[tag], v175\n\t"
        "v_or_b32_e32 v186, v186, v187\n\t"
        "v_cmp_ne_u32_e32 vcc, 0, v186\n\t"
        "v_sub_f32_e32 v198, 1.0, v185\n\t"
        "v_add_f32_e32 v190, v156, v168\n\t"
        "v_add_f32_e32 v191, v157, v170\n\t"
        "v_add_f32_e32 v192, v158, v172\n\t"
        "v_add_f32_e32 v193, v159, v174\n\t"
        "s_cbranch_vccz .Lo9%=\n\t"
        "s_sleep %c[xslp]\n\t"
        "s_lshl_b32 s82, s83, 2\n\t"
        "buffer_load_dwordx4 v[168:171], v246, %[rd], s82 offen offset:0 sc1\n\t"
        "buffer_load_dwordx4 v[172:175], v246, %[rd], s82 offen offset:1024 sc1\n\t"
        "s_add_u32 s97, s97, 1\n\t"
        "s_cmp_lt_u32 s97, 0x200000\n\t"
        "s_cbranch_scc0 .Lgu%=\n\t"
        "s_waitcnt vmcnt(0)\n\t"
        "s_branch .Lo8%=\n\t"
        ".Lo9%=:\n\t"
        "v_cvt_pk_bf16_f32 v198, 0, v198\n\t"
        "v_cvt_pk_bf16_f32 v190, 0, v190\n\t"
        "v_cvt_pk_bf16_f32 v191, 0, v191\n\t"
        "v_cvt_pk_bf16_f32 v192, 0, v192\n\t"
        "v_cvt_pk_bf16_f32 v193, 0, v193\n\t"
        "v_lshlrev_b32_e32 v186, 16, v182\n\t"
        "v_lshlrev_b32_e32 v194, 16, v178\n\t"
        "v_and_b32_e32 v187, 0xffff0000, v182\n\t"
        "v_and_b32_e32 v195, 0xffff0000, v178\n\t"
        "v_lshlrev_b32_e32 v188, 16, v183\n\t"
        "v_lshlrev_b32_e32 v196, 16, v179\n\t"
        "v_and_b32_e32 v189, 0xffff0000, v183\n\t"
        "v_and_b32_e32 v197, 0xffff0000, v179\n\t"
        "v_mul_f32_e32 v186, v186, v194\n\t"
        "v_mul_f32_e32 v187, v187, v195\n\t"
        "v_mul_f32_e32 v188, v188, v196\n\t"
        "v_mul_f32_e32 v189, v189, v197\n\t"
        "v_mul_f32_e32 v190, v198, v190\n\t"
        "v_mul_f32_e32 v191, v198, v191\n\t"
        "v_mul_f32_e32 v192, v198, v192\n\t"
        "v_mul_f32_e32 v193, v198, v193\n\t"
        "v_cvt_pk_bf16_f32 v186, 0, v186\n\t"
        "v_cvt_pk_bf16_f32 v187, 0, v187\n\t"
        "v_cvt_pk_bf16_f32 v188, 0, v188\n\t"
        "v_cvt_pk_bf16_f32 v189, 0, v189\n\t"
        "v_cvt_pk_bf16_f32 v190, 0, v190\n\t"
        "v_cvt_pk_bf16_f32 v191, 0, v191\n\t"
        "v_cvt_pk_bf16_f32 v192, 0, v192\n\t"
        "v_cvt_pk_bf16_f32 v193, 0, v193\n\t"
        "v_mul_f32_e32 v186, v185, v186\n\t"
        "v_mul_f32_e32 v187, v185, v187\n\t"
        "v_mul_f32_e32 v188, v185, v188\n\t"
        "v_mul_f32_e32 v189, v185, v189\n\t"
        "v_cvt_pk_bf16_f32 v186, 0, v186\n\t"
        "v_cvt_pk_bf16_f32 v187, 0, v187\n\t"
        "v_cvt_pk_bf16_f32 v188, 0, v188\n\t"
        "v_cvt_pk_bf16_f32 v189, 0, v189\n\t"
        "v_add_f32_e32 v186, v186, v190\n\t"
        "v_add_f32_e32 v187, v187, v191\n\t"
        "v_add_f32_e32 v188, v188, v192\n\t"
        "v_add_f32_e32 v189, v189, v193\n\t"
        "v_cvt_pk_bf16_f32 v200, v186, v187\n\t"
        "v_cvt_pk_bf16_f32 v201, v188, v189\n\t"
        ".Lo10%=:\n\t"
        "buffer_store_dwordx2 v[200:201], v245, %[ro], s83 offen\n\t"
        "s_mov_b32 s39, -1\n\t"
        ".Lo4%=:\n\t"
        "s_cmp_eq_u32 s37, 15\n\t"
        "s_cbranch_scc1 .Lsol%=\n\t"
        "s_mov_b32 s97, 0\n\t"
        ".Lo11%=:\n\t"
        "s_lshr_b32 s80, s40, s85\n\t"
        "s_andn2_b32 s80, 3, s80\n\t"
        "s_cbranch_scc0 .Lo12%=\n\t"
        "s_cmp_eq_u32 s97, 0\n\t"
        "s_cbranch_scc1 .Lo11%=x\n\t"
        "s_sleep %c[oslp]\n\t"
        ".Lo11%=x:\n\t"
        "buffer_load_dword v220, v242, %[rd], 0 offen sc1\n\t"
        "s_add_u32 s97, s97, 1\n\t"
        "s_cmp_lt_u32 s97, 0x200000\n\t"
        "s_cbranch_scc0 .Lgu%=\n\t"
        "s_waitcnt vmcnt(0)\n\t"
        "v_cmp_eq_u32_e32 vcc, 32, v220\n\t"
        "s_nop 1\n\t"
        "s_or_b32 s40, s40, vcc_lo\n\t"
        "s_branch .Lo11%=\n\t"
        ".Lo12%=:\n\t"
        ".Lgo%=:\n\t"
        "s_lshl_b32 s84, s44, 17\n\t"
        "buffer_load_dwordx4 v[0:3], v240, %[rd], s84 offen offset:0 sc1\n\t"
        "buffer_load_dwordx4 v[4:7], v240, %[rd], s84 offen offset:1024 sc1\n\t"
        "buffer_load_dwordx4 v[8:11], v240, %[rd], s84 offen offset:2048 sc1\n\t"
        "buffer_load_dwordx4 v[12:15], v240, %[rd], s84 offen offset:3072 sc1\n\t"
        "s_add_u32 s84, s84, 8192\n\t"
        "buffer_load_dwordx4 v[16:19], v240, %[rd], s84 offen offset:0 sc1\n\t"
        "buffer_load_dwordx4 v[20:23], v240, %[rd], s84 offen offset:1024 sc1\n\t"
        "buffer_load_dwordx4 v[24:27], v240, %[rd], s84 offen offset:2048 sc1\n\t"
        "buffer_load_dwordx4 v[28:31], v240, %[rd], s84 offen offset:3072 sc1\n\t"
        "s_add_u32 s84, s84, 8192\n\t"
        "buffer_load_dwordx4 v[32:35], v240, %[rd], s84 offen offset:0 sc1\n\t"
        "buffer_load_dwordx4 v[36:39], v240, %[rd], s84 offen offset:1024 sc1\n\t"
        "buffer_load_dwordx4 v[40:43], v240, %[rd], s84 offen offset:2048 sc1\n\t"
        "buffer_load_dwordx4 v[44:47], v240, %[rd], s84 offen offset:3072 sc1\n\t"
        "s_add_u32 s84, s84, 8192\n\t"
        "buffer_load_dwordx4 v[48:51], v240, %[rd], s84 offen offset:0 sc1\n\t"
        "buffer_load_dwordx4 v[52:55], v240, %[rd], s84 offen offset:1024 sc1\n\t"
        "buffer_load_dwordx4 v[56:59], v240, %[rd], s84 offen offset:2048 sc1\n\t"
        "buffer_load_dwordx4 v[60:63], v240, %[rd], s84 offen offset:3072 sc1\n\t"
        "s_sub_u32 s84, s84, 20480\n\t"
        "buffer_load_dwordx4 v[64:67], v240, %[rd], s84 offen offset:0 sc1\n\t"
        "buffer_load_dwordx4 v[68:71], v240, %[rd], s84 offen offset:1024 sc1\n\t"
        "buffer_load_dwordx4 v[72:75], v240, %[rd], s84 offen offset:2048 sc1\n\t"
        "buffer_load_dwordx4 v[76:79], v240, %[rd], s84 offen offset:3072 sc1\n\t"
        "s_add_u32 s84, s84, 8192\n\t"
        "buffer_load_dwordx4 v[80:83], v240, %[rd], s84 offen offset:0 sc1\n\t"
        "buffer_load_dwordx4 v[84:87], v240, %[rd], s84 offen offset:1024 sc1\n\t"
        "buffer_load_dwordx4 v[88:91], v240, %[rd], s84 offen offset:2048 sc1\n\t"
        "buffer_load_dwordx4 v[92:95], v240, %[rd], s84 offen offset:3072 sc1\n\t"
        "s_add_u32 s84, s84, 8192\n\t"
        "buffer_load_dwordx4 v[96:99], v240, %[rd], s84 offen offset:0 sc1\n\t"
        "buffer_load_dwordx4 v[100:103], v240, %[rd], s84 offen offset:1024 sc1\n\t"
        "buffer_load_dwordx4 v[104:107], v240, %[rd], s84 offen offset:2048 sc1\n\t"
        "buffer_load_dwordx4 v[108:111], v240, %[rd], s84 offen offset:3072 sc1\n\t"
        "s_add_u32 s84, s84, 8192\n\t"
        "buffer_load_dwordx4 v[112:115], v240, %[rd], s84 offen offset:0 sc1\n\t"
        "buffer_load_dwordx4 v[116:119], v240, %[rd], s84 offen offset:1024 sc1\n\t"
        "buffer_load_dwordx4 v[120:123], v240, %[rd], s84 offen offset:2048 sc1\n\t"
        "buffer_load_dwordx4 v[124:127], v240, %[rd], s84 offen offset:3072 sc1\n\t"
        "s_add_u32 s84, s84, 8192\n\t"
        "buffer_store_dword v0, off, %[ro], s36\n\t"
        "buffer_store_dword v0, off, %[ro], s36\n\t"
        "buffer_store_dword v0, off, %[ro], s36\n\t"
        "buffer_store_dword v0, off, %[ro], s36\n\t"
        "buffer_store_dword v0, off, %[ro], s36\n\t"
        "buffer_store_dword v0, off, %[ro], s36\n\t"
        "buffer_store_dword v0, off, %[ro], s36\n\t"
        "buffer_store_dword v0, off, %[ro], s36\n\t"
        "buffer_store_dword v0, off, %[ro], s36\n\t"
        "buffer_store_dword v0, off, %[ro], s36\n\t"
        "buffer_store_dword v0, off, %[ro], s36\n\t"
        "buffer_store_dword v0, off, %[ro], s36\n\t"
        "buffer_store_dword v0, off, %[ro], s36\n\t"
        "buffer_store_dword v0, off, %[ro], s36\n\t"
        "buffer_store_dword v0, off, %[ro], s36\n\t"
        "buffer_store_dword v0, off, %[ro], s36\n\t"
        "buffer_store_dword v0, off, %[ro], s36\n\t"
        ".Lrnd%=:\n\t"
        "s_lshl_b32 s85, s37, 1\n\t"
        "s_and_b32 s86, s40, 0x3fffffff\n\t"
        "s_lshr_b32 s86, s86, s85\n\t"
        "s_andn2_b32 s80, 12, s86\n\t"
        "s_cselect_b32 s41, 0, 1\n\t"
        "s_add_u32 s84, s44, 8\n\t"
        "s_lshl_b32 s84, s84, 17\n\t"
        "s_cmp_eq_u32 s41, 0\n\t"
        "s_cselect_b32 s84, s36, s84\n\t"
        "s_cselect_b32 s87, 0, s36\n\t"
        "s_andn2_b32 s80, 48, s86\n\t"
        "s_cselect_b32 s80, 0, s36\n\t"
        "v_add_u32_e32 v222, s43, v247\n\t"
        "s_sub_u32 s81, 33280, s43\n\t"
        "v_add_u32_e32 v223, s81, v248\n\t"
        "buffer_load_dword v220, v242, %[rd], s80 offen sc1\n\t"
        "s_waitcnt vmcnt(49)\n\t"
        "v_mfma_f32_16x16x32_bf16 v[128:131], a[128:131], v[0:3], 0\n\t"
        "ds_read_b128 v[186:189], v223 offset:0\n\t"
        "ds_read_b128 v[190:193], v223 offset:8320\n\t"
        "v_mfma_f32_16x16x32_bf16 v[132:135], a[132:135], v[0:3], 0\n\t"
        "ds_read_b128 v[194:197], v223 offset:16640\n\t"
        "ds_read_b128 v[198:201], v223 offset:24960\n\t"
        "v_mfma_f32_16x16x32_bf16 v[136:139], a[136:139], v[0:3], 0\n\t"
        "s_nop 0\n\t"
        "s_nop 0\n\t"
        "v_mfma_f32_16x16x32_bf16 v[140:143], a[140:143], v[0:3], 0\n\t"
        "s_nop 0\n\t"
        "s_nop 0\n\t"
        "buffer_load_dwordx4 v[0:3], v240, %[rd], s84 offen offset:0 sc1\n\t"
        "s_waitcnt vmcnt(49)\n\t"
        "v_mfma_f32_16x16x32_bf16 v[128:131], a[144:147], v[4:7], v[128:131]\n\t"
        "s_nop 0\n\t"
        "s_nop 0\n\t"
        "v_mfma_f32_16x16x32_bf16 v[132:135], a[148:151], v[4:7], v[132:135]\n\t"
        "s_waitcnt lgkmcnt(0)\n\t"
        "v_add_f32_e32 v186, v186, v190\n\t"
        "v_mfma_f32_16x16x32_bf16 v[136:139], a[152:155], v[4:7], v[136:139]\n\t"
        "v_add_f32_e32 v187, v187, v191\n\t"
        "v_add_f32_e32 v188, v188, v192\n\t"
        "v_mfma_f32_16x16x32_bf16 v[140:143], a[156:159], v[4:7], v[140:143]\n\t"
        "v_add_f32_e32 v189, v189, v193\n\t"
        "v_add_f32_e32 v194, v194, v198\n\t"
        "buffer_load_dwordx4 v[4:7], v240, %[rd], s84 offen offset:1024 sc1\n\t"
        "s_waitcnt vmcnt(49)\n\t"
        "v_mfma_f32_16x16x32_bf16 v[128:131], a[160:163], v[8:11], v[128:131]\n\t"
        "v_add_f32_e32 v195, v195, v199\n\t"
        "v_add_f32_e32 v196, v196, v200\n\t"
        "v_mfma_f32_16x16x32_bf16 v[132:135], a[164:167], v[8:11], v[132:135]\n\t"
        "v_add_f32_e32 v197, v197, v201\n\t"
        "v_add_f32_e32 v204, v186, v194\n\t"
        "v_mfma_f32_16x16x32_bf16 v[136:139], a[168:171], v[8:11], v[136:139]\n\t"
        "v_add_f32_e32 v206, v187, v195\n\t"
        "v_add_f32_e32 v208, v188, v196\n\t"
        "v_mfma_f32_16x16x32_bf16 v[140:143], a[172:175], v[8:11], v[140:143]\n\t"
        "v_add_f32_e32 v210, v189, v197\n\t"
        "s_add_i32 s80, s38, 0\n\t"
        "buffer_load_dwordx4 v[8:11], v240, %[rd], s84 offen offset:2048 sc1\n\t"
        "s_waitcnt vmcnt(47)\n\t"
        "v_mfma_f32_16x16x32_bf16 v[128:131], a[176:179], v[12:15], v[128:131]\n\t"
        "s_lshl_b32 s80, s80, 17\n\t"
        "s_or_b32 s81, s38, %[own]\n\t"
        "v_mfma_f32_16x16x32_bf16 v[132:135], a[180:183], v[12:15], v[132:135]\n\t"
        "s_cmp_lt_i32 s81, 0\n\t"
        "s_cselect_b32 s82, s36, s80\n\t"
        "buffer_store_dwordx4 v[204:207], v246, %[rd], s82 offen offset:0 sc1\n\t"
        "v_mfma_f32_16x16x32_bf16 v[136:139], a[184:187], v[12:15], v[136:139]\n\t"
        "buffer_store_dwordx4 v[208:211], v246, %[rd], s82 offen offset:1024 sc1\n\t"
        "v_mov_b32_e32 v144, v204\n\t"
        "v_mfma_f32_16x16x32_bf16 v[140:143], a[188:191], v[12:15], v[140:143]\n\t"
        "v_mov_b32_e32 v145, v206\n\t"
        "v_mov_b32_e32 v146, v208\n\t"
        "buffer_load_dwordx4 v[12:15], v240, %[rd], s84 offen offset:3072 sc1\n\t"
        "s_add_u32 s84, s84, 8192\n\t"
        "s_waitcnt vmcnt(49)\n\t"
        "v_mfma_f32_16x16x32_bf16 v[128:131], a[192:195], v[16:19], v[128:131]\n\t"
        "v_mov_b32_e32 v147, v210\n\t"
        "ds_read_b128 v[186:189], v223 offset:4160\n\t"
        "v_mfma_f32_16x16x32_bf16 v[132:135], a[196:199], v[16:19], v[132:135]\n\t"
        "ds_read_b128 v[190:193], v223 offset:12480\n\t"
        "ds_read_b128 v[194:197], v223 offset:20800\n\t"
        "v_mfma_f32_16x16x32_bf16 v[136:139], a[200:203], v[16:19], v[136:139]\n\t"
        "ds_read_b128 v[198:201], v223 offset:29120\n\t"
        "s_nop 0\n\t"
        "v_mfma_f32_16x16x32_bf16 v[140:143], a[204:207], v[16:19], v[140:143]\n\t"
        "s_nop 0\n\t"
        "s_nop 0\n\t"
        "buffer_load_dwordx4 v[16:19], v240, %[rd], s84 offen offset:0 sc1\n\t"
        "s_waitcnt vmcnt(49)\n\t"
        "v_mfma_f32_16x16x32_bf16 v[128:131], a[208:211], v[20:23], v[128:131]\n\t"
        "s_nop 0\n\t"
        "s_nop 0\n\t"
        "v_mfma_f32_16x16x32_bf16 v[132:135], a[212:215], v[20:23], v[132:135]\n\t"
        "s_nop 0\n\t"
        "s_waitcnt lgkmcnt(0)\n\t"
        "v_mfma_f32_16x16x32_bf16 v[136:139], a[216:219], v[20:23], v[136:139]\n\t"
        "v_add_f32_e32 v186, v186, v190\n\t"
        "v_add_f32_e32 v187, v187, v191\n\t"
        "v_mfma_f32_16x16x32_bf16 v[140:143], a[220:223], v[20:23], v[140:143]\n\t"
        "v_add_f32_e32 v188, v188, v192\n\t"
        "v_add_f32_e32 v189, v189, v193\n\t"
        "buffer_load_dwordx4 v[20:23], v240, %[rd], s84 offen offset:1024 sc1\n\t"
        "s_waitcnt vmcnt(49)\n\t"
        "v_mfma_f32_16x16x32_bf16 v[128:131], a[224:227], v[24:27], v[128:131]\n\t"
        "v_add_f32_e32 v194, v194, v198\n\t"
        "v_add_f32_e32 v195, v195, v199\n\t"
        "v_mfma_f32_16x16x32_bf16 v[132:135], a[228:231], v[24:27], v[132:135]\n\t"
        "v_add_f32_e32 v196, v196, v200\n\t"
        "v_add_f32_e32 v197, v197, v201\n\t"
        "v_mfma_f32_16x16x32_bf16 v[136:139], a[232:235], v[24:27], v[136:139]\n\t"
        "v_add_f32_e32 v212, v186, v194\n\t"
        "v_add_f32_e32 v214, v187, v195\n\t"
        "v_mfma_f32_16x16x32_bf16 v[140:143], a[236:239], v[24:27], v[140:143]\n\t"
        "v_add_f32_e32 v216, v188, v196\n\t"
        "v_add_f32_e32 v218, v189, v197\n\t"
        "buffer_load_dwordx4 v[24:27], v240, %[rd], s84 offen offset:2048 sc1\n\t"
        "s_waitcnt vmcnt(47)\n\t"
        "v_mfma_f32_16x16x32_bf16 v[128:131], a[240:243], v[28:31], v[128:131]\n\t"
        "s_add_i32 s80, s38, 1\n\t"
        "s_lshl_b32 s80, s80, 17\n\t"
        "v_mfma_f32_16x16x32_bf16 v[132:135], a[244:247], v[28:31], v[132:135]\n\t"
        "s_or_b32 s81, s38, %[own]\n\t"
        "s_cmp_lt_i32 s81, 0\n\t"
        "s_cselect_b32 s82, s36, s80\n\t"
        "v_mfma_f32_16x16x32_bf16 v[136:139], a[248:251], v[28:31], v[136:139]\n\t"
        "buffer_store_dwordx4 v[212:215], v246, %[rd], s82 offen offset:0 sc1\n\t"
        "buffer_store_dwordx4 v[216:219], v246, %[rd], s82 offen offset:1024 sc1\n\t"
        "v_mfma_f32_16x16x32_bf16 v[140:143], a[252:255], v[28:31], v[140:143]\n\t"
        "v_mov_b32_e32 v148, v212\n\t"
        "v_mov_b32_e32 v149, v214\n\t"
        "buffer_load_dwordx4 v[28:31], v240, %[rd], s84 offen offset:3072 sc1\n\t"
        "s_add_u32 s84, s84, 8192\n\t"
        "s_waitcnt vmcnt(49)\n\t"
        "v_mfma_f32_16x16x32_bf16 v[128:131], a[0:3], v[32:35], v[128:131]\n\t"
        "v_mov_b32_e32 v150, v216\n\t"
        "v_mov_b32_e32 v151, v218\n\t"
        "v_mfma_f32_16x16x32_bf16 v[132:135], a[4:7], v[32:35], v[132:135]\n\t"
        "s_waitcnt vmcnt(34)\n\t"
        "s_add_i32 s80, s39, 0\n\t"
        "v_mfma_f32_16x16x32_bf16 v[136:139], a[8:11], v[32:35], v[136:139]\n\t"
        "s_lshl_b32 s80, s80, 15\n\t"
        "s_or_b32 s81, s39, %[snd]\n\t"
        "v_mfma_f32_16x16x32_bf16 v[140:143], a[12:15], v[32:35], v[140:143]\n\t"
        "s_cmp_lt_i32 s81, 0\n\t"
        "s_cselect_b32 s83, s36, s80\n\t"
        "s_cselect_b64 s[88:89], 0, -1\n\t"
        "v_xor_b32_e32 v224, %[tag], v161\n\t"
        "buffer_load_dwordx4 v[32:35], v240, %[rd], s84 offen offset:0 sc1\n\t"
        "s_waitcnt vmcnt(49)\n\t"
        "v_mfma_f32_16x16x32_bf16 v[128:131], a[16:19], v[36:39], v[128:131]\n\t"
        "v_xor_b32_e32 v225, %[tag], v163\n\t"
        "v_xor_b32_e32 v226, %[tag], v165\n\t"
        "v_mfma_f32_16x16x32_bf16 v[132:135], a[20:23], v[36:39], v[132:135]\n\t"
        "v_or3_b32 v224, v224, v225, v226\n\t"
        "v_xor_b32_e32 v225, %[tag], v167\n\t"
        "v_mfma_f32_16x16x32_bf16 v[136:139], a[24:27], v[36:39], v[136:139]\n\t"
        "v_or_b32_e32 v224, v224, v225\n\t"
        "v_cmp_ne_u32_e32 vcc, 0, v224\n\t"
        "v_mfma_f32_16x16x32_bf16 v[140:143], a[28:31], v[36:39], v[140:143]\n\t"
        "v_sub_f32_e32 v236, 1.0, v184\n\t"
        "v_cvt_pk_bf16_f32 v236, 0, v236\n\t"
        "s_and_b64 vcc, vcc, s[88:89]\n\t"
        "s_cbranch_scc1 .Lo13%=\n\t"
        ".Lo14%=:\n\t"
        "buffer_load_dwordx4 v[36:39], v240, %[rd], s84 offen offset:1024 sc1\n\t"
        "s_waitcnt vmcnt(49)\n\t"
        "v_mfma_f32_16x16x32_bf16 v[128:131], a[32:35], v[40:43], v[128:131]\n\t"
        "v_add_f32_e32 v228, v152, v160\n\t"
        "v_add_f32_e32 v229, v153, v162\n\t"
        "v_mfma_f32_16x16x32_bf16 v[132:135], a[36:39], v[40:43], v[132:135]\n\t"
        "v_add_f32_e32 v230, v154, v164\n\t"
        "v_add_f32_e32 v231, v155, v166\n\t"
        "v_mfma_f32_16x16x32_bf16 v[136:139], a[40:43], v[40:43], v[136:139]\n\t"
        "v_cvt_pk_bf16_f32 v228, 0, v228\n\t"
        "v_cvt_pk_bf16_f32 v229, 0, v229\n\t"
        "v_mfma_f32_16x16x32_bf16 v[140:143], a[44:47], v[40:43], v[140:143]\n\t"
        "v_cvt_pk_bf16_f32 v230, 0, v230\n\t"
        "v_cvt_pk_bf16_f32 v231, 0, v231\n\t"
        "buffer_load_dwordx4 v[40:43], v240, %[rd], s84 offen offset:2048 sc1\n\t"
        "s_waitcnt vmcnt(49)\n\t"
        "v_mfma_f32_16x16x32_bf16 v[128:131], a[48:51], v[44:47], v[128:131]\n\t"
        "v_lshlrev_b32_e32 v224, 16, v180\n\t"
        "v_lshlrev_b32_e32 v232, 16, v176\n\t"
        "v_mfma_f32_16x16x32_bf16 v[132:135], a[52:55], v[44:47], v[132:135]\n\t"
        "v_and_b32_e32 v225, 0xffff0000, v180\n\t"
        "v_and_b32_e32 v233, 0xffff0000, v176\n\t"
        "v_mfma_f32_16x16x32_bf16 v[136:139], a[56:59], v[44:47], v[136:139]\n\t"
        "v_lshlrev_b32_e32 v226, 16, v181\n\t"
        "v_lshlrev_b32_e32 v234, 16, v177\n\t"
        "v_mfma_f32_16x16x32_bf16 v[140:143], a[60:63], v[44:47], v[140:143]\n\t"
        "v_and_b32_e32 v227, 0xffff0000, v181\n\t"
        "v_and_b32_e32 v235, 0xffff0000, v177\n\t"
        "buffer_load_dwordx4 v[44:47], v240, %[rd], s84 offen offset:3072 sc1\n\t"
        "s_add_u32 s84, s84, 8192\n\t"
        "s_waitcnt vmcnt(49)\n\t"
        "v_mfma_f32_16x16x32_bf16 v[128:131], a[64:67], v[48:51], v[128:131]\n\t"
        "v_mul_f32_e32 v224, v224, v232\n\t"
        "v_mul_f32_e32 v225, v225, v233\n\t"
        "v_mfma_f32_16x16x32_bf16 v[132:135], a[68:71], v[48:51], v[132:135]\n\t"
        "v_mul_f32_e32 v226, v226, v234\n\t"
        "v_mul_f32_e32 v227, v227, v235\n\t"
        "v_mfma_f32_16x16x32_bf16 v[136:139], a[72:75], v[48:51], v[136:139]\n\t"
        "v_mul_f32_e32 v228, v236, v228\n\t"
        "v_mul_f32_e32 v229, v236, v229\n\t"
        "v_mfma_f32_16x16x32_bf16 v[140:143], a[76:79], v[48:51], v[140:143]\n\t"
        "v_mul_f32_e32 v230, v236, v230\n\t"
        "v_mul_f32_e32 v231, v236, v231\n\t"
        "buffer_load_dwordx4 v[48:51], v240, %[rd], s84 offen offset:0 sc1\n\t"
        "s_waitcnt vmcnt(49)\n\t"
        "v_mfma_f32_16x16x32_bf16 v[128:131], a[80:83], v[52:55], v[128:131]\n\t"
        "v_cvt_pk_bf16_f32 v224, 0, v224\n\t"
        "v_cvt_pk_bf16_f32 v225, 0, v225\n\t"
        "v_mfma_f32_16x16x32_bf16 v[132:135], a[84:87], v[52:55], v[132:135]\n\t"
        "v_cvt_pk_bf16_f32 v226, 0, v226\n\t"
        "v_cvt_pk_bf16_f32 v227, 0, v227\n\t"
        "v_mfma_f32_16x16x32_bf16 v[136:139], a[88:91], v[52:55], v[136:139]\n\t"
        "v_cvt_pk_bf16_f32 v228, 0, v228\n\t"
        "v_cvt_pk_bf16_f32 v229, 0, v229\n\t"
        "v_mfma_f32_16x16x32_bf16 v[140:143], a[92:95], v[52:55], v[140:143]\n\t"
        "v_cvt_pk_bf16_f32 v230, 0, v230\n\t"
        "v_cvt_pk_bf16_f32 v231, 0, v231\n\t"
        "buffer_load_dwordx4 v[52:55], v240, %[rd], s84 offen offset:1024 sc1\n\t"
        "s_waitcnt vmcnt(49)\n\t"
        "v_mfma_f32_16x16x32_bf16 v[128:131], a[96:99], v[56:59], v[128:131]\n\t"
        "v_mul_f32_e32 v224, v184, v224\n\t"
        "v_mul_f32_e32 v225, v184, v225\n\t"
        "v_mfma_f32_16x16x32_bf16 v[132:135], a[100:103], v[56:59], v[132:135]\n\t"
        "v_mul_f32_e32 v226, v184, v226\n\t"
        "v_mul_f32_e32 v227, v184, v227\n\t"
        "v_mfma_f32_16x16x32_bf16 v[136:139], a[104:107], v[56:59], v[136:139]\n\t"
        "v_cvt_pk_bf16_f32 v224, 0, v224\n\t"
        "v_cvt_pk_bf16_f32 v225, 0, v225\n\t"
        "v_mfma_f32_16x16x32_bf16 v[140:143], a[108:111], v[56:59], v[140:143]\n\t"
        "v_cvt_pk_bf16_f32 v226, 0, v226\n\t"
        "v_cvt_pk_bf16_f32 v227, 0, v227\n\t"
        "buffer_load_dwordx4 v[56:59], v240, %[rd], s84 offen offset:2048 sc1\n\t"
        "s_waitcnt vmcnt(48)\n\t"
        "v_mfma_f32_16x16x32_bf16 v[128:131], a[112:115], v[60:63], v[128:131]\n\t"
        "v_add_f32_e32 v224, v224, v228\n\t"
        "v_add_f32_e32 v225, v225, v229\n\t"
        "v_mfma_f32_16x16x32_bf16 v[132:135], a[116:119], v[60:63], v[132:135]\n\t"
        "v_add_f32_e32 v226, v226, v230\n\t"
        "v_add_f32_e32 v227, v227, v231\n\t"
        "v_mfma_f32_16x16x32_bf16 v[136:139], a[120:123], v[60:63], v[136:139]\n\t"
        "v_cvt_pk_bf16_f32 v238, v224, v225\n\t"
        "v_cvt_pk_bf16_f32 v239, v226, v227\n\t"
        "v_mfma_f32_16x16x32_bf16 v[140:143], a[124:127], v[60:63], v[140:143]\n\t"
        "buffer_store_dwordx2 v[238:239], v245, %[ro], s83 offen\n\t"
        "s_add_i32 s80, s38, 0\n\t"
        "buffer_load_dwordx4 v[60:63], v240, %[rd], s84 offen offset:3072 sc1\n\t"
        "s_sub_u32 s84, s84, 20480\n\t"
        "ds_write_b128 v222, v[128:131] offset:0\n\t"
        "ds_write_b128 v222, v[132:135] offset:1040\n\t"
        "ds_write_b128 v222, v[136:139] offset:2080\n\t"
        "s_nop 0\n\t"
        "ds_write_b128 v222, v[140:143] offset:3120\n\t"
        "s_waitcnt vmcnt(47)\n\t"
        "v_mfma_f32_16x16x32_bf16 v[128:131], a[128:131], v[64:67], 0\n\t"
        "s_lshl_b32 s80, s80, 6\n\t"
        "s_or_b32 s81, s38, %[snd]\n\t"
        "v_mfma_f32_16x16x32_bf16 v[132:135], a[132:135], v[64:67], 0\n\t"
        "s_cmp_lt_i32 s81, 0\n\t"
        "s_cselect_b32 s82, s36, s80\n\t"
        "buffer_load_dword v184, v244, %[rd], s82 offen sc1\n\t"
        "v_mfma_f32_16x16x32_bf16 v[136:139], a[136:139], v[64:67], 0\n\t"
        "s_lshl_b32 s80, s80, 1\n\t"
        "s_cmp_lt_i32 s81, 0\n\t"
        "s_cselect_b32 s82, s36, s80\n\t"
        "v_mfma_f32_16x16x32_bf16 v[140:143], a[140:143], v[64:67], 0\n\t"
        "buffer_load_dwordx2 v[180:181], v243, %[rd], s82 offen sc1\n\t"
        "s_lshl_b32 s80, s80, 8\n\t"
        "buffer_load_dwordx4 v[64:67], v240, %[rd], s84 offen offset:0 sc1\n\t"
        "s_waitcnt vmcnt(46)\n\t"
        "v_mfma_f32_16x16x32_bf16 v[128:131], a[144:147], v[68:71], v[128:131]\n\t"
        "s_cmp_lt_i32 s81, 0\n\t"
        "s_cselect_b32 s82, s36, s80\n\t"
        "buffer_load_dwordx2 v[176:177], v245, %[rx], s82 offen\n\t"
        "v_mfma_f32_16x16x32_bf16 v[132:135], a[148:151], v[68:71], v[132:135]\n\t"
        "s_add_i32 s80, s38, 0\n\t"
        "s_lshl_b32 s80, s80, 17\n\t"
        "v_mfma_f32_16x16x32_bf16 v[136:139], a[152:155], v[68:71], v[136:139]\n\t"
        "s_or_b32 s81, s38, %[snd]\n\t"
        "s_cmp_lt_i32 s81, 0\n\t"
        "s_cselect_b32 s82, s36, s80\n\t"
        "v_mfma_f32_16x16x32_bf16 v[140:143], a[156:159], v[68:71], v[140:143]\n\t"
        "buffer_load_dwordx4 v[160:163], v246, %[rd], s82 offen offset:0 sc1\n\t"
        "buffer_load_dwordx4 v[164:167], v246, %[rd], s82 offen offset:1024 sc1\n\t"
        "buffer_load_dwordx4 v[68:71], v240, %[rd], s84 offen offset:1024 sc1\n\t"
        "s_waitcnt vmcnt(49)\n\t"
        "v_mfma_f32_16x16x32_bf16 v[128:131], a[160:163], v[72:75], v[128:131]\n\t"
        "s_waitcnt vmcnt(34)\n\t"
        "v_mfma_f32_16x16x32_bf16 v[132:135], a[164:167], v[72:75], v[132:135]\n\t"
        "s_add_i32 s80, s39, 1\n\t"
        "s_lshl_b32 s80, s80, 15\n\t"
        "v_mfma_f32_16x16x32_bf16 v[136:139], a[168:171], v[72:75], v[136:139]\n\t"
        "s_or_b32 s81, s39, %[snd]\n\t"
        "s_cmp_lt_i32 s81, 0\n\t"
        "s_cselect_b32 s83, s36, s80\n\t"
        "s_cselect_b64 s[88:89], 0, -1\n\t"
        "v_mfma_f32_16x16x32_bf16 v[140:143], a[172:175], v[72:75], v[140:143]\n\t"
        "v_xor_b32_e32 v224, %[tag], v169\n\t"
        "v_xor_b32_e32 v225, %[tag], v171\n\t"
        "buffer_load_dwordx4 v[72:75], v240, %[rd], s84 offen offset:2048 sc1\n\t"
        "s_waitcnt vmcnt(49)\n\t"
        "v_mfma_f32_16x16x32_bf16 v[128:131], a[176:179], v[76:79], v[128:131]\n\t"
        "v_xor_b32_e32 v226, %[tag], v173\n\t"
        "v_or3_b32 v224, v224, v225, v226\n\t"
        "v_mfma_f32_16x16x32_bf16 v[132:135], a[180:183], v[76:79], v[132:135]\n\t"
        "v_xor_b32_e32 v225, %[tag], v175\n\t"
        "v_or_b32_e32 v224, v224, v225\n\t"
        "v_mfma_f32_16x16x32_bf16 v[136:139], a[184:187], v[76:79], v[136:139]\n\t"
        "v_cmp_ne_u32_e32 vcc, 0, v224\n\t"
        "v_sub_f32_e32 v236, 1.0, v185\n\t"
        "v_mfma_f32_16x16x32_bf16 v[140:143], a[188:191], v[76:79], v[140:143]\n\t"
        "v_cvt_pk_bf16_f32 v236, 0, v236\n\t"
        "s_and_b64 vcc, vcc, s[88:89]\n\t"
        "s_cbranch_scc1 .Lo15%=\n\t"
        ".Lo16%=:\n\t"
        "v_add_f32_e32 v228, v156, v168\n\t"
        "buffer_load_dwordx4 v[76:79], v240, %[rd], s84 offen offset:3072 sc1\n\t"
        "s_add_u32 s84, s84, 8192\n\t"
        "s_waitcnt vmcnt(49)\n\t"
        "v_mfma_f32_16x16x32_bf16 v[128:131], a[192:195], v[80:83], v[128:131]\n\t"
        "v_add_f32_e32 v229, v157, v170\n\t"
        "v_add_f32_e32 v230, v158, v172\n\t"
        "v_mfma_f32_16x16x32_bf16 v[132:135], a[196:199], v[80:83], v[132:135]\n\t"
        "v_add_f32_e32 v231, v159, v174\n\t"
        "v_cvt_pk_bf16_f32 v228, 0, v228\n\t"
        "v_mfma_f32_16x16x32_bf16 v[136:139], a[200:203], v[80:83], v[136:139]\n\t"
        "v_cvt_pk_bf16_f32 v229, 0, v229\n\t"
        "v_cvt_pk_bf16_f32 v230, 0, v230\n\t"
        "v_mfma_f32_16x16x32_bf16 v[140:143], a[204:207], v[80:83], v[140:143]\n\t"
        "v_cvt_pk_bf16_f32 v231, 0, v231\n\t"
        "v_lshlrev_b32_e32 v224, 16, v182\n\t"
        "buffer_load_dwordx4 v[80:83], v240, %[rd], s84 offen offset:0 sc1\n\t"
        "s_waitcnt vmcnt(49)\n\t"
        "v_mfma_f32_16x16x32_bf16 v[128:131], a[208:211], v[84:87], v[128:131]\n\t"
        "v_lshlrev_b32_e32 v232, 16, v178\n\t"
        "v_and_b32_e32 v225, 0xffff0000, v182\n\t"
        "v_mfma_f32_16x16x32_bf16 v[132:135], a[212:215], v[84:87], v[132:135]\n\t"
        "v_and_b32_e32 v233, 0xffff0000, v178\n\t"
        "v_lshlrev_b32_e32 v226, 16, v183\n\t"
        "v_mfma_f32_16x16x32_bf16 v[136:139], a[216:219], v[84:87], v[136:139]\n\t"
        "v_lshlrev_b32_e32 v234, 16, v179\n\t"
        "v_and_b32_e32 v227, 0xffff0000, v183\n\t"
        "v_mfma_f32_16x16x32_bf16 v[140:143], a[220:223], v[84:87], v[140:143]\n\t"
        "v_and_b32_e32 v235, 0xffff0000, v179\n\t"
        "v_mul_f32_e32 v224, v224, v232\n\t"
        "buffer_load_dwordx4 v[84:87], v240, %[rd], s84 offen offset:1024 sc1\n\t"
        "s_waitcnt vmcnt(49)\n\t"
        "v_mfma_f32_16x16x32_bf16 v[128:131], a[224:227], v[88:91], v[128:131]\n\t"
        "v_mul_f32_e32 v225, v225, v233\n\t"
        "v_mul_f32_e32 v226, v226, v234\n\t"
        "v_mfma_f32_16x16x32_bf16 v[132:135], a[228:231], v[88:91], v[132:135]\n\t"
        "v_mul_f32_e32 v227, v227, v235\n\t"
        "v_mul_f32_e32 v228, v236, v228\n\t"
        "v_mfma_f32_16x16x32_bf16 v[136:139], a[232:235], v[88:91], v[136:139]\n\t"
        "v_mul_f32_e32 v229, v236, v229\n\t"
        "v_mul_f32_e32 v230, v236, v230\n\t"
        "v_mfma_f32_16x16x32_bf16 v[140:143], a[236:239], v[88:91], v[140:143]\n\t"
        "v_mul_f32_e32 v231, v236, v231\n\t"
        "v_cvt_pk_bf16_f32 v224, 0, v224\n\t"
        "buffer_load_dwordx4 v[88:91], v240, %[rd], s84 offen offset:2048 sc1\n\t"
        "s_waitcnt vmcnt(49)\n\t"
        "v_mfma_f32_16x16x32_bf16 v[128:131], a[240:243], v[92:95], v[128:131]\n\t"
        "v_cvt_pk_bf16_f32 v225, 0, v225\n\t"
        "v_cvt_pk_bf16_f32 v226, 0, v226\n\t"
        "v_mfma_f32_16x16x32_bf16 v[132:135], a[244:247], v[92:95], v[132:135]\n\t"
        "v_cvt_pk_bf16_f32 v227, 0, v227\n\t"
        "v_cvt_pk_bf16_f32 v228, 0, v228\n\t"
        "v_mfma_f32_16x16x32_bf16 v[136:139], a[248:251], v[92:95], v[136:139]\n\t"
        "v_cvt_pk_bf16_f32 v229, 0, v229\n\t"
        "v_cvt_pk_bf16_f32 v230, 0, v230\n\t"
        "v_mfma_f32_16x16x32_bf16 v[140:143], a[252:255], v[92:95], v[140:143]\n\t"
        "v_cvt_pk_bf16_f32 v231, 0, v231\n\t"
        "v_mul_f32_e32 v224, v185, v224\n\t"
        "buffer_load_dwordx4 v[92:95], v240, %[rd], s84 offen offset:3072 sc1\n\t"
        "s_add_u32 s84, s84, 8192\n\t"
        "buffer_load_dword v221, v242, %[rd], s87 offen sc1\n\t"
        "s_waitcnt vmcnt(49)\n\t"
        "v_mfma_f32_16x16x32_bf16 v[128:131], a[0:3], v[96:99], v[128:131]\n\t"
        "v_mul_f32_e32 v225, v185, v225\n\t"
        "v_mul_f32_e32 v226, v185, v226\n\t"
        "v_mfma_f32_16x16x32_bf16 v[132:135], a[4:7], v[96:99], v[132:135]\n\t"
        "v_mul_f32_e32 v227, v185, v227\n\t"
        "v_cvt_pk_bf16_f32 v224, 0, v224\n\t"
        "v_mfma_f32_16x16x32_bf16 v[136:139], a[8:11], v[96:99], v[136:139]\n\t"
        "v_cvt_pk_bf16_f32 v225, 0, v225\n\t"
        "v_cvt_pk_bf16_f32 v226, 0, v226\n\t"
        "v_mfma_f32_16x16x32_bf16 v[140:143], a[12:15], v[96:99], v[140:143]\n\t"
        "v_cvt_pk_bf16_f32 v227, 0, v227\n\t"
        "v_add_f32_e32 v224, v224, v228\n\t"
        "buffer_load_dwordx4 v[96:99], v240, %[rd], s84 offen offset:0 sc1\n\t"
        "s_waitcnt vmcnt(48)\n\t"
        "v_mfma_f32_16x16x32_bf16 v[128:131], a[16:19], v[100:103], v[128:131]\n\t"
        "v_add_f32_e32 v225, v225, v229\n\t"
        "v_add_f32_e32 v226, v226, v230\n\t"
        "v_mfma_f32_16x16x32_bf16 v[132:135], a[20:23], v[100:103], v[132:135]\n\t"
        "v_add_f32_e32 v227, v227, v231\n\t"
        "v_cvt_pk_bf16_f32 v238, v224, v225\n\t"
        "v_mfma_f32_16x16x32_bf16 v[136:139], a[24:27], v[100:103], v[136:139]\n\t"
        "v_cvt_pk_bf16_f32 v239, v226, v227\n\t"
        "buffer_store_dwordx2 v[238:239], v245, %[ro], s83 offen\n\t"
        "v_mfma_f32_16x16x32_bf16 v[140:143], a[28:31], v[100:103], v[140:143]\n\t"
        "s_add_i32 s80, s38, 1\n\t"
        "s_lshl_b32 s80, s80, 6\n\t"
        "buffer_load_dwordx4 v[100:103], v240, %[rd], s84 offen offset:1024 sc1\n\t"
        "s_waitcnt vmcnt(47)\n\t"
        "v_mfma_f32_16x16x32_bf16 v[128:131], a[32:35], v[104:107], v[128:131]\n\t"
        "s_or_b32 s81, s38, %[snd]\n\t"
        "s_cmp_lt_i32 s81, 0\n\t"
        "s_cselect_b32 s82, s36, s80\n\t"
        "v_mfma_f32_16x16x32_bf16 v[132:135], a[36:39], v[104:107], v[132:135]\n\t"
        "buffer_load_dword v185, v244, %[rd], s82 offen sc1\n\t"
        "s_lshl_b32 s80, s80, 1\n\t"
        "v_mfma_f32_16x16x32_bf16 v[136:139], a[40:43], v[104:107], v[136:139]\n\t"
        "s_cmp_lt_i32 s81, 0\n\t"
        "s_cselect_b32 s82, s36, s80\n\t"
        "buffer_load_dwordx2 v[182:183], v243, %[rd], s82 offen sc1\n\t"
        "v_mfma_f32_16x16x32_bf16 v[140:143], a[44:47], v[104:107], v[140:143]\n\t"
        "s_lshl_b32 s80, s80, 8\n\t"
        "s_cmp_lt_i32 s81, 0\n\t"
        "s_cselect_b32 s82, s36, s80\n\t"
        "buffer_load_dwordx4 v[104:107], v240, %[rd], s84 offen offset:2048 sc1\n\t"
        "s_waitcnt vmcnt(46)\n\t"
        "v_mfma_f32_16x16x32_bf16 v[128:131], a[48:51], v[108:111], v[128:131]\n\t"
        "buffer_load_dwordx2 v[178:179], v245, %[rx], s82 offen\n\t"
        "s_add_i32 s80, s38, 1\n\t"
        "v_mfma_f32_16x16x32_bf16 v[132:135], a[52:55], v[108:111], v[132:135]\n\t"
        "s_lshl_b32 s80, s80, 17\n\t"
        "s_or_b32 s81, s38, %[snd]\n\t"
        "v_mfma_f32_16x16x32_bf16 v[136:139], a[56:59], v[108:111], v[136:139]\n\t"
        "s_cmp_lt_i32 s81, 0\n\t"
        "s_cselect_b32 s82, s36, s80\n\t"
        "buffer_load_dwordx4 v[168:171], v246, %[rd], s82 offen offset:0 sc1\n\t"
        "v_mfma_f32_16x16x32_bf16 v[140:143], a[60:63], v[108:111], v[140:143]\n\t"
        "buffer_load_dwordx4 v[172:175], v246, %[rd], s82 offen offset:1024 sc1\n\t"
        "buffer_load_dwordx4 v[108:111], v240, %[rd], s84 offen offset:3072 sc1\n\t"
        "s_add_u32 s84, s84, 8192\n\t"
        "s_waitcnt vmcnt(49)\n\t"
        "v_mfma_f32_16x16x32_bf16 v[128:131], a[64:67], v[112:115], v[128:131]\n\t"
        "v_mfma_f32_16x16x32_bf16 v[132:135], a[68:71], v[112:115], v[132:135]\n\t"
        "v_mfma_f32_16x16x32_bf16 v[136:139], a[72:75], v[112:115], v[136:139]\n\t"
        "v_mfma_f32_16x16x32_bf16 v[140:143], a[76:79], v[112:115], v[140:143]\n\t"
        "buffer_load_dwordx4 v[112:115], v240, %[rd], s84 offen offset:0 sc1\n\t"
        "s_waitcnt vmcnt(49)\n\t"
        "v_mfma_f32_16x16x32_bf16 v[128:131], a[80:83], v[116:119], v[128:131]\n\t"
        "v_mfma_f32_16x16x32_bf16 v[132:135], a[84:87], v[116:119], v[132:135]\n\t"
        "v_mfma_f32_16x16x32_bf16 v[136:139], a[88:91], v[116:119], v[136:139]\n\t"
        "v_mfma_f32_16x16x32_bf16 v[140:143], a[92:95], v[116:119], v[140:143]\n\t"
        "buffer_load_dwordx4 v[116:119], v240, %[rd], s84 offen offset:1024 sc1\n\t"
        "s_waitcnt vmcnt(49)\n\t"
        "v_mfma_f32_16x16x32_bf16 v[128:131], a[96:99], v[120:123], v[128:131]\n\t"
        "v_mfma_f32_16x16x32_bf16 v[132:135], a[100:103], v[120:123], v[132:135]\n\t"
        "v_mfma_f32_16x16x32_bf16 v[136:139], a[104:107], v[120:123], v[136:139]\n\t"
        "v_mfma_f32_16x16x32_bf16 v[140:143], a[108:111], v[120:123], v[140:143]\n\t"
        "buffer_load_dwordx4 v[120:123], v240, %[rd], s84 offen offset:2048 sc1\n\t"
        "s_waitcnt vmcnt(49)\n\t"
        "v_mfma_f32_16x16x32_bf16 v[128:131], a[112:115], v[124:127], v[128:131]\n\t"
        "v_mfma_f32_16x16x32_bf16 v[132:135], a[116:119], v[124:127], v[132:135]\n\t"
        "v_mfma_f32_16x16x32_bf16 v[136:139], a[120:123], v[124:127], v[136:139]\n\t"
        "v_mfma_f32_16x16x32_bf16 v[140:143], a[124:127], v[124:127], v[140:143]\n\t"
        "buffer_load_dwordx4 v[124:127], v240, %[rd], s84 offen offset:3072 sc1\n\t"
        "s_add_u32 s84, s84, 8192\n\t"
        "s_nop 2\n\t"
        "ds_write_b128 v222, v[128:131] offset:4160\n\t"
        "ds_write_b128 v222, v[132:135] offset:5200\n\t"
        "ds_write_b128 v222, v[136:139] offset:6240\n\t"
        "ds_write_b128 v222, v[140:143] offset:7280\n\t"
        "s_waitcnt vmcnt(49)\n\t"
        "v_cmp_eq_u32_e32 vcc, 32, v220\n\t"
        "s_nop 1\n\t"
        "s_or_b32 s40, s40, vcc_lo\n\t"
        "s_waitcnt lgkmcnt(0)\n\t"
        "s_barrier\n\t"
        "s_cmp_eq_u32 s41, 0\n\t"
        "s_cbranch_scc1 .Lfl%=\n\t"
        "v_mov_b32_e32 v152, v144\n\t"
        "v_mov_b32_e32 v153, v145\n\t"
        "v_mov_b32_e32 v154, v146\n\t"
        "v_mov_b32_e32 v155, v147\n\t"
        "v_mov_b32_e32 v156, v148\n\t"
        "v_mov_b32_e32 v157, v149\n\t"
        "v_mov_b32_e32 v158, v150\n\t"
        "v_mov_b32_e32 v159, v151\n\t"
        "s_mov_b32 s39, s38\n\t"
        "s_mov_b32 s38, s44\n\t"
        "s_add_u32 s37, s37, 1\n\t"
        "s_add_u32 s44, s44, 8\n\t"
        "s_sub_u32 s43, 33280, s43\n\t"
        "s_branch .Lrnd%=\n\t"
        ".Lfl%=:\n\t"
        "v_add_u32_e32 v223, s43, v248\n\t"
        "buffer_load_dword v220, v242, %[rd], 0 offen sc1\n\t"
        "ds_read_b128 v[186:189], v223 offset:0\n\t"
        "ds_read_b128 v[190:193], v223 offset:8320\n\t"
        "ds_read_b128 v[194:197], v223 offset:16640\n\t"
        "ds_read_b128 v[198:201], v223 offset:24960\n\t"
        "s_waitcnt lgkmcnt(0)\n\t"
        "v_add_f32_e32 v186, v186, v190\n\t"
        "v_add_f32_e32 v187, v187, v191\n\t"
        "v_add_f32_e32 v188, v188, v192\n\t"
        "v_add_f32_e32 v189, v189, v193\n\t"
        "v_add_f32_e32 v194, v194, v198\n\t"
        "v_add_f32_e32 v195, v195, v199\n\t"
        "v_add_f32_e32 v196, v196, v200\n\t"
        "v_add_f32_e32 v197, v197, v201\n\t"
        "v_add_f32_e32 v204, v186, v194\n\t"
        "v_add_f32_e32 v206, v187, v195\n\t"
        "v_add_f32_e32 v208, v188, v196\n\t"
        "v_add_f32_e32 v210, v189, v197\n\t"
        "s_add_i32 s80, s44, 0\n\t"
        "s_lshl_b32 s80, s80, 17\n\t"
        "s_or_b32 s81, s44, %[own]\n\t"
        "s_cmp_lt_i32 s81, 0\n\t"
        "s_cselect_b32 s82, s36, s80\n\t"
        "buffer_store_dwordx4 v[204:207], v246, %[rd], s82 offen offset:0 sc1\n\t"
        "buffer_store_dwordx4 v[208:211], v246, %[rd], s82 offen offset:1024 sc1\n\t"
        "v_mov_b32_e32 v128, v204\n\t"
        "v_mov_b32_e32 v129, v206\n\t"
        "v_mov_b32_e32 v130, v208\n\t"
        "v_mov_b32_e32 v131, v210\n\t"
        "ds_read_b128 v[186:189], v223 offset:4160\n\t"
        "ds_read_b128 v[190:193], v223 offset:12480\n\t"
        "ds_read_b128 v[194:197], v223 offset:20800\n\t"
        "ds_read_b128 v[198:201], v223 offset:29120\n\t"
        "s_waitcnt lgkmcnt(0)\n\t"
        "v_add_f32_e32 v186, v186, v190\n\t"
        "v_add_f32_e32 v187, v187, v191\n\t"
        "v_add_f32_e32 v188, v188, v192\n\t"
        "v_add_f32_e32 v189, v189, v193\n\t"
        "v_add_f32_e32 v194, v194, v198\n\t"
        "v_add_f32_e32 v195, v195, v199\n\t"
        "v_add_f32_e32 v196, v196, v200\n\t"
        "v_add_f32_e32 v197, v197, v201\n\t"
        "v_add_f32_e32 v212, v186, v194\n\t"
        "v_add_f32_e32 v214, v187, v195\n\t"
        "v_add_f32_e32 v216, v188, v196\n\t"
        "v_add_f32_e32 v218, v189, v197\n\t"
        "s_add_i32 s80, s44, 1\n\t"
        "s_lshl_b32 s80, s80, 17\n\t"
        "s_or_b32 s81, s44, %[own]\n\t"
        "s_cmp_lt_i32 s81, 0\n\t"
        "s_cselect_b32 s82, s36, s80\n\t"
        "buffer_store_dwordx4 v[212:215], v246, %[rd], s82 offen offset:0 sc1\n\t"
        "buffer_store_dwordx4 v[216:219], v246, %[rd], s82 offen offset:1024 sc1\n\t"
        "v_mov_b32_e32 v132, v212\n\t"
        "v_mov_b32_e32 v133, v214\n\t"
        "v_mov_b32_e32 v134, v216\n\t"
        "v_mov_b32_e32 v135, v218\n\t"
        "s_waitcnt vmcnt(4)\n\t"
        "v_cmp_eq_u32_e32 vcc, 32, v220\n\t"
        "s_nop 1\n\t"
        "s_or_b32 s40, s40, vcc_lo\n\t"
        "s_add_i32 s80, s38, 0\n\t"
        "s_lshl_b32 s80, s80, 15\n\t"
        "s_or_b32 s81, s38, %[snd]\n\t"
        "s_cmp_lt_i32 s81, 0\n\t"
        "s_cselect_b32 s83, s36, s80\n\t"
        "s_cbranch_scc1 .Lo19%=\n\t"
        "s_mov_b32 s97, 0\n\t"
        ".Lo17%=:\n\t"
        "v_xor_b32_e32 v186, %[tag], v161\n\t"
        "v_xor_b32_e32 v187, %[tag], v163\n\t"
        "v_xor_b32_e32 v188, %[tag], v165\n\t"
        "v_or3_b32 v186, v186, v187, v188\n\t"
        "v_xor_b32_e32 v187, %[tag], v167\n\t"
        "v_or_b32_e32 v186, v186, v187\n\t"
        "v_cmp_ne_u32_e32 vcc, 0, v186\n\t"
        "v_sub_f32_e32 v198, 1.0, v184\n\t"
        "v_add_f32_e32 v190, v144, v160\n\t"
        "v_add_f32_e32 v191, v145, v162\n\t"
        "v_add_f32_e32 v192, v146, v164\n\t"
        "v_add_f32_e32 v193, v147, v166\n\t"
        "s_cbranch_vccz .Lo18%=\n\t"
        "s_sleep %c[xslp]\n\t"
        "s_lshl_b32 s82, s83, 2\n\t"
        "buffer_load_dwordx4 v[160:163], v246, %[rd], s82 offen offset:0 sc1\n\t"
        "buffer_load_dwordx4 v[164:167], v246, %[rd], s82 offen offset:1024 sc1\n\t"
        "s_add_u32 s97, s97, 1\n\t"
        "s_cmp_lt_u32 s97, 0x200000\n\t"
        "s_cbranch_scc0 .Lgu%=\n\t"
        "s_waitcnt vmcnt(0)\n\t"
        "s_branch .Lo17%=\n\t"
        ".Lo18%=:\n\t"
        "v_cvt_pk_bf16_f32 v198, 0, v198\n\t"
        "v_cvt_pk_bf16_f32 v190, 0, v190\n\t"
        "v_cvt_pk_bf16_f32 v191, 0, v191\n\t"
        "v_cvt_pk_bf16_f32 v192, 0, v192\n\t"
        "v_cvt_pk_bf16_f32 v193, 0, v193\n\t"
        "v_lshlrev_b32_e32 v186, 16, v180\n\t"
        "v_lshlrev_b32_e32 v194, 16, v176\n\t"
        "v_and_b32_e32 v187, 0xffff0000, v180\n\t"
        "v_and_b32_e32 v195, 0xffff0000, v176\n\t"
        "v_lshlrev_b32_e32 v188, 16, v181\n\t"
        "v_lshlrev_b32_e32 v196, 16, v177\n\t"
        "v_and_b32_e32 v189, 0xffff0000, v181\n\t"
        "v_and_b32_e32 v197, 0xffff0000, v177\n\t"
        "v_mul_f32_e32 v186, v186, v194\n\t"
        "v_mul_f32_e32 v187, v187, v195\n\t"
        "v_mul_f32_e32 v188, v188, v196\n\t"
        "v_mul_f32_e32 v189, v189, v197\n\t"
        "v_mul_f32_e32 v190, v198, v190\n\t"
        "v_mul_f32_e32 v191, v198, v191\n\t"
        "v_mul_f32_e32 v192, v198, v192\n\t"
        "v_mul_f32_e32 v193, v198, v193\n\t"
        "v_cvt_pk_bf16_f32 v186, 0, v186\n\t"
        "v_cvt_pk_bf16_f32 v187, 0, v187\n\t"
        "v_cvt_pk_bf16_f32 v188, 0, v188\n\t"
        "v_cvt_pk_bf16_f32 v189, 0, v189\n\t"
        "v_cvt_pk_bf16_f32 v190, 0, v190\n\t"
        "v_cvt_pk_bf16_f32 v191, 0, v191\n\t"
        "v_cvt_pk_bf16_f32 v192, 0, v192\n\t"
        "v_cvt_pk_bf16_f32 v193, 0, v193\n\t"
        "v_mul_f32_e32 v186, v184, v186\n\t"
        "v_mul_f32_e32 v187, v184, v187\n\t"
        "v_mul_f32_e32 v188, v184, v188\n\t"
        "v_mul_f32_e32 v189, v184, v189\n\t"
        "v_cvt_pk_bf16_f32 v186, 0, v186\n\t"
        "v_cvt_pk_bf16_f32 v187, 0, v187\n\t"
        "v_cvt_pk_bf16_f32 v188, 0, v188\n\t"
        "v_cvt_pk_bf16_f32 v189, 0, v189\n\t"
        "v_add_f32_e32 v186, v186, v190\n\t"
        "v_add_f32_e32 v187, v187, v191\n\t"
        "v_add_f32_e32 v188, v188, v192\n\t"
        "v_add_f32_e32 v189, v189, v193\n\t"
        "v_cvt_pk_bf16_f32 v200, v186, v187\n\t"
        "v_cvt_pk_bf16_f32 v201, v188, v189\n\t"
        ".Lo19%=:\n\t"
        "buffer_store_dwordx2 v[200:201], v245, %[ro], s83 offen\n\t"
        "s_add_i32 s80, s38, 1\n\t"
        "s_lshl_b32 s80, s80, 15\n\t"
        "s_or_b32 s81, s38, %[snd]\n\t"
        "s_cmp_lt_i32 s81, 0\n\t"
        "s_cselect_b32 s83, s36, s80\n\t"
        "s_cbranch_scc1 .Lo22%=\n\t"
        "s_mov_b32 s97, 0\n\t"
        ".Lo20%=:\n\t"
        "v_xor_b32_e32 v186, %[tag], v169\n\t"
        "v_xor_b32_e32 v187, %[tag], v171\n\t"
        "v_xor_b32_e32 v188, %[tag], v173\n\t"
        "v_or3_b32 v186, v186, v187, v188\n\t"
        "v_xor_b32_e32 v187, %[tag], v175\n\t"
        "v_or_b32_e32 v186, v186, v187\n\t"
        "v_cmp_ne_u32_e32 vcc, 0, v186\n\t"
        "v_sub_f32_e32 v198, 1.0, v185\n\t"
        "v_add_f32_e32 v190, v148, v168\n\t"
        "v_add_f32_e32 v191, v149, v170\n\t"
        "v_add_f32_e32 v192, v150, v172\n\t"
        "v_add_f32_e32 v193, v151, v174\n\t"
        "s_cbranch_vccz .Lo21%=\n\t"
        "s_sleep %c[xslp]\n\t"
        "s_lshl_b32 s82, s83, 2\n\t"
        "buffer_load_dwordx4 v[168:171], v246, %[rd], s82 offen offset:0 sc1\n\t"
        "buffer_load_dwordx4 v[172:175], v246, %[rd], s82 offen offset:1024 sc1\n\t"
        "s_add_u32 s97, s97, 1\n\t"
        "s_cmp_lt_u32 s97, 0x200000\n\t"
        "s_cbranch_scc0 .Lgu%=\n\t"
        "s_waitcnt vmcnt(0)\n\t"
        "s_branch .Lo20%=\n\t"
        ".Lo21%=:\n\t"
        "v_cvt_pk_bf16_f32 v198, 0, v198\n\t"
        "v_cvt_pk_bf16_f32 v190, 0, v190\n\t"
        "v_cvt_pk_bf16_f32 v191, 0, v191\n\t"
        "v_cvt_pk_bf16_f32 v192, 0, v192\n\t"
        "v_cvt_pk_bf16_f32 v193, 0, v193\n\t"
        "v_lshlrev_b32_e32 v186, 16, v182\n\t"
        "v_lshlrev_b32_e32 v194, 16, v178\n\t"
        "v_and_b32_e32 v187, 0xffff0000, v182\n\t"
        "v_and_b32_e32 v195, 0xffff0000, v178\n\t"
        "v_lshlrev_b32_e32 v188, 16, v183\n\t"
        "v_lshlrev_b32_e32 v196, 16, v179\n\t"
        "v_and_b32_e32 v189, 0xffff0000, v183\n\t"
        "v_and_b32_e32 v197, 0xffff0000, v179\n\t"
        "v_mul_f32_e32 v186, v186, v194\n\t"
        "v_mul_f32_e32 v187, v187, v195\n\t"
        "v_mul_f32_e32 v188, v188, v196\n\t"
        "v_mul_f32_e32 v189, v189, v197\n\t"
        "v_mul_f32_e32 v190, v198, v190\n\t"
        "v_mul_f32_e32 v191, v198, v191\n\t"
        "v_mul_f32_e32 v192, v198, v192\n\t"
        "v_mul_f32_e32 v193, v198, v193\n\t"
        "v_cvt_pk_bf16_f32 v186, 0, v186\n\t"
        "v_cvt_pk_bf16_f32 v187, 0, v187\n\t"
        "v_cvt_pk_bf16_f32 v188, 0, v188\n\t"
        "v_cvt_pk_bf16_f32 v189, 0, v189\n\t"
        "v_cvt_pk_bf16_f32 v190, 0, v190\n\t"
        "v_cvt_pk_bf16_f32 v191, 0, v191\n\t"
        "v_cvt_pk_bf16_f32 v192, 0, v192\n\t"
        "v_cvt_pk_bf16_f32 v193, 0, v193\n\t"
        "v_mul_f32_e32 v186, v185, v186\n\t"
        "v_mul_f32_e32 v187, v185, v187\n\t"
        "v_mul_f32_e32 v188, v185, v188\n\t"
        "v_mul_f32_e32 v189, v185, v189\n\t"
        "v_cvt_pk_bf16_f32 v186, 0, v186\n\t"
        "v_cvt_pk_bf16_f32 v187, 0, v187\n\t"
        "v_cvt_pk_bf16_f32 v188, 0, v188\n\t"
        "v_cvt_pk_bf16_f32 v189, 0, v189\n\t"
        "v_add_f32_e32 v186, v186, v190\n\t"
        "v_add_f32_e32 v187, v187, v191\n\t"
        "v_add_f32_e32 v188, v188, v192\n\t"
        "v_add_f32_e32 v189, v189, v193\n\t"
        "v_cvt_pk_bf16_f32 v200, v186, v187\n\t"
        "v_cvt_pk_bf16_f32 v201, v188, v189\n\t"
        ".Lo22%=:\n\t"
        "buffer_store_dwordx2 v[200:201], v245, %[ro], s83 offen\n\t"
        "s_add_i32 s80, s44, 0\n\t"
        "s_lshl_b32 s80, s80, 6\n\t"
        "s_or_b32 s81, s44, %[snd]\n\t"
        "s_cmp_lt_i32 s81, 0\n\t"
        "s_cselect_b32 s82, s36, s80\n\t"
        "buffer_load_dword v184, v244, %[rd], s82 offen sc1\n\t"
        "s_lshl_b32 s80, s80, 1\n\t"
        "s_cmp_lt_i32 s81, 0\n\t"
        "s_cselect_b32 s82, s36, s80\n\t"
        "buffer_load_dwordx2 v[180:181], v243, %[rd], s82 offen sc1\n\t"
        "s_lshl_b32 s80, s80, 8\n\t"
        "s_cmp_lt_i32 s81, 0\n\t"
        "s_cselect_b32 s82, s36, s80\n\t"
        "buffer_load_dwordx2 v[176:177], v245, %[rx], s82 offen\n\t"
        "s_add_i32 s80, s44, 1\n\t"
        "s_lshl_b32 s80, s80, 6\n\t"
        "s_or_b32 s81, s44, %[snd]\n\t"
        "s_cmp_lt_i32 s81, 0\n\t"
        "s_cselect_b32 s82, s36, s80\n\t"
        "buffer_load_dword v185, v244, %[rd], s82 offen sc1\n\t"
        "s_lshl_b32 s80, s80, 1\n\t"
        "s_cmp_lt_i32 s81, 0\n\t"
        "s_cselect_b32 s82, s36, s80\n\t"
        "buffer_load_dwordx2 v[182:183], v243, %[rd], s82 offen sc1\n\t"
        "s_lshl_b32 s80, s80, 8\n\t"
        "s_cmp_lt_i32 s81, 0\n\t"
        "s_cselect_b32 s82, s36, s80\n\t"
        "buffer_load_dwordx2 v[178:179], v245, %[rx], s82 offen\n\t"
        "s_sleep %c[ydel]\n\t"
        "s_add_i32 s80, s44, 0\n\t"
        "s_lshl_b32 s80, s80, 17\n\t"
        "s_or_b32 s81, s44, %[snd]\n\t"
        "s_cmp_lt_i32 s81, 0\n\t"
        "s_cselect_b32 s82, s36, s80\n\t"
        "buffer_load_dwordx4 v[160:163], v246, %[rd], s82 offen offset:0 sc1\n\t"
        "buffer_load_dwordx4 v[164:167], v246, %[rd], s82 offen offset:1024 sc1\n\t"
        "s_add_i32 s80, s44, 1\n\t"
        "s_lshl_b32 s80, s80, 17\n\t"
        "s_or_b32 s81, s44, %[snd]\n\t"
        "s_cmp_lt_i32 s81, 0\n\t"
        "s_cselect_b32 s82, s36, s80\n\t"
        "buffer_load_dwordx4 v[168:171], v246, %[rd], s82 offen offset:0 sc1\n\t"
        "buffer_load_dwordx4 v[172:175], v246, %[rd], s82 offen offset:1024 sc1\n\t"
        "s_mov_b32 s38, -1\n\t"
        "v_mov_b32_e32 v152, v128\n\t"
        "v_mov_b32_e32 v153, v129\n\t"
        "v_mov_b32_e32 v154, v130\n\t"
        "v_mov_b32_e32 v155, v131\n\t"
        "v_mov_b32_e32 v156, v132\n\t"
        "v_mov_b32_e32 v157, v133\n\t"
        "v_mov_b32_e32 v158, v134\n\t"
        "v_mov_b32_e32 v159, v135\n\t"
        "s_mov_b32 s39, s44\n\t"
        "s_add_u32 s37, s37, 1\n\t"
        "s_add_u32 s44, s44, 8\n\t"
        "s_sub_u32 s43, 33280, s43\n\t"
        "s_branch .Lnew%=\n\t"
        ".Lsol%=:\n\t"
        "s_xor_b32 s44, %[rho], 122\n\t"
        "s_mov_b32 s37, 30\n\t"
        ".Lone%=:\n\t"
        ".Lhk0%=:\n\t"
        "s_cbranch_execz .Lhk1%=\n\t"
        "s_add_i32 s80, s44, 0\n\t"
        "s_lshl_b32 s80, s80, 6\n\t"
        "s_or_b32 s81, s44, %[snd]\n\t"
        "s_cmp_lt_i32 s81, 0\n\t"
        "s_cselect_b32 s82, s36, s80\n\t"
        "buffer_load_dword v184, v244, %[rd], s82 offen sc1\n\t"
        "s_lshl_b32 s80, s80, 1\n\t"
        "s_cmp_lt_i32 s81, 0\n\t"
        ".Lhk1%=:\n\t"
        "s_cbranch_execz .Lhk2%=\n\t"
        "s_cselect_b32 s82, s36, s80\n\t"
        "buffer_load_dwordx2 v[180:181], v243, %[rd], s82 offen sc1\n\t"
        "s_lshl_b32 s80, s80, 8\n\t"
        "s_cmp_lt_i32 s81, 0\n\t"
        "s_cselect_b32 s82, s36, s80\n\t"
        "buffer_load_dwordx2 v[176:177], v245, %[rx], s82 offen\n\t"
        "v_add_u32_e32 v222, s43, v247\n\t"
        "v_add_u32_e32 v223, s43, v248\n\t"
        ".Lhk2%=:\n\t"
        "s_cbranch_execz .Lhk3%=\n\t"
        "s_mov_b32 s97, 0\n\t"
        ".Lo23%=:\n\t"
        "s_lshr_b32 s80, s40, s37\n\t"
        "s_andn2_b32 s80, 1, s80\n\t"
        "s_cbranch_scc0 .Lo24%=\n\t"
        "s_cmp_eq_u32 s97, 0\n\t"
        "s_cbranch_scc1 .Lo23%=x\n\t"
        "s_sleep %c[oslp]\n\t"
        ".Lo23%=x:\n\t"
        "buffer_load_dword v220, v242, %[rd], 0 offen sc1\n\t"
        ".Lhk3%=:\n\t"
        "s_cbranch_execz .Lhk4%=\n\t"
        "s_add_u32 s97, s97, 1\n\t"
        "s_cmp_lt_u32 s97, 0x200000\n\t"
        "s_cbranch_scc0 .Lgu%=\n\t"
        "s_waitcnt vmcnt(0)\n\t"
        "v_cmp_eq_u32_e32 vcc, 32, v220\n\t"
        "s_nop 1\n\t"
        "s_or_b32 s40, s40, vcc_lo\n\t"
        "s_branch .Lo23%=\n\t"
        ".Lo24%=:\n\t"
        ".Lhk4%=:\n\t"
        "s_cbranch_execz .Lhk5%=\n\t"
        "s_lshr_b32 s84, s44, 1\n\t"
        "s_lshl_b32 s84, s84, 18\n\t"
        "s_and_b32 s80, s44, 1\n\t"
        "s_lshl_b32 s80, s80, 12\n\t"
        "s_or_b32 s84, s84, s80\n\t"
        "buffer_load_dwordx4 v[0:3], v240, %[rd], s84 offen offset:0 sc1\n\t"
        "buffer_load_dwordx4 v[4:7], v240, %[rd], s84 offen offset:1024 sc1\n\t"
        "buffer_load_dwordx4 v[8:11], v240, %[rd], s84 offen offset:2048 sc1\n\t"
        ".Lhk5%=:\n\t"
        "s_cbranch_execz .Lhk6%=\n\t"
        "buffer_load_dwordx4 v[12:15], v240, %[rd], s84 offen offset:3072 sc1\n\t"
        "s_add_u32 s84, s84, 8192\n\t"
        "buffer_load_dwordx4 v[16:19], v240, %[rd], s84 offen offset:0 sc1\n\t"
        "buffer_load_dwordx4 v[20:23], v240, %[rd], s84 offen offset:1024 sc1\n\t"
        "buffer_load_dwordx4 v[24:27], v240, %[rd], s84 offen offset:2048 sc1\n\t"
        "buffer_load_dwordx4 v[28:31], v240, %[rd], s84 offen offset:3072 sc1\n\t"
        "s_add_u32 s84, s84, 8192\n\t"
        "buffer_load_dwordx4 v[32:35], v240, %[rd], s84 offen offset:0 sc1\n\t"
        ".Lhk6%=:\n\t"
        "s_cbranch_execz .Lhk7%=\n\t"
        "buffer_load_dwordx4 v[36:39], v240, %[rd], s84 offen offset:1024 sc1\n\t"
        "buffer_load_dwordx4 v[40:43], v240, %[rd], s84 offen offset:2048 sc1\n\t"
        "buffer_load_dwordx4 v[44:47], v240, %[rd], s84 offen offset:3072 sc1\n\t"
        "s_add_u32 s84, s84, 8192\n\t"
        "buffer_load_dwordx4 v[48:51], v240, %[rd], s84 offen offset:0 sc1\n\t"
        "buffer_load_dwordx4 v[52:55], v240, %[rd], s84 offen offset:1024 sc1\n\t"
        "buffer_load_dwordx4 v[56:59], v240, %[rd], s84 offen offset:2048 sc1\n\t"
        "buffer_load_dwordx4 v[60:63], v240, %[rd], s84 offen offset:3072 sc1\n\t"
        ".Lhk7%=:\n\t"
        "s_cbranch_execz .Lhk8%=\n\t"
        "s_sub_u32 s84, s84, 20480\n\t"
        "s_waitcnt vmcnt(15)\n\t"
        "v_mfma_f32_16x16x32_bf16 v[128:131], a[128:131], v[0:3], 0\n\t"
        "v_mfma_f32_16x16x32_bf16 v[132:135], a[132:135], v[0:3], 0\n\t"
        "v_mfma_f32_16x16x32_bf16 v[136:139], a[136:139], v[0:3], 0\n\t"
        "v_mfma_f32_16x16x32_bf16 v[140:143], a[140:143], v[0:3], 0\n\t"
        "s_waitcnt vmcnt(14)\n\t"
        "v_mfma_f32_16x16x32_bf16 v[128:131], a[144:147], v[4:7], v[128:131]\n\t"
        ".Lhk8%=:\n\t"
        "s_cbranch_execz .Lhk9%=\n\t"
        "v_mfma_f32_16x16x32_bf16 v[132:135], a[148:151], v[4:7], v[132:135]\n\t"
        "v_mfma_f32_16x16x32_bf16 v[136:139], a[152:155], v[4:7], v[136:139]\n\t"
        "v_mfma_f32_16x16x32_bf16 v[140:143], a[156:159], v[4:7], v[140:143]\n\t"
        "s_waitcnt vmcnt(13)\n\t"
        "v_mfma_f32_16x16x32_bf16 v[128:131], a[160:163], v[8:11], v[128:131]\n\t"
        "v_mfma_f32_16x16x32_bf16 v[132:135], a[164:167], v[8:11], v[132:135]\n\t"
        "v_mfma_f32_16x16x32_bf16 v[136:139], a[168:171], v[8:11], v[136:139]\n\t"
        "v_mfma_f32_16x16x32_bf16 v[140:143], a[172:175], v[8:11], v[140:143]\n\t"
        ".Lhk9%=:\n\t"
        "s_cbranch_execz .Lhk10%=\n\t"
        "s_waitcnt vmcnt(12)\n\t"
        "v_mfma_f32_16x16x32_bf16 v[128:131], a[176:179], v[12:15], v[128:131]\n\t"
        "v_mfma_f32_16x16x32_bf16 v[132:135], a[180:183], v[12:15], v[132:135]\n\t"
        "v_mfma_f32_16x16x32_bf16 v[136:139], a[184:187], v[12:15], v[136:139]\n\t"
        "v_mfma_f32_16x16x32_bf16 v[140:143], a[188:191], v[12:15], v[140:143]\n\t"
        "s_waitcnt vmcnt(11)\n\t"
        "v_mfma_f32_16x16x32_bf16 v[128:131], a[192:195], v[16:19], v[128:131]\n\t"
        "v_mfma_f32_16x16x32_bf16 v[132:135], a[196:199], v[16:19], v[132:135]\n\t"
        ".Lhk10%=:\n\t"
        "s_cbranch_execz .Lhk11%=\n\t"
        "v_mfma_f32_16x16x32_bf16 v[136:139], a[200:203], v[16:19], v[136:139]\n\t"
        "v_mfma_f32_16x16x32_bf16 v[140:143], a[204:207], v[16:19], v[140:143]\n\t"
        "s_waitcnt vmcnt(10)\n\t"
        "v_mfma_f32_16x16x32_bf16 v[128:131], a[208:211], v[20:23], v[128:131]\n\t"
        "v_mfma_f32_16x16x32_bf16 v[132:135], a[212:215], v[20:23], v[132:135]\n\t"
        "v_mfma_f32_16x16x32_bf16 v[136:139], a[216:219], v[20:23], v[136:139]\n\t"
        "v_mfma_f32_16x16x32_bf16 v[140:143], a[220:223], v[20:23], v[140:143]\n\t"
        "s_waitcnt vmcnt(9)\n\t"
        ".Lhk11%=:\n\t"
        "s_cbranch_execz .Lhk12%=\n\t"
        "v_mfma_f32_16x16x32_bf16 v[128:131], a[224:227], v[24:27], v[128:131]\n\t"
        "v_mfma_f32_16x16x32_bf16 v[132:135], a[228:231], v[24:27], v[132:135]\n\t"
        "v_mfma_f32_16x16x32_bf16 v[136:139], a[232:235], v[24:27], v[136:139]\n\t"
        "v_mfma_f32_16x16x32_bf16 v[140:143], a[236:239], v[24:27], v[140:143]\n\t"
        "s_waitcnt vmcnt(8)\n\t"
        "v_mfma_f32_16x16x32_bf16 v[128:131], a[240:243], v[28:31], v[128:131]\n\t"
        "v_mfma_f32_16x16x32_bf16 v[132:135], a[244:247], v[28:31], v[132:135]\n\t"
        "v_mfma_f32_16x16x32_bf16 v[136:139], a[248:251], v[28:31], v[136:139]\n\t"
        ".Lhk12%=:\n\t"
        "s_cbranch_execz .Lhk13%=\n\t"
        "v_mfma_f32_16x16x32_bf16 v[140:143], a[252:255], v[28:31], v[140:143]\n\t"
        "s_waitcnt vmcnt(7)\n\t"
        "v_mfma_f32_16x16x32_bf16 v[128:131], a[0:3], v[32:35], v[128:131]\n\t"
        "v_mfma_f32_16x16x32_bf16 v[132:135], a[4:7], v[32:35], v[132:135]\n\t"
        "v_mfma_f32_16x16x32_bf16 v[136:139], a[8:11], v[32:35], v[136:139]\n\t"
        "v_mfma_f32_16x16x32_bf16 v[140:143], a[12:15], v[32:35], v[140:143]\n\t"
        "s_waitcnt vmcnt(6)\n\t"
        "v_mfma_f32_16x16x32_bf16 v[128:131], a[16:19], v[36:39], v[128:131]\n\t"
        ".Lhk13%=:\n\t"
        "s_cbranch_execz .Lhk14%=\n\t"
        "v_mfma_f32_16x16x32_bf16 v[132:135], a[20:23], v[36:39], v[132:135]\n\t"
        "v_mfma_f32_16x16x32_bf16 v[136:139], a[24:27], v[36:39], v[136:139]\n\t"
        "v_mfma_f32_16x16x32_bf16 v[140:143], a[28:31], v[36:39], v[140:143]\n\t"
        "s_waitcnt vmcnt(5)\n\t"
        "v_mfma_f32_16x16x32_bf16 v[128:131], a[32:35], v[40:43], v[128:131]\n\t"
        "v_mfma_f32_16x16x32_bf16 v[132:135], a[36:39], v[40:43], v[132:135]\n\t"
        "v_mfma_f32_16x16x32_bf16 v[136:139], a[40:43], v[40:43], v[136:139]\n\t"
        "v_mfma_f32_16x16x32_bf16 v[140:143], a[44:47], v[40:43], v[140:143]\n\t"
        ".Lhk14%=:\n\t"
        "s_cbranch_execz .Lhk15%=\n\t"
        "s_waitcnt vmcnt(4)\n\t"
        "v_mfma_f32_16x16x32_bf16 v[128:131], a[48:51], v[44:47], v[128:131]\n\t"
        "v_mfma_f32_16x16x32_bf16 v[132:135], a[52:55], v[44:47], v[132:135]\n\t"
        "v_mfma_f32_16x16x32_bf16 v[136:139], a[56:59], v[44:47], v[136:139]\n\t"
        "v_mfma_f32_16x16x32_bf16 v[140:143], a[60:63], v[44:47], v[140:143]\n\t"
        "s_waitcnt vmcnt(3)\n\t"
        "v_mfma_f32_16x16x32_bf16 v[128:131], a[64:67], v[48:51], v[128:131]\n\t"
        "v_mfma_f32_16x16x32_bf16 v[132:135], a[68:71], v[48:51], v[132:135]\n\t"
        ".Lhk15%=:\n\t"
        "s_cbranch_execz .Lhk16%=\n\t"
        "v_mfma_f32_16x16x32_bf16 v[136:139], a[72:75], v[48:51], v[136:139]\n\t"
        "v_mfma_f32_16x16x32_bf16 v[140:143], a[76:79], v[48:51], v[140:143]\n\t"
        "s_waitcnt vmcnt(2)\n\t"
        "v_mfma_f32_16x16x32_bf16 v[128:131], a[80:83], v[52:55], v[128:131]\n\t"
        "v_mfma_f32_16x16x32_bf16 v[132:135], a[84:87], v[52:55], v[132:135]\n\t"
        "v_mfma_f32_16x16x32_bf16 v[136:139], a[88:91], v[52:55], v[136:139]\n\t"
        "v_mfma_f32_16x16x32_bf16 v[140:143], a[92:95], v[52:55], v[140:143]\n\t"
        "s_waitcnt vmcnt(1)\n\t"
        ".Lhk16%=:\n\t"
        "s_cbranch_execz .Lhk17%=\n\t"
        "v_mfma_f32_16x16x32_bf16 v[128:131], a[96:99], v[56:59], v[128:131]\n\t"
        "v_mfma_f32_16x16x32_bf16 v[132:135], a[100:103], v[56:59], v[132:135]\n\t"
        "v_mfma_f32_16x16x32_bf16 v[136:139], a[104:107], v[56:59], v[136:139]\n\t"
        "v_mfma_f32_16x16x32_bf16 v[140:143], a[108:111], v[56:59], v[140:143]\n\t"
        "s_waitcnt vmcnt(0)\n\t"
        "v_mfma_f32_16x16x32_bf16 v[128:131], a[112:115], v[60:63], v[128:131]\n\t"
        "v_mfma_f32_16x16x32_bf16 v[132:135], a[116:119], v[60:63], v[132:135]\n\t"
        "v_mfma_f32_16x16x32_bf16 v[136:139], a[120:123], v[60:63], v[136:139]\n\t"
        ".Lhk17%=:\n\t"
        "s_cbranch_execz .Lhk18%=\n\t"
        "v_mfma_f32_16x16x32_bf16 v[140:143], a[124:127], v[60:63], v[140:143]\n\t"
        "s_nop 4\n\t"
        "ds_write_b128 v222, v[128:131] offset:0\n\t"
        "ds_write_b128 v222, v[132:135] offset:1040\n\t"
        "ds_write_b128 v222, v[136:139] offset:2080\n\t"
        "ds_write_b128 v222, v[140:143] offset:3120\n\t"
        "s_waitcnt lgkmcnt(0)\n\t"
        "s_barrier\n\t"
        ".Lhk18%=:\n\t"
        "s_cbranch_execz .Lhk19%=\n\t"
        "ds_read_b128 v[186:189], v223 offset:0\n\t"
        "ds_read_b128 v[190:193], v223 offset:8320\n\t"
        "ds_read_b128 v[194:197], v223 offset:16640\n\t"
        "ds_read_b128 v[198:201], v223 offset:24960\n\t"
        "s_waitcnt lgkmcnt(0)\n\t"
        "v_add_f32_e32 v186, v186, v190\n\t"
        "v_add_f32_e32 v187, v187, v191\n\t"
        "v_add_f32_e32 v188, v188, v192\n\t"
        ".Lhk19%=:\n\t"
        "s_cbranch_execz .Lhk20%=\n\t"
        "v_add_f32_e32 v189, v189, v193\n\t"
        "v_add_f32_e32 v194, v194, v198\n\t"
        "v_add_f32_e32 v195, v195, v199\n\t"
        "v_add_f32_e32 v196, v196, v200\n\t"
        "v_add_f32_e32 v197, v197, v201\n\t"
        "v_add_f32_e32 v204, v186, v194\n\t"
        "v_add_f32_e32 v206, v187, v195\n\t"
        "v_add_f32_e32 v208, v188, v196\n\t"
        ".Lhk20%=:\n\t"
        "s_cbranch_execz .Lhk21%=\n\t"
        "v_add_f32_e32 v210, v189, v197\n\t"
        "s_add_i32 s80, s44, 0\n\t"
        "s_lshl_b32 s80, s80, 17\n\t"
        "s_or_b32 s81, s44, %[own]\n\t"
        "s_cmp_lt_i32 s81, 0\n\t"
        "s_cselect_b32 s82, s36, s80\n\t"
        "buffer_store_dwordx4 v[204:207], v246, %[rd], s82 offen offset:0 sc1\n\t"
        "buffer_store_dwordx4 v[208:211], v246, %[rd], s82 offen offset:1024 sc1\n\t"
        ".Lhk21%=:\n\t"
        "s_cbranch_execz .Lhk22%=\n\t"
        "v_mov_b32_e32 v144, v204\n\t"
        "v_mov_b32_e32 v145, v206\n\t"
        "v_mov_b32_e32 v146, v208\n\t"
        "v_mov_b32_e32 v147, v210\n\t"
        "s_sleep %c[xdel]\n\t"
        "s_add_i32 s80, s44, 0\n\t"
        "s_lshl_b32 s80, s80, 17\n\t"
        "s_or_b32 s81, s44, %[snd]\n\t"
        ".Lhk22%=:\n\t"
        "s_cbranch_execz .Lhk23%=\n\t"
        "s_cmp_lt_i32 s81, 0\n\t"
        "s_cselect_b32 s82, s36, s80\n\t"
        "buffer_load_dwordx4 v[160:163], v246, %[rd], s82 offen offset:0 sc1\n\t"
        "buffer_load_dwordx4 v[164:167], v246, %[rd], s82 offen offset:1024 sc1\n\t"
        "s_waitcnt vmcnt(0)\n\t"
        "s_add_i32 s80, s44, 0\n\t"
        "s_lshl_b32 s80, s80, 15\n\t"
        "s_or_b32 s81, s44, %[snd]\n\t"
        ".Lhk23%=:\n\t"
        "s_cbranch_execz .Lhk24%=\n\t"
        "s_cmp_lt_i32 s81, 0\n\t"
        "s_cselect_b32 s83, s36, s80\n\t"
        "s_cbranch_scc1 .Lo27%=\n\t"
        "s_mov_b32 s97, 0\n\t"
        ".Lo25%=:\n\t"
        "v_xor_b32_e32 v186, %[tag], v161\n\t"
        "v_xor_b32_e32 v187, %[tag], v163\n\t"
        "v_xor_b32_e32 v188, %[tag], v165\n\t"
        "v_or3_b32 v186, v186, v187, v188\n\t"
        ".Lhk24%=:\n\t"
        "s_cbranch_execz .Lhk25%=\n\t"
        "v_xor_b32_e32 v187, %[tag], v167\n\t"
        "v_or_b32_e32 v186, v186, v187\n\t"
        "v_cmp_ne_u32_e32 vcc, 0, v186\n\t"
        "v_sub_f32_e32 v198, 1.0, v184\n\t"
        "v_add_f32_e32 v190, v144, v160\n\t"
        "v_add_f32_e32 v191, v145, v162\n\t"
        "v_add_f32_e32 v192, v146, v164\n\t"
        "v_add_f32_e32 v193, v147, v166\n\t"
        ".Lhk25%=:\n\t"
        "s_cbranch_execz .Lhk26%=\n\t"
        "s_cbranch_vccz .Lo26%=\n\t"
        "s_sleep %c[xslp]\n\t"
        "s_lshl_b32 s82, s83, 2\n\t"
        "buffer_load_dwordx4 v[160:163], v246, %[rd], s82 offen offset:0 sc1\n\t"
        "buffer_load_dwordx4 v[164:167], v246, %[rd], s82 offen offset:1024 sc1\n\t"
        "s_add_u32 s97, s97, 1\n\t"
        "s_cmp_lt_u32 s97, 0x200000\n\t"
        "s_cbranch_scc0 .Lgu%=\n\t"
        ".Lhk26%=:\n\t"
        "s_cbranch_execz .Lhk27%=\n\t"
        "s_waitcnt vmcnt(0)\n\t"
        "s_branch .Lo25%=\n\t"
        ".Lo26%=:\n\t"
        "v_cvt_pk_bf16_f32 v198, 0, v198\n\t"
        "v_cvt_pk_bf16_f32 v190, 0, v190\n\t"
        "v_cvt_pk_bf16_f32 v191, 0, v191\n\t"
        "v_cvt_pk_bf16_f32 v192, 0, v192\n\t"
        "v_cvt_pk_bf16_f32 v193, 0, v193\n\t"
        "v_lshlrev_b32_e32 v186, 16, v180\n\t"
        ".Lhk27%=:\n\t"
        "s_cbranch_execz .Lhk28%=\n\t"
        "v_lshlrev_b32_e32 v194, 16, v176\n\t"
        "v_and_b32_e32 v187, 0xffff0000, v180\n\t"
        "v_and_b32_e32 v195, 0xffff0000, v176\n\t"
        "v_lshlrev_b32_e32 v188, 16, v181\n\t"
        "v_lshlrev_b32_e32 v196, 16, v177\n\t"
        "v_and_b32_e32 v189, 0xffff0000, v181\n\t"
        "v_and_b32_e32 v197, 0xffff0000, v177\n\t"
        "v_mul_f32_e32 v186, v186, v194\n\t"
        ".Lhk28%=:\n\t"
        "s_cbranch_execz .Lhk29%=\n\t"
        "v_mul_f32_e32 v187, v187, v195\n\t"
        "v_mul_f32_e32 v188, v188, v196\n\t"
        "v_mul_f32_e32 v189, v189, v197\n\t"
        "v_mul_f32_e32 v190, v198, v190\n\t"
        "v_mul_f32_e32 v191, v198, v191\n\t"
        "v_mul_f32_e32 v192, v198, v192\n\t"
        "v_mul_f32_e32 v193, v198, v193\n\t"
        "v_cvt_pk_bf16_f32 v186, 0, v186\n\t"
        ".Lhk29%=:\n\t"
        "s_cbranch_execz .Lhk30%=\n\t"
        "v_cvt_pk_bf16_f32 v187, 0, v187\n\t"
        "v_cvt_pk_bf16_f32 v188, 0, v188\n\t"
        "v_cvt_pk_bf16_f32 v189, 0, v189\n\t"
        "v_cvt_pk_bf16_f32 v190, 0, v190\n\t"
        "v_cvt_pk_bf16_f32 v191, 0, v191\n\t"
        "v_cvt_pk_bf16_f32 v192, 0, v192\n\t"
        "v_cvt_pk_bf16_f32 v193, 0, v193\n\t"
        "v_mul_f32_e32 v186, v184, v186\n\t"
        ".Lhk30%=:\n\t"
        "s_cbranch_execz .Lhk31%=\n\t"
        "v_mul_f32_e32 v187, v184, v187\n\t"
        "v_mul_f32_e32 v188, v184, v188\n\t"
        "v_mul_f32_e32 v189, v184, v189\n\t"
        "v_cvt_pk_bf16_f32 v186, 0, v186\n\t"
        "v_cvt_pk_bf16_f32 v187, 0, v187\n\t"
        "v_cvt_pk_bf16_f32 v188, 0, v188\n\t"
        "v_cvt_pk_bf16_f32 v189, 0, v189\n\t"
        "v_add_f32_e32 v186, v186, v190\n\t"
        ".Lhk31%=:\n\t"
        "s_cbranch_execz .Lhk32%=\n\t"
        "v_add_f32_e32 v187, v187, v191\n\t"
        "v_add_f32_e32 v188, v188, v192\n\t"
        "v_add_f32_e32 v189, v189, v193\n\t"
        "v_cvt_pk_bf16_f32 v200, v186, v187\n\t"
        "v_cvt_pk_bf16_f32 v201, v188, v189\n\t"
        ".Lo27%=:\n\t"
        "buffer_store_dwordx2 v[200:201], v245, %[ro], s83 offen\n\t"
        "s_add_u32 s37, s37, 1\n\t"
        "s_xor_b32 s44, s44, 6\n\t"
        ".Lhk32%=:\n\t"
        "s_cbranch_execz .Lhk33%=\n\t"
        "s_sub_u32 s43, 33280, s43\n\t"
        "s_cmp_lt_u32 s37, 32\n\t"
        "s_cbranch_scc1 .Lone%=\n\t"
        "buffer_store_dword v205, off, %[rd], %[ep] sc1\n\t"
        "s_endpgm\n\t"
        ".Lo13%=:\n\t"
        "s_mov_b32 s97, 0\n\t"
        "s_lshl_b32 s82, s83, 2\n\t"
        ".Lo13%=r:\n\t"
        "s_sleep %c[xslp]\n\t"
        "buffer_load_dwordx4 v[160:163], v246, %[rd], s82 offen offset:0 sc1\n\t"
        "buffer_load_dwordx4 v[164:167], v246, %[rd], s82 offen offset:1024 sc1\n\t"
        "s_add_u32 s97, s97, 1\n\t"
        "s_cmp_lt_u32 s97, 0x200000\n\t"
        "s_cbranch_scc0 .Lgu%=\n\t"
        "s_waitcnt vmcnt(0)\n\t"
        "v_xor_b32_e32 v224, %[tag], v161\n\t"
        "v_xor_b32_e32 v225, %[tag], v163\n\t"
        "v_xor_b32_e32 v226, %[tag], v165\n\t"
        "v_or3_b32 v224, v224, v225, v226\n\t"
        "v_xor_b32_e32 v225, %[tag], v167\n\t"
        "v_or_b32_e32 v224, v224, v225\n\t"
        "v_cmp_ne_u32_e32 vcc, 0, v224\n\t"
        "s_nop 3\n\t"
        "s_cbranch_vccnz .Lo13%=r\n\t"
        "s_branch .Lo14%=\n\t"
        ".Lo15%=:\n\t"
        "s_mov_b32 s97, 0\n\t"
        "s_lshl_b32 s82, s83, 2\n\t"
        ".Lo15%=r:\n\t"
        "s_sleep %c[xslp]\n\t"
        "buffer_load_dwordx4 v[168:171], v246, %[rd], s82 offen offset:0 sc1\n\t"
        "buffer_load_dwordx4 v[172:175], v246, %[rd], s82 offen offset:1024 sc1\n\t"
        "s_add_u32 s97, s97, 1\n\t"
        "s_cmp_lt_u32 s97, 0x200000\n\t"
        "s_cbranch_scc0 .Lgu%=\n\t"
        "s_waitcnt vmcnt(0)\n\t"
        "v_xor_b32_e32 v224, %[tag], v169\n\t"
        "v_xor_b32_e32 v225, %[tag], v171\n\t"
        "v_xor_b32_e32 v226, %[tag], v173\n\t"
        "v_or3_b32 v224, v224, v225, v226\n\t"
        "v_xor_b32_e32 v225, %[tag], v175\n\t"
        "v_or_b32_e32 v224, v224, v225\n\t"
        "v_cmp_ne_u32_e32 vcc, 0, v224\n\t"
        "s_nop 3\n\t"
        "s_cbranch_vccnz .Lo15%=r\n\t"
        "s_branch .Lo16%=\n\t"
        ".Lgu%=:\n\t"
        "v_mov_b32_e32 v0, 128\n\t"
        "s_mov_b32 s80, %c[ferr]\n\t"
        "buffer_store_dword v0, off, %[rd], s80 sc1\n\t"
        "s_endpgm\n\t"
        ".Lhk33%=:\n\t"
        "s_branch .Lhr%="
        :: [rd] "s"(rd), [ro] "s"(ro), [rx] "s"(rx), [w] "s"(w), [rho] "s"(rho), [tag] "s"(tag), [own] "s"(own), [snd] "s"(snd), [ep] "s"(ep), [blk] "s"(j), [t0] "s"(g_t0),
          [ferr] "i"(F_ERR), [fprep] "i"(F_PREP), [fconv] "i"(F_CONV), [fprog] "i"(F_PROG), [dbg] "i"(W_DBG), [oslp] "i"(O_SLP), [xslp] "i"(X_SLP), [xdel] "i"(X_DEL), [ydel] "i"(Y_DEL), "{v[176:191]}"(pc)
        : "memory", "vcc", "scc", "s36", "s37", "s38", "s39", "s40", "s41", "s42", "s43", "s44", "s45", "s46", "s47", "s80", "s81", "s82", "s83", "s84", "s85", "s86", "s87", "s88", "s89", "s90", "s91", "s92", "s93", "s94", "s95", "s96", "s97", "s98", "s99", WO_REGS);
}

__device__ __forceinline__ void bulk_block(const Params& p, rsrc_t r, unsigned char* lds, const int wv, const int j, const unsigned tag, const unsigned g_t0) {
    const int x = (j & 7) >> 1, i32 = (j >> 3) * 2 + (j & 1);
    auto flags = [&](int mt, int nmt, int h0, int nh, int mat) {
        DRAIN(); __syncthreads();
        const int tid = otid(); if (tid < nmt * nh) st4(r, F_KVQ + ((mt + tid / nh) * 32 + h0 + tid % nh) * 64 + mat * 4, tag); };
#pragma unroll 1
    for (int t = 0; t < 2; ++t) {
        const int mt = 2 * t + (i32 & 1), mat = (i32 >> 1) & 1, hd = 8 * x + (i32 >> 2), rot = ((i32 >> 1) & 7) * 2;
        const bf16_t* Bm = mat ? p.Wv : p.Wk;
        if (mat) gemm<1, 4, 4, 8>(r, p.Aq, Bm, mt * 128, hd * 128, lds, W_VF, wv, rot, nullptr, t == 1, t == 0 ? Bm : nullptr, (mt + 2) * 128, hd * 128);
        else gemm<0, 4, 4, 8>(r, p.Aq, Bm, mt * 128, hd * 128, lds, W_KX, wv, rot, nullptr, t == 1, t == 0 ? Bm : nullptr, (mt + 2) * 128, hd * 128);
        flags(mt, 1, hd, 1, mat);
    }
    const int hp = 4 * x + (i32 >> 3), q8 = i32 & 7, rot = ROT2 * (i32 >> 2);
    if (SKEW) wait_long(rtnow() + (unsigned)((q8 & 3) * 4 + x) * SKEW);
    {
        const int m = q8 < 4 ? 4 + (q8 & 1) : q8 - 4, mat = q8 < 4 ? q8 >> 1 : 2;
        const bf16_t* Bn = mat == 2 ? p.Wq : mat == 1 ? p.Wv : p.Wk;
        const int mt = 4 + (q8 & 3), mv = q8 >> 2;
        const bf16_t* tk = (wv == 1 && j < 32) ? p.Aq + (64 + j) * 16384 : nullptr;
        if (mv) gemm<1, 4, 8, 4, 0, NoHook, 8>(r, p.Aq, p.Wv, mt * 128, hp * 256, lds, W_VF, wv, rot, tk, false, Bn, m * 256, hp * 256);
        else gemm<0, 4, 8, 4, 0, NoHook, 8>(r, p.Aq, p.Wk, mt * 128, hp * 256, lds, W_KX, wv, rot, tk, false, Bn, m * 256, hp * 256);
        flags(mt, 1, 2 * hp, 2, mv);
        const bf16_t* tl = (wv == 1 && j < 32) ? p.Aq + (96 + j) * 16384 : nullptr;
        if (mat == 1) gemm<1, 8, 8, 4>(r, p.Aq, Bn, m * 256, hp * 256, lds, W_VF, wv, rot, tl, true);
        else gemm<0, 8, 8, 4>(r, p.Aq, Bn, m * 256, hp * 256, lds, mat == 2 ? W_QX : W_KX, wv, rot, tl, true);
        flags(2 * m, 2, 2 * hp, 2, mat);
    }
    if (CVH == 4 && !conv_block(p, r, 128 + j, 1, lds, wv, j, tag, 0u)) return;
    {
        unsigned wi = 0u;
        const int l = otid() & 63; const v4u sw = mk_srd(p.Wo, 4096 * 1024 * 2); const unsigned wa = (unsigned)(((i32 & 1) * 2048 + wv * 512 + (l >> 3)) * 2048 + (i32 >> 1) * 128 + (l & 7) * 16);
        unsigned long long tab;
        asm volatile("s_getpc_b64 %0\n\ts_branch .Lwo%=\n\t"
                     "buffer_load_dwordx4 a[128:131], v191, s[92:95], s91 offen\n\ts_setpc_b64 s[96:97]\n\t" "buffer_load_dwordx4 a[132:135], v191, s[92:95], s91 offen\n\ts_setpc_b64 s[96:97]\n\t"
                     "buffer_load_dwordx4 a[136:139], v191, s[92:95], s91 offen\n\ts_setpc_b64 s[96:97]\n\t" "buffer_load_dwordx4 a[140:143], v191, s[92:95], s91 offen\n\ts_setpc_b64 s[96:97]\n\t"
                     "buffer_load_dwordx4 a[144:147], v191, s[92:95], s91 offen\n\ts_setpc_b64 s[96:97]\n\t" "buffer_load_dwordx4 a[148:151], v191, s[92:95], s91 offen\n\ts_setpc_b64 s[96:97]\n\t"
                     "buffer_load_dwordx4 a[152:155], v191, s[92:95], s91 offen\n\ts_setpc_b64 s[96:97]\n\t" "buffer_load_dwordx4 a[156:159], v191, s[92:95], s91 offen\n\ts_setpc_b64 s[96:97]\n\t"
                     "buffer_load_dwordx4 a[160:163], v191, s[92:95], s91 offen\n\ts_setpc_b64 s[96:97]\n\t" "buffer_load_dwordx4 a[164:167], v191, s[92:95], s91 offen\n\ts_setpc_b64 s[96:97]\n\t"
                     "buffer_load_dwordx4 a[168:171], v191, s[92:95], s91 offen\n\ts_setpc_b64 s[96:97]\n\t" "buffer_load_dwordx4 a[172:175], v191, s[92:95], s91 offen\n\ts_setpc_b64 s[96:97]\n\t"
                     "buffer_load_dwordx4 a[176:179], v191, s[92:95], s91 offen\n\ts_setpc_b64 s[96:97]\n\t" "buffer_load_dwordx4 a[180:183], v191, s[92:95], s91 offen\n\ts_setpc_b64 s[96:97]\n\t"
                     "buffer_load_dwordx4 a[184:187], v191, s[92:95], s91 offen\n\ts_setpc_b64 s[96:97]\n\t" "buffer_load_dwordx4 a[188:191], v191, s[92:95], s91 offen\n\ts_setpc_b64 s[96:97]\n\t"
                     "buffer_load_dwordx4 a[192:195], v191, s[92:95], s91 offen\n\ts_setpc_b64 s[96:97]\n\t" "buffer_load_dwordx4 a[196:199], v191, s[92:95], s91 offen\n\ts_setpc_b64 s[96:97]\n\t"
                     "buffer_load_dwordx4 a[200:203], v191, s[92:95], s91 offen\n\ts_setpc_b64 s[96:97]\n\t" "buffer_load_dwordx4 a[204:207], v191, s[92:95], s91 offen\n\ts_setpc_b64 s[96:97]\n\t"
                     "buffer_load_dwordx4 a[208:211], v191, s[92:95], s91 offen\n\ts_setpc_b64 s[96:97]\n\t" "buffer_load_dwordx4 a[212:215], v191, s[92:95], s91 offen\n\ts_setpc_b64 s[96:97]\n\t"
                     "buffer_load_dwordx4 a[216:219], v191, s[92:95], s91 offen\n\ts_setpc_b64 s[96:97]\n\t" "buffer_load_dwordx4 a[220:223], v191, s[92:95], s91 offen\n\ts_setpc_b64 s[96:97]\n\t"
                     "buffer_load_dwordx4 a[224:227], v191, s[92:95], s91 offen\n\ts_setpc_b64 s[96:97]\n\t" "buffer_load_dwordx4 a[228:231], v191, s[92:95], s91 offen\n\ts_setpc_b64 s[96:97]\n\t"
                     "buffer_load_dwordx4 a[232:235], v191, s[92:95], s91 offen\n\ts_setpc_b64 s[96:97]\n\t" "buffer_load_dwordx4 a[236:239], v191, s[92:95], s91 offen\n\ts_setpc_b64 s[96:97]\n\t"
                     "buffer_load_dwordx4 a[240:243], v191, s[92:95], s91 offen\n\ts_setpc_b64 s[96:97]\n\t" "buffer_load_dwordx4 a[244:247], v191, s[92:95], s91 offen\n\ts_setpc_b64 s[96:97]\n\t"
                     "buffer_load_dwordx4 a[248:251], v191, s[92:95], s91 offen\n\ts_setpc_b64 s[96:97]\n\t" "buffer_load_dwordx4 a[252:255], v191, s[92:95], s91 offen\n\ts_setpc_b64 s[96:97]\n\t"
                     "buffer_load_dwordx4 v[192:195], v191, s[92:95], s91 offen\n\ts_setpc_b64 s[96:97]\n\t" "buffer_load_dwordx4 v[196:199], v191, s[92:95], s91 offen\n\ts_setpc_b64 s[96:97]\n\t"
                     "buffer_load_dwordx4 v[200:203], v191, s[92:95], s91 offen\n\ts_setpc_b64 s[96:97]\n\t" "buffer_load_dwordx4 v[204:207], v191, s[92:95], s91 offen\n\ts_setpc_b64 s[96:97]\n\t"
                     "buffer_load_dwordx4 v[208:211], v191, s[92:95], s91 offen\n\ts_setpc_b64 s[96:97]\n\t" "buffer_load_dwordx4 v[212:215], v191, s[92:95], s91 offen\n\ts_setpc_b64 s[96:97]\n\t"
                     "buffer_load_dwordx4 v[216:219], v191, s[92:95], s91 offen\n\ts_setpc_b64 s[96:97]\n\t" "buffer_load_dwordx4 v[220:223], v191, s[92:95], s91 offen\n\ts_setpc_b64 s[96:97]\n\t"
                     "buffer_load_dwordx4 v[224:227], v191, s[92:95], s91 offen\n\ts_setpc_b64 s[96:97]\n\t" "buffer_load_dwordx4 v[228:231], v191, s[92:95], s91 offen\n\ts_setpc_b64 s[96:97]\n\t"
                     "buffer_load_dwordx4 v[232:235], v191, s[92:95], s91 offen\n\ts_setpc_b64 s[96:97]\n\t" "buffer_load_dwordx4 v[236:239], v191, s[92:95], s91 offen\n\ts_setpc_b64 s[96:97]\n\t"
                     "buffer_load_dwordx4 v[240:243], v191, s[92:95], s91 offen\n\ts_setpc_b64 s[96:97]\n\t" "buffer_load_dwordx4 v[244:247], v191, s[92:95], s91 offen\n\ts_setpc_b64 s[96:97]\n\t"
                     "buffer_load_dwordx4 v[248:251], v191, s[92:95], s91 offen\n\ts_setpc_b64 s[96:97]\n\t" "buffer_load_dwordx4 v[252:255], v191, s[92:95], s91 offen\n\ts_setpc_b64 s[96:97]\n\t"
                     "buffer_load_dwordx4 v191, s[92:95], s91 offen lds\n\ts_setpc_b64 s[96:97]\n.Lwo%=:" : "=s"(tab));
        auto hook = [&](int) {
            const unsigned q = min(wi, 63u);
            asm volatile("s_mov_b32 m0, %1\n\ts_swappc_b64 s[96:97], %0"
                         :: "s"(tab + (min(q, 48u) * 12u + 4u)), "s"(WO_L + wv * 16384 + (q - 48u) * 1024u), "{v191}"(wa), "{s[92:95]}"(sw), "{s91}"(q * 16384u) : "memory", "m0", "s96", "s97", WO_REGS);
            ++wi; };
#pragma unroll 1
        for (int t = 0; t < 2; ++t) {
            const int mt = t ? 8 + q8 : 12 + (q8 & 3), mv = t ? 2 : q8 >> 2;
            if (mv == 1) gemm<1, 4, 8, 4, 1>(r, p.Aq, p.Wv, mt * 128, hp * 256, lds, W_VF, wv, rot, nullptr, false, p.Wq, (8 + q8) * 128, hp * 256, hook);
            else gemm<0, 4, 8, 4, 1>(r, p.Aq, t ? p.Wq : p.Wk, mt * 128, hp * 256, lds, t ? W_QX : W_KX, wv, rot, nullptr, t == 1, t ? nullptr : p.Wq, (8 + q8) * 128, hp * 256, hook);
            flags(mt, 1, 2 * hp, 2, mv);
        }
        oproj_block(p, wv, i32, x, j, tag, g_t0);
    }
}

__device__ __forceinline__ void help_block(const Params& p, rsrc_t r, unsigned char* lds, const int wv, const int k, const unsigned tag, const unsigned far) {
    const int j = -32 + k; (void)j;
    {
        const unsigned th = rtnow() + HELP_D + (unsigned)k * SKEWP, OOB = 0xF0000000u;
        const int l = otid() & 63, n16 = l & 15;
        const rsrc_t rh = __builtin_amdgcn_make_buffer_rsrc(p.ws + hx_base(p.ws), 0, U_END * 16384, 0x00020000);
        const unsigned un = (unsigned)(wv * 32 + (k & 30)) * 16384u + (k & 1) * 4096u, ca = l == 0 ? un : OOB;
        v4u z[4]; unsigned best[4] = {~0u, ~0u, ~0u, ~0u};
#pragma unroll
        for (int i = 0; i < 4; ++i) z[i] = (v4u){~0u, ~0u, ~0u, ~0u};
        for (int i = 0; i < CAL_N; ++i)
#pragma unroll
            for (int q = 0; q < 4; ++q) {
                const unsigned t0 = (unsigned)__builtin_amdgcn_s_memtime(); __builtin_amdgcn_s_waitcnt(0);
                st16(rh, ca + q * 8192, z[0]); DRAIN();
                const unsigned t1 = (unsigned)__builtin_amdgcn_s_memtime(); __builtin_amdgcn_s_waitcnt(0);
                best[q] = min(best[q], t1 - t0); }
        const unsigned up = min(best[1], best[3]) < min(best[0], best[2]) ? 1u : 0u, vote = (up ^ far) | ((up ? best[2] < best[0] : best[3] < best[1]) ? 2u : 0u);
        if (wv == 0 && k < 8) {
#pragma unroll
            for (int i = 0; i < 4; ++i) z[i] = __builtin_elementwise_min(*(const v4u*)(p.mem0 + ((n16 & 1) + 2 * (n16 >> 2)) * 1024 + ((k & 3) * 4 + i) * 64 + (l >> 4) * 16 + (n16 & 2) * 4), (v4u){~1u, ~1u, ~1u, ~1u}); }
#pragma unroll
        for (int i = 0; i < 16; ++i) st16(rh, un + (i >> 2) * 8192 + (i & 3) * 1024 + l * 16, z[i & 3]);
        st4(r, (wv == 3 && l == 0) ? F_PROG + k * 128 : OOB, 0u);
        st16(r, HO_KVM + (63 * 32 + k) * 4096 + wv * 1024 + l * 16, (v4u){~0u, ~0u, ~0u, ~0u});
        DRAIN();
        st8(r, l == 0 ? F_CAL + k * 32 + wv * 8 : OOB, (v2u){tag, vote});
        wait_long(th);
    }
    prep_block(p, r, k, wv, tag);
    if (!conv_block(p, r, k * CVH, CVH, lds, wv, j, tag, (unsigned)k * SKEWH)) return;
    if (ATD) wait_long(rtnow() + ATD);
    attn_block(p, wv, k, tag);
}

__global__ void __launch_bounds__(256, 1) k_main(Params p) {
    extern __shared__ __attribute__((aligned(16))) unsigned char lds[];
    const unsigned g_t0 = DIAG ? rtnow() : 0u;
    rsrc_t r = __builtin_amdgcn_make_buffer_rsrc(p.ws, 0, W_TOTAL, 0x00020000);
    const int wv = __builtin_amdgcn_readfirstlane(threadIdx.x >> 6);
    int role = blockIdx.x;
    unsigned xcc; asm volatile("s_getreg_b32 %0, hwreg(HW_REG_XCC_ID)" : "=s"(xcc)); xcc &= 7u;
    unsigned old = 0u, xr = xcc, rk = blockIdx.x >> 3;
    if (MIXH) { if (xcc == 3u) { xr = 4u + (rk >> 3); rk = 24u + (rk & 7u); } else if (xcc > 3u && rk >= 24u) { xr = 3u; rk = (xcc - 4u) * 8u + (rk & 7u); } }
    unsigned slot = (xr >> 2) * 128u + (rk >> 1) * 8u + (xr & 3u) * 2u + (rk & 1u);
    const unsigned tag = (__builtin_amdgcn_readfirstlane(ld4(r, (threadIdx.x & 63) == 1 ? F_CLM + slot * 64 + 4 : F_EP)) * 2654435761u + 0x9E3779B9u) | 1u;
    if (threadIdx.x == 0) old = __hip_atomic_exchange((unsigned*)(p.ws + F_CLM + slot * 64), tag, __ATOMIC_RELAXED, __HIP_MEMORY_SCOPE_AGENT);
    if (threadIdx.x == 0) {
        for (int i = 0; i < 256 && old == tag; ++i) { slot = (slot + 1u) & 255u; old = __hip_atomic_exchange((unsigned*)(p.ws + F_CLM + slot * 64), tag, __ATOMIC_RELAXED, __HIP_MEMORY_SCOPE_AGENT); }
        *(volatile __attribute__((address_space(3))) int*)(size_t)(unsigned)(size_t)lds = (int)slot;
    }
    __syncthreads();
    role = __builtin_amdgcn_readfirstlane(*(volatile __attribute__((address_space(3))) int*)(size_t)(unsigned)(size_t)lds) & 255;
    __syncthreads();
    if (DIAG >= 10 && blockIdx.x < 8 && threadIdx.x == 0) st4(r, F_PROG + 72 + xcc * 128, g_t0);
    if (role >= 128) bulk_block(p, r, lds, wv, role - 128, tag, g_t0);
    else if ((role & 6) == 6) help_block(p, r, lds, wv, (role >> 3) * 2 + (role & 1), tag, xcc >> 2);
    else rec_block(p, wv, role, tag);
}

extern "C" void kernel_launch(void* const* d_in, const int* in_sizes, int n_in, void* d_out, int out_size, void* d_ws, size_t ws_size, hipStream_t stream) {
    Params p;
    p.Aq = (const bf16_t*)d_in[0]; p.Ak = (const bf16_t*)d_in[1]; p.Av = (const bf16_t*)d_in[2];
    p.shift_k = (const bf16_t*)d_in[3]; p.ssm_k = (const bf16_t*)d_in[4]; p.ssm_D = (const bf16_t*)d_in[5];
    p.mem0 = (const bf16_t*)d_in[7]; p.Wq = (const bf16_t*)d_in[8]; p.Wk = (const bf16_t*)d_in[9];
    p.Wv = (const bf16_t*)d_in[10]; p.Wo = (const bf16_t*)d_in[11]; p.gate_w = (const bf16_t*)d_in[13];
    p.out = (bf16_t*)d_out; p.ws = (unsigned char*)d_ws;
    (void)hipFuncSetAttribute((const void*)k_main, hipFuncAttributeMaxDynamicSharedMemorySize, L_TOTAL);
    k_main<<<256, 256, L_TOTAL, stream>>>(p);
}
```

```cpp
#include <hip/hip_runtime.h>
#include <stdint.h>
#include <type_traits>
typedef unsigned short bf16_t;
using bf16x8 = __attribute__((ext_vector_type(8))) short;
using f32x4 = __attribute__((ext_vector_type(4))) float;
typedef unsigned v4u __attribute__((ext_vector_type(4)));
typedef unsigned v2u __attribute__((ext_vector_type(2)));
typedef __amdgpu_buffer_rsrc_t rsrc_t;

typedef __bf16 hbf2 __attribute__((ext_vector_type(2)));
typedef float f32x2 __attribute__((ext_vector_type(2)));
__device__ __forceinline__ unsigned pack2(float a, float b) { f32x2 v = {a, b}; return __builtin_bit_cast(unsigned, __builtin_convertvector(v, hbf2)); }
__device__ __forceinline__ float rbfh(float f) { return __uint_as_float(pack2(0.f, f)); }
__device__ __forceinline__ f32x2 rbf2(f32x2 f) { return (f32x2){rbfh(f.x), rbfh(f.y)}; }

constexpr int S = 2048, D = 1024;

constexpr unsigned W_FLG = 0;
constexpr unsigned F_KVQ = W_FLG;
constexpr unsigned F_EP = W_FLG + 32768;
constexpr unsigned F_ERR = W_FLG + 32768 + 256;
constexpr unsigned F_PREP = W_FLG + 32768 + 512;
constexpr unsigned F_CAL = W_FLG + 73728;
constexpr unsigned F_CLM = W_FLG + 77824;
constexpr unsigned F_CONV = W_FLG + 33792;
constexpr unsigned F_PROG = W_FLG + 36864;
constexpr unsigned F_ATT = W_FLG + 40960;
constexpr unsigned F_RDY = W_FLG + 49152;
constexpr unsigned FLG_BYTES = 94208;
constexpr unsigned HO_KVM = W_FLG + FLG_BYTES;
constexpr unsigned W_GATE = HO_KVM + 64 * 32 * 4096;
constexpr unsigned W_QX = W_GATE + 8192;
constexpr unsigned W_KX = W_QX + S * 4096 * 2;
constexpr unsigned W_VF = W_KX + S * 4096 * 2;
constexpr unsigned DBG_BYTES = 0;
constexpr unsigned W_DBG = W_VF + 64 * 32 * 8192;
constexpr unsigned W_U = W_DBG + DBG_BYTES;
constexpr unsigned W_TOTAL = W_U + S * D * 2;

__device__ __forceinline__ v4u ld16(rsrc_t r, unsigned off) { return __builtin_amdgcn_raw_buffer_load_b128(r, off, 0, 16); }
__device__ __forceinline__ unsigned ld4(rsrc_t r, unsigned off) { return __builtin_amdgcn_raw_buffer_load_b32(r, off, 0, 16); }
__device__ __forceinline__ void st16(rsrc_t r, unsigned off, v4u v) { __builtin_amdgcn_raw_buffer_store_b128(v, r, off, 0, 16); }
__device__ __forceinline__ void st8(rsrc_t r, unsigned off, v2u v) { __builtin_amdgcn_raw_buffer_store_b64(v, r, off, 0, 16); }
__device__ __forceinline__ void st4(rsrc_t r, unsigned off, unsigned v) { __builtin_amdgcn_raw_buffer_store_b32(v, r, off, 0, 16); }
__device__ __forceinline__ bf16x8 as_frag(v4u v) { return __builtin_bit_cast(bf16x8, v); }
#define D1 128
#define D2 82
#define LC0 7
#define LA0 8
#define TR_UP 4
#define TR_DN 2
#define L_DEC 15
#define UP_SH 2
#define EARLY_FROM 58
#define P_SLP 2
#define O_SLP 1
#define TOL 2
#define A_SLP 0
#define PEN2 20
#define A_UP 6
#define CT0 2
#define CPF 1
#define PH_M 3
#define SKEW 20
#define ROT2 2
#define HX_SH 0u
#define FINE_N 0
#define PEN0 32
#define X0C 24
#define X0A 20
#define PEN 40
#define MEMBAR() asm volatile("" ::: "memory")
#define ENDPGM() asm volatile("s_endpgm" ::: "memory")
#define DRAIN() asm volatile("s_waitcnt vmcnt(0)" ::: "memory")

typedef short v4i16_t __attribute__((ext_vector_type(4)));
__device__ __forceinline__ v2u trb(unsigned lds_addr) { return __builtin_bit_cast(v2u, __builtin_amdgcn_ds_read_tr16_b64_v4i16((__attribute__((address_space(3))) v4i16_t*)(size_t)lds_addr)); }
__device__ __forceinline__ int otid_(int wv) { int t; asm volatile("v_mbcnt_lo_u32_b32 %0, -1, 0\n\tv_mbcnt_hi_u32_b32 %0, -1, %0" : "=v"(t)); return wv * 64 + t; }
#define otid() otid_(wv)
__device__ __forceinline__ bool wait_prep(rsrc_t r, int l, unsigned tag) {
    for (unsigned spin = 0;; ++spin) { MEMBAR();
        const unsigned v = ld4(r, F_PREP + (l & 31) * 4);
        if (__all(v == tag)) return true;
        if (spin > (1u << 20)) { st4(r, F_ERR, 512u); return false; }
        __builtin_amdgcn_s_sleep(8); }
}
__device__ __forceinline__ unsigned rtnow() { return (unsigned)__builtin_amdgcn_s_memrealtime(); }
__device__ __forceinline__ void wait_long(unsigned target) { for (int i = 0; i < 4096; ++i) { if ((int)(rtnow() - target) >= 0) break; __builtin_amdgcn_s_sleep(8); } }
constexpr int L_TAB = 20480;
constexpr int L_QK = 24576;
__device__ __forceinline__ unsigned img(int f) { return (unsigned)(L_QK + f * 16 + (f >> 4) * 128); }
constexpr int L_FL = 30720, L_Z = L_FL + 128, L_LOG = 31744, L_W = 32768, L_V = 131072;
constexpr int L_TOTAL = 163840;
constexpr unsigned U_END = 96;
#define PMIN 82
#define V_SLP 2
#define R_SLP 0
constexpr unsigned WO_L = 98304;
#define HELP_D 200
#define V_DEL 24
#define XR 8
#define KEYM 3
#define DIAG 0
#define NEARX 0
#define MIXH 1
#define SKEWH 10
#define SKEWP 8
#define NEARY 0
#define CS1 0x8000u
#define CS2 0x10000u
#define X_SLP 2
#define X_DEL 10
#define DN_SH 1
#define AT_SLP 18
#define VA_SLP 0
#define C_SLP 0
#define BT_SLP 0
#define L_SLP 10
#define Y_DEL 6
#define H_SLP 0
#define CAL_N 3
#define STEP_MIN 180u

struct Params {
    const bf16_t *Aq, *Ak, *Av, *shift_k, *ssm_k, *ssm_D, *mem0, *Wq, *Wk, *Wv, *Wo, *gate_w;
    bf16_t* out; unsigned char* ws;
};
__host__ __device__ __forceinline__ unsigned hx_base(const void* ws) { const size_t a = (size_t)ws; return (unsigned)((((a + W_TOTAL + 0x7FFFFFu) >> 23) << 23) - a) + HX_SH; }

typedef short v4s __attribute__((ext_vector_type(4)));
typedef unsigned v32u __attribute__((ext_vector_type(32)));
typedef unsigned v16u __attribute__((ext_vector_type(16)));
typedef unsigned v8u __attribute__((ext_vector_type(8)));
typedef unsigned v12u __attribute__((ext_vector_type(12)));
__device__ __forceinline__ v4u mk_srd(const void* p, unsigned bytes) { const size_t a = (size_t)p; v4u d;
    d.x = __builtin_amdgcn_readfirstlane((unsigned)a); d.y = __builtin_amdgcn_readfirstlane((unsigned)(a >> 32) & 0xFFFFu); d.z = __builtin_amdgcn_readfirstlane(bytes); d.w = 0x00020000u; return d; }
__device__ __forceinline__ bf16x8 ldsa(unsigned addr) { return *(const __attribute__((address_space(3))) bf16x8*)(size_t)addr; }
__device__ __forceinline__ void rec_block(const Params& p, const int wv, const int b, const unsigned tag) {
    const int w = wv, tid = otid(), l = tid & 63, n16 = l & 15, g = l >> 4, c = n16 >> 2;
    const int x = (b & 7) >> 1, i32 = (b >> 3) * 2 + (b & 1);
    const unsigned OOB = 0xF0000000u;
    const v4u rhd = mk_srd(p.ws + hx_base(p.ws), U_END * 16384), wsd = mk_srd(p.ws, W_TOTAL);
    const int G = x - 1, vi = G * 32 + i32, h = x == 0 ? i32 : vi >> 1;
    const unsigned pr_a = F_PROG + h * 128, cal = F_CAL - w * 1024, kvo = HO_KVM + h * 4096 + (x ? 2048 : 0);
    if (CPF) {
        unsigned lo, hi, l2; asm volatile("s_getpc_b64 s[98:99]\n.Lpc%=:\n\ts_add_u32 %2, s98, rec_v_code-.Lpc%=\n\ts_add_u32 %0, s98, rec_s_code-.Lpc%=\n\ts_addc_u32 %1, s99, 0" : "=s"(lo), "=s"(hi), "=s"(l2) :: "s98", "s99", "scc");
        const size_t a = (((size_t)hi << 32) | lo) + (size_t)(long long)((x ? (int)(l2 - lo) : 0) + ((i32 * 4 + w) % 12) * 1024 - 4096 + l * 16);
        asm volatile("s_mov_b32 m0, %1\n\ts_nop 0\n\tglobal_load_lds_dwordx4 %0, off" :: "v"(a & ~(size_t)15), "s"(x ? L_V + 16384 : 0) : "memory", "m0");
    }
    typedef __attribute__((address_space(3))) volatile unsigned lu32;
    auto cpy = [](int k) -> unsigned { return k == 1 ? 0u : k * 32768u + 16384u; };
    auto cun = [](int u) -> unsigned { return ((u & 1) ? CS1 : 0u) ^ ((u & 2) ? CS2 : 0u); };
    unsigned nA = cpy(x), nW = cpy(w), nT = 64 * 16384 + (x ? 0 : h * 8), nS = 64 * 16384 + x * 1024, nC = 32 * 16384 + (x == 2 ? 16384 : 0), b0 = 65 * 16384 + (h >> 2) * 32768 + (h & 3) * 2048;
    unsigned pob = w == 0 ? nA + (i32 >> 4) * 32768 + (i32 & 15) * 512 : (w == 1 && x) ? (nC ^ cun(i32 >> 3)) + (i32 & 7) * 1024 : (w == 2 && i32 == 0) ? nS : OOB;
    if (x == 0) {
        const v4u sq = mk_srd(p.Wq, 1024 * 4096 * 2), sk = mk_srd(p.Wk, 1024 * 4096 * 2);
        const unsigned m0b = L_W + w * 1024, f = (c & 1) | ((g & 1) << 1) | ((c >> 1) << 2);
        const unsigned t0 = L_W + (16 * (g >> 1) + 4 * (g & 1) + (c & 1) + 8 * (c >> 1)) * 256 + (((2 * w) ^ f) << 5) + (n16 & 3) * 8;
        const unsigned dv = (4 * w + g) * 8192 + h * 256 + ((n16 ^ (((g & 1) | (w << 1)) << 1)) << 4);
        v8u pa;
#pragma unroll
        for (int q = 0; q < 8; ++q) pa[q] = (t0 + ((w + (q >> 1)) & 3) * 32768) ^ ((q & 1) << 5);
        const unsigned kx = W_KX + h * 8192, vx = W_VF + h * 8192 + w * 1024, kf = F_KVQ + h * 64;
        const unsigned aq = img(8 * g + (n16 >> 2)) + (n16 & 1) * 8;
        const v8u pc = {(unsigned)tid * 16, (unsigned)(L_W + w * 32768 + l * 16), (n16 & 2) ? 0xEEEEu : 0x4444u, aq,
                  (unsigned)(((w + 1) & 3) * 1024 + l * 16), (unsigned)(((w + 2) & 3) * 1024 + l * 16), (unsigned)(((w + 3) & 3) * 1024 + l * 16), img(32 * w + n16) + g * 4};
        const v2u pd = {(n16 & 2) ? (unsigned)L_Z : aq + w * 768, (n16 & 2) ? aq + w * 768 : (unsigned)L_Z};
        *(__attribute__((address_space(3))) v4u*)(size_t)(L_TAB + tid * 16) = (v4u){(unsigned)(L_QK + 3072 + g * 384 + n16 * 8),
                  n16 < 8 ? (unsigned)((((n16 >> 2) * 16384) ^ cun((h >> 1) & 3)) + ((h & 1) * 4 + (h >> 3)) * 1024 + w * 256 + (g >> 1) * 128 + (n16 & 2) * 32 + (g & 1) * 32 + (n16 & 1) * 16) : OOB,
                  (unsigned)((32 * w + 16 * (g & 1) + n16) * 16 + (g >> 1) * 8), (unsigned)(L_QK + 3072 + w * 768 + ((n16 & 1) + 2 * (n16 >> 2)) * 32 + g * 8 + (n16 & 2) * 2)};
        if (tid < 64) ((lu32*)(size_t)L_FL)[tid] = 0u;
    asm volatile(
        "s_add_u32 s80, %[m0b], 0\n\t"
        "s_mov_b32 s81, 0\n\t"
        "s_mov_b32 s82, 4\n\t"
        ".Ld0%=:\n\t"
        "s_mov_b32 m0, s80\n\t"
        "s_add_u32 s80, s80, 0x8000\n\t"
        "buffer_load_dwordx4 v96, %[sq], s81 offen lds\n\t"
        "s_add_u32 m0, m0, 0x1000\n\t"
        "s_add_u32 s81, s81, 0x20000\n\t"
        "buffer_load_dwordx4 v96, %[sq], s81 offen lds\n\t"
        "s_add_u32 m0, m0, 0x1000\n\t"
        "s_add_u32 s81, s81, 0x20000\n\t"
        "buffer_load_dwordx4 v96, %[sq], s81 offen lds\n\t"
        "s_add_u32 m0, m0, 0x1000\n\t"
        "s_add_u32 s81, s81, 0x20000\n\t"
        "buffer_load_dwordx4 v96, %[sq], s81 offen lds\n\t"
        "s_add_u32 m0, m0, 0x1000\n\t"
        "s_add_u32 s81, s81, 0x20000\n\t"
        "s_sub_u32 s82, s82, 1\n\t"
        "s_cmp_lg_u32 s82, 0\n\t"
        "s_cbranch_scc1 .Ld0%=\n\t"
        "s_add_u32 s80, %[m0b], 16384\n\t"
        "s_mov_b32 s81, 0\n\t"
        "s_mov_b32 s82, 4\n\t"
        ".Ld1%=:\n\t"
        "s_mov_b32 m0, s80\n\t"
        "s_add_u32 s80, s80, 0x8000\n\t"
        "buffer_load_dwordx4 v96, %[sk], s81 offen lds\n\t"
        "s_add_u32 m0, m0, 0x1000\n\t"
        "s_add_u32 s81, s81, 0x20000\n\t"
        "buffer_load_dwordx4 v96, %[sk], s81 offen lds\n\t"
        "s_add_u32 m0, m0, 0x1000\n\t"
        "s_add_u32 s81, s81, 0x20000\n\t"
        "buffer_load_dwordx4 v96, %[sk], s81 offen lds\n\t"
        "s_add_u32 m0, m0, 0x1000\n\t"
        "s_add_u32 s81, s81, 0x20000\n\t"
        "buffer_load_dwordx4 v96, %[sk], s81 offen lds\n\t"
        "s_add_u32 m0, m0, 0x1000\n\t"
        "s_add_u32 s81, s81, 0x20000\n\t"
        "s_sub_u32 s82, s82, 1\n\t"
        "s_cmp_lg_u32 s82, 0\n\t"
        "s_cbranch_scc1 .Ld1%=\n\t"
        "s_waitcnt vmcnt(16)\n\t"
        "s_barrier\n\t"
        "ds_read_b64_tr_b16 v[0:1], v98 offset:0\n\t"
        "ds_read_b64_tr_b16 v[2:3], v98 offset:512\n\t"
        "ds_read_b64_tr_b16 v[4:5], v98 offset:8192\n\t"
        "ds_read_b64_tr_b16 v[6:7], v98 offset:8704\n\t"
        "ds_read_b64_tr_b16 v[8:9], v99 offset:0\n\t"
        "ds_read_b64_tr_b16 v[10:11], v99 offset:512\n\t"
        "ds_read_b64_tr_b16 v[12:13], v99 offset:8192\n\t"
        "ds_read_b64_tr_b16 v[14:15], v99 offset:8704\n\t"
        "ds_read_b64_tr_b16 a[16:17], v100 offset:0\n\t"
        "ds_read_b64_tr_b16 a[18:19], v100 offset:512\n\t"
        "ds_read_b64_tr_b16 a[20:21], v100 offset:8192\n\t"
        "ds_read_b64_tr_b16 a[22:23], v100 offset:8704\n\t"
        "ds_read_b64_tr_b16 a[24:25], v101 offset:0\n\t"
        "ds_read_b64_tr_b16 a[26:27], v101 offset:512\n\t"
        "ds_read_b64_tr_b16 a[28:29], v101 offset:8192\n\t"
        "ds_read_b64_tr_b16 a[30:31], v101 offset:8704\n\t"
        "ds_read_b64_tr_b16 a[32:33], v102 offset:0\n\t"
        "ds_read_b64_tr_b16 a[34:35], v102 offset:512\n\t"
        "ds_read_b64_tr_b16 a[36:37], v102 offset:8192\n\t"
        "ds_read_b64_tr_b16 a[38:39], v102 offset:8704\n\t"
        "ds_read_b64_tr_b16 a[40:41], v103 offset:0\n\t"
        "ds_read_b64_tr_b16 a[42:43], v103 offset:512\n\t"
        "ds_read_b64_tr_b16 a[44:45], v103 offset:8192\n\t"
        "ds_read_b64_tr_b16 a[46:47], v103 offset:8704\n\t"
        "ds_read_b64_tr_b16 a[48:49], v104 offset:0\n\t"
        "ds_read_b64_tr_b16 a[50:51], v104 offset:512\n\t"
        "ds_read_b64_tr_b16 a[52:53], v104 offset:8192\n\t"
        "ds_read_b64_tr_b16 a[54:55], v104 offset:8704\n\t"
        "ds_read_b64_tr_b16 a[56:57], v105 offset:0\n\t"
        "ds_read_b64_tr_b16 a[58:59], v105 offset:512\n\t"
        "ds_read_b64_tr_b16 a[60:61], v105 offset:8192\n\t"
        "ds_read_b64_tr_b16 a[62:63], v105 offset:8704\n\t"
        "s_waitcnt lgkmcnt(0)\n\t"
        "s_barrier\n\t"
        "s_add_u32 s80, %[m0b], 0\n\t"
        "s_mov_b32 s81, 2097152\n\t"
        "s_mov_b32 s82, 4\n\t"
        ".Ld2%=:\n\t"
        "s_mov_b32 m0, s80\n\t"
        "s_add_u32 s80, s80, 0x8000\n\t"
        "buffer_load_dwordx4 v96, %[sq], s81 offen lds\n\t"
        "s_add_u32 m0, m0, 0x1000\n\t"
        "s_add_u32 s81, s81, 0x20000\n\t"
        "buffer_load_dwordx4 v96, %[sq], s81 offen lds\n\t"
        "s_add_u32 m0, m0, 0x1000\n\t"
        "s_add_u32 s81, s81, 0x20000\n\t"
        "buffer_load_dwordx4 v96, %[sq], s81 offen lds\n\t"
        "s_add_u32 m0, m0, 0x1000\n\t"
        "s_add_u32 s81, s81, 0x20000\n\t"
        "buffer_load_dwordx4 v96, %[sq], s81 offen lds\n\t"
        "s_add_u32 m0, m0, 0x1000\n\t"
        "s_add_u32 s81, s81, 0x20000\n\t"
        "s_sub_u32 s82, s82, 1\n\t"
        "s_cmp_lg_u32 s82, 0\n\t"
        "s_cbranch_scc1 .Ld2%=\n\t"
        "s_waitcnt vmcnt(16)\n\t"
        "s_barrier\n\t"
        "ds_read_b64_tr_b16 v[16:17], v98 offset:16384\n\t"
        "ds_read_b64_tr_b16 v[18:19], v98 offset:16896\n\t"
        "ds_read_b64_tr_b16 v[20:21], v98 offset:24576\n\t"
        "ds_read_b64_tr_b16 v[22:23], v98 offset:25088\n\t"
        "ds_read_b64_tr_b16 v[24:25], v99 offset:16384\n\t"
        "ds_read_b64_tr_b16 v[26:27], v99 offset:16896\n\t"
        "ds_read_b64_tr_b16 v[28:29], v99 offset:24576\n\t"
        "ds_read_b64_tr_b16 v[30:31], v99 offset:25088\n\t"
        "ds_read_b64_tr_b16 a[80:81], v100 offset:16384\n\t"
        "ds_read_b64_tr_b16 a[82:83], v100 offset:16896\n\t"
        "ds_read_b64_tr_b16 a[84:85], v100 offset:24576\n\t"
        "ds_read_b64_tr_b16 a[86:87], v100 offset:25088\n\t"
        "ds_read_b64_tr_b16 a[88:89], v101 offset:16384\n\t"
        "ds_read_b64_tr_b16 a[90:91], v101 offset:16896\n\t"
        "ds_read_b64_tr_b16 a[92:93], v101 offset:24576\n\t"
        "ds_read_b64_tr_b16 a[94:95], v101 offset:25088\n\t"
        "ds_read_b64_tr_b16 a[96:97], v102 offset:16384\n\t"
        "ds_read_b64_tr_b16 a[98:99], v102 offset:16896\n\t"
        "ds_read_b64_tr_b16 a[100:101], v102 offset:24576\n\t"
        "ds_read_b64_tr_b16 a[102:103], v102 offset:25088\n\t"
        "ds_read_b64_tr_b16 a[104:105], v103 offset:16384\n\t"
        "ds_read_b64_tr_b16 a[106:107], v103 offset:16896\n\t"
        "ds_read_b64_tr_b16 a[108:109], v103 offset:24576\n\t"
        "ds_read_b64_tr_b16 a[110:111], v103 offset:25088\n\t"
        "ds_read_b64_tr_b16 a[112:113], v104 offset:16384\n\t"
        "ds_read_b64_tr_b16 a[114:115], v104 offset:16896\n\t"
        "ds_read_b64_tr_b16 a[116:117], v104 offset:24576\n\t"
        "ds_read_b64_tr_b16 a[118:119], v104 offset:25088\n\t"
        "ds_read_b64_tr_b16 a[120:121], v105 offset:16384\n\t"
        "ds_read_b64_tr_b16 a[122:123], v105 offset:16896\n\t"
        "ds_read_b64_tr_b16 a[124:125], v105 offset:24576\n\t"
        "ds_read_b64_tr_b16 a[126:127], v105 offset:25088\n\t"
        "s_waitcnt lgkmcnt(0)\n\t"
        "s_barrier\n\t"
        "s_add_u32 s80, %[m0b], 16384\n\t"
        "s_mov_b32 s81, 2097152\n\t"
        "s_mov_b32 s82, 4\n\t"
        ".Ld3%=:\n\t"
        "s_mov_b32 m0, s80\n\t"
        "s_add_u32 s80, s80, 0x8000\n\t"
        "buffer_load_dwordx4 v96, %[sk], s81 offen lds\n\t"
        "s_add_u32 m0, m0, 0x1000\n\t"
        "s_add_u32 s81, s81, 0x20000\n\t"
        "buffer_load_dwordx4 v96, %[sk], s81 offen lds\n\t"
        "s_add_u32 m0, m0, 0x1000\n\t"
        "s_add_u32 s81, s81, 0x20000\n\t"
        "buffer_load_dwordx4 v96, %[sk], s81 offen lds\n\t"
        "s_add_u32 m0, m0, 0x1000\n\t"
        "s_add_u32 s81, s81, 0x20000\n\t"
        "buffer_load_dwordx4 v96, %[sk], s81 offen lds\n\t"
        "s_add_u32 m0, m0, 0x1000\n\t"
        "s_add_u32 s81, s81, 0x20000\n\t"
        "s_sub_u32 s82, s82, 1\n\t"
        "s_cmp_lg_u32 s82, 0\n\t"
        "s_cbranch_scc1 .Ld3%=\n\t"
        "s_waitcnt vmcnt(16)\n\t"
        "s_barrier\n\t"
        "ds_read_b64_tr_b16 v[32:33], v98 offset:0\n\t"
        "ds_read_b64_tr_b16 v[34:35], v98 offset:512\n\t"
        "ds_read_b64_tr_b16 v[36:37], v98 offset:8192\n\t"
        "ds_read_b64_tr_b16 v[38:39], v98 offset:8704\n\t"
        "ds_read_b64_tr_b16 v[40:41], v99 offset:0\n\t"
        "ds_read_b64_tr_b16 v[42:43], v99 offset:512\n\t"
        "ds_read_b64_tr_b16 v[44:45], v99 offset:8192\n\t"
        "ds_read_b64_tr_b16 v[46:47], v99 offset:8704\n\t"
        "ds_read_b64_tr_b16 a[144:145], v100 offset:0\n\t"
        "ds_read_b64_tr_b16 a[146:147], v100 offset:512\n\t"
        "ds_read_b64_tr_b16 a[148:149], v100 offset:8192\n\t"
        "ds_read_b64_tr_b16 a[150:151], v100 offset:8704\n\t"
        "ds_read_b64_tr_b16 a[152:153], v101 offset:0\n\t"
        "ds_read_b64_tr_b16 a[154:155], v101 offset:512\n\t"
        "ds_read_b64_tr_b16 a[156:157], v101 offset:8192\n\t"
        "ds_read_b64_tr_b16 a[158:159], v101 offset:8704\n\t"
        "ds_read_b64_tr_b16 a[160:161], v102 offset:0\n\t"
        "ds_read_b64_tr_b16 a[162:163], v102 offset:512\n\t"
        "ds_read_b64_tr_b16 a[164:165], v102 offset:8192\n\t"
        "ds_read_b64_tr_b16 a[166:167], v102 offset:8704\n\t"
        "ds_read_b64_tr_b16 a[168:169], v103 offset:0\n\t"
        "ds_read_b64_tr_b16 a[170:171], v103 offset:512\n\t"
        "ds_read_b64_tr_b16 a[172:173], v103 offset:8192\n\t"
        "ds_read_b64_tr_b16 a[174:175], v103 offset:8704\n\t"
        "ds_read_b64_tr_b16 a[176:177], v104 offset:0\n\t"
        "ds_read_b64_tr_b16 a[178:179], v104 offset:512\n\t"
        "ds_read_b64_tr_b16 a[180:181], v104 offset:8192\n\t"
        "ds_read_b64_tr_b16 a[182:183], v104 offset:8704\n\t"
        "ds_read_b64_tr_b16 a[184:185], v105 offset:0\n\t"
        "ds_read_b64_tr_b16 a[186:187], v105 offset:512\n\t"
        "ds_read_b64_tr_b16 a[188:189], v105 offset:8192\n\t"
        "ds_read_b64_tr_b16 a[190:191], v105 offset:8704\n\t"
        "s_waitcnt lgkmcnt(0)\n\t"
        "s_barrier\n\t"
        "s_add_u32 s80, %[m0b], 0\n\t"
        "s_mov_b32 s81, 4194304\n\t"
        "s_mov_b32 s82, 4\n\t"
        ".Ld4%=:\n\t"
        "s_mov_b32 m0, s80\n\t"
        "s_add_u32 s80, s80, 0x8000\n\t"
        "buffer_load_dwordx4 v96, %[sq], s81 offen lds\n\t"
        "s_add_u32 m0, m0, 0x1000\n\t"
        "s_add_u32 s81, s81, 0x20000\n\t"
        "buffer_load_dwordx4 v96, %[sq], s81 offen lds\n\t"
        "s_add_u32 m0, m0, 0x1000\n\t"
        "s_add_u32 s81, s81, 0x20000\n\t"
        "buffer_load_dwordx4 v96, %[sq], s81 offen lds\n\t"
        "s_add_u32 m0, m0, 0x1000\n\t"
        "s_add_u32 s81, s81, 0x20000\n\t"
        "buffer_load_dwordx4 v96, %[sq], s81 offen lds\n\t"
        "s_add_u32 m0, m0, 0x1000\n\t"
        "s_add_u32 s81, s81, 0x20000\n\t"
        "s_sub_u32 s82, s82, 1\n\t"
        "s_cmp_lg_u32 s82, 0\n\t"
        "s_cbranch_scc1 .Ld4%=\n\t"
        "s_mov_b64 exec, 0\n\t"
        "s_branch .Lhk0%=\n\t"
        ".Lhr%=:\n\t"
        "s_mov_b64 exec, -1\n\t"
        "s_waitcnt vmcnt(16)\n\t"
        "s_barrier\n\t"
        "ds_read_b64_tr_b16 v[48:49], v98 offset:16384\n\t"
        "ds_read_b64_tr_b16 v[50:51], v98 offset:16896\n\t"
        "ds_read_b64_tr_b16 v[52:53], v98 offset:24576\n\t"
        "ds_read_b64_tr_b16 v[54:55], v98 offset:25088\n\t"
        "ds_read_b64_tr_b16 v[56:57], v99 offset:16384\n\t"
        "ds_read_b64_tr_b16 v[58:59], v99 offset:16896\n\t"
        "ds_read_b64_tr_b16 v[60:61], v99 offset:24576\n\t"
        "ds_read_b64_tr_b16 v[62:63], v99 offset:25088\n\t"
        "ds_read_b64_tr_b16 a[208:209], v100 offset:16384\n\t"
        "ds_read_b64_tr_b16 a[210:211], v100 offset:16896\n\t"
        "ds_read_b64_tr_b16 a[212:213], v100 offset:24576\n\t"
        "ds_read_b64_tr_b16 a[214:215], v100 offset:25088\n\t"
        "ds_read_b64_tr_b16 a[216:217], v101 offset:16384\n\t"
        "ds_read_b64_tr_b16 a[218:219], v101 offset:16896\n\t"
        "ds_read_b64_tr_b16 a[220:221], v101 offset:24576\n\t"
        "ds_read_b64_tr_b16 a[222:223], v101 offset:25088\n\t"
        "ds_read_b64_tr_b16 a[224:225], v102 offset:16384\n\t"
        "ds_read_b64_tr_b16 a[226:227], v102 offset:16896\n\t"
        "ds_read_b64_tr_b16 a[228:229], v102 offset:24576\n\t"
        "ds_read_b64_tr_b16 a[230:231], v102 offset:25088\n\t"
        "ds_read_b64_tr_b16 a[232:233], v103 offset:16384\n\t"
        "ds_read_b64_tr_b16 a[234:235], v103 offset:16896\n\t"
        "ds_read_b64_tr_b16 a[236:237], v103 offset:24576\n\t"
        "ds_read_b64_tr_b16 a[238:239], v103 offset:25088\n\t"
        "ds_read_b64_tr_b16 a[240:241], v104 offset:16384\n\t"
        "ds_read_b64_tr_b16 a[242:243], v104 offset:16896\n\t"
        "ds_read_b64_tr_b16 a[244:245], v104 offset:24576\n\t"
        "ds_read_b64_tr_b16 a[246:247], v104 offset:25088\n\t"
        "ds_read_b64_tr_b16 a[248:249], v105 offset:16384\n\t"
        "ds_read_b64_tr_b16 a[250:251], v105 offset:16896\n\t"
        "ds_read_b64_tr_b16 a[252:253], v105 offset:24576\n\t"
        "ds_read_b64_tr_b16 a[254:255], v105 offset:25088\n\t"
        "s_waitcnt lgkmcnt(0)\n\t"
        "s_barrier\n\t"
        "s_add_u32 s80, %[m0b], 16384\n\t"
        "s_mov_b32 s81, 4194304\n\t"
        "s_mov_b32 s82, 4\n\t"
        ".Ld5%=:\n\t"
        "s_mov_b32 m0, s80\n\t"
        "s_add_u32 s80, s80, 0x8000\n\t"
        "buffer_load_dwordx4 v96, %[sk], s81 offen lds\n\t"
        "s_add_u32 m0, m0, 0x1000\n\t"
        "s_add_u32 s81, s81, 0x20000\n\t"
        "buffer_load_dwordx4 v96, %[sk], s81 offen lds\n\t"
        "s_add_u32 m0, m0, 0x1000\n\t"
        "s_add_u32 s81, s81, 0x20000\n\t"
        "buffer_load_dwordx4 v96, %[sk], s81 offen lds\n\t"
        "s_add_u32 m0, m0, 0x1000\n\t"
        "s_add_u32 s81, s81, 0x20000\n\t"
        "buffer_load_dwordx4 v96, %[sk], s81 offen lds\n\t"
        "s_add_u32 m0, m0, 0x1000\n\t"
        "s_add_u32 s81, s81, 0x20000\n\t"
        "s_sub_u32 s82, s82, 1\n\t"
        "s_cmp_lg_u32 s82, 0\n\t"
        "s_cbranch_scc1 .Ld5%=\n\t"
        "s_waitcnt vmcnt(16)\n\t"
        "s_barrier\n\t"
        "ds_read_b64_tr_b16 v[128:129], v98 offset:0\n\t"
        "ds_read_b64_tr_b16 v[130:131], v98 offset:512\n\t"
        "ds_read_b64_tr_b16 v[132:133], v98 offset:8192\n\t"
        "ds_read_b64_tr_b16 v[134:135], v98 offset:8704\n\t"
        "ds_read_b64_tr_b16 v[136:137], v99 offset:0\n\t"
        "ds_read_b64_tr_b16 v[138:139], v99 offset:512\n\t"
        "ds_read_b64_tr_b16 v[140:141], v99 offset:8192\n\t"
        "ds_read_b64_tr_b16 v[142:143], v99 offset:8704\n\t"
        "ds_read_b64_tr_b16 v[144:145], v100 offset:0\n\t"
        "ds_read_b64_tr_b16 v[146:147], v100 offset:512\n\t"
        "ds_read_b64_tr_b16 v[148:149], v100 offset:8192\n\t"
        "ds_read_b64_tr_b16 v[150:151], v100 offset:8704\n\t"
        "ds_read_b64_tr_b16 v[152:153], v101 offset:0\n\t"
        "ds_read_b64_tr_b16 v[154:155], v101 offset:512\n\t"
        "ds_read_b64_tr_b16 v[156:157], v101 offset:8192\n\t"
        "ds_read_b64_tr_b16 v[158:159], v101 offset:8704\n\t"
        "ds_read_b64_tr_b16 v[160:161], v102 offset:0\n\t"
        "ds_read_b64_tr_b16 v[162:163], v102 offset:512\n\t"
        "ds_read_b64_tr_b16 v[164:165], v102 offset:8192\n\t"
        "ds_read_b64_tr_b16 v[166:167], v102 offset:8704\n\t"
        "ds_read_b64_tr_b16 v[168:169], v103 offset:0\n\t"
        "ds_read_b64_tr_b16 v[170:171], v103 offset:512\n\t"
        "ds_read_b64_tr_b16 v[172:173], v103 offset:8192\n\t"
        "ds_read_b64_tr_b16 v[174:175], v103 offset:8704\n\t"
        "ds_read_b64_tr_b16 v[176:177], v104 offset:0\n\t"
        "ds_read_b64_tr_b16 v[178:179], v104 offset:512\n\t"
        "ds_read_b64_tr_b16 v[180:181], v104 offset:8192\n\t"
        "ds_read_b64_tr_b16 v[182:183], v104 offset:8704\n\t"
        "ds_read_b64_tr_b16 v[184:185], v105 offset:0\n\t"
        "ds_read_b64_tr_b16 v[186:187], v105 offset:512\n\t"
        "ds_read_b64_tr_b16 v[188:189], v105 offset:8192\n\t"
        "ds_read_b64_tr_b16 v[190:191], v105 offset:8704\n\t"
        "s_waitcnt lgkmcnt(0)\n\t"
        "s_barrier\n\t"
        "s_add_u32 s80, %[m0b], 0\n\t"
        "s_mov_b32 s81, 6291456\n\t"
        "s_mov_b32 s82, 4\n\t"
        ".Ld6%=:\n\t"
        "s_mov_b32 m0, s80\n\t"
        "s_add_u32 s80, s80, 0x8000\n\t"
        "buffer_load_dwordx4 v96, %[sq], s81 offen lds\n\t"
        "s_add_u32 m0, m0, 0x1000\n\t"
        "s_add_u32 s81, s81, 0x20000\n\t"
        "buffer_load_dwordx4 v96, %[sq], s81 offen lds\n\t"
        "s_add_u32 m0, m0, 0x1000\n\t"
        "s_add_u32 s81, s81, 0x20000\n\t"
        "buffer_load_dwordx4 v96, %[sq], s81 offen lds\n\t"
        "s_add_u32 m0, m0, 0x1000\n\t"
        "s_add_u32 s81, s81, 0x20000\n\t"
        "buffer_load_dwordx4 v96, %[sq], s81 offen lds\n\t"
        "s_add_u32 m0, m0, 0x1000\n\t"
        "s_add_u32 s81, s81, 0x20000\n\t"
        "s_sub_u32 s82, s82, 1\n\t"
        "s_cmp_lg_u32 s82, 0\n\t"
        "s_cbranch_scc1 .Ld6%=\n\t"
        "s_waitcnt vmcnt(16)\n\t"
        "s_barrier\n\t"
        "ds_read_b64_tr_b16 v[192:193], v98 offset:16384\n\t"
        "ds_read_b64_tr_b16 v[194:195], v98 offset:16896\n\t"
        "ds_read_b64_tr_b16 v[196:197], v98 offset:24576\n\t"
        "ds_read_b64_tr_b16 v[198:199], v98 offset:25088\n\t"
        "ds_read_b64_tr_b16 v[200:201], v99 offset:16384\n\t"
        "ds_read_b64_tr_b16 v[202:203], v99 offset:16896\n\t"
        "ds_read_b64_tr_b16 v[204:205], v99 offset:24576\n\t"
        "ds_read_b64_tr_b16 v[206:207], v99 offset:25088\n\t"
        "ds_read_b64_tr_b16 v[208:209], v100 offset:16384\n\t"
        "ds_read_b64_tr_b16 v[210:211], v100 offset:16896\n\t"
        "ds_read_b64_tr_b16 v[212:213], v100 offset:24576\n\t"
        "ds_read_b64_tr_b16 v[214:215], v100 offset:25088\n\t"
        "ds_read_b64_tr_b16 v[216:217], v101 offset:16384\n\t"
        "ds_read_b64_tr_b16 v[218:219], v101 offset:16896\n\t"
        "ds_read_b64_tr_b16 v[220:221], v101 offset:24576\n\t"
        "ds_read_b64_tr_b16 v[222:223], v101 offset:25088\n\t"
        "ds_read_b64_tr_b16 v[224:225], v102 offset:16384\n\t"
        "ds_read_b64_tr_b16 v[226:227], v102 offset:16896\n\t"
        "ds_read_b64_tr_b16 v[228:229], v102 offset:24576\n\t"
        "ds_read_b64_tr_b16 v[230:231], v102 offset:25088\n\t"
        "ds_read_b64_tr_b16 v[232:233], v103 offset:16384\n\t"
        "ds_read_b64_tr_b16 v[234:235], v103 offset:16896\n\t"
        "ds_read_b64_tr_b16 v[236:237], v103 offset:24576\n\t"
        "ds_read_b64_tr_b16 v[238:239], v103 offset:25088\n\t"
        "ds_read_b64_tr_b16 v[240:241], v104 offset:16384\n\t"
        "ds_read_b64_tr_b16 v[242:243], v104 offset:16896\n\t"
        "ds_read_b64_tr_b16 v[244:245], v104 offset:24576\n\t"
        "ds_read_b64_tr_b16 v[246:247], v104 offset:25088\n\t"
        "ds_read_b64_tr_b16 v[248:249], v105 offset:16384\n\t"
        "ds_read_b64_tr_b16 v[250:251], v105 offset:16896\n\t"
        "ds_read_b64_tr_b16 v[252:253], v105 offset:24576\n\t"
        "ds_read_b64_tr_b16 v[254:255], v105 offset:25088\n\t"
        "s_waitcnt lgkmcnt(0)\n\t"
        "s_barrier\n\t"
        "s_add_u32 s80, %[m0b], 16384\n\t"
        "s_mov_b32 s81, 6291456\n\t"
        "s_mov_b32 s82, 4\n\t"
        ".Ld7%=:\n\t"
        "s_mov_b32 m0, s80\n\t"
        "s_add_u32 s80, s80, 0x8000\n\t"
        "buffer_load_dwordx4 v96, %[sk], s81 offen lds\n\t"
        "s_add_u32 m0, m0, 0x1000\n\t"
        "s_add_u32 s81, s81, 0x20000\n\t"
        "buffer_load_dwordx4 v96, %[sk], s81 offen lds\n\t"
        "s_add_u32 m0, m0, 0x1000\n\t"
        "s_add_u32 s81, s81, 0x20000\n\t"
        "buffer_load_dwordx4 v96, %[sk], s81 offen lds\n\t"
        "s_add_u32 m0, m0, 0x1000\n\t"
        "s_add_u32 s81, s81, 0x20000\n\t"
        "buffer_load_dwordx4 v96, %[sk], s81 offen lds\n\t"
        "s_add_u32 m0, m0, 0x1000\n\t"
        "s_add_u32 s81, s81, 0x20000\n\t"
        "s_sub_u32 s82, s82, 1\n\t"
        "s_cmp_lg_u32 s82, 0\n\t"
        "s_cbranch_scc1 .Ld7%=\n\t"
        "s_waitcnt vmcnt(16)\n\t"
        "s_barrier\n\t"
        "ds_read_b64_tr_b16 a[0:1], v98 offset:0\n\t"
        "ds_read_b64_tr_b16 a[2:3], v98 offset:512\n\t"
        "ds_read_b64_tr_b16 a[4:5], v98 offset:8192\n\t"
        "ds_read_b64_tr_b16 a[6:7], v98 offset:8704\n\t"
        "ds_read_b64_tr_b16 a[8:9], v99 offset:0\n\t"
        "ds_read_b64_tr_b16 a[10:11], v99 offset:512\n\t"
        "ds_read_b64_tr_b16 a[12:13], v99 offset:8192\n\t"
        "ds_read_b64_tr_b16 a[14:15], v99 offset:8704\n\t"
        "ds_read_b64_tr_b16 v[64:65], v100 offset:0\n\t"
        "ds_read_b64_tr_b16 v[66:67], v100 offset:512\n\t"
        "ds_read_b64_tr_b16 v[68:69], v100 offset:8192\n\t"
        "ds_read_b64_tr_b16 v[70:71], v100 offset:8704\n\t"
        "ds_read_b64_tr_b16 v[72:73], v101 offset:0\n\t"
        "ds_read_b64_tr_b16 v[74:75], v101 offset:512\n\t"
        "ds_read_b64_tr_b16 v[76:77], v101 offset:8192\n\t"
        "ds_read_b64_tr_b16 v[78:79], v101 offset:8704\n\t"
        "ds_read_b64_tr_b16 v[80:81], v102 offset:0\n\t"
        "ds_read_b64_tr_b16 v[82:83], v102 offset:512\n\t"
        "ds_read_b64_tr_b16 v[84:85], v102 offset:8192\n\t"
        "ds_read_b64_tr_b16 v[86:87], v102 offset:8704\n\t"
        "ds_read_b64_tr_b16 v[88:89], v103 offset:0\n\t"
        "ds_read_b64_tr_b16 v[90:91], v103 offset:512\n\t"
        "ds_read_b64_tr_b16 v[92:93], v103 offset:8192\n\t"
        "ds_read_b64_tr_b16 v[94:95], v103 offset:8704\n\t"
        "ds_read_b64_tr_b16 a[128:129], v104 offset:0\n\t"
        "ds_read_b64_tr_b16 a[130:131], v104 offset:512\n\t"
        "ds_read_b64_tr_b16 a[132:133], v104 offset:8192\n\t"
        "ds_read_b64_tr_b16 a[134:135], v104 offset:8704\n\t"
        "ds_read_b64_tr_b16 a[136:137], v105 offset:0\n\t"
        "ds_read_b64_tr_b16 a[138:139], v105 offset:512\n\t"
        "ds_read_b64_tr_b16 a[140:141], v105 offset:8192\n\t"
        "ds_read_b64_tr_b16 a[142:143], v105 offset:8704\n\t"
        "s_waitcnt lgkmcnt(0)\n\t"
        "s_barrier\n\t"
        "ds_write_b128 v121, v[0:3] offset:0\n\t"
        "ds_write_b128 v121, v[4:7] offset:1024\n\t"
        "ds_write_b128 v121, v[8:11] offset:2048\n\t"
        "ds_write_b128 v121, v[12:15] offset:3072\n\t"
        "ds_write_b128 v121, v[16:19] offset:4096\n\t"
        "ds_write_b128 v121, v[20:23] offset:5120\n\t"
        "ds_write_b128 v121, v[24:27] offset:6144\n\t"
        "ds_write_b128 v121, v[28:31] offset:7168\n\t"
        "ds_write_b128 v121, v[32:35] offset:8192\n\t"
        "ds_write_b128 v121, v[36:39] offset:9216\n\t"
        "ds_write_b128 v121, v[40:43] offset:10240\n\t"
        "ds_write_b128 v121, v[44:47] offset:11264\n\t"
        "ds_write_b128 v121, v[48:51] offset:12288\n\t"
        "ds_write_b128 v121, v[52:55] offset:13312\n\t"
        "ds_write_b128 v121, v[56:59] offset:14336\n\t"
        "ds_write_b128 v121, v[60:63] offset:15360\n\t"
        "s_waitcnt lgkmcnt(0)\n\t"
        "s_mov_b32 s64, %[kf]\n\t"
        "s_mov_b32 s36, 0xf0000000\n\t"
        "buffer_load_dwordx4 v[116:119], v120, %[wsd], %[cal] offen sc1\n\t"
        "buffer_load_dwordx2 v[114:115], off, %[wsd], s64 sc1\n\t"
        "s_waitcnt vmcnt(2)\n\t"
        "s_barrier\n\t"
        "ds_read_b64_tr_b16 a[64:65], v98 offset:16384\n\t"
        "ds_read_b64_tr_b16 a[66:67], v98 offset:16896\n\t"
        "ds_read_b64_tr_b16 a[68:69], v98 offset:24576\n\t"
        "ds_read_b64_tr_b16 a[70:71], v98 offset:25088\n\t"
        "ds_read_b64_tr_b16 a[72:73], v99 offset:16384\n\t"
        "ds_read_b64_tr_b16 a[74:75], v99 offset:16896\n\t"
        "ds_read_b64_tr_b16 a[76:77], v99 offset:24576\n\t"
        "ds_read_b64_tr_b16 a[78:79], v99 offset:25088\n\t"
        "ds_read_b64_tr_b16 v[0:1], v100 offset:16384\n\t"
        "ds_read_b64_tr_b16 v[2:3], v100 offset:16896\n\t"
        "ds_read_b64_tr_b16 v[4:5], v100 offset:24576\n\t"
        "ds_read_b64_tr_b16 v[6:7], v100 offset:25088\n\t"
        "ds_read_b64_tr_b16 v[8:9], v101 offset:16384\n\t"
        "ds_read_b64_tr_b16 v[10:11], v101 offset:16896\n\t"
        "ds_read_b64_tr_b16 v[12:13], v101 offset:24576\n\t"
        "ds_read_b64_tr_b16 v[14:15], v101 offset:25088\n\t"
        "ds_read_b64_tr_b16 v[16:17], v102 offset:16384\n\t"
        "ds_read_b64_tr_b16 v[18:19], v102 offset:16896\n\t"
        "ds_read_b64_tr_b16 v[20:21], v102 offset:24576\n\t"
        "ds_read_b64_tr_b16 v[22:23], v102 offset:25088\n\t"
        "ds_read_b64_tr_b16 v[24:25], v103 offset:16384\n\t"
        "ds_read_b64_tr_b16 v[26:27], v103 offset:16896\n\t"
        "ds_read_b64_tr_b16 v[28:29], v103 offset:24576\n\t"
        "ds_read_b64_tr_b16 v[30:31], v103 offset:25088\n\t"
        "ds_read_b64_tr_b16 a[192:193], v104 offset:16384\n\t"
        "ds_read_b64_tr_b16 a[194:195], v104 offset:16896\n\t"
        "ds_read_b64_tr_b16 a[196:197], v104 offset:24576\n\t"
        "ds_read_b64_tr_b16 a[198:199], v104 offset:25088\n\t"
        "ds_read_b64_tr_b16 a[200:201], v105 offset:16384\n\t"
        "ds_read_b64_tr_b16 a[202:203], v105 offset:16896\n\t"
        "ds_read_b64_tr_b16 a[204:205], v105 offset:24576\n\t"
        "ds_read_b64_tr_b16 a[206:207], v105 offset:25088\n\t"
        "s_waitcnt vmcnt(0)\n\t"
        "s_mov_b32 s97, 0\n\t"
        ".Lg0%=:\n\t"
        "v_cmp_ne_u32_e32 vcc, %[tag], v116\n\t"
        "v_cmp_ne_u32_e64 s[82:83], %[tag], v118\n\t"
        "s_nop 1\n\t"
        "s_or_b64 vcc, vcc, s[82:83]\n\t"
        "s_cbranch_scc0 .Lg1%=\n\t"
        "s_sleep 4\n\t"
        "buffer_load_dwordx4 v[116:119], v120, %[wsd], %[cal] offen sc1\n\t"
        "s_add_u32 s97, s97, 1\n\t"
        "s_cmp_lt_u32 s97, 0x100000\n\t"
        "s_cbranch_scc0 .Lgu%=\n\t"
        "s_waitcnt vmcnt(0)\n\t"
        "s_branch .Lg0%=\n\t"
        ".Lg1%=:\n\t"
        "s_mov_b32 s84, 0x55555555\n\t"
        "s_mov_b32 s85, s84\n\t"
        "v_and_b32_e32 v116, 1, v117\n\t"
        "v_and_b32_e32 v118, 1, v119\n\t"
        "v_cmp_ne_u32_e32 vcc, 0, v116\n\t"
        "v_cmp_ne_u32_e64 s[82:83], 0, v118\n\t"
        "s_nop 0\n\t"
        "s_and_b64 s[82:83], s[82:83], s[84:85]\n\t"
        "s_bcnt1_i32_b64 s87, s[82:83]\n\t"
        "s_and_b64 s[82:83], vcc, s[84:85]\n\t"
        "s_bcnt1_i32_b64 s86, s[82:83]\n\t"
        "s_andn2_b64 s[82:83], vcc, s[84:85]\n\t"
        "s_bcnt1_i32_b64 s88, s[82:83]\n\t"
        "v_and_b32_e32 v116, 2, v117\n\t"
        "v_and_b32_e32 v118, 2, v119\n\t"
        "v_cmp_ne_u32_e32 vcc, 0, v116\n\t"
        "v_cmp_ne_u32_e64 s[82:83], 0, v118\n\t"
        "s_nop 0\n\t"
        "s_and_b64 s[82:83], s[82:83], s[84:85]\n\t"
        "s_bcnt1_i32_b64 s90, s[82:83]\n\t"
        "s_and_b64 s[82:83], vcc, s[84:85]\n\t"
        "s_bcnt1_i32_b64 s89, s[82:83]\n\t"
        "s_andn2_b64 s[82:83], vcc, s[84:85]\n\t"
        "s_bcnt1_i32_b64 s91, s[82:83]\n\t"
        "s_add_u32 s80, s86, s87\n\t"
        "s_add_u32 s80, s80, s88\n\t"
        "s_cmp_ge_u32 s80, 48\n\t"
        "s_cselect_b32 s80, 8, 24\n\t"
        "s_cmp_gt_u32 s86, s80\n\t"
        "s_cselect_b32 s86, %c[NX1], %c[NX0]\n\t"
        "s_cmp_gt_u32 s89, 16\n\t"
        "s_cselect_b32 s81, %c[NY1], %c[NY0]\n\t"
        "s_or_b32 s86, s86, s81\n\t"
        "s_cmp_gt_u32 s87, s80\n\t"
        "s_cselect_b32 s87, %c[NX1], %c[NX0]\n\t"
        "s_cmp_gt_u32 s90, 16\n\t"
        "s_cselect_b32 s81, %c[NY1], %c[NY0]\n\t"
        "s_or_b32 s87, s87, s81\n\t"
        "s_cmp_gt_u32 s88, s80\n\t"
        "s_cselect_b32 s88, %c[NX1], %c[NX0]\n\t"
        "s_cmp_gt_u32 s91, 16\n\t"
        "s_cselect_b32 s81, %c[NY1], %c[NY0]\n\t"
        "s_or_b32 s88, s88, s81\n\t"
        "s_xor_b32 %[nA], %[nA], s86\n\t"
        "s_xor_b32 %[nW], %[nW], s86\n\t"
        "s_xor_b32 %[nC], %[nC], s87\n\t"
        "s_xor_b32 %[nS], %[nS], s88\n\t"
        "s_xor_b32 %[nT], %[nT], s88\n\t"
        "s_xor_b32 %[b0], %[b0], s88\n\t"
        "s_cmp_eq_u32 %[w], 1\n\t"
        "s_cselect_b32 s80, s87, s86\n\t"
        "s_cmp_eq_u32 %[w], 2\n\t"
        "s_cselect_b32 s80, s88, s80\n\t"
        "s_xor_b32 %[pob], %[pob], s80\n\t"
        "s_lshl_b32 s80, %[w], 10\n\t"
        "s_sub_u32 %[nS], %[nS], s80\n\t"
        "s_sub_u32 %[pob], %[pob], s80\n\t"
        "ds_read_b32 v116, v120 offset:20484\n\t"
        "s_and_b32 s80, s87, 0x4000\n\t"
        "s_xor_b32 %[nC], %[nC], s80\n\t"
        "s_lshl_b32 s81, %[w], 8\n\t"
        "s_add_u32 s81, s81, 24576\n\t"
        "s_sub_u32 %[b0], %[b0], s81\n\t"
        "s_waitcnt lgkmcnt(0)\n\t"
        "v_xor_b32_e32 v116, s80, v116\n\t"
        "ds_write_b32 v120, v116 offset:20484\n\t"
        "s_mov_b32 s36, 0xf0000000\n\t"
        "s_mov_b32 s38, 0xff00ff00\n\t"
        "s_mov_b32 s39, 0xff00ff00\n\t"
        "s_mov_b32 s40, 0\n\t"
        "s_mov_b32 s37, 0\n\t"
        "s_brev_b32 s54, 1\n\t"
        "s_mov_b32 s46, %c[iD1]\n\t"
        "s_mov_b32 s45, %c[iD1]\n\t"
        "s_mov_b32 s47, %c[iD2]\n\t"
        "s_mov_b32 s48, 1\n\t"
        "s_mov_b32 s50, %c[LC]\n\t"
        "s_mov_b32 s51, %c[LA]\n\t"
        "s_mov_b32 s52, %c[E1]\n\t"
        "s_mov_b32 s53, %c[E2]\n\t"
        "s_mov_b32 s43, 0\n\t"
        "s_mov_b32 s61, %[b0]\n\t"
        "s_add_u32 s62, %[b0], 0x40000\n\t"
        "s_mov_b32 s75, 31744\n\t"
        "s_cmp_eq_u32 s64, s36\n\t"
        "s_cbranch_scc1 .Lk10%=\n\t"
        "s_mov_b32 s97, 0\n\t"
        ".Lk00%=:\n\t"
        "v_readfirstlane_b32 s82, v114\n\t"
        "v_readfirstlane_b32 s83, v115\n\t"
        "s_xor_b32 s82, s82, %[tag]\n\t"
        "s_xor_b32 s83, s83, %[tag]\n\t"
        "s_or_b32 s82, s82, s83\n\t"
        "s_cbranch_scc0 .Lk10%=\n\t"
        "s_sleep 3\n\t"
        "buffer_load_dwordx2 v[114:115], off, %[wsd], s64 sc1\n\t"
        "s_add_u32 s97, s97, 1\n\t"
        "s_cmp_lt_u32 s97, 0x200000\n\t"
        "s_cbranch_scc0 .Lgu%=\n\t"
        "s_waitcnt vmcnt(0)\n\t"
        "s_branch .Lk00%=\n\t"
        ".Lk10%=:\n\t"
        "s_add_u32 s80, s40, 0\n\t"
        "s_min_u32 s80, s80, 63\n\t"
        "s_lshl_b32 s80, s80, 18\n\t"
        "s_add_u32 s69, s80, %[kx]\n\t"
        "s_add_u32 s70, s69, 0x1000\n\t"
        "s_lshl_b32 s80, s40, 18\n\t"
        "s_add_u32 s71, s80, %[vx]\n\t"
        "s_add_u32 s80, s40, 1\n\t"
        "s_lshl_b32 s81, s80, 9\n\t"
        "s_add_u32 s81, s81, %[kf]\n\t"
        "s_and_b32 s80, s80, 0x43\n\t"
        "s_cselect_b32 s64, s36, s81\n\t"
        "buffer_load_dwordx4 v[104:107], v120, %[wsd], s69 offen sc1\n\t"
        "buffer_load_dwordx4 v[108:111], v120, %[wsd], s70 offen sc1\n\t"
        "s_waitcnt lgkmcnt(0)\n\t"
        "s_barrier\n\t"
        "ds_write_b128 v121, v[64:67] offset:16384\n\t"
        "ds_write_b128 v121, v[68:71] offset:17408\n\t"
        "ds_write_b128 v121, v[72:75] offset:18432\n\t"
        "ds_write_b128 v121, v[76:79] offset:19456\n\t"
        "ds_write_b128 v121, v[80:83] offset:20480\n\t"
        "ds_write_b128 v121, v[84:87] offset:21504\n\t"
        "ds_write_b128 v121, v[88:91] offset:22528\n\t"
        "ds_write_b128 v121, v[92:95] offset:23552\n\t"
        "ds_write_b128 v121, v[0:3] offset:24576\n\t"
        "ds_write_b128 v121, v[4:7] offset:25600\n\t"
        "ds_write_b128 v121, v[8:11] offset:26624\n\t"
        "ds_write_b128 v121, v[12:15] offset:27648\n\t"
        "ds_write_b128 v121, v[16:19] offset:28672\n\t"
        "ds_write_b128 v121, v[20:23] offset:29696\n\t"
        "ds_write_b128 v121, v[24:27] offset:30720\n\t"
        "ds_write_b128 v121, v[28:31] offset:31744\n\t"
        "s_waitcnt lgkmcnt(0)\n\t"
        "rec_s_code:\n\t"
        "ds_read_b128 v[16:19], v121 offset:0\n\t"
        "ds_read_b128 v[20:23], v121 offset:1024\n\t"
        "ds_read_b128 v[24:27], v121 offset:2048\n\t"
        "ds_read_b128 v[28:31], v121 offset:3072\n\t"
        "ds_read_b128 v[32:35], v121 offset:4096\n\t"
        "ds_read_b128 v[36:39], v121 offset:5120\n\t"
        "ds_read_b128 v[40:43], v121 offset:6144\n\t"
        "ds_read_b128 v[44:47], v121 offset:7168\n\t"
        "ds_read_b128 v[56:59], v121 offset:8192\n\t"
        "ds_read_b128 v[60:63], v121 offset:9216\n\t"
        "ds_read_b128 v[64:67], v121 offset:10240\n\t"
        "ds_read_b128 v[68:71], v121 offset:11264\n\t"
        "ds_read_b128 v[72:75], v121 offset:12288\n\t"
        "ds_read_b128 v[76:79], v121 offset:13312\n\t"
        "ds_read_b128 v[80:83], v121 offset:14336\n\t"
        "ds_read_b128 v[84:87], v121 offset:15360\n\t"
        "s_and_b32 s80, s40, 3\n\t"
        "s_lshl_b32 s80, s80, 17\n\t"
        "s_add_u32 s57, s80, %[nA]\n\t"
        "s_add_u32 s58, s57, 0x1000\n\t"
        "s_add_u32 s59, s57, 0x8000\n\t"
        "s_add_u32 s60, s57, 0x9000\n\t"
        "s_and_b32 s80, s40, 3\n\t"
        "s_lshl_b32 s80, s80, 17\n\t"
        "s_add_u32 s63, s80, %[nC]\n\t"
        "s_add_u32 s66, s80, %[nS]\n\t"
        "s_add_u32 s81, s80, 0x20000\n\t"
        "s_and_b32 s81, s81, 0x60000\n\t"
        "s_add_u32 s67, s81, %[nW]\n\t"
        "s_add_u32 s79, s81, %[nT]\n\t"
        "s_cmp_eq_u32 %[w], 2\n\t"
        "s_cselect_b32 s81, s80, s81\n\t"
        "s_xor_b32 s81, s81, 0x40000\n\t"
        "s_add_u32 s68, s81, %[pob]\n\t"
        "s_lshl_b32 s72, s40, 17\n\t"
        "s_add_u32 s72, s72, %[kvo]\n\t"
        "s_mov_b32 s88, 0\n\t"
        "s_mov_b32 s89, 0\n\t"
        "s_mov_b32 s90, 0\n\t"
        "v_mov_b32_e32 v88, 0\n\t"
        "v_mov_b32_e32 v89, 0\n\t"
        "v_mov_b32_e32 v90, 0\n\t"
        "v_mov_b32_e32 v91, 0\n\t"
        "v_mov_b32_e32 v92, 0\n\t"
        "v_mov_b32_e32 v93, 0\n\t"
        "v_mov_b32_e32 v94, 0\n\t"
        "v_mov_b32_e32 v95, 0\n\t"
        "v_mov_b32_e32 v96, 0\n\t"
        "v_mov_b32_e32 v97, 0\n\t"
        "v_mov_b32_e32 v98, 0\n\t"
        "v_mov_b32_e32 v99, 0\n\t"
        "v_mov_b32_e32 v100, 0\n\t"
        "v_mov_b32_e32 v101, 0\n\t"
        "v_mov_b32_e32 v102, 0\n\t"
        "v_mov_b32_e32 v103, 0\n\t"
        "v_mov_b32_e32 v52, v112\n\t"
        "v_mov_b32_e32 v53, v113\n\t"
        "v_mov_b32_e32 v49, 0\n\t"
        "v_lshlrev_b32_e32 v51, 6, v120\n\t"
        "s_cmp_eq_u32 %[w], 0\n\t"
        "s_cselect_b32 s73, %[nT], s36\n\t"
        "s_cselect_b32 s74, %[pra], s36\n\t"
        "v_mov_b32_e32 v50, s40\n\t"
        "s_cmp_ge_u32 s40, 60\n\t"
        "s_cselect_b32 s65, s74, s36\n\t"
        "s_waitcnt lgkmcnt(0)\n\t"
        "s_barrier\n\t"
        "s_memrealtime s[98:99]\n\t"
        "s_waitcnt lgkmcnt(0)\n\t"
        "s_mov_b32 s41, s98\n\t"
        "v_mov_b32_e32 v48, s98\n\t"
        ".p2align 6\n\t"
        ".Lloop%=:\n\t"
        "s_mov_b32 s97, 0\n\t"
        "s_movk_i32 s96, 0x1000\n\t"
        ".Lwa%=:\n\t"
        "s_memrealtime s[98:99]\n\t"
        "s_addk_i32 s96, -1\n\t"
        "s_cmp_lt_i32 s96, 0\n\t"
        "s_cbranch_scc1 .Lta%=\n\t"
        "s_waitcnt lgkmcnt(0)\n\t"
        "s_sub_i32 s98, s98, s41\n\t"
        "s_cmp_lt_i32 s98, %c[iFT]\n\t"
        "s_cbranch_scc1 .Lwa%=\n\t"
        "s_cmp_ge_i32 s98, 0\n\t"
        "s_cbranch_scc1 .Lxa%=\n\t"
        ".Lqa%=:\n\t"
        "s_nop %c[iFN]\n\t"
        "s_addk_i32 s98, 1\n\t"
        "s_cmp_lt_i32 s98, 0\n\t"
        "s_cbranch_scc1 .Lqa%=\n\t"
        ".Lxa%=:\n\t"
        "buffer_load_dwordx4 v[0:3], v120, %[srd], s57 offen sc1\n\t"
        "s_mov_b32 s80, s88\n\t"
        "s_add_i32 s81, s92, s50\n\t"
        "s_sub_i32 s81, s81, 4\n\t"
        "s_cmp_le_i32 s81, s44\n\t"
        "s_cselect_b32 s80, s80, 0\n\t"
        "s_cmp_lt_i32 s50, %c[LC24]\n\t"
        "s_cselect_b32 s80, s80, 0\n\t"
        "s_lshl2_add_u32 s50, s80, s50\n\t"
        "buffer_load_dwordx4 v[4:7], v120, %[srd], s58 offen sc1\n\t"
        "s_sub_i32 s50, s50, s89\n\t"
        "s_max_i32 s50, s50, %c[LC]\n\t"
        "s_lshl_b32 s80, s92, 1\n\t"
        "s_sub_i32 s81, s80, s52\n\t"
        "s_ashr_i32 s82, s81, %c[UPSH]\n\t"
        "s_max_i32 s82, s82, %c[TRUP]\n\t"
        "s_add_i32 s82, s82, s52\n\t"
        "s_min_i32 s82, s82, s80\n\t"
        "buffer_load_dwordx4 v[8:11], v120, %[srd], s59 offen sc1\n\t"
        "s_ashr_i32 s83, s81, %c[DNSH]\n\t"
        "s_min_i32 s83, s83, -%c[TRDN]\n\t"
        "s_add_i32 s83, s83, s52\n\t"
        "s_max_i32 s83, s83, s80\n\t"
        "s_cmp_gt_i32 s81, 0\n\t"
        "s_cselect_b32 s82, s82, s83\n\t"
        "s_cmp_lg_u32 s90, 0\n\t"
        "s_cselect_b32 s52, s82, s52\n\t"
        "buffer_load_dwordx4 v[12:15], v120, %[srd], s60 offen sc1\n\t"
        "s_add_i32 s80, s52, 1\n\t"
        "s_ashr_i32 s80, s80, 1\n\t"
        "s_add_i32 s45, s80, s50\n\t"
        "s_add_u32 s80, s40, 1\n\t"
        "s_min_u32 s80, s80, 63\n\t"
        "s_lshl_b32 s80, s80, 18\n\t"
        "s_add_u32 s69, s80, %[kx]\n\t"
        "s_add_u32 s70, s69, 0x1000\n\t"
        "s_lshl_b32 s80, s40, 18\n\t"
        "s_add_u32 s71, s80, %[vx]\n\t"
        "s_add_u32 s80, s40, 2\n\t"
        "s_lshl_b32 s81, s80, 9\n\t"
        "s_add_u32 s81, s81, %[kf]\n\t"
        "s_and_b32 s80, s80, 0x43\n\t"
        "s_cselect_b32 s64, s36, s81\n\t"
        "s_nop 15\n\t"
        "s_nop 7\n\t"
        "s_mov_b64 exec, 7\n\t"
        "buffer_store_dwordx2 v[48:49], v51, %[srd], s73 offen sc1\n\t"
        "s_mov_b64 exec, -1\n\t"
        "buffer_load_dwordx4 v[112:115], v120, %[wsd], s71 offen offset:0 sc1\n\t"
        "buffer_load_dwordx4 v[116:119], v120, %[wsd], s71 offen offset:1024 sc1\n\t"
        ".Lgo%=:\n\t"
        "s_waitcnt vmcnt(6)\n\t"
        "v_smfmac_f32_16x16x64_bf16 v[88:91], v[0:3], v[16:23], v122\n\t"
        "ds_write_b128 v120, v[0:3] offset:0\n\t"
        "v_smfmac_f32_16x16x64_bf16 v[92:95], v[0:3], v[24:31], v122\n\t"
        "v_smfmac_f32_16x16x64_bf16 v[96:99], v[0:3], v[32:39], v122\n\t"
        "v_smfmac_f32_16x16x64_bf16 v[100:103], v[0:3], v[40:47], v122\n\t"
        "s_waitcnt vmcnt(5)\n\t"
        "v_smfmac_f32_16x16x64_bf16 v[88:91], v[4:7], v[56:63], v122\n\t"
        "ds_write_b128 v120, v[4:7] offset:4096\n\t"
        "v_smfmac_f32_16x16x64_bf16 v[92:95], v[4:7], v[64:71], v122\n\t"
        "v_smfmac_f32_16x16x64_bf16 v[96:99], v[4:7], v[72:79], v122\n\t"
        "v_smfmac_f32_16x16x64_bf16 v[100:103], v[4:7], v[80:87], v122\n\t"
        "s_waitcnt vmcnt(4)\n\t"
        "v_smfmac_f32_16x16x64_bf16 v[88:91], v[8:11], v[128:135], v122\n\t"
        "ds_write_b128 v120, v[8:11] offset:8192\n\t"
        "v_smfmac_f32_16x16x64_bf16 v[92:95], v[8:11], v[136:143], v122\n\t"
        "s_waitcnt lgkmcnt(1)\n\t"
        "s_barrier\n\t"
        "v_smfmac_f32_16x16x64_bf16 v[96:99], v[8:11], v[192:199], v122\n\t"
        "ds_read_b128 v[16:19], v124 offset:0\n\t"
        "v_smfmac_f32_16x16x64_bf16 v[100:103], v[8:11], v[200:207], v122\n\t"
        "ds_read_b128 v[20:23], v125 offset:0\n\t"
        "s_waitcnt vmcnt(3)\n\t"
        "v_smfmac_f32_16x16x64_bf16 v[88:91], v[12:15], a[0:7], v122\n\t"
        "ds_write_b128 v120, v[12:15] offset:12288\n\t"
        "v_smfmac_f32_16x16x64_bf16 v[92:95], v[12:15], a[8:15], v122\n\t"
        "buffer_store_dword v50, off, %[wsd], s65 sc1\n\t"
        "v_smfmac_f32_16x16x64_bf16 v[96:99], v[12:15], a[64:71], v122\n\t"
        "ds_read_b128 v[24:27], v126 offset:0\n\t"
        "v_smfmac_f32_16x16x64_bf16 v[100:103], v[12:15], a[72:79], v122\n\t"
        "ds_read_b128 v[28:31], v124 offset:4096\n\t"
        "s_waitcnt lgkmcnt(4)\n\t"
        "v_smfmac_f32_16x16x64_bf16 v[88:91], v[16:19], a[16:23], v122\n\t"
        "ds_read_b128 v[32:35], v125 offset:4096\n\t"
        "v_smfmac_f32_16x16x64_bf16 v[92:95], v[16:19], a[24:31], v122\n\t"
        "ds_read_b128 v[36:39], v126 offset:4096\n\t"
        "s_waitcnt lgkmcnt(5)\n\t"
        "v_smfmac_f32_16x16x64_bf16 v[88:91], v[20:23], a[32:39], v122\n\t"
        "ds_read_b128 v[72:75], v121 offset:16384\n\t"
        "v_smfmac_f32_16x16x64_bf16 v[92:95], v[20:23], a[40:47], v122\n\t"
        "s_waitcnt lgkmcnt(5)\n\t"
        "s_barrier\n\t"
        "ds_read_b128 v[76:79], v121 offset:17408\n\t"
        "s_waitcnt lgkmcnt(5)\n\t"
        "v_smfmac_f32_16x16x64_bf16 v[88:91], v[24:27], a[48:55], v122\n\t"
        "ds_read_b128 v[0:3], v124 offset:12288\n\t"
        "v_smfmac_f32_16x16x64_bf16 v[92:95], v[24:27], a[56:63], v122\n\t"
        "ds_read_b128 v[80:83], v121 offset:18432\n\t"
        "s_waitcnt lgkmcnt(6)\n\t"
        "v_smfmac_f32_16x16x64_bf16 v[88:91], v[28:31], a[144:151], v122\n\t"
        "ds_read_b128 v[84:87], v121 offset:19456\n\t"
        "v_smfmac_f32_16x16x64_bf16 v[92:95], v[28:31], a[152:159], v122\n\t"
        "ds_read_b128 v[40:43], v124 offset:8192\n\t"
        "s_waitcnt lgkmcnt(7)\n\t"
        "v_smfmac_f32_16x16x64_bf16 v[88:91], v[32:35], a[160:167], v122\n\t"
        "ds_read_b128 v[44:47], v125 offset:8192\n\t"
        "v_mov_b32_e32 v12, 0x3f803f80\n\t"
        "v_smfmac_f32_16x16x64_bf16 v[92:95], v[32:35], a[168:175], v122\n\t"
        "ds_read_b128 v[56:59], v121 offset:20480\n\t"
        "v_mov_b32_e32 v13, 0x3f803f80\n\t"
        "s_waitcnt lgkmcnt(8)\n\t"
        "v_smfmac_f32_16x16x64_bf16 v[88:91], v[36:39], a[176:183], v122\n\t"
        "ds_read_b128 v[60:63], v121 offset:21504\n\t"
        "v_mov_b32_e32 v14, 0x3f803f80\n\t"
        "v_smfmac_f32_16x16x64_bf16 v[92:95], v[36:39], a[184:191], v122\n\t"
        "ds_read_b128 v[4:7], v125 offset:12288\n\t"
        "v_mov_b32_e32 v15, 0x3f803f80\n\t"
        "s_waitcnt lgkmcnt(8)\n\t"
        "s_waitcnt lgkmcnt(7)\n\t"
        "v_smfmac_f32_16x16x64_bf16 v[88:91], v[0:3], v[72:79], v122\n\t"
        "ds_read_b128 v[64:67], v121 offset:22528\n\t"
        "s_waitcnt lgkmcnt(6)\n\t"
        "v_smfmac_f32_16x16x64_bf16 v[92:95], v[0:3], v[80:87], v122\n\t"
        "ds_read_b128 v[68:71], v121 offset:23552\n\t"
        "s_waitcnt lgkmcnt(6)\n\t"
        "v_smfmac_f32_16x16x64_bf16 v[88:91], v[40:43], v[144:151], v122\n\t"
        "ds_read_b128 v[48:51], v126 offset:8192\n\t"
        "v_smfmac_f32_16x16x64_bf16 v[92:95], v[40:43], v[152:159], v122\n\t"
        "ds_read_b128 v[8:11], v126 offset:12288\n\t"
        "s_waitcnt lgkmcnt(7)\n\t"
        "v_smfmac_f32_16x16x64_bf16 v[88:91], v[44:47], v[160:167], v122\n\t"
        "ds_read_b128 v[72:75], v121 offset:24576\n\t"
        "v_smfmac_f32_16x16x64_bf16 v[92:95], v[44:47], v[168:175], v122\n\t"
        "ds_read_b128 v[76:79], v121 offset:25600\n\t"
        "s_waitcnt lgkmcnt(7)\n\t"
        "s_waitcnt lgkmcnt(6)\n\t"
        "v_smfmac_f32_16x16x64_bf16 v[88:91], v[4:7], v[56:63], v122\n\t"
        "ds_read_b128 v[80:83], v121 offset:26624\n\t"
        "s_waitcnt lgkmcnt(5)\n\t"
        "v_smfmac_f32_16x16x64_bf16 v[92:95], v[4:7], v[64:71], v122\n\t"
        "ds_read_b128 v[84:87], v121 offset:27648\n\t"
        "s_waitcnt lgkmcnt(5)\n\t"
        "v_smfmac_f32_16x16x64_bf16 v[88:91], v[48:51], v[176:183], v122\n\t"
        "v_mov_b32_e32 v64, 0\n\t"
        "v_smfmac_f32_16x16x64_bf16 v[92:95], v[48:51], v[184:191], v122\n\t"
        "v_mov_b32_e32 v65, 0\n\t"
        "s_waitcnt lgkmcnt(4)\n\t"
        "v_smfmac_f32_16x16x64_bf16 v[88:91], v[8:11], a[128:135], v122\n\t"
        "v_mov_b32_e32 v66, 0\n\t"
        "v_smfmac_f32_16x16x64_bf16 v[92:95], v[8:11], a[136:143], v122\n\t"
        "v_mov_b32_e32 v67, 0\n\t"
        "s_waitcnt lgkmcnt(2)\n\t"
        "v_smfmac_f32_16x16x64_bf16 v[96:99], v[0:3], v[72:79], v122\n\t"
        "ds_read_b128 v[72:75], v121 offset:28672\n\t"
        "s_waitcnt lgkmcnt(1)\n\t"
        "v_smfmac_f32_16x16x64_bf16 v[100:103], v[0:3], v[80:87], v122\n\t"
        "ds_read_b128 v[76:79], v121 offset:29696\n\t"
        "v_smfmac_f32_16x16x64_bf16 v[96:99], v[16:19], a[80:87], v122\n\t"
        "v_add_f32_e32 v88, v88, v90\n\t"
        "v_add_f32_e32 v89, v89, v91\n\t"
        "v_smfmac_f32_16x16x64_bf16 v[100:103], v[16:19], a[88:95], v122\n\t"
        "v_cvt_pk_bf16_f32 v90, v88, v89\n\t"
        "v_add_f32_e32 v92, v92, v94\n\t"
        "v_smfmac_f32_16x16x64_bf16 v[96:99], v[20:23], a[96:103], v122\n\t"
        "ds_write_b32 v127, v90\n\t"
        "v_add_f32_e32 v93, v93, v95\n\t"
        "v_cvt_pk_bf16_f32 v91, v92, v93\n\t"
        "v_smfmac_f32_16x16x64_bf16 v[100:103], v[20:23], a[104:111], v122\n\t"
        "ds_write_b32 v127, v91 offset:384\n\t"
        "v_cmp_u_f32_e32 vcc, v88, v89\n\t"
        "v_smfmac_f32_16x16x64_bf16 v[96:99], v[24:27], a[112:119], v122\n\t"
        "ds_read_b64_tr_b16 v[56:57], v52 offset:0\n\t"
        "ds_read_b64_tr_b16 v[58:59], v52 offset:64\n\t"
        "v_smfmac_f32_16x16x64_bf16 v[100:103], v[24:27], a[120:127], v122\n\t"
        "ds_read_b64_tr_b16 v[60:61], v53 offset:0\n\t"
        "ds_read_b64_tr_b16 v[62:63], v53 offset:64\n\t"
        "v_smfmac_f32_16x16x64_bf16 v[96:99], v[28:31], a[208:215], v122\n\t"
        "s_cbranch_vccnz .Lf1%=\n\t"
        ".Lr1%=:\n\t"
        "ds_read_b128 v[80:83], v121 offset:30720\n\t"
        "v_smfmac_f32_16x16x64_bf16 v[100:103], v[28:31], a[216:223], v122\n\t"
        "ds_read_b128 v[84:87], v121 offset:31744\n\t"
        "v_mov_b32_e32 v91, 0\n\t"
        "v_smfmac_f32_16x16x64_bf16 v[96:99], v[32:35], a[224:231], v122\n\t"
        "v_mov_b32_e32 v94, 0\n\t"
        "v_smfmac_f32_16x16x64_bf16 v[100:103], v[32:35], a[232:239], v122\n\t"
        "ds_read_b128 v[32:35], v120 offset:20480\n\t"
        "v_smfmac_f32_16x16x64_bf16 v[96:99], v[36:39], a[240:247], v122\n\t"
        "s_waitcnt lgkmcnt(3)\n\t"
        "s_barrier\n\t"
        "v_mfma_f32_16x16x32_bf16 v[68:71], v[104:107], v[56:59], 0\n\t"
        "ds_read_b64_tr_b16 v[16:17], v123 offset:0\n\t"
        "v_mfma_f32_16x16x32_bf16 v[68:71], v[108:111], v[60:63], v[68:71]\n\t"
        "ds_read_b64_tr_b16 v[18:19], v123 offset:64\n\t"
        "v_smfmac_f32_16x16x64_bf16 v[100:103], v[36:39], a[248:255], v122\n\t"
        "buffer_load_dword v88, v127, %[srd], s61 offen offset:0 sc1\n\t"
        "v_smfmac_f32_16x16x64_bf16 v[96:99], v[8:11], a[192:199], v122\n\t"
        "buffer_load_dword v89, v127, %[srd], s61 offen offset:256 sc1\n\t"
        "v_smfmac_f32_16x16x64_bf16 v[100:103], v[8:11], a[200:207], v122\n\t"
        "s_nop 1\n\t"
        "ds_write_b128 v120, v[68:71] offset:16384\n\t"
        "v_smfmac_f32_16x16x64_bf16 v[96:99], v[4:7], v[72:79], v122\n\t"
        "ds_read_b64_tr_b16 v[20:21], v123 offset:768\n\t"
        "s_waitcnt lgkmcnt(5)\n\t"
        "v_smfmac_f32_16x16x64_bf16 v[100:103], v[4:7], v[80:87], v122\n\t"
        "ds_read_b64_tr_b16 v[22:23], v123 offset:832\n\t"
        "v_smfmac_f32_16x16x64_bf16 v[96:99], v[40:43], v[208:215], v122\n\t"
        "ds_read_b64_tr_b16 v[24:25], v123 offset:1536\n\t"
        "ds_read_b64_tr_b16 v[26:27], v123 offset:1600\n\t"
        "v_smfmac_f32_16x16x64_bf16 v[100:103], v[40:43], v[216:223], v122\n\t"
        "buffer_load_dwordx4 v[84:87], v120, %[srd], s66 offen sc1\n\t"
        "v_smfmac_f32_16x16x64_bf16 v[96:99], v[44:47], v[224:231], v122\n\t"
        "s_waitcnt lgkmcnt(4)\n\t"
        "s_barrier\n\t"
        "ds_read_b128 v[36:39], v124 offset:16384\n\t"
        "v_smfmac_f32_16x16x64_bf16 v[100:103], v[44:47], v[232:239], v122\n\t"
        "ds_read_b128 v[56:59], v125 offset:16384\n\t"
        "v_smfmac_f32_16x16x64_bf16 v[96:99], v[48:51], v[240:247], v122\n\t"
        "ds_read_b128 v[60:63], v126 offset:16384\n\t"
        "v_smfmac_f32_16x16x64_bf16 v[100:103], v[48:51], v[248:255], v122\n\t"
        "ds_read_b64_tr_b16 v[28:29], v123 offset:2304\n\t"
        "ds_read_b64_tr_b16 v[30:31], v123 offset:2368\n\t"
        "s_waitcnt lgkmcnt(4)\n\t"
        "s_cmp_eq_u32 s40, 63\n\t"
        "s_nop 1\n\t"
        "v_add_f32_e32 v96, v96, v98\n\t"
        "v_add_f32_e32 v97, v97, v99\n\t"
        "s_waitcnt lgkmcnt(2)\n\t"
        "v_cvt_pk_bf16_f32 v50, v96, v97\n\t"
        "v_pk_add_f32 v[100:101], v[100:101], v[102:103]\n\t"
        "ds_write_b32 v35, v50\n\t"
        "v_cvt_pk_bf16_f32 v51, v100, v101\n\t"
        "ds_write_b32 v35, v51 offset:384\n\t"
        "v_pk_add_f32 v[56:57], v[56:57], v[60:61]\n\t"
        "s_cbranch_scc1 .Lend%=\n\t"
        "v_pk_add_f32 v[58:59], v[58:59], v[62:63]\n\t"
        "v_pk_add_f32 v[68:69], v[68:69], v[36:37]\n\t"
        "v_pk_add_f32 v[70:71], v[70:71], v[38:39]\n\t"
        "v_pk_add_f32 v[68:69], v[68:69], v[56:57]\n\t"
        "v_pk_add_f32 v[70:71], v[70:71], v[58:59]\n\t"
        "v_max3_f32 v2, v68, v69, v70\n\t"
        "v_max_f32_e32 v2, v2, v71\n\t"
        "s_nop 1\n\t"
        "v_max_f32_dpp v3, v2, v2 row_ror:8 row_mask:0xf bank_mask:0xf\n\t"
        "v_max_f32_dpp v1, v2, v2 row_ror:8 row_mask:0xf bank_mask:0xf\n\t"
        "s_nop 1\n\t"
        "v_permlane16_swap_b32_e32 v3, v1\n\t"
        "s_waitcnt lgkmcnt(0)\n\t"
        "s_barrier\n\t"
        "ds_read_b64_tr_b16 v[72:73], v32 offset:0\n\t"
        "v_max_f32_e32 v2, v3, v1\n\t"
        "ds_read_b64_tr_b16 v[74:75], v32 offset:128\n\t"
        "v_max_f32_e32 v3, v3, v1\n\t"
        "ds_read_b64_tr_b16 v[76:77], v32 offset:1536\n\t"
        "s_nop 0\n\t"
        "v_permlane32_swap_b32_e32 v2, v3\n\t"
        "ds_read_b64_tr_b16 v[78:79], v32 offset:1664\n\t"
        "v_max_f32_e32 v0, v2, v3\n\t"
        "v_mul_f32_e32 v2, 0xbe0293ee, v0\n\t"
        "v_fmamk_f32 v68, v68, 0x3e0293ee, v2\n\t"
        "v_fmamk_f32 v69, v69, 0x3e0293ee, v2\n\t"
        "v_fmamk_f32 v70, v70, 0x3e0293ee, v2\n\t"
        "v_fmamk_f32 v71, v71, 0x3e0293ee, v2\n\t"
        "v_exp_f32_e32 v68, v68\n\t"
        "s_waitcnt lgkmcnt(2)\n\t"
        "v_smfmac_f32_16x16x64_bf16 v[64:67], v[72:75], v[16:23], v122\n\t"
        "s_waitcnt lgkmcnt(0)\n\t"
        "v_smfmac_f32_16x16x64_bf16 v[64:67], v[76:79], v[24:31], v122\n\t"
        "s_waitcnt vmcnt(4)\n\t"
        "v_exp_f32_e32 v69, v69\n\t"
        "v_exp_f32_e32 v70, v70\n\t"
        "v_cvt_pk_bf16_f32 v36, v68, v69\n\t"
        "v_exp_f32_e32 v71, v71\n\t"
        "s_nop 0\n\t"
        "v_mov_b32_dpp v38, v36 row_ror:8 row_mask:0xf bank_mask:0xf\n\t"
        "v_cvt_pk_bf16_f32 v37, v70, v71\n\t"
        "s_nop 1\n\t"
        "v_mov_b32_dpp v39, v37 row_ror:8 row_mask:0xf bank_mask:0xf\n\t"
        "v_pk_add_f32 v[64:65], v[64:65], v[66:67]\n\t"
        "s_nop 0\n\t"
        "v_mfma_f32_16x16x32_bf16 v[96:99], v[12:15], v[36:39], 0\n\t"
        "v_fmamk_f32 v66, v64, 0x3e0293ee, v2\n\t"
        "v_fmamk_f32 v67, v65, 0x3e0293ee, v2\n\t"
        "v_mfma_f32_16x16x32_bf16 v[40:43], v[112:115], v[36:39], 0\n\t"
        "v_max_f32_e32 v3, v66, v67\n\t"
        "v_exp_f32_e32 v66, v66\n\t"
        "v_mfma_f32_16x16x32_bf16 v[44:47], v[116:119], v[36:39], 0\n\t"
        "v_cmp_nge_f32_e32 vcc, 0x41a00000, v3\n\t"
        "v_exp_f32_e32 v67, v67\n\t"
        "v_mov_b32_e32 v10, 1.0\n\t"
        "v_cvt_pk_bf16_f32 v90, v66, v67\n\t"
        "v_cvt_pk_bf16_f32 v95, v66, v67\n\t"
        "s_cbranch_vccnz .Lfm%=\n\t"
        ".Lrm%=:\n\t"
        "s_waitcnt vmcnt(1)\n\t"
        "v_mfma_f32_16x16x16_bf16 v[96:99], v[12:13], v[90:91], v[96:99]\n\t"
        "v_max_u32_e32 v54, v88, v89\n\t"
        "v_mfma_f32_16x16x16_bf16 v[40:43], v[88:89], v[90:91], v[40:43]\n\t"
        "v_cmp_eq_u32_e32 vcc, -1, v54\n\t"
        "v_mfma_f32_16x16x16_bf16 v[44:47], v[88:89], v[94:95], v[44:47]\n\t"
        "s_nop 1\n\t"
        "v_mov_b32_e32 v55, v55\n\t"
        "s_nop 0\n\t"
        "v_rcp_f32_e32 v2, v96\n\t"
        "s_cbranch_vccnz .Lfb%=\n\t"
        ".Lrb%=:\n\t"
        "v_pk_mul_f32 v[40:41], v[40:41], v[2:3] op_sel_hi:[1,0]\n\t"
        "v_pk_mul_f32 v[42:43], v[42:43], v[2:3] op_sel_hi:[1,0]\n\t"
        "v_cvt_pk_bf16_f32 v100, v40, v41\n\t"
        "v_pk_mul_f32 v[44:45], v[44:45], v[2:3] op_sel_hi:[1,0]\n\t"
        "v_pk_mul_f32 v[46:47], v[46:47], v[2:3] op_sel_hi:[1,0]\n\t"
        "v_cvt_pk_bf16_f32 v101, v42, v43\n\t"
        "v_cvt_pk_bf16_f32 v102, v44, v45\n\t"
        "v_min_u32_e32 v100, -2, v100\n\t"
        "v_cvt_pk_bf16_f32 v103, v46, v47\n\t"
        "v_min_u32_e32 v102, -2, v102\n\t"
        "buffer_store_dwordx4 v[100:103], v33, %[srd], s63 offen sc1\n\t"
        "s_memrealtime s[76:77]\n\t"
        "v_mov_b32_e32 v8, -1\n\t"
        "v_mov_b32_e32 v9, -1\n\t"
        "v_mov_b32_e32 v10, -1\n\t"
        "v_mov_b32_e32 v11, -1\n\t"
        "v_permlane16_swap_b32_e32 v50, v51\n\t"
        "v_permlane16_swap_b32_e32 v88, v89\n\t"
        "v_mov_b32_e32 v14, s40\n\t"
        "s_waitcnt lgkmcnt(0)\n\t"
        "s_waitcnt vmcnt(1)\n\t"
        "s_mov_b32 s97, 0\n\t"
        ".Lsr%=:\n\t"
        "s_mov_b32 exec_hi, 0xffff\n\t"
        "v_subrev_u32_e32 v0, s41, v84\n\t"
        "v_subrev_u32_e32 v1, s41, v86\n\t"
        "v_max_i32_e32 v0, v0, v1\n\t"
        "v_max_u32_e32 v1, v85, v87\n\t"
        "v_cmp_eq_u32_e32 vcc, -1, v1\n\t"
        "v_max_i32_dpp v0, v0, v0 quad_perm:[1,0,3,2] row_mask:0xf bank_mask:0xf\n\t"
        "v_or_b32_e32 v2, v85, v87\n\t"
        "s_nop 0\n\t"
        "v_max_i32_dpp v0, v0, v0 quad_perm:[2,3,0,1] row_mask:0xf bank_mask:0xf\n\t"
        "s_nop 1\n\t"
        "v_max_i32_dpp v0, v0, v0 row_ror:4 row_mask:0xf bank_mask:0xf\n\t"
        "s_nop 1\n\t"
        "v_max_i32_dpp v0, v0, v0 row_ror:8 row_mask:0xf bank_mask:0xf\n\t"
        "s_cbranch_vccnz .Lsf%=\n\t"
        "v_readlane_b32 s85, v0, 0\n\t"
        "v_readlane_b32 s81, v0, 16\n\t"
        "v_readlane_b32 s56, v0, 32\n\t"
        "v_and_b32_e32 v1, 1, v2\n\t"
        "v_cmp_ne_u32_e64 s[86:87], 0, v1\n\t"
        "v_and_b32_e32 v1, 2, v2\n\t"
        "v_cmp_ne_u32_e64 s[88:89], 0, v1\n\t"
        "v_and_b32_e32 v1, 4, v2\n\t"
        "v_cmp_ne_u32_e64 s[90:91], 0, v1\n\t"
        "s_max_i32 s56, s56, s81\n\t"
        "s_add_u32 s92, s85, s41\n\t"
        "s_sub_u32 s92, s92, s43\n\t"
        "s_add_i32 s93, s56, s47\n\t"
        "s_mov_b32 s44, s46\n\t"
        "s_mov_b32 s46, s45\n\t"
        "s_mov_b32 s94, s48\n\t"
        "s_mov_b32 s43, s41\n\t"
        "s_cmp_gt_i32 s56, s54\n\t"
        "s_cbranch_scc1 .Lmv%=\n\t"
        "s_mov_b32 s37, 0\n\t"
        "s_mov_b32 s48, 0\n\t"
        "s_mov_b32 s95, 0\n\t"
        ".Lsd%=:\n\t"
        "s_mov_b32 exec_hi, -1\n\t"
        "s_add_u32 s42, s43, s46\n\t"
        "s_sleep %c[HSLP]\n\t"
        "buffer_load_dwordx2 v[48:49], off, %[wsd], s64 sc1\n\t"
        "buffer_load_dwordx4 v[104:107], v120, %[wsd], s69 offen sc1\n\t"
        "buffer_load_dwordx4 v[108:111], v120, %[wsd], s70 offen sc1\n\t"
        "buffer_store_dwordx2 v[50:51], v34, %[wsd], s72 offen sc1\n\t"
        "buffer_store_dwordx2 v[88:89], v34, %[wsd], s72 offen offset:2048 sc1\n\t"
        "s_lshl_b32 s80, %[w], 8\n\t"
        "s_add_u32 s80, s80, s61\n\t"
        "s_add_u32 s80, s80, 24576\n\t"
        "buffer_store_dwordx2 v[8:9], v34, %[srd], s80 offen sc1\n\t"
        "s_cmp_eq_u32 %[w], 0\n\t"
        "s_cselect_b32 exec_hi, 0, -1\n\t"
        "buffer_store_dwordx4 v[8:11], v120, %[srd], s68 offen sc1\n\t"
        "s_mov_b32 exec_hi, -1\n\t"
        "s_mov_b64 exec, 1\n\t"
        "buffer_store_dword v14, off, %[wsd], s74 sc1\n\t"
        "s_mov_b64 exec, -1\n\t"
        "ds_read_b128 v[16:19], v121 offset:0\n\t"
        "ds_read_b128 v[20:23], v121 offset:1024\n\t"
        "ds_read_b128 v[24:27], v121 offset:2048\n\t"
        "ds_read_b128 v[28:31], v121 offset:3072\n\t"
        "ds_read_b128 v[32:35], v121 offset:4096\n\t"
        "ds_read_b128 v[36:39], v121 offset:5120\n\t"
        "ds_read_b128 v[40:43], v121 offset:6144\n\t"
        "ds_read_b128 v[44:47], v121 offset:7168\n\t"
        "ds_read_b128 v[56:59], v121 offset:8192\n\t"
        "ds_read_b128 v[60:63], v121 offset:9216\n\t"
        "ds_read_b128 v[64:67], v121 offset:10240\n\t"
        "ds_read_b128 v[68:71], v121 offset:11264\n\t"
        "ds_read_b128 v[72:75], v121 offset:12288\n\t"
        "ds_read_b128 v[76:79], v121 offset:13312\n\t"
        "ds_read_b128 v[80:83], v121 offset:14336\n\t"
        "ds_read_b128 v[84:87], v121 offset:15360\n\t"
        "s_cmp_ge_u32 s40, %c[iCT0]\n\t"
        "s_cselect_b32 s85, 1, 0\n\t"
        "s_cmp_lg_u64 s[86:87], 0\n\t"
        "s_cselect_b32 s86, s85, 0\n\t"
        "s_cmp_lg_u64 s[88:89], 0\n\t"
        "s_cselect_b32 s87, s85, 0\n\t"
        "s_cmp_lg_u64 s[90:91], 0\n\t"
        "s_cselect_b32 s88, s85, 0\n\t"
        "s_or_b32 s80, s94, s86\n\t"
        "s_or_b32 s80, s80, s87\n\t"
        "s_andn2_b32 s90, s85, s80\n\t"
        "s_cmp_le_i32 s92, s44\n\t"
        "s_cselect_b32 s80, s85, 0\n\t"
        "s_andn2_b32 s91, s80, s88\n\t"
        "s_and_b32 s80, s40, %c[LDEC]\n\t"
        "s_cselect_b32 s89, 0, 1\n\t"
        "s_andn2_b32 s80, s86, s94\n\t"
        "s_cmp_lt_i32 s51, %c[LA24]\n\t"
        "s_cselect_b32 s80, s80, 0\n\t"
        "s_lshl2_add_u32 s51, s80, s51\n\t"
        "s_sub_i32 s51, s51, s89\n\t"
        "s_max_i32 s51, s51, %c[LA]\n\t"
        "s_lshl_b32 s80, s93, 1\n\t"
        "s_sub_i32 s81, s80, s53\n\t"
        "s_ashr_i32 s82, s81, %c[UPSH]\n\t"
        "s_max_i32 s82, s82, %c[TRUP]\n\t"
        "s_add_i32 s82, s82, s53\n\t"
        "s_min_i32 s82, s82, s80\n\t"
        "s_ashr_i32 s83, s81, %c[DNSH]\n\t"
        "s_min_i32 s83, s83, -%c[TRDN]\n\t"
        "s_add_i32 s83, s83, s53\n\t"
        "s_max_i32 s83, s83, s80\n\t"
        "s_cmp_gt_i32 s81, 0\n\t"
        "s_cselect_b32 s82, s82, s83\n\t"
        "s_cmp_lg_u32 s91, 0\n\t"
        "s_cselect_b32 s53, s82, s53\n\t"
        "s_add_i32 s80, s53, 1\n\t"
        "s_ashr_i32 s80, s80, 1\n\t"
        "s_add_i32 s47, s80, s51\n\t"
        "s_add_u32 s81, s47, %c[AUP]\n\t"
        "s_min_i32 s80, s95, s81\n\t"
        "s_max_i32 s47, s47, s80\n\t"
        "s_add_u32 s41, s42, s47\n\t"
        "s_cmp_eq_u32 s40, 0\n\t"
        "s_cselect_b32 s51, %c[LA], s51\n\t"
        "v_mov_b32_e32 v88, 0\n\t"
        "v_mov_b32_e32 v89, 0\n\t"
        "v_mov_b32_e32 v90, 0\n\t"
        "v_mov_b32_e32 v91, 0\n\t"
        "v_mov_b32_e32 v92, 0\n\t"
        "v_mov_b32_e32 v93, 0\n\t"
        "v_mov_b32_e32 v94, 0\n\t"
        "v_mov_b32_e32 v95, 0\n\t"
        "v_mov_b32_e32 v96, 0\n\t"
        "v_mov_b32_e32 v97, 0\n\t"
        "v_mov_b32_e32 v98, 0\n\t"
        "v_mov_b32_e32 v99, 0\n\t"
        "v_mov_b32_e32 v100, 0\n\t"
        "v_mov_b32_e32 v101, 0\n\t"
        "v_mov_b32_e32 v102, 0\n\t"
        "v_mov_b32_e32 v103, 0\n\t"
        "s_add_u32 s40, s40, 1\n\t"
        "v_mov_b32_e32 v6, 30720\n\t"
        "ds_read_b64 v[6:7], v6\n\t"
        "s_waitcnt lgkmcnt(0)\n\t"
        "v_readfirstlane_b32 s80, v6\n\t"
        "v_readfirstlane_b32 s81, v7\n\t"
        "s_cmp_eq_u32 s80, s40\n\t"
        "s_cselect_b32 s84, 1, 0\n\t"
        "s_cmp_eq_u32 s81, s40\n\t"
        "s_cselect_b32 s81, 2, 0\n\t"
        "s_or_b32 s84, s84, s81\n\t"
        "s_mov_b32 s85, s79\n\t"
        "s_and_b32 s80, s40, 3\n\t"
        "s_lshl_b32 s80, s80, 17\n\t"
        "s_add_u32 s57, s80, %[nA]\n\t"
        "s_add_u32 s58, s57, 0x1000\n\t"
        "s_add_u32 s59, s57, 0x8000\n\t"
        "s_add_u32 s60, s57, 0x9000\n\t"
        "s_and_b32 s80, s40, 3\n\t"
        "s_lshl_b32 s80, s80, 17\n\t"
        "s_add_u32 s63, s80, %[nC]\n\t"
        "s_add_u32 s66, s80, %[nS]\n\t"
        "s_add_u32 s81, s80, 0x20000\n\t"
        "s_and_b32 s81, s81, 0x60000\n\t"
        "s_add_u32 s67, s81, %[nW]\n\t"
        "s_add_u32 s79, s81, %[nT]\n\t"
        "s_cmp_eq_u32 %[w], 2\n\t"
        "s_cselect_b32 s81, s80, s81\n\t"
        "s_xor_b32 s81, s81, 0x40000\n\t"
        "s_add_u32 s68, s81, %[pob]\n\t"
        "s_lshl_b32 s72, s40, 17\n\t"
        "s_add_u32 s72, s72, %[kvo]\n\t"
        "v_lshlrev_b32_e32 v51, 6, v120\n\t"
        "s_cmp_eq_u32 %[w], 0\n\t"
        "s_cselect_b32 s73, s85, s36\n\t"
        "s_cselect_b32 s74, %[pra], s36\n\t"
        "v_mov_b32_e32 v50, s40\n\t"
        "s_cmp_ge_u32 s40, 60\n\t"
        "s_cselect_b32 s65, s74, s36\n\t"
        "s_mov_b32 s80, s61\n\t"
        "s_mov_b32 s61, s62\n\t"
        "s_mov_b32 s62, s80\n\t"
        "s_waitcnt vmcnt(7)\n\t"
        "s_cmp_eq_u32 s64, s36\n\t"
        "s_cbranch_scc1 .Lk11%=\n\t"
        "s_mov_b32 s97, 0\n\t"
        ".Lk01%=:\n\t"
        "v_readfirstlane_b32 s82, v48\n\t"
        "v_readfirstlane_b32 s83, v49\n\t"
        "s_xor_b32 s82, s82, %[tag]\n\t"
        "s_xor_b32 s83, s83, %[tag]\n\t"
        "s_or_b32 s82, s82, s83\n\t"
        "s_cbranch_scc0 .Lk11%=\n\t"
        "s_sleep 3\n\t"
        "buffer_load_dwordx2 v[48:49], off, %[wsd], s64 sc1\n\t"
        "s_add_u32 s97, s97, 1\n\t"
        "s_cmp_lt_u32 s97, 0x200000\n\t"
        "s_cbranch_scc0 .Lgu%=\n\t"
        "s_waitcnt vmcnt(0)\n\t"
        "s_branch .Lk01%=\n\t"
        ".Lk11%=:\n\t"
        "v_mov_b32_e32 v48, s76\n\t"
        "v_mov_b32_e32 v49, s84\n\t"
        "s_branch .Lloop%=\n\t"
        ".Lmv%=:\n\t"
        ".Lhk0%=:\n\t"
        "s_cbranch_execz .Lhk1%=\n\t"
        "s_mov_b32 s48, 1\n\t"
        "s_cmp_eq_u32 s54, %c[iTOL]\n\t"
        "s_cbranch_scc1 .Lm1%=\n\t"
        "s_max_i32 s80, s85, s56\n\t"
        "s_add_u32 s43, s41, s80\n\t"
        "s_add_u32 s43, s43, %c[iPEN0]\n\t"
        ".Lhk1%=:\n\t"
        "s_cbranch_execz .Lhk2%=\n\t"
        "s_mov_b32 s54, %c[iTOL]\n\t"
        "s_add_u32 s46, s46, %c[iX0C]\n\t"
        "s_mov_b32 s95, 0\n\t"
        "s_add_u32 s51, s51, %c[iX0A]\n\t"
        "v_and_b32_e32 v0, 3, v84\n\t"
        "v_and_b32_e32 v1, 3, v86\n\t"
        ".Lhk2%=:\n\t"
        "s_cbranch_execz .Lhk3%=\n\t"
        "v_lshlrev_b32_e64 v0, v0, 1\n\t"
        "v_lshlrev_b32_e64 v1, v1, 1\n\t"
        "v_or_b32_e32 v0, v0, v1\n\t"
        "s_mov_b32 s80, 0\n\t"
        "v_and_b32_e32 v1, 1, v0\n\t"
        "v_cmp_ne_u32_e32 vcc, 0, v1\n\t"
        ".Lhk3%=:\n\t"
        "s_cbranch_execz .Lhk4%=\n\t"
        "s_nop 1\n\t"
        "s_cmp_lg_u64 vcc, 0\n\t"
        "s_cselect_b32 s81, 2, 0\n\t"
        "s_or_b32 s80, s80, s81\n\t"
        "v_and_b32_e32 v1, 2, v0\n\t"
        "v_cmp_ne_u32_e32 vcc, 0, v1\n\t"
        ".Lhk4%=:\n\t"
        "s_cbranch_execz .Lhk5%=\n\t"
        "s_nop 1\n\t"
        "s_cmp_lg_u64 vcc, 0\n\t"
        "s_cselect_b32 s81, 4, 0\n\t"
        "s_or_b32 s80, s80, s81\n\t"
        "v_and_b32_e32 v1, 4, v0\n\t"
        "v_cmp_ne_u32_e32 vcc, 0, v1\n\t"
        ".Lhk5%=:\n\t"
        "s_cbranch_execz .Lhk6%=\n\t"
        "s_nop 1\n\t"
        "s_cmp_lg_u64 vcc, 0\n\t"
        "s_cselect_b32 s81, 8, 0\n\t"
        "s_or_b32 s80, s80, s81\n\t"
        "v_and_b32_e32 v1, 8, v0\n\t"
        "v_cmp_ne_u32_e32 vcc, 0, v1\n\t"
        ".Lhk6%=:\n\t"
        "s_cbranch_execz .Lhk7%=\n\t"
        "s_nop 1\n\t"
        "s_cmp_lg_u64 vcc, 0\n\t"
        "s_cselect_b32 s81, 16, 0\n\t"
        "s_or_b32 s80, s80, s81\n\t"
        "s_lshr_b32 s80, 0x1adf1210, s80\n\t"
        "s_and_b32 s55, s80, 3\n\t"
        ".Lhk7%=:\n\t"
        "s_cbranch_execz .Lhk8%=\n\t"
        "s_branch .Lal%=\n\t"
        ".Lm1%=:\n\t"
        "s_add_u32 s80, s37, %c[iPEN2]\n\t"
        "s_min_i32 s80, s80, %c[iPENM]\n\t"
        "s_cmp_lg_u32 s37, 0\n\t"
        "s_cselect_b32 s37, s80, %c[iPEN]\n\t"
        "s_add_u32 s80, s56, s37\n\t"
        ".Lhk8%=:\n\t"
        "s_cbranch_execz .Lhk9%=\n\t"
        "s_max_i32 s80, s80, %c[iPMIN]\n\t"
        "s_add_u32 s43, s41, s80\n\t"
        "s_add_u32 s95, s93, %c[LA]\n\t"
        "s_add_u32 s46, s46, %c[iXR]\n\t"
        ".Lal%=:\n\t"
        "s_sub_u32 s80, s55, s43\n\t"
        "s_and_b32 s80, s80, %c[iPHM]\n\t"
        ".Lhk9%=:\n\t"
        "s_cbranch_execz .Lhk10%=\n\t"
        "s_add_u32 s43, s43, s80\n\t"
        "s_branch .Lsd%=\n\t"
        ".Lsf%=:\n\t"
        "s_mov_b32 exec_hi, -1\n\t"
        "s_sleep 2\n\t"
        "buffer_load_dwordx4 v[84:87], v120, %[srd], s66 offen sc1\n\t"
        "s_add_u32 s97, s97, 1\n\t"
        ".Lhk10%=:\n\t"
        "s_cbranch_execz .Lhk11%=\n\t"
        "s_cmp_lt_u32 s97, 0x100000\n\t"
        "s_cbranch_scc0 .Lgu%=\n\t"
        "s_waitcnt vmcnt(0)\n\t"
        "s_branch .Lsr%=\n\t"
        ".Lf1%=:\n\t"
        "v_max_u32_e32 v54, v0, v2\n\t"
        "v_max3_u32 v54, v54, v1, v3\n\t"
        ".Lhk11%=:\n\t"
        "s_cbranch_execz .Lhk12%=\n\t"
        "v_max3_u32 v54, v54, v4, v6\n\t"
        "v_max3_u32 v54, v54, v5, v7\n\t"
        "v_max3_u32 v54, v54, v8, v10\n\t"
        "v_max3_u32 v54, v54, v9, v11\n\t"
        "s_sleep %c[RSLP]\n\t"
        "buffer_load_dwordx4 v[0:3], v120, %[srd], s57 offen sc1\n\t"
        ".Lhk12%=:\n\t"
        "s_cbranch_execz .Lhk13%=\n\t"
        "buffer_load_dwordx4 v[4:7], v120, %[srd], s58 offen sc1\n\t"
        "buffer_load_dwordx4 v[8:11], v120, %[srd], s59 offen sc1\n\t"
        "buffer_load_dwordx4 v[12:15], v120, %[srd], s60 offen sc1\n\t"
        "buffer_store_dword v0, off, %[wsd], s36\n\t"
        "buffer_store_dword v0, off, %[wsd], s36\n\t"
        "buffer_store_dword v0, off, %[wsd], s36\n\t"
        ".Lhk13%=:\n\t"
        "s_cbranch_execz .Lhk14%=\n\t"
        "ds_read_b128 v[92:95], v120 offset:0\n\t"
        "ds_read_b128 v[68:71], v120 offset:4096\n\t"
        "v_max3_u32 v54, v54, v16, v18\n\t"
        "v_max3_u32 v54, v54, v17, v19\n\t"
        "v_max3_u32 v54, v54, v20, v22\n\t"
        "v_max3_u32 v54, v54, v21, v23\n\t"
        ".Lhk14%=:\n\t"
        "s_cbranch_execz .Lhk15%=\n\t"
        "v_max3_u32 v54, v54, v24, v26\n\t"
        "v_max3_u32 v54, v54, v25, v27\n\t"
        "v_max3_u32 v54, v54, v28, v30\n\t"
        "v_max3_u32 v54, v54, v29, v31\n\t"
        "v_max3_u32 v54, v54, v32, v34\n\t"
        "v_max3_u32 v54, v54, v33, v35\n\t"
        ".Lhk15%=:\n\t"
        "s_cbranch_execz .Lhk16%=\n\t"
        "v_max3_u32 v54, v54, v36, v38\n\t"
        "v_max3_u32 v54, v54, v37, v39\n\t"
        "v_max3_u32 v54, v54, v40, v42\n\t"
        "v_max3_u32 v54, v54, v41, v43\n\t"
        "v_max3_u32 v54, v54, v44, v46\n\t"
        "v_max3_u32 v54, v54, v45, v47\n\t"
        ".Lhk16%=:\n\t"
        "s_cbranch_execz .Lhk17%=\n\t"
        "v_max3_u32 v54, v54, v48, v50\n\t"
        "v_max3_u32 v54, v54, v49, v51\n\t"
        "s_waitcnt lgkmcnt(0)\n\t"
        "v_max3_u32 v54, v54, v92, v94\n\t"
        "v_max3_u32 v54, v54, v93, v95\n\t"
        "v_max3_u32 v54, v54, v68, v70\n\t"
        ".Lhk17%=:\n\t"
        "s_cbranch_execz .Lhk18%=\n\t"
        "v_max3_u32 v54, v54, v69, v71\n\t"
        "ds_read_b128 v[92:95], v120 offset:8192\n\t"
        "ds_read_b128 v[68:71], v120 offset:12288\n\t"
        "s_waitcnt lgkmcnt(0)\n\t"
        "v_max3_u32 v54, v54, v92, v94\n\t"
        "v_max3_u32 v54, v54, v93, v95\n\t"
        ".Lhk18%=:\n\t"
        "s_cbranch_execz .Lhk19%=\n\t"
        "v_max3_u32 v54, v54, v68, v70\n\t"
        "v_max3_u32 v54, v54, v69, v71\n\t"
        "v_cmp_eq_u32_e32 vcc, -1, v54\n\t"
        "s_nop 1\n\t"
        "v_mov_b32_e32 v55, v55\n\t"
        "s_nop 0\n\t"
        ".Lhk19%=:\n\t"
        "s_cbranch_execz .Lhk20%=\n\t"
        "s_cbranch_vccz .Lf2%=\n\t"
        "ds_read_b128 v[16:19], v121 offset:0\n\t"
        "ds_read_b128 v[20:23], v121 offset:1024\n\t"
        "ds_read_b128 v[24:27], v121 offset:2048\n\t"
        "ds_read_b128 v[28:31], v121 offset:3072\n\t"
        "ds_read_b128 v[32:35], v121 offset:4096\n\t"
        ".Lhk20%=:\n\t"
        "s_cbranch_execz .Lhk21%=\n\t"
        "ds_read_b128 v[36:39], v121 offset:5120\n\t"
        "ds_read_b128 v[40:43], v121 offset:6144\n\t"
        "ds_read_b128 v[44:47], v121 offset:7168\n\t"
        "ds_read_b128 v[56:59], v121 offset:8192\n\t"
        "ds_read_b128 v[60:63], v121 offset:9216\n\t"
        "ds_read_b128 v[64:67], v121 offset:10240\n\t"
        ".Lhk21%=:\n\t"
        "s_cbranch_execz .Lhk22%=\n\t"
        "ds_read_b128 v[68:71], v121 offset:11264\n\t"
        "ds_read_b128 v[72:75], v121 offset:12288\n\t"
        "ds_read_b128 v[76:79], v121 offset:13312\n\t"
        "ds_read_b128 v[80:83], v121 offset:14336\n\t"
        "ds_read_b128 v[84:87], v121 offset:15360\n\t"
        "s_add_u32 s80, s40, 1\n\t"
        ".Lhk22%=:\n\t"
        "s_cbranch_execz .Lhk23%=\n\t"
        "v_mov_b32_e32 v54, 30720\n\t"
        "v_mov_b32_e32 v55, s80\n\t"
        "ds_write_b32 v54, v55\n\t"
        "s_add_u32 s97, s97, 1\n\t"
        "s_cmp_lt_u32 s97, 0x100000\n\t"
        "s_cbranch_scc0 .Lgu%=\n\t"
        ".Lhk23%=:\n\t"
        "s_cbranch_execz .Lhk24%=\n\t"
        "s_mov_b32 s65, s36\n\t"
        "v_mov_b32_e32 v88, 0\n\t"
        "v_mov_b32_e32 v89, 0\n\t"
        "v_mov_b32_e32 v90, 0\n\t"
        "v_mov_b32_e32 v91, 0\n\t"
        "v_mov_b32_e32 v92, 0\n\t"
        ".Lhk24%=:\n\t"
        "s_cbranch_execz .Lhk25%=\n\t"
        "v_mov_b32_e32 v93, 0\n\t"
        "v_mov_b32_e32 v94, 0\n\t"
        "v_mov_b32_e32 v95, 0\n\t"
        "v_mov_b32_e32 v96, 0\n\t"
        "v_mov_b32_e32 v97, 0\n\t"
        "v_mov_b32_e32 v98, 0\n\t"
        ".Lhk25%=:\n\t"
        "s_cbranch_execz .Lhk26%=\n\t"
        "v_mov_b32_e32 v99, 0\n\t"
        "v_mov_b32_e32 v100, 0\n\t"
        "v_mov_b32_e32 v101, 0\n\t"
        "v_mov_b32_e32 v102, 0\n\t"
        "v_mov_b32_e32 v103, 0\n\t"
        "s_waitcnt lgkmcnt(0)\n\t"
        ".Lhk26%=:\n\t"
        "s_cbranch_execz .Lhk27%=\n\t"
        "s_barrier\n\t"
        "s_branch .Lgo%=\n\t"
        ".Lf2%=:\n\t"
        "s_waitcnt vmcnt(0)\n\t"
        "v_mov_b32_e32 v12, 0x3f803f80\n\t"
        "v_mov_b32_e32 v13, 0x3f803f80\n\t"
        "v_mov_b32_e32 v14, 0x3f803f80\n\t"
        ".Lhk27%=:\n\t"
        "s_cbranch_execz .Lhk28%=\n\t"
        "v_mov_b32_e32 v15, 0x3f803f80\n\t"
        "ds_read_b128 v[0:3], v124 offset:12288\n\t"
        "ds_read_b128 v[4:7], v125 offset:12288\n\t"
        "ds_read_b128 v[8:11], v126 offset:12288\n\t"
        "s_waitcnt lgkmcnt(0)\n\t"
        "s_branch .Lr1%=\n\t"
        ".Lfb%=:\n\t"
        ".Lhk28%=:\n\t"
        "s_cbranch_execz .Lhk29%=\n\t"
        "s_nop 7\n\t"
        "s_mov_b32 s97, 0\n\t"
        "s_add_u32 s80, s40, 1\n\t"
        "v_mov_b32_e32 v54, 30724\n\t"
        "v_mov_b32_e32 v55, s80\n\t"
        "ds_write_b32 v54, v55\n\t"
        ".Lfp%=:\n\t"
        ".Lhk29%=:\n\t"
        "s_cbranch_execz .Lhk30%=\n\t"
        "s_sleep %c[ASLP]\n\t"
        "buffer_load_dword v88, v127, %[srd], s61 offen offset:0 sc1\n\t"
        "buffer_load_dword v89, v127, %[srd], s61 offen offset:256 sc1\n\t"
        "s_add_u32 s97, s97, 1\n\t"
        "s_cmp_lt_u32 s97, 0x100000\n\t"
        "s_cbranch_scc0 .Lgu%=\n\t"
        ".Lhk30%=:\n\t"
        "s_cbranch_execz .Lhk31%=\n\t"
        "s_waitcnt vmcnt(0)\n\t"
        "v_max_u32_e32 v54, v88, v89\n\t"
        "v_cmp_eq_u32_e32 vcc, -1, v54\n\t"
        "s_nop 1\n\t"
        "v_mov_b32_e32 v55, v55\n\t"
        "s_nop 0\n\t"
        ".Lhk31%=:\n\t"
        "s_cbranch_execz .Lhk32%=\n\t"
        "s_cbranch_vccnz .Lfp%=\n\t"
        "v_mfma_f32_16x16x32_bf16 v[40:43], v[112:115], v[36:39], 0\n\t"
        "v_mfma_f32_16x16x32_bf16 v[44:47], v[116:119], v[36:39], 0\n\t"
        "s_nop 1\n\t"
        "v_mov_b32_e32 v55, v55\n\t"
        "s_nop 1\n\t"
        ".Lhk32%=:\n\t"
        "s_cbranch_execz .Lhk33%=\n\t"
        "v_mov_b32_e32 v55, v55\n\t"
        "s_nop 0\n\t"
        "v_pk_mul_f32 v[40:41], v[40:41], v[10:11] op_sel_hi:[1,0]\n\t"
        "v_pk_mul_f32 v[42:43], v[42:43], v[10:11] op_sel_hi:[1,0]\n\t"
        "v_pk_mul_f32 v[44:45], v[44:45], v[10:11] op_sel_hi:[1,0]\n\t"
        "v_pk_mul_f32 v[46:47], v[46:47], v[10:11] op_sel_hi:[1,0]\n\t"
        ".Lhk33%=:\n\t"
        "s_cbranch_execz .Lhk34%=\n\t"
        "v_mfma_f32_16x16x16_bf16 v[40:43], v[88:89], v[90:91], v[40:43]\n\t"
        "s_nop 0\n\t"
        "v_mfma_f32_16x16x16_bf16 v[44:47], v[88:89], v[94:95], v[44:47]\n\t"
        "s_nop 7\n\t"
        "s_branch .Lrb%=\n\t"
        ".Lfm%=:\n\t"
        "v_max3_f32 v3, v0, v64, v65\n\t"
        ".Lhk34%=:\n\t"
        "s_cbranch_execz .Lhk35%=\n\t"
        "v_max3_f32 v1, v0, v64, v65\n\t"
        "s_nop 1\n\t"
        "v_permlane16_swap_b32_e32 v3, v1\n\t"
        "v_max_f32_e32 v2, v3, v1\n\t"
        "v_max_f32_e32 v3, v3, v1\n\t"
        "s_nop 1\n\t"
        ".Lhk35%=:\n\t"
        "s_cbranch_execz .Lhk36%=\n\t"
        "v_permlane32_swap_b32_e32 v2, v3\n\t"
        "v_max_f32_e32 v2, v2, v3\n\t"
        "v_mul_f32_e32 v2, 0xbe0293ee, v2\n\t"
        "v_fmamk_f32 v10, v0, 0x3e0293ee, v2\n\t"
        "v_fmamk_f32 v66, v64, 0x3e0293ee, v2\n\t"
        "v_exp_f32_e32 v10, v10\n\t"
        ".Lhk36%=:\n\t"
        "s_cbranch_execz .Lhk37%=\n\t"
        "v_fmamk_f32 v67, v65, 0x3e0293ee, v2\n\t"
        "v_exp_f32_e32 v66, v66\n\t"
        "v_exp_f32_e32 v67, v67\n\t"
        "v_mul_f32_e32 v96, v96, v10\n\t"
        "v_pk_mul_f32 v[40:41], v[40:41], v[10:11] op_sel_hi:[1,0]\n\t"
        "v_pk_mul_f32 v[42:43], v[42:43], v[10:11] op_sel_hi:[1,0]\n\t"
        ".Lhk37%=:\n\t"
        "s_cbranch_execz .Lhk38%=\n\t"
        "v_pk_mul_f32 v[44:45], v[44:45], v[10:11] op_sel_hi:[1,0]\n\t"
        "v_pk_mul_f32 v[46:47], v[46:47], v[10:11] op_sel_hi:[1,0]\n\t"
        "v_cvt_pk_bf16_f32 v90, v66, v67\n\t"
        "v_cvt_pk_bf16_f32 v95, v66, v67\n\t"
        "s_nop 1\n\t"
        "s_branch .Lrm%=\n\t"
        ".Lta%=:\n\t"
        ".Lhk38%=:\n\t"
        "s_cbranch_execz .Lhk39%=\n\t"
        "s_waitcnt lgkmcnt(0)\n\t"
        "s_branch .Lxa%=\n\t"
        ".Lgu%=:\n\t"
        "s_mov_b64 exec, -1\n\t"
        "v_mov_b32_e32 v54, %c[ferr]\n\t"
        "v_mov_b32_e32 v55, 1\n\t"
        "buffer_store_dword v55, v54, %[wsd], 0 offen sc1\n\t"
        ".Lhk39%=:\n\t"
        "s_cbranch_execz .Lhk40%=\n\t"
        "s_endpgm\n\t"
        ".Lend%=:\n\t"
        "v_min_u32_e32 v50, -2, v50\n\t"
        "v_min_u32_e32 v51, -2, v51\n\t"
        "s_nop 1\n\t"
        "v_permlane16_swap_b32_e32 v50, v51\n\t"
        "buffer_store_dwordx2 v[50:51], v34, %[wsd], s72 offen sc1\n\t"
        ".Lhk40%=:\n\t"
        "s_cbranch_execz .Lhk41%=\n\t"
        "s_endpgm\n\t"
        ".Lhk41%=:\n\t"
        "s_branch .Lhr%="
        : [nA] "+s"(nA), [nS] "+s"(nS), [nT] "+s"(nT), [nW] "+s"(nW), [nC] "+s"(nC), [b0] "+s"(b0), [pob] "+s"(pob)
        : [srd] "s"(rhd), [wsd] "s"(wsd), [w] "s"(w), [tag] "s"(tag), [kx] "s"(kx), [vx] "s"(vx), [kf] "s"(kf), [kvo] "s"(kvo), [cal] "s"(cal), [sq] "s"(sq), [sk] "s"(sk), [m0b] "s"(m0b),
          [pra] "s"(pr_a), [ferr] "i"(F_ERR), [iD1] "i"(D1), [iD2] "i"(D2), [LC] "i"(LC0), [LA] "i"(LA0), [LC24] "i"(LC0 + 24), [LA24] "i"(LA0 + 24), [E1] "i"(2 * (D1 - LC0)), [E2] "i"(2 * (D2 - LA0)),
          [iPEN] "i"(PEN), [iPEN2] "i"(PEN2), [iPENM] "i"(PEN + 2 * PEN2), [iPEN0] "i"(PEN0), [iTOL] "i"(TOL), [iCT0] "i"(CT0), [iX0C] "i"(X0C), [iX0A] "i"(X0A), [iPHM] "i"(PH_M),
          [iFT] "i"(-3), [iFN] "i"(FINE_N), [AUP] "i"(A_UP), [ASLP] "i"(A_SLP), [LDEC] "i"(L_DEC), [UPSH] "i"(UP_SH), [TRUP] "i"(TR_UP), [TRDN] "i"(TR_DN), [HSLP] "i"(H_SLP), [iPMIN] "i"(PMIN), [VSLP] "i"(V_SLP), [RSLP] "i"(R_SLP), [iVD] "i"(V_DEL), [iXR] "i"(XR), [VASL] "i"(VA_SLP), [CSLP] "i"(C_SLP), [DNSH] "i"(DN_SH), [NX0] "i"(NEARX), [NX1] "i"(8192 - NEARX), [iCS1] "i"(CS1), [iCS2] "i"(CS2), [NY0] "i"(NEARY), [NY1] "i"(16384 - NEARY),
          "{v[120:127]}"(pc), "{v[112:113]}"(pd), "{v96}"(dv), "{v[98:105]}"(pa)
        : "memory", "vcc", "scc", "m0", "s36", "s37", "s38", "s39", "s40", "s41", "s42", "s43", "s44", "s45", "s46", "s47", "s48", "s49", "s50", "s51", "s52", "s53", "s54", "s55", "s56", "s57", "s58", "s59", "s60", "s61", "s62", "s63", "s64", "s65", "s66", "s67", "s68", "s69", "s70", "s71", "s72", "s73", "s74", "s75", "s76", "s77", "s78", "s79", "s80", "s81", "s82", "s83", "s84", "s85", "s86", "s87", "s88", "s89", "s90", "s91", "s92", "s93", "s94", "s95", "s96", "s97", "s98", "s99");
    } else {
        const int cb = i32;
        const v4u sv = mk_srd(p.Wv, 1024 * 4096 * 2), so = mk_srd(p.Wo, 1024 * 4096 * 2);
        const unsigned key = KEYM & ((cb & 1) * 2 + G), tu = cb >> 2, m0b = w * 32768, f = (g & 1) | ((c >> 1) << 1);
        const unsigned t0 = m0b + (16 * (g >> 1) + 4 * (g & 1) + (c & 1) + 8 * (c >> 1)) * 128 + (f << 5) + (n16 & 3) * 8, t1 = m0b + (16 * g + 4 * (c >> 1) + (c & 1)) * 64 + ((c >> 1) << 5) + (n16 & 3) * 8;
        v8u pa;
        pa[0] = (64 * w + (l >> 3)) * 8192 + vi * 128 + (((l & 7) ^ ((l >> 5) << 1)) << 4); pa[1] = pa[0] ^ 64u;
        pa[2] = (1024 * w + (l >> 2)) * 2048 + cb * 64 + (((l & 3) ^ ((g & 1) << 1)) << 4);
#pragma unroll
        for (int q = 0; q < 4; ++q) pa[3 + q] = t0 ^ (((w + q) & 3) << 5);
        pa[7] = t1;
        const v2u pb = {t1 ^ 32u, (unsigned)((w * 1024 + cb * 32) * 2048 + l * 1024) + (((3u - (so.x >> 7)) & 7u) << 7)};
        const v16u pc = {(unsigned)tid * 16, (n16 & 2) ? 0xEEEEu : 0x4444u, (unsigned)((vi & 1) * 1024 + w * 256 + n16 * 16 + g * 4), (unsigned)(4096 + tid * 16),
                   w < 3 ? (unsigned)((cb >> 4) * 32768 + ((cb >> 1) & 7) * 1024 + ((cb & 1) * 2 + (l >> 5)) * 256 + G * 128 + (l & 31) * 4) : OOB,
                   tid < 3 ? (unsigned)(l * 1024 + (32 + vi) * 8) : OOB, 0u, (unsigned)(L_V + 8192 + w * 512 + (g & 1) * 256 + (g >> 1) * 64 + n16 * 4),
                   (unsigned)(L_V + (((l >> 2) & 1) + ((l >> 3) & 2)) * 128 + (l >> 5) * 64 + (l & 8) * 4 + (l & 3) * 8), 0u,
                   (unsigned)(L_V + w * 2048 + l * 8), (unsigned)(L_V + ((w + 1) & 3) * 2048 + 512 + l * 8), (unsigned)(L_V + ((w + 2) & 3) * 2048 + 1024 + l * 8), (unsigned)(L_V + ((w + 3) & 3) * 2048 + 1536 + l * 8), 0u, 0u};
        if (tid < 2) ((lu32*)(size_t)(L_V + L_FL))[tid] = ~0u;
    asm volatile(
        "s_mov_b32 s82, 3\n\t"
        ".Ld0%=:\n\t"
        "s_xor_b32 s80, s82, %[key]\n\t"
        "s_mul_i32 s81, s80, 131072\n\t"
        "s_lshl_b32 s80, s80, 11\n\t"
        "s_add_u32 s80, s80, %[m0b]\n\t"
        "s_add_u32 m0, s80, 0\n\t"
        "s_add_u32 s81, s81, 0\n\t"
        "buffer_load_dwordx4 v96, %[sv], s81 offen lds\n\t"
        "s_add_u32 m0, m0, 0x400\n\t"
        "s_add_u32 s81, s81, 65536\n\t"
        "buffer_load_dwordx4 v97, %[sv], s81 offen lds\n\t"
        "s_sub_u32 s82, s82, 1\n\t"
        "s_cbranch_scc0 .Ld0%=\n\t"
        "s_mov_b32 s82, 3\n\t"
        ".Ld1%=:\n\t"
        "s_xor_b32 s80, s82, %[key]\n\t"
        "s_mul_i32 s81, s80, 131072\n\t"
        "s_lshl_b32 s80, s80, 11\n\t"
        "s_add_u32 s80, s80, %[m0b]\n\t"
        "s_add_u32 m0, s80, 8192\n\t"
        "s_add_u32 s81, s81, 2097152\n\t"
        "buffer_load_dwordx4 v96, %[sv], s81 offen lds\n\t"
        "s_add_u32 m0, m0, 0x400\n\t"
        "s_add_u32 s81, s81, 65536\n\t"
        "buffer_load_dwordx4 v97, %[sv], s81 offen lds\n\t"
        "s_sub_u32 s82, s82, 1\n\t"
        "s_cbranch_scc0 .Ld1%=\n\t"
        "s_cmp_eq_u32 %[tu], 0\n\t"
        "s_cselect_b32 s81, 0, 0x7fff0000\n\t"
        "buffer_load_dword v110, v105, %[so], s81 offen\n\t"
        "s_mov_b32 s82, 3\n\t"
        ".Ld2%=:\n\t"
        "s_xor_b32 s80, s82, %[key]\n\t"
        "s_mul_i32 s81, s80, 131072\n\t"
        "s_lshl_b32 s80, s80, 11\n\t"
        "s_add_u32 s80, s80, %[m0b]\n\t"
        "s_add_u32 m0, s80, 16384\n\t"
        "s_add_u32 s81, s81, 4194304\n\t"
        "buffer_load_dwordx4 v96, %[sv], s81 offen lds\n\t"
        "s_add_u32 m0, m0, 0x400\n\t"
        "s_add_u32 s81, s81, 65536\n\t"
        "buffer_load_dwordx4 v97, %[sv], s81 offen lds\n\t"
        "s_sub_u32 s82, s82, 1\n\t"
        "s_cbranch_scc0 .Ld2%=\n\t"
        "s_cmp_eq_u32 %[tu], 1\n\t"
        "s_cselect_b32 s81, 0, 0x7fff0000\n\t"
        "buffer_load_dword v110, v105, %[so], s81 offen\n\t"
        "s_mov_b32 s82, 3\n\t"
        ".Ld3%=:\n\t"
        "s_xor_b32 s80, s82, %[key]\n\t"
        "s_mul_i32 s81, s80, 131072\n\t"
        "s_lshl_b32 s80, s80, 11\n\t"
        "s_add_u32 s80, s80, %[m0b]\n\t"
        "s_add_u32 m0, s80, 24576\n\t"
        "s_add_u32 s81, s81, 6291456\n\t"
        "buffer_load_dwordx4 v96, %[sv], s81 offen lds\n\t"
        "s_add_u32 m0, m0, 0x400\n\t"
        "s_add_u32 s81, s81, 65536\n\t"
        "buffer_load_dwordx4 v97, %[sv], s81 offen lds\n\t"
        "s_sub_u32 s82, s82, 1\n\t"
        "s_cbranch_scc0 .Ld3%=\n\t"
        "s_waitcnt vmcnt(26)\n\t"
        "ds_read_b64_tr_b16 v[128:129], v99 offset:0\n\t"
        "ds_read_b64_tr_b16 v[130:131], v99 offset:256\n\t"
        "ds_read_b64_tr_b16 v[132:133], v99 offset:4096\n\t"
        "ds_read_b64_tr_b16 v[134:135], v99 offset:4352\n\t"
        "ds_read_b64_tr_b16 v[136:137], v100 offset:0\n\t"
        "ds_read_b64_tr_b16 v[138:139], v100 offset:256\n\t"
        "ds_read_b64_tr_b16 v[140:141], v100 offset:4096\n\t"
        "ds_read_b64_tr_b16 v[142:143], v100 offset:4352\n\t"
        "ds_read_b64_tr_b16 v[144:145], v101 offset:0\n\t"
        "ds_read_b64_tr_b16 v[146:147], v101 offset:256\n\t"
        "ds_read_b64_tr_b16 v[148:149], v101 offset:4096\n\t"
        "ds_read_b64_tr_b16 v[150:151], v101 offset:4352\n\t"
        "ds_read_b64_tr_b16 v[152:153], v102 offset:0\n\t"
        "ds_read_b64_tr_b16 v[154:155], v102 offset:256\n\t"
        "ds_read_b64_tr_b16 v[156:157], v102 offset:4096\n\t"
        "ds_read_b64_tr_b16 v[158:159], v102 offset:4352\n\t"
        "s_waitcnt lgkmcnt(0)\n\t"
        "s_cmp_eq_u32 %[tu], 2\n\t"
        "s_cselect_b32 s81, 0, 0x7fff0000\n\t"
        "buffer_load_dword v110, v105, %[so], s81 offen\n\t"
        "s_mov_b32 s82, 3\n\t"
        ".Ld4%=:\n\t"
        "s_xor_b32 s80, s82, %[key]\n\t"
        "s_mul_i32 s81, s80, 65536\n\t"
        "s_lshl_b32 s80, s80, 11\n\t"
        "s_add_u32 s80, s80, %[m0b]\n\t"
        "s_add_u32 m0, s80, 0\n\t"
        "s_add_u32 s81, s81, 0\n\t"
        "buffer_load_dwordx4 v98, %[so], s81 offen lds\n\t"
        "s_add_u32 m0, m0, 0x400\n\t"
        "s_add_u32 s81, s81, 32768\n\t"
        "buffer_load_dwordx4 v98, %[so], s81 offen lds\n\t"
        "s_sub_u32 s82, s82, 1\n\t"
        "s_cbranch_scc0 .Ld4%=\n\t"
        "s_waitcnt vmcnt(27)\n\t"
        "ds_read_b64_tr_b16 v[160:161], v99 offset:8192\n\t"
        "ds_read_b64_tr_b16 v[162:163], v99 offset:8448\n\t"
        "ds_read_b64_tr_b16 v[164:165], v99 offset:12288\n\t"
        "ds_read_b64_tr_b16 v[166:167], v99 offset:12544\n\t"
        "ds_read_b64_tr_b16 v[168:169], v100 offset:8192\n\t"
        "ds_read_b64_tr_b16 v[170:171], v100 offset:8448\n\t"
        "ds_read_b64_tr_b16 v[172:173], v100 offset:12288\n\t"
        "ds_read_b64_tr_b16 v[174:175], v100 offset:12544\n\t"
        "ds_read_b64_tr_b16 v[176:177], v101 offset:8192\n\t"
        "ds_read_b64_tr_b16 v[178:179], v101 offset:8448\n\t"
        "ds_read_b64_tr_b16 v[180:181], v101 offset:12288\n\t"
        "ds_read_b64_tr_b16 v[182:183], v101 offset:12544\n\t"
        "ds_read_b64_tr_b16 v[184:185], v102 offset:8192\n\t"
        "ds_read_b64_tr_b16 v[186:187], v102 offset:8448\n\t"
        "ds_read_b64_tr_b16 v[188:189], v102 offset:12288\n\t"
        "ds_read_b64_tr_b16 v[190:191], v102 offset:12544\n\t"
        "s_waitcnt lgkmcnt(0)\n\t"
        "s_cmp_eq_u32 %[tu], 3\n\t"
        "s_cselect_b32 s81, 0, 0x7fff0000\n\t"
        "buffer_load_dword v110, v105, %[so], s81 offen\n\t"
        "s_mov_b32 s82, 3\n\t"
        ".Ld5%=:\n\t"
        "s_xor_b32 s80, s82, %[key]\n\t"
        "s_mul_i32 s81, s80, 65536\n\t"
        "s_lshl_b32 s80, s80, 11\n\t"
        "s_add_u32 s80, s80, %[m0b]\n\t"
        "s_add_u32 m0, s80, 8192\n\t"
        "s_add_u32 s81, s81, 262144\n\t"
        "buffer_load_dwordx4 v98, %[so], s81 offen lds\n\t"
        "s_add_u32 m0, m0, 0x400\n\t"
        "s_add_u32 s81, s81, 32768\n\t"
        "buffer_load_dwordx4 v98, %[so], s81 offen lds\n\t"
        "s_sub_u32 s82, s82, 1\n\t"
        "s_cbranch_scc0 .Ld5%=\n\t"
        "s_waitcnt vmcnt(27)\n\t"
        "ds_read_b64_tr_b16 v[192:193], v99 offset:16384\n\t"
        "ds_read_b64_tr_b16 v[194:195], v99 offset:16640\n\t"
        "ds_read_b64_tr_b16 v[196:197], v99 offset:20480\n\t"
        "ds_read_b64_tr_b16 v[198:199], v99 offset:20736\n\t"
        "ds_read_b64_tr_b16 v[200:201], v100 offset:16384\n\t"
        "ds_read_b64_tr_b16 v[202:203], v100 offset:16640\n\t"
        "ds_read_b64_tr_b16 v[204:205], v100 offset:20480\n\t"
        "ds_read_b64_tr_b16 v[206:207], v100 offset:20736\n\t"
        "ds_read_b64_tr_b16 v[208:209], v101 offset:16384\n\t"
        "ds_read_b64_tr_b16 v[210:211], v101 offset:16640\n\t"
        "ds_read_b64_tr_b16 v[212:213], v101 offset:20480\n\t"
        "ds_read_b64_tr_b16 v[214:215], v101 offset:20736\n\t"
        "ds_read_b64_tr_b16 v[216:217], v102 offset:16384\n\t"
        "ds_read_b64_tr_b16 v[218:219], v102 offset:16640\n\t"
        "ds_read_b64_tr_b16 v[220:221], v102 offset:20480\n\t"
        "ds_read_b64_tr_b16 v[222:223], v102 offset:20736\n\t"
        "s_waitcnt lgkmcnt(0)\n\t"
        "s_cmp_eq_u32 %[tu], 4\n\t"
        "s_cselect_b32 s81, 0, 0x7fff0000\n\t"
        "buffer_load_dword v110, v105, %[so], s81 offen\n\t"
        "s_mov_b32 s82, 3\n\t"
        ".Ld6%=:\n\t"
        "s_xor_b32 s80, s82, %[key]\n\t"
        "s_mul_i32 s81, s80, 65536\n\t"
        "s_lshl_b32 s80, s80, 11\n\t"
        "s_add_u32 s80, s80, %[m0b]\n\t"
        "s_add_u32 m0, s80, 16384\n\t"
        "s_add_u32 s81, s81, 524288\n\t"
        "buffer_load_dwordx4 v98, %[so], s81 offen lds\n\t"
        "s_add_u32 m0, m0, 0x400\n\t"
        "s_add_u32 s81, s81, 32768\n\t"
        "buffer_load_dwordx4 v98, %[so], s81 offen lds\n\t"
        "s_sub_u32 s82, s82, 1\n\t"
        "s_cbranch_scc0 .Ld6%=\n\t"
        "s_waitcnt vmcnt(27)\n\t"
        "ds_read_b64_tr_b16 v[224:225], v99 offset:24576\n\t"
        "ds_read_b64_tr_b16 v[226:227], v99 offset:24832\n\t"
        "ds_read_b64_tr_b16 v[228:229], v99 offset:28672\n\t"
        "ds_read_b64_tr_b16 v[230:231], v99 offset:28928\n\t"
        "ds_read_b64_tr_b16 v[232:233], v100 offset:24576\n\t"
        "ds_read_b64_tr_b16 v[234:235], v100 offset:24832\n\t"
        "ds_read_b64_tr_b16 v[236:237], v100 offset:28672\n\t"
        "ds_read_b64_tr_b16 v[238:239], v100 offset:28928\n\t"
        "ds_read_b64_tr_b16 v[240:241], v101 offset:24576\n\t"
        "ds_read_b64_tr_b16 v[242:243], v101 offset:24832\n\t"
        "ds_read_b64_tr_b16 v[244:245], v101 offset:28672\n\t"
        "ds_read_b64_tr_b16 v[246:247], v101 offset:28928\n\t"
        "ds_read_b64_tr_b16 v[248:249], v102 offset:24576\n\t"
        "ds_read_b64_tr_b16 v[250:251], v102 offset:24832\n\t"
        "ds_read_b64_tr_b16 v[252:253], v102 offset:28672\n\t"
        "ds_read_b64_tr_b16 v[254:255], v102 offset:28928\n\t"
        "s_waitcnt lgkmcnt(0)\n\t"
        "s_cmp_eq_u32 %[tu], 5\n\t"
        "s_cselect_b32 s81, 0, 0x7fff0000\n\t"
        "buffer_load_dword v110, v105, %[so], s81 offen\n\t"
        "s_mov_b32 s82, 3\n\t"
        ".Ld7%=:\n\t"
        "s_xor_b32 s80, s82, %[key]\n\t"
        "s_mul_i32 s81, s80, 65536\n\t"
        "s_lshl_b32 s80, s80, 11\n\t"
        "s_add_u32 s80, s80, %[m0b]\n\t"
        "s_add_u32 m0, s80, 24576\n\t"
        "s_add_u32 s81, s81, 786432\n\t"
        "buffer_load_dwordx4 v98, %[so], s81 offen lds\n\t"
        "s_add_u32 m0, m0, 0x400\n\t"
        "s_add_u32 s81, s81, 32768\n\t"
        "buffer_load_dwordx4 v98, %[so], s81 offen lds\n\t"
        "s_sub_u32 s82, s82, 1\n\t"
        "s_cbranch_scc0 .Ld7%=\n\t"
        "s_mov_b64 exec, 0\n\t"
        "s_branch .Lhk0%=\n\t"
        ".Lhr%=:\n\t"
        "s_mov_b64 exec, -1\n\t"
        "s_waitcnt vmcnt(27)\n\t"
        "ds_read_b64_tr_b16 a[0:1], v103 offset:0\n\t"
        "ds_read_b64_tr_b16 a[2:3], v103 offset:128\n\t"
        "ds_read_b64_tr_b16 a[4:5], v103 offset:4096\n\t"
        "ds_read_b64_tr_b16 a[6:7], v103 offset:4224\n\t"
        "ds_read_b64_tr_b16 a[8:9], v104 offset:0\n\t"
        "ds_read_b64_tr_b16 a[10:11], v104 offset:128\n\t"
        "ds_read_b64_tr_b16 a[12:13], v104 offset:4096\n\t"
        "ds_read_b64_tr_b16 a[14:15], v104 offset:4224\n\t"
        "ds_read_b64_tr_b16 a[16:17], v103 offset:512\n\t"
        "ds_read_b64_tr_b16 a[18:19], v103 offset:640\n\t"
        "ds_read_b64_tr_b16 a[20:21], v103 offset:4608\n\t"
        "ds_read_b64_tr_b16 a[22:23], v103 offset:4736\n\t"
        "ds_read_b64_tr_b16 a[24:25], v104 offset:512\n\t"
        "ds_read_b64_tr_b16 a[26:27], v104 offset:640\n\t"
        "ds_read_b64_tr_b16 a[28:29], v104 offset:4608\n\t"
        "ds_read_b64_tr_b16 a[30:31], v104 offset:4736\n\t"
        "s_waitcnt lgkmcnt(0)\n\t"
        "s_cmp_eq_u32 %[tu], 6\n\t"
        "s_cselect_b32 s81, 0, 0x7fff0000\n\t"
        "buffer_load_dword v110, v105, %[so], s81 offen\n\t"
        "s_mov_b32 s82, 3\n\t"
        ".Ld8%=:\n\t"
        "s_xor_b32 s80, s82, %[key]\n\t"
        "s_mul_i32 s81, s80, 65536\n\t"
        "s_lshl_b32 s80, s80, 11\n\t"
        "s_add_u32 s80, s80, %[m0b]\n\t"
        "s_add_u32 m0, s80, 0\n\t"
        "s_add_u32 s81, s81, 1048576\n\t"
        "buffer_load_dwordx4 v98, %[so], s81 offen lds\n\t"
        "s_add_u32 m0, m0, 0x400\n\t"
        "s_add_u32 s81, s81, 32768\n\t"
        "buffer_load_dwordx4 v98, %[so], s81 offen lds\n\t"
        "s_sub_u32 s82, s82, 1\n\t"
        "s_cbranch_scc0 .Ld8%=\n\t"
        "s_waitcnt vmcnt(27)\n\t"
        "ds_read_b64_tr_b16 a[32:33], v103 offset:8192\n\t"
        "ds_read_b64_tr_b16 a[34:35], v103 offset:8320\n\t"
        "ds_read_b64_tr_b16 a[36:37], v103 offset:12288\n\t"
        "ds_read_b64_tr_b16 a[38:39], v103 offset:12416\n\t"
        "ds_read_b64_tr_b16 a[40:41], v104 offset:8192\n\t"
        "ds_read_b64_tr_b16 a[42:43], v104 offset:8320\n\t"
        "ds_read_b64_tr_b16 a[44:45], v104 offset:12288\n\t"
        "ds_read_b64_tr_b16 a[46:47], v104 offset:12416\n\t"
        "ds_read_b64_tr_b16 a[48:49], v103 offset:8704\n\t"
        "ds_read_b64_tr_b16 a[50:51], v103 offset:8832\n\t"
        "ds_read_b64_tr_b16 a[52:53], v103 offset:12800\n\t"
        "ds_read_b64_tr_b16 a[54:55], v103 offset:12928\n\t"
        "ds_read_b64_tr_b16 a[56:57], v104 offset:8704\n\t"
        "ds_read_b64_tr_b16 a[58:59], v104 offset:8832\n\t"
        "ds_read_b64_tr_b16 a[60:61], v104 offset:12800\n\t"
        "ds_read_b64_tr_b16 a[62:63], v104 offset:12928\n\t"
        "s_waitcnt lgkmcnt(0)\n\t"
        "s_cmp_eq_u32 %[tu], 7\n\t"
        "s_cselect_b32 s81, 0, 0x7fff0000\n\t"
        "buffer_load_dword v110, v105, %[so], s81 offen\n\t"
        "s_mov_b32 s82, 3\n\t"
        ".Ld9%=:\n\t"
        "s_xor_b32 s80, s82, %[key]\n\t"
        "s_mul_i32 s81, s80, 65536\n\t"
        "s_lshl_b32 s80, s80, 11\n\t"
        "s_add_u32 s80, s80, %[m0b]\n\t"
        "s_add_u32 m0, s80, 8192\n\t"
        "s_add_u32 s81, s81, 1310720\n\t"
        "buffer_load_dwordx4 v98, %[so], s81 offen lds\n\t"
        "s_add_u32 m0, m0, 0x400\n\t"
        "s_add_u32 s81, s81, 32768\n\t"
        "buffer_load_dwordx4 v98, %[so], s81 offen lds\n\t"
        "s_sub_u32 s82, s82, 1\n\t"
        "s_cbranch_scc0 .Ld9%=\n\t"
        "s_waitcnt vmcnt(27)\n\t"
        "ds_read_b64_tr_b16 a[64:65], v103 offset:16384\n\t"
        "ds_read_b64_tr_b16 a[66:67], v103 offset:16512\n\t"
        "ds_read_b64_tr_b16 a[68:69], v103 offset:20480\n\t"
        "ds_read_b64_tr_b16 a[70:71], v103 offset:20608\n\t"
        "ds_read_b64_tr_b16 a[72:73], v104 offset:16384\n\t"
        "ds_read_b64_tr_b16 a[74:75], v104 offset:16512\n\t"
        "ds_read_b64_tr_b16 a[76:77], v104 offset:20480\n\t"
        "ds_read_b64_tr_b16 a[78:79], v104 offset:20608\n\t"
        "ds_read_b64_tr_b16 a[80:81], v103 offset:16896\n\t"
        "ds_read_b64_tr_b16 a[82:83], v103 offset:17024\n\t"
        "ds_read_b64_tr_b16 a[84:85], v103 offset:20992\n\t"
        "ds_read_b64_tr_b16 a[86:87], v103 offset:21120\n\t"
        "ds_read_b64_tr_b16 a[88:89], v104 offset:16896\n\t"
        "ds_read_b64_tr_b16 a[90:91], v104 offset:17024\n\t"
        "ds_read_b64_tr_b16 a[92:93], v104 offset:20992\n\t"
        "ds_read_b64_tr_b16 a[94:95], v104 offset:21120\n\t"
        "buffer_load_dwordx4 v[0:3], v112, %[wsd], %[cal] offen sc1\n\t"
        "s_waitcnt lgkmcnt(0)\n\t"
        "s_mov_b32 s82, 3\n\t"
        ".Ld10%=:\n\t"
        "s_xor_b32 s80, s82, %[key]\n\t"
        "s_mul_i32 s81, s80, 65536\n\t"
        "s_lshl_b32 s80, s80, 11\n\t"
        "s_add_u32 s80, s80, %[m0b]\n\t"
        "s_add_u32 m0, s80, 16384\n\t"
        "s_add_u32 s81, s81, 1572864\n\t"
        "buffer_load_dwordx4 v98, %[so], s81 offen lds\n\t"
        "s_add_u32 m0, m0, 0x400\n\t"
        "s_add_u32 s81, s81, 32768\n\t"
        "buffer_load_dwordx4 v98, %[so], s81 offen lds\n\t"
        "s_sub_u32 s82, s82, 1\n\t"
        "s_cbranch_scc0 .Ld10%=\n\t"
        "s_waitcnt vmcnt(27)\n\t"
        "ds_read_b64_tr_b16 a[96:97], v103 offset:24576\n\t"
        "ds_read_b64_tr_b16 a[98:99], v103 offset:24704\n\t"
        "ds_read_b64_tr_b16 a[100:101], v103 offset:28672\n\t"
        "ds_read_b64_tr_b16 a[102:103], v103 offset:28800\n\t"
        "ds_read_b64_tr_b16 a[104:105], v104 offset:24576\n\t"
        "ds_read_b64_tr_b16 a[106:107], v104 offset:24704\n\t"
        "ds_read_b64_tr_b16 a[108:109], v104 offset:28672\n\t"
        "ds_read_b64_tr_b16 a[110:111], v104 offset:28800\n\t"
        "ds_read_b64_tr_b16 a[112:113], v103 offset:25088\n\t"
        "ds_read_b64_tr_b16 a[114:115], v103 offset:25216\n\t"
        "ds_read_b64_tr_b16 a[116:117], v103 offset:29184\n\t"
        "ds_read_b64_tr_b16 a[118:119], v103 offset:29312\n\t"
        "ds_read_b64_tr_b16 a[120:121], v104 offset:25088\n\t"
        "ds_read_b64_tr_b16 a[122:123], v104 offset:25216\n\t"
        "ds_read_b64_tr_b16 a[124:125], v104 offset:29184\n\t"
        "ds_read_b64_tr_b16 a[126:127], v104 offset:29312\n\t"
        "s_waitcnt lgkmcnt(0)\n\t"
        "s_mov_b32 s82, 3\n\t"
        ".Ld11%=:\n\t"
        "s_xor_b32 s80, s82, %[key]\n\t"
        "s_mul_i32 s81, s80, 65536\n\t"
        "s_lshl_b32 s80, s80, 11\n\t"
        "s_add_u32 s80, s80, %[m0b]\n\t"
        "s_add_u32 m0, s80, 24576\n\t"
        "s_add_u32 s81, s81, 1835008\n\t"
        "buffer_load_dwordx4 v98, %[so], s81 offen lds\n\t"
        "s_add_u32 m0, m0, 0x400\n\t"
        "s_add_u32 s81, s81, 32768\n\t"
        "buffer_load_dwordx4 v98, %[so], s81 offen lds\n\t"
        "s_sub_u32 s82, s82, 1\n\t"
        "s_cbranch_scc0 .Ld11%=\n\t"
        "s_waitcnt vmcnt(26)\n\t"
        "ds_read_b64_tr_b16 a[128:129], v103 offset:0\n\t"
        "ds_read_b64_tr_b16 a[130:131], v103 offset:128\n\t"
        "ds_read_b64_tr_b16 a[132:133], v103 offset:4096\n\t"
        "ds_read_b64_tr_b16 a[134:135], v103 offset:4224\n\t"
        "ds_read_b64_tr_b16 a[136:137], v104 offset:0\n\t"
        "ds_read_b64_tr_b16 a[138:139], v104 offset:128\n\t"
        "ds_read_b64_tr_b16 a[140:141], v104 offset:4096\n\t"
        "ds_read_b64_tr_b16 a[142:143], v104 offset:4224\n\t"
        "ds_read_b64_tr_b16 a[144:145], v103 offset:512\n\t"
        "ds_read_b64_tr_b16 a[146:147], v103 offset:640\n\t"
        "ds_read_b64_tr_b16 a[148:149], v103 offset:4608\n\t"
        "ds_read_b64_tr_b16 a[150:151], v103 offset:4736\n\t"
        "ds_read_b64_tr_b16 a[152:153], v104 offset:512\n\t"
        "ds_read_b64_tr_b16 a[154:155], v104 offset:640\n\t"
        "ds_read_b64_tr_b16 a[156:157], v104 offset:4608\n\t"
        "ds_read_b64_tr_b16 a[158:159], v104 offset:4736\n\t"
        "s_waitcnt lgkmcnt(0)\n\t"
        "s_waitcnt vmcnt(17)\n\t"
        "ds_read_b64_tr_b16 a[160:161], v103 offset:8192\n\t"
        "ds_read_b64_tr_b16 a[162:163], v103 offset:8320\n\t"
        "ds_read_b64_tr_b16 a[164:165], v103 offset:12288\n\t"
        "ds_read_b64_tr_b16 a[166:167], v103 offset:12416\n\t"
        "ds_read_b64_tr_b16 a[168:169], v104 offset:8192\n\t"
        "ds_read_b64_tr_b16 a[170:171], v104 offset:8320\n\t"
        "ds_read_b64_tr_b16 a[172:173], v104 offset:12288\n\t"
        "ds_read_b64_tr_b16 a[174:175], v104 offset:12416\n\t"
        "ds_read_b64_tr_b16 a[176:177], v103 offset:8704\n\t"
        "ds_read_b64_tr_b16 a[178:179], v103 offset:8832\n\t"
        "ds_read_b64_tr_b16 a[180:181], v103 offset:12800\n\t"
        "ds_read_b64_tr_b16 a[182:183], v103 offset:12928\n\t"
        "ds_read_b64_tr_b16 a[184:185], v104 offset:8704\n\t"
        "ds_read_b64_tr_b16 a[186:187], v104 offset:8832\n\t"
        "ds_read_b64_tr_b16 a[188:189], v104 offset:12800\n\t"
        "ds_read_b64_tr_b16 a[190:191], v104 offset:12928\n\t"
        "s_waitcnt lgkmcnt(0)\n\t"
        "s_waitcnt vmcnt(8)\n\t"
        "ds_read_b64_tr_b16 a[192:193], v103 offset:16384\n\t"
        "ds_read_b64_tr_b16 a[194:195], v103 offset:16512\n\t"
        "ds_read_b64_tr_b16 a[196:197], v103 offset:20480\n\t"
        "ds_read_b64_tr_b16 a[198:199], v103 offset:20608\n\t"
        "ds_read_b64_tr_b16 a[200:201], v104 offset:16384\n\t"
        "ds_read_b64_tr_b16 a[202:203], v104 offset:16512\n\t"
        "ds_read_b64_tr_b16 a[204:205], v104 offset:20480\n\t"
        "ds_read_b64_tr_b16 a[206:207], v104 offset:20608\n\t"
        "ds_read_b64_tr_b16 a[208:209], v103 offset:16896\n\t"
        "ds_read_b64_tr_b16 a[210:211], v103 offset:17024\n\t"
        "ds_read_b64_tr_b16 a[212:213], v103 offset:20992\n\t"
        "ds_read_b64_tr_b16 a[214:215], v103 offset:21120\n\t"
        "ds_read_b64_tr_b16 a[216:217], v104 offset:16896\n\t"
        "ds_read_b64_tr_b16 a[218:219], v104 offset:17024\n\t"
        "ds_read_b64_tr_b16 a[220:221], v104 offset:20992\n\t"
        "ds_read_b64_tr_b16 a[222:223], v104 offset:21120\n\t"
        "s_mov_b32 s97, 0\n\t"
        ".Lg0%=:\n\t"
        "v_cmp_ne_u32_e32 vcc, %[tag], v0\n\t"
        "v_cmp_ne_u32_e64 s[82:83], %[tag], v2\n\t"
        "s_nop 1\n\t"
        "s_or_b64 vcc, vcc, s[82:83]\n\t"
        "s_cbranch_scc0 .Lg1%=\n\t"
        "s_sleep 4\n\t"
        "buffer_load_dwordx4 v[0:3], v112, %[wsd], %[cal] offen sc1\n\t"
        "s_add_u32 s97, s97, 1\n\t"
        "s_cmp_lt_u32 s97, 0x100000\n\t"
        "s_cbranch_scc0 .Lgu%=\n\t"
        "s_waitcnt vmcnt(0)\n\t"
        "s_branch .Lg0%=\n\t"
        ".Lg1%=:\n\t"
        "s_mov_b32 s84, 0x55555555\n\t"
        "s_mov_b32 s85, s84\n\t"
        "v_and_b32_e32 v0, 1, v1\n\t"
        "v_and_b32_e32 v2, 1, v3\n\t"
        "v_cmp_ne_u32_e32 vcc, 0, v0\n\t"
        "v_cmp_ne_u32_e64 s[82:83], 0, v2\n\t"
        "s_nop 0\n\t"
        "s_and_b64 s[82:83], s[82:83], s[84:85]\n\t"
        "s_bcnt1_i32_b64 s87, s[82:83]\n\t"
        "s_and_b64 s[82:83], vcc, s[84:85]\n\t"
        "s_bcnt1_i32_b64 s86, s[82:83]\n\t"
        "s_andn2_b64 s[82:83], vcc, s[84:85]\n\t"
        "s_bcnt1_i32_b64 s88, s[82:83]\n\t"
        "v_and_b32_e32 v0, 2, v1\n\t"
        "v_and_b32_e32 v2, 2, v3\n\t"
        "v_cmp_ne_u32_e32 vcc, 0, v0\n\t"
        "v_cmp_ne_u32_e64 s[82:83], 0, v2\n\t"
        "s_nop 0\n\t"
        "s_and_b64 s[82:83], s[82:83], s[84:85]\n\t"
        "s_bcnt1_i32_b64 s90, s[82:83]\n\t"
        "s_and_b64 s[82:83], vcc, s[84:85]\n\t"
        "s_bcnt1_i32_b64 s89, s[82:83]\n\t"
        "s_andn2_b64 s[82:83], vcc, s[84:85]\n\t"
        "s_bcnt1_i32_b64 s91, s[82:83]\n\t"
        "s_add_u32 s80, s86, s87\n\t"
        "s_add_u32 s80, s80, s88\n\t"
        "s_cmp_ge_u32 s80, 48\n\t"
        "s_cselect_b32 s80, 8, 24\n\t"
        "s_cmp_gt_u32 s86, s80\n\t"
        "s_cselect_b32 s86, %c[NX1], %c[NX0]\n\t"
        "s_cmp_gt_u32 s89, 16\n\t"
        "s_cselect_b32 s81, %c[NY1], %c[NY0]\n\t"
        "s_or_b32 s86, s86, s81\n\t"
        "s_cmp_gt_u32 s87, s80\n\t"
        "s_cselect_b32 s87, %c[NX1], %c[NX0]\n\t"
        "s_cmp_gt_u32 s90, 16\n\t"
        "s_cselect_b32 s81, %c[NY1], %c[NY0]\n\t"
        "s_or_b32 s87, s87, s81\n\t"
        "s_cmp_gt_u32 s88, s80\n\t"
        "s_cselect_b32 s88, %c[NX1], %c[NX0]\n\t"
        "s_cmp_gt_u32 s91, 16\n\t"
        "s_cselect_b32 s81, %c[NY1], %c[NY0]\n\t"
        "s_or_b32 s88, s88, s81\n\t"
        "s_xor_b32 %[nA], %[nA], s86\n\t"
        "s_xor_b32 %[nW], %[nW], s86\n\t"
        "s_xor_b32 %[nC], %[nC], s87\n\t"
        "s_xor_b32 %[nS], %[nS], s88\n\t"
        "s_xor_b32 %[nT], %[nT], s88\n\t"
        "s_xor_b32 %[b0], %[b0], s88\n\t"
        "s_cmp_eq_u32 %[w], 1\n\t"
        "s_cselect_b32 s80, s87, s86\n\t"
        "s_cmp_eq_u32 %[w], 2\n\t"
        "s_cselect_b32 s80, s88, s80\n\t"
        "s_xor_b32 %[pob], %[pob], s80\n\t"
        "s_lshl_b32 s80, %[w], 10\n\t"
        "s_sub_u32 %[nS], %[nS], s80\n\t"
        "s_sub_u32 %[pob], %[pob], s80\n\t"
        "s_mov_b32 s36, 0xf0000000\n\t"
        "s_mov_b32 s38, 0xff00ff00\n\t"
        "s_mov_b32 s39, 0xff00ff00\n\t"
        "s_mov_b32 s40, 0\n\t"
        "s_mov_b32 s37, 0\n\t"
        "s_brev_b32 s54, 1\n\t"
        "s_mov_b32 s46, %c[iD1]\n\t"
        "s_mov_b32 s45, %c[iD1]\n\t"
        "s_mov_b32 s47, %c[iD2]\n\t"
        "s_mov_b32 s48, 1\n\t"
        "s_mov_b32 s50, %c[LC]\n\t"
        "s_mov_b32 s51, %c[LA]\n\t"
        "s_mov_b32 s52, %c[E1]\n\t"
        "s_mov_b32 s53, %c[E2]\n\t"
        "s_mov_b32 s43, 0\n\t"
        "s_mov_b32 s61, %[b0]\n\t"
        "s_add_u32 s62, %[b0], 0x40000\n\t"
        "s_mov_b32 s75, 162816\n\t"
        "s_waitcnt lgkmcnt(0)\n\t"
        "s_waitcnt vmcnt(0)\n\t"
        "ds_read_b64_tr_b16 a[224:225], v103 offset:24576\n\t"
        "ds_read_b64_tr_b16 a[226:227], v103 offset:24704\n\t"
        "ds_read_b64_tr_b16 a[228:229], v103 offset:28672\n\t"
        "ds_read_b64_tr_b16 a[230:231], v103 offset:28800\n\t"
        "ds_read_b64_tr_b16 a[232:233], v104 offset:24576\n\t"
        "ds_read_b64_tr_b16 a[234:235], v104 offset:24704\n\t"
        "ds_read_b64_tr_b16 a[236:237], v104 offset:28672\n\t"
        "ds_read_b64_tr_b16 a[238:239], v104 offset:28800\n\t"
        "ds_read_b64_tr_b16 a[240:241], v103 offset:25088\n\t"
        "ds_read_b64_tr_b16 a[242:243], v103 offset:25216\n\t"
        "ds_read_b64_tr_b16 a[244:245], v103 offset:29184\n\t"
        "ds_read_b64_tr_b16 a[246:247], v103 offset:29312\n\t"
        "ds_read_b64_tr_b16 a[248:249], v104 offset:25088\n\t"
        "ds_read_b64_tr_b16 a[250:251], v104 offset:25216\n\t"
        "ds_read_b64_tr_b16 a[252:253], v104 offset:29184\n\t"
        "ds_read_b64_tr_b16 a[254:255], v104 offset:29312\n\t"
        "s_waitcnt lgkmcnt(0)\n\t"
        "rec_v_code:\n\t"
        "s_and_b32 s80, s40, 3\n\t"
        "s_lshl_b32 s80, s80, 17\n\t"
        "s_add_u32 s57, s80, %[nA]\n\t"
        "s_add_u32 s58, s57, 0x1000\n\t"
        "s_add_u32 s59, s57, 0x8000\n\t"
        "s_add_u32 s60, s57, 0x9000\n\t"
        "v_mov_b32_e32 v92, -1\n\t"
        "v_mov_b32_e32 v93, -1\n\t"
        "v_mov_b32_e32 v94, -1\n\t"
        "v_mov_b32_e32 v95, -1\n\t"
        "v_mov_b32_e32 v48, 0\n\t"
        "v_mov_b32_e32 v49, 0\n\t"
        "v_mov_b32_e32 v50, 0\n\t"
        "v_mov_b32_e32 v51, 0\n\t"
        "v_mov_b32_e32 v52, 0\n\t"
        "v_mov_b32_e32 v53, 0\n\t"
        "v_mov_b32_e32 v54, 0\n\t"
        "v_mov_b32_e32 v55, 0\n\t"
        "v_mov_b32_e32 v56, 0\n\t"
        "v_mov_b32_e32 v57, 0\n\t"
        "v_mov_b32_e32 v58, 0\n\t"
        "v_mov_b32_e32 v59, 0\n\t"
        "v_mov_b32_e32 v60, 0\n\t"
        "v_mov_b32_e32 v61, 0\n\t"
        "v_mov_b32_e32 v62, 0\n\t"
        "v_mov_b32_e32 v63, 0\n\t"
        "s_barrier\n\t"
        "s_memrealtime s[76:77]\n\t"
        "s_mov_b32 s79, %[nT]\n\t"
        "s_add_u32 s61, %[b0], 0x40000\n\t"
        "s_mov_b32 s62, %[b0]\n\t"
        "s_sub_u32 s75, s75, 16\n\t"
        "s_waitcnt lgkmcnt(0)\n\t"
        "s_mov_b32 s41, s76\n\t"
        "s_mov_b32 s69, s76\n\t"
        ".p2align 6\n\t"
        ".Lloop%=:\n\t"
        "s_mov_b32 s97, 0\n\t"
        "s_movk_i32 s96, 0x1000\n\t"
        ".Lwa%=:\n\t"
        "s_memrealtime s[98:99]\n\t"
        "s_addk_i32 s96, -1\n\t"
        "s_cmp_lt_i32 s96, 0\n\t"
        "s_cbranch_scc1 .Lta%=\n\t"
        "s_waitcnt lgkmcnt(0)\n\t"
        "s_sub_i32 s98, s98, s69\n\t"
        "s_cmp_lt_i32 s98, %c[iFT]\n\t"
        "s_cbranch_scc1 .Lwa%=\n\t"
        "s_cmp_ge_i32 s98, 0\n\t"
        "s_cbranch_scc1 .Lxa%=\n\t"
        ".Lqa%=:\n\t"
        "s_nop %c[iFN]\n\t"
        "s_addk_i32 s98, 1\n\t"
        "s_cmp_lt_i32 s98, 0\n\t"
        "s_cbranch_scc1 .Lqa%=\n\t"
        ".Lxa%=:\n\t"
        "buffer_load_dwordx4 v[0:3], v112, %[srd], s57 offen sc1\n\t"
        "s_nop 7\n\t"
        "buffer_load_dwordx4 v[4:7], v112, %[srd], s58 offen sc1\n\t"
        "s_nop 7\n\t"
        "buffer_load_dwordx4 v[8:11], v112, %[srd], s59 offen sc1\n\t"
        "s_nop 7\n\t"
        "buffer_load_dwordx4 v[12:15], v112, %[srd], s60 offen sc1\n\t"
        "v_mov_b32_e32 v80, 161792\n\t"
        "ds_read_b64 v[80:81], v80\n\t"
        "s_waitcnt lgkmcnt(0)\n\t"
        "v_readfirstlane_b32 s80, v80\n\t"
        "v_readfirstlane_b32 s81, v81\n\t"
        "s_cmp_eq_u32 s80, s40\n\t"
        "s_cselect_b32 s84, 1, 0\n\t"
        "s_cmp_eq_u32 s81, s40\n\t"
        "s_cselect_b32 s81, 4, 0\n\t"
        "s_or_b32 s84, s84, s81\n\t"
        "v_mov_b32_e32 v106, s76\n\t"
        "v_mov_b32_e32 v107, s84\n\t"
        "buffer_store_dwordx2 v[106:107], v117, %[srd], s79 offen sc1\n\t"
        "s_and_b32 s80, s40, 3\n\t"
        "s_lshl_b32 s80, s80, 17\n\t"
        "s_add_u32 s63, s80, %[nC]\n\t"
        "s_add_u32 s66, s80, %[nS]\n\t"
        "s_add_u32 s81, s80, 0x20000\n\t"
        "s_and_b32 s81, s81, 0x60000\n\t"
        "s_add_u32 s67, s81, %[nW]\n\t"
        "s_add_u32 s79, s81, %[nT]\n\t"
        "s_cmp_eq_u32 %[w], 2\n\t"
        "s_cselect_b32 s81, s80, s81\n\t"
        "s_xor_b32 s81, s81, 0x40000\n\t"
        "s_add_u32 s68, s81, %[pob]\n\t"
        "s_lshl_b32 s72, s40, 17\n\t"
        "s_add_u32 s72, s72, %[kvo]\n\t"
        "s_mov_b32 s80, s61\n\t"
        "s_mov_b32 s61, s62\n\t"
        "s_mov_b32 s62, s80\n\t"
        ".Lgo%=:\n\t"
        "s_waitcnt vmcnt(4)\n\t"
        "v_smfmac_f32_16x16x64_bf16 v[52:55], v[0:3], v[136:143], v113\n\t"
        "v_smfmac_f32_16x16x64_bf16 v[56:59], v[0:3], v[144:151], v113\n\t"
        "v_smfmac_f32_16x16x64_bf16 v[60:63], v[0:3], v[152:159], v113\n\t"
        "v_smfmac_f32_16x16x64_bf16 v[48:51], v[0:3], v[128:135], v113\n\t"
        "s_waitcnt vmcnt(3)\n\t"
        "v_smfmac_f32_16x16x64_bf16 v[52:55], v[4:7], v[168:175], v113\n\t"
        "v_smfmac_f32_16x16x64_bf16 v[56:59], v[4:7], v[176:183], v113\n\t"
        "v_smfmac_f32_16x16x64_bf16 v[60:63], v[4:7], v[184:191], v113\n\t"
        "v_smfmac_f32_16x16x64_bf16 v[48:51], v[4:7], v[160:167], v113\n\t"
        "s_waitcnt vmcnt(2)\n\t"
        "v_smfmac_f32_16x16x64_bf16 v[52:55], v[8:11], v[200:207], v113\n\t"
        "v_smfmac_f32_16x16x64_bf16 v[56:59], v[8:11], v[208:215], v113\n\t"
        "v_smfmac_f32_16x16x64_bf16 v[60:63], v[8:11], v[216:223], v113\n\t"
        "v_smfmac_f32_16x16x64_bf16 v[48:51], v[8:11], v[192:199], v113\n\t"
        "s_waitcnt vmcnt(1)\n\t"
        "v_smfmac_f32_16x16x64_bf16 v[52:55], v[12:15], v[232:239], v113\n\t"
        "v_smfmac_f32_16x16x64_bf16 v[56:59], v[12:15], v[240:247], v113\n\t"
        "v_smfmac_f32_16x16x64_bf16 v[60:63], v[12:15], v[248:255], v113\n\t"
        "v_smfmac_f32_16x16x64_bf16 v[48:51], v[12:15], v[224:231], v113\n\t"
        "s_nop 1\n\t"
        "v_mov_b32_e32 v104, v104\n\t"
        "s_nop 1\n\t"
        "v_add_f32_e32 v98, v52, v54\n\t"
        "v_add_f32_e32 v99, v53, v55\n\t"
        "ds_write_b64 v123, v[98:99]\n\t"
        "v_add_f32_e32 v100, v56, v58\n\t"
        "v_add_f32_e32 v101, v57, v59\n\t"
        "ds_write_b64 v124, v[100:101]\n\t"
        "v_add_f32_e32 v102, v60, v62\n\t"
        "v_add_f32_e32 v103, v61, v63\n\t"
        "ds_write_b64 v125, v[102:103]\n\t"
        "v_pk_add_f32 v[96:97], v[48:49], v[50:51]\n\t"
        "v_cmp_u_f32_e32 vcc, v96, v97\n\t"
        "s_nop 2\n\t"
        "v_mov_b32_e32 v104, v104\n\t"
        "s_cbranch_vccnz .Lfa%=\n\t"
        ".Lra%=:\n\t"
        "v_mov_b32_e32 v104, v104\n\t"
        "s_nop 2\n\t"
        "v_mov_b32_e32 v104, v104\n\t"
        "s_nop 2\n\t"
        "v_mov_b32_e32 v104, v104\n\t"
        "s_nop 2\n\t"
        "v_mov_b32_e32 v104, v104\n\t"
        "s_waitcnt lgkmcnt(0)\n\t"
        "s_barrier\n\t"
        "v_mov_b32_e32 v104, v104\n\t"
        "ds_read_b64 v[98:99], v122 offset:512\n\t"
        "ds_read_b64 v[100:101], v122 offset:1024\n\t"
        "ds_read_b64 v[102:103], v122 offset:1536\n\t"
        "v_mov_b32_e32 v104, v104\n\t"
        "s_nop 2\n\t"
        "v_mov_b32_e32 v104, v104\n\t"
        "s_nop 2\n\t"
        "v_mov_b32_e32 v104, v104\n\t"
        "s_waitcnt lgkmcnt(2)\n\t"
        "v_pk_add_f32 v[96:97], v[96:97], v[98:99]\n\t"
        "s_waitcnt lgkmcnt(0)\n\t"
        "v_pk_add_f32 v[100:101], v[100:101], v[102:103]\n\t"
        "v_pk_add_f32 v[96:97], v[96:97], v[100:101]\n\t"
        "v_cvt_pk_bf16_f32 v80, v96, v97\n\t"
        "v_min_u32_e32 v80, -2, v80\n\t"
        "buffer_store_dword v80, v114, %[srd], s61 offen sc1\n\t"
        "s_cmp_eq_u32 s40, 63\n\t"
        "s_cbranch_scc1 .Lend%=\n\t"
        "v_mov_b32_e32 v64, 0\n\t"
        "v_mov_b32_e32 v65, 0\n\t"
        "v_mov_b32_e32 v66, 0\n\t"
        "v_mov_b32_e32 v67, 0\n\t"
        "v_mov_b32_e32 v68, 0\n\t"
        "v_mov_b32_e32 v69, 0\n\t"
        "v_mov_b32_e32 v70, 0\n\t"
        "v_mov_b32_e32 v71, 0\n\t"
        "v_mov_b32_e32 v72, 0\n\t"
        "v_mov_b32_e32 v73, 0\n\t"
        "v_mov_b32_e32 v74, 0\n\t"
        "v_mov_b32_e32 v75, 0\n\t"
        "v_mov_b32_e32 v76, 0\n\t"
        "v_mov_b32_e32 v77, 0\n\t"
        "v_mov_b32_e32 v78, 0\n\t"
        "v_mov_b32_e32 v79, 0\n\t"
        "s_xor_b32 s64, s63, %c[iCS1]\n\t"
        "s_xor_b32 s65, s63, %c[iCS2]\n\t"
        "s_xor_b32 s73, s64, %c[iCS2]\n\t"
        "s_sleep %c[VSLP]\n\t"
        "buffer_load_dwordx4 v[88:91], v112, %[srd], s66 offen sc1\n\t"
        "s_cmp_eq_u32 %[w], 0\n\t"
        "s_cselect_b32 exec_hi, 0, -1\n\t"
        "buffer_store_dwordx4 v[92:95], v112, %[srd], s68 offen sc1\n\t"
        "s_mov_b32 exec_hi, -1\n\t"
        "s_waitcnt lgkmcnt(0)\n\t"
        "v_mov_b32_e32 v48, 0\n\t"
        "v_mov_b32_e32 v49, 0\n\t"
        "v_mov_b32_e32 v50, 0\n\t"
        "v_mov_b32_e32 v51, 0\n\t"
        "v_mov_b32_e32 v52, 0\n\t"
        "v_mov_b32_e32 v53, 0\n\t"
        "v_mov_b32_e32 v54, 0\n\t"
        "v_mov_b32_e32 v55, 0\n\t"
        "v_mov_b32_e32 v56, 0\n\t"
        "v_mov_b32_e32 v57, 0\n\t"
        "v_mov_b32_e32 v58, 0\n\t"
        "v_mov_b32_e32 v59, 0\n\t"
        "v_mov_b32_e32 v60, 0\n\t"
        "v_mov_b32_e32 v61, 0\n\t"
        "v_mov_b32_e32 v62, 0\n\t"
        "v_mov_b32_e32 v63, 0\n\t"
        "s_add_u32 s81, s40, 1\n\t"
        "s_and_b32 s80, s81, 3\n\t"
        "s_lshl_b32 s80, s80, 17\n\t"
        "s_add_u32 s57, s80, %[nA]\n\t"
        "s_add_u32 s58, s57, 0x1000\n\t"
        "s_add_u32 s59, s57, 0x8000\n\t"
        "s_add_u32 s60, s57, 0x9000\n\t"
        "s_waitcnt vmcnt(1)\n\t"
        "s_mov_b32 s97, 0\n\t"
        ".Lsr%=:\n\t"
        "s_mov_b32 exec_hi, 0xffff\n\t"
        "v_subrev_u32_e32 v4, s41, v88\n\t"
        "v_subrev_u32_e32 v5, s41, v90\n\t"
        "v_max_i32_e32 v4, v4, v5\n\t"
        "v_max_u32_e32 v5, v89, v91\n\t"
        "v_cmp_eq_u32_e32 vcc, -1, v5\n\t"
        "v_max_i32_dpp v4, v4, v4 quad_perm:[1,0,3,2] row_mask:0xf bank_mask:0xf\n\t"
        "v_or_b32_e32 v6, v89, v91\n\t"
        "s_nop 0\n\t"
        "v_max_i32_dpp v4, v4, v4 quad_perm:[2,3,0,1] row_mask:0xf bank_mask:0xf\n\t"
        "s_nop 1\n\t"
        "v_max_i32_dpp v4, v4, v4 row_ror:4 row_mask:0xf bank_mask:0xf\n\t"
        "s_nop 1\n\t"
        "v_max_i32_dpp v4, v4, v4 row_ror:8 row_mask:0xf bank_mask:0xf\n\t"
        "s_cbranch_vccnz .Lsf%=\n\t"
        "v_readlane_b32 s85, v4, 0\n\t"
        "v_readlane_b32 s81, v4, 16\n\t"
        "v_readlane_b32 s56, v4, 32\n\t"
        "v_and_b32_e32 v5, 1, v6\n\t"
        "v_cmp_ne_u32_e64 s[86:87], 0, v5\n\t"
        "v_and_b32_e32 v5, 2, v6\n\t"
        "v_cmp_ne_u32_e64 s[88:89], 0, v5\n\t"
        "v_and_b32_e32 v5, 4, v6\n\t"
        "v_cmp_ne_u32_e64 s[90:91], 0, v5\n\t"
        "s_max_i32 s56, s56, s81\n\t"
        "s_add_u32 s92, s85, s41\n\t"
        "s_sub_u32 s92, s92, s43\n\t"
        "s_add_i32 s93, s56, s47\n\t"
        "s_mov_b32 s44, s46\n\t"
        "s_mov_b32 s46, s45\n\t"
        "s_mov_b32 s94, s48\n\t"
        "s_mov_b32 s43, s41\n\t"
        "s_cmp_gt_i32 s56, s54\n\t"
        "s_cbranch_scc1 .Lmv%=\n\t"
        "s_mov_b32 s37, 0\n\t"
        "s_mov_b32 s48, 0\n\t"
        "s_mov_b32 s95, 0\n\t"
        ".Lsd%=:\n\t"
        "s_mov_b32 exec_hi, -1\n\t"
        "s_add_u32 s42, s43, s46\n\t"
        "s_mov_b32 s97, 0\n\t"
        "s_movk_i32 s96, 0x1000\n\t"
        ".Lwc%=:\n\t"
        "s_memrealtime s[98:99]\n\t"
        "s_addk_i32 s96, -1\n\t"
        "s_cmp_lt_i32 s96, 0\n\t"
        "s_cbranch_scc1 .Ltc%=\n\t"
        "s_waitcnt lgkmcnt(0)\n\t"
        "s_sub_i32 s98, s98, s42\n\t"
        "s_cmp_lt_i32 s98, %c[iFT]\n\t"
        "s_cbranch_scc1 .Lwc%=\n\t"
        "s_cmp_ge_i32 s98, 0\n\t"
        "s_cbranch_scc1 .Lxc%=\n\t"
        ".Lqc%=:\n\t"
        "s_nop %c[iFN]\n\t"
        "s_addk_i32 s98, 1\n\t"
        "s_cmp_lt_i32 s98, 0\n\t"
        "s_cbranch_scc1 .Lqc%=\n\t"
        ".Lxc%=:\n\t"
        "buffer_load_dwordx4 v[16:19], v112, %[srd], s63 offen sc1\n\t"
        "s_cmp_ge_u32 s40, %c[iCT0]\n\t"
        "s_cselect_b32 s85, 1, 0\n\t"
        "s_cmp_lg_u64 s[86:87], 0\n\t"
        "s_cselect_b32 s86, s85, 0\n\t"
        "s_cmp_lg_u64 s[88:89], 0\n\t"
        "s_cselect_b32 s87, s85, 0\n\t"
        "s_cmp_lg_u64 s[90:91], 0\n\t"
        "s_cselect_b32 s88, s85, 0\n\t"
        "buffer_load_dwordx4 v[20:23], v115, %[srd], s63 offen sc1\n\t"
        "s_or_b32 s80, s94, s86\n\t"
        "s_or_b32 s80, s80, s87\n\t"
        "s_andn2_b32 s90, s85, s80\n\t"
        "s_cmp_le_i32 s92, s44\n\t"
        "s_cselect_b32 s80, s85, 0\n\t"
        "s_andn2_b32 s91, s80, s88\n\t"
        "s_and_b32 s80, s40, %c[LDEC]\n\t"
        "s_cselect_b32 s89, 0, 1\n\t"
        "buffer_load_dwordx4 v[24:27], v112, %[srd], s64 offen sc1\n\t"
        "s_andn2_b32 s80, s86, s94\n\t"
        "s_cmp_lt_i32 s51, %c[LA24]\n\t"
        "s_cselect_b32 s80, s80, 0\n\t"
        "s_lshl2_add_u32 s51, s80, s51\n\t"
        "s_sub_i32 s51, s51, s89\n\t"
        "s_max_i32 s51, s51, %c[LA]\n\t"
        "s_lshl_b32 s80, s93, 1\n\t"
        "s_sub_i32 s81, s80, s53\n\t"
        "buffer_load_dwordx4 v[28:31], v115, %[srd], s64 offen sc1\n\t"
        "s_ashr_i32 s82, s81, %c[UPSH]\n\t"
        "s_max_i32 s82, s82, %c[TRUP]\n\t"
        "s_add_i32 s82, s82, s53\n\t"
        "s_min_i32 s82, s82, s80\n\t"
        "s_ashr_i32 s83, s81, %c[DNSH]\n\t"
        "s_min_i32 s83, s83, -%c[TRDN]\n\t"
        "s_add_i32 s83, s83, s53\n\t"
        "s_max_i32 s83, s83, s80\n\t"
        "buffer_load_dwordx4 v[32:35], v112, %[srd], s65 offen sc1\n\t"
        "s_cmp_gt_i32 s81, 0\n\t"
        "s_cselect_b32 s82, s82, s83\n\t"
        "s_cmp_lg_u32 s91, 0\n\t"
        "s_cselect_b32 s53, s82, s53\n\t"
        "s_add_i32 s80, s53, 1\n\t"
        "s_ashr_i32 s80, s80, 1\n\t"
        "s_add_i32 s47, s80, s51\n\t"
        "s_add_u32 s81, s47, %c[AUP]\n\t"
        "buffer_load_dwordx4 v[36:39], v115, %[srd], s65 offen sc1\n\t"
        "s_min_i32 s80, s95, s81\n\t"
        "s_max_i32 s47, s47, s80\n\t"
        "s_add_u32 s41, s42, s47\n\t"
        "s_cmp_eq_u32 s40, 0\n\t"
        "s_cselect_b32 s51, %c[LA], s51\n\t"
        "s_mov_b32 s80, s88\n\t"
        "s_add_i32 s81, s92, s50\n\t"
        "s_sub_i32 s81, s81, 4\n\t"
        "buffer_load_dwordx4 v[40:43], v112, %[srd], s73 offen sc1\n\t"
        "s_cmp_le_i32 s81, s44\n\t"
        "s_cselect_b32 s80, s80, 0\n\t"
        "s_cmp_lt_i32 s50, %c[LC24]\n\t"
        "s_cselect_b32 s80, s80, 0\n\t"
        "s_lshl2_add_u32 s50, s80, s50\n\t"
        "s_sub_i32 s50, s50, s89\n\t"
        "s_max_i32 s50, s50, %c[LC]\n\t"
        "s_lshl_b32 s80, s92, 1\n\t"
        "buffer_load_dwordx4 v[44:47], v115, %[srd], s73 offen sc1\n\t"
        "s_sub_i32 s81, s80, s52\n\t"
        "s_ashr_i32 s82, s81, %c[UPSH]\n\t"
        "s_max_i32 s82, s82, %c[TRUP]\n\t"
        "s_add_i32 s82, s82, s52\n\t"
        "s_min_i32 s82, s82, s80\n\t"
        "s_ashr_i32 s83, s81, %c[DNSH]\n\t"
        "s_min_i32 s83, s83, -%c[TRDN]\n\t"
        "s_add_i32 s83, s83, s52\n\t"
        "s_max_i32 s83, s83, s80\n\t"
        "s_cmp_gt_i32 s81, 0\n\t"
        "s_cselect_b32 s82, s82, s83\n\t"
        "s_cmp_lg_u32 s90, 0\n\t"
        "s_cselect_b32 s52, s82, s52\n\t"
        "s_add_i32 s80, s52, 1\n\t"
        "s_ashr_i32 s80, s80, 1\n\t"
        "s_add_i32 s45, s80, s50\n\t"
        ".p2align 6\n\t"
        ".Lrc%=:\n\t"
        "s_waitcnt vmcnt(7)\n\t"
        "v_smfmac_f32_16x16x64_bf16 v[64:67], v[16:19], a[0:7], v113\n\t"
        "v_smfmac_f32_16x16x64_bf16 v[68:71], v[16:19], a[8:15], v113\n\t"
        "v_smfmac_f32_16x16x64_bf16 v[72:75], v[16:19], a[16:23], v113\n\t"
        "v_smfmac_f32_16x16x64_bf16 v[76:79], v[16:19], a[24:31], v113\n\t"
        "s_waitcnt vmcnt(6)\n\t"
        "v_smfmac_f32_16x16x64_bf16 v[64:67], v[20:23], a[32:39], v113\n\t"
        "v_smfmac_f32_16x16x64_bf16 v[68:71], v[20:23], a[40:47], v113\n\t"
        "v_smfmac_f32_16x16x64_bf16 v[72:75], v[20:23], a[48:55], v113\n\t"
        "v_smfmac_f32_16x16x64_bf16 v[76:79], v[20:23], a[56:63], v113\n\t"
        "s_waitcnt vmcnt(5)\n\t"
        "v_smfmac_f32_16x16x64_bf16 v[64:67], v[24:27], a[64:71], v113\n\t"
        "v_smfmac_f32_16x16x64_bf16 v[68:71], v[24:27], a[72:79], v113\n\t"
        "v_smfmac_f32_16x16x64_bf16 v[72:75], v[24:27], a[80:87], v113\n\t"
        "v_smfmac_f32_16x16x64_bf16 v[76:79], v[24:27], a[88:95], v113\n\t"
        "s_waitcnt vmcnt(4)\n\t"
        "v_smfmac_f32_16x16x64_bf16 v[64:67], v[28:31], a[96:103], v113\n\t"
        "v_smfmac_f32_16x16x64_bf16 v[68:71], v[28:31], a[104:111], v113\n\t"
        "v_smfmac_f32_16x16x64_bf16 v[72:75], v[28:31], a[112:119], v113\n\t"
        "v_smfmac_f32_16x16x64_bf16 v[76:79], v[28:31], a[120:127], v113\n\t"
        "s_waitcnt vmcnt(3)\n\t"
        "v_smfmac_f32_16x16x64_bf16 v[64:67], v[32:35], a[128:135], v113\n\t"
        "v_smfmac_f32_16x16x64_bf16 v[68:71], v[32:35], a[136:143], v113\n\t"
        "v_smfmac_f32_16x16x64_bf16 v[72:75], v[32:35], a[144:151], v113\n\t"
        "v_smfmac_f32_16x16x64_bf16 v[76:79], v[32:35], a[152:159], v113\n\t"
        "s_waitcnt vmcnt(2)\n\t"
        "v_smfmac_f32_16x16x64_bf16 v[64:67], v[36:39], a[160:167], v113\n\t"
        "v_smfmac_f32_16x16x64_bf16 v[68:71], v[36:39], a[168:175], v113\n\t"
        "v_smfmac_f32_16x16x64_bf16 v[72:75], v[36:39], a[176:183], v113\n\t"
        "v_smfmac_f32_16x16x64_bf16 v[76:79], v[36:39], a[184:191], v113\n\t"
        "s_waitcnt vmcnt(1)\n\t"
        "v_smfmac_f32_16x16x64_bf16 v[64:67], v[40:43], a[192:199], v113\n\t"
        "v_smfmac_f32_16x16x64_bf16 v[68:71], v[40:43], a[200:207], v113\n\t"
        "v_smfmac_f32_16x16x64_bf16 v[72:75], v[40:43], a[208:215], v113\n\t"
        "v_smfmac_f32_16x16x64_bf16 v[76:79], v[40:43], a[216:223], v113\n\t"
        "s_waitcnt vmcnt(0)\n\t"
        "v_smfmac_f32_16x16x64_bf16 v[64:67], v[44:47], a[224:231], v113\n\t"
        "s_nop 2\n\t"
        "v_smfmac_f32_16x16x64_bf16 v[68:71], v[44:47], a[232:239], v113\n\t"
        "s_cmp_gt_u32 %[w], 2\n\t"
        "s_nop 1\n\t"
        "v_smfmac_f32_16x16x64_bf16 v[72:75], v[44:47], a[240:247], v113\n\t"
        "v_add_f32_e32 v80, v64, v66\n\t"
        "s_nop 0\n\t"
        "v_smfmac_f32_16x16x64_bf16 v[76:79], v[44:47], a[248:255], v113\n\t"
        "v_add_f32_e32 v81, v65, v67\n\t"
        "v_add_f32_e32 v82, v68, v70\n\t"
        "v_add_f32_e32 v83, v69, v71\n\t"
        "s_mov_b32 exec_lo, 0\n\t"
        "s_nop 0\n\t"
        "v_add_f32_e32 v80, v72, v74\n\t"
        "v_add_f32_e32 v81, v73, v75\n\t"
        "s_nop 0\n\t"
        "v_pk_add_f32 v[82:83], v[76:77], v[78:79]\n\t"
        "s_mov_b32 exec_lo, -1\n\t"
        "s_nop 0\n\t"
        "v_permlane32_swap_b32_e32 v80, v82\n\t"
        "v_add_f32_e32 v80, v80, v82\n\t"
        "ds_write_b32 v119, v80\n\t"
        "v_permlane32_swap_b32_e32 v81, v83\n\t"
        "v_add_f32_e32 v81, v81, v83\n\t"
        "ds_write_b32 v119, v81 offset:128\n\t"
        "v_cmp_u_f32_e32 vcc, v80, v81\n\t"
        "s_nop 2\n\t"
        "v_mov_b32_e32 v104, v104\n\t"
        "s_cbranch_vccnz .Lfc%=\n\t"
        ".Lrd%=:\n\t"
        "v_mov_b32_e32 v104, v104\n\t"
        "s_cbranch_scc1 .Lno%=\n\t"
        "v_mov_b32_e32 v104, v104\n\t"
        "s_nop 2\n\t"
        "v_mov_b32_e32 v104, v104\n\t"
        "s_waitcnt lgkmcnt(0)\n\t"
        "s_barrier\n\t"
        "v_mov_b32_e32 v104, v104\n\t"
        "ds_read_b64 v[0:1], v120 offset:8192\n\t"
        "ds_read_b64 v[2:3], v120 offset:8704\n\t"
        "ds_read_b64 v[4:5], v120 offset:9216\n\t"
        "ds_read_b64 v[6:7], v120 offset:9728\n\t"
        "v_mov_b32_e32 v104, v104\n\t"
        "s_nop 2\n\t"
        "v_mov_b32_e32 v104, v104\n\t"
        "s_nop 2\n\t"
        "v_mov_b32_e32 v104, v104\n\t"
        "s_waitcnt lgkmcnt(2)\n\t"
        "v_pk_add_f32 v[0:1], v[0:1], v[2:3]\n\t"
        "s_waitcnt lgkmcnt(0)\n\t"
        "v_pk_add_f32 v[4:5], v[4:5], v[6:7]\n\t"
        "v_pk_add_f32 v[0:1], v[0:1], v[4:5]\n\t"
        "v_cvt_pk_bf16_f32 v0, v0, v1\n\t"
        "v_min_u32_e32 v0, -2, v0\n\t"
        "buffer_store_dword v0, v116, %[srd], s67 offen sc1\n\t"
        ".Lnp%=:\n\t"
        "s_memrealtime s[76:77]\n\t"
        "s_add_u32 s40, s40, 1\n\t"
        "s_waitcnt lgkmcnt(0)\n\t"
        "s_add_u32 s81, s76, %c[iVD]\n\t"
        "s_sub_u32 s80, s76, s41\n\t"
        "s_cmp_gt_i32 s80, %c[iTOL]\n\t"
        "s_cselect_b32 s69, s81, s41\n\t"
        "s_branch .Lloop%=\n\t"
        ".Lmv%=:\n\t"
        ".Lhk0%=:\n\t"
        "s_cbranch_execz .Lhk1%=\n\t"
        "s_mov_b32 s48, 1\n\t"
        "s_cmp_eq_u32 s54, %c[iTOL]\n\t"
        "s_cbranch_scc1 .Lm1%=\n\t"
        "s_max_i32 s80, s85, s56\n\t"
        "s_add_u32 s43, s41, s80\n\t"
        "s_add_u32 s43, s43, %c[iPEN0]\n\t"
        ".Lhk1%=:\n\t"
        "s_cbranch_execz .Lhk2%=\n\t"
        "s_mov_b32 s54, %c[iTOL]\n\t"
        "s_add_u32 s46, s46, %c[iX0C]\n\t"
        "s_mov_b32 s95, 0\n\t"
        "s_add_u32 s51, s51, %c[iX0A]\n\t"
        "v_and_b32_e32 v4, 3, v88\n\t"
        "v_and_b32_e32 v5, 3, v90\n\t"
        ".Lhk2%=:\n\t"
        "s_cbranch_execz .Lhk3%=\n\t"
        "v_lshlrev_b32_e64 v4, v4, 1\n\t"
        "v_lshlrev_b32_e64 v5, v5, 1\n\t"
        "v_or_b32_e32 v4, v4, v5\n\t"
        "s_mov_b32 s80, 0\n\t"
        "v_and_b32_e32 v5, 1, v4\n\t"
        "v_cmp_ne_u32_e32 vcc, 0, v5\n\t"
        ".Lhk3%=:\n\t"
        "s_cbranch_execz .Lhk4%=\n\t"
        "s_nop 1\n\t"
        "s_cmp_lg_u64 vcc, 0\n\t"
        "s_cselect_b32 s81, 2, 0\n\t"
        "s_or_b32 s80, s80, s81\n\t"
        "v_and_b32_e32 v5, 2, v4\n\t"
        "v_cmp_ne_u32_e32 vcc, 0, v5\n\t"
        ".Lhk4%=:\n\t"
        "s_cbranch_execz .Lhk5%=\n\t"
        "s_nop 1\n\t"
        "s_cmp_lg_u64 vcc, 0\n\t"
        "s_cselect_b32 s81, 4, 0\n\t"
        "s_or_b32 s80, s80, s81\n\t"
        "v_and_b32_e32 v5, 4, v4\n\t"
        "v_cmp_ne_u32_e32 vcc, 0, v5\n\t"
        ".Lhk5%=:\n\t"
        "s_cbranch_execz .Lhk6%=\n\t"
        "s_nop 1\n\t"
        "s_cmp_lg_u64 vcc, 0\n\t"
        "s_cselect_b32 s81, 8, 0\n\t"
        "s_or_b32 s80, s80, s81\n\t"
        "v_and_b32_e32 v5, 8, v4\n\t"
        "v_cmp_ne_u32_e32 vcc, 0, v5\n\t"
        ".Lhk6%=:\n\t"
        "s_cbranch_execz .Lhk7%=\n\t"
        "s_nop 1\n\t"
        "s_cmp_lg_u64 vcc, 0\n\t"
        "s_cselect_b32 s81, 16, 0\n\t"
        "s_or_b32 s80, s80, s81\n\t"
        "s_lshr_b32 s80, 0x1adf1210, s80\n\t"
        "s_and_b32 s55, s80, 3\n\t"
        ".Lhk7%=:\n\t"
        "s_cbranch_execz .Lhk8%=\n\t"
        "s_branch .Lal%=\n\t"
        ".Lm1%=:\n\t"
        "s_add_u32 s80, s37, %c[iPEN2]\n\t"
        "s_min_i32 s80, s80, %c[iPENM]\n\t"
        "s_cmp_lg_u32 s37, 0\n\t"
        "s_cselect_b32 s37, s80, %c[iPEN]\n\t"
        "s_add_u32 s80, s56, s37\n\t"
        ".Lhk8%=:\n\t"
        "s_cbranch_execz .Lhk9%=\n\t"
        "s_max_i32 s80, s80, %c[iPMIN]\n\t"
        "s_add_u32 s43, s41, s80\n\t"
        "s_add_u32 s95, s93, %c[LA]\n\t"
        "s_add_u32 s46, s46, %c[iXR]\n\t"
        ".Lal%=:\n\t"
        "s_sub_u32 s80, s55, s43\n\t"
        "s_and_b32 s80, s80, %c[iPHM]\n\t"
        ".Lhk9%=:\n\t"
        "s_cbranch_execz .Lhk10%=\n\t"
        "s_add_u32 s43, s43, s80\n\t"
        "s_branch .Lsd%=\n\t"
        ".Lsf%=:\n\t"
        "s_mov_b32 exec_hi, -1\n\t"
        "s_sleep 2\n\t"
        "buffer_load_dwordx4 v[88:91], v112, %[srd], s66 offen sc1\n\t"
        "s_add_u32 s97, s97, 1\n\t"
        ".Lhk10%=:\n\t"
        "s_cbranch_execz .Lhk11%=\n\t"
        "s_cmp_lt_u32 s97, 0x100000\n\t"
        "s_cbranch_scc0 .Lgu%=\n\t"
        "s_waitcnt vmcnt(0)\n\t"
        "s_branch .Lsr%=\n\t"
        ".Lfa%=:\n\t"
        "v_max_u32_e32 v84, v0, v2\n\t"
        "v_max3_u32 v84, v84, v1, v3\n\t"
        ".Lhk11%=:\n\t"
        "s_cbranch_execz .Lhk12%=\n\t"
        "v_max3_u32 v84, v84, v4, v6\n\t"
        "v_max3_u32 v84, v84, v5, v7\n\t"
        "v_max3_u32 v84, v84, v8, v10\n\t"
        "v_max3_u32 v84, v84, v9, v11\n\t"
        "v_max3_u32 v84, v84, v12, v14\n\t"
        "v_max3_u32 v84, v84, v13, v15\n\t"
        ".Lhk12%=:\n\t"
        "s_cbranch_execz .Lhk13%=\n\t"
        "v_cmp_eq_u32_e32 vcc, -1, v84\n\t"
        "s_nop 1\n\t"
        "v_mov_b32_e32 v104, v104\n\t"
        "s_nop 0\n\t"
        "s_cbranch_vccz .Lra%=\n\t"
        "s_add_u32 s80, s40, 1\n\t"
        ".Lhk13%=:\n\t"
        "s_cbranch_execz .Lhk14%=\n\t"
        "v_mov_b32_e32 v80, 161792\n\t"
        "v_mov_b32_e32 v81, s80\n\t"
        "ds_write_b32 v80, v81\n\t"
        "v_mov_b32_e32 v48, 0\n\t"
        "v_mov_b32_e32 v49, 0\n\t"
        "v_mov_b32_e32 v50, 0\n\t"
        ".Lhk14%=:\n\t"
        "s_cbranch_execz .Lhk15%=\n\t"
        "v_mov_b32_e32 v51, 0\n\t"
        "v_mov_b32_e32 v52, 0\n\t"
        "v_mov_b32_e32 v53, 0\n\t"
        "v_mov_b32_e32 v54, 0\n\t"
        "v_mov_b32_e32 v55, 0\n\t"
        "v_mov_b32_e32 v56, 0\n\t"
        ".Lhk15%=:\n\t"
        "s_cbranch_execz .Lhk16%=\n\t"
        "v_mov_b32_e32 v57, 0\n\t"
        "v_mov_b32_e32 v58, 0\n\t"
        "v_mov_b32_e32 v59, 0\n\t"
        "v_mov_b32_e32 v60, 0\n\t"
        "v_mov_b32_e32 v61, 0\n\t"
        "v_mov_b32_e32 v62, 0\n\t"
        ".Lhk16%=:\n\t"
        "s_cbranch_execz .Lhk17%=\n\t"
        "v_mov_b32_e32 v63, 0\n\t"
        "s_sleep %c[VASL]\n\t"
        "buffer_load_dwordx4 v[0:3], v112, %[srd], s57 offen sc1\n\t"
        "buffer_load_dwordx4 v[4:7], v112, %[srd], s58 offen sc1\n\t"
        "buffer_load_dwordx4 v[8:11], v112, %[srd], s59 offen sc1\n\t"
        "buffer_load_dwordx4 v[12:15], v112, %[srd], s60 offen sc1\n\t"
        ".Lhk17%=:\n\t"
        "s_cbranch_execz .Lhk18%=\n\t"
        "buffer_store_dword v0, off, %[wsd], s36\n\t"
        "s_add_u32 s97, s97, 1\n\t"
        "s_cmp_lt_u32 s97, 0x100000\n\t"
        "s_cbranch_scc0 .Lgu%=\n\t"
        "s_branch .Lgo%=\n\t"
        ".Lfc%=:\n\t"
        "v_max_u32_e32 v84, v16, v18\n\t"
        ".Lhk18%=:\n\t"
        "s_cbranch_execz .Lhk19%=\n\t"
        "v_max3_u32 v84, v84, v20, v22\n\t"
        "v_max3_u32 v84, v84, v24, v26\n\t"
        "v_max3_u32 v84, v84, v28, v30\n\t"
        "v_max3_u32 v84, v84, v32, v34\n\t"
        "v_max3_u32 v84, v84, v36, v38\n\t"
        "v_max3_u32 v84, v84, v40, v42\n\t"
        ".Lhk19%=:\n\t"
        "s_cbranch_execz .Lhk20%=\n\t"
        "v_max3_u32 v84, v84, v44, v46\n\t"
        "v_cmp_eq_u32_e32 vcc, -1, v84\n\t"
        "s_nop 1\n\t"
        "v_mov_b32_e32 v104, v104\n\t"
        "s_nop 0\n\t"
        "s_cbranch_vccz .Lrd%=\n\t"
        ".Lhk20%=:\n\t"
        "s_cbranch_execz .Lhk21%=\n\t"
        "s_sleep %c[CSLP]\n\t"
        "buffer_load_dwordx4 v[16:19], v112, %[srd], s63 offen sc1\n\t"
        "v_mov_b32_e32 v64, 0\n\t"
        "v_mov_b32_e32 v65, 0\n\t"
        "buffer_load_dwordx4 v[20:23], v115, %[srd], s63 offen sc1\n\t"
        "v_mov_b32_e32 v66, 0\n\t"
        ".Lhk21%=:\n\t"
        "s_cbranch_execz .Lhk22%=\n\t"
        "v_mov_b32_e32 v67, 0\n\t"
        "buffer_load_dwordx4 v[24:27], v112, %[srd], s64 offen sc1\n\t"
        "v_mov_b32_e32 v68, 0\n\t"
        "v_mov_b32_e32 v69, 0\n\t"
        "buffer_load_dwordx4 v[28:31], v115, %[srd], s64 offen sc1\n\t"
        "v_mov_b32_e32 v70, 0\n\t"
        ".Lhk22%=:\n\t"
        "s_cbranch_execz .Lhk23%=\n\t"
        "v_mov_b32_e32 v71, 0\n\t"
        "buffer_load_dwordx4 v[32:35], v112, %[srd], s65 offen sc1\n\t"
        "v_mov_b32_e32 v72, 0\n\t"
        "v_mov_b32_e32 v73, 0\n\t"
        "buffer_load_dwordx4 v[36:39], v115, %[srd], s65 offen sc1\n\t"
        "v_mov_b32_e32 v74, 0\n\t"
        ".Lhk23%=:\n\t"
        "s_cbranch_execz .Lhk24%=\n\t"
        "v_mov_b32_e32 v75, 0\n\t"
        "buffer_load_dwordx4 v[40:43], v112, %[srd], s73 offen sc1\n\t"
        "v_mov_b32_e32 v76, 0\n\t"
        "v_mov_b32_e32 v77, 0\n\t"
        "buffer_load_dwordx4 v[44:47], v115, %[srd], s73 offen sc1\n\t"
        "v_mov_b32_e32 v78, 0\n\t"
        ".Lhk24%=:\n\t"
        "s_cbranch_execz .Lhk25%=\n\t"
        "v_mov_b32_e32 v79, 0\n\t"
        "s_add_u32 s80, s40, 1\n\t"
        "v_mov_b32_e32 v80, 161796\n\t"
        "v_mov_b32_e32 v81, s80\n\t"
        "ds_write_b32 v80, v81\n\t"
        "s_add_u32 s97, s97, 1\n\t"
        ".Lhk25%=:\n\t"
        "s_cbranch_execz .Lhk26%=\n\t"
        "s_cmp_lt_u32 s97, 0x100000\n\t"
        "s_cbranch_scc0 .Lgu%=\n\t"
        "s_branch .Lrc%=\n\t"
        ".Lno%=:\n\t"
        "s_waitcnt lgkmcnt(0)\n\t"
        "s_barrier\n\t"
        "s_branch .Lnp%=\n\t"
        ".Lta%=:\n\t"
        ".Lhk26%=:\n\t"
        "s_cbranch_execz .Lhk27%=\n\t"
        "s_waitcnt lgkmcnt(0)\n\t"
        "s_branch .Lxa%=\n\t"
        ".Ltc%=:\n\t"
        "s_waitcnt lgkmcnt(0)\n\t"
        "s_branch .Lxc%=\n\t"
        ".Lgu%=:\n\t"
        "s_mov_b64 exec, -1\n\t"
        "v_mov_b32_e32 v80, %c[ferr]\n\t"
        ".Lhk27%=:\n\t"
        "s_cbranch_execz .Lhk28%=\n\t"
        "v_mov_b32_e32 v81, 2\n\t"
        "buffer_store_dword v81, v80, %[wsd], 0 offen sc1\n\t"
        "s_endpgm\n\t"
        ".Lend%=:\n\t"
        "buffer_store_dword v80, v114, %[wsd], s72 offen sc1\n\t"
        "s_endpgm\n\t"
        ".Lhk28%=:\n\t"
        "s_branch .Lhr%="
        : [nA] "+s"(nA), [nS] "+s"(nS), [nT] "+s"(nT), [nW] "+s"(nW), [nC] "+s"(nC), [b0] "+s"(b0), [pob] "+s"(pob)
        : [srd] "s"(rhd), [wsd] "s"(wsd), [w] "s"(w), [tag] "s"(tag), [kvo] "s"(kvo), [cal] "s"(cal), [sv] "s"(sv), [so] "s"(so), [m0b] "s"(m0b), [key] "s"(key), [tu] "s"(tu),
          [ferr] "i"(F_ERR), [iD1] "i"(D1), [iD2] "i"(D2), [LC] "i"(LC0), [LA] "i"(LA0), [LC24] "i"(LC0 + 24), [LA24] "i"(LA0 + 24), [E1] "i"(2 * (D1 - LC0)), [E2] "i"(2 * (D2 - LA0)),
          [iPEN] "i"(PEN), [iPEN2] "i"(PEN2), [iPENM] "i"(PEN + 2 * PEN2), [iPEN0] "i"(PEN0), [iTOL] "i"(TOL), [iCT0] "i"(CT0), [iX0C] "i"(X0C), [iX0A] "i"(X0A), [iPHM] "i"(PH_M),
          [iFT] "i"(-3), [iFN] "i"(FINE_N), [AUP] "i"(A_UP), [ASLP] "i"(A_SLP), [LDEC] "i"(L_DEC), [UPSH] "i"(UP_SH), [TRUP] "i"(TR_UP), [TRDN] "i"(TR_DN), [HSLP] "i"(H_SLP), [iPMIN] "i"(PMIN), [VSLP] "i"(V_SLP), [RSLP] "i"(R_SLP), [iVD] "i"(V_DEL), [iXR] "i"(XR), [VASL] "i"(VA_SLP), [CSLP] "i"(C_SLP), [DNSH] "i"(DN_SH), [NX0] "i"(NEARX), [NX1] "i"(8192 - NEARX), [iCS1] "i"(CS1), [iCS2] "i"(CS2), [NY0] "i"(NEARY), [NY1] "i"(16384 - NEARY),
          "{v[112:127]}"(pc), "{v[96:103]}"(pa), "{v[104:105]}"(pb)
        : "memory", "vcc", "scc", "m0", "s36", "s37", "s38", "s39", "s40", "s41", "s42", "s43", "s44", "s45", "s46", "s47", "s48", "s49", "s50", "s51", "s52", "s53", "s54", "s55", "s56", "s57", "s58", "s59", "s60", "s61", "s62", "s63", "s64", "s65", "s66", "s67", "s68", "s69", "s70", "s71", "s72", "s73", "s74", "s75", "s76", "s77", "s78", "s79", "s80", "s81", "s82", "s83", "s84", "s85", "s86", "s87", "s88", "s89", "s90", "s91", "s92", "s93", "s94", "s95", "s96", "s97", "s98", "s99");
    }
}

using f32x16 = __attribute__((ext_vector_type(16))) float;
constexpr int CV_RP = 80;
constexpr int CV_U = 96 * CV_RP;
constexpr int CV_KP = 4416;
constexpr int CV_K0 = 4 * CV_U;
constexpr int CV_Y = CV_K0 + 16 * CV_KP;
__device__ __forceinline__ float lo16(unsigned x) { return __uint_as_float(x << 16); }
__device__ __forceinline__ float hi16(unsigned x) { return __uint_as_float(x & 0xFFFF0000u); }
__device__ __forceinline__ void prep_block(const Params& p, rsrc_t r, int k4, const int wv, const unsigned tag) {
    const int tid = otid(), w = wv, l = tid & 63;
    const int cu = (k4 & 3) * 32 + w * 8 + (l >> 3), oc0 = (k4 >> 2) * 8 + (l & 7);
    const rsrc_t rk = __builtin_amdgcn_make_buffer_rsrc((void*)p.Ak, 0, S * D * 2, 0x00020000), rv = __builtin_amdgcn_make_buffer_rsrc((void*)p.Av, 0, S * D * 2, 0x00020000);
    v4u sk[4], kn[11], vn[8], gw[2];
    auto fetch = [&](int oc) {
#pragma unroll
        for (int j = 0; j < 11; ++j) kn[j] = __builtin_amdgcn_raw_buffer_load_b128(rk, (unsigned)((oc * 8 + j - 3) * 2048 + cu * 16), 0, 2);
#pragma unroll
        for (int i = 0; i < 8; ++i) vn[i] = __builtin_amdgcn_raw_buffer_load_b128(rv, (unsigned)((oc * 8 + i) * 2048 + cu * 16), 0, 2); };
    fetch(oc0);
#pragma unroll
    for (int q = 0; q < 4; ++q) sk[q] = *(const v4u*)(p.shift_k + cu * 32 + q * 8);
#pragma unroll
    for (int q = 0; q < 2; ++q) gw[q] = *(const v4u*)(p.gate_w + q * 512 + l * 8);
    st4(r, tid < 128 ? F_RDY + k4 * 512 + tid * 4 : tid < 132 ? F_ATT + (k4 * 4 + tid - 128) * 64 : (tid == 192 && k4 == 0) ? F_CONV : 0xF0000000u, 0u);
#pragma unroll 1
    for (int it = 0; it < 4; ++it) {
        const int oc = oc0 + 64 * it, tk = (it * 32 + k4) * 16 + w;
        v4u kr[11], vr[8], xg[4][2];
#pragma unroll
        for (int j = 0; j < 11; ++j) kr[j] = kn[j];
#pragma unroll
        for (int i = 0; i < 8; ++i) vr[i] = vn[i];
        __builtin_amdgcn_sched_barrier(0);
#pragma unroll
        for (int i = 0; i < 4; ++i)
#pragma unroll
            for (int q = 0; q < 2; ++q) xg[i][q] = *(const v4u*)(p.Aq + (size_t)(tk + 4 * i) * D + q * 512 + l * 8);
        fetch(it < 3 ? oc + 64 : 4096);
        __builtin_amdgcn_sched_barrier(0);
#pragma unroll
        for (int c8 = 0; c8 < 8; ++c8) {
            const unsigned s01 = sk[c8 >> 1][(c8 & 1) * 2], s23 = sk[c8 >> 1][(c8 & 1) * 2 + 1];
            const float sv[4] = {hi16(s23), lo16(s23), hi16(s01), lo16(s01)};
            float kk[11], u[8];
#pragma unroll
            for (int j = 0; j < 11; ++j) kk[j] = (c8 & 1) ? hi16(kr[j][c8 >> 1]) : lo16(kr[j][c8 >> 1]);
            f32x2 acc[4];
#pragma unroll
            for (int j = 0; j < 4; ++j)
#pragma unroll
                for (int i = 0; i < 4; ++i) { const f32x2 t = rbf2((f32x2){kk[i + j], kk[i + j + 4]} * sv[j]); acc[i] = j == 0 ? t : rbf2(acc[i] + t); }
#pragma unroll
            for (int i = 0; i < 8; ++i) u[i] = acc[i & 3][i >> 2] * ((c8 & 1) ? hi16(vr[i][c8 >> 1]) : lo16(vr[i][c8 >> 1]));
            st16(r, W_U + (cu * 8 + c8) * 4096 + oc * 16, (v4u){pack2(u[7], u[6]), pack2(u[5], u[4]), pack2(u[3], u[2]), pack2(u[1], u[0])});
        }
        float ga[4];
#pragma unroll
        for (int i = 0; i < 4; ++i) { float acc = 0.f;
#pragma unroll
            for (int q = 0; q < 2; ++q)
#pragma unroll
                for (int e = 0; e < 4; ++e) { const unsigned xa = xg[i][q][e], gb = gw[q][e]; acc += rbfh(lo16(xa) * lo16(gb)); acc += rbfh(hi16(xa) * hi16(gb)); }
            ga[i] = acc; }
        for (int o = 32; o > 0; o >>= 1)
#pragma unroll
            for (int i = 0; i < 4; ++i) ga[i] += __shfl_xor(ga[i], o);
        const float z = rbfh(l == 0 ? ga[0] : l == 1 ? ga[1] : l == 2 ? ga[2] : ga[3]);
        st4(r, l < 4 ? W_GATE + (tk + 4 * l) * 4 : 0xF0000000u, __float_as_uint(rbfh(1.f / (1.f + expf(-z)))));
    }
    DRAIN(); __syncthreads();
    if (tid == 0) st4(r, F_PREP + k4 * 4, tag);
}
__device__ __forceinline__ bool conv_block(const Params& p, rsrc_t r, const int g0, const int n, unsigned char* lds, const int wv, const int j, const unsigned tag, const unsigned skew) {
    (void)j;
    const int tid = otid(), w = wv, l = tid & 63;
    const rsrc_t rs = __builtin_amdgcn_make_buffer_rsrc((void*)p.ssm_k, 0, S * D * 2, 0x00020000);
    v4u kr[4], uv[4]; v2u kx[4]; unsigned dn;
    auto fetch_k = [&](int ch) {
#pragma unroll
        for (int i = 0; i < 4; ++i) { const unsigned o = (unsigned)(ch * 4096 + (i * 64 + l) * 16);
            kr[i] = __builtin_amdgcn_raw_buffer_load_b128(rs, o, 0, 2); kx[i] = __builtin_amdgcn_raw_buffer_load_b64(rs, (i == 3 && l == 63) ? 0xF0000000u : o + 16, 0, 2); }
        dn = p.ssm_D[ch & 1023]; };
    auto fetch_u = [&](unsigned o) {
#pragma unroll
        for (int i = 0; i < 4; ++i) uv[i] = ld16(r, o + (i * 64 + l) * 16); };
    fetch_k(g0 * 4 + w);
    unsigned char* const uc = lds + w * CV_U; unsigned char* const kc = lds + CV_K0 + w * 4 * CV_KP;
    {
        const v4u z = {0u, 0u, 0u, 0u};
#pragma unroll
        for (int i = 0; i < 3; ++i) *(v4u*)(uc + min(i * 64 + l, 159) * 16) = z;
#pragma unroll
        for (int i = 0; i < 2; ++i) { const int q = min(i * 64 + l, 79), e = q % 20; *(v4u*)(kc + (q / 20) * CV_KP + e * 16 + (e < 8 ? 0 : 4096)) = z; }
    }
    if (!wait_prep(r, l, tag)) return false;
    wait_long(rtnow() + skew);
    fetch_u(W_U + (g0 * 4 + w) * 4096);
    const int b = l & 31, kg = l >> 5, cc = (b + 1) & 3;
#pragma unroll 1
    for (int it = 0; it < n; ++it) {
        const unsigned dv = dn;
#pragma unroll
        for (int i = 0; i < 4; ++i) { const v4u d = kr[i]; const v2u e = kx[i]; unsigned char* q = kc + 128 + (i * 64 + l) * 16;
            const unsigned s0 = __builtin_amdgcn_alignbit(d.y, d.x, 16), s1 = __builtin_amdgcn_alignbit(d.z, d.y, 16), s2 = __builtin_amdgcn_alignbit(d.w, d.z, 16), s3 = __builtin_amdgcn_alignbit(e.x, d.w, 16), s4 = __builtin_amdgcn_alignbit(e.y, e.x, 16);
            *(v4u*)q = d; *(v4u*)(q + CV_KP) = (v4u){s0, s1, s2, s3}; *(v4u*)(q + 2 * CV_KP) = (v4u){d.y, d.z, d.w, e.x}; *(v4u*)(q + 3 * CV_KP) = (v4u){s1, s2, s3, s4};
            if (i == 0 && l == 0) { *(unsigned*)(q + CV_KP - 4) = d.x << 16; *(unsigned*)(q + 2 * CV_KP - 4) = d.x; *(v2u*)(q + 3 * CV_KP - 8) = (v2u){d.x << 16, s0}; } }
#pragma unroll
        for (int i = 0; i < 4; ++i) *(v4u*)(uc + (32 + i * 16 + (l >> 2)) * CV_RP + (l & 3) * 16) = uv[i];
        __builtin_amdgcn_wave_barrier();
        __builtin_amdgcn_sched_barrier(0);
        fetch_k(it + 1 < n ? (g0 + it + 1) * 4 + w : 1024); fetch_u(it + 1 < n ? W_U + ((g0 + it + 1) * 4 + w) * 4096 : 0xF0000000u);
        __builtin_amdgcn_sched_barrier(0);
        f32x16 acc0, acc1, ad0, ad1;
#pragma unroll
        for (int i = 0; i < 16; ++i) { acc0[i] = 0.f; acc1[i] = 0.f; ad0[i] = 0.f; ad1[i] = 0.f; }
        typedef const __attribute__((address_space(3))) v2u* l2p;
        unsigned kb = (unsigned)(size_t)(kc + cc * CV_KP + (b - 8 * kg - 7 + 64 - cc) * 2) - 32, kb8 = kb + 8;
        asm volatile("" : "+v"(kb), "+v"(kb8));
        const unsigned char* ub = uc + (32 + b) * CV_RP + kg * 16;
        v2u ka[4][2], kz[4][2]; bf16x8 fa[4], fz[4];
        auto ld_k = [&](int set, int un, int hi) { const int dl = un >> 1, kh = un & 1; const unsigned o = (unsigned)((32 * dl - 16 * kh + 16) * 2 + hi * 2048);
            if (hi) { kz[set][0] = *(l2p)(size_t)(kb + o); kz[set][1] = *(l2p)(size_t)(kb8 + o); } else { ka[set][0] = *(l2p)(size_t)(kb + o); ka[set][1] = *(l2p)(size_t)(kb8 + o); } };
        auto ld_f = [&](int set, int un) { const int dl = un >> 1, kh = un & 1;
            fa[set] = *(const bf16x8*)(ub + (32 - dl) * CV_RP + kh * 32); fz[set] = *(const bf16x8*)(ub - dl * CV_RP + kh * 32); };
#pragma unroll
        for (int q = 0; q < 2; ++q) { ld_k(q, q, 0); ld_f(q, q); ld_k(q, q, 1); }
#pragma unroll
        for (int kh = 0; kh < 2; ++kh) { const int e = 7 - (b - 16 * kh - 8 * kg); const unsigned x = dv << ((e & 1) * 16); const int q = e >> 1;
            const v4u di = {q == 0 ? x : 0u, q == 1 ? x : 0u, q == 2 ? x : 0u, q == 3 ? x : 0u};
            asm volatile("s_nop 1\n\tv_mfma_f32_32x32x16_bf16 %0, %2, %4, %0\n\tv_mfma_f32_32x32x16_bf16 %1, %3, %4, %1" : "+v"(ad1), "+v"(ad0) : "v"(fa[kh]), "v"(fz[kh]), "v"(di)); }
#pragma unroll 1
        for (int u0 = 0; u0 < 64; u0 += 4) {
#pragma unroll
            for (int q = 0; q < 4; ++q) {
                const int un = u0 + q + 2, ns = (q + 2) & 3;
                const v4u kav = {ka[q][0].x, ka[q][0].y, ka[q][1].x, ka[q][1].y}, kzv = {kz[q][0].x, kz[q][0].y, kz[q][1].x, kz[q][1].y};
                __builtin_amdgcn_sched_barrier(0);
                asm volatile("s_nop 1\n\tv_mfma_f32_32x32x16_bf16 %0, %1, %2, %0" : "+v"(acc1) : "v"(fa[q]), "v"(kav));
                ld_k(ns, un, 0);
                __builtin_amdgcn_sched_barrier(0);
                asm volatile("v_mfma_f32_32x32x16_bf16 %0, %1, %2, %0" : "+v"(acc0) : "v"(fz[q]), "v"(kav));
                ld_f(ns, un);
                __builtin_amdgcn_sched_barrier(0);
                asm volatile("v_mfma_f32_32x32x16_bf16 %0, %1, %2, %0" : "+v"(acc1) : "v"(fz[q]), "v"(kzv));
                ld_k(ns, un, 1);
            }
        }
        asm volatile("s_nop 7\n\ts_nop 7" : "+v"(acc0), "+v"(acc1), "+v"(ad0), "+v"(ad1));
        unsigned char* const yb = lds + CV_Y + (it & 1) * 16384;
#pragma unroll
        for (int i = 0; i < 32; i += 2) { const int row = (i >> 4) * 32 + (i & 3) + 8 * ((i & 15) >> 2) + 4 * kg;
            const unsigned y = pack2(rbfh(i < 16 ? acc0[i] : acc1[i - 16]) + rbfh(i < 16 ? ad0[i] : ad1[i - 16]), rbfh(i < 16 ? acc0[i + 1] : acc1[i - 15]) + rbfh(i < 16 ? ad0[i + 1] : ad1[i - 15]));
            *(unsigned short*)(yb + (row * 32 + b) * 8 + w * 2) = (unsigned short)y; *(unsigned short*)(yb + (row * 32 + 32 + b) * 8 + w * 2) = (unsigned short)(y >> 16); }
        asm volatile("s_waitcnt lgkmcnt(0)\n\ts_barrier" ::: "memory");
#pragma unroll
        for (int i = 0; i < 4; ++i) { const int t = tid + 256 * i; st16(r, W_U + (g0 + it) * 16384 + t * 16, *(const v4u*)(yb + t * 16)); }
    }
    DRAIN(); __syncthreads();
    if (tid == 0) __hip_atomic_fetch_add((unsigned*)(p.ws + F_CONV), (unsigned)n, __ATOMIC_RELAXED, __HIP_MEMORY_SCOPE_AGENT);
    return true;
}

__device__ __forceinline__ void dma16(unsigned ldsdst, unsigned voff, const v4u& srd, unsigned soff) {
    asm volatile("s_mov_b32 m0, %0\n\ts_nop 0\n\tbuffer_load_dwordx4 %1, %2, %3 offen lds" :: "s"(ldsdst), "v"(voff), "s"(srd), "s"(soff) : "m0", "memory"); }
__device__ __forceinline__ v4u pack_acc(const f32x4& a, const f32x4& b) {
    float x0, x1, x2, x3, y0, y1, y2, y3;
    asm volatile("v_accvgpr_read_b32 %0, %8\n\tv_accvgpr_read_b32 %1, %9\n\tv_accvgpr_read_b32 %2, %10\n\tv_accvgpr_read_b32 %3, %11\n\t"
                 "v_accvgpr_read_b32 %4, %12\n\tv_accvgpr_read_b32 %5, %13\n\tv_accvgpr_read_b32 %6, %14\n\tv_accvgpr_read_b32 %7, %15"
                 : "=v"(x0), "=v"(x1), "=v"(x2), "=v"(x3), "=v"(y0), "=v"(y1), "=v"(y2), "=v"(y3)
                 : "a"(a[0]), "a"(a[1]), "a"(a[2]), "a"(a[3]), "a"(b[0]), "a"(b[1]), "a"(b[2]), "a"(b[3]));
    return (v4u){pack2(x0, x1), pack2(x2, x3), pack2(y0, y1), pack2(y2, y3)}; }
#define SB() __builtin_amdgcn_sched_barrier(0)
#define WO_REGS "a128", "a129", "a130", "a131", "a132", "a133", "a134", "a135", "a136", "a137", "a138", "a139", "a140", "a141", "a142", "a143", "a144", "a145", "a146", "a147", "a148", "a149", "a150", "a151", "a152", "a153", "a154", "a155", "a156", "a157", "a158", "a159", "a160", "a161", "a162", "a163", "a164", "a165", "a166", "a167", "a168", "a169", "a170", "a171", "a172", "a173", "a174", "a175", "a176", "a177", "a178", "a179", "a180", "a181", "a182", "a183", "a184", "a185", "a186", "a187", "a188", "a189", "a190", "a191", "a192", "a193", "a194", "a195", "a196", "a197", "a198", "a199", "a200", "a201", "a202", "a203", "a204", "a205", "a206", "a207", "a208", "a209", "a210", "a211", "a212", "a213", "a214", "a215", "a216", "a217", "a218", "a219", "a220", "a221", "a222", "a223", "a224", "a225", "a226", "a227", "a228", "a229", "a230", "a231", "a232", "a233", "a234", "a235", "a236", "a237", "a238", "a239", "a240", "a241", "a242", "a243", "a244", "a245", "a246", "a247", "a248", "a249", "a250", "a251", "a252", "a253", "a254", "a255", "v192", "v193", "v194", "v195", "v196", "v197", "v198", "v199", "v200", "v201", "v202", "v203", "v204", "v205", "v206", "v207", "v208", "v209", "v210", "v211", "v212", "v213", "v214", "v215", "v216", "v217", "v218", "v219", "v220", "v221", "v222", "v223", "v224", "v225", "v226", "v227", "v228", "v229", "v230", "v231", "v232", "v233", "v234", "v235", "v236", "v237", "v238", "v239", "v240", "v241", "v242", "v243", "v244", "v245", "v246", "v247", "v248", "v249", "v250", "v251", "v252", "v253", "v254", "v255"
struct NoHook { __device__ void operator()(int) const {} };
template <int MODE, int MI, int NJ, int NS, int HX = 0, class HOOK = NoHook, int MN = MI>
__device__ __forceinline__ void gemm(rsrc_t r, const bf16_t* __restrict__ A, const bf16_t* __restrict__ B, int m0, int n0, unsigned char* lds, unsigned coff, const int wv,
                                     const int rot, const void* tk = nullptr,
                                     const bool have = false, const bf16_t* Bn = nullptr, int m0n = 0, int n0n = 0, HOOK&& hook = NoHook()) {
    constexpr int AB = MN * 4096, BB = NJ * 2048, B0 = (NS / 2) * AB, NB = NJ / 2, NM = MI * NJ, NR = MI + NJ, BP = NJ * 64;
    static_assert(NS % 2 == 0 && 32 % NS == 0 && B0 <= 65536 && NS * BB <= 65536 && (NJ == 4 || NJ == 8), "ring");
    const int tid = otid();
    const int w = wv, l = tid & 63, n16 = l & 15, g = l >> 4;
    const int wm = w >> 1, wn = w & 1;
    const unsigned lb = __builtin_amdgcn_readfirstlane((unsigned)(size_t)lds);
    const v4u sa = mk_srd(A, 2048 * 1024 * 2); v4u sb = mk_srd(B, 1024 * 4096 * 2);
    f32x4 acc[MI][NJ];
#pragma unroll
    for (int i = 0; i < MI; ++i)
#pragma unroll
        for (int j = 0; j < NJ; ++j) acc[i][j] = (f32x4){0.f, 0.f, 0.f, 0.f};
    unsigned va = (unsigned)((m0 + 8 * MI * w + (l >> 3)) * 2048 + (((l & 7) ^ (l >> 3)) << 4));
    unsigned vb[2];
    if (NJ == 8) { vb[0] = (unsigned)((8 * w + (l >> 5)) * 8192 + n0 * 2) + (unsigned)(((l & 31) << 4) ^ ((l >> 5) << 5) ^ (wn << 7)); vb[1] = vb[0] ^ 64u; }
    else { vb[0] = (unsigned)((8 * w + g) * 8192 + n0 * 2) + (unsigned)((n16 << 4) ^ (g << 5) ^ (wn << 7)); vb[1] = vb[0]; }
    auto rk = [&](int kt) { return (kt & ~31) | ((kt + rot) & 31); };
    unsigned la = lb + w * MI * 1024;
    auto st_a = [&](int buf, int kt, int q) { dma16(la + buf * AB + q * 1024, va, sa, (unsigned)(rk(kt) * 64 + q * 16384)); };
    auto st_b = [&](int buf, int kt, int x) { dma16(lb + B0 + buf * BB + (w * NB + x) * 1024, vb[x & 1], sb, (unsigned)((rk(kt) * 32 + x * (8 / NB)) * 8192)); };
    const unsigned f = (unsigned)((n16 >> 2) | ((g & 1) << 2));
    constexpr int NQ = MODE == 1 ? NJ : NJ / 2;
    unsigned ra[2], rb[NQ];
    ra[0] = lb + (16 * MI * wm + n16) * 128 + ((g ^ (n16 & 7)) << 4); ra[1] = ra[0] ^ 64u;
    {   const unsigned row = lb + B0 + (8 * g + (n16 >> 2)) * BP + (NJ == 8 ? 256 * wn : 0), g0 = NJ == 8 ? 0u : 4u * wn;
#pragma unroll
        for (int j = 0; j < NQ; ++j) {
            if (MODE == 1) rb[j] = row + (((g0 + j) ^ f) << 5) + 8 * (n16 & 3);
            else rb[j] = row + (((g0 + 2 * j + ((n16 & 3) >> 1)) ^ f) << 5) + (n16 & 1) * 16;
            asm volatile("" : "+v"(rb[j])); } }
    bf16x8 af[2][MI], bfr[2][NJ];
    auto rd = [&](int set, int buf, int x) {
        if (x < NJ) { const unsigned a = rb[MODE == 1 ? x : (x >> 1)] + (MODE == 1 ? 0 : (x & 1) * 8) + buf * BB;
            const v2u lo = trb(a), hi = trb(a + 4 * BP); v4u v = {lo.x, lo.y, hi.x, hi.y}; bfr[set][x] = as_frag(v); }
        else af[set][x - NJ] = ldsa(ra[buf & 1] + (buf >> 1) * AB + (x - NJ) * 2048); };
    auto mf = [&](int set, int i, int j) {
        if (HX) asm volatile("v_mfma_f32_16x16x32_bf16 %0, %1, %2, %0" : "+a"(acc[i][j]) : "v"(MODE == 1 ? af[set][i] : bfr[set][j]), "v"(MODE == 1 ? bfr[set][j] : af[set][i]) : WO_REGS);
        else if (MODE == 1) asm volatile("v_mfma_f32_16x16x32_bf16 %0, %1, %2, %0" : "+a"(acc[i][j]) : "v"(af[set][i]), "v"(bfr[set][j]));
        else asm volatile("v_mfma_f32_16x16x32_bf16 %0, %1, %2, %0" : "+a"(acc[i][j]) : "v"(bfr[set][j]), "v"(af[set][i])); };
    if (!have) {
#pragma unroll
        for (int t = 0; t < NS; ++t) {
            if (t & 1) {
#pragma unroll
                for (int q = 0; q < MI; ++q) st_a(t >> 1, t - 1, q); }
#pragma unroll
            for (int q = 0; q < NB; ++q) st_b(t, t, q);
            if (HX) hook(t); }
    }
#pragma unroll
    for (int i = 0; i < MI; ++i)
#pragma unroll
        for (int j = 0; j < NJ; ++j) asm volatile("" : "+a"(acc[i][j]));
    asm volatile("s_nop 1" ::: "memory");
    if (!have) {
        asm volatile("s_waitcnt vmcnt(%c0)\n\ts_barrier" :: "i"((NS - 1) * (NB + HX) + (NS / 2 - 1) * MI) : "memory");
    }
#pragma unroll
    for (int x = 0; x < NR; ++x) rd(0, 0, x);
    constexpr int HEAD = NM / 8;
    const v4u st = mk_srd(tk, tk ? 32768 : 0); unsigned tdum = 0u;
#pragma unroll 1
    for (int k0 = 0; k0 < 32; k0 += NS) {
        int kb = k0 + NS;
        if (kb == 32) {
            kb = Bn ? 0 : 0x10100;
            if (Bn) { va += (unsigned)((m0n - m0 + 8 * (MN - MI) * w) * 2048); la = lb + w * MN * 1024; vb[0] += (unsigned)((n0n - n0) * 2); vb[1] += (unsigned)((n0n - n0) * 2); sb = mk_srd(Bn, 1024 * 4096 * 2); } }
        if (NS == 4) asm volatile("buffer_load_dword %0, %1, %2, %3 offen" : "+v"(tdum) : "v"(l < 32 ? (unsigned)l * 128u : 0xF0000000u), "s"(st), "s"((unsigned)k0 * 1024u) : "memory");
#pragma unroll
        for (int u = 0; u < NS; ++u) {
            SB();
#pragma unroll
            for (int q = 0; q < HEAD; ++q) mf(u & 1, q / NJ, q % NJ);
            SB();
            asm volatile("s_waitcnt vmcnt(%c0) lgkmcnt(0)\n\ts_barrier" :: "i"((NS - 2) * (NB + HX) + (NS / 2 - 1 - (u & 1)) * MI) : "memory");
            SB();
            constexpr int span = NM - HEAD;
#pragma unroll
            for (int q = HEAD; q < NM; ++q) {
                mf(u & 1, q / NJ, q % NJ);
                const int x = q - HEAD, nd = NB + ((u & 1) ? MI : 0);
#pragma unroll
                for (int y = 0; y < NR; ++y) if ((y * (span - HEAD)) / NR == x) rd((u & 1) ^ 1, (u + 1) % NS, y);
#pragma unroll
                for (int y = 0; y < nd; ++y) if ((y * span + span / 2) / nd == x) { if ((u & 1) && y < MI) st_a(u >> 1, kb + u - 1, y); else st_b(u, kb + u, y - ((u & 1) ? MI : 0)); }
                SB(); }
            if (MN > MI && (u & 1) && k0 == 32 - NS) {
#pragma unroll
                for (int y = MI; y < MN; ++y) st_a(u >> 1, kb + u - 1, y); }
            if (HX) hook(NS + k0 + u);
        }
    }
    asm volatile("s_nop 7\n\ts_nop 3" : "+v"(tdum) :: "memory");
    SB();
    const int col = n0 + 16 * NJ * wn, head = col >> 7, sub = (col & 127) >> 4, seg0 = (m0 >> 5) + (MI / 2) * wm;
#pragma unroll
    for (int sg = 0; sg < MI / 2; ++sg) {
        const unsigned base = coff + (unsigned)(((seg0 + sg) * 32 + head) * 8192 + l * 16);
#pragma unroll
        for (int q = 0; q < NJ; ++q) {
            if (HX) asm volatile("" ::: WO_REGS);
            if (MODE == 0) { const int tt = q / (NJ / 2), kk = q % (NJ / 2); st16(r, base + (tt * 4 + (sub >> 1) + kk) * 1024, pack_acc(acc[2 * sg + tt][2 * kk], acc[2 * sg + tt][2 * kk + 1])); }
            else st16(r, base + (sub + q) * 1024, pack_acc(acc[2 * sg][q], acc[2 * sg + 1][q])); }
    }
}

__device__ __forceinline__ void attn_block(const Params& p, const int wv, const int h, const unsigned tag) {
    const int l = otid() & 63, n16 = l & 15, g = l >> 4;
    const unsigned OOB = 0x80000000u;
    const v4u rd = mk_srd(p.ws, W_TOTAL);
    const unsigned kx = W_KX + h * 8192, qx = W_QX + h * 8192, vx = W_VF + h * 8192, km = HO_KVM + h * 4096;
    const v8u pc = {(unsigned)l * 16, (unsigned)(n16 * 16 + (g & 1) * 8), F_KVQ + ((l >> 2) * 32 + h) * 64 + min(l & 3, 2) * 4, F_PROG + h * 128, (unsigned)(wv * 2048 + l * 16),
                    (unsigned)(wv * 2048 + (8 * g + (n16 >> 2)) * 16 + (n16 & 1) * 8), (l & 31) ? OOB : F_ATT + (l >> 5) * 64, l < 32 ? F_RDY + l * 128 : OOB};
    const int dq = n16 - 4 * g;
    asm volatile(
        "s_mov_b32 s36, 0xf0000000\n\t"
        "s_mov_b32 s37, %[w]\n\t"
        "s_mov_b32 s38, s36\n\t"
        "s_mov_b32 s40, 0\n\t"
        "s_memrealtime s[98:99]\n\t"
        "s_waitcnt lgkmcnt(0)\n\t"
        "s_sub_u32 s41, s98, 0x100000\n\t"
        "s_mov_b64 s[42:43], 0\n\t"
        "s_mov_b32 s44, -1\n\t"
        "s_mov_b32 s45, -1\n\t"
        "s_mov_b32 s56, 0\n\t"
        "s_mov_b32 s57, -1\n\t"
        "s_lshl_b32 s58, %[w], 10\n\t"
        "s_add_u32 s58, s58, 16384\n\t"
        "v_cmp_le_i32_e64 s[48:49], 0, v252\n\t"
        "v_cmp_le_i32_e64 s[50:51], 1, v252\n\t"
        "v_cmp_le_i32_e64 s[52:53], 2, v252\n\t"
        "v_cmp_le_i32_e64 s[54:55], 3, v252\n\t"
        "v_mov_b32_e32 v253, 0xce6e6b28\n\t"
        "v_mov_b32_e32 v254, 0xff800000\n\t"
        ".Ltop%=:\n\t"
        "s_cmp_eq_u32 s38, 0\n\t"
        "s_cbranch_scc1 .La2%=\n\t"
        "s_mov_b32 s97, 0\n\t"
        ".La1%=:\n\t"
        "buffer_load_dword v240, v246, %[rd], 0 offen sc1\n\t"
        "buffer_load_dword v241, v247, %[rd], 0 offen sc1\n\t"
        "s_add_u32 s97, s97, 1\n\t"
        "s_cmp_lt_u32 s97, 0x200000\n\t"
        "s_cbranch_scc0 .Lgu%=\n\t"
        "s_memrealtime s[98:99]\n\t"
        "s_waitcnt vmcnt(0) lgkmcnt(0)\n\t"
        "v_cmp_eq_u32_e32 vcc, %[tag], v240\n\t"
        "s_nop 1\n\t"
        "s_or_b64 s[42:43], s[42:43], vcc\n\t"
        "v_readfirstlane_b32 s80, v241\n\t"
        "s_nop 0\n\t"
        "s_cmp_gt_u32 s80, s40\n\t"
        "s_cselect_b32 s41, s98, s41\n\t"
        "s_max_u32 s40, s40, s80\n\t"
        "s_and_b32 s80, s37, 60\n\t"
        "s_lshr_b64 s[80:81], s[42:43], s80\n\t"
        "s_andn2_b32 s80, 15, s80\n\t"
        "s_cbranch_scc0 .La1%=x\n\t"
        "s_sleep 16\n\t"
        "s_branch .La1%=\n\t"
        ".La1%=x:\n\t"
        "s_cmp_gt_u32 s40, s37\n\t"
        "s_cselect_b32 s39, 0, s36\n\t"
        "s_lshl_b32 s80, s37, 18\n\t"
        "s_or_b32 s80, s80, 0\n\t"
        "s_lshl_b32 s81, s37, 17\n\t"
        "s_add_u32 s81, s81, %[km]\n\t"
        "s_or_b32 s81, s81, s39\n\t"
        "s_add_u32 s82, s80, %[kx]\n\t"
        "s_add_u32 s83, s82, 0x1000\n\t"
        "buffer_load_dwordx4 a[0:3], v244, %[rd], s82 offen offset:0 sc1\n\t"
        "buffer_load_dwordx4 a[4:7], v244, %[rd], s82 offen offset:1024 sc1\n\t"
        "buffer_load_dwordx4 a[8:11], v244, %[rd], s82 offen offset:2048 sc1\n\t"
        "buffer_load_dwordx4 a[12:15], v244, %[rd], s82 offen offset:3072 sc1\n\t"
        "buffer_load_dwordx4 a[16:19], v244, %[rd], s83 offen offset:0 sc1\n\t"
        "buffer_load_dwordx4 a[20:23], v244, %[rd], s83 offen offset:1024 sc1\n\t"
        "buffer_load_dwordx4 a[24:27], v244, %[rd], s83 offen offset:2048 sc1\n\t"
        "buffer_load_dwordx4 a[28:31], v244, %[rd], s83 offen offset:3072 sc1\n\t"
        "s_cmp_eq_u64 s[42:43], -1\n\t"
        "s_cbranch_scc1 .La3%=\n\t"
        "s_sleep %c[aslp]\n\t"
        ".La3%=:\n\t"
        "s_sleep %c[bslp]\n\t"
        "s_add_u32 s82, s80, %[qx]\n\t"
        "s_add_u32 s83, s82, 0x1000\n\t"
        "buffer_load_dwordx4 a[32:35], v244, %[rd], s82 offen offset:0 sc1\n\t"
        "buffer_load_dwordx4 a[36:39], v244, %[rd], s82 offen offset:1024 sc1\n\t"
        "buffer_load_dwordx4 a[40:43], v244, %[rd], s82 offen offset:2048 sc1\n\t"
        "buffer_load_dwordx4 a[44:47], v244, %[rd], s82 offen offset:3072 sc1\n\t"
        "buffer_load_dwordx4 a[48:51], v244, %[rd], s83 offen offset:0 sc1\n\t"
        "buffer_load_dwordx4 a[52:55], v244, %[rd], s83 offen offset:1024 sc1\n\t"
        "buffer_load_dwordx4 a[56:59], v244, %[rd], s83 offen offset:2048 sc1\n\t"
        "buffer_load_dwordx4 a[60:63], v244, %[rd], s83 offen offset:3072 sc1\n\t"
        "s_cmp_eq_u64 s[42:43], -1\n\t"
        "s_cbranch_scc1 .La4%=\n\t"
        "s_sleep %c[aslp]\n\t"
        ".La4%=:\n\t"
        "s_sleep %c[bslp]\n\t"
        "buffer_load_dwordx4 a[96:99], v244, %[rd], s81 offen offset:0 sc1\n\t"
        "buffer_load_dwordx4 a[100:103], v244, %[rd], s81 offen offset:1024 sc1\n\t"
        "buffer_load_dwordx2 a[104:105], v245, %[rd], s81 offen offset:2048 sc1\n\t"
        "buffer_load_dwordx2 a[106:107], v245, %[rd], s81 offen offset:2304 sc1\n\t"
        "buffer_load_dwordx2 a[108:109], v245, %[rd], s81 offen offset:2560 sc1\n\t"
        "buffer_load_dwordx2 a[110:111], v245, %[rd], s81 offen offset:2816 sc1\n\t"
        "buffer_load_dwordx2 a[112:113], v245, %[rd], s81 offen offset:3072 sc1\n\t"
        "buffer_load_dwordx2 a[114:115], v245, %[rd], s81 offen offset:3328 sc1\n\t"
        "buffer_load_dwordx2 a[116:117], v245, %[rd], s81 offen offset:3584 sc1\n\t"
        "buffer_load_dwordx2 a[118:119], v245, %[rd], s81 offen offset:3840 sc1\n\t"
        "s_add_u32 s82, s80, %[vx]\n\t"
        "s_add_u32 s83, s82, 0x1000\n\t"
        "buffer_load_dwordx4 a[64:67], v244, %[rd], s82 offen offset:0 sc1\n\t"
        "buffer_load_dwordx4 a[68:71], v244, %[rd], s82 offen offset:1024 sc1\n\t"
        "buffer_load_dwordx4 a[72:75], v244, %[rd], s82 offen offset:2048 sc1\n\t"
        "buffer_load_dwordx4 a[76:79], v244, %[rd], s82 offen offset:3072 sc1\n\t"
        "buffer_load_dwordx4 a[80:83], v244, %[rd], s83 offen offset:0 sc1\n\t"
        "buffer_load_dwordx4 a[84:87], v244, %[rd], s83 offen offset:1024 sc1\n\t"
        "buffer_load_dwordx4 a[88:91], v244, %[rd], s83 offen offset:2048 sc1\n\t"
        "buffer_load_dwordx4 a[92:95], v244, %[rd], s83 offen offset:3072 sc1\n\t"
        "s_cmp_eq_u64 s[42:43], -1\n\t"
        "s_cbranch_scc1 .La5%=\n\t"
        "s_sleep %c[aslp]\n\t"
        ".La5%=:\n\t"
        "s_sleep %c[bslp]\n\t"
        "buffer_store_dword v0, off, %[rd], s36\n\t"
        "buffer_store_dword v0, off, %[rd], s36\n\t"
        "buffer_store_dword v0, off, %[rd], s36\n\t"
        "buffer_store_dword v0, off, %[rd], s36\n\t"
        "buffer_store_dword v0, off, %[rd], s36\n\t"
        "buffer_store_dword v0, off, %[rd], s36\n\t"
        "buffer_store_dword v0, off, %[rd], s36\n\t"
        "buffer_store_dword v0, off, %[rd], s36\n\t"
        "buffer_store_dword v0, off, %[rd], s36\n\t"
        ".La2%=:\n\t"
        "s_cmp_gt_u32 s40, s37\n\t"
        "s_cselect_b32 s80, s39, 0\n\t"
        "s_cmp_eq_u32 s80, 0\n\t"
        "s_cbranch_scc1 .La6%=\n\t"
        "s_mov_b32 s39, 0\n\t"
        "s_lshl_b32 s81, s37, 17\n\t"
        "s_add_u32 s81, s81, %[km]\n\t"
        "buffer_load_dwordx4 a[96:99], v244, %[rd], s81 offen offset:0 sc1\n\t"
        "buffer_load_dwordx4 a[100:103], v244, %[rd], s81 offen offset:1024 sc1\n\t"
        "buffer_load_dwordx2 a[104:105], v245, %[rd], s81 offen offset:2048 sc1\n\t"
        "buffer_load_dwordx2 a[106:107], v245, %[rd], s81 offen offset:2304 sc1\n\t"
        "buffer_load_dwordx2 a[108:109], v245, %[rd], s81 offen offset:2560 sc1\n\t"
        "buffer_load_dwordx2 a[110:111], v245, %[rd], s81 offen offset:2816 sc1\n\t"
        "buffer_load_dwordx2 a[112:113], v245, %[rd], s81 offen offset:3072 sc1\n\t"
        "buffer_load_dwordx2 a[114:115], v245, %[rd], s81 offen offset:3328 sc1\n\t"
        "buffer_load_dwordx2 a[116:117], v245, %[rd], s81 offen offset:3584 sc1\n\t"
        "buffer_load_dwordx2 a[118:119], v245, %[rd], s81 offen offset:3840 sc1\n\t"
        "buffer_store_dword v0, off, %[rd], s36\n\t"
        "buffer_store_dword v0, off, %[rd], s36\n\t"
        "buffer_store_dword v0, off, %[rd], s36\n\t"
        "buffer_store_dword v0, off, %[rd], s36\n\t"
        "buffer_store_dword v0, off, %[rd], s36\n\t"
        "buffer_store_dword v0, off, %[rd], s36\n\t"
        "buffer_store_dword v0, off, %[rd], s36\n\t"
        "buffer_store_dword v0, off, %[rd], s36\n\t"
        ".La6%=:\n\t"
        "s_add_u32 s86, s37, 4\n\t"
        "s_and_b32 s80, s86, 60\n\t"
        "s_lshr_b64 s[80:81], s[42:43], s80\n\t"
        "s_andn2_b32 s80, 15, s80\n\t"
        "s_cselect_b32 s46, s36, 0\n\t"
        "s_cmp_lt_u32 s86, 64\n\t"
        "s_cselect_b32 s46, s46, s36\n\t"
        "s_cmp_gt_u32 s40, s86\n\t"
        "s_cselect_b32 s47, s46, s36\n\t"
        "s_lshl_b32 s80, s86, 18\n\t"
        "s_or_b32 s80, s80, s46\n\t"
        "s_lshl_b32 s81, s86, 17\n\t"
        "s_add_u32 s81, s81, %[km]\n\t"
        "s_or_b32 s81, s81, s47\n\t"
        "s_add_u32 s82, s80, %[kx]\n\t"
        "s_add_u32 s83, s82, 0x1000\n\t"
        "buffer_load_dwordx4 v[0:3], v244, %[rd], s82 offen offset:0 sc1\n\t"
        "buffer_load_dwordx4 v[4:7], v244, %[rd], s82 offen offset:1024 sc1\n\t"
        "buffer_load_dwordx4 v[8:11], v244, %[rd], s82 offen offset:2048 sc1\n\t"
        "buffer_load_dwordx4 v[12:15], v244, %[rd], s82 offen offset:3072 sc1\n\t"
        "buffer_load_dwordx4 v[16:19], v244, %[rd], s83 offen offset:0 sc1\n\t"
        "buffer_load_dwordx4 v[20:23], v244, %[rd], s83 offen offset:1024 sc1\n\t"
        "buffer_load_dwordx4 v[24:27], v244, %[rd], s83 offen offset:2048 sc1\n\t"
        "buffer_load_dwordx4 v[28:31], v244, %[rd], s83 offen offset:3072 sc1\n\t"
        "s_cmp_eq_u64 s[42:43], -1\n\t"
        "s_cbranch_scc1 .La7%=\n\t"
        "s_sleep %c[aslp]\n\t"
        ".La7%=:\n\t"
        "s_sleep %c[bslp]\n\t"
        "s_add_u32 s82, s80, %[qx]\n\t"
        "s_add_u32 s83, s82, 0x1000\n\t"
        "buffer_load_dwordx4 v[32:35], v244, %[rd], s82 offen offset:0 sc1\n\t"
        "buffer_load_dwordx4 v[36:39], v244, %[rd], s82 offen offset:1024 sc1\n\t"
        "buffer_load_dwordx4 v[40:43], v244, %[rd], s82 offen offset:2048 sc1\n\t"
        "buffer_load_dwordx4 v[44:47], v244, %[rd], s82 offen offset:3072 sc1\n\t"
        "buffer_load_dwordx4 v[48:51], v244, %[rd], s83 offen offset:0 sc1\n\t"
        "buffer_load_dwordx4 v[52:55], v244, %[rd], s83 offen offset:1024 sc1\n\t"
        "buffer_load_dwordx4 v[56:59], v244, %[rd], s83 offen offset:2048 sc1\n\t"
        "buffer_load_dwordx4 v[60:63], v244, %[rd], s83 offen offset:3072 sc1\n\t"
        "s_cmp_eq_u64 s[42:43], -1\n\t"
        "s_cbranch_scc1 .La8%=\n\t"
        "s_sleep %c[aslp]\n\t"
        ".La8%=:\n\t"
        "s_sleep %c[bslp]\n\t"
        "buffer_load_dwordx4 v[96:99], v244, %[rd], s81 offen offset:0 sc1\n\t"
        "buffer_load_dwordx4 v[100:103], v244, %[rd], s81 offen offset:1024 sc1\n\t"
        "buffer_load_dwordx2 v[104:105], v245, %[rd], s81 offen offset:2048 sc1\n\t"
        "buffer_load_dwordx2 v[106:107], v245, %[rd], s81 offen offset:2304 sc1\n\t"
        "buffer_load_dwordx2 v[108:109], v245, %[rd], s81 offen offset:2560 sc1\n\t"
        "buffer_load_dwordx2 v[110:111], v245, %[rd], s81 offen offset:2816 sc1\n\t"
        "buffer_load_dwordx2 v[112:113], v245, %[rd], s81 offen offset:3072 sc1\n\t"
        "buffer_load_dwordx2 v[114:115], v245, %[rd], s81 offen offset:3328 sc1\n\t"
        "buffer_load_dwordx2 v[116:117], v245, %[rd], s81 offen offset:3584 sc1\n\t"
        "buffer_load_dwordx2 v[118:119], v245, %[rd], s81 offen offset:3840 sc1\n\t"
        "s_add_u32 s82, s80, %[vx]\n\t"
        "s_add_u32 s83, s82, 0x1000\n\t"
        "buffer_load_dwordx4 v[64:67], v244, %[rd], s82 offen offset:0 sc1\n\t"
        "buffer_load_dwordx4 v[68:71], v244, %[rd], s82 offen offset:1024 sc1\n\t"
        "buffer_load_dwordx4 v[72:75], v244, %[rd], s82 offen offset:2048 sc1\n\t"
        "buffer_load_dwordx4 v[76:79], v244, %[rd], s82 offen offset:3072 sc1\n\t"
        "buffer_load_dwordx4 v[80:83], v244, %[rd], s83 offen offset:0 sc1\n\t"
        "buffer_load_dwordx4 v[84:87], v244, %[rd], s83 offen offset:1024 sc1\n\t"
        "buffer_load_dwordx4 v[88:91], v244, %[rd], s83 offen offset:2048 sc1\n\t"
        "buffer_load_dwordx4 v[92:95], v244, %[rd], s83 offen offset:3072 sc1\n\t"
        "s_cmp_eq_u64 s[42:43], -1\n\t"
        "s_cbranch_scc1 .La9%=\n\t"
        "s_sleep %c[aslp]\n\t"
        ".La9%=:\n\t"
        "s_sleep %c[bslp]\n\t"
        "s_waitcnt vmcnt(42)\n\t"
        "s_memrealtime s[98:99]\n\t"
        "v_mfma_f32_16x16x32_bf16 v[136:139], a[0:3], a[32:35], 0\n\t"
        "v_mfma_f32_16x16x32_bf16 v[148:151], a[0:3], a[48:51], 0\n\t"
        "v_mfma_f32_16x16x32_bf16 v[152:155], a[16:19], a[48:51], 0\n\t"
        "v_mfma_f32_16x16x32_bf16 v[136:139], a[4:7], a[36:39], v[136:139]\n\t"
        "v_mfma_f32_16x16x32_bf16 v[148:151], a[4:7], a[52:55], v[148:151]\n\t"
        "v_mfma_f32_16x16x32_bf16 v[152:155], a[20:23], a[52:55], v[152:155]\n\t"
        "v_mfma_f32_16x16x32_bf16 v[136:139], a[8:11], a[40:43], v[136:139]\n\t"
        "v_mfma_f32_16x16x32_bf16 v[148:151], a[8:11], a[56:59], v[148:151]\n\t"
        "v_mfma_f32_16x16x32_bf16 v[152:155], a[24:27], a[56:59], v[152:155]\n\t"
        "v_mfma_f32_16x16x32_bf16 v[136:139], a[12:15], a[44:47], v[136:139]\n\t"
        "v_mfma_f32_16x16x32_bf16 v[148:151], a[12:15], a[60:63], v[148:151]\n\t"
        "v_mfma_f32_16x16x32_bf16 v[152:155], a[28:31], a[60:63], v[152:155]\n\t"
        "s_waitcnt lgkmcnt(0)\n\t"
        "v_cmp_eq_u32_e32 vcc, %[tag], v240\n\t"
        "s_nop 1\n\t"
        "s_or_b64 s[42:43], s[42:43], vcc\n\t"
        "v_readfirstlane_b32 s80, v241\n\t"
        "s_nop 0\n\t"
        "s_cmp_gt_u32 s80, s40\n\t"
        "s_cselect_b32 s41, s98, s41\n\t"
        "s_max_u32 s40, s40, s80\n\t"
        "v_readlane_b32 s84, v242, 0\n\t"
        "v_readlane_b32 s85, v242, 32\n\t"
        "v_mov_b32_e32 v243, 32\n\t"
        "s_and_b32 s80, s45, 3\n\t"
        "s_lshl_b32 s80, s80, 12\n\t"
        "s_lshr_b32 s81, s45, 2\n\t"
        "s_lshl_b32 s81, s81, 3\n\t"
        "s_add_u32 s80, s80, s81\n\t"
        "s_lshr_b32 s82, s45, 1\n\t"
        "s_xnor_b32 s81, s45, s82\n\t"
        "s_and_b32 s81, s81, 1\n\t"
        "s_lshl_b32 s81, s81, 13\n\t"
        "s_lshl_b32 s82, s82, 2\n\t"
        "s_add_u32 s81, s81, s82\n\t"
        "s_cmp_lt_i32 s45, 60\n\t"
        "s_cselect_b32 s80, s80, s81\n\t"
        "s_cselect_b32 s81, 4, 0x1000\n\t"
        "s_add_u32 s81, s81, s80\n\t"
        "s_cmp_eq_u32 s84, 31\n\t"
        "s_cselect_b32 s80, s80, s36\n\t"
        "s_cmp_eq_u32 s85, 31\n\t"
        "s_cselect_b32 s81, s81, s36\n\t"
        "s_cmp_lt_i32 s45, 0\n\t"
        "s_cselect_b32 s80, s36, s80\n\t"
        "s_cselect_b32 s81, s36, s81\n\t"
        "buffer_store_dword v243, v251, %[rd], s80 offen sc1\n\t"
        "buffer_store_dword v243, v251, %[rd], s81 offen sc1\n\t"
        "s_add_u32 s86, s37, 8\n\t"
        "s_cmp_lg_u64 s[42:43], -1\n\t"
        "s_cselect_b32 s84, 0, s36\n\t"
        "s_cmp_gt_u32 s40, s86\n\t"
        "s_cselect_b32 s85, s36, s39\n\t"
        "buffer_load_dword v240, v246, %[rd], s84 offen sc1\n\t"
        "buffer_load_dword v241, v247, %[rd], s85 offen sc1\n\t"
        "v_cvt_pk_bf16_f32 v136, 0, v136\n\t"
        "v_cvt_pk_bf16_f32 v137, 0, v137\n\t"
        "v_cvt_pk_bf16_f32 v138, 0, v138\n\t"
        "v_cvt_pk_bf16_f32 v139, 0, v139\n\t"
        "v_mul_f32_e32 v136, 0x3db504f3, v136\n\t"
        "v_mul_f32_e32 v137, 0x3db504f3, v137\n\t"
        "v_mul_f32_e32 v138, 0x3db504f3, v138\n\t"
        "v_mul_f32_e32 v139, 0x3db504f3, v139\n\t"
        "v_cvt_pk_bf16_f32 v136, 0, v136\n\t"
        "v_cvt_pk_bf16_f32 v137, 0, v137\n\t"
        "v_cvt_pk_bf16_f32 v138, 0, v138\n\t"
        "v_cvt_pk_bf16_f32 v139, 0, v139\n\t"
        "v_cndmask_b32_e64 v136, v253, v136, s[48:49]\n\t"
        "v_cndmask_b32_e64 v137, v253, v137, s[50:51]\n\t"
        "v_cndmask_b32_e64 v138, v253, v138, s[52:53]\n\t"
        "v_cndmask_b32_e64 v139, v253, v139, s[54:55]\n\t"
        "v_cvt_pk_bf16_f32 v148, 0, v148\n\t"
        "v_cvt_pk_bf16_f32 v149, 0, v149\n\t"
        "v_cvt_pk_bf16_f32 v150, 0, v150\n\t"
        "v_cvt_pk_bf16_f32 v151, 0, v151\n\t"
        "v_mul_f32_e32 v148, 0x3db504f3, v148\n\t"
        "v_mul_f32_e32 v149, 0x3db504f3, v149\n\t"
        "v_mul_f32_e32 v150, 0x3db504f3, v150\n\t"
        "v_mul_f32_e32 v151, 0x3db504f3, v151\n\t"
        "v_cvt_pk_bf16_f32 v148, 0, v148\n\t"
        "v_cvt_pk_bf16_f32 v149, 0, v149\n\t"
        "v_cvt_pk_bf16_f32 v150, 0, v150\n\t"
        "v_cvt_pk_bf16_f32 v151, 0, v151\n\t"
        "v_cvt_pk_bf16_f32 v152, 0, v152\n\t"
        "v_cvt_pk_bf16_f32 v153, 0, v153\n\t"
        "v_cvt_pk_bf16_f32 v154, 0, v154\n\t"
        "v_cvt_pk_bf16_f32 v155, 0, v155\n\t"
        "v_mul_f32_e32 v152, 0x3db504f3, v152\n\t"
        "v_mul_f32_e32 v153, 0x3db504f3, v153\n\t"
        "v_mul_f32_e32 v154, 0x3db504f3, v154\n\t"
        "v_mul_f32_e32 v155, 0x3db504f3, v155\n\t"
        "v_cvt_pk_bf16_f32 v152, 0, v152\n\t"
        "v_cvt_pk_bf16_f32 v153, 0, v153\n\t"
        "v_cvt_pk_bf16_f32 v154, 0, v154\n\t"
        "v_cvt_pk_bf16_f32 v155, 0, v155\n\t"
        "v_cndmask_b32_e64 v152, v253, v152, s[48:49]\n\t"
        "v_cndmask_b32_e64 v153, v253, v153, s[50:51]\n\t"
        "v_cndmask_b32_e64 v154, v253, v154, s[52:53]\n\t"
        "v_cndmask_b32_e64 v155, v253, v155, s[54:55]\n\t"
        "v_max3_f32 v172, v136, v137, v253\n\t"
        "v_max3_f32 v172, v172, v138, v139\n\t"
        "v_mov_b32_e32 v140, v253\n\t"
        "v_max_f32_e32 v174, v148, v149\n\t"
        "v_max3_f32 v174, v174, v150, v151\n\t"
        "v_max3_f32 v174, v174, v152, v153\n\t"
        "v_max3_f32 v174, v174, v154, v155\n\t"
        "s_cmp_eq_u32 s39, 0\n\t"
        "s_cbranch_scc1 .La11%=\n\t"
        "s_waitcnt vmcnt(0)\n\t"
        "s_lshl_b32 s80, s44, 7\n\t"
        "s_cmp_lt_i32 s44, 0\n\t"
        "s_cselect_b32 s80, s36, s80\n\t"
        "v_mov_b32_e32 v242, 1\n\t"
        "buffer_atomic_add v242, v250, %[rd], s80 offen sc0 sc1\n\t"
        "s_waitcnt vmcnt(0)\n\t"
        "v_readlane_b32 s84, v242, 0\n\t"
        "v_readlane_b32 s85, v242, 32\n\t"
        "v_mov_b32_e32 v243, 32\n\t"
        "s_and_b32 s80, s44, 3\n\t"
        "s_lshl_b32 s80, s80, 12\n\t"
        "s_lshr_b32 s81, s44, 2\n\t"
        "s_lshl_b32 s81, s81, 3\n\t"
        "s_add_u32 s80, s80, s81\n\t"
        "s_lshr_b32 s82, s44, 1\n\t"
        "s_xnor_b32 s81, s44, s82\n\t"
        "s_and_b32 s81, s81, 1\n\t"
        "s_lshl_b32 s81, s81, 13\n\t"
        "s_lshl_b32 s82, s82, 2\n\t"
        "s_add_u32 s81, s81, s82\n\t"
        "s_cmp_lt_i32 s44, 60\n\t"
        "s_cselect_b32 s80, s80, s81\n\t"
        "s_cselect_b32 s81, 4, 0x1000\n\t"
        "s_add_u32 s81, s81, s80\n\t"
        "s_cmp_eq_u32 s84, 31\n\t"
        "s_cselect_b32 s80, s80, s36\n\t"
        "s_cmp_eq_u32 s85, 31\n\t"
        "s_cselect_b32 s81, s81, s36\n\t"
        "s_cmp_lt_i32 s44, 0\n\t"
        "s_cselect_b32 s80, s36, s80\n\t"
        "s_cselect_b32 s81, s36, s81\n\t"
        "buffer_store_dword v243, v251, %[rd], s80 offen sc1\n\t"
        "buffer_store_dword v243, v251, %[rd], s81 offen sc1\n\t"
        "s_mov_b32 s44, -1\n\t"
        "s_mov_b32 s97, 0\n\t"
        "s_min_u32 s87, s37, 62\n\t"
        ".La10%=:\n\t"
        "s_cmp_gt_u32 s40, s87\n\t"
        "s_cbranch_scc1 .La10%=x\n\t"
        "s_memrealtime s[98:99]\n\t"
        "s_add_u32 s97, s97, 1\n\t"
        "s_cmp_lt_u32 s97, 0x400000\n\t"
        "s_cbranch_scc0 .Lgu%=\n\t"
        "s_sub_u32 s80, s87, s40\n\t"
        "s_cmp_ge_u32 s87, %c[efrom]\n\t"
        "s_subb_u32 s80, s80, 0\n\t"
        "s_mul_i32 s80, s80, %c[smin]\n\t"
        "s_waitcnt lgkmcnt(0)\n\t"
        "s_sub_u32 s81, s98, s41\n\t"
        "s_cmp_ge_i32 s81, s80\n\t"
        "s_cbranch_scc1 .La10%=p\n\t"
        "s_sleep 16\n\t"
        "s_branch .La10%=\n\t"
        ".La10%=p:\n\t"
        "buffer_load_dword v241, v247, %[rd], 0 offen sc1\n\t"
        "s_waitcnt vmcnt(0)\n\t"
        "v_readfirstlane_b32 s80, v241\n\t"
        "s_nop 0\n\t"
        "s_cmp_gt_u32 s80, s40\n\t"
        "s_cselect_b32 s41, s98, s41\n\t"
        "s_max_u32 s40, s40, s80\n\t"
        "s_cmp_gt_u32 s40, s87\n\t"
        "s_cbranch_scc1 .La10%=x\n\t"
        "s_sleep %c[pslp]\n\t"
        "s_branch .La10%=\n\t"
        ".La10%=x:\n\t"
        "s_lshl_b32 s81, s37, 17\n\t"
        "s_add_u32 s81, s81, %[km]\n\t"
        "buffer_load_dwordx4 a[96:99], v244, %[rd], s81 offen offset:0 sc1\n\t"
        "buffer_load_dwordx4 a[100:103], v244, %[rd], s81 offen offset:1024 sc1\n\t"
        "buffer_load_dwordx2 a[104:105], v245, %[rd], s81 offen offset:2048 sc1\n\t"
        "buffer_load_dwordx2 a[106:107], v245, %[rd], s81 offen offset:2304 sc1\n\t"
        "buffer_load_dwordx2 a[108:109], v245, %[rd], s81 offen offset:2560 sc1\n\t"
        "buffer_load_dwordx2 a[110:111], v245, %[rd], s81 offen offset:2816 sc1\n\t"
        "buffer_load_dwordx2 a[112:113], v245, %[rd], s81 offen offset:3072 sc1\n\t"
        "buffer_load_dwordx2 a[114:115], v245, %[rd], s81 offen offset:3328 sc1\n\t"
        "buffer_load_dwordx2 a[116:117], v245, %[rd], s81 offen offset:3584 sc1\n\t"
        "buffer_load_dwordx2 a[118:119], v245, %[rd], s81 offen offset:3840 sc1\n\t"
        "s_waitcnt vmcnt(0)\n\t"
        ".La11%=:\n\t"
        "ds_write_b128 v248, a[96:99] offset:0\n\t"
        "ds_write_b128 v248, a[100:103] offset:1024\n\t"
        "ds_read_b64_tr_b16 v[120:121], v249 offset:0\n\t"
        "ds_read_b64_tr_b16 v[122:123], v249 offset:64\n\t"
        "ds_read_b64_tr_b16 v[124:125], v249 offset:512\n\t"
        "ds_read_b64_tr_b16 v[126:127], v249 offset:576\n\t"
        "ds_read_b64_tr_b16 v[128:129], v249 offset:1024\n\t"
        "ds_read_b64_tr_b16 v[130:131], v249 offset:1088\n\t"
        "ds_read_b64_tr_b16 v[132:133], v249 offset:1536\n\t"
        "ds_read_b64_tr_b16 v[134:135], v249 offset:1600\n\t"
        "s_waitcnt lgkmcnt(6)\n\t"
        "v_mfma_f32_16x16x32_bf16 v[144:147], v[120:123], a[32:35], 0\n\t"
        "v_mfma_f32_16x16x32_bf16 v[156:159], v[120:123], a[48:51], 0\n\t"
        "s_waitcnt lgkmcnt(4)\n\t"
        "v_mfma_f32_16x16x32_bf16 v[144:147], v[124:127], a[36:39], v[144:147]\n\t"
        "v_mfma_f32_16x16x32_bf16 v[156:159], v[124:127], a[52:55], v[156:159]\n\t"
        "s_waitcnt lgkmcnt(2)\n\t"
        "v_mfma_f32_16x16x32_bf16 v[144:147], v[128:131], a[40:43], v[144:147]\n\t"
        "v_mfma_f32_16x16x32_bf16 v[156:159], v[128:131], a[56:59], v[156:159]\n\t"
        "s_waitcnt lgkmcnt(0)\n\t"
        "v_mfma_f32_16x16x32_bf16 v[144:147], v[132:135], a[44:47], v[144:147]\n\t"
        "v_mfma_f32_16x16x32_bf16 v[156:159], v[132:135], a[60:63], v[156:159]\n\t"
        "s_nop 1\n\t"
        "v_mov_b32_e32 v252, v252\n\t"
        "s_nop 1\n\t"
        "v_mov_b32_e32 v252, v252\n\t"
        "s_nop 0\n\t"
        "v_cvt_pk_bf16_f32 v144, 0, v144\n\t"
        "v_cvt_pk_bf16_f32 v145, 0, v145\n\t"
        "v_cvt_pk_bf16_f32 v146, 0, v146\n\t"
        "v_cvt_pk_bf16_f32 v147, 0, v147\n\t"
        "v_mul_f32_e32 v144, 0x3db504f3, v144\n\t"
        "v_mul_f32_e32 v145, 0x3db504f3, v145\n\t"
        "v_mul_f32_e32 v146, 0x3db504f3, v146\n\t"
        "v_mul_f32_e32 v147, 0x3db504f3, v147\n\t"
        "v_cvt_pk_bf16_f32 v144, 0, v144\n\t"
        "v_cvt_pk_bf16_f32 v145, 0, v145\n\t"
        "v_cvt_pk_bf16_f32 v146, 0, v146\n\t"
        "v_cvt_pk_bf16_f32 v147, 0, v147\n\t"
        "v_cndmask_b32_e64 v144, v144, v254, s[56:57]\n\t"
        "v_cndmask_b32_e64 v145, v145, v254, s[56:57]\n\t"
        "v_cndmask_b32_e64 v146, v146, v254, s[56:57]\n\t"
        "v_cndmask_b32_e64 v147, v147, v254, s[56:57]\n\t"
        "v_max3_f32 v172, v172, v144, v145\n\t"
        "v_max3_f32 v173, v172, v146, v147\n\t"
        "v_max3_f32 v172, v172, v146, v147\n\t"
        "s_nop 1\n\t"
        "v_permlane16_swap_b32_e32 v172, v173\n\t"
        "v_max_f32_e32 v172, v172, v173\n\t"
        "v_mov_b32_e32 v173, v172\n\t"
        "s_nop 1\n\t"
        "v_permlane32_swap_b32_e32 v172, v173\n\t"
        "v_max_f32_e32 v172, v172, v173\n\t"
        "v_sub_f32_e32 v136, v136, v172\n\t"
        "v_sub_f32_e32 v137, v137, v172\n\t"
        "v_sub_f32_e32 v138, v138, v172\n\t"
        "v_sub_f32_e32 v139, v139, v172\n\t"
        "v_sub_f32_e32 v140, v140, v172\n\t"
        "v_sub_f32_e32 v144, v144, v172\n\t"
        "v_sub_f32_e32 v145, v145, v172\n\t"
        "v_sub_f32_e32 v146, v146, v172\n\t"
        "v_sub_f32_e32 v147, v147, v172\n\t"
        "v_mul_f32_e32 v136, 0x3fb8aa3b, v136\n\t"
        "v_mul_f32_e32 v137, 0x3fb8aa3b, v137\n\t"
        "v_mul_f32_e32 v138, 0x3fb8aa3b, v138\n\t"
        "v_mul_f32_e32 v139, 0x3fb8aa3b, v139\n\t"
        "v_mul_f32_e32 v140, 0x3fb8aa3b, v140\n\t"
        "v_mul_f32_e32 v144, 0x3fb8aa3b, v144\n\t"
        "v_mul_f32_e32 v145, 0x3fb8aa3b, v145\n\t"
        "v_mul_f32_e32 v146, 0x3fb8aa3b, v146\n\t"
        "v_mul_f32_e32 v147, 0x3fb8aa3b, v147\n\t"
        "v_exp_f32_e32 v136, v136\n\t"
        "v_exp_f32_e32 v137, v137\n\t"
        "v_exp_f32_e32 v138, v138\n\t"
        "v_exp_f32_e32 v139, v139\n\t"
        "v_exp_f32_e32 v140, v140\n\t"
        "v_exp_f32_e32 v144, v144\n\t"
        "v_exp_f32_e32 v145, v145\n\t"
        "v_exp_f32_e32 v146, v146\n\t"
        "v_exp_f32_e32 v147, v147\n\t"
        "v_mul_f32_e32 v172, 4.0, v140\n\t"
        "v_add_f32_e32 v172, v172, v136\n\t"
        "v_add_f32_e32 v172, v172, v137\n\t"
        "v_add_f32_e32 v172, v172, v138\n\t"
        "v_add_f32_e32 v172, v172, v139\n\t"
        "v_add_f32_e32 v172, v172, v144\n\t"
        "v_add_f32_e32 v172, v172, v145\n\t"
        "v_add_f32_e32 v172, v172, v146\n\t"
        "v_add_f32_e32 v172, v172, v147\n\t"
        "v_mov_b32_e32 v173, v172\n\t"
        "s_nop 1\n\t"
        "v_permlane16_swap_b32_e32 v172, v173\n\t"
        "v_add_f32_e32 v172, v172, v173\n\t"
        "v_mov_b32_e32 v173, v172\n\t"
        "s_nop 1\n\t"
        "v_permlane32_swap_b32_e32 v172, v173\n\t"
        "v_add_f32_e32 v172, v172, v173\n\t"
        "v_rcp_f32_e32 v172, v172\n\t"
        "s_nop 0\n\t"
        "v_mul_f32_e32 v136, v136, v172\n\t"
        "v_mul_f32_e32 v137, v137, v172\n\t"
        "v_mul_f32_e32 v138, v138, v172\n\t"
        "v_mul_f32_e32 v139, v139, v172\n\t"
        "v_mul_f32_e32 v140, v140, v172\n\t"
        "v_mul_f32_e32 v144, v144, v172\n\t"
        "v_mul_f32_e32 v145, v145, v172\n\t"
        "v_mul_f32_e32 v146, v146, v172\n\t"
        "v_mul_f32_e32 v147, v147, v172\n\t"
        "v_cvt_pk_bf16_f32 v160, v136, v137\n\t"
        "v_cvt_pk_bf16_f32 v161, v138, v139\n\t"
        "v_cvt_pk_bf16_f32 v162, v140, v140\n\t"
        "v_cvt_pk_bf16_f32 v163, v140, v140\n\t"
        "v_cvt_pk_bf16_f32 v164, v144, v145\n\t"
        "v_cvt_pk_bf16_f32 v165, v146, v147\n\t"
        "v_mfma_f32_16x16x32_bf16 v[176:179], a[64:67], v[160:163], 0\n\t"
        "v_cvt_pk_bf16_f32 v156, 0, v156\n\t"
        "v_cvt_pk_bf16_f32 v157, 0, v157\n\t"
        "v_cvt_pk_bf16_f32 v158, 0, v158\n\t"
        "v_mfma_f32_16x16x32_bf16 v[180:183], a[68:71], v[160:163], 0\n\t"
        "v_cvt_pk_bf16_f32 v159, 0, v159\n\t"
        "v_mul_f32_e32 v156, 0x3db504f3, v156\n\t"
        "v_mul_f32_e32 v157, 0x3db504f3, v157\n\t"
        "v_mfma_f32_16x16x32_bf16 v[184:187], a[72:75], v[160:163], 0\n\t"
        "v_mul_f32_e32 v158, 0x3db504f3, v158\n\t"
        "v_mul_f32_e32 v159, 0x3db504f3, v159\n\t"
        "v_cvt_pk_bf16_f32 v156, 0, v156\n\t"
        "v_mfma_f32_16x16x32_bf16 v[188:191], a[76:79], v[160:163], 0\n\t"
        "v_cvt_pk_bf16_f32 v157, 0, v157\n\t"
        "v_cvt_pk_bf16_f32 v158, 0, v158\n\t"
        "v_cvt_pk_bf16_f32 v159, 0, v159\n\t"
        "v_mfma_f32_16x16x32_bf16 v[192:195], a[80:83], v[160:163], 0\n\t"
        "v_cndmask_b32_e64 v156, v156, v254, s[56:57]\n\t"
        "v_cndmask_b32_e64 v157, v157, v254, s[56:57]\n\t"
        "v_cndmask_b32_e64 v158, v158, v254, s[56:57]\n\t"
        "v_mfma_f32_16x16x32_bf16 v[196:199], a[84:87], v[160:163], 0\n\t"
        "v_cndmask_b32_e64 v159, v159, v254, s[56:57]\n\t"
        "v_max3_f32 v174, v174, v156, v157\n\t"
        "v_max3_f32 v175, v174, v158, v159\n\t"
        "v_mfma_f32_16x16x32_bf16 v[200:203], a[88:91], v[160:163], 0\n\t"
        "v_max3_f32 v174, v174, v158, v159\n\t"
        "s_nop 1\n\t"
        "v_permlane16_swap_b32_e32 v174, v175\n\t"
        "v_max_f32_e32 v174, v174, v175\n\t"
        "v_mfma_f32_16x16x32_bf16 v[204:207], a[92:95], v[160:163], 0\n\t"
        "v_mov_b32_e32 v175, v174\n\t"
        "s_nop 1\n\t"
        "v_permlane32_swap_b32_e32 v174, v175\n\t"
        "v_max_f32_e32 v174, v174, v175\n\t"
        "v_mfma_f32_16x16x16_bf16 v[176:179], a[104:105], v[164:165], v[176:179]\n\t"
        "v_sub_f32_e32 v148, v148, v174\n\t"
        "v_sub_f32_e32 v149, v149, v174\n\t"
        "v_sub_f32_e32 v150, v150, v174\n\t"
        "v_mfma_f32_16x16x16_bf16 v[180:183], a[106:107], v[164:165], v[180:183]\n\t"
        "v_sub_f32_e32 v151, v151, v174\n\t"
        "v_sub_f32_e32 v152, v152, v174\n\t"
        "v_sub_f32_e32 v153, v153, v174\n\t"
        "v_mfma_f32_16x16x16_bf16 v[184:187], a[108:109], v[164:165], v[184:187]\n\t"
        "v_sub_f32_e32 v154, v154, v174\n\t"
        "v_sub_f32_e32 v155, v155, v174\n\t"
        "v_sub_f32_e32 v156, v156, v174\n\t"
        "v_mfma_f32_16x16x16_bf16 v[188:191], a[110:111], v[164:165], v[188:191]\n\t"
        "v_sub_f32_e32 v157, v157, v174\n\t"
        "v_sub_f32_e32 v158, v158, v174\n\t"
        "v_sub_f32_e32 v159, v159, v174\n\t"
        "v_mfma_f32_16x16x16_bf16 v[192:195], a[112:113], v[164:165], v[192:195]\n\t"
        "v_mul_f32_e32 v148, 0x3fb8aa3b, v148\n\t"
        "v_mul_f32_e32 v149, 0x3fb8aa3b, v149\n\t"
        "v_mul_f32_e32 v150, 0x3fb8aa3b, v150\n\t"
        "v_mfma_f32_16x16x16_bf16 v[196:199], a[114:115], v[164:165], v[196:199]\n\t"
        "v_mul_f32_e32 v151, 0x3fb8aa3b, v151\n\t"
        "v_mul_f32_e32 v152, 0x3fb8aa3b, v152\n\t"
        "v_mul_f32_e32 v153, 0x3fb8aa3b, v153\n\t"
        "v_mfma_f32_16x16x16_bf16 v[200:203], a[116:117], v[164:165], v[200:203]\n\t"
        "v_mul_f32_e32 v154, 0x3fb8aa3b, v154\n\t"
        "v_mul_f32_e32 v155, 0x3fb8aa3b, v155\n\t"
        "v_mul_f32_e32 v156, 0x3fb8aa3b, v156\n\t"
        "v_mfma_f32_16x16x16_bf16 v[204:207], a[118:119], v[164:165], v[204:207]\n\t"
        "v_mul_f32_e32 v157, 0x3fb8aa3b, v157\n\t"
        "v_mul_f32_e32 v158, 0x3fb8aa3b, v158\n\t"
        "v_mul_f32_e32 v159, 0x3fb8aa3b, v159\n\t"
        "v_exp_f32_e32 v148, v148\n\t"
        "v_exp_f32_e32 v149, v149\n\t"
        "v_exp_f32_e32 v150, v150\n\t"
        "v_exp_f32_e32 v151, v151\n\t"
        "v_exp_f32_e32 v152, v152\n\t"
        "v_exp_f32_e32 v153, v153\n\t"
        "v_exp_f32_e32 v154, v154\n\t"
        "v_exp_f32_e32 v155, v155\n\t"
        "v_exp_f32_e32 v156, v156\n\t"
        "v_exp_f32_e32 v157, v157\n\t"
        "v_exp_f32_e32 v158, v158\n\t"
        "v_exp_f32_e32 v159, v159\n\t"
        "v_add_f32_e32 v174, v148, v149\n\t"
        "v_add_f32_e32 v174, v174, v150\n\t"
        "v_add_f32_e32 v174, v174, v151\n\t"
        "v_add_f32_e32 v174, v174, v152\n\t"
        "v_add_f32_e32 v174, v174, v153\n\t"
        "v_add_f32_e32 v174, v174, v154\n\t"
        "v_add_f32_e32 v174, v174, v155\n\t"
        "v_add_f32_e32 v174, v174, v156\n\t"
        "v_add_f32_e32 v174, v174, v157\n\t"
        "v_add_f32_e32 v174, v174, v158\n\t"
        "v_add_f32_e32 v174, v174, v159\n\t"
        "v_mov_b32_e32 v175, v174\n\t"
        "s_nop 1\n\t"
        "v_permlane16_swap_b32_e32 v174, v175\n\t"
        "v_add_f32_e32 v174, v174, v175\n\t"
        "v_mov_b32_e32 v175, v174\n\t"
        "s_nop 1\n\t"
        "v_permlane32_swap_b32_e32 v174, v175\n\t"
        "v_add_f32_e32 v174, v174, v175\n\t"
        "v_rcp_f32_e32 v174, v174\n\t"
        "s_nop 0\n\t"
        "v_mul_f32_e32 v148, v148, v174\n\t"
        "v_mul_f32_e32 v149, v149, v174\n\t"
        "v_mul_f32_e32 v150, v150, v174\n\t"
        "v_mul_f32_e32 v151, v151, v174\n\t"
        "v_mul_f32_e32 v152, v152, v174\n\t"
        "v_mul_f32_e32 v153, v153, v174\n\t"
        "v_mul_f32_e32 v154, v154, v174\n\t"
        "v_mul_f32_e32 v155, v155, v174\n\t"
        "v_mul_f32_e32 v156, v156, v174\n\t"
        "v_mul_f32_e32 v157, v157, v174\n\t"
        "v_mul_f32_e32 v158, v158, v174\n\t"
        "v_mul_f32_e32 v159, v159, v174\n\t"
        "v_cvt_pk_bf16_f32 v166, v148, v149\n\t"
        "v_cvt_pk_bf16_f32 v167, v150, v151\n\t"
        "v_cvt_pk_bf16_f32 v168, v152, v153\n\t"
        "v_cvt_pk_bf16_f32 v169, v154, v155\n\t"
        "v_cvt_pk_bf16_f32 v170, v156, v157\n\t"
        "v_cvt_pk_bf16_f32 v171, v158, v159\n\t"
        "s_waitcnt vmcnt(38)\n\t"
        "s_lshl_b32 s80, s44, 7\n\t"
        "s_cmp_lt_i32 s44, 0\n\t"
        "s_cselect_b32 s80, s36, s80\n\t"
        "v_mov_b32_e32 v242, 1\n\t"
        "buffer_atomic_add v242, v250, %[rd], s80 offen sc0 sc1\n\t"
        "s_lshl_b32 s86, s37, 18\n\t"
        "s_add_u32 s86, s86, %[qx]\n\t"
        "s_add_u32 s87, s86, 0x1000\n\t"
        "v_mfma_f32_16x16x32_bf16 v[208:211], a[64:67], v[166:169], 0\n\t"
        "v_cvt_pk_bf16_f32 v176, v176, v177\n\t"
        "v_cvt_pk_bf16_f32 v177, v178, v179\n\t"
        "v_mfma_f32_16x16x32_bf16 v[212:215], a[68:71], v[166:169], 0\n\t"
        "v_cvt_pk_bf16_f32 v178, v180, v181\n\t"
        "v_cvt_pk_bf16_f32 v179, v182, v183\n\t"
        "v_mfma_f32_16x16x32_bf16 v[216:219], a[72:75], v[166:169], 0\n\t"
        "buffer_store_dwordx4 v[176:179], v244, %[rd], s86 offen offset:0 sc1\n\t"
        "v_cvt_pk_bf16_f32 v184, v184, v185\n\t"
        "v_mfma_f32_16x16x32_bf16 v[220:223], a[76:79], v[166:169], 0\n\t"
        "v_cvt_pk_bf16_f32 v185, v186, v187\n\t"
        "v_cvt_pk_bf16_f32 v186, v188, v189\n\t"
        "v_mfma_f32_16x16x32_bf16 v[224:227], a[80:83], v[166:169], 0\n\t"
        "v_cvt_pk_bf16_f32 v187, v190, v191\n\t"
        "buffer_store_dwordx4 v[184:187], v244, %[rd], s86 offen offset:1024 sc1\n\t"
        "v_mfma_f32_16x16x32_bf16 v[228:231], a[84:87], v[166:169], 0\n\t"
        "v_cvt_pk_bf16_f32 v192, v192, v193\n\t"
        "v_cvt_pk_bf16_f32 v193, v194, v195\n\t"
        "v_mfma_f32_16x16x32_bf16 v[232:235], a[88:91], v[166:169], 0\n\t"
        "v_cvt_pk_bf16_f32 v194, v196, v197\n\t"
        "v_cvt_pk_bf16_f32 v195, v198, v199\n\t"
        "v_mfma_f32_16x16x32_bf16 v[236:239], a[92:95], v[166:169], 0\n\t"
        "buffer_store_dwordx4 v[192:195], v244, %[rd], s86 offen offset:2048 sc1\n\t"
        "v_cvt_pk_bf16_f32 v200, v200, v201\n\t"
        "v_mfma_f32_16x16x16_bf16 v[208:211], a[104:105], v[170:171], v[208:211]\n\t"
        "v_cvt_pk_bf16_f32 v201, v202, v203\n\t"
        "v_cvt_pk_bf16_f32 v202, v204, v205\n\t"
        "v_mfma_f32_16x16x16_bf16 v[212:215], a[106:107], v[170:171], v[212:215]\n\t"
        "v_cvt_pk_bf16_f32 v203, v206, v207\n\t"
        "buffer_store_dwordx4 v[200:203], v244, %[rd], s86 offen offset:3072 sc1\n\t"
        "v_mfma_f32_16x16x16_bf16 v[216:219], a[108:109], v[170:171], v[216:219]\n\t"
        "v_mfma_f32_16x16x16_bf16 v[220:223], a[110:111], v[170:171], v[220:223]\n\t"
        "v_mfma_f32_16x16x16_bf16 v[224:227], a[112:113], v[170:171], v[224:227]\n\t"
        "v_mfma_f32_16x16x16_bf16 v[228:231], a[114:115], v[170:171], v[228:231]\n\t"
        "v_mfma_f32_16x16x16_bf16 v[232:235], a[116:117], v[170:171], v[232:235]\n\t"
        "v_mfma_f32_16x16x16_bf16 v[236:239], a[118:119], v[170:171], v[236:239]\n\t"
        "v_cvt_pk_bf16_f32 v208, v208, v209\n\t"
        "v_cvt_pk_bf16_f32 v209, v210, v211\n\t"
        "v_cvt_pk_bf16_f32 v210, v212, v213\n\t"
        "v_cvt_pk_bf16_f32 v211, v214, v215\n\t"
        "buffer_store_dwordx4 v[208:211], v244, %[rd], s87 offen offset:0 sc1\n\t"
        "v_cvt_pk_bf16_f32 v216, v216, v217\n\t"
        "v_cvt_pk_bf16_f32 v217, v218, v219\n\t"
        "v_cvt_pk_bf16_f32 v218, v220, v221\n\t"
        "v_cvt_pk_bf16_f32 v219, v222, v223\n\t"
        "buffer_store_dwordx4 v[216:219], v244, %[rd], s87 offen offset:1024 sc1\n\t"
        "v_cvt_pk_bf16_f32 v224, v224, v225\n\t"
        "v_cvt_pk_bf16_f32 v225, v226, v227\n\t"
        "v_cvt_pk_bf16_f32 v226, v228, v229\n\t"
        "v_cvt_pk_bf16_f32 v227, v230, v231\n\t"
        "buffer_store_dwordx4 v[224:227], v244, %[rd], s87 offen offset:2048 sc1\n\t"
        "v_cvt_pk_bf16_f32 v232, v232, v233\n\t"
        "v_cvt_pk_bf16_f32 v233, v234, v235\n\t"
        "v_cvt_pk_bf16_f32 v234, v236, v237\n\t"
        "v_cvt_pk_bf16_f32 v235, v238, v239\n\t"
        "buffer_store_dwordx4 v[232:235], v244, %[rd], s87 offen offset:3072 sc1\n\t"
        "s_mov_b32 s45, s44\n\t"
        "s_mov_b32 s44, s37\n\t"
        "s_add_u32 s80, s37, 4\n\t"
        "s_cmp_gt_u32 s40, s80\n\t"
        "s_cselect_b32 s80, s46, 1\n\t"
        "s_cmp_eq_u32 s80, 0\n\t"
        "s_cbranch_scc1 .La12%=\n\t"
        "s_waitcnt vmcnt(0)\n\t"
        "v_readlane_b32 s84, v242, 0\n\t"
        "v_readlane_b32 s85, v242, 32\n\t"
        "v_mov_b32_e32 v243, 32\n\t"
        "s_and_b32 s80, s45, 3\n\t"
        "s_lshl_b32 s80, s80, 12\n\t"
        "s_lshr_b32 s81, s45, 2\n\t"
        "s_lshl_b32 s81, s81, 3\n\t"
        "s_add_u32 s80, s80, s81\n\t"
        "s_lshr_b32 s82, s45, 1\n\t"
        "s_xnor_b32 s81, s45, s82\n\t"
        "s_and_b32 s81, s81, 1\n\t"
        "s_lshl_b32 s81, s81, 13\n\t"
        "s_lshl_b32 s82, s82, 2\n\t"
        "s_add_u32 s81, s81, s82\n\t"
        "s_cmp_lt_i32 s45, 60\n\t"
        "s_cselect_b32 s80, s80, s81\n\t"
        "s_cselect_b32 s81, 4, 0x1000\n\t"
        "s_add_u32 s81, s81, s80\n\t"
        "s_cmp_eq_u32 s84, 31\n\t"
        "s_cselect_b32 s80, s80, s36\n\t"
        "s_cmp_eq_u32 s85, 31\n\t"
        "s_cselect_b32 s81, s81, s36\n\t"
        "s_cmp_lt_i32 s45, 0\n\t"
        "s_cselect_b32 s80, s36, s80\n\t"
        "s_cselect_b32 s81, s36, s81\n\t"
        "buffer_store_dword v243, v251, %[rd], s80 offen sc1\n\t"
        "buffer_store_dword v243, v251, %[rd], s81 offen sc1\n\t"
        "s_lshl_b32 s80, s44, 7\n\t"
        "s_cmp_lt_i32 s44, 0\n\t"
        "s_cselect_b32 s80, s36, s80\n\t"
        "v_mov_b32_e32 v242, 1\n\t"
        "buffer_atomic_add v242, v250, %[rd], s80 offen sc0 sc1\n\t"
        "s_waitcnt vmcnt(0)\n\t"
        "v_readlane_b32 s84, v242, 0\n\t"
        "v_readlane_b32 s85, v242, 32\n\t"
        "v_mov_b32_e32 v243, 32\n\t"
        "s_and_b32 s80, s44, 3\n\t"
        "s_lshl_b32 s80, s80, 12\n\t"
        "s_lshr_b32 s81, s44, 2\n\t"
        "s_lshl_b32 s81, s81, 3\n\t"
        "s_add_u32 s80, s80, s81\n\t"
        "s_lshr_b32 s82, s44, 1\n\t"
        "s_xnor_b32 s81, s44, s82\n\t"
        "s_and_b32 s81, s81, 1\n\t"
        "s_lshl_b32 s81, s81, 13\n\t"
        "s_lshl_b32 s82, s82, 2\n\t"
        "s_add_u32 s81, s81, s82\n\t"
        "s_cmp_lt_i32 s44, 60\n\t"
        "s_cselect_b32 s80, s80, s81\n\t"
        "s_cselect_b32 s81, 4, 0x1000\n\t"
        "s_add_u32 s81, s81, s80\n\t"
        "s_cmp_eq_u32 s84, 31\n\t"
        "s_cselect_b32 s80, s80, s36\n\t"
        "s_cmp_eq_u32 s85, 31\n\t"
        "s_cselect_b32 s81, s81, s36\n\t"
        "s_cmp_lt_i32 s44, 0\n\t"
        "s_cselect_b32 s80, s36, s80\n\t"
        "s_cselect_b32 s81, s36, s81\n\t"
        "buffer_store_dword v243, v251, %[rd], s80 offen sc1\n\t"
        "buffer_store_dword v243, v251, %[rd], s81 offen sc1\n\t"
        "s_mov_b32 s44, -1\n\t"
        "s_mov_b32 s45, -1\n\t"
        ".La12%=:\n\t"
        "s_mov_b32 s38, s46\n\t"
        "s_mov_b32 s39, s47\n\t"
        "s_add_u32 s37, s37, 4\n\t"
        "s_cmp_eq_u32 s38, 0\n\t"
        "s_cbranch_scc1 .La14%=\n\t"
        "s_mov_b32 s97, 0\n\t"
        ".La13%=:\n\t"
        "buffer_load_dword v240, v246, %[rd], 0 offen sc1\n\t"
        "buffer_load_dword v241, v247, %[rd], 0 offen sc1\n\t"
        "s_add_u32 s97, s97, 1\n\t"
        "s_cmp_lt_u32 s97, 0x200000\n\t"
        "s_cbranch_scc0 .Lgu%=\n\t"
        "s_memrealtime s[98:99]\n\t"
        "s_waitcnt vmcnt(0) lgkmcnt(0)\n\t"
        "v_cmp_eq_u32_e32 vcc, %[tag], v240\n\t"
        "s_nop 1\n\t"
        "s_or_b64 s[42:43], s[42:43], vcc\n\t"
        "v_readfirstlane_b32 s80, v241\n\t"
        "s_nop 0\n\t"
        "s_cmp_gt_u32 s80, s40\n\t"
        "s_cselect_b32 s41, s98, s41\n\t"
        "s_max_u32 s40, s40, s80\n\t"
        "s_and_b32 s80, s37, 60\n\t"
        "s_lshr_b64 s[80:81], s[42:43], s80\n\t"
        "s_andn2_b32 s80, 15, s80\n\t"
        "s_cbranch_scc0 .La13%=x\n\t"
        "s_sleep 16\n\t"
        "s_branch .La13%=\n\t"
        ".La13%=x:\n\t"
        "s_cmp_gt_u32 s40, s37\n\t"
        "s_cselect_b32 s39, 0, s36\n\t"
        "s_lshl_b32 s80, s37, 18\n\t"
        "s_or_b32 s80, s80, 0\n\t"
        "s_lshl_b32 s81, s37, 17\n\t"
        "s_add_u32 s81, s81, %[km]\n\t"
        "s_or_b32 s81, s81, s39\n\t"
        "s_add_u32 s82, s80, %[kx]\n\t"
        "s_add_u32 s83, s82, 0x1000\n\t"
        "buffer_load_dwordx4 v[0:3], v244, %[rd], s82 offen offset:0 sc1\n\t"
        "buffer_load_dwordx4 v[4:7], v244, %[rd], s82 offen offset:1024 sc1\n\t"
        "buffer_load_dwordx4 v[8:11], v244, %[rd], s82 offen offset:2048 sc1\n\t"
        "buffer_load_dwordx4 v[12:15], v244, %[rd], s82 offen offset:3072 sc1\n\t"
        "buffer_load_dwordx4 v[16:19], v244, %[rd], s83 offen offset:0 sc1\n\t"
        "buffer_load_dwordx4 v[20:23], v244, %[rd], s83 offen offset:1024 sc1\n\t"
        "buffer_load_dwordx4 v[24:27], v244, %[rd], s83 offen offset:2048 sc1\n\t"
        "buffer_load_dwordx4 v[28:31], v244, %[rd], s83 offen offset:3072 sc1\n\t"
        "s_cmp_eq_u64 s[42:43], -1\n\t"
        "s_cbranch_scc1 .La15%=\n\t"
        "s_sleep %c[aslp]\n\t"
        ".La15%=:\n\t"
        "s_sleep %c[bslp]\n\t"
        "s_add_u32 s82, s80, %[qx]\n\t"
        "s_add_u32 s83, s82, 0x1000\n\t"
        "buffer_load_dwordx4 v[32:35], v244, %[rd], s82 offen offset:0 sc1\n\t"
        "buffer_load_dwordx4 v[36:39], v244, %[rd], s82 offen offset:1024 sc1\n\t"
        "buffer_load_dwordx4 v[40:43], v244, %[rd], s82 offen offset:2048 sc1\n\t"
        "buffer_load_dwordx4 v[44:47], v244, %[rd], s82 offen offset:3072 sc1\n\t"
        "buffer_load_dwordx4 v[48:51], v244, %[rd], s83 offen offset:0 sc1\n\t"
        "buffer_load_dwordx4 v[52:55], v244, %[rd], s83 offen offset:1024 sc1\n\t"
        "buffer_load_dwordx4 v[56:59], v244, %[rd], s83 offen offset:2048 sc1\n\t"
        "buffer_load_dwordx4 v[60:63], v244, %[rd], s83 offen offset:3072 sc1\n\t"
        "s_cmp_eq_u64 s[42:43], -1\n\t"
        "s_cbranch_scc1 .La16%=\n\t"
        "s_sleep %c[aslp]\n\t"
        ".La16%=:\n\t"
        "s_sleep %c[bslp]\n\t"
        "buffer_load_dwordx4 v[96:99], v244, %[rd], s81 offen offset:0 sc1\n\t"
        "buffer_load_dwordx4 v[100:103], v244, %[rd], s81 offen offset:1024 sc1\n\t"
        "buffer_load_dwordx2 v[104:105], v245, %[rd], s81 offen offset:2048 sc1\n\t"
        "buffer_load_dwordx2 v[106:107], v245, %[rd], s81 offen offset:2304 sc1\n\t"
        "buffer_load_dwordx2 v[108:109], v245, %[rd], s81 offen offset:2560 sc1\n\t"
        "buffer_load_dwordx2 v[110:111], v245, %[rd], s81 offen offset:2816 sc1\n\t"
        "buffer_load_dwordx2 v[112:113], v245, %[rd], s81 offen offset:3072 sc1\n\t"
        "buffer_load_dwordx2 v[114:115], v245, %[rd], s81 offen offset:3328 sc1\n\t"
        "buffer_load_dwordx2 v[116:117], v245, %[rd], s81 offen offset:3584 sc1\n\t"
        "buffer_load_dwordx2 v[118:119], v245, %[rd], s81 offen offset:3840 sc1\n\t"
        "s_add_u32 s82, s80, %[vx]\n\t"
        "s_add_u32 s83, s82, 0x1000\n\t"
        "buffer_load_dwordx4 v[64:67], v244, %[rd], s82 offen offset:0 sc1\n\t"
        "buffer_load_dwordx4 v[68:71], v244, %[rd], s82 offen offset:1024 sc1\n\t"
        "buffer_load_dwordx4 v[72:75], v244, %[rd], s82 offen offset:2048 sc1\n\t"
        "buffer_load_dwordx4 v[76:79], v244, %[rd], s82 offen offset:3072 sc1\n\t"
        "buffer_load_dwordx4 v[80:83], v244, %[rd], s83 offen offset:0 sc1\n\t"
        "buffer_load_dwordx4 v[84:87], v244, %[rd], s83 offen offset:1024 sc1\n\t"
        "buffer_load_dwordx4 v[88:91], v244, %[rd], s83 offen offset:2048 sc1\n\t"
        "buffer_load_dwordx4 v[92:95], v244, %[rd], s83 offen offset:3072 sc1\n\t"
        "s_cmp_eq_u64 s[42:43], -1\n\t"
        "s_cbranch_scc1 .La17%=\n\t"
        "s_sleep %c[aslp]\n\t"
        ".La17%=:\n\t"
        "s_sleep %c[bslp]\n\t"
        "buffer_store_dword v0, off, %[rd], s36\n\t"
        "buffer_store_dword v0, off, %[rd], s36\n\t"
        "buffer_store_dword v0, off, %[rd], s36\n\t"
        "buffer_store_dword v0, off, %[rd], s36\n\t"
        "buffer_store_dword v0, off, %[rd], s36\n\t"
        "buffer_store_dword v0, off, %[rd], s36\n\t"
        "buffer_store_dword v0, off, %[rd], s36\n\t"
        "buffer_store_dword v0, off, %[rd], s36\n\t"
        "buffer_store_dword v0, off, %[rd], s36\n\t"
        ".La14%=:\n\t"
        "s_cmp_gt_u32 s40, s37\n\t"
        "s_cselect_b32 s80, s39, 0\n\t"
        "s_cmp_eq_u32 s80, 0\n\t"
        "s_cbranch_scc1 .La18%=\n\t"
        "s_mov_b32 s39, 0\n\t"
        "s_lshl_b32 s81, s37, 17\n\t"
        "s_add_u32 s81, s81, %[km]\n\t"
        "buffer_load_dwordx4 v[96:99], v244, %[rd], s81 offen offset:0 sc1\n\t"
        "buffer_load_dwordx4 v[100:103], v244, %[rd], s81 offen offset:1024 sc1\n\t"
        "buffer_load_dwordx2 v[104:105], v245, %[rd], s81 offen offset:2048 sc1\n\t"
        "buffer_load_dwordx2 v[106:107], v245, %[rd], s81 offen offset:2304 sc1\n\t"
        "buffer_load_dwordx2 v[108:109], v245, %[rd], s81 offen offset:2560 sc1\n\t"
        "buffer_load_dwordx2 v[110:111], v245, %[rd], s81 offen offset:2816 sc1\n\t"
        "buffer_load_dwordx2 v[112:113], v245, %[rd], s81 offen offset:3072 sc1\n\t"
        "buffer_load_dwordx2 v[114:115], v245, %[rd], s81 offen offset:3328 sc1\n\t"
        "buffer_load_dwordx2 v[116:117], v245, %[rd], s81 offen offset:3584 sc1\n\t"
        "buffer_load_dwordx2 v[118:119], v245, %[rd], s81 offen offset:3840 sc1\n\t"
        "buffer_store_dword v0, off, %[rd], s36\n\t"
        "buffer_store_dword v0, off, %[rd], s36\n\t"
        "buffer_store_dword v0, off, %[rd], s36\n\t"
        "buffer_store_dword v0, off, %[rd], s36\n\t"
        "buffer_store_dword v0, off, %[rd], s36\n\t"
        "buffer_store_dword v0, off, %[rd], s36\n\t"
        "buffer_store_dword v0, off, %[rd], s36\n\t"
        "buffer_store_dword v0, off, %[rd], s36\n\t"
        ".La18%=:\n\t"
        "s_add_u32 s86, s37, 4\n\t"
        "s_and_b32 s80, s86, 60\n\t"
        "s_lshr_b64 s[80:81], s[42:43], s80\n\t"
        "s_andn2_b32 s80, 15, s80\n\t"
        "s_cselect_b32 s46, s36, 0\n\t"
        "s_cmp_lt_u32 s86, 64\n\t"
        "s_cselect_b32 s46, s46, s36\n\t"
        "s_cmp_gt_u32 s40, s86\n\t"
        "s_cselect_b32 s47, s46, s36\n\t"
        "s_lshl_b32 s80, s86, 18\n\t"
        "s_or_b32 s80, s80, s46\n\t"
        "s_lshl_b32 s81, s86, 17\n\t"
        "s_add_u32 s81, s81, %[km]\n\t"
        "s_or_b32 s81, s81, s47\n\t"
        "s_add_u32 s82, s80, %[kx]\n\t"
        "s_add_u32 s83, s82, 0x1000\n\t"
        "buffer_load_dwordx4 a[0:3], v244, %[rd], s82 offen offset:0 sc1\n\t"
        "buffer_load_dwordx4 a[4:7], v244, %[rd], s82 offen offset:1024 sc1\n\t"
        "buffer_load_dwordx4 a[8:11], v244, %[rd], s82 offen offset:2048 sc1\n\t"
        "buffer_load_dwordx4 a[12:15], v244, %[rd], s82 offen offset:3072 sc1\n\t"
        "buffer_load_dwordx4 a[16:19], v244, %[rd], s83 offen offset:0 sc1\n\t"
        "buffer_load_dwordx4 a[20:23], v244, %[rd], s83 offen offset:1024 sc1\n\t"
        "buffer_load_dwordx4 a[24:27], v244, %[rd], s83 offen offset:2048 sc1\n\t"
        "buffer_load_dwordx4 a[28:31], v244, %[rd], s83 offen offset:3072 sc1\n\t"
        "s_cmp_eq_u64 s[42:43], -1\n\t"
        "s_cbranch_scc1 .La19%=\n\t"
        "s_sleep %c[aslp]\n\t"
        ".La19%=:\n\t"
        "s_sleep %c[bslp]\n\t"
        "s_add_u32 s82, s80, %[qx]\n\t"
        "s_add_u32 s83, s82, 0x1000\n\t"
        "buffer_load_dwordx4 a[32:35], v244, %[rd], s82 offen offset:0 sc1\n\t"
        "buffer_load_dwordx4 a[36:39], v244, %[rd], s82 offen offset:1024 sc1\n\t"
        "buffer_load_dwordx4 a[40:43], v244, %[rd], s82 offen offset:2048 sc1\n\t"
        "buffer_load_dwordx4 a[44:47], v244, %[rd], s82 offen offset:3072 sc1\n\t"
        "buffer_load_dwordx4 a[48:51], v244, %[rd], s83 offen offset:0 sc1\n\t"
        "buffer_load_dwordx4 a[52:55], v244, %[rd], s83 offen offset:1024 sc1\n\t"
        "buffer_load_dwordx4 a[56:59], v244, %[rd], s83 offen offset:2048 sc1\n\t"
        "buffer_load_dwordx4 a[60:63], v244, %[rd], s83 offen offset:3072 sc1\n\t"
        "s_cmp_eq_u64 s[42:43], -1\n\t"
        "s_cbranch_scc1 .La20%=\n\t"
        "s_sleep %c[aslp]\n\t"
        ".La20%=:\n\t"
        "s_sleep %c[bslp]\n\t"
        "buffer_load_dwordx4 a[96:99], v244, %[rd], s81 offen offset:0 sc1\n\t"
        "buffer_load_dwordx4 a[100:103], v244, %[rd], s81 offen offset:1024 sc1\n\t"
        "buffer_load_dwordx2 a[104:105], v245, %[rd], s81 offen offset:2048 sc1\n\t"
        "buffer_load_dwordx2 a[106:107], v245, %[rd], s81 offen offset:2304 sc1\n\t"
        "buffer_load_dwordx2 a[108:109], v245, %[rd], s81 offen offset:2560 sc1\n\t"
        "buffer_load_dwordx2 a[110:111], v245, %[rd], s81 offen offset:2816 sc1\n\t"
        "buffer_load_dwordx2 a[112:113], v245, %[rd], s81 offen offset:3072 sc1\n\t"
        "buffer_load_dwordx2 a[114:115], v245, %[rd], s81 offen offset:3328 sc1\n\t"
        "buffer_load_dwordx2 a[116:117], v245, %[rd], s81 offen offset:3584 sc1\n\t"
        "buffer_load_dwordx2 a[118:119], v245, %[rd], s81 offen offset:3840 sc1\n\t"
        "s_add_u32 s82, s80, %[vx]\n\t"
        "s_add_u32 s83, s82, 0x1000\n\t"
        "buffer_load_dwordx4 a[64:67], v244, %[rd], s82 offen offset:0 sc1\n\t"
        "buffer_load_dwordx4 a[68:71], v244, %[rd], s82 offen offset:1024 sc1\n\t"
        "buffer_load_dwordx4 a[72:75], v244, %[rd], s82 offen offset:2048 sc1\n\t"
        "buffer_load_dwordx4 a[76:79], v244, %[rd], s82 offen offset:3072 sc1\n\t"
        "buffer_load_dwordx4 a[80:83], v244, %[rd], s83 offen offset:0 sc1\n\t"
        "buffer_load_dwordx4 a[84:87], v244, %[rd], s83 offen offset:1024 sc1\n\t"
        "buffer_load_dwordx4 a[88:91], v244, %[rd], s83 offen offset:2048 sc1\n\t"
        "buffer_load_dwordx4 a[92:95], v244, %[rd], s83 offen offset:3072 sc1\n\t"
        "s_cmp_eq_u64 s[42:43], -1\n\t"
        "s_cbranch_scc1 .La21%=\n\t"
        "s_sleep %c[aslp]\n\t"
        ".La21%=:\n\t"
        "s_sleep %c[bslp]\n\t"
        "s_waitcnt vmcnt(42)\n\t"
        "s_memrealtime s[98:99]\n\t"
        "v_mfma_f32_16x16x32_bf16 v[136:139], v[0:3], v[32:35], 0\n\t"
        "v_mfma_f32_16x16x32_bf16 v[148:151], v[0:3], v[48:51], 0\n\t"
        "v_mfma_f32_16x16x32_bf16 v[152:155], v[16:19], v[48:51], 0\n\t"
        "v_mfma_f32_16x16x32_bf16 v[136:139], v[4:7], v[36:39], v[136:139]\n\t"
        "v_mfma_f32_16x16x32_bf16 v[148:151], v[4:7], v[52:55], v[148:151]\n\t"
        "v_mfma_f32_16x16x32_bf16 v[152:155], v[20:23], v[52:55], v[152:155]\n\t"
        "v_mfma_f32_16x16x32_bf16 v[136:139], v[8:11], v[40:43], v[136:139]\n\t"
        "v_mfma_f32_16x16x32_bf16 v[148:151], v[8:11], v[56:59], v[148:151]\n\t"
        "v_mfma_f32_16x16x32_bf16 v[152:155], v[24:27], v[56:59], v[152:155]\n\t"
        "v_mfma_f32_16x16x32_bf16 v[136:139], v[12:15], v[44:47], v[136:139]\n\t"
        "v_mfma_f32_16x16x32_bf16 v[148:151], v[12:15], v[60:63], v[148:151]\n\t"
        "v_mfma_f32_16x16x32_bf16 v[152:155], v[28:31], v[60:63], v[152:155]\n\t"
        "s_waitcnt lgkmcnt(0)\n\t"
        "v_cmp_eq_u32_e32 vcc, %[tag], v240\n\t"
        "s_nop 1\n\t"
        "s_or_b64 s[42:43], s[42:43], vcc\n\t"
        "v_readfirstlane_b32 s80, v241\n\t"
        "s_nop 0\n\t"
        "s_cmp_gt_u32 s80, s40\n\t"
        "s_cselect_b32 s41, s98, s41\n\t"
        "s_max_u32 s40, s40, s80\n\t"
        "v_readlane_b32 s84, v242, 0\n\t"
        "v_readlane_b32 s85, v242, 32\n\t"
        "v_mov_b32_e32 v243, 32\n\t"
        "s_and_b32 s80, s45, 3\n\t"
        "s_lshl_b32 s80, s80, 12\n\t"
        "s_lshr_b32 s81, s45, 2\n\t"
        "s_lshl_b32 s81, s81, 3\n\t"
        "s_add_u32 s80, s80, s81\n\t"
        "s_lshr_b32 s82, s45, 1\n\t"
        "s_xnor_b32 s81, s45, s82\n\t"
        "s_and_b32 s81, s81, 1\n\t"
        "s_lshl_b32 s81, s81, 13\n\t"
        "s_lshl_b32 s82, s82, 2\n\t"
        "s_add_u32 s81, s81, s82\n\t"
        "s_cmp_lt_i32 s45, 60\n\t"
        "s_cselect_b32 s80, s80, s81\n\t"
        "s_cselect_b32 s81, 4, 0x1000\n\t"
        "s_add_u32 s81, s81, s80\n\t"
        "s_cmp_eq_u32 s84, 31\n\t"
        "s_cselect_b32 s80, s80, s36\n\t"
        "s_cmp_eq_u32 s85, 31\n\t"
        "s_cselect_b32 s81, s81, s36\n\t"
        "s_cmp_lt_i32 s45, 0\n\t"
        "s_cselect_b32 s80, s36, s80\n\t"
        "s_cselect_b32 s81, s36, s81\n\t"
        "buffer_store_dword v243, v251, %[rd], s80 offen sc1\n\t"
        "buffer_store_dword v243, v251, %[rd], s81 offen sc1\n\t"
        "s_add_u32 s86, s37, 8\n\t"
        "s_cmp_lg_u64 s[42:43], -1\n\t"
        "s_cselect_b32 s84, 0, s36\n\t"
        "s_cmp_gt_u32 s40, s86\n\t"
        "s_cselect_b32 s85, s36, s39\n\t"
        "buffer_load_dword v240, v246, %[rd], s84 offen sc1\n\t"
        "buffer_load_dword v241, v247, %[rd], s85 offen sc1\n\t"
        "v_cvt_pk_bf16_f32 v136, 0, v136\n\t"
        "v_cvt_pk_bf16_f32 v137, 0, v137\n\t"
        "v_cvt_pk_bf16_f32 v138, 0, v138\n\t"
        "v_cvt_pk_bf16_f32 v139, 0, v139\n\t"
        "v_mul_f32_e32 v136, 0x3db504f3, v136\n\t"
        "v_mul_f32_e32 v137, 0x3db504f3, v137\n\t"
        "v_mul_f32_e32 v138, 0x3db504f3, v138\n\t"
        "v_mul_f32_e32 v139, 0x3db504f3, v139\n\t"
        "v_cvt_pk_bf16_f32 v136, 0, v136\n\t"
        "v_cvt_pk_bf16_f32 v137, 0, v137\n\t"
        "v_cvt_pk_bf16_f32 v138, 0, v138\n\t"
        "v_cvt_pk_bf16_f32 v139, 0, v139\n\t"
        "v_cndmask_b32_e64 v136, v253, v136, s[48:49]\n\t"
        "v_cndmask_b32_e64 v137, v253, v137, s[50:51]\n\t"
        "v_cndmask_b32_e64 v138, v253, v138, s[52:53]\n\t"
        "v_cndmask_b32_e64 v139, v253, v139, s[54:55]\n\t"
        "v_cvt_pk_bf16_f32 v148, 0, v148\n\t"
        "v_cvt_pk_bf16_f32 v149, 0, v149\n\t"
        "v_cvt_pk_bf16_f32 v150, 0, v150\n\t"
        "v_cvt_pk_bf16_f32 v151, 0, v151\n\t"
        "v_mul_f32_e32 v148, 0x3db504f3, v148\n\t"
        "v_mul_f32_e32 v149, 0x3db504f3, v149\n\t"
        "v_mul_f32_e32 v150, 0x3db504f3, v150\n\t"
        "v_mul_f32_e32 v151, 0x3db504f3, v151\n\t"
        "v_cvt_pk_bf16_f32 v148, 0, v148\n\t"
        "v_cvt_pk_bf16_f32 v149, 0, v149\n\t"
        "v_cvt_pk_bf16_f32 v150, 0, v150\n\t"
        "v_cvt_pk_bf16_f32 v151, 0, v151\n\t"
        "v_cvt_pk_bf16_f32 v152, 0, v152\n\t"
        "v_cvt_pk_bf16_f32 v153, 0, v153\n\t"
        "v_cvt_pk_bf16_f32 v154, 0, v154\n\t"
        "v_cvt_pk_bf16_f32 v155, 0, v155\n\t"
        "v_mul_f32_e32 v152, 0x3db504f3, v152\n\t"
        "v_mul_f32_e32 v153, 0x3db504f3, v153\n\t"
        "v_mul_f32_e32 v154, 0x3db504f3, v154\n\t"
        "v_mul_f32_e32 v155, 0x3db504f3, v155\n\t"
        "v_cvt_pk_bf16_f32 v152, 0, v152\n\t"
        "v_cvt_pk_bf16_f32 v153, 0, v153\n\t"
        "v_cvt_pk_bf16_f32 v154, 0, v154\n\t"
        "v_cvt_pk_bf16_f32 v155, 0, v155\n\t"
        "v_cndmask_b32_e64 v152, v253, v152, s[48:49]\n\t"
        "v_cndmask_b32_e64 v153, v253, v153, s[50:51]\n\t"
        "v_cndmask_b32_e64 v154, v253, v154, s[52:53]\n\t"
        "v_cndmask_b32_e64 v155, v253, v155, s[54:55]\n\t"
        "v_max3_f32 v172, v136, v137, v253\n\t"
        "v_max3_f32 v172, v172, v138, v139\n\t"
        "v_mov_b32_e32 v140, v253\n\t"
        "v_max_f32_e32 v174, v148, v149\n\t"
        "v_max3_f32 v174, v174, v150, v151\n\t"
        "v_max3_f32 v174, v174, v152, v153\n\t"
        "v_max3_f32 v174, v174, v154, v155\n\t"
        "s_cmp_eq_u32 s39, 0\n\t"
        "s_cbranch_scc1 .La23%=\n\t"
        "s_waitcnt vmcnt(0)\n\t"
        "s_lshl_b32 s80, s44, 7\n\t"
        "s_cmp_lt_i32 s44, 0\n\t"
        "s_cselect_b32 s80, s36, s80\n\t"
        "v_mov_b32_e32 v242, 1\n\t"
        "buffer_atomic_add v242, v250, %[rd], s80 offen sc0 sc1\n\t"
        "s_waitcnt vmcnt(0)\n\t"
        "v_readlane_b32 s84, v242, 0\n\t"
        "v_readlane_b32 s85, v242, 32\n\t"
        "v_mov_b32_e32 v243, 32\n\t"
        "s_and_b32 s80, s44, 3\n\t"
        "s_lshl_b32 s80, s80, 12\n\t"
        "s_lshr_b32 s81, s44, 2\n\t"
        "s_lshl_b32 s81, s81, 3\n\t"
        "s_add_u32 s80, s80, s81\n\t"
        "s_lshr_b32 s82, s44, 1\n\t"
        "s_xnor_b32 s81, s44, s82\n\t"
        "s_and_b32 s81, s81, 1\n\t"
        "s_lshl_b32 s81, s81, 13\n\t"
        "s_lshl_b32 s82, s82, 2\n\t"
        "s_add_u32 s81, s81, s82\n\t"
        "s_cmp_lt_i32 s44, 60\n\t"
        "s_cselect_b32 s80, s80, s81\n\t"
        "s_cselect_b32 s81, 4, 0x1000\n\t"
        "s_add_u32 s81, s81, s80\n\t"
        "s_cmp_eq_u32 s84, 31\n\t"
        "s_cselect_b32 s80, s80, s36\n\t"
        "s_cmp_eq_u32 s85, 31\n\t"
        "s_cselect_b32 s81, s81, s36\n\t"
        "s_cmp_lt_i32 s44, 0\n\t"
        "s_cselect_b32 s80, s36, s80\n\t"
        "s_cselect_b32 s81, s36, s81\n\t"
        "buffer_store_dword v243, v251, %[rd], s80 offen sc1\n\t"
        "buffer_store_dword v243, v251, %[rd], s81 offen sc1\n\t"
        "s_mov_b32 s44, -1\n\t"
        "s_mov_b32 s97, 0\n\t"
        "s_min_u32 s87, s37, 62\n\t"
        ".La22%=:\n\t"
        "s_cmp_gt_u32 s40, s87\n\t"
        "s_cbranch_scc1 .La22%=x\n\t"
        "s_memrealtime s[98:99]\n\t"
        "s_add_u32 s97, s97, 1\n\t"
        "s_cmp_lt_u32 s97, 0x400000\n\t"
        "s_cbranch_scc0 .Lgu%=\n\t"
        "s_sub_u32 s80, s87, s40\n\t"
        "s_cmp_ge_u32 s87, %c[efrom]\n\t"
        "s_subb_u32 s80, s80, 0\n\t"
        "s_mul_i32 s80, s80, %c[smin]\n\t"
        "s_waitcnt lgkmcnt(0)\n\t"
        "s_sub_u32 s81, s98, s41\n\t"
        "s_cmp_ge_i32 s81, s80\n\t"
        "s_cbranch_scc1 .La22%=p\n\t"
        "s_sleep 16\n\t"
        "s_branch .La22%=\n\t"
        ".La22%=p:\n\t"
        "buffer_load_dword v241, v247, %[rd], 0 offen sc1\n\t"
        "s_waitcnt vmcnt(0)\n\t"
        "v_readfirstlane_b32 s80, v241\n\t"
        "s_nop 0\n\t"
        "s_cmp_gt_u32 s80, s40\n\t"
        "s_cselect_b32 s41, s98, s41\n\t"
        "s_max_u32 s40, s40, s80\n\t"
        "s_cmp_gt_u32 s40, s87\n\t"
        "s_cbranch_scc1 .La22%=x\n\t"
        "s_sleep %c[pslp]\n\t"
        "s_branch .La22%=\n\t"
        ".La22%=x:\n\t"
        "s_lshl_b32 s81, s37, 17\n\t"
        "s_add_u32 s81, s81, %[km]\n\t"
        "s_cmp_lg_u32 s37, 63\n\t"
        "s_cbranch_scc1 .La22%=l\n\t"
        "s_sleep %c[lslp]\n\t"
        ".La22%=l:\n\t"
        "buffer_load_dwordx4 v[96:99], v244, %[rd], s81 offen offset:0 sc1\n\t"
        "buffer_load_dwordx4 v[100:103], v244, %[rd], s81 offen offset:1024 sc1\n\t"
        "buffer_load_dwordx2 v[104:105], v245, %[rd], s81 offen offset:2048 sc1\n\t"
        "buffer_load_dwordx2 v[106:107], v245, %[rd], s81 offen offset:2304 sc1\n\t"
        "buffer_load_dwordx2 v[108:109], v245, %[rd], s81 offen offset:2560 sc1\n\t"
        "buffer_load_dwordx2 v[110:111], v245, %[rd], s81 offen offset:2816 sc1\n\t"
        "buffer_load_dwordx2 v[112:113], v245, %[rd], s81 offen offset:3072 sc1\n\t"
        "buffer_load_dwordx2 v[114:115], v245, %[rd], s81 offen offset:3328 sc1\n\t"
        "buffer_load_dwordx2 v[116:117], v245, %[rd], s81 offen offset:3584 sc1\n\t"
        "buffer_load_dwordx2 v[118:119], v245, %[rd], s81 offen offset:3840 sc1\n\t"
        "s_waitcnt vmcnt(0)\n\t"
        "s_cmp_lg_u32 s37, 63\n\t"
        "s_cbranch_scc1 .La23%=\n\t"
        "s_add_u32 s97, s97, 1\n\t"
        "s_cmp_lt_u32 s97, 0x400000\n\t"
        "s_cbranch_scc0 .Lgu%=\n\t"
        "v_max_u32_e32 v120, v96, v97\n\t"
        "v_max3_u32 v120, v120, v98, v99\n\t"
        "v_max3_u32 v120, v120, v100, v101\n\t"
        "v_max3_u32 v120, v120, v102, v103\n\t"
        "v_max3_u32 v120, v120, v104, v105\n\t"
        "v_max3_u32 v120, v120, v106, v107\n\t"
        "v_max3_u32 v120, v120, v108, v109\n\t"
        "v_max3_u32 v120, v120, v110, v111\n\t"
        "v_max3_u32 v120, v120, v112, v113\n\t"
        "v_max3_u32 v120, v120, v114, v115\n\t"
        "v_max3_u32 v120, v120, v116, v117\n\t"
        "v_max3_u32 v120, v120, v118, v119\n\t"
        "v_cmp_eq_u32_e32 vcc, -1, v120\n\t"
        "s_nop 1\n\t"
        "v_mov_b32_e32 v252, v252\n\t"
        "s_nop 0\n\t"
        "s_cbranch_vccnz .La22%=l\n\t"
        ".La23%=:\n\t"
        "ds_write_b128 v248, v[96:99] offset:0\n\t"
        "ds_write_b128 v248, v[100:103] offset:1024\n\t"
        "ds_read_b64_tr_b16 v[120:121], v249 offset:0\n\t"
        "ds_read_b64_tr_b16 v[122:123], v249 offset:64\n\t"
        "ds_read_b64_tr_b16 v[124:125], v249 offset:512\n\t"
        "ds_read_b64_tr_b16 v[126:127], v249 offset:576\n\t"
        "ds_read_b64_tr_b16 v[128:129], v249 offset:1024\n\t"
        "ds_read_b64_tr_b16 v[130:131], v249 offset:1088\n\t"
        "ds_read_b64_tr_b16 v[132:133], v249 offset:1536\n\t"
        "ds_read_b64_tr_b16 v[134:135], v249 offset:1600\n\t"
        "s_waitcnt lgkmcnt(6)\n\t"
        "v_mfma_f32_16x16x32_bf16 v[144:147], v[120:123], v[32:35], 0\n\t"
        "v_mfma_f32_16x16x32_bf16 v[156:159], v[120:123], v[48:51], 0\n\t"
        "s_waitcnt lgkmcnt(4)\n\t"
        "v_mfma_f32_16x16x32_bf16 v[144:147], v[124:127], v[36:39], v[144:147]\n\t"
        "v_mfma_f32_16x16x32_bf16 v[156:159], v[124:127], v[52:55], v[156:159]\n\t"
        "s_waitcnt lgkmcnt(2)\n\t"
        "v_mfma_f32_16x16x32_bf16 v[144:147], v[128:131], v[40:43], v[144:147]\n\t"
        "v_mfma_f32_16x16x32_bf16 v[156:159], v[128:131], v[56:59], v[156:159]\n\t"
        "s_waitcnt lgkmcnt(0)\n\t"
        "v_mfma_f32_16x16x32_bf16 v[144:147], v[132:135], v[44:47], v[144:147]\n\t"
        "v_mfma_f32_16x16x32_bf16 v[156:159], v[132:135], v[60:63], v[156:159]\n\t"
        "s_nop 1\n\t"
        "v_mov_b32_e32 v252, v252\n\t"
        "s_nop 1\n\t"
        "v_mov_b32_e32 v252, v252\n\t"
        "s_nop 0\n\t"
        "v_cvt_pk_bf16_f32 v144, 0, v144\n\t"
        "v_cvt_pk_bf16_f32 v145, 0, v145\n\t"
        "v_cvt_pk_bf16_f32 v146, 0, v146\n\t"
        "v_cvt_pk_bf16_f32 v147, 0, v147\n\t"
        "v_mul_f32_e32 v144, 0x3db504f3, v144\n\t"
        "v_mul_f32_e32 v145, 0x3db504f3, v145\n\t"
        "v_mul_f32_e32 v146, 0x3db504f3, v146\n\t"
        "v_mul_f32_e32 v147, 0x3db504f3, v147\n\t"
        "v_cvt_pk_bf16_f32 v144, 0, v144\n\t"
        "v_cvt_pk_bf16_f32 v145, 0, v145\n\t"
        "v_cvt_pk_bf16_f32 v146, 0, v146\n\t"
        "v_cvt_pk_bf16_f32 v147, 0, v147\n\t"
        "v_cndmask_b32_e64 v144, v144, v254, s[56:57]\n\t"
        "v_cndmask_b32_e64 v145, v145, v254, s[56:57]\n\t"
        "v_cndmask_b32_e64 v146, v146, v254, s[56:57]\n\t"
        "v_cndmask_b32_e64 v147, v147, v254, s[56:57]\n\t"
        "v_max3_f32 v172, v172, v144, v145\n\t"
        "v_max3_f32 v173, v172, v146, v147\n\t"
        "v_max3_f32 v172, v172, v146, v147\n\t"
        "s_nop 1\n\t"
        "v_permlane16_swap_b32_e32 v172, v173\n\t"
        "v_max_f32_e32 v172, v172, v173\n\t"
        "v_mov_b32_e32 v173, v172\n\t"
        "s_nop 1\n\t"
        "v_permlane32_swap_b32_e32 v172, v173\n\t"
        "v_max_f32_e32 v172, v172, v173\n\t"
        "v_sub_f32_e32 v136, v136, v172\n\t"
        "v_sub_f32_e32 v137, v137, v172\n\t"
        "v_sub_f32_e32 v138, v138, v172\n\t"
        "v_sub_f32_e32 v139, v139, v172\n\t"
        "v_sub_f32_e32 v140, v140, v172\n\t"
        "v_sub_f32_e32 v144, v144, v172\n\t"
        "v_sub_f32_e32 v145, v145, v172\n\t"
        "v_sub_f32_e32 v146, v146, v172\n\t"
        "v_sub_f32_e32 v147, v147, v172\n\t"
        "v_mul_f32_e32 v136, 0x3fb8aa3b, v136\n\t"
        "v_mul_f32_e32 v137, 0x3fb8aa3b, v137\n\t"
        "v_mul_f32_e32 v138, 0x3fb8aa3b, v138\n\t"
        "v_mul_f32_e32 v139, 0x3fb8aa3b, v139\n\t"
        "v_mul_f32_e32 v140, 0x3fb8aa3b, v140\n\t"
        "v_mul_f32_e32 v144, 0x3fb8aa3b, v144\n\t"
        "v_mul_f32_e32 v145, 0x3fb8aa3b, v145\n\t"
        "v_mul_f32_e32 v146, 0x3fb8aa3b, v146\n\t"
        "v_mul_f32_e32 v147, 0x3fb8aa3b, v147\n\t"
        "v_exp_f32_e32 v136, v136\n\t"
        "v_exp_f32_e32 v137, v137\n\t"
        "v_exp_f32_e32 v138, v138\n\t"
        "v_exp_f32_e32 v139, v139\n\t"
        "v_exp_f32_e32 v140, v140\n\t"
        "v_exp_f32_e32 v144, v144\n\t"
        "v_exp_f32_e32 v145, v145\n\t"
        "v_exp_f32_e32 v146, v146\n\t"
        "v_exp_f32_e32 v147, v147\n\t"
        "v_mul_f32_e32 v172, 4.0, v140\n\t"
        "v_add_f32_e32 v172, v172, v136\n\t"
        "v_add_f32_e32 v172, v172, v137\n\t"
        "v_add_f32_e32 v172, v172, v138\n\t"
        "v_add_f32_e32 v172, v172, v139\n\t"
        "v_add_f32_e32 v172, v172, v144\n\t"
        "v_add_f32_e32 v172, v172, v145\n\t"
        "v_add_f32_e32 v172, v172, v146\n\t"
        "v_add_f32_e32 v172, v172, v147\n\t"
        "v_mov_b32_e32 v173, v172\n\t"
        "s_nop 1\n\t"
        "v_permlane16_swap_b32_e32 v172, v173\n\t"
        "v_add_f32_e32 v172, v172, v173\n\t"
        "v_mov_b32_e32 v173, v172\n\t"
        "s_nop 1\n\t"
        "v_permlane32_swap_b32_e32 v172, v173\n\t"
        "v_add_f32_e32 v172, v172, v173\n\t"
        "v_rcp_f32_e32 v172, v172\n\t"
        "s_nop 0\n\t"
        "v_mul_f32_e32 v136, v136, v172\n\t"
        "v_mul_f32_e32 v137, v137, v172\n\t"
        "v_mul_f32_e32 v138, v138, v172\n\t"
        "v_mul_f32_e32 v139, v139, v172\n\t"
        "v_mul_f32_e32 v140, v140, v172\n\t"
        "v_mul_f32_e32 v144, v144, v172\n\t"
        "v_mul_f32_e32 v145, v145, v172\n\t"
        "v_mul_f32_e32 v146, v146, v172\n\t"
        "v_mul_f32_e32 v147, v147, v172\n\t"
        "v_cvt_pk_bf16_f32 v160, v136, v137\n\t"
        "v_cvt_pk_bf16_f32 v161, v138, v139\n\t"
        "v_cvt_pk_bf16_f32 v162, v140, v140\n\t"
        "v_cvt_pk_bf16_f32 v163, v140, v140\n\t"
        "v_cvt_pk_bf16_f32 v164, v144, v145\n\t"
        "v_cvt_pk_bf16_f32 v165, v146, v147\n\t"
        "v_mfma_f32_16x16x32_bf16 v[176:179], v[64:67], v[160:163], 0\n\t"
        "v_cvt_pk_bf16_f32 v156, 0, v156\n\t"
        "v_cvt_pk_bf16_f32 v157, 0, v157\n\t"
        "v_cvt_pk_bf16_f32 v158, 0, v158\n\t"
        "v_mfma_f32_16x16x32_bf16 v[180:183], v[68:71], v[160:163], 0\n\t"
        "v_cvt_pk_bf16_f32 v159, 0, v159\n\t"
        "v_mul_f32_e32 v156, 0x3db504f3, v156\n\t"
        "v_mul_f32_e32 v157, 0x3db504f3, v157\n\t"
        "v_mfma_f32_16x16x32_bf16 v[184:187], v[72:75], v[160:163], 0\n\t"
        "v_mul_f32_e32 v158, 0x3db504f3, v158\n\t"
        "v_mul_f32_e32 v159, 0x3db504f3, v159\n\t"
        "v_cvt_pk_bf16_f32 v156, 0, v156\n\t"
        "v_mfma_f32_16x16x32_bf16 v[188:191], v[76:79], v[160:163], 0\n\t"
        "v_cvt_pk_bf16_f32 v157, 0, v157\n\t"
        "v_cvt_pk_bf16_f32 v158, 0, v158\n\t"
        "v_cvt_pk_bf16_f32 v159, 0, v159\n\t"
        "v_mfma_f32_16x16x32_bf16 v[192:195], v[80:83], v[160:163], 0\n\t"
        "v_cndmask_b32_e64 v156, v156, v254, s[56:57]\n\t"
        "v_cndmask_b32_e64 v157, v157, v254, s[56:57]\n\t"
        "v_cndmask_b32_e64 v158, v158, v254, s[56:57]\n\t"
        "v_mfma_f32_16x16x32_bf16 v[196:199], v[84:87], v[160:163], 0\n\t"
        "v_cndmask_b32_e64 v159, v159, v254, s[56:57]\n\t"
        "v_max3_f32 v174, v174, v156, v157\n\t"
        "v_max3_f32 v175, v174, v158, v159\n\t"
        "v_mfma_f32_16x16x32_bf16 v[200:203], v[88:91], v[160:163], 0\n\t"
        "v_max3_f32 v174, v174, v158, v159\n\t"
        "s_nop 1\n\t"
        "v_permlane16_swap_b32_e32 v174, v175\n\t"
        "v_max_f32_e32 v174, v174, v175\n\t"
        "v_mfma_f32_16x16x32_bf16 v[204:207], v[92:95], v[160:163], 0\n\t"
        "v_mov_b32_e32 v175, v174\n\t"
        "s_nop 1\n\t"
        "v_permlane32_swap_b32_e32 v174, v175\n\t"
        "v_max_f32_e32 v174, v174, v175\n\t"
        "v_mfma_f32_16x16x16_bf16 v[176:179], v[104:105], v[164:165], v[176:179]\n\t"
        "v_sub_f32_e32 v148, v148, v174\n\t"
        "v_sub_f32_e32 v149, v149, v174\n\t"
        "v_sub_f32_e32 v150, v150, v174\n\t"
        "v_mfma_f32_16x16x16_bf16 v[180:183], v[106:107], v[164:165], v[180:183]\n\t"
        "v_sub_f32_e32 v151, v151, v174\n\t"
        "v_sub_f32_e32 v152, v152, v174\n\t"
        "v_sub_f32_e32 v153, v153, v174\n\t"
        "v_mfma_f32_16x16x16_bf16 v[184:187], v[108:109], v[164:165], v[184:187]\n\t"
        "v_sub_f32_e32 v154, v154, v174\n\t"
        "v_sub_f32_e32 v155, v155, v174\n\t"
        "v_sub_f32_e32 v156, v156, v174\n\t"
        "v_mfma_f32_16x16x16_bf16 v[188:191], v[110:111], v[164:165], v[188:191]\n\t"
        "v_sub_f32_e32 v157, v157, v174\n\t"
        "v_sub_f32_e32 v158, v158, v174\n\t"
        "v_sub_f32_e32 v159, v159, v174\n\t"
        "v_mfma_f32_16x16x16_bf16 v[192:195], v[112:113], v[164:165], v[192:195]\n\t"
        "v_mul_f32_e32 v148, 0x3fb8aa3b, v148\n\t"
        "v_mul_f32_e32 v149, 0x3fb8aa3b, v149\n\t"
        "v_mul_f32_e32 v150, 0x3fb8aa3b, v150\n\t"
        "v_mfma_f32_16x16x16_bf16 v[196:199], v[114:115], v[164:165], v[196:199]\n\t"
        "v_mul_f32_e32 v151, 0x3fb8aa3b, v151\n\t"
        "v_mul_f32_e32 v152, 0x3fb8aa3b, v152\n\t"
        "v_mul_f32_e32 v153, 0x3fb8aa3b, v153\n\t"
        "v_mfma_f32_16x16x16_bf16 v[200:203], v[116:117], v[164:165], v[200:203]\n\t"
        "v_mul_f32_e32 v154, 0x3fb8aa3b, v154\n\t"
        "v_mul_f32_e32 v155, 0x3fb8aa3b, v155\n\t"
        "v_mul_f32_e32 v156, 0x3fb8aa3b, v156\n\t"
        "v_mfma_f32_16x16x16_bf16 v[204:207], v[118:119], v[164:165], v[204:207]\n\t"
        "v_mul_f32_e32 v157, 0x3fb8aa3b, v157\n\t"
        "v_mul_f32_e32 v158, 0x3fb8aa3b, v158\n\t"
        "v_mul_f32_e32 v159, 0x3fb8aa3b, v159\n\t"
        "v_exp_f32_e32 v148, v148\n\t"
        "v_exp_f32_e32 v149, v149\n\t"
        "v_exp_f32_e32 v150, v150\n\t"
        "v_exp_f32_e32 v151, v151\n\t"
        "v_exp_f32_e32 v152, v152\n\t"
        "v_exp_f32_e32 v153, v153\n\t"
        "v_exp_f32_e32 v154, v154\n\t"
        "v_exp_f32_e32 v155, v155\n\t"
        "v_exp_f32_e32 v156, v156\n\t"
        "v_exp_f32_e32 v157, v157\n\t"
        "v_exp_f32_e32 v158, v158\n\t"
        "v_exp_f32_e32 v159, v159\n\t"
        "v_add_f32_e32 v174, v148, v149\n\t"
        "v_add_f32_e32 v174, v174, v150\n\t"
        "v_add_f32_e32 v174, v174, v151\n\t"
        "v_add_f32_e32 v174, v174, v152\n\t"
        "v_add_f32_e32 v174, v174, v153\n\t"
        "v_add_f32_e32 v174, v174, v154\n\t"
        "v_add_f32_e32 v174, v174, v155\n\t"
        "v_add_f32_e32 v174, v174, v156\n\t"
        "v_add_f32_e32 v174, v174, v157\n\t"
        "v_add_f32_e32 v174, v174, v158\n\t"
        "v_add_f32_e32 v174, v174, v159\n\t"
        "v_mov_b32_e32 v175, v174\n\t"
        "s_nop 1\n\t"
        "v_permlane16_swap_b32_e32 v174, v175\n\t"
        "v_add_f32_e32 v174, v174, v175\n\t"
        "v_mov_b32_e32 v175, v174\n\t"
        "s_nop 1\n\t"
        "v_permlane32_swap_b32_e32 v174, v175\n\t"
        "v_add_f32_e32 v174, v174, v175\n\t"
        "v_rcp_f32_e32 v174, v174\n\t"
        "s_nop 0\n\t"
        "v_mul_f32_e32 v148, v148, v174\n\t"
        "v_mul_f32_e32 v149, v149, v174\n\t"
        "v_mul_f32_e32 v150, v150, v174\n\t"
        "v_mul_f32_e32 v151, v151, v174\n\t"
        "v_mul_f32_e32 v152, v152, v174\n\t"
        "v_mul_f32_e32 v153, v153, v174\n\t"
        "v_mul_f32_e32 v154, v154, v174\n\t"
        "v_mul_f32_e32 v155, v155, v174\n\t"
        "v_mul_f32_e32 v156, v156, v174\n\t"
        "v_mul_f32_e32 v157, v157, v174\n\t"
        "v_mul_f32_e32 v158, v158, v174\n\t"
        "v_mul_f32_e32 v159, v159, v174\n\t"
        "v_cvt_pk_bf16_f32 v166, v148, v149\n\t"
        "v_cvt_pk_bf16_f32 v167, v150, v151\n\t"
        "v_cvt_pk_bf16_f32 v168, v152, v153\n\t"
        "v_cvt_pk_bf16_f32 v169, v154, v155\n\t"
        "v_cvt_pk_bf16_f32 v170, v156, v157\n\t"
        "v_cvt_pk_bf16_f32 v171, v158, v159\n\t"
        "s_waitcnt vmcnt(38)\n\t"
        "s_lshl_b32 s80, s44, 7\n\t"
        "s_cmp_lt_i32 s44, 0\n\t"
        "s_cselect_b32 s80, s36, s80\n\t"
        "v_mov_b32_e32 v242, 1\n\t"
        "buffer_atomic_add v242, v250, %[rd], s80 offen sc0 sc1\n\t"
        "s_lshl_b32 s86, s37, 18\n\t"
        "s_add_u32 s86, s86, %[qx]\n\t"
        "s_add_u32 s87, s86, 0x1000\n\t"
        "v_mfma_f32_16x16x32_bf16 v[208:211], v[64:67], v[166:169], 0\n\t"
        "v_cvt_pk_bf16_f32 v176, v176, v177\n\t"
        "v_cvt_pk_bf16_f32 v177, v178, v179\n\t"
        "v_mfma_f32_16x16x32_bf16 v[212:215], v[68:71], v[166:169], 0\n\t"
        "v_cvt_pk_bf16_f32 v178, v180, v181\n\t"
        "v_cvt_pk_bf16_f32 v179, v182, v183\n\t"
        "v_mfma_f32_16x16x32_bf16 v[216:219], v[72:75], v[166:169], 0\n\t"
        "buffer_store_dwordx4 v[176:179], v244, %[rd], s86 offen offset:0 sc1\n\t"
        "v_cvt_pk_bf16_f32 v184, v184, v185\n\t"
        "v_mfma_f32_16x16x32_bf16 v[220:223], v[76:79], v[166:169], 0\n\t"
        "v_cvt_pk_bf16_f32 v185, v186, v187\n\t"
        "v_cvt_pk_bf16_f32 v186, v188, v189\n\t"
        "v_mfma_f32_16x16x32_bf16 v[224:227], v[80:83], v[166:169], 0\n\t"
        "v_cvt_pk_bf16_f32 v187, v190, v191\n\t"
        "buffer_store_dwordx4 v[184:187], v244, %[rd], s86 offen offset:1024 sc1\n\t"
        "v_mfma_f32_16x16x32_bf16 v[228:231], v[84:87], v[166:169], 0\n\t"
        "v_cvt_pk_bf16_f32 v192, v192, v193\n\t"
        "v_cvt_pk_bf16_f32 v193, v194, v195\n\t"
        "v_mfma_f32_16x16x32_bf16 v[232:235], v[88:91], v[166:169], 0\n\t"
        "v_cvt_pk_bf16_f32 v194, v196, v197\n\t"
        "v_cvt_pk_bf16_f32 v195, v198, v199\n\t"
        "v_mfma_f32_16x16x32_bf16 v[236:239], v[92:95], v[166:169], 0\n\t"
        "buffer_store_dwordx4 v[192:195], v244, %[rd], s86 offen offset:2048 sc1\n\t"
        "v_cvt_pk_bf16_f32 v200, v200, v201\n\t"
        "v_mfma_f32_16x16x16_bf16 v[208:211], v[104:105], v[170:171], v[208:211]\n\t"
        "v_cvt_pk_bf16_f32 v201, v202, v203\n\t"
        "v_cvt_pk_bf16_f32 v202, v204, v205\n\t"
        "v_mfma_f32_16x16x16_bf16 v[212:215], v[106:107], v[170:171], v[212:215]\n\t"
        "v_cvt_pk_bf16_f32 v203, v206, v207\n\t"
        "buffer_store_dwordx4 v[200:203], v244, %[rd], s86 offen offset:3072 sc1\n\t"
        "v_mfma_f32_16x16x16_bf16 v[216:219], v[108:109], v[170:171], v[216:219]\n\t"
        "v_mfma_f32_16x16x16_bf16 v[220:223], v[110:111], v[170:171], v[220:223]\n\t"
        "v_mfma_f32_16x16x16_bf16 v[224:227], v[112:113], v[170:171], v[224:227]\n\t"
        "v_mfma_f32_16x16x16_bf16 v[228:231], v[114:115], v[170:171], v[228:231]\n\t"
        "v_mfma_f32_16x16x16_bf16 v[232:235], v[116:117], v[170:171], v[232:235]\n\t"
        "v_mfma_f32_16x16x16_bf16 v[236:239], v[118:119], v[170:171], v[236:239]\n\t"
        "v_cvt_pk_bf16_f32 v208, v208, v209\n\t"
        "v_cvt_pk_bf16_f32 v209, v210, v211\n\t"
        "v_cvt_pk_bf16_f32 v210, v212, v213\n\t"
        "v_cvt_pk_bf16_f32 v211, v214, v215\n\t"
        "buffer_store_dwordx4 v[208:211], v244, %[rd], s87 offen offset:0 sc1\n\t"
        "v_cvt_pk_bf16_f32 v216, v216, v217\n\t"
        "v_cvt_pk_bf16_f32 v217, v218, v219\n\t"
        "v_cvt_pk_bf16_f32 v218, v220, v221\n\t"
        "v_cvt_pk_bf16_f32 v219, v222, v223\n\t"
        "buffer_store_dwordx4 v[216:219], v244, %[rd], s87 offen offset:1024 sc1\n\t"
        "v_cvt_pk_bf16_f32 v224, v224, v225\n\t"
        "v_cvt_pk_bf16_f32 v225, v226, v227\n\t"
        "v_cvt_pk_bf16_f32 v226, v228, v229\n\t"
        "v_cvt_pk_bf16_f32 v227, v230, v231\n\t"
        "buffer_store_dwordx4 v[224:227], v244, %[rd], s87 offen offset:2048 sc1\n\t"
        "v_cvt_pk_bf16_f32 v232, v232, v233\n\t"
        "v_cvt_pk_bf16_f32 v233, v234, v235\n\t"
        "v_cvt_pk_bf16_f32 v234, v236, v237\n\t"
        "v_cvt_pk_bf16_f32 v235, v238, v239\n\t"
        "buffer_store_dwordx4 v[232:235], v244, %[rd], s87 offen offset:3072 sc1\n\t"
        "s_mov_b32 s45, s44\n\t"
        "s_mov_b32 s44, s37\n\t"
        "s_add_u32 s80, s37, 4\n\t"
        "s_cmp_gt_u32 s40, s80\n\t"
        "s_cselect_b32 s80, s46, 1\n\t"
        "s_cmp_eq_u32 s80, 0\n\t"
        "s_cbranch_scc1 .La24%=\n\t"
        "s_waitcnt vmcnt(0)\n\t"
        "v_readlane_b32 s84, v242, 0\n\t"
        "v_readlane_b32 s85, v242, 32\n\t"
        "v_mov_b32_e32 v243, 32\n\t"
        "s_and_b32 s80, s45, 3\n\t"
        "s_lshl_b32 s80, s80, 12\n\t"
        "s_lshr_b32 s81, s45, 2\n\t"
        "s_lshl_b32 s81, s81, 3\n\t"
        "s_add_u32 s80, s80, s81\n\t"
        "s_lshr_b32 s82, s45, 1\n\t"
        "s_xnor_b32 s81, s45, s82\n\t"
        "s_and_b32 s81, s81, 1\n\t"
        "s_lshl_b32 s81, s81, 13\n\t"
        "s_lshl_b32 s82, s82, 2\n\t"
        "s_add_u32 s81, s81, s82\n\t"
        "s_cmp_lt_i32 s45, 60\n\t"
        "s_cselect_b32 s80, s80, s81\n\t"
        "s_cselect_b32 s81, 4, 0x1000\n\t"
        "s_add_u32 s81, s81, s80\n\t"
        "s_cmp_eq_u32 s84, 31\n\t"
        "s_cselect_b32 s80, s80, s36\n\t"
        "s_cmp_eq_u32 s85, 31\n\t"
        "s_cselect_b32 s81, s81, s36\n\t"
        "s_cmp_lt_i32 s45, 0\n\t"
        "s_cselect_b32 s80, s36, s80\n\t"
        "s_cselect_b32 s81, s36, s81\n\t"
        "buffer_store_dword v243, v251, %[rd], s80 offen sc1\n\t"
        "buffer_store_dword v243, v251, %[rd], s81 offen sc1\n\t"
        "s_lshl_b32 s80, s44, 7\n\t"
        "s_cmp_lt_i32 s44, 0\n\t"
        "s_cselect_b32 s80, s36, s80\n\t"
        "v_mov_b32_e32 v242, 1\n\t"
        "buffer_atomic_add v242, v250, %[rd], s80 offen sc0 sc1\n\t"
        "s_waitcnt vmcnt(0)\n\t"
        "v_readlane_b32 s84, v242, 0\n\t"
        "v_readlane_b32 s85, v242, 32\n\t"
        "v_mov_b32_e32 v243, 32\n\t"
        "s_and_b32 s80, s44, 3\n\t"
        "s_lshl_b32 s80, s80, 12\n\t"
        "s_lshr_b32 s81, s44, 2\n\t"
        "s_lshl_b32 s81, s81, 3\n\t"
        "s_add_u32 s80, s80, s81\n\t"
        "s_lshr_b32 s82, s44, 1\n\t"
        "s_xnor_b32 s81, s44, s82\n\t"
        "s_and_b32 s81, s81, 1\n\t"
        "s_lshl_b32 s81, s81, 13\n\t"
        "s_lshl_b32 s82, s82, 2\n\t"
        "s_add_u32 s81, s81, s82\n\t"
        "s_cmp_lt_i32 s44, 60\n\t"
        "s_cselect_b32 s80, s80, s81\n\t"
        "s_cselect_b32 s81, 4, 0x1000\n\t"
        "s_add_u32 s81, s81, s80\n\t"
        "s_cmp_eq_u32 s84, 31\n\t"
        "s_cselect_b32 s80, s80, s36\n\t"
        "s_cmp_eq_u32 s85, 31\n\t"
        "s_cselect_b32 s81, s81, s36\n\t"
        "s_cmp_lt_i32 s44, 0\n\t"
        "s_cselect_b32 s80, s36, s80\n\t"
        "s_cselect_b32 s81, s36, s81\n\t"
        "buffer_store_dword v243, v251, %[rd], s80 offen sc1\n\t"
        "buffer_store_dword v243, v251, %[rd], s81 offen sc1\n\t"
        "s_mov_b32 s44, -1\n\t"
        "s_mov_b32 s45, -1\n\t"
        ".La24%=:\n\t"
        "s_mov_b32 s38, s46\n\t"
        "s_mov_b32 s39, s47\n\t"
        "s_add_u32 s37, s37, 4\n\t"
        "s_cmp_lt_u32 s37, 64\n\t"
        "s_cbranch_scc1 .Ltop%=\n\t"
        "s_endpgm\n\t"
        ".Lgu%=:\n\t"
        "v_mov_b32_e32 v0, 64\n\t"
        "s_mov_b32 s80, %c[ferr]\n\t"
        "buffer_store_dword v0, off, %[rd], s80 sc1\n\t"
        "s_endpgm"
        :: [rd] "s"(rd), [w] "s"(wv), [tag] "s"(tag), [kx] "s"(kx), [qx] "s"(qx), [vx] "s"(vx), [km] "s"(km), [blk] "s"(h),
          [ferr] "i"(F_ERR), [dbg] "i"(W_DBG), [pslp] "i"(P_SLP), [lslp] "i"(L_SLP), [aslp] "i"(AT_SLP), [bslp] "i"(BT_SLP), [efrom] "i"(EARLY_FROM), [smin] "i"(STEP_MIN), "{v[244:251]}"(pc), "{v252}"(dq)
        : "memory", "vcc", "scc", "s36", "s37", "s38", "s39", "s40", "s41", "s42", "s43", "s44", "s45", "s46", "s47", "s48", "s49", "s50", "s51", "s52", "s53", "s54", "s55", "s56", "s57", "s58", "s59", "s80", "s81", "s82", "s83", "s84", "s85", "s86", "s87", "s88", "s89", "s90", "s91", "s92", "s93", "s94", "s95", "s96", "s97", "s98", "s99");
}

constexpr int OP_P = 144;
__device__ __forceinline__ void oproj_block(const Params& p, const int wv, const int nt, const int rho, const int j, const unsigned tag, const unsigned g_t0) {
    const int w = wv, l = otid() & 63, n16 = l & 15, g = l >> 4;
    const v4u rd = mk_srd(p.ws, W_TOTAL), ro = mk_srd(p.out, S * D * 2), rx = mk_srd(p.Aq, S * D * 2);
    const unsigned snd = (unsigned)((w >> 1) ^ nt) << 31, own = snd ^ 0x80000000u;
    const unsigned ep = (nt == 0 && rho == 0 && w == 0) ? F_EP : 0xF0000000u;
    const int n0 = (nt >> 1) * 64 + 4 * n16, row = 4 * w + g;
    const unsigned a_b = W_QX + ((nt & 1) * 16 + w * 4) * 8192 + l * 16, tr = (4 * g + (n16 >> 2)), tc = (n16 & 3) * 8;
    const v16u pc = {a_b, a_b + 4096, F_RDY + (rho * 32 + nt) * 128 + (l & 31) * 4, W_U + (n0 >> 2) * 16384 + row * 8, W_GATE + row * 4, (unsigned)((row * 1024 + n0) * 2),
                     W_KX + ((nt >> 1) * 4 + w) * 2048 + l * 16,
                     (unsigned)(w * 8320 + g * 256 + ((n16 + 4 * g) & 15) * 16), (unsigned)((n16 >> 2) * 1040 + (n16 & 3) * 256 + ((row + 4 * n16) & 15) * 16),
                     (unsigned)(w * 10240 + (l >> 3) * OP_P + (l & 7) * 16), w * 10240 + tr * OP_P + tc, WO_L + w * 16384 + tr * 128 + tc, 0u, 0u, 0u, 0u};
    asm volatile(
        "ds_write_b128 v185, v[192:195] offset:0\n\t"
        "ds_write_b128 v185, v[196:199] offset:1152\n\t"
        "ds_write_b128 v185, v[200:203] offset:2304\n\t"
        "ds_write_b128 v185, v[204:207] offset:3456\n\t"
        "ds_read_b64_tr_b16 a[0:1], v186 offset:0\n\t"
        "ds_read_b64_tr_b16 a[2:3], v186 offset:2304\n\t"
        "ds_read_b64_tr_b16 a[4:5], v186 offset:32\n\t"
        "ds_read_b64_tr_b16 a[6:7], v186 offset:2336\n\t"
        "ds_read_b64_tr_b16 a[8:9], v186 offset:64\n\t"
        "ds_read_b64_tr_b16 a[10:11], v186 offset:2368\n\t"
        "ds_read_b64_tr_b16 a[12:13], v186 offset:96\n\t"
        "ds_read_b64_tr_b16 a[14:15], v186 offset:2400\n\t"
        "ds_write_b128 v185, v[208:211] offset:5120\n\t"
        "ds_write_b128 v185, v[212:215] offset:6272\n\t"
        "ds_write_b128 v185, v[216:219] offset:7424\n\t"
        "ds_write_b128 v185, v[220:223] offset:8576\n\t"
        "ds_read_b64_tr_b16 a[16:17], v186 offset:5120\n\t"
        "ds_read_b64_tr_b16 a[18:19], v186 offset:7424\n\t"
        "ds_read_b64_tr_b16 a[20:21], v186 offset:5152\n\t"
        "ds_read_b64_tr_b16 a[22:23], v186 offset:7456\n\t"
        "ds_read_b64_tr_b16 a[24:25], v186 offset:5184\n\t"
        "ds_read_b64_tr_b16 a[26:27], v186 offset:7488\n\t"
        "ds_read_b64_tr_b16 a[28:29], v186 offset:5216\n\t"
        "ds_read_b64_tr_b16 a[30:31], v186 offset:7520\n\t"
        "ds_write_b128 v185, v[224:227] offset:0\n\t"
        "ds_write_b128 v185, v[228:231] offset:1152\n\t"
        "ds_write_b128 v185, v[232:235] offset:2304\n\t"
        "ds_write_b128 v185, v[236:239] offset:3456\n\t"
        "ds_read_b64_tr_b16 a[32:33], v186 offset:0\n\t"
        "ds_read_b64_tr_b16 a[34:35], v186 offset:2304\n\t"
        "ds_read_b64_tr_b16 a[36:37], v186 offset:32\n\t"
        "ds_read_b64_tr_b16 a[38:39], v186 offset:2336\n\t"
        "ds_read_b64_tr_b16 a[40:41], v186 offset:64\n\t"
        "ds_read_b64_tr_b16 a[42:43], v186 offset:2368\n\t"
        "ds_read_b64_tr_b16 a[44:45], v186 offset:96\n\t"
        "ds_read_b64_tr_b16 a[46:47], v186 offset:2400\n\t"
        "ds_write_b128 v185, v[240:243] offset:5120\n\t"
        "ds_write_b128 v185, v[244:247] offset:6272\n\t"
        "ds_write_b128 v185, v[248:251] offset:7424\n\t"
        "ds_write_b128 v185, v[252:255] offset:8576\n\t"
        "ds_read_b64_tr_b16 a[48:49], v186 offset:5120\n\t"
        "ds_read_b64_tr_b16 a[50:51], v186 offset:7424\n\t"
        "ds_read_b64_tr_b16 a[52:53], v186 offset:5152\n\t"
        "ds_read_b64_tr_b16 a[54:55], v186 offset:7456\n\t"
        "ds_read_b64_tr_b16 a[56:57], v186 offset:5184\n\t"
        "ds_read_b64_tr_b16 a[58:59], v186 offset:7488\n\t"
        "ds_read_b64_tr_b16 a[60:61], v186 offset:5216\n\t"
        "ds_read_b64_tr_b16 a[62:63], v186 offset:7520\n\t"
        "ds_read_b64_tr_b16 a[64:65], v187 offset:0\n\t"
        "ds_read_b64_tr_b16 a[66:67], v187 offset:2048\n\t"
        "ds_read_b64_tr_b16 a[68:69], v187 offset:32\n\t"
        "ds_read_b64_tr_b16 a[70:71], v187 offset:2080\n\t"
        "ds_read_b64_tr_b16 a[72:73], v187 offset:64\n\t"
        "ds_read_b64_tr_b16 a[74:75], v187 offset:2112\n\t"
        "ds_read_b64_tr_b16 a[76:77], v187 offset:96\n\t"
        "ds_read_b64_tr_b16 a[78:79], v187 offset:2144\n\t"
        "ds_read_b64_tr_b16 a[80:81], v187 offset:4096\n\t"
        "ds_read_b64_tr_b16 a[82:83], v187 offset:6144\n\t"
        "ds_read_b64_tr_b16 a[84:85], v187 offset:4128\n\t"
        "ds_read_b64_tr_b16 a[86:87], v187 offset:6176\n\t"
        "ds_read_b64_tr_b16 a[88:89], v187 offset:4160\n\t"
        "ds_read_b64_tr_b16 a[90:91], v187 offset:6208\n\t"
        "ds_read_b64_tr_b16 a[92:93], v187 offset:4192\n\t"
        "ds_read_b64_tr_b16 a[94:95], v187 offset:6240\n\t"
        "ds_read_b64_tr_b16 a[96:97], v187 offset:8192\n\t"
        "ds_read_b64_tr_b16 a[98:99], v187 offset:10240\n\t"
        "ds_read_b64_tr_b16 a[100:101], v187 offset:8224\n\t"
        "ds_read_b64_tr_b16 a[102:103], v187 offset:10272\n\t"
        "ds_read_b64_tr_b16 a[104:105], v187 offset:8256\n\t"
        "ds_read_b64_tr_b16 a[106:107], v187 offset:10304\n\t"
        "ds_read_b64_tr_b16 a[108:109], v187 offset:8288\n\t"
        "ds_read_b64_tr_b16 a[110:111], v187 offset:10336\n\t"
        "ds_read_b64_tr_b16 a[112:113], v187 offset:12288\n\t"
        "ds_read_b64_tr_b16 a[114:115], v187 offset:14336\n\t"
        "ds_read_b64_tr_b16 a[116:117], v187 offset:12320\n\t"
        "ds_read_b64_tr_b16 a[118:119], v187 offset:14368\n\t"
        "ds_read_b64_tr_b16 a[120:121], v187 offset:12352\n\t"
        "ds_read_b64_tr_b16 a[122:123], v187 offset:14400\n\t"
        "ds_read_b64_tr_b16 a[124:125], v187 offset:12384\n\t"
        "ds_read_b64_tr_b16 a[126:127], v187 offset:14432\n\t"
        "ds_write_b128 v185, a[128:131] offset:0\n\t"
        "ds_write_b128 v185, a[132:135] offset:1152\n\t"
        "ds_write_b128 v185, a[136:139] offset:2304\n\t"
        "ds_write_b128 v185, a[140:143] offset:3456\n\t"
        "s_waitcnt lgkmcnt(0)\n\t"
        "ds_read_b64_tr_b16 a[128:129], v186 offset:0\n\t"
        "ds_read_b64_tr_b16 a[130:131], v186 offset:2304\n\t"
        "ds_read_b64_tr_b16 a[132:133], v186 offset:32\n\t"
        "ds_read_b64_tr_b16 a[134:135], v186 offset:2336\n\t"
        "ds_read_b64_tr_b16 a[136:137], v186 offset:64\n\t"
        "ds_read_b64_tr_b16 a[138:139], v186 offset:2368\n\t"
        "ds_read_b64_tr_b16 a[140:141], v186 offset:96\n\t"
        "ds_read_b64_tr_b16 a[142:143], v186 offset:2400\n\t"
        "ds_write_b128 v185, a[144:147] offset:5120\n\t"
        "ds_write_b128 v185, a[148:151] offset:6272\n\t"
        "ds_write_b128 v185, a[152:155] offset:7424\n\t"
        "ds_write_b128 v185, a[156:159] offset:8576\n\t"
        "s_waitcnt lgkmcnt(0)\n\t"
        "ds_read_b64_tr_b16 a[144:145], v186 offset:5120\n\t"
        "ds_read_b64_tr_b16 a[146:147], v186 offset:7424\n\t"
        "ds_read_b64_tr_b16 a[148:149], v186 offset:5152\n\t"
        "ds_read_b64_tr_b16 a[150:151], v186 offset:7456\n\t"
        "ds_read_b64_tr_b16 a[152:153], v186 offset:5184\n\t"
        "ds_read_b64_tr_b16 a[154:155], v186 offset:7488\n\t"
        "ds_read_b64_tr_b16 a[156:157], v186 offset:5216\n\t"
        "ds_read_b64_tr_b16 a[158:159], v186 offset:7520\n\t"
        "ds_write_b128 v185, a[160:163] offset:0\n\t"
        "ds_write_b128 v185, a[164:167] offset:1152\n\t"
        "ds_write_b128 v185, a[168:171] offset:2304\n\t"
        "ds_write_b128 v185, a[172:175] offset:3456\n\t"
        "s_waitcnt lgkmcnt(0)\n\t"
        "ds_read_b64_tr_b16 a[160:161], v186 offset:0\n\t"
        "ds_read_b64_tr_b16 a[162:163], v186 offset:2304\n\t"
        "ds_read_b64_tr_b16 a[164:165], v186 offset:32\n\t"
        "ds_read_b64_tr_b16 a[166:167], v186 offset:2336\n\t"
        "ds_read_b64_tr_b16 a[168:169], v186 offset:64\n\t"
        "ds_read_b64_tr_b16 a[170:171], v186 offset:2368\n\t"
        "ds_read_b64_tr_b16 a[172:173], v186 offset:96\n\t"
        "ds_read_b64_tr_b16 a[174:175], v186 offset:2400\n\t"
        "ds_write_b128 v185, a[176:179] offset:5120\n\t"
        "ds_write_b128 v185, a[180:183] offset:6272\n\t"
        "ds_write_b128 v185, a[184:187] offset:7424\n\t"
        "ds_write_b128 v185, a[188:191] offset:8576\n\t"
        "s_waitcnt lgkmcnt(0)\n\t"
        "ds_read_b64_tr_b16 a[176:177], v186 offset:5120\n\t"
        "ds_read_b64_tr_b16 a[178:179], v186 offset:7424\n\t"
        "ds_read_b64_tr_b16 a[180:181], v186 offset:5152\n\t"
        "ds_read_b64_tr_b16 a[182:183], v186 offset:7456\n\t"
        "ds_read_b64_tr_b16 a[184:185], v186 offset:5184\n\t"
        "ds_read_b64_tr_b16 a[186:187], v186 offset:7488\n\t"
        "ds_read_b64_tr_b16 a[188:189], v186 offset:5216\n\t"
        "ds_read_b64_tr_b16 a[190:191], v186 offset:7520\n\t"
        "ds_write_b128 v185, a[192:195] offset:0\n\t"
        "ds_write_b128 v185, a[196:199] offset:1152\n\t"
        "ds_write_b128 v185, a[200:203] offset:2304\n\t"
        "ds_write_b128 v185, a[204:207] offset:3456\n\t"
        "s_waitcnt lgkmcnt(0)\n\t"
        "ds_read_b64_tr_b16 a[192:193], v186 offset:0\n\t"
        "ds_read_b64_tr_b16 a[194:195], v186 offset:2304\n\t"
        "ds_read_b64_tr_b16 a[196:197], v186 offset:32\n\t"
        "ds_read_b64_tr_b16 a[198:199], v186 offset:2336\n\t"
        "ds_read_b64_tr_b16 a[200:201], v186 offset:64\n\t"
        "ds_read_b64_tr_b16 a[202:203], v186 offset:2368\n\t"
        "ds_read_b64_tr_b16 a[204:205], v186 offset:96\n\t"
        "ds_read_b64_tr_b16 a[206:207], v186 offset:2400\n\t"
        "ds_write_b128 v185, a[208:211] offset:5120\n\t"
        "ds_write_b128 v185, a[212:215] offset:6272\n\t"
        "ds_write_b128 v185, a[216:219] offset:7424\n\t"
        "ds_write_b128 v185, a[220:223] offset:8576\n\t"
        "s_waitcnt lgkmcnt(0)\n\t"
        "ds_read_b64_tr_b16 a[208:209], v186 offset:5120\n\t"
        "ds_read_b64_tr_b16 a[210:211], v186 offset:7424\n\t"
        "ds_read_b64_tr_b16 a[212:213], v186 offset:5152\n\t"
        "ds_read_b64_tr_b16 a[214:215], v186 offset:7456\n\t"
        "ds_read_b64_tr_b16 a[216:217], v186 offset:5184\n\t"
        "ds_read_b64_tr_b16 a[218:219], v186 offset:7488\n\t"
        "ds_read_b64_tr_b16 a[220:221], v186 offset:5216\n\t"
        "ds_read_b64_tr_b16 a[222:223], v186 offset:7520\n\t"
        "ds_write_b128 v185, a[224:227] offset:0\n\t"
        "ds_write_b128 v185, a[228:231] offset:1152\n\t"
        "ds_write_b128 v185, a[232:235] offset:2304\n\t"
        "ds_write_b128 v185, a[236:239] offset:3456\n\t"
        "s_waitcnt lgkmcnt(0)\n\t"
        "ds_read_b64_tr_b16 a[224:225], v186 offset:0\n\t"
        "ds_read_b64_tr_b16 a[226:227], v186 offset:2304\n\t"
        "ds_read_b64_tr_b16 a[228:229], v186 offset:32\n\t"
        "ds_read_b64_tr_b16 a[230:231], v186 offset:2336\n\t"
        "ds_read_b64_tr_b16 a[232:233], v186 offset:64\n\t"
        "ds_read_b64_tr_b16 a[234:235], v186 offset:2368\n\t"
        "ds_read_b64_tr_b16 a[236:237], v186 offset:96\n\t"
        "ds_read_b64_tr_b16 a[238:239], v186 offset:2400\n\t"
        "ds_write_b128 v185, a[240:243] offset:5120\n\t"
        "ds_write_b128 v185, a[244:247] offset:6272\n\t"
        "ds_write_b128 v185, a[248:251] offset:7424\n\t"
        "ds_write_b128 v185, a[252:255] offset:8576\n\t"
        "s_waitcnt lgkmcnt(0)\n\t"
        "ds_read_b64_tr_b16 a[240:241], v186 offset:5120\n\t"
        "ds_read_b64_tr_b16 a[242:243], v186 offset:7424\n\t"
        "ds_read_b64_tr_b16 a[244:245], v186 offset:5152\n\t"
        "ds_read_b64_tr_b16 a[246:247], v186 offset:7456\n\t"
        "ds_read_b64_tr_b16 a[248:249], v186 offset:5184\n\t"
        "ds_read_b64_tr_b16 a[250:251], v186 offset:7488\n\t"
        "ds_read_b64_tr_b16 a[252:253], v186 offset:5216\n\t"
        "ds_read_b64_tr_b16 a[254:255], v186 offset:7520\n\t"
        "s_waitcnt lgkmcnt(0)\n\t"
        "v_mov_b32_e32 v240, v176\n\t"
        "v_mov_b32_e32 v241, v177\n\t"
        "v_mov_b32_e32 v242, v178\n\t"
        "v_mov_b32_e32 v243, v179\n\t"
        "v_mov_b32_e32 v244, v180\n\t"
        "v_mov_b32_e32 v245, v181\n\t"
        "v_mov_b32_e32 v246, v182\n\t"
        "v_mov_b32_e32 v247, v183\n\t"
        "v_mov_b32_e32 v248, v184\n\t"
        "v_mov_b32_e32 v205, %[tag]\n\t"
        "v_mov_b32_e32 v207, %[tag]\n\t"
        "v_mov_b32_e32 v209, %[tag]\n\t"
        "v_mov_b32_e32 v211, %[tag]\n\t"
        "v_mov_b32_e32 v213, %[tag]\n\t"
        "v_mov_b32_e32 v215, %[tag]\n\t"
        "v_mov_b32_e32 v217, %[tag]\n\t"
        "v_mov_b32_e32 v219, %[tag]\n\t"
        "s_mov_b32 s36, 0xf0000000\n\t"
        "s_mov_b32 s37, 0\n\t"
        "s_mov_b32 s38, -1\n\t"
        "s_mov_b32 s39, -1\n\t"
        "s_mov_b32 s40, 0\n\t"
        "s_mov_b32 s43, 0\n\t"
        "s_lshl_b32 s45, %[w], 10\n\t"
        "s_add_u32 s45, s45, 69632\n\t"
        "s_lshl_b32 s44, %[rho], 1\n\t"
        "s_mov_b64 exec, 0\n\t"
        "s_branch .Lhk0%=\n\t"
        ".Lhr%=:\n\t"
        "s_mov_b64 exec, -1\n\t"
        "s_barrier\n\t"
        "s_mov_b32 s97, 0\n\t"
        "v_and_b32_e32 v186, 0x7c, v242\n\t"
        "s_mov_b32 s80, %c[fprep]\n\t"
        "s_mov_b32 s81, %c[fconv]\n\t"
        ".Lo1%=:\n\t"
        "buffer_load_dword v187, v186, %[rd], s80 offen sc1\n\t"
        "s_add_u32 s97, s97, 1\n\t"
        "s_cmp_lt_u32 s97, 0x200000\n\t"
        "s_cbranch_scc0 .Lgu%=\n\t"
        "s_waitcnt vmcnt(0)\n\t"
        "v_cmp_ne_u32_e32 vcc, %[tag], v187\n\t"
        "s_nop 3\n\t"
        "s_cbranch_vccz .Lo2%=\n\t"
        "s_sleep 8\n\t"
        "s_branch .Lo1%=\n\t"
        ".Lo2%=:\n\t"
        "buffer_load_dword v187, off, %[rd], s81 sc1\n\t"
        "s_add_u32 s97, s97, 1\n\t"
        "s_cmp_lt_u32 s97, 0x200000\n\t"
        "s_cbranch_scc0 .Lgu%=\n\t"
        "s_waitcnt vmcnt(0)\n\t"
        "v_cmp_gt_u32_e32 vcc, 0x100, v187\n\t"
        "s_nop 3\n\t"
        "s_cbranch_vccz .Lo3%=\n\t"
        "s_sleep 8\n\t"
        "s_branch .Lo2%=\n\t"
        ".Lo3%=:\n\t"
        ".Lnew%=:\n\t"
        "s_lshl_b32 s85, s37, 1\n\t"
        "s_and_b32 s80, s40, 0x3fffffff\n\t"
        "s_lshr_b32 s80, s80, s85\n\t"
        "s_andn2_b32 s80, 3, s80\n\t"
        "s_cbranch_scc0 .Lgo%=\n\t"
        "s_cmp_lt_i32 s39, 0\n\t"
        "s_cbranch_scc1 .Lo4%=\n\t"
        "s_waitcnt vmcnt(0)\n\t"
        "s_add_i32 s80, s39, 0\n\t"
        "s_lshl_b32 s80, s80, 15\n\t"
        "s_or_b32 s81, s39, %[snd]\n\t"
        "s_cmp_lt_i32 s81, 0\n\t"
        "s_cselect_b32 s83, s36, s80\n\t"
        "s_cbranch_scc1 .Lo7%=\n\t"
        "s_mov_b32 s97, 0\n\t"
        ".Lo5%=:\n\t"
        "v_xor_b32_e32 v186, %[tag], v161\n\t"
        "v_xor_b32_e32 v187, %[tag], v163\n\t"
        "v_xor_b32_e32 v188, %[tag], v165\n\t"
        "v_or3_b32 v186, v186, v187, v188\n\t"
        "v_xor_b32_e32 v187, %[tag], v167\n\t"
        "v_or_b32_e32 v186, v186, v187\n\t"
        "v_cmp_ne_u32_e32 vcc, 0, v186\n\t"
        "v_sub_f32_e32 v198, 1.0, v184\n\t"
        "v_add_f32_e32 v190, v152, v160\n\t"
        "v_add_f32_e32 v191, v153, v162\n\t"
        "v_add_f32_e32 v192, v154, v164\n\t"
        "v_add_f32_e32 v193, v155, v166\n\t"
        "s_cbranch_vccz .Lo6%=\n\t"
        "s_sleep %c[xslp]\n\t"
        "s_lshl_b32 s82, s83, 2\n\t"
        "buffer_load_dwordx4 v[160:163], v246, %[rd], s82 offen offset:0 sc1\n\t"
        "buffer_load_dwordx4 v[164:167], v246, %[rd], s82 offen offset:1024 sc1\n\t"
        "s_add_u32 s97, s97, 1\n\t"
        "s_cmp_lt_u32 s97, 0x200000\n\t"
        "s_cbranch_scc0 .Lgu%=\n\t"
        "s_waitcnt vmcnt(0)\n\t"
        "s_branch .Lo5%=\n\t"
        ".Lo6%=:\n\t"
        "v_cvt_pk_bf16_f32 v198, 0, v198\n\t"
        "v_cvt_pk_bf16_f32 v190, 0, v190\n\t"
        "v_cvt_pk_bf16_f32 v191, 0, v191\n\t"
        "v_cvt_pk_bf16_f32 v192, 0, v192\n\t"
        "v_cvt_pk_bf16_f32 v193, 0, v193\n\t"
        "v_lshlrev_b32_e32 v186, 16, v180\n\t"
        "v_lshlrev_b32_e32 v194, 16, v176\n\t"
        "v_and_b32_e32 v187, 0xffff0000, v180\n\t"
        "v_and_b32_e32 v195, 0xffff0000, v176\n\t"
        "v_lshlrev_b32_e32 v188, 16, v181\n\t"
        "v_lshlrev_b32_e32 v196, 16, v177\n\t"
        "v_and_b32_e32 v189, 0xffff0000, v181\n\t"
        "v_and_b32_e32 v197, 0xffff0000, v177\n\t"
        "v_mul_f32_e32 v186, v186, v194\n\t"
        "v_mul_f32_e32 v187, v187, v195\n\t"
        "v_mul_f32_e32 v188, v188, v196\n\t"
        "v_mul_f32_e32 v189, v189, v197\n\t"
        "v_mul_f32_e32 v190, v198, v190\n\t"
        "v_mul_f32_e32 v191, v198, v191\n\t"
        "v_mul_f32_e32 v192, v198, v192\n\t"
        "v_mul_f32_e32 v193, v198, v193\n\t"
        "v_cvt_pk_bf16_f32 v186, 0, v186\n\t"
        "v_cvt_pk_bf16_f32 v187, 0, v187\n\t"
        "v_cvt_pk_bf16_f32 v188, 0, v188\n\t"
        "v_cvt_pk_bf16_f32 v189, 0, v189\n\t"
        "v_cvt_pk_bf16_f32 v190, 0, v190\n\t"
        "v_cvt_pk_bf16_f32 v191, 0, v191\n\t"
        "v_cvt_pk_bf16_f32 v192, 0, v192\n\t"
        "v_cvt_pk_bf16_f32 v193, 0, v193\n\t"
        "v_mul_f32_e32 v186, v184, v186\n\t"
        "v_mul_f32_e32 v187, v184, v187\n\t"
        "v_mul_f32_e32 v188, v184, v188\n\t"
        "v_mul_f32_e32 v189, v184, v189\n\t"
        "v_cvt_pk_bf16_f32 v186, 0, v186\n\t"
        "v_cvt_pk_bf16_f32 v187, 0, v187\n\t"
        "v_cvt_pk_bf16_f32 v188, 0, v188\n\t"
        "v_cvt_pk_bf16_f32 v189, 0, v189\n\t"
        "v_add_f32_e32 v186, v186, v190\n\t"
        "v_add_f32_e32 v187, v187, v191\n\t"
        "v_add_f32_e32 v188, v188, v192\n\t"
        "v_add_f32_e32 v189, v189, v193\n\t"
        "v_cvt_pk_bf16_f32 v200, v186, v187\n\t"
        "v_cvt_pk_bf16_f32 v201, v188, v189\n\t"
        ".Lo7%=:\n\t"
        "buffer_store_dwordx2 v[200:201], v245, %[ro], s83 offen\n\t"
        "s_add_i32 s80, s39, 1\n\t"
        "s_lshl_b32 s80, s80, 15\n\t"
        "s_or_b32 s81, s39, %[snd]\n\t"
        "s_cmp_lt_i32 s81, 0\n\t"
        "s_cselect_b32 s83, s36, s80\n\t"
        "s_cbranch_scc1 .Lo10%=\n\t"
        "s_mov_b32 s97, 0\n\t"
        ".Lo8%=:\n\t"
        "v_xor_b32_e32 v186, %[tag], v169\n\t"
        "v_xor_b32_e32 v187, %[tag], v171\n\t"
        "v_xor_b32_e32 v188, %[tag], v173\n\t"
        "v_or3_b32 v186, v186, v187, v188\n\t"
        "v_xor_b32_e32 v187, %[tag], v175\n\t"
        "v_or_b32_e32 v186, v186, v187\n\t"
        "v_cmp_ne_u32_e32 vcc, 0, v186\n\t"
        "v_sub_f32_e32 v198, 1.0, v185\n\t"
        "v_add_f32_e32 v190, v156, v168\n\t"
        "v_add_f32_e32 v191, v157, v170\n\t"
        "v_add_f32_e32 v192, v158, v172\n\t"
        "v_add_f32_e32 v193, v159, v174\n\t"
        "s_cbranch_vccz .Lo9%=\n\t"
        "s_sleep %c[xslp]\n\t"
        "s_lshl_b32 s82, s83, 2\n\t"
        "buffer_load_dwordx4 v[168:171], v246, %[rd], s82 offen offset:0 sc1\n\t"
        "buffer_load_dwordx4 v[172:175], v246, %[rd], s82 offen offset:1024 sc1\n\t"
        "s_add_u32 s97, s97, 1\n\t"
        "s_cmp_lt_u32 s97, 0x200000\n\t"
        "s_cbranch_scc0 .Lgu%=\n\t"
        "s_waitcnt vmcnt(0)\n\t"
        "s_branch .Lo8%=\n\t"
        ".Lo9%=:\n\t"
        "v_cvt_pk_bf16_f32 v198, 0, v198\n\t"
        "v_cvt_pk_bf16_f32 v190, 0, v190\n\t"
        "v_cvt_pk_bf16_f32 v191, 0, v191\n\t"
        "v_cvt_pk_bf16_f32 v192, 0, v192\n\t"
        "v_cvt_pk_bf16_f32 v193, 0, v193\n\t"
        "v_lshlrev_b32_e32 v186, 16, v182\n\t"
        "v_lshlrev_b32_e32 v194, 16, v178\n\t"
        "v_and_b32_e32 v187, 0xffff0000, v182\n\t"
        "v_and_b32_e32 v195, 0xffff0000, v178\n\t"
        "v_lshlrev_b32_e32 v188, 16, v183\n\t"
        "v_lshlrev_b32_e32 v196, 16, v179\n\t"
        "v_and_b32_e32 v189, 0xffff0000, v183\n\t"
        "v_and_b32_e32 v197, 0xffff0000, v179\n\t"
        "v_mul_f32_e32 v186, v186, v194\n\t"
        "v_mul_f32_e32 v187, v187, v195\n\t"
        "v_mul_f32_e32 v188, v188, v196\n\t"
        "v_mul_f32_e32 v189, v189, v197\n\t"
        "v_mul_f32_e32 v190, v198, v190\n\t"
        "v_mul_f32_e32 v191, v198, v191\n\t"
        "v_mul_f32_e32 v192, v198, v192\n\t"
        "v_mul_f32_e32 v193, v198, v193\n\t"
        "v_cvt_pk_bf16_f32 v186, 0, v186\n\t"
        "v_cvt_pk_bf16_f32 v187, 0, v187\n\t"
        "v_cvt_pk_bf16_f32 v188, 0, v188\n\t"
        "v_cvt_pk_bf16_f32 v189, 0, v189\n\t"
        "v_cvt_pk_bf16_f32 v190, 0, v190\n\t"
        "v_cvt_pk_bf16_f32 v191, 0, v191\n\t"
        "v_cvt_pk_bf16_f32 v192, 0, v192\n\t"
        "v_cvt_pk_bf16_f32 v193, 0, v193\n\t"
        "v_mul_f32_e32 v186, v185, v186\n\t"
        "v_mul_f32_e32 v187, v185, v187\n\t"
        "v_mul_f32_e32 v188, v185, v188\n\t"
        "v_mul_f32_e32 v189, v185, v189\n\t"
        "v_cvt_pk_bf16_f32 v186, 0, v186\n\t"
        "v_cvt_pk_bf16_f32 v187, 0, v187\n\t"
        "v_cvt_pk_bf16_f32 v188, 0, v188\n\t"
        "v_cvt_pk_bf16_f32 v189, 0, v189\n\t"
        "v_add_f32_e32 v186, v186, v190\n\t"
        "v_add_f32_e32 v187, v187, v191\n\t"
        "v_add_f32_e32 v188, v188, v192\n\t"
        "v_add_f32_e32 v189, v189, v193\n\t"
        "v_cvt_pk_bf16_f32 v200, v186, v187\n\t"
        "v_cvt_pk_bf16_f32 v201, v188, v189\n\t"
        ".Lo10%=:\n\t"
        "buffer_store_dwordx2 v[200:201], v245, %[ro], s83 offen\n\t"
        "s_mov_b32 s39, -1\n\t"
        ".Lo4%=:\n\t"
        "s_cmp_eq_u32 s37, 15\n\t"
        "s_cbranch_scc1 .Lsol%=\n\t"
        "s_mov_b32 s97, 0\n\t"
        ".Lo11%=:\n\t"
        "s_lshr_b32 s80, s40, s85\n\t"
        "s_andn2_b32 s80, 3, s80\n\t"
        "s_cbranch_scc0 .Lo12%=\n\t"
        "s_cmp_eq_u32 s97, 0\n\t"
        "s_cbranch_scc1 .Lo11%=x\n\t"
        "s_sleep %c[oslp]\n\t"
        ".Lo11%=x:\n\t"
        "buffer_load_dword v220, v242, %[rd], 0 offen sc1\n\t"
        "s_add_u32 s97, s97, 1\n\t"
        "s_cmp_lt_u32 s97, 0x200000\n\t"
        "s_cbranch_scc0 .Lgu%=\n\t"
        "s_waitcnt vmcnt(0)\n\t"
        "v_cmp_eq_u32_e32 vcc, 32, v220\n\t"
        "s_nop 1\n\t"
        "s_or_b32 s40, s40, vcc_lo\n\t"
        "s_branch .Lo11%=\n\t"
        ".Lo12%=:\n\t"
        ".Lgo%=:\n\t"
        "s_lshl_b32 s84, s44, 17\n\t"
        "buffer_load_dwordx4 v[0:3], v240, %[rd], s84 offen offset:0 sc1\n\t"
        "buffer_load_dwordx4 v[4:7], v240, %[rd], s84 offen offset:1024 sc1\n\t"
        "buffer_load_dwordx4 v[8:11], v240, %[rd], s84 offen offset:2048 sc1\n\t"
        "buffer_load_dwordx4 v[12:15], v240, %[rd], s84 offen offset:3072 sc1\n\t"
        "s_add_u32 s84, s84, 8192\n\t"
        "buffer_load_dwordx4 v[16:19], v240, %[rd], s84 offen offset:0 sc1\n\t"
        "buffer_load_dwordx4 v[20:23], v240, %[rd], s84 offen offset:1024 sc1\n\t"
        "buffer_load_dwordx4 v[24:27], v240, %[rd], s84 offen offset:2048 sc1\n\t"
        "buffer_load_dwordx4 v[28:31], v240, %[rd], s84 offen offset:3072 sc1\n\t"
        "s_add_u32 s84, s84, 8192\n\t"
        "buffer_load_dwordx4 v[32:35], v240, %[rd], s84 offen offset:0 sc1\n\t"
        "buffer_load_dwordx4 v[36:39], v240, %[rd], s84 offen offset:1024 sc1\n\t"
        "buffer_load_dwordx4 v[40:43], v240, %[rd], s84 offen offset:2048 sc1\n\t"
        "buffer_load_dwordx4 v[44:47], v240, %[rd], s84 offen offset:3072 sc1\n\t"
        "s_add_u32 s84, s84, 8192\n\t"
        "buffer_load_dwordx4 v[48:51], v240, %[rd], s84 offen offset:0 sc1\n\t"
        "buffer_load_dwordx4 v[52:55], v240, %[rd], s84 offen offset:1024 sc1\n\t"
        "buffer_load_dwordx4 v[56:59], v240, %[rd], s84 offen offset:2048 sc1\n\t"
        "buffer_load_dwordx4 v[60:63], v240, %[rd], s84 offen offset:3072 sc1\n\t"
        "s_sub_u32 s84, s84, 20480\n\t"
        "buffer_load_dwordx4 v[64:67], v240, %[rd], s84 offen offset:0 sc1\n\t"
        "buffer_load_dwordx4 v[68:71], v240, %[rd], s84 offen offset:1024 sc1\n\t"
        "buffer_load_dwordx4 v[72:75], v240, %[rd], s84 offen offset:2048 sc1\n\t"
        "buffer_load_dwordx4 v[76:79], v240, %[rd], s84 offen offset:3072 sc1\n\t"
        "s_add_u32 s84, s84, 8192\n\t"
        "buffer_load_dwordx4 v[80:83], v240, %[rd], s84 offen offset:0 sc1\n\t"
        "buffer_load_dwordx4 v[84:87], v240, %[rd], s84 offen offset:1024 sc1\n\t"
        "buffer_load_dwordx4 v[88:91], v240, %[rd], s84 offen offset:2048 sc1\n\t"
        "buffer_load_dwordx4 v[92:95], v240, %[rd], s84 offen offset:3072 sc1\n\t"
        "s_add_u32 s84, s84, 8192\n\t"
        "buffer_load_dwordx4 v[96:99], v240, %[rd], s84 offen offset:0 sc1\n\t"
        "buffer_load_dwordx4 v[100:103], v240, %[rd], s84 offen offset:1024 sc1\n\t"
        "buffer_load_dwordx4 v[104:107], v240, %[rd], s84 offen offset:2048 sc1\n\t"
        "buffer_load_dwordx4 v[108:111], v240, %[rd], s84 offen offset:3072 sc1\n\t"
        "s_add_u32 s84, s84, 8192\n\t"
        "buffer_load_dwordx4 v[112:115], v240, %[rd], s84 offen offset:0 sc1\n\t"
        "buffer_load_dwordx4 v[116:119], v240, %[rd], s84 offen offset:1024 sc1\n\t"
        "buffer_load_dwordx4 v[120:123], v240, %[rd], s84 offen offset:2048 sc1\n\t"
        "buffer_load_dwordx4 v[124:127], v240, %[rd], s84 offen offset:3072 sc1\n\t"
        "s_add_u32 s84, s84, 8192\n\t"
        "buffer_store_dword v0, off, %[ro], s36\n\t"
        "buffer_store_dword v0, off, %[ro], s36\n\t"
        "buffer_store_dword v0, off, %[ro], s36\n\t"
        "buffer_store_dword v0, off, %[ro], s36\n\t"
        "buffer_store_dword v0, off, %[ro], s36\n\t"
        "buffer_store_dword v0, off, %[ro], s36\n\t"
        "buffer_store_dword v0, off, %[ro], s36\n\t"
        "buffer_store_dword v0, off, %[ro], s36\n\t"
        "buffer_store_dword v0, off, %[ro], s36\n\t"
        "buffer_store_dword v0, off, %[ro], s36\n\t"
        "buffer_store_dword v0, off, %[ro], s36\n\t"
        "buffer_store_dword v0, off, %[ro], s36\n\t"
        "buffer_store_dword v0, off, %[ro], s36\n\t"
        "buffer_store_dword v0, off, %[ro], s36\n\t"
        "buffer_store_dword v0, off, %[ro], s36\n\t"
        "buffer_store_dword v0, off, %[ro], s36\n\t"
        "buffer_store_dword v0, off, %[ro], s36\n\t"
        ".Lrnd%=:\n\t"
        "s_lshl_b32 s85, s37, 1\n\t"
        "s_and_b32 s86, s40, 0x3fffffff\n\t"
        "s_lshr_b32 s86, s86, s85\n\t"
        "s_andn2_b32 s80, 12, s86\n\t"
        "s_cselect_b32 s41, 0, 1\n\t"
        "s_add_u32 s84, s44, 8\n\t"
        "s_lshl_b32 s84, s84, 17\n\t"
        "s_cmp_eq_u32 s41, 0\n\t"
        "s_cselect_b32 s84, s36, s84\n\t"
        "s_cselect_b32 s87, 0, s36\n\t"
        "s_andn2_b32 s80, 48, s86\n\t"
        "s_cselect_b32 s80, 0, s36\n\t"
        "v_add_u32_e32 v222, s43, v247\n\t"
        "s_sub_u32 s81, 33280, s43\n\t"
        "v_add_u32_e32 v223, s81, v248\n\t"
        "buffer_load_dword v220, v242, %[rd], s80 offen sc1\n\t"
        "s_waitcnt vmcnt(49)\n\t"
        "v_mfma_f32_16x16x32_bf16 v[128:131], a[128:131], v[0:3], 0\n\t"
        "ds_read_b128 v[186:189], v223 offset:0\n\t"
        "ds_read_b128 v[190:193], v223 offset:8320\n\t"
        "v_mfma_f32_16x16x32_bf16 v[132:135], a[132:135], v[0:3], 0\n\t"
        "ds_read_b128 v[194:197], v223 offset:16640\n\t"
        "ds_read_b128 v[198:201], v223 offset:24960\n\t"
        "v_mfma_f32_16x16x32_bf16 v[136:139], a[136:139], v[0:3], 0\n\t"
        "s_nop 0\n\t"
        "s_nop 0\n\t"
        "v_mfma_f32_16x16x32_bf16 v[140:143], a[140:143], v[0:3], 0\n\t"
        "s_nop 0\n\t"
        "s_nop 0\n\t"
        "buffer_load_dwordx4 v[0:3], v240, %[rd], s84 offen offset:0 sc1\n\t"
        "s_waitcnt vmcnt(49)\n\t"
        "v_mfma_f32_16x16x32_bf16 v[128:131], a[144:147], v[4:7], v[128:131]\n\t"
        "s_nop 0\n\t"
        "s_nop 0\n\t"
        "v_mfma_f32_16x16x32_bf16 v[132:135], a[148:151], v[4:7], v[132:135]\n\t"
        "s_waitcnt lgkmcnt(0)\n\t"
        "v_add_f32_e32 v186, v186, v190\n\t"
        "v_mfma_f32_16x16x32_bf16 v[136:139], a[152:155], v[4:7], v[136:139]\n\t"
        "v_add_f32_e32 v187, v187, v191\n\t"
        "v_add_f32_e32 v188, v188, v192\n\t"
        "v_mfma_f32_16x16x32_bf16 v[140:143], a[156:159], v[4:7], v[140:143]\n\t"
        "v_add_f32_e32 v189, v189, v193\n\t"
        "v_add_f32_e32 v194, v194, v198\n\t"
        "buffer_load_dwordx4 v[4:7], v240, %[rd], s84 offen offset:1024 sc1\n\t"
        "s_waitcnt vmcnt(49)\n\t"
        "v_mfma_f32_16x16x32_bf16 v[128:131], a[160:163], v[8:11], v[128:131]\n\t"
        "v_add_f32_e32 v195, v195, v199\n\t"
        "v_add_f32_e32 v196, v196, v200\n\t"
        "v_mfma_f32_16x16x32_bf16 v[132:135], a[164:167], v[8:11], v[132:135]\n\t"
        "v_add_f32_e32 v197, v197, v201\n\t"
        "v_add_f32_e32 v204, v186, v194\n\t"
        "v_mfma_f32_16x16x32_bf16 v[136:139], a[168:171], v[8:11], v[136:139]\n\t"
        "v_add_f32_e32 v206, v187, v195\n\t"
        "v_add_f32_e32 v208, v188, v196\n\t"
        "v_mfma_f32_16x16x32_bf16 v[140:143], a[172:175], v[8:11], v[140:143]\n\t"
        "v_add_f32_e32 v210, v189, v197\n\t"
        "s_add_i32 s80, s38, 0\n\t"
        "buffer_load_dwordx4 v[8:11], v240, %[rd], s84 offen offset:2048 sc1\n\t"
        "s_waitcnt vmcnt(47)\n\t"
        "v_mfma_f32_16x16x32_bf16 v[128:131], a[176:179], v[12:15], v[128:131]\n\t"
        "s_lshl_b32 s80, s80, 17\n\t"
        "s_or_b32 s81, s38, %[own]\n\t"
        "v_mfma_f32_16x16x32_bf16 v[132:135], a[180:183], v[12:15], v[132:135]\n\t"
        "s_cmp_lt_i32 s81, 0\n\t"
        "s_cselect_b32 s82, s36, s80\n\t"
        "buffer_store_dwordx4 v[204:207], v246, %[rd], s82 offen offset:0 sc1\n\t"
        "v_mfma_f32_16x16x32_bf16 v[136:139], a[184:187], v[12:15], v[136:139]\n\t"
        "buffer_store_dwordx4 v[208:211], v246, %[rd], s82 offen offset:1024 sc1\n\t"
        "v_mov_b32_e32 v144, v204\n\t"
        "v_mfma_f32_16x16x32_bf16 v[140:143], a[188:191], v[12:15], v[140:143]\n\t"
        "v_mov_b32_e32 v145, v206\n\t"
        "v_mov_b32_e32 v146, v208\n\t"
        "buffer_load_dwordx4 v[12:15], v240, %[rd], s84 offen offset:3072 sc1\n\t"
        "s_add_u32 s84, s84, 8192\n\t"
        "s_waitcnt vmcnt(49)\n\t"
        "v_mfma_f32_16x16x32_bf16 v[128:131], a[192:195], v[16:19], v[128:131]\n\t"
        "v_mov_b32_e32 v147, v210\n\t"
        "ds_read_b128 v[186:189], v223 offset:4160\n\t"
        "v_mfma_f32_16x16x32_bf16 v[132:135], a[196:199], v[16:19], v[132:135]\n\t"
        "ds_read_b128 v[190:193], v223 offset:12480\n\t"
        "ds_read_b128 v[194:197], v223 offset:20800\n\t"
        "v_mfma_f32_16x16x32_bf16 v[136:139], a[200:203], v[16:19], v[136:139]\n\t"
        "ds_read_b128 v[198:201], v223 offset:29120\n\t"
        "s_nop 0\n\t"
        "v_mfma_f32_16x16x32_bf16 v[140:143], a[204:207], v[16:19], v[140:143]\n\t"
        "s_nop 0\n\t"
        "s_nop 0\n\t"
        "buffer_load_dwordx4 v[16:19], v240, %[rd], s84 offen offset:0 sc1\n\t"
        "s_waitcnt vmcnt(49)\n\t"
        "v_mfma_f32_16x16x32_bf16 v[128:131], a[208:211], v[20:23], v[128:131]\n\t"
        "s_nop 0\n\t"
        "s_nop 0\n\t"
        "v_mfma_f32_16x16x32_bf16 v[132:135], a[212:215], v[20:23], v[132:135]\n\t"
        "s_nop 0\n\t"
        "s_waitcnt lgkmcnt(0)\n\t"
        "v_mfma_f32_16x16x32_bf16 v[136:139], a[216:219], v[20:23], v[136:139]\n\t"
        "v_add_f32_e32 v186, v186, v190\n\t"
        "v_add_f32_e32 v187, v187, v191\n\t"
        "v_mfma_f32_16x16x32_bf16 v[140:143], a[220:223], v[20:23], v[140:143]\n\t"
        "v_add_f32_e32 v188, v188, v192\n\t"
        "v_add_f32_e32 v189, v189, v193\n\t"
        "buffer_load_dwordx4 v[20:23], v240, %[rd], s84 offen offset:1024 sc1\n\t"
        "s_waitcnt vmcnt(49)\n\t"
        "v_mfma_f32_16x16x32_bf16 v[128:131], a[224:227], v[24:27], v[128:131]\n\t"
        "v_add_f32_e32 v194, v194, v198\n\t"
        "v_add_f32_e32 v195, v195, v199\n\t"
        "v_mfma_f32_16x16x32_bf16 v[132:135], a[228:231], v[24:27], v[132:135]\n\t"
        "v_add_f32_e32 v196, v196, v200\n\t"
        "v_add_f32_e32 v197, v197, v201\n\t"
        "v_mfma_f32_16x16x32_bf16 v[136:139], a[232:235], v[24:27], v[136:139]\n\t"
        "v_add_f32_e32 v212, v186, v194\n\t"
        "v_add_f32_e32 v214, v187, v195\n\t"
        "v_mfma_f32_16x16x32_bf16 v[140:143], a[236:239], v[24:27], v[140:143]\n\t"
        "v_add_f32_e32 v216, v188, v196\n\t"
        "v_add_f32_e32 v218, v189, v197\n\t"
        "buffer_load_dwordx4 v[24:27], v240, %[rd], s84 offen offset:2048 sc1\n\t"
        "s_waitcnt vmcnt(47)\n\t"
        "v_mfma_f32_16x16x32_bf16 v[128:131], a[240:243], v[28:31], v[128:131]\n\t"
        "s_add_i32 s80, s38, 1\n\t"
        "s_lshl_b32 s80, s80, 17\n\t"
        "v_mfma_f32_16x16x32_bf16 v[132:135], a[244:247], v[28:31], v[132:135]\n\t"
        "s_or_b32 s81, s38, %[own]\n\t"
        "s_cmp_lt_i32 s81, 0\n\t"
        "s_cselect_b32 s82, s36, s80\n\t"
        "v_mfma_f32_16x16x32_bf16 v[136:139], a[248:251], v[28:31], v[136:139]\n\t"
        "buffer_store_dwordx4 v[212:215], v246, %[rd], s82 offen offset:0 sc1\n\t"
        "buffer_store_dwordx4 v[216:219], v246, %[rd], s82 offen offset:1024 sc1\n\t"
        "v_mfma_f32_16x16x32_bf16 v[140:143], a[252:255], v[28:31], v[140:143]\n\t"
        "v_mov_b32_e32 v148, v212\n\t"
        "v_mov_b32_e32 v149, v214\n\t"
        "buffer_load_dwordx4 v[28:31], v240, %[rd], s84 offen offset:3072 sc1\n\t"
        "s_add_u32 s84, s84, 8192\n\t"
        "s_waitcnt vmcnt(49)\n\t"
        "v_mfma_f32_16x16x32_bf16 v[128:131], a[0:3], v[32:35], v[128:131]\n\t"
        "v_mov_b32_e32 v150, v216\n\t"
        "v_mov_b32_e32 v151, v218\n\t"
        "v_mfma_f32_16x16x32_bf16 v[132:135], a[4:7], v[32:35], v[132:135]\n\t"
        "s_waitcnt vmcnt(34)\n\t"
        "s_add_i32 s80, s39, 0\n\t"
        "v_mfma_f32_16x16x32_bf16 v[136:139], a[8:11], v[32:35], v[136:139]\n\t"
        "s_lshl_b32 s80, s80, 15\n\t"
        "s_or_b32 s81, s39, %[snd]\n\t"
        "v_mfma_f32_16x16x32_bf16 v[140:143], a[12:15], v[32:35], v[140:143]\n\t"
        "s_cmp_lt_i32 s81, 0\n\t"
        "s_cselect_b32 s83, s36, s80\n\t"
        "s_cselect_b64 s[88:89], 0, -1\n\t"
        "v_xor_b32_e32 v224, %[tag], v161\n\t"
        "buffer_load_dwordx4 v[32:35], v240, %[rd], s84 offen offset:0 sc1\n\t"
        "s_waitcnt vmcnt(49)\n\t"
        "v_mfma_f32_16x16x32_bf16 v[128:131], a[16:19], v[36:39], v[128:131]\n\t"
        "v_xor_b32_e32 v225, %[tag], v163\n\t"
        "v_xor_b32_e32 v226, %[tag], v165\n\t"
        "v_mfma_f32_16x16x32_bf16 v[132:135], a[20:23], v[36:39], v[132:135]\n\t"
        "v_or3_b32 v224, v224, v225, v226\n\t"
        "v_xor_b32_e32 v225, %[tag], v167\n\t"
        "v_mfma_f32_16x16x32_bf16 v[136:139], a[24:27], v[36:39], v[136:139]\n\t"
        "v_or_b32_e32 v224, v224, v225\n\t"
        "v_cmp_ne_u32_e32 vcc, 0, v224\n\t"
        "v_mfma_f32_16x16x32_bf16 v[140:143], a[28:31], v[36:39], v[140:143]\n\t"
        "v_sub_f32_e32 v236, 1.0, v184\n\t"
        "v_cvt_pk_bf16_f32 v236, 0, v236\n\t"
        "s_and_b64 vcc, vcc, s[88:89]\n\t"
        "s_cbranch_scc1 .Lo13%=\n\t"
        ".Lo14%=:\n\t"
        "buffer_load_dwordx4 v[36:39], v240, %[rd], s84 offen offset:1024 sc1\n\t"
        "s_waitcnt vmcnt(49)\n\t"
        "v_mfma_f32_16x16x32_bf16 v[128:131], a[32:35], v[40:43], v[128:131]\n\t"
        "v_add_f32_e32 v228, v152, v160\n\t"
        "v_add_f32_e32 v229, v153, v162\n\t"
        "v_mfma_f32_16x16x32_bf16 v[132:135], a[36:39], v[40:43], v[132:135]\n\t"
        "v_add_f32_e32 v230, v154, v164\n\t"
        "v_add_f32_e32 v231, v155, v166\n\t"
        "v_mfma_f32_16x16x32_bf16 v[136:139], a[40:43], v[40:43], v[136:139]\n\t"
        "v_cvt_pk_bf16_f32 v228, 0, v228\n\t"
        "v_cvt_pk_bf16_f32 v229, 0, v229\n\t"
        "v_mfma_f32_16x16x32_bf16 v[140:143], a[44:47], v[40:43], v[140:143]\n\t"
        "v_cvt_pk_bf16_f32 v230, 0, v230\n\t"
        "v_cvt_pk_bf16_f32 v231, 0, v231\n\t"
        "buffer_load_dwordx4 v[40:43], v240, %[rd], s84 offen offset:2048 sc1\n\t"
        "s_waitcnt vmcnt(49)\n\t"
        "v_mfma_f32_16x16x32_bf16 v[128:131], a[48:51], v[44:47], v[128:131]\n\t"
        "v_lshlrev_b32_e32 v224, 16, v180\n\t"
        "v_lshlrev_b32_e32 v232, 16, v176\n\t"
        "v_mfma_f32_16x16x32_bf16 v[132:135], a[52:55], v[44:47], v[132:135]\n\t"
        "v_and_b32_e32 v225, 0xffff0000, v180\n\t"
        "v_and_b32_e32 v233, 0xffff0000, v176\n\t"
        "v_mfma_f32_16x16x32_bf16 v[136:139], a[56:59], v[44:47], v[136:139]\n\t"
        "v_lshlrev_b32_e32 v226, 16, v181\n\t"
        "v_lshlrev_b32_e32 v234, 16, v177\n\t"
        "v_mfma_f32_16x16x32_bf16 v[140:143], a[60:63], v[44:47], v[140:143]\n\t"
        "v_and_b32_e32 v227, 0xffff0000, v181\n\t"
        "v_and_b32_e32 v235, 0xffff0000, v177\n\t"
        "buffer_load_dwordx4 v[44:47], v240, %[rd], s84 offen offset:3072 sc1\n\t"
        "s_add_u32 s84, s84, 8192\n\t"
        "s_waitcnt vmcnt(49)\n\t"
        "v_mfma_f32_16x16x32_bf16 v[128:131], a[64:67], v[48:51], v[128:131]\n\t"
        "v_mul_f32_e32 v224, v224, v232\n\t"
        "v_mul_f32_e32 v225, v225, v233\n\t"
        "v_mfma_f32_16x16x32_bf16 v[132:135], a[68:71], v[48:51], v[132:135]\n\t"
        "v_mul_f32_e32 v226, v226, v234\n\t"
        "v_mul_f32_e32 v227, v227, v235\n\t"
        "v_mfma_f32_16x16x32_bf16 v[136:139], a[72:75], v[48:51], v[136:139]\n\t"
        "v_mul_f32_e32 v228, v236, v228\n\t"
        "v_mul_f32_e32 v229, v236, v229\n\t"
        "v_mfma_f32_16x16x32_bf16 v[140:143], a[76:79], v[48:51], v[140:143]\n\t"
        "v_mul_f32_e32 v230, v236, v230\n\t"
        "v_mul_f32_e32 v231, v236, v231\n\t"
        "buffer_load_dwordx4 v[48:51], v240, %[rd], s84 offen offset:0 sc1\n\t"
        "s_waitcnt vmcnt(49)\n\t"
        "v_mfma_f32_16x16x32_bf16 v[128:131], a[80:83], v[52:55], v[128:131]\n\t"
        "v_cvt_pk_bf16_f32 v224, 0, v224\n\t"
        "v_cvt_pk_bf16_f32 v225, 0, v225\n\t"
        "v_mfma_f32_16x16x32_bf16 v[132:135], a[84:87], v[52:55], v[132:135]\n\t"
        "v_cvt_pk_bf16_f32 v226, 0, v226\n\t"
        "v_cvt_pk_bf16_f32 v227, 0, v227\n\t"
        "v_mfma_f32_16x16x32_bf16 v[136:139], a[88:91], v[52:55], v[136:139]\n\t"
        "v_cvt_pk_bf16_f32 v228, 0, v228\n\t"
        "v_cvt_pk_bf16_f32 v229, 0, v229\n\t"
        "v_mfma_f32_16x16x32_bf16 v[140:143], a[92:95], v[52:55], v[140:143]\n\t"
        "v_cvt_pk_bf16_f32 v230, 0, v230\n\t"
        "v_cvt_pk_bf16_f32 v231, 0, v231\n\t"
        "buffer_load_dwordx4 v[52:55], v240, %[rd], s84 offen offset:1024 sc1\n\t"
        "s_waitcnt vmcnt(49)\n\t"
        "v_mfma_f32_16x16x32_bf16 v[128:131], a[96:99], v[56:59], v[128:131]\n\t"
        "v_mul_f32_e32 v224, v184, v224\n\t"
        "v_mul_f32_e32 v225, v184, v225\n\t"
        "v_mfma_f32_16x16x32_bf16 v[132:135], a[100:103], v[56:59], v[132:135]\n\t"
        "v_mul_f32_e32 v226, v184, v226\n\t"
        "v_mul_f32_e32 v227, v184, v227\n\t"
        "v_mfma_f32_16x16x32_bf16 v[136:139], a[104:107], v[56:59], v[136:139]\n\t"
        "v_cvt_pk_bf16_f32 v224, 0, v224\n\t"
        "v_cvt_pk_bf16_f32 v225, 0, v225\n\t"
        "v_mfma_f32_16x16x32_bf16 v[140:143], a[108:111], v[56:59], v[140:143]\n\t"
        "v_cvt_pk_bf16_f32 v226, 0, v226\n\t"
        "v_cvt_pk_bf16_f32 v227, 0, v227\n\t"
        "buffer_load_dwordx4 v[56:59], v240, %[rd], s84 offen offset:2048 sc1\n\t"
        "s_waitcnt vmcnt(48)\n\t"
        "v_mfma_f32_16x16x32_bf16 v[128:131], a[112:115], v[60:63], v[128:131]\n\t"
        "v_add_f32_e32 v224, v224, v228\n\t"
        "v_add_f32_e32 v225, v225, v229\n\t"
        "v_mfma_f32_16x16x32_bf16 v[132:135], a[116:119], v[60:63], v[132:135]\n\t"
        "v_add_f32_e32 v226, v226, v230\n\t"
        "v_add_f32_e32 v227, v227, v231\n\t"
        "v_mfma_f32_16x16x32_bf16 v[136:139], a[120:123], v[60:63], v[136:139]\n\t"
        "v_cvt_pk_bf16_f32 v238, v224, v225\n\t"
        "v_cvt_pk_bf16_f32 v239, v226, v227\n\t"
        "v_mfma_f32_16x16x32_bf16 v[140:143], a[124:127], v[60:63], v[140:143]\n\t"
        "buffer_store_dwordx2 v[238:239], v245, %[ro], s83 offen\n\t"
        "s_add_i32 s80, s38, 0\n\t"
        "buffer_load_dwordx4 v[60:63], v240, %[rd], s84 offen offset:3072 sc1\n\t"
        "s_sub_u32 s84, s84, 20480\n\t"
        "ds_write_b128 v222, v[128:131] offset:0\n\t"
        "ds_write_b128 v222, v[132:135] offset:1040\n\t"
        "ds_write_b128 v222, v[136:139] offset:2080\n\t"
        "s_nop 0\n\t"
        "ds_write_b128 v222, v[140:143] offset:3120\n\t"
        "s_waitcnt vmcnt(47)\n\t"
        "v_mfma_f32_16x16x32_bf16 v[128:131], a[128:131], v[64:67], 0\n\t"
        "s_lshl_b32 s80, s80, 6\n\t"
        "s_or_b32 s81, s38, %[snd]\n\t"
        "v_mfma_f32_16x16x32_bf16 v[132:135], a[132:135], v[64:67], 0\n\t"
        "s_cmp_lt_i32 s81, 0\n\t"
        "s_cselect_b32 s82, s36, s80\n\t"
        "buffer_load_dword v184, v244, %[rd], s82 offen sc1\n\t"
        "v_mfma_f32_16x16x32_bf16 v[136:139], a[136:139], v[64:67], 0\n\t"
        "s_lshl_b32 s80, s80, 1\n\t"
        "s_cmp_lt_i32 s81, 0\n\t"
        "s_cselect_b32 s82, s36, s80\n\t"
        "v_mfma_f32_16x16x32_bf16 v[140:143], a[140:143], v[64:67], 0\n\t"
        "buffer_load_dwordx2 v[180:181], v243, %[rd], s82 offen sc1\n\t"
        "s_lshl_b32 s80, s80, 8\n\t"
        "buffer_load_dwordx4 v[64:67], v240, %[rd], s84 offen offset:0 sc1\n\t"
        "s_waitcnt vmcnt(46)\n\t"
        "v_mfma_f32_16x16x32_bf16 v[128:131], a[144:147], v[68:71], v[128:131]\n\t"
        "s_cmp_lt_i32 s81, 0\n\t"
        "s_cselect_b32 s82, s36, s80\n\t"
        "buffer_load_dwordx2 v[176:177], v245, %[rx], s82 offen\n\t"
        "v_mfma_f32_16x16x32_bf16 v[132:135], a[148:151], v[68:71], v[132:135]\n\t"
        "s_add_i32 s80, s38, 0\n\t"
        "s_lshl_b32 s80, s80, 17\n\t"
        "v_mfma_f32_16x16x32_bf16 v[136:139], a[152:155], v[68:71], v[136:139]\n\t"
        "s_or_b32 s81, s38, %[snd]\n\t"
        "s_cmp_lt_i32 s81, 0\n\t"
        "s_cselect_b32 s82, s36, s80\n\t"
        "v_mfma_f32_16x16x32_bf16 v[140:143], a[156:159], v[68:71], v[140:143]\n\t"
        "buffer_load_dwordx4 v[160:163], v246, %[rd], s82 offen offset:0 sc1\n\t"
        "buffer_load_dwordx4 v[164:167], v246, %[rd], s82 offen offset:1024 sc1\n\t"
        "buffer_load_dwordx4 v[68:71], v240, %[rd], s84 offen offset:1024 sc1\n\t"
        "s_waitcnt vmcnt(49)\n\t"
        "v_mfma_f32_16x16x32_bf16 v[128:131], a[160:163], v[72:75], v[128:131]\n\t"
        "s_waitcnt vmcnt(34)\n\t"
        "v_mfma_f32_16x16x32_bf16 v[132:135], a[164:167], v[72:75], v[132:135]\n\t"
        "s_add_i32 s80, s39, 1\n\t"
        "s_lshl_b32 s80, s80, 15\n\t"
        "v_mfma_f32_16x16x32_bf16 v[136:139], a[168:171], v[72:75], v[136:139]\n\t"
        "s_or_b32 s81, s39, %[snd]\n\t"
        "s_cmp_lt_i32 s81, 0\n\t"
        "s_cselect_b32 s83, s36, s80\n\t"
        "s_cselect_b64 s[88:89], 0, -1\n\t"
        "v_mfma_f32_16x16x32_bf16 v[140:143], a[172:175], v[72:75], v[140:143]\n\t"
        "v_xor_b32_e32 v224, %[tag], v169\n\t"
        "v_xor_b32_e32 v225, %[tag], v171\n\t"
        "buffer_load_dwordx4 v[72:75], v240, %[rd], s84 offen offset:2048 sc1\n\t"
        "s_waitcnt vmcnt(49)\n\t"
        "v_mfma_f32_16x16x32_bf16 v[128:131], a[176:179], v[76:79], v[128:131]\n\t"
        "v_xor_b32_e32 v226, %[tag], v173\n\t"
        "v_or3_b32 v224, v224, v225, v226\n\t"
        "v_mfma_f32_16x16x32_bf16 v[132:135], a[180:183], v[76:79], v[132:135]\n\t"
        "v_xor_b32_e32 v225, %[tag], v175\n\t"
        "v_or_b32_e32 v224, v224, v225\n\t"
        "v_mfma_f32_16x16x32_bf16 v[136:139], a[184:187], v[76:79], v[136:139]\n\t"
        "v_cmp_ne_u32_e32 vcc, 0, v224\n\t"
        "v_sub_f32_e32 v236, 1.0, v185\n\t"
        "v_mfma_f32_16x16x32_bf16 v[140:143], a[188:191], v[76:79], v[140:143]\n\t"
        "v_cvt_pk_bf16_f32 v236, 0, v236\n\t"
        "s_and_b64 vcc, vcc, s[88:89]\n\t"
        "s_cbranch_scc1 .Lo15%=\n\t"
        ".Lo16%=:\n\t"
        "v_add_f32_e32 v228, v156, v168\n\t"
        "buffer_load_dwordx4 v[76:79], v240, %[rd], s84 offen offset:3072 sc1\n\t"
        "s_add_u32 s84, s84, 8192\n\t"
        "s_waitcnt vmcnt(49)\n\t"
        "v_mfma_f32_16x16x32_bf16 v[128:131], a[192:195], v[80:83], v[128:131]\n\t"
        "v_add_f32_e32 v229, v157, v170\n\t"
        "v_add_f32_e32 v230, v158, v172\n\t"
        "v_mfma_f32_16x16x32_bf16 v[132:135], a[196:199], v[80:83], v[132:135]\n\t"
        "v_add_f32_e32 v231, v159, v174\n\t"
        "v_cvt_pk_bf16_f32 v228, 0, v228\n\t"
        "v_mfma_f32_16x16x32_bf16 v[136:139], a[200:203], v[80:83], v[136:139]\n\t"
        "v_cvt_pk_bf16_f32 v229, 0, v229\n\t"
        "v_cvt_pk_bf16_f32 v230, 0, v230\n\t"
        "v_mfma_f32_16x16x32_bf16 v[140:143], a[204:207], v[80:83], v[140:143]\n\t"
        "v_cvt_pk_bf16_f32 v231, 0, v231\n\t"
        "v_lshlrev_b32_e32 v224, 16, v182\n\t"
        "buffer_load_dwordx4 v[80:83], v240, %[rd], s84 offen offset:0 sc1\n\t"
        "s_waitcnt vmcnt(49)\n\t"
        "v_mfma_f32_16x16x32_bf16 v[128:131], a[208:211], v[84:87], v[128:131]\n\t"
        "v_lshlrev_b32_e32 v232, 16, v178\n\t"
        "v_and_b32_e32 v225, 0xffff0000, v182\n\t"
        "v_mfma_f32_16x16x32_bf16 v[132:135], a[212:215], v[84:87], v[132:135]\n\t"
        "v_and_b32_e32 v233, 0xffff0000, v178\n\t"
        "v_lshlrev_b32_e32 v226, 16, v183\n\t"
        "v_mfma_f32_16x16x32_bf16 v[136:139], a[216:219], v[84:87], v[136:139]\n\t"
        "v_lshlrev_b32_e32 v234, 16, v179\n\t"
        "v_and_b32_e32 v227, 0xffff0000, v183\n\t"
        "v_mfma_f32_16x16x32_bf16 v[140:143], a[220:223], v[84:87], v[140:143]\n\t"
        "v_and_b32_e32 v235, 0xffff0000, v179\n\t"
        "v_mul_f32_e32 v224, v224, v232\n\t"
        "buffer_load_dwordx4 v[84:87], v240, %[rd], s84 offen offset:1024 sc1\n\t"
        "s_waitcnt vmcnt(49)\n\t"
        "v_mfma_f32_16x16x32_bf16 v[128:131], a[224:227], v[88:91], v[128:131]\n\t"
        "v_mul_f32_e32 v225, v225, v233\n\t"
        "v_mul_f32_e32 v226, v226, v234\n\t"
        "v_mfma_f32_16x16x32_bf16 v[132:135], a[228:231], v[88:91], v[132:135]\n\t"
        "v_mul_f32_e32 v227, v227, v235\n\t"
        "v_mul_f32_e32 v228, v236, v228\n\t"
        "v_mfma_f32_16x16x32_bf16 v[136:139], a[232:235], v[88:91], v[136:139]\n\t"
        "v_mul_f32_e32 v229, v236, v229\n\t"
        "v_mul_f32_e32 v230, v236, v230\n\t"
        "v_mfma_f32_16x16x32_bf16 v[140:143], a[236:239], v[88:91], v[140:143]\n\t"
        "v_mul_f32_e32 v231, v236, v231\n\t"
        "v_cvt_pk_bf16_f32 v224, 0, v224\n\t"
        "buffer_load_dwordx4 v[88:91], v240, %[rd], s84 offen offset:2048 sc1\n\t"
        "s_waitcnt vmcnt(49)\n\t"
        "v_mfma_f32_16x16x32_bf16 v[128:131], a[240:243], v[92:95], v[128:131]\n\t"
        "v_cvt_pk_bf16_f32 v225, 0, v225\n\t"
        "v_cvt_pk_bf16_f32 v226, 0, v226\n\t"
        "v_mfma_f32_16x16x32_bf16 v[132:135], a[244:247], v[92:95], v[132:135]\n\t"
        "v_cvt_pk_bf16_f32 v227, 0, v227\n\t"
        "v_cvt_pk_bf16_f32 v228, 0, v228\n\t"
        "v_mfma_f32_16x16x32_bf16 v[136:139], a[248:251], v[92:95], v[136:139]\n\t"
        "v_cvt_pk_bf16_f32 v229, 0, v229\n\t"
        "v_cvt_pk_bf16_f32 v230, 0, v230\n\t"
        "v_mfma_f32_16x16x32_bf16 v[140:143], a[252:255], v[92:95], v[140:143]\n\t"
        "v_cvt_pk_bf16_f32 v231, 0, v231\n\t"
        "v_mul_f32_e32 v224, v185, v224\n\t"
        "buffer_load_dwordx4 v[92:95], v240, %[rd], s84 offen offset:3072 sc1\n\t"
        "s_add_u32 s84, s84, 8192\n\t"
        "buffer_load_dword v221, v242, %[rd], s87 offen sc1\n\t"
        "s_waitcnt vmcnt(49)\n\t"
        "v_mfma_f32_16x16x32_bf16 v[128:131], a[0:3], v[96:99], v[128:131]\n\t"
        "v_mul_f32_e32 v225, v185, v225\n\t"
        "v_mul_f32_e32 v226, v185, v226\n\t"
        "v_mfma_f32_16x16x32_bf16 v[132:135], a[4:7], v[96:99], v[132:135]\n\t"
        "v_mul_f32_e32 v227, v185, v227\n\t"
        "v_cvt_pk_bf16_f32 v224, 0, v224\n\t"
        "v_mfma_f32_16x16x32_bf16 v[136:139], a[8:11], v[96:99], v[136:139]\n\t"
        "v_cvt_pk_bf16_f32 v225, 0, v225\n\t"
        "v_cvt_pk_bf16_f32 v226, 0, v226\n\t"
        "v_mfma_f32_16x16x32_bf16 v[140:143], a[12:15], v[96:99], v[140:143]\n\t"
        "v_cvt_pk_bf16_f32 v227, 0, v227\n\t"
        "v_add_f32_e32 v224, v224, v228\n\t"
        "buffer_load_dwordx4 v[96:99], v240, %[rd], s84 offen offset:0 sc1\n\t"
        "s_waitcnt vmcnt(48)\n\t"
        "v_mfma_f32_16x16x32_bf16 v[128:131], a[16:19], v[100:103], v[128:131]\n\t"
        "v_add_f32_e32 v225, v225, v229\n\t"
        "v_add_f32_e32 v226, v226, v230\n\t"
        "v_mfma_f32_16x16x32_bf16 v[132:135], a[20:23], v[100:103], v[132:135]\n\t"
        "v_add_f32_e32 v227, v227, v231\n\t"
        "v_cvt_pk_bf16_f32 v238, v224, v225\n\t"
        "v_mfma_f32_16x16x32_bf16 v[136:139], a[24:27], v[100:103], v[136:139]\n\t"
        "v_cvt_pk_bf16_f32 v239, v226, v227\n\t"
        "buffer_store_dwordx2 v[238:239], v245, %[ro], s83 offen\n\t"
        "v_mfma_f32_16x16x32_bf16 v[140:143], a[28:31], v[100:103], v[140:143]\n\t"
        "s_add_i32 s80, s38, 1\n\t"
        "s_lshl_b32 s80, s80, 6\n\t"
        "buffer_load_dwordx4 v[100:103], v240, %[rd], s84 offen offset:1024 sc1\n\t"
        "s_waitcnt vmcnt(47)\n\t"
        "v_mfma_f32_16x16x32_bf16 v[128:131], a[32:35], v[104:107], v[128:131]\n\t"
        "s_or_b32 s81, s38, %[snd]\n\t"
        "s_cmp_lt_i32 s81, 0\n\t"
        "s_cselect_b32 s82, s36, s80\n\t"
        "v_mfma_f32_16x16x32_bf16 v[132:135], a[36:39], v[104:107], v[132:135]\n\t"
        "buffer_load_dword v185, v244, %[rd], s82 offen sc1\n\t"
        "s_lshl_b32 s80, s80, 1\n\t"
        "v_mfma_f32_16x16x32_bf16 v[136:139], a[40:43], v[104:107], v[136:139]\n\t"
        "s_cmp_lt_i32 s81, 0\n\t"
        "s_cselect_b32 s82, s36, s80\n\t"
        "buffer_load_dwordx2 v[182:183], v243, %[rd], s82 offen sc1\n\t"
        "v_mfma_f32_16x16x32_bf16 v[140:143], a[44:47], v[104:107], v[140:143]\n\t"
        "s_lshl_b32 s80, s80, 8\n\t"
        "s_cmp_lt_i32 s81, 0\n\t"
        "s_cselect_b32 s82, s36, s80\n\t"
        "buffer_load_dwordx4 v[104:107], v240, %[rd], s84 offen offset:2048 sc1\n\t"
        "s_waitcnt vmcnt(46)\n\t"
        "v_mfma_f32_16x16x32_bf16 v[128:131], a[48:51], v[108:111], v[128:131]\n\t"
        "buffer_load_dwordx2 v[178:179], v245, %[rx], s82 offen\n\t"
        "s_add_i32 s80, s38, 1\n\t"
        "v_mfma_f32_16x16x32_bf16 v[132:135], a[52:55], v[108:111], v[132:135]\n\t"
        "s_lshl_b32 s80, s80, 17\n\t"
        "s_or_b32 s81, s38, %[snd]\n\t"
        "v_mfma_f32_16x16x32_bf16 v[136:139], a[56:59], v[108:111], v[136:139]\n\t"
        "s_cmp_lt_i32 s81, 0\n\t"
        "s_cselect_b32 s82, s36, s80\n\t"
        "buffer_load_dwordx4 v[168:171], v246, %[rd], s82 offen offset:0 sc1\n\t"
        "v_mfma_f32_16x16x32_bf16 v[140:143], a[60:63], v[108:111], v[140:143]\n\t"
        "buffer_load_dwordx4 v[172:175], v246, %[rd], s82 offen offset:1024 sc1\n\t"
        "buffer_load_dwordx4 v[108:111], v240, %[rd], s84 offen offset:3072 sc1\n\t"
        "s_add_u32 s84, s84, 8192\n\t"
        "s_waitcnt vmcnt(49)\n\t"
        "v_mfma_f32_16x16x32_bf16 v[128:131], a[64:67], v[112:115], v[128:131]\n\t"
        "v_mfma_f32_16x16x32_bf16 v[132:135], a[68:71], v[112:115], v[132:135]\n\t"
        "v_mfma_f32_16x16x32_bf16 v[136:139], a[72:75], v[112:115], v[136:139]\n\t"
        "v_mfma_f32_16x16x32_bf16 v[140:143], a[76:79], v[112:115], v[140:143]\n\t"
        "buffer_load_dwordx4 v[112:115], v240, %[rd], s84 offen offset:0 sc1\n\t"
        "s_waitcnt vmcnt(49)\n\t"
        "v_mfma_f32_16x16x32_bf16 v[128:131], a[80:83], v[116:119], v[128:131]\n\t"
        "v_mfma_f32_16x16x32_bf16 v[132:135], a[84:87], v[116:119], v[132:135]\n\t"
        "v_mfma_f32_16x16x32_bf16 v[136:139], a[88:91], v[116:119], v[136:139]\n\t"
        "v_mfma_f32_16x16x32_bf16 v[140:143], a[92:95], v[116:119], v[140:143]\n\t"
        "buffer_load_dwordx4 v[116:119], v240, %[rd], s84 offen offset:1024 sc1\n\t"
        "s_waitcnt vmcnt(49)\n\t"
        "v_mfma_f32_16x16x32_bf16 v[128:131], a[96:99], v[120:123], v[128:131]\n\t"
        "v_mfma_f32_16x16x32_bf16 v[132:135], a[100:103], v[120:123], v[132:135]\n\t"
        "v_mfma_f32_16x16x32_bf16 v[136:139], a[104:107], v[120:123], v[136:139]\n\t"
        "v_mfma_f32_16x16x32_bf16 v[140:143], a[108:111], v[120:123], v[140:143]\n\t"
        "buffer_load_dwordx4 v[120:123], v240, %[rd], s84 offen offset:2048 sc1\n\t"
        "s_waitcnt vmcnt(49)\n\t"
        "v_mfma_f32_16x16x32_bf16 v[128:131], a[112:115], v[124:127], v[128:131]\n\t"
        "v_mfma_f32_16x16x32_bf16 v[132:135], a[116:119], v[124:127], v[132:135]\n\t"
        "v_mfma_f32_16x16x32_bf16 v[136:139], a[120:123], v[124:127], v[136:139]\n\t"
        "v_mfma_f32_16x16x32_bf16 v[140:143], a[124:127], v[124:127], v[140:143]\n\t"
        "buffer_load_dwordx4 v[124:127], v240, %[rd], s84 offen offset:3072 sc1\n\t"
        "s_add_u32 s84, s84, 8192\n\t"
        "s_nop 2\n\t"
        "ds_write_b128 v222, v[128:131] offset:4160\n\t"
        "ds_write_b128 v222, v[132:135] offset:5200\n\t"
        "ds_write_b128 v222, v[136:139] offset:6240\n\t"
        "ds_write_b128 v222, v[140:143] offset:7280\n\t"
        "s_waitcnt vmcnt(49)\n\t"
        "v_cmp_eq_u32_e32 vcc, 32, v220\n\t"
        "s_nop 1\n\t"
        "s_or_b32 s40, s40, vcc_lo\n\t"
        "s_waitcnt lgkmcnt(0)\n\t"
        "s_barrier\n\t"
        "s_cmp_eq_u32 s41, 0\n\t"
        "s_cbranch_scc1 .Lfl%=\n\t"
        "v_mov_b32_e32 v152, v144\n\t"
        "v_mov_b32_e32 v153, v145\n\t"
        "v_mov_b32_e32 v154, v146\n\t"
        "v_mov_b32_e32 v155, v147\n\t"
        "v_mov_b32_e32 v156, v148\n\t"
        "v_mov_b32_e32 v157, v149\n\t"
        "v_mov_b32_e32 v158, v150\n\t"
        "v_mov_b32_e32 v159, v151\n\t"
        "s_mov_b32 s39, s38\n\t"
        "s_mov_b32 s38, s44\n\t"
        "s_add_u32 s37, s37, 1\n\t"
        "s_add_u32 s44, s44, 8\n\t"
        "s_sub_u32 s43, 33280, s43\n\t"
        "s_branch .Lrnd%=\n\t"
        ".Lfl%=:\n\t"
        "v_add_u32_e32 v223, s43, v248\n\t"
        "buffer_load_dword v220, v242, %[rd], 0 offen sc1\n\t"
        "ds_read_b128 v[186:189], v223 offset:0\n\t"
        "ds_read_b128 v[190:193], v223 offset:8320\n\t"
        "ds_read_b128 v[194:197], v223 offset:16640\n\t"
        "ds_read_b128 v[198:201], v223 offset:24960\n\t"
        "s_waitcnt lgkmcnt(0)\n\t"
        "v_add_f32_e32 v186, v186, v190\n\t"
        "v_add_f32_e32 v187, v187, v191\n\t"
        "v_add_f32_e32 v188, v188, v192\n\t"
        "v_add_f32_e32 v189, v189, v193\n\t"
        "v_add_f32_e32 v194, v194, v198\n\t"
        "v_add_f32_e32 v195, v195, v199\n\t"
        "v_add_f32_e32 v196, v196, v200\n\t"
        "v_add_f32_e32 v197, v197, v201\n\t"
        "v_add_f32_e32 v204, v186, v194\n\t"
        "v_add_f32_e32 v206, v187, v195\n\t"
        "v_add_f32_e32 v208, v188, v196\n\t"
        "v_add_f32_e32 v210, v189, v197\n\t"
        "s_add_i32 s80, s44, 0\n\t"
        "s_lshl_b32 s80, s80, 17\n\t"
        "s_or_b32 s81, s44, %[own]\n\t"
        "s_cmp_lt_i32 s81, 0\n\t"
        "s_cselect_b32 s82, s36, s80\n\t"
        "buffer_store_dwordx4 v[204:207], v246, %[rd], s82 offen offset:0 sc1\n\t"
        "buffer_store_dwordx4 v[208:211], v246, %[rd], s82 offen offset:1024 sc1\n\t"
        "v_mov_b32_e32 v128, v204\n\t"
        "v_mov_b32_e32 v129, v206\n\t"
        "v_mov_b32_e32 v130, v208\n\t"
        "v_mov_b32_e32 v131, v210\n\t"
        "ds_read_b128 v[186:189], v223 offset:4160\n\t"
        "ds_read_b128 v[190:193], v223 offset:12480\n\t"
        "ds_read_b128 v[194:197], v223 offset:20800\n\t"
        "ds_read_b128 v[198:201], v223 offset:29120\n\t"
        "s_waitcnt lgkmcnt(0)\n\t"
        "v_add_f32_e32 v186, v186, v190\n\t"
        "v_add_f32_e32 v187, v187, v191\n\t"
        "v_add_f32_e32 v188, v188, v192\n\t"
        "v_add_f32_e32 v189, v189, v193\n\t"
        "v_add_f32_e32 v194, v194, v198\n\t"
        "v_add_f32_e32 v195, v195, v199\n\t"
        "v_add_f32_e32 v196, v196, v200\n\t"
        "v_add_f32_e32 v197, v197, v201\n\t"
        "v_add_f32_e32 v212, v186, v194\n\t"
        "v_add_f32_e32 v214, v187, v195\n\t"
        "v_add_f32_e32 v216, v188, v196\n\t"
        "v_add_f32_e32 v218, v189, v197\n\t"
        "s_add_i32 s80, s44, 1\n\t"
        "s_lshl_b32 s80, s80, 17\n\t"
        "s_or_b32 s81, s44, %[own]\n\t"
        "s_cmp_lt_i32 s81, 0\n\t"
        "s_cselect_b32 s82, s36, s80\n\t"
        "buffer_store_dwordx4 v[212:215], v246, %[rd], s82 offen offset:0 sc1\n\t"
        "buffer_store_dwordx4 v[216:219], v246, %[rd], s82 offen offset:1024 sc1\n\t"
        "v_mov_b32_e32 v132, v212\n\t"
        "v_mov_b32_e32 v133, v214\n\t"
        "v_mov_b32_e32 v134, v216\n\t"
        "v_mov_b32_e32 v135, v218\n\t"
        "s_waitcnt vmcnt(4)\n\t"
        "v_cmp_eq_u32_e32 vcc, 32, v220\n\t"
        "s_nop 1\n\t"
        "s_or_b32 s40, s40, vcc_lo\n\t"
        "s_add_i32 s80, s38, 0\n\t"
        "s_lshl_b32 s80, s80, 15\n\t"
        "s_or_b32 s81, s38, %[snd]\n\t"
        "s_cmp_lt_i32 s81, 0\n\t"
        "s_cselect_b32 s83, s36, s80\n\t"
        "s_cbranch_scc1 .Lo19%=\n\t"
        "s_mov_b32 s97, 0\n\t"
        ".Lo17%=:\n\t"
        "v_xor_b32_e32 v186, %[tag], v161\n\t"
        "v_xor_b32_e32 v187, %[tag], v163\n\t"
        "v_xor_b32_e32 v188, %[tag], v165\n\t"
        "v_or3_b32 v186, v186, v187, v188\n\t"
        "v_xor_b32_e32 v187, %[tag], v167\n\t"
        "v_or_b32_e32 v186, v186, v187\n\t"
        "v_cmp_ne_u32_e32 vcc, 0, v186\n\t"
        "v_sub_f32_e32 v198, 1.0, v184\n\t"
        "v_add_f32_e32 v190, v144, v160\n\t"
        "v_add_f32_e32 v191, v145, v162\n\t"
        "v_add_f32_e32 v192, v146, v164\n\t"
        "v_add_f32_e32 v193, v147, v166\n\t"
        "s_cbranch_vccz .Lo18%=\n\t"
        "s_sleep %c[xslp]\n\t"
        "s_lshl_b32 s82, s83, 2\n\t"
        "buffer_load_dwordx4 v[160:163], v246, %[rd], s82 offen offset:0 sc1\n\t"
        "buffer_load_dwordx4 v[164:167], v246, %[rd], s82 offen offset:1024 sc1\n\t"
        "s_add_u32 s97, s97, 1\n\t"
        "s_cmp_lt_u32 s97, 0x200000\n\t"
        "s_cbranch_scc0 .Lgu%=\n\t"
        "s_waitcnt vmcnt(0)\n\t"
        "s_branch .Lo17%=\n\t"
        ".Lo18%=:\n\t"
        "v_cvt_pk_bf16_f32 v198, 0, v198\n\t"
        "v_cvt_pk_bf16_f32 v190, 0, v190\n\t"
        "v_cvt_pk_bf16_f32 v191, 0, v191\n\t"
        "v_cvt_pk_bf16_f32 v192, 0, v192\n\t"
        "v_cvt_pk_bf16_f32 v193, 0, v193\n\t"
        "v_lshlrev_b32_e32 v186, 16, v180\n\t"
        "v_lshlrev_b32_e32 v194, 16, v176\n\t"
        "v_and_b32_e32 v187, 0xffff0000, v180\n\t"
        "v_and_b32_e32 v195, 0xffff0000, v176\n\t"
        "v_lshlrev_b32_e32 v188, 16, v181\n\t"
        "v_lshlrev_b32_e32 v196, 16, v177\n\t"
        "v_and_b32_e32 v189, 0xffff0000, v181\n\t"
        "v_and_b32_e32 v197, 0xffff0000, v177\n\t"
        "v_mul_f32_e32 v186, v186, v194\n\t"
        "v_mul_f32_e32 v187, v187, v195\n\t"
        "v_mul_f32_e32 v188, v188, v196\n\t"
        "v_mul_f32_e32 v189, v189, v197\n\t"
        "v_mul_f32_e32 v190, v198, v190\n\t"
        "v_mul_f32_e32 v191, v198, v191\n\t"
        "v_mul_f32_e32 v192, v198, v192\n\t"
        "v_mul_f32_e32 v193, v198, v193\n\t"
        "v_cvt_pk_bf16_f32 v186, 0, v186\n\t"
        "v_cvt_pk_bf16_f32 v187, 0, v187\n\t"
        "v_cvt_pk_bf16_f32 v188, 0, v188\n\t"
        "v_cvt_pk_bf16_f32 v189, 0, v189\n\t"
        "v_cvt_pk_bf16_f32 v190, 0, v190\n\t"
        "v_cvt_pk_bf16_f32 v191, 0, v191\n\t"
        "v_cvt_pk_bf16_f32 v192, 0, v192\n\t"
        "v_cvt_pk_bf16_f32 v193, 0, v193\n\t"
        "v_mul_f32_e32 v186, v184, v186\n\t"
        "v_mul_f32_e32 v187, v184, v187\n\t"
        "v_mul_f32_e32 v188, v184, v188\n\t"
        "v_mul_f32_e32 v189, v184, v189\n\t"
        "v_cvt_pk_bf16_f32 v186, 0, v186\n\t"
        "v_cvt_pk_bf16_f32 v187, 0, v187\n\t"
        "v_cvt_pk_bf16_f32 v188, 0, v188\n\t"
        "v_cvt_pk_bf16_f32 v189, 0, v189\n\t"
        "v_add_f32_e32 v186, v186, v190\n\t"
        "v_add_f32_e32 v187, v187, v191\n\t"
        "v_add_f32_e32 v188, v188, v192\n\t"
        "v_add_f32_e32 v189, v189, v193\n\t"
        "v_cvt_pk_bf16_f32 v200, v186, v187\n\t"
        "v_cvt_pk_bf16_f32 v201, v188, v189\n\t"
        ".Lo19%=:\n\t"
        "buffer_store_dwordx2 v[200:201], v245, %[ro], s83 offen\n\t"
        "s_add_i32 s80, s38, 1\n\t"
        "s_lshl_b32 s80, s80, 15\n\t"
        "s_or_b32 s81, s38, %[snd]\n\t"
        "s_cmp_lt_i32 s81, 0\n\t"
        "s_cselect_b32 s83, s36, s80\n\t"
        "s_cbranch_scc1 .Lo22%=\n\t"
        "s_mov_b32 s97, 0\n\t"
        ".Lo20%=:\n\t"
        "v_xor_b32_e32 v186, %[tag], v169\n\t"
        "v_xor_b32_e32 v187, %[tag], v171\n\t"
        "v_xor_b32_e32 v188, %[tag], v173\n\t"
        "v_or3_b32 v186, v186, v187, v188\n\t"
        "v_xor_b32_e32 v187, %[tag], v175\n\t"
        "v_or_b32_e32 v186, v186, v187\n\t"
        "v_cmp_ne_u32_e32 vcc, 0, v186\n\t"
        "v_sub_f32_e32 v198, 1.0, v185\n\t"
        "v_add_f32_e32 v190, v148, v168\n\t"
        "v_add_f32_e32 v191, v149, v170\n\t"
        "v_add_f32_e32 v192, v150, v172\n\t"
        "v_add_f32_e32 v193, v151, v174\n\t"
        "s_cbranch_vccz .Lo21%=\n\t"
        "s_sleep %c[xslp]\n\t"
        "s_lshl_b32 s82, s83, 2\n\t"
        "buffer_load_dwordx4 v[168:171], v246, %[rd], s82 offen offset:0 sc1\n\t"
        "buffer_load_dwordx4 v[172:175], v246, %[rd], s82 offen offset:1024 sc1\n\t"
        "s_add_u32 s97, s97, 1\n\t"
        "s_cmp_lt_u32 s97, 0x200000\n\t"
        "s_cbranch_scc0 .Lgu%=\n\t"
        "s_waitcnt vmcnt(0)\n\t"
        "s_branch .Lo20%=\n\t"
        ".Lo21%=:\n\t"
        "v_cvt_pk_bf16_f32 v198, 0, v198\n\t"
        "v_cvt_pk_bf16_f32 v190, 0, v190\n\t"
        "v_cvt_pk_bf16_f32 v191, 0, v191\n\t"
        "v_cvt_pk_bf16_f32 v192, 0, v192\n\t"
        "v_cvt_pk_bf16_f32 v193, 0, v193\n\t"
        "v_lshlrev_b32_e32 v186, 16, v182\n\t"
        "v_lshlrev_b32_e32 v194, 16, v178\n\t"
        "v_and_b32_e32 v187, 0xffff0000, v182\n\t"
        "v_and_b32_e32 v195, 0xffff0000, v178\n\t"
        "v_lshlrev_b32_e32 v188, 16, v183\n\t"
        "v_lshlrev_b32_e32 v196, 16, v179\n\t"
        "v_and_b32_e32 v189, 0xffff0000, v183\n\t"
        "v_and_b32_e32 v197, 0xffff0000, v179\n\t"
        "v_mul_f32_e32 v186, v186, v194\n\t"
        "v_mul_f32_e32 v187, v187, v195\n\t"
        "v_mul_f32_e32 v188, v188, v196\n\t"
        "v_mul_f32_e32 v189, v189, v197\n\t"
        "v_mul_f32_e32 v190, v198, v190\n\t"
        "v_mul_f32_e32 v191, v198, v191\n\t"
        "v_mul_f32_e32 v192, v198, v192\n\t"
        "v_mul_f32_e32 v193, v198, v193\n\t"
        "v_cvt_pk_bf16_f32 v186, 0, v186\n\t"
        "v_cvt_pk_bf16_f32 v187, 0, v187\n\t"
        "v_cvt_pk_bf16_f32 v188, 0, v188\n\t"
        "v_cvt_pk_bf16_f32 v189, 0, v189\n\t"
        "v_cvt_pk_bf16_f32 v190, 0, v190\n\t"
        "v_cvt_pk_bf16_f32 v191, 0, v191\n\t"
        "v_cvt_pk_bf16_f32 v192, 0, v192\n\t"
        "v_cvt_pk_bf16_f32 v193, 0, v193\n\t"
        "v_mul_f32_e32 v186, v185, v186\n\t"
        "v_mul_f32_e32 v187, v185, v187\n\t"
        "v_mul_f32_e32 v188, v185, v188\n\t"
        "v_mul_f32_e32 v189, v185, v189\n\t"
        "v_cvt_pk_bf16_f32 v186, 0, v186\n\t"
        "v_cvt_pk_bf16_f32 v187, 0, v187\n\t"
        "v_cvt_pk_bf16_f32 v188, 0, v188\n\t"
        "v_cvt_pk_bf16_f32 v189, 0, v189\n\t"
        "v_add_f32_e32 v186, v186, v190\n\t"
        "v_add_f32_e32 v187, v187, v191\n\t"
        "v_add_f32_e32 v188, v188, v192\n\t"
        "v_add_f32_e32 v189, v189, v193\n\t"
        "v_cvt_pk_bf16_f32 v200, v186, v187\n\t"
        "v_cvt_pk_bf16_f32 v201, v188, v189\n\t"
        ".Lo22%=:\n\t"
        "buffer_store_dwordx2 v[200:201], v245, %[ro], s83 offen\n\t"
        "s_add_i32 s80, s44, 0\n\t"
        "s_lshl_b32 s80, s80, 6\n\t"
        "s_or_b32 s81, s44, %[snd]\n\t"
        "s_cmp_lt_i32 s81, 0\n\t"
        "s_cselect_b32 s82, s36, s80\n\t"
        "buffer_load_dword v184, v244, %[rd], s82 offen sc1\n\t"
        "s_lshl_b32 s80, s80, 1\n\t"
        "s_cmp_lt_i32 s81, 0\n\t"
        "s_cselect_b32 s82, s36, s80\n\t"
        "buffer_load_dwordx2 v[180:181], v243, %[rd], s82 offen sc1\n\t"
        "s_lshl_b32 s80, s80, 8\n\t"
        "s_cmp_lt_i32 s81, 0\n\t"
        "s_cselect_b32 s82, s36, s80\n\t"
        "buffer_load_dwordx2 v[176:177], v245, %[rx], s82 offen\n\t"
        "s_add_i32 s80, s44, 1\n\t"
        "s_lshl_b32 s80, s80, 6\n\t"
        "s_or_b32 s81, s44, %[snd]\n\t"
        "s_cmp_lt_i32 s81, 0\n\t"
        "s_cselect_b32 s82, s36, s80\n\t"
        "buffer_load_dword v185, v244, %[rd], s82 offen sc1\n\t"
        "s_lshl_b32 s80, s80, 1\n\t"
        "s_cmp_lt_i32 s81, 0\n\t"
        "s_cselect_b32 s82, s36, s80\n\t"
        "buffer_load_dwordx2 v[182:183], v243, %[rd], s82 offen sc1\n\t"
        "s_lshl_b32 s80, s80, 8\n\t"
        "s_cmp_lt_i32 s81, 0\n\t"
        "s_cselect_b32 s82, s36, s80\n\t"
        "buffer_load_dwordx2 v[178:179], v245, %[rx], s82 offen\n\t"
        "s_sleep %c[ydel]\n\t"
        "s_add_i32 s80, s44, 0\n\t"
        "s_lshl_b32 s80, s80, 17\n\t"
        "s_or_b32 s81, s44, %[snd]\n\t"
        "s_cmp_lt_i32 s81, 0\n\t"
        "s_cselect_b32 s82, s36, s80\n\t"
        "buffer_load_dwordx4 v[160:163], v246, %[rd], s82 offen offset:0 sc1\n\t"
        "buffer_load_dwordx4 v[164:167], v246, %[rd], s82 offen offset:1024 sc1\n\t"
        "s_add_i32 s80, s44, 1\n\t"
        "s_lshl_b32 s80, s80, 17\n\t"
        "s_or_b32 s81, s44, %[snd]\n\t"
        "s_cmp_lt_i32 s81, 0\n\t"
        "s_cselect_b32 s82, s36, s80\n\t"
        "buffer_load_dwordx4 v[168:171], v246, %[rd], s82 offen offset:0 sc1\n\t"
        "buffer_load_dwordx4 v[172:175], v246, %[rd], s82 offen offset:1024 sc1\n\t"
        "s_mov_b32 s38, -1\n\t"
        "v_mov_b32_e32 v152, v128\n\t"
        "v_mov_b32_e32 v153, v129\n\t"
        "v_mov_b32_e32 v154, v130\n\t"
        "v_mov_b32_e32 v155, v131\n\t"
        "v_mov_b32_e32 v156, v132\n\t"
        "v_mov_b32_e32 v157, v133\n\t"
        "v_mov_b32_e32 v158, v134\n\t"
        "v_mov_b32_e32 v159, v135\n\t"
        "s_mov_b32 s39, s44\n\t"
        "s_add_u32 s37, s37, 1\n\t"
        "s_add_u32 s44, s44, 8\n\t"
        "s_sub_u32 s43, 33280, s43\n\t"
        "s_branch .Lnew%=\n\t"
        ".Lsol%=:\n\t"
        "s_xor_b32 s44, %[rho], 122\n\t"
        "s_mov_b32 s37, 30\n\t"
        ".Lone%=:\n\t"
        ".Lhk0%=:\n\t"
        "s_cbranch_execz .Lhk1%=\n\t"
        "s_add_i32 s80, s44, 0\n\t"
        "s_lshl_b32 s80, s80, 6\n\t"
        "s_or_b32 s81, s44, %[snd]\n\t"
        "s_cmp_lt_i32 s81, 0\n\t"
        "s_cselect_b32 s82, s36, s80\n\t"
        "buffer_load_dword v184, v244, %[rd], s82 offen sc1\n\t"
        "s_lshl_b32 s80, s80, 1\n\t"
        "s_cmp_lt_i32 s81, 0\n\t"
        ".Lhk1%=:\n\t"
        "s_cbranch_execz .Lhk2%=\n\t"
        "s_cselect_b32 s82, s36, s80\n\t"
        "buffer_load_dwordx2 v[180:181], v243, %[rd], s82 offen sc1\n\t"
        "s_lshl_b32 s80, s80, 8\n\t"
        "s_cmp_lt_i32 s81, 0\n\t"
        "s_cselect_b32 s82, s36, s80\n\t"
        "buffer_load_dwordx2 v[176:177], v245, %[rx], s82 offen\n\t"
        "v_add_u32_e32 v222, s43, v247\n\t"
        "v_add_u32_e32 v223, s43, v248\n\t"
        ".Lhk2%=:\n\t"
        "s_cbranch_execz .Lhk3%=\n\t"
        "s_mov_b32 s97, 0\n\t"
        ".Lo23%=:\n\t"
        "s_lshr_b32 s80, s40, s37\n\t"
        "s_andn2_b32 s80, 1, s80\n\t"
        "s_cbranch_scc0 .Lo24%=\n\t"
        "s_cmp_eq_u32 s97, 0\n\t"
        "s_cbranch_scc1 .Lo23%=x\n\t"
        "s_sleep %c[oslp]\n\t"
        ".Lo23%=x:\n\t"
        "buffer_load_dword v220, v242, %[rd], 0 offen sc1\n\t"
        ".Lhk3%=:\n\t"
        "s_cbranch_execz .Lhk4%=\n\t"
        "s_add_u32 s97, s97, 1\n\t"
        "s_cmp_lt_u32 s97, 0x200000\n\t"
        "s_cbranch_scc0 .Lgu%=\n\t"
        "s_waitcnt vmcnt(0)\n\t"
        "v_cmp_eq_u32_e32 vcc, 32, v220\n\t"
        "s_nop 1\n\t"
        "s_or_b32 s40, s40, vcc_lo\n\t"
        "s_branch .Lo23%=\n\t"
        ".Lo24%=:\n\t"
        ".Lhk4%=:\n\t"
        "s_cbranch_execz .Lhk5%=\n\t"
        "s_lshr_b32 s84, s44, 1\n\t"
        "s_lshl_b32 s84, s84, 18\n\t"
        "s_and_b32 s80, s44, 1\n\t"
        "s_lshl_b32 s80, s80, 12\n\t"
        "s_or_b32 s84, s84, s80\n\t"
        "buffer_load_dwordx4 v[0:3], v240, %[rd], s84 offen offset:0 sc1\n\t"
        "buffer_load_dwordx4 v[4:7], v240, %[rd], s84 offen offset:1024 sc1\n\t"
        "buffer_load_dwordx4 v[8:11], v240, %[rd], s84 offen offset:2048 sc1\n\t"
        ".Lhk5%=:\n\t"
        "s_cbranch_execz .Lhk6%=\n\t"
        "buffer_load_dwordx4 v[12:15], v240, %[rd], s84 offen offset:3072 sc1\n\t"
        "s_add_u32 s84, s84, 8192\n\t"
        "buffer_load_dwordx4 v[16:19], v240, %[rd], s84 offen offset:0 sc1\n\t"
        "buffer_load_dwordx4 v[20:23], v240, %[rd], s84 offen offset:1024 sc1\n\t"
        "buffer_load_dwordx4 v[24:27], v240, %[rd], s84 offen offset:2048 sc1\n\t"
        "buffer_load_dwordx4 v[28:31], v240, %[rd], s84 offen offset:3072 sc1\n\t"
        "s_add_u32 s84, s84, 8192\n\t"
        "buffer_load_dwordx4 v[32:35], v240, %[rd], s84 offen offset:0 sc1\n\t"
        ".Lhk6%=:\n\t"
        "s_cbranch_execz .Lhk7%=\n\t"
        "buffer_load_dwordx4 v[36:39], v240, %[rd], s84 offen offset:1024 sc1\n\t"
        "buffer_load_dwordx4 v[40:43], v240, %[rd], s84 offen offset:2048 sc1\n\t"
        "buffer_load_dwordx4 v[44:47], v240, %[rd], s84 offen offset:3072 sc1\n\t"
        "s_add_u32 s84, s84, 8192\n\t"
        "buffer_load_dwordx4 v[48:51], v240, %[rd], s84 offen offset:0 sc1\n\t"
        "buffer_load_dwordx4 v[52:55], v240, %[rd], s84 offen offset:1024 sc1\n\t"
        "buffer_load_dwordx4 v[56:59], v240, %[rd], s84 offen offset:2048 sc1\n\t"
        "buffer_load_dwordx4 v[60:63], v240, %[rd], s84 offen offset:3072 sc1\n\t"
        ".Lhk7%=:\n\t"
        "s_cbranch_execz .Lhk8%=\n\t"
        "s_sub_u32 s84, s84, 20480\n\t"
        "s_waitcnt vmcnt(15)\n\t"
        "v_mfma_f32_16x16x32_bf16 v[128:131], a[128:131], v[0:3], 0\n\t"
        "v_mfma_f32_16x16x32_bf16 v[132:135], a[132:135], v[0:3], 0\n\t"
        "v_mfma_f32_16x16x32_bf16 v[136:139], a[136:139], v[0:3], 0\n\t"
        "v_mfma_f32_16x16x32_bf16 v[140:143], a[140:143], v[0:3], 0\n\t"
        "s_waitcnt vmcnt(14)\n\t"
        "v_mfma_f32_16x16x32_bf16 v[128:131], a[144:147], v[4:7], v[128:131]\n\t"
        ".Lhk8%=:\n\t"
        "s_cbranch_execz .Lhk9%=\n\t"
        "v_mfma_f32_16x16x32_bf16 v[132:135], a[148:151], v[4:7], v[132:135]\n\t"
        "v_mfma_f32_16x16x32_bf16 v[136:139], a[152:155], v[4:7], v[136:139]\n\t"
        "v_mfma_f32_16x16x32_bf16 v[140:143], a[156:159], v[4:7], v[140:143]\n\t"
        "s_waitcnt vmcnt(13)\n\t"
        "v_mfma_f32_16x16x32_bf16 v[128:131], a[160:163], v[8:11], v[128:131]\n\t"
        "v_mfma_f32_16x16x32_bf16 v[132:135], a[164:167], v[8:11], v[132:135]\n\t"
        "v_mfma_f32_16x16x32_bf16 v[136:139], a[168:171], v[8:11], v[136:139]\n\t"
        "v_mfma_f32_16x16x32_bf16 v[140:143], a[172:175], v[8:11], v[140:143]\n\t"
        ".Lhk9%=:\n\t"
        "s_cbranch_execz .Lhk10%=\n\t"
        "s_waitcnt vmcnt(12)\n\t"
        "v_mfma_f32_16x16x32_bf16 v[128:131], a[176:179], v[12:15], v[128:131]\n\t"
        "v_mfma_f32_16x16x32_bf16 v[132:135], a[180:183], v[12:15], v[132:135]\n\t"
        "v_mfma_f32_16x16x32_bf16 v[136:139], a[184:187], v[12:15], v[136:139]\n\t"
        "v_mfma_f32_16x16x32_bf16 v[140:143], a[188:191], v[12:15], v[140:143]\n\t"
        "s_waitcnt vmcnt(11)\n\t"
        "v_mfma_f32_16x16x32_bf16 v[128:131], a[192:195], v[16:19], v[128:131]\n\t"
        "v_mfma_f32_16x16x32_bf16 v[132:135], a[196:199], v[16:19], v[132:135]\n\t"
        ".Lhk10%=:\n\t"
        "s_cbranch_execz .Lhk11%=\n\t"
        "v_mfma_f32_16x16x32_bf16 v[136:139], a[200:203], v[16:19], v[136:139]\n\t"
        "v_mfma_f32_16x16x32_bf16 v[140:143], a[204:207], v[16:19], v[140:143]\n\t"
        "s_waitcnt vmcnt(10)\n\t"
        "v_mfma_f32_16x16x32_bf16 v[128:131], a[208:211], v[20:23], v[128:131]\n\t"
        "v_mfma_f32_16x16x32_bf16 v[132:135], a[212:215], v[20:23], v[132:135]\n\t"
        "v_mfma_f32_16x16x32_bf16 v[136:139], a[216:219], v[20:23], v[136:139]\n\t"
        "v_mfma_f32_16x16x32_bf16 v[140:143], a[220:223], v[20:23], v[140:143]\n\t"
        "s_waitcnt vmcnt(9)\n\t"
        ".Lhk11%=:\n\t"
        "s_cbranch_execz .Lhk12%=\n\t"
        "v_mfma_f32_16x16x32_bf16 v[128:131], a[224:227], v[24:27], v[128:131]\n\t"
        "v_mfma_f32_16x16x32_bf16 v[132:135], a[228:231], v[24:27], v[132:135]\n\t"
        "v_mfma_f32_16x16x32_bf16 v[136:139], a[232:235], v[24:27], v[136:139]\n\t"
        "v_mfma_f32_16x16x32_bf16 v[140:143], a[236:239], v[24:27], v[140:143]\n\t"
        "s_waitcnt vmcnt(8)\n\t"
        "v_mfma_f32_16x16x32_bf16 v[128:131], a[240:243], v[28:31], v[128:131]\n\t"
        "v_mfma_f32_16x16x32_bf16 v[132:135], a[244:247], v[28:31], v[132:135]\n\t"
        "v_mfma_f32_16x16x32_bf16 v[136:139], a[248:251], v[28:31], v[136:139]\n\t"
        ".Lhk12%=:\n\t"
        "s_cbranch_execz .Lhk13%=\n\t"
        "v_mfma_f32_16x16x32_bf16 v[140:143], a[252:255], v[28:31], v[140:143]\n\t"
        "s_waitcnt vmcnt(7)\n\t"
        "v_mfma_f32_16x16x32_bf16 v[128:131], a[0:3], v[32:35], v[128:131]\n\t"
        "v_mfma_f32_16x16x32_bf16 v[132:135], a[4:7], v[32:35], v[132:135]\n\t"
        "v_mfma_f32_16x16x32_bf16 v[136:139], a[8:11], v[32:35], v[136:139]\n\t"
        "v_mfma_f32_16x16x32_bf16 v[140:143], a[12:15], v[32:35], v[140:143]\n\t"
        "s_waitcnt vmcnt(6)\n\t"
        "v_mfma_f32_16x16x32_bf16 v[128:131], a[16:19], v[36:39], v[128:131]\n\t"
        ".Lhk13%=:\n\t"
        "s_cbranch_execz .Lhk14%=\n\t"
        "v_mfma_f32_16x16x32_bf16 v[132:135], a[20:23], v[36:39], v[132:135]\n\t"
        "v_mfma_f32_16x16x32_bf16 v[136:139], a[24:27], v[36:39], v[136:139]\n\t"
        "v_mfma_f32_16x16x32_bf16 v[140:143], a[28:31], v[36:39], v[140:143]\n\t"
        "s_waitcnt vmcnt(5)\n\t"
        "v_mfma_f32_16x16x32_bf16 v[128:131], a[32:35], v[40:43], v[128:131]\n\t"
        "v_mfma_f32_16x16x32_bf16 v[132:135], a[36:39], v[40:43], v[132:135]\n\t"
        "v_mfma_f32_16x16x32_bf16 v[136:139], a[40:43], v[40:43], v[136:139]\n\t"
        "v_mfma_f32_16x16x32_bf16 v[140:143], a[44:47], v[40:43], v[140:143]\n\t"
        ".Lhk14%=:\n\t"
        "s_cbranch_execz .Lhk15%=\n\t"
        "s_waitcnt vmcnt(4)\n\t"
        "v_mfma_f32_16x16x32_bf16 v[128:131], a[48:51], v[44:47], v[128:131]\n\t"
        "v_mfma_f32_16x16x32_bf16 v[132:135], a[52:55], v[44:47], v[132:135]\n\t"
        "v_mfma_f32_16x16x32_bf16 v[136:139], a[56:59], v[44:47], v[136:139]\n\t"
        "v_mfma_f32_16x16x32_bf16 v[140:143], a[60:63], v[44:47], v[140:143]\n\t"
        "s_waitcnt vmcnt(3)\n\t"
        "v_mfma_f32_16x16x32_bf16 v[128:131], a[64:67], v[48:51], v[128:131]\n\t"
        "v_mfma_f32_16x16x32_bf16 v[132:135], a[68:71], v[48:51], v[132:135]\n\t"
        ".Lhk15%=:\n\t"
        "s_cbranch_execz .Lhk16%=\n\t"
        "v_mfma_f32_16x16x32_bf16 v[136:139], a[72:75], v[48:51], v[136:139]\n\t"
        "v_mfma_f32_16x16x32_bf16 v[140:143], a[76:79], v[48:51], v[140:143]\n\t"
        "s_waitcnt vmcnt(2)\n\t"
        "v_mfma_f32_16x16x32_bf16 v[128:131], a[80:83], v[52:55], v[128:131]\n\t"
        "v_mfma_f32_16x16x32_bf16 v[132:135], a[84:87], v[52:55], v[132:135]\n\t"
        "v_mfma_f32_16x16x32_bf16 v[136:139], a[88:91], v[52:55], v[136:139]\n\t"
        "v_mfma_f32_16x16x32_bf16 v[140:143], a[92:95], v[52:55], v[140:143]\n\t"
        "s_waitcnt vmcnt(1)\n\t"
        ".Lhk16%=:\n\t"
        "s_cbranch_execz .Lhk17%=\n\t"
        "v_mfma_f32_16x16x32_bf16 v[128:131], a[96:99], v[56:59], v[128:131]\n\t"
        "v_mfma_f32_16x16x32_bf16 v[132:135], a[100:103], v[56:59], v[132:135]\n\t"
        "v_mfma_f32_16x16x32_bf16 v[136:139], a[104:107], v[56:59], v[136:139]\n\t"
        "v_mfma_f32_16x16x32_bf16 v[140:143], a[108:111], v[56:59], v[140:143]\n\t"
        "s_waitcnt vmcnt(0)\n\t"
        "v_mfma_f32_16x16x32_bf16 v[128:131], a[112:115], v[60:63], v[128:131]\n\t"
        "v_mfma_f32_16x16x32_bf16 v[132:135], a[116:119], v[60:63], v[132:135]\n\t"
        "v_mfma_f32_16x16x32_bf16 v[136:139], a[120:123], v[60:63], v[136:139]\n\t"
        ".Lhk17%=:\n\t"
        "s_cbranch_execz .Lhk18%=\n\t"
        "v_mfma_f32_16x16x32_bf16 v[140:143], a[124:127], v[60:63], v[140:143]\n\t"
        "s_nop 4\n\t"
        "ds_write_b128 v222, v[128:131] offset:0\n\t"
        "ds_write_b128 v222, v[132:135] offset:1040\n\t"
        "ds_write_b128 v222, v[136:139] offset:2080\n\t"
        "ds_write_b128 v222, v[140:143] offset:3120\n\t"
        "s_waitcnt lgkmcnt(0)\n\t"
        "s_barrier\n\t"
        ".Lhk18%=:\n\t"
        "s_cbranch_execz .Lhk19%=\n\t"
        "ds_read_b128 v[186:189], v223 offset:0\n\t"
        "ds_read_b128 v[190:193], v223 offset:8320\n\t"
        "ds_read_b128 v[194:197], v223 offset:16640\n\t"
        "ds_read_b128 v[198:201], v223 offset:24960\n\t"
        "s_waitcnt lgkmcnt(0)\n\t"
        "v_add_f32_e32 v186, v186, v190\n\t"
        "v_add_f32_e32 v187, v187, v191\n\t"
        "v_add_f32_e32 v188, v188, v192\n\t"
        ".Lhk19%=:\n\t"
        "s_cbranch_execz .Lhk20%=\n\t"
        "v_add_f32_e32 v189, v189, v193\n\t"
        "v_add_f32_e32 v194, v194, v198\n\t"
        "v_add_f32_e32 v195, v195, v199\n\t"
        "v_add_f32_e32 v196, v196, v200\n\t"
        "v_add_f32_e32 v197, v197, v201\n\t"
        "v_add_f32_e32 v204, v186, v194\n\t"
        "v_add_f32_e32 v206, v187, v195\n\t"
        "v_add_f32_e32 v208, v188, v196\n\t"
        ".Lhk20%=:\n\t"
        "s_cbranch_execz .Lhk21%=\n\t"
        "v_add_f32_e32 v210, v189, v197\n\t"
        "s_add_i32 s80, s44, 0\n\t"
        "s_lshl_b32 s80, s80, 17\n\t"
        "s_or_b32 s81, s44, %[own]\n\t"
        "s_cmp_lt_i32 s81, 0\n\t"
        "s_cselect_b32 s82, s36, s80\n\t"
        "buffer_store_dwordx4 v[204:207], v246, %[rd], s82 offen offset:0 sc1\n\t"
        "buffer_store_dwordx4 v[208:211], v246, %[rd], s82 offen offset:1024 sc1\n\t"
        ".Lhk21%=:\n\t"
        "s_cbranch_execz .Lhk22%=\n\t"
        "v_mov_b32_e32 v144, v204\n\t"
        "v_mov_b32_e32 v145, v206\n\t"
        "v_mov_b32_e32 v146, v208\n\t"
        "v_mov_b32_e32 v147, v210\n\t"
        "s_sleep %c[xdel]\n\t"
        "s_add_i32 s80, s44, 0\n\t"
        "s_lshl_b32 s80, s80, 17\n\t"
        "s_or_b32 s81, s44, %[snd]\n\t"
        ".Lhk22%=:\n\t"
        "s_cbranch_execz .Lhk23%=\n\t"
        "s_cmp_lt_i32 s81, 0\n\t"
        "s_cselect_b32 s82, s36, s80\n\t"
        "buffer_load_dwordx4 v[160:163], v246, %[rd], s82 offen offset:0 sc1\n\t"
        "buffer_load_dwordx4 v[164:167], v246, %[rd], s82 offen offset:1024 sc1\n\t"
        "s_waitcnt vmcnt(0)\n\t"
        "s_add_i32 s80, s44, 0\n\t"
        "s_lshl_b32 s80, s80, 15\n\t"
        "s_or_b32 s81, s44, %[snd]\n\t"
        ".Lhk23%=:\n\t"
        "s_cbranch_execz .Lhk24%=\n\t"
        "s_cmp_lt_i32 s81, 0\n\t"
        "s_cselect_b32 s83, s36, s80\n\t"
        "s_cbranch_scc1 .Lo27%=\n\t"
        "s_mov_b32 s97, 0\n\t"
        ".Lo25%=:\n\t"
        "v_xor_b32_e32 v186, %[tag], v161\n\t"
        "v_xor_b32_e32 v187, %[tag], v163\n\t"
        "v_xor_b32_e32 v188, %[tag], v165\n\t"
        "v_or3_b32 v186, v186, v187, v188\n\t"
        ".Lhk24%=:\n\t"
        "s_cbranch_execz .Lhk25%=\n\t"
        "v_xor_b32_e32 v187, %[tag], v167\n\t"
        "v_or_b32_e32 v186, v186, v187\n\t"
        "v_cmp_ne_u32_e32 vcc, 0, v186\n\t"
        "v_sub_f32_e32 v198, 1.0, v184\n\t"
        "v_add_f32_e32 v190, v144, v160\n\t"
        "v_add_f32_e32 v191, v145, v162\n\t"
        "v_add_f32_e32 v192, v146, v164\n\t"
        "v_add_f32_e32 v193, v147, v166\n\t"
        ".Lhk25%=:\n\t"
        "s_cbranch_execz .Lhk26%=\n\t"
        "s_cbranch_vccz .Lo26%=\n\t"
        "s_sleep %c[xslp]\n\t"
        "s_lshl_b32 s82, s83, 2\n\t"
        "buffer_load_dwordx4 v[160:163], v246, %[rd], s82 offen offset:0 sc1\n\t"
        "buffer_load_dwordx4 v[164:167], v246, %[rd], s82 offen offset:1024 sc1\n\t"
        "s_add_u32 s97, s97, 1\n\t"
        "s_cmp_lt_u32 s97, 0x200000\n\t"
        "s_cbranch_scc0 .Lgu%=\n\t"
        ".Lhk26%=:\n\t"
        "s_cbranch_execz .Lhk27%=\n\t"
        "s_waitcnt vmcnt(0)\n\t"
        "s_branch .Lo25%=\n\t"
        ".Lo26%=:\n\t"
        "v_cvt_pk_bf16_f32 v198, 0, v198\n\t"
        "v_cvt_pk_bf16_f32 v190, 0, v190\n\t"
        "v_cvt_pk_bf16_f32 v191, 0, v191\n\t"
        "v_cvt_pk_bf16_f32 v192, 0, v192\n\t"
        "v_cvt_pk_bf16_f32 v193, 0, v193\n\t"
        "v_lshlrev_b32_e32 v186, 16, v180\n\t"
        ".Lhk27%=:\n\t"
        "s_cbranch_execz .Lhk28%=\n\t"
        "v_lshlrev_b32_e32 v194, 16, v176\n\t"
        "v_and_b32_e32 v187, 0xffff0000, v180\n\t"
        "v_and_b32_e32 v195, 0xffff0000, v176\n\t"
        "v_lshlrev_b32_e32 v188, 16, v181\n\t"
        "v_lshlrev_b32_e32 v196, 16, v177\n\t"
        "v_and_b32_e32 v189, 0xffff0000, v181\n\t"
        "v_and_b32_e32 v197, 0xffff0000, v177\n\t"
        "v_mul_f32_e32 v186, v186, v194\n\t"
        ".Lhk28%=:\n\t"
        "s_cbranch_execz .Lhk29%=\n\t"
        "v_mul_f32_e32 v187, v187, v195\n\t"
        "v_mul_f32_e32 v188, v188, v196\n\t"
        "v_mul_f32_e32 v189, v189, v197\n\t"
        "v_mul_f32_e32 v190, v198, v190\n\t"
        "v_mul_f32_e32 v191, v198, v191\n\t"
        "v_mul_f32_e32 v192, v198, v192\n\t"
        "v_mul_f32_e32 v193, v198, v193\n\t"
        "v_cvt_pk_bf16_f32 v186, 0, v186\n\t"
        ".Lhk29%=:\n\t"
        "s_cbranch_execz .Lhk30%=\n\t"
        "v_cvt_pk_bf16_f32 v187, 0, v187\n\t"
        "v_cvt_pk_bf16_f32 v188, 0, v188\n\t"
        "v_cvt_pk_bf16_f32 v189, 0, v189\n\t"
        "v_cvt_pk_bf16_f32 v190, 0, v190\n\t"
        "v_cvt_pk_bf16_f32 v191, 0, v191\n\t"
        "v_cvt_pk_bf16_f32 v192, 0, v192\n\t"
        "v_cvt_pk_bf16_f32 v193, 0, v193\n\t"
        "v_mul_f32_e32 v186, v184, v186\n\t"
        ".Lhk30%=:\n\t"
        "s_cbranch_execz .Lhk31%=\n\t"
        "v_mul_f32_e32 v187, v184, v187\n\t"
        "v_mul_f32_e32 v188, v184, v188\n\t"
        "v_mul_f32_e32 v189, v184, v189\n\t"
        "v_cvt_pk_bf16_f32 v186, 0, v186\n\t"
        "v_cvt_pk_bf16_f32 v187, 0, v187\n\t"
        "v_cvt_pk_bf16_f32 v188, 0, v188\n\t"
        "v_cvt_pk_bf16_f32 v189, 0, v189\n\t"
        "v_add_f32_e32 v186, v186, v190\n\t"
        ".Lhk31%=:\n\t"
        "s_cbranch_execz .Lhk32%=\n\t"
        "v_add_f32_e32 v187, v187, v191\n\t"
        "v_add_f32_e32 v188, v188, v192\n\t"
        "v_add_f32_e32 v189, v189, v193\n\t"
        "v_cvt_pk_bf16_f32 v200, v186, v187\n\t"
        "v_cvt_pk_bf16_f32 v201, v188, v189\n\t"
        ".Lo27%=:\n\t"
        "buffer_store_dwordx2 v[200:201], v245, %[ro], s83 offen\n\t"
        "s_add_u32 s37, s37, 1\n\t"
        "s_xor_b32 s44, s44, 6\n\t"
        ".Lhk32%=:\n\t"
        "s_cbranch_execz .Lhk33%=\n\t"
        "s_sub_u32 s43, 33280, s43\n\t"
        "s_cmp_lt_u32 s37, 32\n\t"
        "s_cbranch_scc1 .Lone%=\n\t"
        "buffer_store_dword v205, off, %[rd], %[ep] sc1\n\t"
        "s_endpgm\n\t"
        ".Lo13%=:\n\t"
        "s_mov_b32 s97, 0\n\t"
        "s_lshl_b32 s82, s83, 2\n\t"
        ".Lo13%=r:\n\t"
        "s_sleep %c[xslp]\n\t"
        "buffer_load_dwordx4 v[160:163], v246, %[rd], s82 offen offset:0 sc1\n\t"
        "buffer_load_dwordx4 v[164:167], v246, %[rd], s82 offen offset:1024 sc1\n\t"
        "s_add_u32 s97, s97, 1\n\t"
        "s_cmp_lt_u32 s97, 0x200000\n\t"
        "s_cbranch_scc0 .Lgu%=\n\t"
        "s_waitcnt vmcnt(0)\n\t"
        "v_xor_b32_e32 v224, %[tag], v161\n\t"
        "v_xor_b32_e32 v225, %[tag], v163\n\t"
        "v_xor_b32_e32 v226, %[tag], v165\n\t"
        "v_or3_b32 v224, v224, v225, v226\n\t"
        "v_xor_b32_e32 v225, %[tag], v167\n\t"
        "v_or_b32_e32 v224, v224, v225\n\t"
        "v_cmp_ne_u32_e32 vcc, 0, v224\n\t"
        "s_nop 3\n\t"
        "s_cbranch_vccnz .Lo13%=r\n\t"
        "s_branch .Lo14%=\n\t"
        ".Lo15%=:\n\t"
        "s_mov_b32 s97, 0\n\t"
        "s_lshl_b32 s82, s83, 2\n\t"
        ".Lo15%=r:\n\t"
        "s_sleep %c[xslp]\n\t"
        "buffer_load_dwordx4 v[168:171], v246, %[rd], s82 offen offset:0 sc1\n\t"
        "buffer_load_dwordx4 v[172:175], v246, %[rd], s82 offen offset:1024 sc1\n\t"
        "s_add_u32 s97, s97, 1\n\t"
        "s_cmp_lt_u32 s97, 0x200000\n\t"
        "s_cbranch_scc0 .Lgu%=\n\t"
        "s_waitcnt vmcnt(0)\n\t"
        "v_xor_b32_e32 v224, %[tag], v169\n\t"
        "v_xor_b32_e32 v225, %[tag], v171\n\t"
        "v_xor_b32_e32 v226, %[tag], v173\n\t"
        "v_or3_b32 v224, v224, v225, v226\n\t"
        "v_xor_b32_e32 v225, %[tag], v175\n\t"
        "v_or_b32_e32 v224, v224, v225\n\t"
        "v_cmp_ne_u32_e32 vcc, 0, v224\n\t"
        "s_nop 3\n\t"
        "s_cbranch_vccnz .Lo15%=r\n\t"
        "s_branch .Lo16%=\n\t"
        ".Lgu%=:\n\t"
        "v_mov_b32_e32 v0, 128\n\t"
        "s_mov_b32 s80, %c[ferr]\n\t"
        "buffer_store_dword v0, off, %[rd], s80 sc1\n\t"
        "s_endpgm\n\t"
        ".Lhk33%=:\n\t"
        "s_branch .Lhr%="
        :: [rd] "s"(rd), [ro] "s"(ro), [rx] "s"(rx), [w] "s"(w), [rho] "s"(rho), [tag] "s"(tag), [own] "s"(own), [snd] "s"(snd), [ep] "s"(ep), [blk] "s"(j), [t0] "s"(g_t0),
          [ferr] "i"(F_ERR), [fprep] "i"(F_PREP), [fconv] "i"(F_CONV), [fprog] "i"(F_PROG), [dbg] "i"(W_DBG), [oslp] "i"(O_SLP), [xslp] "i"(X_SLP), [xdel] "i"(X_DEL), [ydel] "i"(Y_DEL), "{v[176:191]}"(pc)
        : "memory", "vcc", "scc", "s36", "s37", "s38", "s39", "s40", "s41", "s42", "s43", "s44", "s45", "s46", "s47", "s80", "s81", "s82", "s83", "s84", "s85", "s86", "s87", "s88", "s89", "s90", "s91", "s92", "s93", "s94", "s95", "s96", "s97", "s98", "s99", WO_REGS);
}

__device__ __forceinline__ void bulk_block(const Params& p, rsrc_t r, unsigned char* lds, const int wv, const int j, const unsigned tag, const unsigned g_t0) {
    const int x = (j & 7) >> 1, i32 = (j >> 3) * 2 + (j & 1);
    auto flags = [&](int mt, int nmt, int h0, int nh, int mat) {
        DRAIN(); __syncthreads();
        const int tid = otid(); if (tid < nmt * nh) st4(r, F_KVQ + ((mt + tid / nh) * 32 + h0 + tid % nh) * 64 + mat * 4, tag); };
#pragma unroll 1
    for (int t = 0; t < 2; ++t) {
        const int mt = 2 * t + (i32 & 1), mat = (i32 >> 1) & 1, hd = 8 * x + (i32 >> 2), rot = ((i32 >> 1) & 7) * 2;
        const bf16_t* Bm = mat ? p.Wv : p.Wk;
        if (mat) gemm<1, 4, 4, 8>(r, p.Aq, Bm, mt * 128, hd * 128, lds, W_VF, wv, rot, nullptr, t == 1, t == 0 ? Bm : nullptr, (mt + 2) * 128, hd * 128);
        else gemm<0, 4, 4, 8>(r, p.Aq, Bm, mt * 128, hd * 128, lds, W_KX, wv, rot, nullptr, t == 1, t == 0 ? Bm : nullptr, (mt + 2) * 128, hd * 128);
        flags(mt, 1, hd, 1, mat);
    }
    const int hp = 4 * x + (i32 >> 3), q8 = i32 & 7, rot = ROT2 * (i32 >> 2);
    if (SKEW) wait_long(rtnow() + (unsigned)((q8 & 3) * 4 + x) * SKEW);
    {
        const int m = q8 < 4 ? 4 + (q8 & 1) : q8 - 4, mat = q8 < 4 ? q8 >> 1 : 2;
        const bf16_t* Bn = mat == 2 ? p.Wq : mat == 1 ? p.Wv : p.Wk;
        const int mt = 4 + (q8 & 3), mv = q8 >> 2;
        const bf16_t* tk = (wv == 1 && j < 32) ? p.Aq + (64 + j) * 16384 : nullptr;
        if (mv) gemm<1, 4, 8, 4, 0, NoHook, 8>(r, p.Aq, p.Wv, mt * 128, hp * 256, lds, W_VF, wv, rot, tk, false, Bn, m * 256, hp * 256);
        else gemm<0, 4, 8, 4, 0, NoHook, 8>(r, p.Aq, p.Wk, mt * 128, hp * 256, lds, W_KX, wv, rot, tk, false, Bn, m * 256, hp * 256);
        flags(mt, 1, 2 * hp, 2, mv);
        const bf16_t* tl = (wv == 1 && j < 32) ? p.Aq + (96 + j) * 16384 : nullptr;
        if (mat == 1) gemm<1, 8, 8, 4>(r, p.Aq, Bn, m * 256, hp * 256, lds, W_VF, wv, rot, tl, true);
        else gemm<0, 8, 8, 4>(r, p.Aq, Bn, m * 256, hp * 256, lds, mat == 2 ? W_QX : W_KX, wv, rot, tl, true);
        flags(2 * m, 2, 2 * hp, 2, mat);
    }
    {
        unsigned wi = 0u;
        const int l = otid() & 63; const v4u sw = mk_srd(p.Wo, 4096 * 1024 * 2); const unsigned wa = (unsigned)(((i32 & 1) * 2048 + wv * 512 + (l >> 3)) * 2048 + (i32 >> 1) * 128 + (l & 7) * 16);
        unsigned long long tab;
        asm volatile("s_getpc_b64 %0\n\ts_branch .Lwo%=\n\t"
                     "buffer_load_dwordx4 a[128:131], v191, s[92:95], s91 offen\n\ts_setpc_b64 s[96:97]\n\t" "buffer_load_dwordx4 a[132:135], v191, s[92:95], s91 offen\n\ts_setpc_b64 s[96:97]\n\t"
                     "buffer_load_dwordx4 a[136:139], v191, s[92:95], s91 offen\n\ts_setpc_b64 s[96:97]\n\t" "buffer_load_dwordx4 a[140:143], v191, s[92:95], s91 offen\n\ts_setpc_b64 s[96:97]\n\t"
                     "buffer_load_dwordx4 a[144:147], v191, s[92:95], s91 offen\n\ts_setpc_b64 s[96:97]\n\t" "buffer_load_dwordx4 a[148:151], v191, s[92:95], s91 offen\n\ts_setpc_b64 s[96:97]\n\t"
                     "buffer_load_dwordx4 a[152:155], v191, s[92:95], s91 offen\n\ts_setpc_b64 s[96:97]\n\t" "buffer_load_dwordx4 a[156:159], v191, s[92:95], s91 offen\n\ts_setpc_b64 s[96:97]\n\t"
                     "buffer_load_dwordx4 a[160:163], v191, s[92:95], s91 offen\n\ts_setpc_b64 s[96:97]\n\t" "buffer_load_dwordx4 a[164:167], v191, s[92:95], s91 offen\n\ts_setpc_b64 s[96:97]\n\t"
                     "buffer_load_dwordx4 a[168:171], v191, s[92:95], s91 offen\n\ts_setpc_b64 s[96:97]\n\t" "buffer_load_dwordx4 a[172:175], v191, s[92:95], s91 offen\n\ts_setpc_b64 s[96:97]\n\t"
                     "buffer_load_dwordx4 a[176:179], v191, s[92:95], s91 offen\n\ts_setpc_b64 s[96:97]\n\t" "buffer_load_dwordx4 a[180:183], v191, s[92:95], s91 offen\n\ts_setpc_b64 s[96:97]\n\t"
                     "buffer_load_dwordx4 a[184:187], v191, s[92:95], s91 offen\n\ts_setpc_b64 s[96:97]\n\t" "buffer_load_dwordx4 a[188:191], v191, s[92:95], s91 offen\n\ts_setpc_b64 s[96:97]\n\t"
                     "buffer_load_dwordx4 a[192:195], v191, s[92:95], s91 offen\n\ts_setpc_b64 s[96:97]\n\t" "buffer_load_dwordx4 a[196:199], v191, s[92:95], s91 offen\n\ts_setpc_b64 s[96:97]\n\t"
                     "buffer_load_dwordx4 a[200:203], v191, s[92:95], s91 offen\n\ts_setpc_b64 s[96:97]\n\t" "buffer_load_dwordx4 a[204:207], v191, s[92:95], s91 offen\n\ts_setpc_b64 s[96:97]\n\t"
                     "buffer_load_dwordx4 a[208:211], v191, s[92:95], s91 offen\n\ts_setpc_b64 s[96:97]\n\t" "buffer_load_dwordx4 a[212:215], v191, s[92:95], s91 offen\n\ts_setpc_b64 s[96:97]\n\t"
                     "buffer_load_dwordx4 a[216:219], v191, s[92:95], s91 offen\n\ts_setpc_b64 s[96:97]\n\t" "buffer_load_dwordx4 a[220:223], v191, s[92:95], s91 offen\n\ts_setpc_b64 s[96:97]\n\t"
                     "buffer_load_dwordx4 a[224:227], v191, s[92:95], s91 offen\n\ts_setpc_b64 s[96:97]\n\t" "buffer_load_dwordx4 a[228:231], v191, s[92:95], s91 offen\n\ts_setpc_b64 s[96:97]\n\t"
                     "buffer_load_dwordx4 a[232:235], v191, s[92:95], s91 offen\n\ts_setpc_b64 s[96:97]\n\t" "buffer_load_dwordx4 a[236:239], v191, s[92:95], s91 offen\n\ts_setpc_b64 s[96:97]\n\t"
                     "buffer_load_dwordx4 a[240:243], v191, s[92:95], s91 offen\n\ts_setpc_b64 s[96:97]\n\t" "buffer_load_dwordx4 a[244:247], v191, s[92:95], s91 offen\n\ts_setpc_b64 s[96:97]\n\t"
                     "buffer_load_dwordx4 a[248:251], v191, s[92:95], s91 offen\n\ts_setpc_b64 s[96:97]\n\t" "buffer_load_dwordx4 a[252:255], v191, s[92:95], s91 offen\n\ts_setpc_b64 s[96:97]\n\t"
                     "buffer_load_dwordx4 v[192:195], v191, s[92:95], s91 offen\n\ts_setpc_b64 s[96:97]\n\t" "buffer_load_dwordx4 v[196:199], v191, s[92:95], s91 offen\n\ts_setpc_b64 s[96:97]\n\t"
                     "buffer_load_dwordx4 v[200:203], v191, s[92:95], s91 offen\n\ts_setpc_b64 s[96:97]\n\t" "buffer_load_dwordx4 v[204:207], v191, s[92:95], s91 offen\n\ts_setpc_b64 s[96:97]\n\t"
                     "buffer_load_dwordx4 v[208:211], v191, s[92:95], s91 offen\n\ts_setpc_b64 s[96:97]\n\t" "buffer_load_dwordx4 v[212:215], v191, s[92:95], s91 offen\n\ts_setpc_b64 s[96:97]\n\t"
                     "buffer_load_dwordx4 v[216:219], v191, s[92:95], s91 offen\n\ts_setpc_b64 s[96:97]\n\t" "buffer_load_dwordx4 v[220:223], v191, s[92:95], s91 offen\n\ts_setpc_b64 s[96:97]\n\t"
                     "buffer_load_dwordx4 v[224:227], v191, s[92:95], s91 offen\n\ts_setpc_b64 s[96:97]\n\t" "buffer_load_dwordx4 v[228:231], v191, s[92:95], s91 offen\n\ts_setpc_b64 s[96:97]\n\t"
                     "buffer_load_dwordx4 v[232:235], v191, s[92:95], s91 offen\n\ts_setpc_b64 s[96:97]\n\t" "buffer_load_dwordx4 v[236:239], v191, s[92:95], s91 offen\n\ts_setpc_b64 s[96:97]\n\t"
                     "buffer_load_dwordx4 v[240:243], v191, s[92:95], s91 offen\n\ts_setpc_b64 s[96:97]\n\t" "buffer_load_dwordx4 v[244:247], v191, s[92:95], s91 offen\n\ts_setpc_b64 s[96:97]\n\t"
                     "buffer_load_dwordx4 v[248:251], v191, s[92:95], s91 offen\n\ts_setpc_b64 s[96:97]\n\t" "buffer_load_dwordx4 v[252:255], v191, s[92:95], s91 offen\n\ts_setpc_b64 s[96:97]\n\t"
                     "buffer_load_dwordx4 v191, s[92:95], s91 offen lds\n\ts_setpc_b64 s[96:97]\n.Lwo%=:" : "=s"(tab));
        auto hook = [&](int) {
            const unsigned q = min(wi, 63u);
            asm volatile("s_mov_b32 m0, %1\n\ts_swappc_b64 s[96:97], %0"
                         :: "s"(tab + (min(q, 48u) * 12u + 4u)), "s"(WO_L + wv * 16384 + (q - 48u) * 1024u), "{v191}"(wa), "{s[92:95]}"(sw), "{s91}"(q * 16384u) : "memory", "m0", "s96", "s97", WO_REGS);
            ++wi; };
#pragma unroll 1
        for (int t = 0; t < 2; ++t) {
            const int mt = t ? 8 + q8 : 12 + (q8 & 3), mv = t ? 2 : q8 >> 2;
            if (mv == 1) gemm<1, 4, 8, 4, 1>(r, p.Aq, p.Wv, mt * 128, hp * 256, lds, W_VF, wv, rot, nullptr, false, p.Wq, (8 + q8) * 128, hp * 256, hook);
            else gemm<0, 4, 8, 4, 1>(r, p.Aq, t ? p.Wq : p.Wk, mt * 128, hp * 256, lds, t ? W_QX : W_KX, wv, rot, nullptr, t == 1, t ? nullptr : p.Wq, (8 + q8) * 128, hp * 256, hook);
            flags(mt, 1, 2 * hp, 2, mv);
        }
        oproj_block(p, wv, i32, x, j, tag, g_t0);
    }
}

__device__ __forceinline__ void help_block(const Params& p, rsrc_t r, unsigned char* lds, const int wv, const int k, const unsigned tag, const unsigned far) {
    const int j = -32 + k; (void)j;
    {
        const unsigned th = rtnow() + HELP_D + (unsigned)k * SKEWP, OOB = 0xF0000000u;
        const int l = otid() & 63, n16 = l & 15;
        const rsrc_t rh = __builtin_amdgcn_make_buffer_rsrc(p.ws + hx_base(p.ws), 0, U_END * 16384, 0x00020000);
        const unsigned un = (unsigned)(wv * 32 + (k & 30)) * 16384u + (k & 1) * 4096u, ca = l == 0 ? un : OOB;
        v4u z[4]; unsigned best[4] = {~0u, ~0u, ~0u, ~0u};
#pragma unroll
        for (int i = 0; i < 4; ++i) z[i] = (v4u){~0u, ~0u, ~0u, ~0u};
        for (int i = 0; i < CAL_N; ++i)
#pragma unroll
            for (int q = 0; q < 4; ++q) {
                const unsigned t0 = (unsigned)__builtin_amdgcn_s_memtime(); __builtin_amdgcn_s_waitcnt(0);
                st16(rh, ca + q * 8192, z[0]); DRAIN();
                const unsigned t1 = (unsigned)__builtin_amdgcn_s_memtime(); __builtin_amdgcn_s_waitcnt(0);
                best[q] = min(best[q], t1 - t0); }
        const unsigned up = min(best[1], best[3]) < min(best[0], best[2]) ? 1u : 0u, vote = (up ^ far) | ((up ? best[2] < best[0] : best[3] < best[1]) ? 2u : 0u);
        if (wv == 0 && k < 8) {
#pragma unroll
            for (int i = 0; i < 4; ++i) z[i] = __builtin_elementwise_min(*(const v4u*)(p.mem0 + ((n16 & 1) + 2 * (n16 >> 2)) * 1024 + ((k & 3) * 4 + i) * 64 + (l >> 4) * 16 + (n16 & 2) * 4), (v4u){~1u, ~1u, ~1u, ~1u}); }
#pragma unroll
        for (int i = 0; i < 16; ++i) st16(rh, un + (i >> 2) * 8192 + (i & 3) * 1024 + l * 16, z[i & 3]);
        st4(r, (wv == 3 && l == 0) ? F_PROG + k * 128 : OOB, 0u);
        st16(r, HO_KVM + (63 * 32 + k) * 4096 + wv * 1024 + l * 16, (v4u){~0u, ~0u, ~0u, ~0u});
        DRAIN();
        st8(r, l == 0 ? F_CAL + k * 32 + wv * 8 : OOB, (v2u){tag, vote});
        wait_long(th);
    }
    prep_block(p, r, k, wv, tag);
    if (!conv_block(p, r, k * 8, 8, lds, wv, j, tag, (unsigned)k * SKEWH)) return;
    attn_block(p, wv, k, tag);
}

__global__ void __launch_bounds__(256, 1) k_main(Params p) {
    extern __shared__ __attribute__((aligned(16))) unsigned char lds[];
    const unsigned g_t0 = DIAG ? rtnow() : 0u;
    rsrc_t r = __builtin_amdgcn_make_buffer_rsrc(p.ws, 0, W_TOTAL, 0x00020000);
    const int wv = __builtin_amdgcn_readfirstlane(threadIdx.x >> 6);
    int role = blockIdx.x;
    unsigned xcc; asm volatile("s_getreg_b32 %0, hwreg(HW_REG_XCC_ID)" : "=s"(xcc)); xcc &= 7u;
    unsigned old = 0u, xr = xcc, rk = blockIdx.x >> 3;
    if (MIXH) { if (xcc == 3u) { xr = 4u + (rk >> 3); rk = 24u + (rk & 7u); } else if (xcc > 3u && rk >= 24u) { xr = 3u; rk = (xcc - 4u) * 8u + (rk & 7u); } }
    unsigned slot = (xr >> 2) * 128u + (rk >> 1) * 8u + (xr & 3u) * 2u + (rk & 1u);
    const unsigned tag = (__builtin_amdgcn_readfirstlane(ld4(r, (threadIdx.x & 63) == 1 ? F_CLM + slot * 64 + 4 : F_EP)) * 2654435761u + 0x9E3779B9u) | 1u;
    if (threadIdx.x == 0) old = __hip_atomic_exchange((unsigned*)(p.ws + F_CLM + slot * 64), tag, __ATOMIC_RELAXED, __HIP_MEMORY_SCOPE_AGENT);
    if (threadIdx.x == 0) {
        for (int i = 0; i < 256 && old == tag; ++i) { slot = (slot + 1u) & 255u; old = __hip_atomic_exchange((unsigned*)(p.ws + F_CLM + slot * 64), tag, __ATOMIC_RELAXED, __HIP_MEMORY_SCOPE_AGENT); }
        *(volatile __attribute__((address_space(3))) int*)(size_t)(unsigned)(size_t)lds = (int)slot;
    }
    __syncthreads();
    role = __builtin_amdgcn_readfirstlane(*(volatile __attribute__((address_space(3))) int*)(size_t)(unsigned)(size_t)lds) & 255;
    __syncthreads();
    if (DIAG >= 10 && blockIdx.x < 8 && threadIdx.x == 0) st4(r, F_PROG + 72 + xcc * 128, g_t0);
    if (role >= 128) bulk_block(p, r, lds, wv, role - 128, tag, g_t0);
    else if ((role & 6) == 6) help_block(p, r, lds, wv, (role >> 3) * 2 + (role & 1), tag, xcc >> 2);
    else rec_block(p, wv, role, tag);
}

extern "C" void kernel_launch(void* const* d_in, const int* in_sizes, int n_in, void* d_out, int out_size, void* d_ws, size_t ws_size, hipStream_t stream) {
    Params p;
    p.Aq = (const bf16_t*)d_in[0]; p.Ak = (const bf16_t*)d_in[1]; p.Av = (const bf16_t*)d_in[2];
    p.shift_k = (const bf16_t*)d_in[3]; p.ssm_k = (const bf16_t*)d_in[4]; p.ssm_D = (const bf16_t*)d_in[5];
    p.mem0 = (const bf16_t*)d_in[7]; p.Wq = (const bf16_t*)d_in[8]; p.Wk = (const bf16_t*)d_in[9];
    p.Wv = (const bf16_t*)d_in[10]; p.Wo = (const bf16_t*)d_in[11]; p.gate_w = (const bf16_t*)d_in[13];
    p.out = (bf16_t*)d_out; p.ws = (unsigned char*)d_ws;
    (void)hipFuncSetAttribute((const void*)k_main, hipFuncAttributeMaxDynamicSharedMemorySize, L_TOTAL);
    k_main<<<256, 256, L_TOTAL, stream>>>(p);
}
```

```cpp
#include <hip/hip_runtime.h>
#include <stdint.h>
#include <type_traits>
typedef unsigned short bf16_t;
using bf16x8 = __attribute__((ext_vector_type(8))) short;
using f32x4 = __attribute__((ext_vector_type(4))) float;
typedef unsigned v4u __attribute__((ext_vector_type(4)));
typedef unsigned v2u __attribute__((ext_vector_type(2)));
typedef __amdgpu_buffer_rsrc_t rsrc_t;

typedef __bf16 hbf2 __attribute__((ext_vector_type(2)));
typedef float f32x2 __attribute__((ext_vector_type(2)));
__device__ __forceinline__ unsigned pack2(float a, float b) { f32x2 v = {a, b}; return __builtin_bit_cast(unsigned, __builtin_convertvector(v, hbf2)); }
__device__ __forceinline__ float rbfh(float f) { return __uint_as_float(pack2(0.f, f)); }
__device__ __forceinline__ f32x2 rbf2(f32x2 f) { return (f32x2){rbfh(f.x), rbfh(f.y)}; }

constexpr int S = 2048, D = 1024;

constexpr unsigned W_FLG = 0;
constexpr unsigned F_KVQ = W_FLG;
constexpr unsigned F_EP = W_FLG + 32768;
constexpr unsigned F_ERR = W_FLG + 32768 + 256;
constexpr unsigned F_PREP = W_FLG + 32768 + 512;
constexpr unsigned F_CAL = W_FLG + 73728;
constexpr unsigned F_CLM = W_FLG + 77824;
constexpr unsigned F_CONV = W_FLG + 33792;
constexpr unsigned F_PROG = W_FLG + 36864;
constexpr unsigned F_ATT = W_FLG + 40960;
constexpr unsigned F_RDY = W_FLG + 49152;
constexpr unsigned FLG_BYTES = 94208;
constexpr unsigned HO_KVM = W_FLG + FLG_BYTES;
constexpr unsigned W_GATE = HO_KVM + 64 * 32 * 4096;
constexpr unsigned W_QX = W_GATE + 8192;
constexpr unsigned W_KX = W_QX + S * 4096 * 2;
constexpr unsigned W_VF = W_KX + S * 4096 * 2;
constexpr unsigned DBG_BYTES = 0;
constexpr unsigned W_DBG = W_VF + 64 * 32 * 8192;
constexpr unsigned W_U = W_DBG + DBG_BYTES;
constexpr unsigned W_TOTAL = W_U + S * D * 2;

__device__ __forceinline__ v4u ld16(rsrc_t r, unsigned off) { return __builtin_amdgcn_raw_buffer_load_b128(r, off, 0, 16); }
__device__ __forceinline__ unsigned ld4(rsrc_t r, unsigned off) { return __builtin_amdgcn_raw_buffer_load_b32(r, off, 0, 16); }
__device__ __forceinline__ void st16(rsrc_t r, unsigned off, v4u v) { __builtin_amdgcn_raw_buffer_store_b128(v, r, off, 0, 16); }
__device__ __forceinline__ void st8(rsrc_t r, unsigned off, v2u v) { __builtin_amdgcn_raw_buffer_store_b64(v, r, off, 0, 16); }
__device__ __forceinline__ void st4(rsrc_t r, unsigned off, unsigned v) { __builtin_amdgcn_raw_buffer_store_b32(v, r, off, 0, 16); }
__device__ __forceinline__ bf16x8 as_frag(v4u v) { return __builtin_bit_cast(bf16x8, v); }
#define D1 128
#define D2 82
#define LC0 7
#define LA0 8
#define TR_UP 4
#define TR_DN 2
#define L_DEC 15
#define UP_SH 2
#define EARLY_FROM 58
#define P_SLP 2
#define O_SLP 1
#define TOL 2
#define A_SLP 0
#define PEN2 20
#define A_UP 6
#define CT0 2
#define CPF 1
#define PH_M 3
#define SKEW 20
#define ROT2 2
#define HX_SH 0u
#define FINE_N 0
#define PEN0 32
#define X0C 24
#define X0A 20
#define PEN 40
#define MEMBAR() asm volatile("" ::: "memory")
#define ENDPGM() asm volatile("s_endpgm" ::: "memory")
#define DRAIN() asm volatile("s_waitcnt vmcnt(0)" ::: "memory")

typedef short v4i16_t __attribute__((ext_vector_type(4)));
__device__ __forceinline__ v2u trb(unsigned lds_addr) { return __builtin_bit_cast(v2u, __builtin_amdgcn_ds_read_tr16_b64_v4i16((__attribute__((address_space(3))) v4i16_t*)(size_t)lds_addr)); }
__device__ __forceinline__ int otid_(int wv) { int t; asm volatile("v_mbcnt_lo_u32_b32 %0, -1, 0\n\tv_mbcnt_hi_u32_b32 %0, -1, %0" : "=v"(t)); return wv * 64 + t; }
#define otid() otid_(wv)
__device__ __forceinline__ bool wait_prep(rsrc_t r, int l, unsigned tag) {
    for (unsigned spin = 0;; ++spin) { MEMBAR();
        const unsigned v = ld4(r, F_PREP + (l & 31) * 4);
        if (__all(v == tag)) return true;
        if (spin > (1u << 20)) { st4(r, F_ERR, 512u); return false; }
        __builtin_amdgcn_s_sleep(8); }
}
__device__ __forceinline__ unsigned rtnow() { return (unsigned)__builtin_amdgcn_s_memrealtime(); }
__device__ __forceinline__ void wait_long(unsigned target) { for (int i = 0; i < 4096; ++i) { if ((int)(rtnow() - target) >= 0) break; __builtin_amdgcn_s_sleep(8); } }
constexpr int L_TAB = 20480;
constexpr int L_QK = 24576;
__device__ __forceinline__ unsigned img(int f) { return (unsigned)(L_QK + f * 16 + (f >> 4) * 128); }
constexpr int L_FL = 30720, L_Z = L_FL + 128, L_LOG = 31744, L_W = 32768, L_V = 131072;
constexpr int L_TOTAL = 163840;
constexpr unsigned U_END = 96;
#define PMIN 82
#define V_SLP 1
#define R_SLP 0
constexpr unsigned WO_L = 98304;
#define HELP_D 200
#define V_DEL 24
#define XR 8
#define KEYM 3
#define DIAG 0
#define NEARX 0
#define MIXH 1
#define SKEWH 10
#define SKEWP 8
#define NEARY 0
#define CS1 0x8000u
#define CS2 0x10000u
#define X_SLP 2
#define X_DEL 10
#define DN_SH 1
#define AT_SLP 12
#define VA_SLP 0
#define C_SLP 0
#define BT_SLP 0
#define L_SLP 10
#define Y_DEL 6
#define ST_M 0x33330000
#define ST_S 32
#define BM_ 10
#define BS_ 3
#define H_SLP 0
#define CAL_N 3
#define STEP_MIN 180u

struct Params {
    const bf16_t *Aq, *Ak, *Av, *shift_k, *ssm_k, *ssm_D, *mem0, *Wq, *Wk, *Wv, *Wo, *gate_w;
    bf16_t* out; unsigned char* ws;
};
__host__ __device__ __forceinline__ unsigned hx_base(const void* ws) { const size_t a = (size_t)ws; return (unsigned)((((a + W_TOTAL + 0x7FFFFFu) >> 23) << 23) - a) + HX_SH; }

typedef short v4s __attribute__((ext_vector_type(4)));
typedef unsigned v32u __attribute__((ext_vector_type(32)));
typedef unsigned v16u __attribute__((ext_vector_type(16)));
typedef unsigned v8u __attribute__((ext_vector_type(8)));
typedef unsigned v12u __attribute__((ext_vector_type(12)));
__device__ __forceinline__ v4u mk_srd(const void* p, unsigned bytes) { const size_t a = (size_t)p; v4u d;
    d.x = __builtin_amdgcn_readfirstlane((unsigned)a); d.y = __builtin_amdgcn_readfirstlane((unsigned)(a >> 32) & 0xFFFFu); d.z = __builtin_amdgcn_readfirstlane(bytes); d.w = 0x00020000u; return d; }
__device__ __forceinline__ bf16x8 ldsa(unsigned addr) { return *(const __attribute__((address_space(3))) bf16x8*)(size_t)addr; }
__device__ __forceinline__ void rec_block(const Params& p, const int wv, const int b, const unsigned tag) {
    const int w = wv, tid = otid(), l = tid & 63, n16 = l & 15, g = l >> 4, c = n16 >> 2;
    const int x = (b & 7) >> 1, i32 = (b >> 3) * 2 + (b & 1);
    const unsigned OOB = 0xF0000000u;
    const v4u rhd = mk_srd(p.ws + hx_base(p.ws), U_END * 16384), wsd = mk_srd(p.ws, W_TOTAL);
    const int G = x - 1, vi = G * 32 + i32, h = x == 0 ? i32 : vi >> 1;
    const unsigned pr_a = F_PROG + h * 128, cal = F_CAL - w * 1024, kvo = HO_KVM + h * 4096;
    if (CPF) {
        unsigned lo, hi, l2; asm volatile("s_getpc_b64 s[98:99]\n.Lpc%=:\n\ts_add_u32 %2, s98, rec_v_code-.Lpc%=\n\ts_add_u32 %0, s98, rec_s_code-.Lpc%=\n\ts_addc_u32 %1, s99, 0" : "=s"(lo), "=s"(hi), "=s"(l2) :: "s98", "s99", "scc");
        const size_t a = (((size_t)hi << 32) | lo) + (size_t)(long long)((x ? (int)(l2 - lo) : 0) + ((i32 * 4 + w) % 12) * 1024 - 4096 + l * 16);
        asm volatile("s_mov_b32 m0, %1\n\ts_nop 0\n\tglobal_load_lds_dwordx4 %0, off" :: "v"(a & ~(size_t)15), "s"(x ? L_V + 16384 : 0) : "memory", "m0");
    }
    typedef __attribute__((address_space(3))) volatile unsigned lu32;
    auto cpy = [](int k) -> unsigned { return k == 1 ? 0u : k * 32768u + 16384u; };
    auto cun = [](int u) -> unsigned { return ((u & 1) ? CS1 : 0u) ^ ((u & 2) ? CS2 : 0u); };
    unsigned nA = cpy(x), nW = cpy(w), nT = 64 * 16384 + (x ? 0 : h * 8), nS = 64 * 16384 + x * 1024, nC = 32 * 16384 + (x == 2 ? 16384 : 0), b0 = 65 * 16384 + (h >> 2) * 32768 + (h & 3) * 2048;
    unsigned pob = w == 0 ? nA + (i32 >> 4) * 32768 + (i32 & 15) * 512 : (w == 1 && x) ? (nC ^ cun(i32 >> 3)) + (i32 & 7) * 1024 : (w == 2 && i32 == 0) ? nS : OOB;
    if (x == 0) {
        const v4u sq = mk_srd(p.Wq, 1024 * 4096 * 2), sk = mk_srd(p.Wv, 1024 * 4096 * 2);
        const unsigned m0b = L_W + w * 1024, f = (c & 1) | ((g & 1) << 1) | ((c >> 1) << 2);
        const unsigned t0 = L_W + (16 * (g >> 1) + 4 * (g & 1) + (c & 1) + 8 * (c >> 1)) * 256 + (((2 * w) ^ f) << 5) + (n16 & 3) * 8;
        const unsigned dv = (4 * w + g) * 8192 + h * 256 + ((n16 ^ (((g & 1) | (w << 1)) << 1)) << 4);
        v8u pa;
#pragma unroll
        for (int q = 0; q < 8; ++q) pa[q] = (t0 + ((w + (q >> 1)) & 3) * 32768) ^ ((q & 1) << 5);
        const unsigned kx = W_KX + h * 8192, vx = W_VF + h * 8192 + w * 1024, kf = F_KVQ + h * 64;
        const unsigned aq = img(8 * g + (n16 >> 2)) + (n16 & 1) * 8;
        const v8u pc = {(unsigned)tid * 16, (unsigned)(L_W + w * 32768 + l * 16), (n16 & 2) ? 0xEEEEu : 0x4444u, img(16 * (g >> 1) + 4 * (g & 1) + (c & 1) + 8 * (c >> 1)) + (n16 & 1) * 8,
                  (unsigned)(((w + 1) & 3) * 1024 + l * 16), (unsigned)(((w + 2) & 3) * 1024 + l * 16), (unsigned)(((w + 3) & 3) * 1024 + l * 16), img(32 * w + n16) + g * 4};
        const v2u pd = {(n16 & 2) ? (unsigned)L_Z : aq + w * 768, (n16 & 2) ? aq + w * 768 : (unsigned)L_Z};
        *(__attribute__((address_space(3))) v4u*)(size_t)(L_TAB + tid * 16) = (v4u){(unsigned)(L_QK + 3072 + g * 384 + n16 * 8),
                  n16 < 8 ? (unsigned)((((n16 >> 2) * 16384) ^ cun((h >> 1) & 3)) + ((h & 1) * 4 + (h >> 3)) * 1024 + w * 256 + (g >> 1) * 128 + (n16 & 2) * 32 + (g & 1) * 32 + (n16 & 1) * 16) : OOB,
                  (unsigned)((32 * w + 16 * (g & 1) + n16) * 16 + (g >> 1) * 8), (unsigned)(L_QK + 3072 + w * 768 + ((n16 & 1) + 2 * (n16 >> 2)) * 32 + g * 8 + (n16 & 2) * 2)};
        if (tid < 64) ((lu32*)(size_t)L_FL)[tid] = 0u;
    asm volatile(
        "s_add_u32 s80, %[m0b], 0\n\t"
        "s_mov_b32 s81, 0\n\t"
        "s_mov_b32 s82, 4\n\t"
        ".Ld0%=:\n\t"
        "s_mov_b32 m0, s80\n\t"
        "s_add_u32 s80, s80, 0x8000\n\t"
        "buffer_load_dwordx4 v96, %[sq], s81 offen lds\n\t"
        "s_add_u32 m0, m0, 0x1000\n\t"
        "s_add_u32 s81, s81, 0x20000\n\t"
        "buffer_load_dwordx4 v96, %[sq], s81 offen lds\n\t"
        "s_add_u32 m0, m0, 0x1000\n\t"
        "s_add_u32 s81, s81, 0x20000\n\t"
        "buffer_load_dwordx4 v96, %[sq], s81 offen lds\n\t"
        "s_add_u32 m0, m0, 0x1000\n\t"
        "s_add_u32 s81, s81, 0x20000\n\t"
        "buffer_load_dwordx4 v96, %[sq], s81 offen lds\n\t"
        "s_add_u32 m0, m0, 0x1000\n\t"
        "s_add_u32 s81, s81, 0x20000\n\t"
        "s_sub_u32 s82, s82, 1\n\t"
        "s_cmp_lg_u32 s82, 0\n\t"
        "s_cbranch_scc1 .Ld0%=\n\t"
        "s_add_u32 s80, %[m0b], 16384\n\t"
        "s_mov_b32 s81, 0\n\t"
        "s_mov_b32 s82, 4\n\t"
        ".Ld1%=:\n\t"
        "s_mov_b32 m0, s80\n\t"
        "s_add_u32 s80, s80, 0x8000\n\t"
        "buffer_load_dwordx4 v96, %[sk], s81 offen lds\n\t"
        "s_add_u32 m0, m0, 0x1000\n\t"
        "s_add_u32 s81, s81, 0x20000\n\t"
        "buffer_load_dwordx4 v96, %[sk], s81 offen lds\n\t"
        "s_add_u32 m0, m0, 0x1000\n\t"
        "s_add_u32 s81, s81, 0x20000\n\t"
        "buffer_load_dwordx4 v96, %[sk], s81 offen lds\n\t"
        "s_add_u32 m0, m0, 0x1000\n\t"
        "s_add_u32 s81, s81, 0x20000\n\t"
        "buffer_load_dwordx4 v96, %[sk], s81 offen lds\n\t"
        "s_add_u32 m0, m0, 0x1000\n\t"
        "s_add_u32 s81, s81, 0x20000\n\t"
        "s_sub_u32 s82, s82, 1\n\t"
        "s_cmp_lg_u32 s82, 0\n\t"
        "s_cbranch_scc1 .Ld1%=\n\t"
        "s_waitcnt vmcnt(16)\n\t"
        "s_barrier\n\t"
        "ds_read_b64_tr_b16 v[0:1], v98 offset:0\n\t"
        "ds_read_b64_tr_b16 v[2:3], v98 offset:512\n\t"
        "ds_read_b64_tr_b16 v[4:5], v98 offset:8192\n\t"
        "ds_read_b64_tr_b16 v[6:7], v98 offset:8704\n\t"
        "ds_read_b64_tr_b16 v[8:9], v99 offset:0\n\t"
        "ds_read_b64_tr_b16 v[10:11], v99 offset:512\n\t"
        "ds_read_b64_tr_b16 v[12:13], v99 offset:8192\n\t"
        "ds_read_b64_tr_b16 v[14:15], v99 offset:8704\n\t"
        "ds_read_b64_tr_b16 a[16:17], v100 offset:0\n\t"
        "ds_read_b64_tr_b16 a[18:19], v100 offset:512\n\t"
        "ds_read_b64_tr_b16 a[20:21], v100 offset:8192\n\t"
        "ds_read_b64_tr_b16 a[22:23], v100 offset:8704\n\t"
        "ds_read_b64_tr_b16 a[24:25], v101 offset:0\n\t"
        "ds_read_b64_tr_b16 a[26:27], v101 offset:512\n\t"
        "ds_read_b64_tr_b16 a[28:29], v101 offset:8192\n\t"
        "ds_read_b64_tr_b16 a[30:31], v101 offset:8704\n\t"
        "ds_read_b64_tr_b16 a[32:33], v102 offset:0\n\t"
        "ds_read_b64_tr_b16 a[34:35], v102 offset:512\n\t"
        "ds_read_b64_tr_b16 a[36:37], v102 offset:8192\n\t"
        "ds_read_b64_tr_b16 a[38:39], v102 offset:8704\n\t"
        "ds_read_b64_tr_b16 a[40:41], v103 offset:0\n\t"
        "ds_read_b64_tr_b16 a[42:43], v103 offset:512\n\t"
        "ds_read_b64_tr_b16 a[44:45], v103 offset:8192\n\t"
        "ds_read_b64_tr_b16 a[46:47], v103 offset:8704\n\t"
        "ds_read_b64_tr_b16 a[48:49], v104 offset:0\n\t"
        "ds_read_b64_tr_b16 a[50:51], v104 offset:512\n\t"
        "ds_read_b64_tr_b16 a[52:53], v104 offset:8192\n\t"
        "ds_read_b64_tr_b16 a[54:55], v104 offset:8704\n\t"
        "ds_read_b64_tr_b16 a[56:57], v105 offset:0\n\t"
        "ds_read_b64_tr_b16 a[58:59], v105 offset:512\n\t"
        "ds_read_b64_tr_b16 a[60:61], v105 offset:8192\n\t"
        "ds_read_b64_tr_b16 a[62:63], v105 offset:8704\n\t"
        "s_waitcnt lgkmcnt(0)\n\t"
        "s_barrier\n\t"
        "s_add_u32 s80, %[m0b], 0\n\t"
        "s_mov_b32 s81, 2097152\n\t"
        "s_mov_b32 s82, 4\n\t"
        ".Ld2%=:\n\t"
        "s_mov_b32 m0, s80\n\t"
        "s_add_u32 s80, s80, 0x8000\n\t"
        "buffer_load_dwordx4 v96, %[sq], s81 offen lds\n\t"
        "s_add_u32 m0, m0, 0x1000\n\t"
        "s_add_u32 s81, s81, 0x20000\n\t"
        "buffer_load_dwordx4 v96, %[sq], s81 offen lds\n\t"
        "s_add_u32 m0, m0, 0x1000\n\t"
        "s_add_u32 s81, s81, 0x20000\n\t"
        "buffer_load_dwordx4 v96, %[sq], s81 offen lds\n\t"
        "s_add_u32 m0, m0, 0x1000\n\t"
        "s_add_u32 s81, s81, 0x20000\n\t"
        "buffer_load_dwordx4 v96, %[sq], s81 offen lds\n\t"
        "s_add_u32 m0, m0, 0x1000\n\t"
        "s_add_u32 s81, s81, 0x20000\n\t"
        "s_sub_u32 s82, s82, 1\n\t"
        "s_cmp_lg_u32 s82, 0\n\t"
        "s_cbranch_scc1 .Ld2%=\n\t"
        "s_waitcnt vmcnt(16)\n\t"
        "s_barrier\n\t"
        "ds_read_b64_tr_b16 v[16:17], v98 offset:16384\n\t"
        "ds_read_b64_tr_b16 v[18:19], v98 offset:16896\n\t"
        "ds_read_b64_tr_b16 v[20:21], v98 offset:24576\n\t"
        "ds_read_b64_tr_b16 v[22:23], v98 offset:25088\n\t"
        "ds_read_b64_tr_b16 v[24:25], v99 offset:16384\n\t"
        "ds_read_b64_tr_b16 v[26:27], v99 offset:16896\n\t"
        "ds_read_b64_tr_b16 v[28:29], v99 offset:24576\n\t"
        "ds_read_b64_tr_b16 v[30:31], v99 offset:25088\n\t"
        "ds_read_b64_tr_b16 a[80:81], v100 offset:16384\n\t"
        "ds_read_b64_tr_b16 a[82:83], v100 offset:16896\n\t"
        "ds_read_b64_tr_b16 a[84:85], v100 offset:24576\n\t"
        "ds_read_b64_tr_b16 a[86:87], v100 offset:25088\n\t"
        "ds_read_b64_tr_b16 a[88:89], v101 offset:16384\n\t"
        "ds_read_b64_tr_b16 a[90:91], v101 offset:16896\n\t"
        "ds_read_b64_tr_b16 a[92:93], v101 offset:24576\n\t"
        "ds_read_b64_tr_b16 a[94:95], v101 offset:25088\n\t"
        "ds_read_b64_tr_b16 a[96:97], v102 offset:16384\n\t"
        "ds_read_b64_tr_b16 a[98:99], v102 offset:16896\n\t"
        "ds_read_b64_tr_b16 a[100:101], v102 offset:24576\n\t"
        "ds_read_b64_tr_b16 a[102:103], v102 offset:25088\n\t"
        "ds_read_b64_tr_b16 a[104:105], v103 offset:16384\n\t"
        "ds_read_b64_tr_b16 a[106:107], v103 offset:16896\n\t"
        "ds_read_b64_tr_b16 a[108:109], v103 offset:24576\n\t"
        "ds_read_b64_tr_b16 a[110:111], v103 offset:25088\n\t"
        "ds_read_b64_tr_b16 a[112:113], v104 offset:16384\n\t"
        "ds_read_b64_tr_b16 a[114:115], v104 offset:16896\n\t"
        "ds_read_b64_tr_b16 a[116:117], v104 offset:24576\n\t"
        "ds_read_b64_tr_b16 a[118:119], v104 offset:25088\n\t"
        "ds_read_b64_tr_b16 a[120:121], v105 offset:16384\n\t"
        "ds_read_b64_tr_b16 a[122:123], v105 offset:16896\n\t"
        "ds_read_b64_tr_b16 a[124:125], v105 offset:24576\n\t"
        "ds_read_b64_tr_b16 a[126:127], v105 offset:25088\n\t"
        "s_waitcnt lgkmcnt(0)\n\t"
        "s_barrier\n\t"
        "s_add_u32 s80, %[m0b], 16384\n\t"
        "s_mov_b32 s81, 2097152\n\t"
        "s_mov_b32 s82, 4\n\t"
        ".Ld3%=:\n\t"
        "s_mov_b32 m0, s80\n\t"
        "s_add_u32 s80, s80, 0x8000\n\t"
        "buffer_load_dwordx4 v96, %[sk], s81 offen lds\n\t"
        "s_add_u32 m0, m0, 0x1000\n\t"
        "s_add_u32 s81, s81, 0x20000\n\t"
        "buffer_load_dwordx4 v96, %[sk], s81 offen lds\n\t"
        "s_add_u32 m0, m0, 0x1000\n\t"
        "s_add_u32 s81, s81, 0x20000\n\t"
        "buffer_load_dwordx4 v96, %[sk], s81 offen lds\n\t"
        "s_add_u32 m0, m0, 0x1000\n\t"
        "s_add_u32 s81, s81, 0x20000\n\t"
        "buffer_load_dwordx4 v96, %[sk], s81 offen lds\n\t"
        "s_add_u32 m0, m0, 0x1000\n\t"
        "s_add_u32 s81, s81, 0x20000\n\t"
        "s_sub_u32 s82, s82, 1\n\t"
        "s_cmp_lg_u32 s82, 0\n\t"
        "s_cbranch_scc1 .Ld3%=\n\t"
        "s_waitcnt vmcnt(16)\n\t"
        "s_barrier\n\t"
        "ds_read_b64_tr_b16 v[32:33], v98 offset:0\n\t"
        "ds_read_b64_tr_b16 v[34:35], v98 offset:512\n\t"
        "ds_read_b64_tr_b16 v[36:37], v98 offset:8192\n\t"
        "ds_read_b64_tr_b16 v[38:39], v98 offset:8704\n\t"
        "ds_read_b64_tr_b16 v[40:41], v99 offset:0\n\t"
        "ds_read_b64_tr_b16 v[42:43], v99 offset:512\n\t"
        "ds_read_b64_tr_b16 v[44:45], v99 offset:8192\n\t"
        "ds_read_b64_tr_b16 v[46:47], v99 offset:8704\n\t"
        "ds_read_b64_tr_b16 a[144:145], v100 offset:0\n\t"
        "ds_read_b64_tr_b16 a[146:147], v100 offset:512\n\t"
        "ds_read_b64_tr_b16 a[148:149], v100 offset:8192\n\t"
        "ds_read_b64_tr_b16 a[150:151], v100 offset:8704\n\t"
        "ds_read_b64_tr_b16 a[152:153], v101 offset:0\n\t"
        "ds_read_b64_tr_b16 a[154:155], v101 offset:512\n\t"
        "ds_read_b64_tr_b16 a[156:157], v101 offset:8192\n\t"
        "ds_read_b64_tr_b16 a[158:159], v101 offset:8704\n\t"
        "ds_read_b64_tr_b16 a[160:161], v102 offset:0\n\t"
        "ds_read_b64_tr_b16 a[162:163], v102 offset:512\n\t"
        "ds_read_b64_tr_b16 a[164:165], v102 offset:8192\n\t"
        "ds_read_b64_tr_b16 a[166:167], v102 offset:8704\n\t"
        "ds_read_b64_tr_b16 a[168:169], v103 offset:0\n\t"
        "ds_read_b64_tr_b16 a[170:171], v103 offset:512\n\t"
        "ds_read_b64_tr_b16 a[172:173], v103 offset:8192\n\t"
        "ds_read_b64_tr_b16 a[174:175], v103 offset:8704\n\t"
        "ds_read_b64_tr_b16 a[176:177], v104 offset:0\n\t"
        "ds_read_b64_tr_b16 a[178:179], v104 offset:512\n\t"
        "ds_read_b64_tr_b16 a[180:181], v104 offset:8192\n\t"
        "ds_read_b64_tr_b16 a[182:183], v104 offset:8704\n\t"
        "ds_read_b64_tr_b16 a[184:185], v105 offset:0\n\t"
        "ds_read_b64_tr_b16 a[186:187], v105 offset:512\n\t"
        "ds_read_b64_tr_b16 a[188:189], v105 offset:8192\n\t"
        "ds_read_b64_tr_b16 a[190:191], v105 offset:8704\n\t"
        "s_waitcnt lgkmcnt(0)\n\t"
        "s_barrier\n\t"
        "s_add_u32 s80, %[m0b], 0\n\t"
        "s_mov_b32 s81, 4194304\n\t"
        "s_mov_b32 s82, 4\n\t"
        ".Ld4%=:\n\t"
        "s_mov_b32 m0, s80\n\t"
        "s_add_u32 s80, s80, 0x8000\n\t"
        "buffer_load_dwordx4 v96, %[sq], s81 offen lds\n\t"
        "s_add_u32 m0, m0, 0x1000\n\t"
        "s_add_u32 s81, s81, 0x20000\n\t"
        "buffer_load_dwordx4 v96, %[sq], s81 offen lds\n\t"
        "s_add_u32 m0, m0, 0x1000\n\t"
        "s_add_u32 s81, s81, 0x20000\n\t"
        "buffer_load_dwordx4 v96, %[sq], s81 offen lds\n\t"
        "s_add_u32 m0, m0, 0x1000\n\t"
        "s_add_u32 s81, s81, 0x20000\n\t"
        "buffer_load_dwordx4 v96, %[sq], s81 offen lds\n\t"
        "s_add_u32 m0, m0, 0x1000\n\t"
        "s_add_u32 s81, s81, 0x20000\n\t"
        "s_sub_u32 s82, s82, 1\n\t"
        "s_cmp_lg_u32 s82, 0\n\t"
        "s_cbranch_scc1 .Ld4%=\n\t"
        "s_mov_b64 exec, 0\n\t"
        "s_branch .Lhk0%=\n\t"
        ".Lhr%=:\n\t"
        "s_mov_b64 exec, -1\n\t"
        "s_waitcnt vmcnt(16)\n\t"
        "s_barrier\n\t"
        "ds_read_b64_tr_b16 v[48:49], v98 offset:16384\n\t"
        "ds_read_b64_tr_b16 v[50:51], v98 offset:16896\n\t"
        "ds_read_b64_tr_b16 v[52:53], v98 offset:24576\n\t"
        "ds_read_b64_tr_b16 v[54:55], v98 offset:25088\n\t"
        "ds_read_b64_tr_b16 v[56:57], v99 offset:16384\n\t"
        "ds_read_b64_tr_b16 v[58:59], v99 offset:16896\n\t"
        "ds_read_b64_tr_b16 v[60:61], v99 offset:24576\n\t"
        "ds_read_b64_tr_b16 v[62:63], v99 offset:25088\n\t"
        "ds_read_b64_tr_b16 a[208:209], v100 offset:16384\n\t"
        "ds_read_b64_tr_b16 a[210:211], v100 offset:16896\n\t"
        "ds_read_b64_tr_b16 a[212:213], v100 offset:24576\n\t"
        "ds_read_b64_tr_b16 a[214:215], v100 offset:25088\n\t"
        "ds_read_b64_tr_b16 a[216:217], v101 offset:16384\n\t"
        "ds_read_b64_tr_b16 a[218:219], v101 offset:16896\n\t"
        "ds_read_b64_tr_b16 a[220:221], v101 offset:24576\n\t"
        "ds_read_b64_tr_b16 a[222:223], v101 offset:25088\n\t"
        "ds_read_b64_tr_b16 a[224:225], v102 offset:16384\n\t"
        "ds_read_b64_tr_b16 a[226:227], v102 offset:16896\n\t"
        "ds_read_b64_tr_b16 a[228:229], v102 offset:24576\n\t"
        "ds_read_b64_tr_b16 a[230:231], v102 offset:25088\n\t"
        "ds_read_b64_tr_b16 a[232:233], v103 offset:16384\n\t"
        "ds_read_b64_tr_b16 a[234:235], v103 offset:16896\n\t"
        "ds_read_b64_tr_b16 a[236:237], v103 offset:24576\n\t"
        "ds_read_b64_tr_b16 a[238:239], v103 offset:25088\n\t"
        "ds_read_b64_tr_b16 a[240:241], v104 offset:16384\n\t"
        "ds_read_b64_tr_b16 a[242:243], v104 offset:16896\n\t"
        "ds_read_b64_tr_b16 a[244:245], v104 offset:24576\n\t"
        "ds_read_b64_tr_b16 a[246:247], v104 offset:25088\n\t"
        "ds_read_b64_tr_b16 a[248:249], v105 offset:16384\n\t"
        "ds_read_b64_tr_b16 a[250:251], v105 offset:16896\n\t"
        "ds_read_b64_tr_b16 a[252:253], v105 offset:24576\n\t"
        "ds_read_b64_tr_b16 a[254:255], v105 offset:25088\n\t"
        "s_waitcnt lgkmcnt(0)\n\t"
        "s_barrier\n\t"
        "s_add_u32 s80, %[m0b], 16384\n\t"
        "s_mov_b32 s81, 4194304\n\t"
        "s_mov_b32 s82, 4\n\t"
        ".Ld5%=:\n\t"
        "s_mov_b32 m0, s80\n\t"
        "s_add_u32 s80, s80, 0x8000\n\t"
        "buffer_load_dwordx4 v96, %[sk], s81 offen lds\n\t"
        "s_add_u32 m0, m0, 0x1000\n\t"
        "s_add_u32 s81, s81, 0x20000\n\t"
        "buffer_load_dwordx4 v96, %[sk], s81 offen lds\n\t"
        "s_add_u32 m0, m0, 0x1000\n\t"
        "s_add_u32 s81, s81, 0x20000\n\t"
        "buffer_load_dwordx4 v96, %[sk], s81 offen lds\n\t"
        "s_add_u32 m0, m0, 0x1000\n\t"
        "s_add_u32 s81, s81, 0x20000\n\t"
        "buffer_load_dwordx4 v96, %[sk], s81 offen lds\n\t"
        "s_add_u32 m0, m0, 0x1000\n\t"
        "s_add_u32 s81, s81, 0x20000\n\t"
        "s_sub_u32 s82, s82, 1\n\t"
        "s_cmp_lg_u32 s82, 0\n\t"
        "s_cbranch_scc1 .Ld5%=\n\t"
        "s_waitcnt vmcnt(16)\n\t"
        "s_barrier\n\t"
        "ds_read_b64_tr_b16 v[128:129], v98 offset:0\n\t"
        "ds_read_b64_tr_b16 v[130:131], v98 offset:512\n\t"
        "ds_read_b64_tr_b16 v[132:133], v98 offset:8192\n\t"
        "ds_read_b64_tr_b16 v[134:135], v98 offset:8704\n\t"
        "ds_read_b64_tr_b16 v[136:137], v99 offset:0\n\t"
        "ds_read_b64_tr_b16 v[138:139], v99 offset:512\n\t"
        "ds_read_b64_tr_b16 v[140:141], v99 offset:8192\n\t"
        "ds_read_b64_tr_b16 v[142:143], v99 offset:8704\n\t"
        "ds_read_b64_tr_b16 v[144:145], v100 offset:0\n\t"
        "ds_read_b64_tr_b16 v[146:147], v100 offset:512\n\t"
        "ds_read_b64_tr_b16 v[148:149], v100 offset:8192\n\t"
        "ds_read_b64_tr_b16 v[150:151], v100 offset:8704\n\t"
        "ds_read_b64_tr_b16 v[152:153], v101 offset:0\n\t"
        "ds_read_b64_tr_b16 v[154:155], v101 offset:512\n\t"
        "ds_read_b64_tr_b16 v[156:157], v101 offset:8192\n\t"
        "ds_read_b64_tr_b16 v[158:159], v101 offset:8704\n\t"
        "ds_read_b64_tr_b16 v[160:161], v102 offset:0\n\t"
        "ds_read_b64_tr_b16 v[162:163], v102 offset:512\n\t"
        "ds_read_b64_tr_b16 v[164:165], v102 offset:8192\n\t"
        "ds_read_b64_tr_b16 v[166:167], v102 offset:8704\n\t"
        "ds_read_b64_tr_b16 v[168:169], v103 offset:0\n\t"
        "ds_read_b64_tr_b16 v[170:171], v103 offset:512\n\t"
        "ds_read_b64_tr_b16 v[172:173], v103 offset:8192\n\t"
        "ds_read_b64_tr_b16 v[174:175], v103 offset:8704\n\t"
        "ds_read_b64_tr_b16 v[176:177], v104 offset:0\n\t"
        "ds_read_b64_tr_b16 v[178:179], v104 offset:512\n\t"
        "ds_read_b64_tr_b16 v[180:181], v104 offset:8192\n\t"
        "ds_read_b64_tr_b16 v[182:183], v104 offset:8704\n\t"
        "ds_read_b64_tr_b16 v[184:185], v105 offset:0\n\t"
        "ds_read_b64_tr_b16 v[186:187], v105 offset:512\n\t"
        "ds_read_b64_tr_b16 v[188:189], v105 offset:8192\n\t"
        "ds_read_b64_tr_b16 v[190:191], v105 offset:8704\n\t"
        "s_waitcnt lgkmcnt(0)\n\t"
        "s_barrier\n\t"
        "s_add_u32 s80, %[m0b], 0\n\t"
        "s_mov_b32 s81, 6291456\n\t"
        "s_mov_b32 s82, 4\n\t"
        ".Ld6%=:\n\t"
        "s_mov_b32 m0, s80\n\t"
        "s_add_u32 s80, s80, 0x8000\n\t"
        "buffer_load_dwordx4 v96, %[sq], s81 offen lds\n\t"
        "s_add_u32 m0, m0, 0x1000\n\t"
        "s_add_u32 s81, s81, 0x20000\n\t"
        "buffer_load_dwordx4 v96, %[sq], s81 offen lds\n\t"
        "s_add_u32 m0, m0, 0x1000\n\t"
        "s_add_u32 s81, s81, 0x20000\n\t"
        "buffer_load_dwordx4 v96, %[sq], s81 offen lds\n\t"
        "s_add_u32 m0, m0, 0x1000\n\t"
        "s_add_u32 s81, s81, 0x20000\n\t"
        "buffer_load_dwordx4 v96, %[sq], s81 offen lds\n\t"
        "s_add_u32 m0, m0, 0x1000\n\t"
        "s_add_u32 s81, s81, 0x20000\n\t"
        "s_sub_u32 s82, s82, 1\n\t"
        "s_cmp_lg_u32 s82, 0\n\t"
        "s_cbranch_scc1 .Ld6%=\n\t"
        "s_waitcnt vmcnt(16)\n\t"
        "s_barrier\n\t"
        "ds_read_b64_tr_b16 v[192:193], v98 offset:16384\n\t"
        "ds_read_b64_tr_b16 v[194:195], v98 offset:16896\n\t"
        "ds_read_b64_tr_b16 v[196:197], v98 offset:24576\n\t"
        "ds_read_b64_tr_b16 v[198:199], v98 offset:25088\n\t"
        "ds_read_b64_tr_b16 v[200:201], v99 offset:16384\n\t"
        "ds_read_b64_tr_b16 v[202:203], v99 offset:16896\n\t"
        "ds_read_b64_tr_b16 v[204:205], v99 offset:24576\n\t"
        "ds_read_b64_tr_b16 v[206:207], v99 offset:25088\n\t"
        "ds_read_b64_tr_b16 v[208:209], v100 offset:16384\n\t"
        "ds_read_b64_tr_b16 v[210:211], v100 offset:16896\n\t"
        "ds_read_b64_tr_b16 v[212:213], v100 offset:24576\n\t"
        "ds_read_b64_tr_b16 v[214:215], v100 offset:25088\n\t"
        "ds_read_b64_tr_b16 v[216:217], v101 offset:16384\n\t"
        "ds_read_b64_tr_b16 v[218:219], v101 offset:16896\n\t"
        "ds_read_b64_tr_b16 v[220:221], v101 offset:24576\n\t"
        "ds_read_b64_tr_b16 v[222:223], v101 offset:25088\n\t"
        "ds_read_b64_tr_b16 v[224:225], v102 offset:16384\n\t"
        "ds_read_b64_tr_b16 v[226:227], v102 offset:16896\n\t"
        "ds_read_b64_tr_b16 v[228:229], v102 offset:24576\n\t"
        "ds_read_b64_tr_b16 v[230:231], v102 offset:25088\n\t"
        "ds_read_b64_tr_b16 v[232:233], v103 offset:16384\n\t"
        "ds_read_b64_tr_b16 v[234:235], v103 offset:16896\n\t"
        "ds_read_b64_tr_b16 v[236:237], v103 offset:24576\n\t"
        "ds_read_b64_tr_b16 v[238:239], v103 offset:25088\n\t"
        "ds_read_b64_tr_b16 v[240:241], v104 offset:16384\n\t"
        "ds_read_b64_tr_b16 v[242:243], v104 offset:16896\n\t"
        "ds_read_b64_tr_b16 v[244:245], v104 offset:24576\n\t"
        "ds_read_b64_tr_b16 v[246:247], v104 offset:25088\n\t"
        "ds_read_b64_tr_b16 v[248:249], v105 offset:16384\n\t"
        "ds_read_b64_tr_b16 v[250:251], v105 offset:16896\n\t"
        "ds_read_b64_tr_b16 v[252:253], v105 offset:24576\n\t"
        "ds_read_b64_tr_b16 v[254:255], v105 offset:25088\n\t"
        "s_waitcnt lgkmcnt(0)\n\t"
        "s_barrier\n\t"
        "s_add_u32 s80, %[m0b], 16384\n\t"
        "s_mov_b32 s81, 6291456\n\t"
        "s_mov_b32 s82, 4\n\t"
        ".Ld7%=:\n\t"
        "s_mov_b32 m0, s80\n\t"
        "s_add_u32 s80, s80, 0x8000\n\t"
        "buffer_load_dwordx4 v96, %[sk], s81 offen lds\n\t"
        "s_add_u32 m0, m0, 0x1000\n\t"
        "s_add_u32 s81, s81, 0x20000\n\t"
        "buffer_load_dwordx4 v96, %[sk], s81 offen lds\n\t"
        "s_add_u32 m0, m0, 0x1000\n\t"
        "s_add_u32 s81, s81, 0x20000\n\t"
        "buffer_load_dwordx4 v96, %[sk], s81 offen lds\n\t"
        "s_add_u32 m0, m0, 0x1000\n\t"
        "s_add_u32 s81, s81, 0x20000\n\t"
        "buffer_load_dwordx4 v96, %[sk], s81 offen lds\n\t"
        "s_add_u32 m0, m0, 0x1000\n\t"
        "s_add_u32 s81, s81, 0x20000\n\t"
        "s_sub_u32 s82, s82, 1\n\t"
        "s_cmp_lg_u32 s82, 0\n\t"
        "s_cbranch_scc1 .Ld7%=\n\t"
        "s_waitcnt vmcnt(16)\n\t"
        "s_barrier\n\t"
        "ds_read_b64_tr_b16 a[0:1], v98 offset:0\n\t"
        "ds_read_b64_tr_b16 a[2:3], v98 offset:512\n\t"
        "ds_read_b64_tr_b16 a[4:5], v98 offset:8192\n\t"
        "ds_read_b64_tr_b16 a[6:7], v98 offset:8704\n\t"
        "ds_read_b64_tr_b16 a[8:9], v99 offset:0\n\t"
        "ds_read_b64_tr_b16 a[10:11], v99 offset:512\n\t"
        "ds_read_b64_tr_b16 a[12:13], v99 offset:8192\n\t"
        "ds_read_b64_tr_b16 a[14:15], v99 offset:8704\n\t"
        "ds_read_b64_tr_b16 v[64:65], v100 offset:0\n\t"
        "ds_read_b64_tr_b16 v[66:67], v100 offset:512\n\t"
        "ds_read_b64_tr_b16 v[68:69], v100 offset:8192\n\t"
        "ds_read_b64_tr_b16 v[70:71], v100 offset:8704\n\t"
        "ds_read_b64_tr_b16 v[72:73], v101 offset:0\n\t"
        "ds_read_b64_tr_b16 v[74:75], v101 offset:512\n\t"
        "ds_read_b64_tr_b16 v[76:77], v101 offset:8192\n\t"
        "ds_read_b64_tr_b16 v[78:79], v101 offset:8704\n\t"
        "ds_read_b64_tr_b16 v[80:81], v102 offset:0\n\t"
        "ds_read_b64_tr_b16 v[82:83], v102 offset:512\n\t"
        "ds_read_b64_tr_b16 v[84:85], v102 offset:8192\n\t"
        "ds_read_b64_tr_b16 v[86:87], v102 offset:8704\n\t"
        "ds_read_b64_tr_b16 v[88:89], v103 offset:0\n\t"
        "ds_read_b64_tr_b16 v[90:91], v103 offset:512\n\t"
        "ds_read_b64_tr_b16 v[92:93], v103 offset:8192\n\t"
        "ds_read_b64_tr_b16 v[94:95], v103 offset:8704\n\t"
        "ds_read_b64_tr_b16 a[128:129], v104 offset:0\n\t"
        "ds_read_b64_tr_b16 a[130:131], v104 offset:512\n\t"
        "ds_read_b64_tr_b16 a[132:133], v104 offset:8192\n\t"
        "ds_read_b64_tr_b16 a[134:135], v104 offset:8704\n\t"
        "ds_read_b64_tr_b16 a[136:137], v105 offset:0\n\t"
        "ds_read_b64_tr_b16 a[138:139], v105 offset:512\n\t"
        "ds_read_b64_tr_b16 a[140:141], v105 offset:8192\n\t"
        "ds_read_b64_tr_b16 a[142:143], v105 offset:8704\n\t"
        "s_waitcnt lgkmcnt(0)\n\t"
        "s_barrier\n\t"
        "ds_write_b128 v121, v[0:3] offset:0\n\t"
        "ds_write_b128 v121, v[4:7] offset:1024\n\t"
        "ds_write_b128 v121, v[8:11] offset:2048\n\t"
        "ds_write_b128 v121, v[12:15] offset:3072\n\t"
        "ds_write_b128 v121, v[16:19] offset:4096\n\t"
        "ds_write_b128 v121, v[20:23] offset:5120\n\t"
        "ds_write_b128 v121, v[24:27] offset:6144\n\t"
        "ds_write_b128 v121, v[28:31] offset:7168\n\t"
        "ds_write_b128 v121, v[32:35] offset:8192\n\t"
        "ds_write_b128 v121, v[36:39] offset:9216\n\t"
        "ds_write_b128 v121, v[40:43] offset:10240\n\t"
        "ds_write_b128 v121, v[44:47] offset:11264\n\t"
        "ds_write_b128 v121, v[48:51] offset:12288\n\t"
        "ds_write_b128 v121, v[52:55] offset:13312\n\t"
        "ds_write_b128 v121, v[56:59] offset:14336\n\t"
        "ds_write_b128 v121, v[60:63] offset:15360\n\t"
        "s_waitcnt lgkmcnt(0)\n\t"
        "s_mov_b32 s64, %[kf]\n\t"
        "s_mov_b32 s36, 0xf0000000\n\t"
        "buffer_load_dwordx4 v[116:119], v120, %[wsd], %[cal] offen sc1\n\t"
        "buffer_load_dwordx2 v[114:115], off, %[wsd], s64 sc1\n\t"
        "s_waitcnt vmcnt(2)\n\t"
        "s_barrier\n\t"
        "ds_read_b64_tr_b16 a[64:65], v98 offset:16384\n\t"
        "ds_read_b64_tr_b16 a[66:67], v98 offset:16896\n\t"
        "ds_read_b64_tr_b16 a[68:69], v98 offset:24576\n\t"
        "ds_read_b64_tr_b16 a[70:71], v98 offset:25088\n\t"
        "ds_read_b64_tr_b16 a[72:73], v99 offset:16384\n\t"
        "ds_read_b64_tr_b16 a[74:75], v99 offset:16896\n\t"
        "ds_read_b64_tr_b16 a[76:77], v99 offset:24576\n\t"
        "ds_read_b64_tr_b16 a[78:79], v99 offset:25088\n\t"
        "ds_read_b64_tr_b16 v[0:1], v100 offset:16384\n\t"
        "ds_read_b64_tr_b16 v[2:3], v100 offset:16896\n\t"
        "ds_read_b64_tr_b16 v[4:5], v100 offset:24576\n\t"
        "ds_read_b64_tr_b16 v[6:7], v100 offset:25088\n\t"
        "ds_read_b64_tr_b16 v[8:9], v101 offset:16384\n\t"
        "ds_read_b64_tr_b16 v[10:11], v101 offset:16896\n\t"
        "ds_read_b64_tr_b16 v[12:13], v101 offset:24576\n\t"
        "ds_read_b64_tr_b16 v[14:15], v101 offset:25088\n\t"
        "ds_read_b64_tr_b16 v[16:17], v102 offset:16384\n\t"
        "ds_read_b64_tr_b16 v[18:19], v102 offset:16896\n\t"
        "ds_read_b64_tr_b16 v[20:21], v102 offset:24576\n\t"
        "ds_read_b64_tr_b16 v[22:23], v102 offset:25088\n\t"
        "ds_read_b64_tr_b16 v[24:25], v103 offset:16384\n\t"
        "ds_read_b64_tr_b16 v[26:27], v103 offset:16896\n\t"
        "ds_read_b64_tr_b16 v[28:29], v103 offset:24576\n\t"
        "ds_read_b64_tr_b16 v[30:31], v103 offset:25088\n\t"
        "ds_read_b64_tr_b16 a[192:193], v104 offset:16384\n\t"
        "ds_read_b64_tr_b16 a[194:195], v104 offset:16896\n\t"
        "ds_read_b64_tr_b16 a[196:197], v104 offset:24576\n\t"
        "ds_read_b64_tr_b16 a[198:199], v104 offset:25088\n\t"
        "ds_read_b64_tr_b16 a[200:201], v105 offset:16384\n\t"
        "ds_read_b64_tr_b16 a[202:203], v105 offset:16896\n\t"
        "ds_read_b64_tr_b16 a[204:205], v105 offset:24576\n\t"
        "ds_read_b64_tr_b16 a[206:207], v105 offset:25088\n\t"
        "s_waitcnt vmcnt(0)\n\t"
        "s_mov_b32 s97, 0\n\t"
        ".Lg0%=:\n\t"
        "v_cmp_ne_u32_e32 vcc, %[tag], v116\n\t"
        "v_cmp_ne_u32_e64 s[82:83], %[tag], v118\n\t"
        "s_nop 1\n\t"
        "s_or_b64 vcc, vcc, s[82:83]\n\t"
        "s_cbranch_scc0 .Lg1%=\n\t"
        "s_sleep 4\n\t"
        "buffer_load_dwordx4 v[116:119], v120, %[wsd], %[cal] offen sc1\n\t"
        "s_add_u32 s97, s97, 1\n\t"
        "s_cmp_lt_u32 s97, 0x100000\n\t"
        "s_cbranch_scc0 .Lgu%=\n\t"
        "s_waitcnt vmcnt(0)\n\t"
        "s_branch .Lg0%=\n\t"
        ".Lg1%=:\n\t"
        "s_mov_b32 s84, 0x55555555\n\t"
        "s_mov_b32 s85, s84\n\t"
        "v_and_b32_e32 v116, 1, v117\n\t"
        "v_and_b32_e32 v118, 1, v119\n\t"
        "v_cmp_ne_u32_e32 vcc, 0, v116\n\t"
        "v_cmp_ne_u32_e64 s[82:83], 0, v118\n\t"
        "s_nop 0\n\t"
        "s_and_b64 s[82:83], s[82:83], s[84:85]\n\t"
        "s_bcnt1_i32_b64 s87, s[82:83]\n\t"
        "s_and_b64 s[82:83], vcc, s[84:85]\n\t"
        "s_bcnt1_i32_b64 s86, s[82:83]\n\t"
        "s_andn2_b64 s[82:83], vcc, s[84:85]\n\t"
        "s_bcnt1_i32_b64 s88, s[82:83]\n\t"
        "v_and_b32_e32 v116, 2, v117\n\t"
        "v_and_b32_e32 v118, 2, v119\n\t"
        "v_cmp_ne_u32_e32 vcc, 0, v116\n\t"
        "v_cmp_ne_u32_e64 s[82:83], 0, v118\n\t"
        "s_nop 0\n\t"
        "s_and_b64 s[82:83], s[82:83], s[84:85]\n\t"
        "s_bcnt1_i32_b64 s90, s[82:83]\n\t"
        "s_and_b64 s[82:83], vcc, s[84:85]\n\t"
        "s_bcnt1_i32_b64 s89, s[82:83]\n\t"
        "s_andn2_b64 s[82:83], vcc, s[84:85]\n\t"
        "s_bcnt1_i32_b64 s91, s[82:83]\n\t"
        "s_add_u32 s80, s86, s87\n\t"
        "s_add_u32 s80, s80, s88\n\t"
        "s_cmp_ge_u32 s80, 48\n\t"
        "s_cselect_b32 s80, 8, 24\n\t"
        "s_cmp_gt_u32 s86, s80\n\t"
        "s_cselect_b32 s86, %c[NX1], %c[NX0]\n\t"
        "s_cmp_gt_u32 s89, 16\n\t"
        "s_cselect_b32 s81, %c[NY1], %c[NY0]\n\t"
        "s_or_b32 s86, s86, s81\n\t"
        "s_cmp_gt_u32 s87, s80\n\t"
        "s_cselect_b32 s87, %c[NX1], %c[NX0]\n\t"
        "s_cmp_gt_u32 s90, 16\n\t"
        "s_cselect_b32 s81, %c[NY1], %c[NY0]\n\t"
        "s_or_b32 s87, s87, s81\n\t"
        "s_cmp_gt_u32 s88, s80\n\t"
        "s_cselect_b32 s88, %c[NX1], %c[NX0]\n\t"
        "s_cmp_gt_u32 s91, 16\n\t"
        "s_cselect_b32 s81, %c[NY1], %c[NY0]\n\t"
        "s_or_b32 s88, s88, s81\n\t"
        "s_xor_b32 %[nA], %[nA], s86\n\t"
        "s_xor_b32 %[nW], %[nW], s86\n\t"
        "s_xor_b32 %[nC], %[nC], s87\n\t"
        "s_xor_b32 %[nS], %[nS], s88\n\t"
        "s_xor_b32 %[nT], %[nT], s88\n\t"
        "s_xor_b32 %[b0], %[b0], s88\n\t"
        "s_cmp_eq_u32 %[w], 1\n\t"
        "s_cselect_b32 s80, s87, s86\n\t"
        "s_cmp_eq_u32 %[w], 2\n\t"
        "s_cselect_b32 s80, s88, s80\n\t"
        "s_xor_b32 %[pob], %[pob], s80\n\t"
        "s_lshl_b32 s80, %[w], 10\n\t"
        "s_sub_u32 %[nS], %[nS], s80\n\t"
        "s_sub_u32 %[pob], %[pob], s80\n\t"
        "ds_read_b32 v116, v120 offset:20484\n\t"
        "s_and_b32 s80, s87, 0x4000\n\t"
        "s_xor_b32 %[nC], %[nC], s80\n\t"
        "s_lshl_b32 s81, %[w], 10\n\t"
        "s_sub_u32 %[b0], %[b0], s81\n\t"
        "s_waitcnt lgkmcnt(0)\n\t"
        "v_xor_b32_e32 v116, s80, v116\n\t"
        "ds_write_b32 v120, v116 offset:20484\n\t"
        "s_mov_b32 s36, 0xf0000000\n\t"
        "s_mov_b32 s38, 0x41a00000\n\t"
        "s_mov_b32 s40, 0\n\t"
        "s_mov_b32 s37, 0\n\t"
        "s_brev_b32 s54, 1\n\t"
        "s_mov_b32 s46, %c[iD1]\n\t"
        "s_mov_b32 s45, %c[iD1]\n\t"
        "s_mov_b32 s47, %c[iD2]\n\t"
        "s_mov_b32 s48, 1\n\t"
        "s_mov_b32 s50, %c[LC]\n\t"
        "s_mov_b32 s51, %c[LA]\n\t"
        "s_mov_b32 s52, %c[E1]\n\t"
        "s_mov_b32 s53, %c[E2]\n\t"
        "s_mov_b32 s43, 0\n\t"
        "s_mov_b32 s61, %[b0]\n\t"
        "s_add_u32 s62, %[b0], 0x40000\n\t"
        "s_mov_b32 s75, 31744\n\t"
        "s_cmp_eq_u32 s64, s36\n\t"
        "s_cbranch_scc1 .Lk10%=\n\t"
        "s_mov_b32 s97, 0\n\t"
        ".Lk00%=:\n\t"
        "v_readfirstlane_b32 s82, v114\n\t"
        "v_readfirstlane_b32 s83, v115\n\t"
        "s_xor_b32 s82, s82, %[tag]\n\t"
        "s_xor_b32 s83, s83, %[tag]\n\t"
        "s_or_b32 s82, s82, s83\n\t"
        "s_cbranch_scc0 .Lk10%=\n\t"
        "s_sleep 3\n\t"
        "buffer_load_dwordx2 v[114:115], off, %[wsd], s64 sc1\n\t"
        "s_add_u32 s97, s97, 1\n\t"
        "s_cmp_lt_u32 s97, 0x200000\n\t"
        "s_cbranch_scc0 .Lgu%=\n\t"
        "s_waitcnt vmcnt(0)\n\t"
        "s_branch .Lk00%=\n\t"
        ".Lk10%=:\n\t"
        "s_add_u32 s80, s40, 0\n\t"
        "s_min_u32 s80, s80, 63\n\t"
        "s_lshl_b32 s80, s80, 18\n\t"
        "s_add_u32 s69, s80, %[kx]\n\t"
        "s_add_u32 s70, s69, 0x1000\n\t"
        "s_lshl_b32 s80, s40, 18\n\t"
        "s_add_u32 s71, s80, %[vx]\n\t"
        "s_add_u32 s80, s40, 1\n\t"
        "s_lshl_b32 s81, s80, 9\n\t"
        "s_add_u32 s81, s81, %[kf]\n\t"
        "s_and_b32 s80, s80, 0x43\n\t"
        "s_cselect_b32 s64, s36, s81\n\t"
        "buffer_load_dwordx4 v[104:107], v120, %[wsd], s69 offen sc1\n\t"
        "buffer_load_dwordx4 v[108:111], v120, %[wsd], s70 offen sc1\n\t"
        "s_waitcnt lgkmcnt(0)\n\t"
        "s_barrier\n\t"
        "ds_write_b128 v121, v[64:67] offset:16384\n\t"
        "ds_write_b128 v121, v[68:71] offset:17408\n\t"
        "ds_write_b128 v121, v[72:75] offset:18432\n\t"
        "ds_write_b128 v121, v[76:79] offset:19456\n\t"
        "ds_write_b128 v121, v[80:83] offset:20480\n\t"
        "ds_write_b128 v121, v[84:87] offset:21504\n\t"
        "ds_write_b128 v121, v[88:91] offset:22528\n\t"
        "ds_write_b128 v121, v[92:95] offset:23552\n\t"
        "ds_write_b128 v121, v[0:3] offset:24576\n\t"
        "ds_write_b128 v121, v[4:7] offset:25600\n\t"
        "ds_write_b128 v121, v[8:11] offset:26624\n\t"
        "ds_write_b128 v121, v[12:15] offset:27648\n\t"
        "ds_write_b128 v121, v[16:19] offset:28672\n\t"
        "ds_write_b128 v121, v[20:23] offset:29696\n\t"
        "ds_write_b128 v121, v[24:27] offset:30720\n\t"
        "ds_write_b128 v121, v[28:31] offset:31744\n\t"
        "s_waitcnt lgkmcnt(0)\n\t"
        "rec_s_code:\n\t"
        "ds_read_b128 v[16:19], v121 offset:0\n\t"
        "ds_read_b128 v[20:23], v121 offset:1024\n\t"
        "ds_read_b128 v[24:27], v121 offset:2048\n\t"
        "ds_read_b128 v[28:31], v121 offset:3072\n\t"
        "ds_read_b128 v[32:35], v121 offset:4096\n\t"
        "ds_read_b128 v[36:39], v121 offset:5120\n\t"
        "ds_read_b128 v[40:43], v121 offset:6144\n\t"
        "ds_read_b128 v[44:47], v121 offset:7168\n\t"
        "ds_read_b128 v[56:59], v121 offset:8192\n\t"
        "ds_read_b128 v[60:63], v121 offset:9216\n\t"
        "ds_read_b128 v[64:67], v121 offset:10240\n\t"
        "ds_read_b128 v[68:71], v121 offset:11264\n\t"
        "ds_read_b128 v[72:75], v121 offset:12288\n\t"
        "ds_read_b128 v[76:79], v121 offset:13312\n\t"
        "ds_read_b128 v[80:83], v121 offset:14336\n\t"
        "ds_read_b128 v[84:87], v121 offset:15360\n\t"
        "s_and_b32 s80, s40, 3\n\t"
        "s_lshl_b32 s80, s80, 17\n\t"
        "s_add_u32 s57, s80, %[nA]\n\t"
        "s_add_u32 s58, s57, 0x1000\n\t"
        "s_add_u32 s59, s57, 0x8000\n\t"
        "s_add_u32 s60, s57, 0x9000\n\t"
        "s_and_b32 s80, s40, 3\n\t"
        "s_lshl_b32 s80, s80, 17\n\t"
        "s_add_u32 s63, s80, %[nC]\n\t"
        "s_add_u32 s66, s80, %[nS]\n\t"
        "s_add_u32 s81, s80, 0x20000\n\t"
        "s_and_b32 s81, s81, 0x60000\n\t"
        "s_add_u32 s67, s81, %[nW]\n\t"
        "s_add_u32 s79, s81, %[nT]\n\t"
        "s_cmp_eq_u32 %[w], 2\n\t"
        "s_cselect_b32 s81, s80, s81\n\t"
        "s_xor_b32 s81, s81, 0x40000\n\t"
        "s_add_u32 s68, s81, %[pob]\n\t"
        "s_lshl_b32 s72, s40, 17\n\t"
        "s_add_u32 s72, s72, %[kvo]\n\t"
        "s_mov_b32 s88, 0\n\t"
        "s_mov_b32 s89, 0\n\t"
        "s_mov_b32 s90, 0\n\t"
        "v_mov_b32_e32 v88, 0\n\t"
        "v_mov_b32_e32 v89, 0\n\t"
        "v_mov_b32_e32 v90, 0\n\t"
        "v_mov_b32_e32 v91, 0\n\t"
        "v_mov_b32_e32 v92, 0\n\t"
        "v_mov_b32_e32 v93, 0\n\t"
        "v_mov_b32_e32 v94, 0\n\t"
        "v_mov_b32_e32 v95, 0\n\t"
        "v_mov_b32_e32 v96, 0\n\t"
        "v_mov_b32_e32 v97, 0\n\t"
        "v_mov_b32_e32 v98, 0\n\t"
        "v_mov_b32_e32 v99, 0\n\t"
        "v_mov_b32_e32 v100, 0\n\t"
        "v_mov_b32_e32 v101, 0\n\t"
        "v_mov_b32_e32 v102, 0\n\t"
        "v_mov_b32_e32 v103, 0\n\t"
        "v_mov_b32_e32 v52, v112\n\t"
        "v_mov_b32_e32 v53, v113\n\t"
        "v_mov_b32_e32 v49, 0\n\t"
        "v_lshlrev_b32_e32 v51, 6, v120\n\t"
        "s_cmp_eq_u32 %[w], 0\n\t"
        "s_cselect_b32 s73, %[nT], s36\n\t"
        "s_cselect_b32 s74, %[pra], s36\n\t"
        "v_mov_b32_e32 v54, s40\n\t"
        "s_cmp_ge_u32 s40, 60\n\t"
        "s_cselect_b32 s65, s74, s36\n\t"
        "s_waitcnt lgkmcnt(0)\n\t"
        "s_barrier\n\t"
        "s_memrealtime s[98:99]\n\t"
        "s_waitcnt lgkmcnt(0)\n\t"
        "s_mov_b32 s41, s98\n\t"
        "v_mov_b32_e32 v48, s98\n\t"
        ".p2align 6\n\t"
        ".Lloop%=:\n\t"
        "s_mov_b32 s97, 0\n\t"
        "s_movk_i32 s96, 0x1000\n\t"
        ".Lwa%=:\n\t"
        "s_memrealtime s[98:99]\n\t"
        "s_addk_i32 s96, -1\n\t"
        "s_cmp_lt_i32 s96, 0\n\t"
        "s_cbranch_scc1 .Lta%=\n\t"
        "s_waitcnt lgkmcnt(0)\n\t"
        "s_sub_i32 s98, s98, s41\n\t"
        "s_cmp_lt_i32 s98, %c[iFT]\n\t"
        "s_cbranch_scc1 .Lwa%=\n\t"
        "s_cmp_ge_i32 s98, 0\n\t"
        "s_cbranch_scc1 .Lxa%=\n\t"
        ".Lqa%=:\n\t"
        "s_nop %c[iFN]\n\t"
        "s_addk_i32 s98, 1\n\t"
        "s_cmp_lt_i32 s98, 0\n\t"
        "s_cbranch_scc1 .Lqa%=\n\t"
        ".Lxa%=:\n\t"
        "buffer_load_dwordx4 v[0:3], v120, %[srd], s57 offen sc1\n\t"
        "s_mov_b32 s80, s88\n\t"
        "s_add_i32 s81, s92, s50\n\t"
        "s_sub_i32 s81, s81, %c[iBM]\n\t"
        "s_cmp_le_i32 s81, s44\n\t"
        "s_cselect_b32 s80, s80, 0\n\t"
        "s_cmp_lt_i32 s50, %c[LC24]\n\t"
        "s_cselect_b32 s80, s80, 0\n\t"
        "s_mul_i32 s80, s80, %c[iBS]\n\t"
        "buffer_load_dwordx4 v[4:7], v120, %[srd], s58 offen sc1\n\t"
        "s_add_u32 s50, s80, s50\n\t"
        "s_sub_i32 s50, s50, s89\n\t"
        "s_max_i32 s50, s50, %c[LC]\n\t"
        "s_lshl_b32 s80, s92, 1\n\t"
        "s_sub_i32 s81, s80, s52\n\t"
        "s_ashr_i32 s82, s81, %c[UPSH]\n\t"
        "s_max_i32 s82, s82, %c[TRUP]\n\t"
        "s_add_i32 s82, s82, s52\n\t"
        "buffer_load_dwordx4 v[8:11], v120, %[srd], s59 offen sc1\n\t"
        "s_min_i32 s82, s82, s80\n\t"
        "s_ashr_i32 s83, s81, %c[DNSH]\n\t"
        "s_min_i32 s83, s83, -%c[TRDN]\n\t"
        "s_add_i32 s83, s83, s52\n\t"
        "s_max_i32 s83, s83, s80\n\t"
        "s_cmp_gt_i32 s81, 0\n\t"
        "s_cselect_b32 s82, s82, s83\n\t"
        "s_cmp_lg_u32 s90, 0\n\t"
        "s_cselect_b32 s52, s82, s52\n\t"
        "buffer_load_dwordx4 v[12:15], v120, %[srd], s60 offen sc1\n\t"
        "s_add_i32 s80, s52, 1\n\t"
        "s_ashr_i32 s80, s80, 1\n\t"
        "s_add_i32 s45, s80, s50\n\t"
        "s_add_u32 s80, s40, 1\n\t"
        "s_min_u32 s80, s80, 63\n\t"
        "s_lshl_b32 s80, s80, 18\n\t"
        "s_add_u32 s69, s80, %[kx]\n\t"
        "s_add_u32 s70, s69, 0x1000\n\t"
        "s_lshl_b32 s80, s40, 18\n\t"
        "s_add_u32 s71, s80, %[vx]\n\t"
        "s_add_u32 s80, s40, 2\n\t"
        "s_lshl_b32 s81, s80, 9\n\t"
        "s_add_u32 s81, s81, %[kf]\n\t"
        "s_and_b32 s80, s80, 0x43\n\t"
        "s_cselect_b32 s64, s36, s81\n\t"
        "s_nop 15\n\t"
        "s_nop 7\n\t"
        "s_mov_b64 exec, 7\n\t"
        "buffer_store_dwordx2 v[48:49], v51, %[srd], s73 offen sc1\n\t"
        "s_mov_b64 exec, -1\n\t"
        "buffer_load_dwordx4 v[112:115], v120, %[wsd], s71 offen offset:0 sc1\n\t"
        "buffer_load_dwordx4 v[116:119], v120, %[wsd], s71 offen offset:1024 sc1\n\t"
        ".Lgo%=:\n\t"
        "s_waitcnt vmcnt(6)\n\t"
        "v_smfmac_f32_16x16x64_bf16 v[88:91], v[0:3], v[16:23], v122\n\t"
        "ds_write_b128 v120, v[0:3] offset:0\n\t"
        "v_smfmac_f32_16x16x64_bf16 v[92:95], v[0:3], v[24:31], v122\n\t"
        "v_smfmac_f32_16x16x64_bf16 v[96:99], v[0:3], v[32:39], v122\n\t"
        "v_smfmac_f32_16x16x64_bf16 v[100:103], v[0:3], v[40:47], v122\n\t"
        "s_waitcnt vmcnt(5)\n\t"
        "v_smfmac_f32_16x16x64_bf16 v[88:91], v[4:7], v[56:63], v122\n\t"
        "ds_write_b128 v120, v[4:7] offset:4096\n\t"
        "v_smfmac_f32_16x16x64_bf16 v[92:95], v[4:7], v[64:71], v122\n\t"
        "v_smfmac_f32_16x16x64_bf16 v[96:99], v[4:7], v[72:79], v122\n\t"
        "v_smfmac_f32_16x16x64_bf16 v[100:103], v[4:7], v[80:87], v122\n\t"
        "s_waitcnt vmcnt(4)\n\t"
        "v_smfmac_f32_16x16x64_bf16 v[88:91], v[8:11], v[128:135], v122\n\t"
        "ds_write_b128 v120, v[8:11] offset:8192\n\t"
        "v_smfmac_f32_16x16x64_bf16 v[92:95], v[8:11], v[136:143], v122\n\t"
        "s_waitcnt lgkmcnt(1)\n\t"
        "s_barrier\n\t"
        "v_smfmac_f32_16x16x64_bf16 v[96:99], v[8:11], v[192:199], v122\n\t"
        "ds_read_b128 v[16:19], v124 offset:0\n\t"
        "v_smfmac_f32_16x16x64_bf16 v[100:103], v[8:11], v[200:207], v122\n\t"
        "ds_read_b128 v[20:23], v125 offset:0\n\t"
        "s_waitcnt vmcnt(3)\n\t"
        "v_smfmac_f32_16x16x64_bf16 v[88:91], v[12:15], a[0:7], v122\n\t"
        "ds_write_b128 v120, v[12:15] offset:12288\n\t"
        "v_smfmac_f32_16x16x64_bf16 v[92:95], v[12:15], a[8:15], v122\n\t"
        "buffer_store_dword v54, off, %[wsd], s65 sc1\n\t"
        "v_smfmac_f32_16x16x64_bf16 v[96:99], v[12:15], a[64:71], v122\n\t"
        "ds_read_b128 v[24:27], v126 offset:0\n\t"
        "v_smfmac_f32_16x16x64_bf16 v[100:103], v[12:15], a[72:79], v122\n\t"
        "ds_read_b128 v[28:31], v124 offset:4096\n\t"
        "s_waitcnt lgkmcnt(4)\n\t"
        "v_smfmac_f32_16x16x64_bf16 v[88:91], v[16:19], a[16:23], v122\n\t"
        "ds_read_b128 v[32:35], v125 offset:4096\n\t"
        "v_smfmac_f32_16x16x64_bf16 v[92:95], v[16:19], a[24:31], v122\n\t"
        "ds_read_b128 v[36:39], v126 offset:4096\n\t"
        "s_waitcnt lgkmcnt(5)\n\t"
        "v_smfmac_f32_16x16x64_bf16 v[88:91], v[20:23], a[32:39], v122\n\t"
        "ds_read_b128 v[72:75], v121 offset:16384\n\t"
        "v_smfmac_f32_16x16x64_bf16 v[92:95], v[20:23], a[40:47], v122\n\t"
        "s_waitcnt lgkmcnt(5)\n\t"
        "s_barrier\n\t"
        "ds_read_b128 v[76:79], v121 offset:17408\n\t"
        "s_waitcnt lgkmcnt(5)\n\t"
        "v_smfmac_f32_16x16x64_bf16 v[88:91], v[24:27], a[48:55], v122\n\t"
        "ds_read_b128 v[0:3], v124 offset:12288\n\t"
        "v_smfmac_f32_16x16x64_bf16 v[92:95], v[24:27], a[56:63], v122\n\t"
        "ds_read_b128 v[80:83], v121 offset:18432\n\t"
        "s_waitcnt lgkmcnt(6)\n\t"
        "v_smfmac_f32_16x16x64_bf16 v[88:91], v[28:31], a[144:151], v122\n\t"
        "ds_read_b128 v[84:87], v121 offset:19456\n\t"
        "v_smfmac_f32_16x16x64_bf16 v[92:95], v[28:31], a[152:159], v122\n\t"
        "ds_read_b128 v[40:43], v124 offset:8192\n\t"
        "s_waitcnt lgkmcnt(7)\n\t"
        "v_smfmac_f32_16x16x64_bf16 v[88:91], v[32:35], a[160:167], v122\n\t"
        "ds_read_b128 v[44:47], v125 offset:8192\n\t"
        "v_mov_b32_e32 v12, 0x3f803f80\n\t"
        "v_smfmac_f32_16x16x64_bf16 v[92:95], v[32:35], a[168:175], v122\n\t"
        "ds_read_b128 v[56:59], v121 offset:20480\n\t"
        "v_mov_b32_e32 v13, 0x3f803f80\n\t"
        "s_waitcnt lgkmcnt(8)\n\t"
        "v_smfmac_f32_16x16x64_bf16 v[88:91], v[36:39], a[176:183], v122\n\t"
        "ds_read_b128 v[60:63], v121 offset:21504\n\t"
        "v_mov_b32_e32 v14, 0x3f803f80\n\t"
        "v_smfmac_f32_16x16x64_bf16 v[92:95], v[36:39], a[184:191], v122\n\t"
        "ds_read_b128 v[4:7], v125 offset:12288\n\t"
        "v_mov_b32_e32 v15, 0x3f803f80\n\t"
        "s_waitcnt lgkmcnt(8)\n\t"
        "s_waitcnt lgkmcnt(7)\n\t"
        "v_smfmac_f32_16x16x64_bf16 v[88:91], v[0:3], v[72:79], v122\n\t"
        "ds_read_b128 v[64:67], v121 offset:22528\n\t"
        "s_waitcnt lgkmcnt(6)\n\t"
        "v_smfmac_f32_16x16x64_bf16 v[92:95], v[0:3], v[80:87], v122\n\t"
        "ds_read_b128 v[68:71], v121 offset:23552\n\t"
        "s_waitcnt lgkmcnt(6)\n\t"
        "v_smfmac_f32_16x16x64_bf16 v[88:91], v[40:43], v[144:151], v122\n\t"
        "ds_read_b128 v[48:51], v126 offset:8192\n\t"
        "v_smfmac_f32_16x16x64_bf16 v[92:95], v[40:43], v[152:159], v122\n\t"
        "ds_read_b128 v[8:11], v126 offset:12288\n\t"
        "s_waitcnt lgkmcnt(7)\n\t"
        "v_smfmac_f32_16x16x64_bf16 v[88:91], v[44:47], v[160:167], v122\n\t"
        "ds_read_b128 v[72:75], v121 offset:24576\n\t"
        "v_smfmac_f32_16x16x64_bf16 v[92:95], v[44:47], v[168:175], v122\n\t"
        "ds_read_b128 v[76:79], v121 offset:25600\n\t"
        "s_waitcnt lgkmcnt(7)\n\t"
        "s_waitcnt lgkmcnt(6)\n\t"
        "v_smfmac_f32_16x16x64_bf16 v[88:91], v[4:7], v[56:63], v122\n\t"
        "ds_read_b128 v[80:83], v121 offset:26624\n\t"
        "s_waitcnt lgkmcnt(5)\n\t"
        "v_smfmac_f32_16x16x64_bf16 v[92:95], v[4:7], v[64:71], v122\n\t"
        "ds_read_b128 v[84:87], v121 offset:27648\n\t"
        "s_waitcnt lgkmcnt(5)\n\t"
        "v_smfmac_f32_16x16x64_bf16 v[88:91], v[48:51], v[176:183], v122\n\t"
        "v_mov_b32_e32 v64, 0\n\t"
        "v_smfmac_f32_16x16x64_bf16 v[92:95], v[48:51], v[184:191], v122\n\t"
        "v_mov_b32_e32 v65, 0\n\t"
        "s_waitcnt lgkmcnt(4)\n\t"
        "v_smfmac_f32_16x16x64_bf16 v[88:91], v[8:11], a[128:135], v122\n\t"
        "v_mov_b32_e32 v66, 0\n\t"
        "v_smfmac_f32_16x16x64_bf16 v[92:95], v[8:11], a[136:143], v122\n\t"
        "v_mov_b32_e32 v67, 0\n\t"
        "s_waitcnt lgkmcnt(2)\n\t"
        "v_smfmac_f32_16x16x64_bf16 v[96:99], v[0:3], v[72:79], v122\n\t"
        "ds_read_b128 v[72:75], v121 offset:28672\n\t"
        "s_waitcnt lgkmcnt(1)\n\t"
        "v_smfmac_f32_16x16x64_bf16 v[100:103], v[0:3], v[80:87], v122\n\t"
        "v_add_f32_e32 v88, v88, v90\n\t"
        "v_add_f32_e32 v89, v89, v91\n\t"
        "v_smfmac_f32_16x16x64_bf16 v[96:99], v[16:19], a[80:87], v122\n\t"
        "v_cvt_pk_bf16_f32 v90, v88, v89\n\t"
        "v_add_f32_e32 v92, v92, v94\n\t"
        "v_smfmac_f32_16x16x64_bf16 v[100:103], v[16:19], a[88:95], v122\n\t"
        "ds_write_b32 v127, v90\n\t"
        "v_add_f32_e32 v93, v93, v95\n\t"
        "v_cvt_pk_bf16_f32 v91, v92, v93\n\t"
        "v_smfmac_f32_16x16x64_bf16 v[96:99], v[20:23], a[96:103], v122\n\t"
        "ds_write_b32 v127, v91 offset:384\n\t"
        "v_cmp_u_f32_e32 vcc, v88, v89\n\t"
        "v_smfmac_f32_16x16x64_bf16 v[100:103], v[20:23], a[104:111], v122\n\t"
        "ds_read_b64_tr_b16 v[56:57], v52 offset:0\n\t"
        "ds_read_b64_tr_b16 v[58:59], v52 offset:64\n\t"
        "v_smfmac_f32_16x16x64_bf16 v[96:99], v[24:27], a[112:119], v122\n\t"
        "ds_read_b64_tr_b16 v[60:61], v53 offset:0\n\t"
        "ds_read_b64_tr_b16 v[62:63], v53 offset:64\n\t"
        "v_smfmac_f32_16x16x64_bf16 v[100:103], v[24:27], a[120:127], v122\n\t"
        "s_cbranch_vccnz .Lf1%=\n\t"
        ".Lr1%=:\n\t"
        "ds_read_b128 v[76:79], v121 offset:29696\n\t"
        "v_smfmac_f32_16x16x64_bf16 v[96:99], v[28:31], a[208:215], v122\n\t"
        "buffer_load_dwordx4 v[88:91], v120, %[srd], s61 offen offset:0 sc1\n\t"
        "v_smfmac_f32_16x16x64_bf16 v[100:103], v[28:31], a[216:223], v122\n\t"
        "buffer_load_dwordx4 v[92:95], v120, %[srd], s61 offen offset:1024 sc1\n\t"
        "v_smfmac_f32_16x16x64_bf16 v[96:99], v[32:35], a[224:231], v122\n\t"
        "s_waitcnt lgkmcnt(1)\n\t"
        "s_barrier\n\t"
        "v_mfma_f32_16x16x32_bf16 v[68:71], v[104:107], v[56:59], 0\n\t"
        "ds_read_b64_tr_b16 v[16:17], v123 offset:0\n\t"
        "v_mfma_f32_16x16x32_bf16 v[68:71], v[108:111], v[60:63], v[68:71]\n\t"
        "ds_read_b64_tr_b16 v[18:19], v123 offset:32\n\t"
        "v_smfmac_f32_16x16x64_bf16 v[100:103], v[32:35], a[232:239], v122\n\t"
        "ds_read_b128 v[80:83], v121 offset:30720\n\t"
        "v_smfmac_f32_16x16x64_bf16 v[96:99], v[36:39], a[240:247], v122\n\t"
        "ds_read_b128 v[84:87], v121 offset:31744\n\t"
        "v_smfmac_f32_16x16x64_bf16 v[100:103], v[36:39], a[248:255], v122\n\t"
        "s_nop 1\n\t"
        "ds_write_b128 v120, v[68:71] offset:16384\n\t"
        "v_smfmac_f32_16x16x64_bf16 v[96:99], v[8:11], a[192:199], v122\n\t"
        "ds_read_b128 v[32:35], v120 offset:20480\n\t"
        "v_smfmac_f32_16x16x64_bf16 v[100:103], v[8:11], a[200:207], v122\n\t"
        "ds_read_b64_tr_b16 v[20:21], v123 offset:768\n\t"
        "s_waitcnt lgkmcnt(7)\n\t"
        "v_smfmac_f32_16x16x64_bf16 v[96:99], v[4:7], v[72:79], v122\n\t"
        "ds_read_b64_tr_b16 v[22:23], v123 offset:800\n\t"
        "s_waitcnt lgkmcnt(4)\n\t"
        "v_smfmac_f32_16x16x64_bf16 v[100:103], v[4:7], v[80:87], v122\n\t"
        "s_waitcnt lgkmcnt(3)\n\t"
        "s_barrier\n\t"
        "ds_read_b128 v[36:39], v124 offset:16384\n\t"
        "v_smfmac_f32_16x16x64_bf16 v[96:99], v[40:43], v[208:215], v122\n\t"
        "ds_read_b128 v[56:59], v125 offset:16384\n\t"
        "v_smfmac_f32_16x16x64_bf16 v[100:103], v[40:43], v[216:223], v122\n\t"
        "ds_read_b128 v[60:63], v126 offset:16384\n\t"
        "v_smfmac_f32_16x16x64_bf16 v[96:99], v[44:47], v[224:231], v122\n\t"
        "ds_read_b64_tr_b16 v[24:25], v123 offset:1536\n\t"
        "ds_read_b64_tr_b16 v[26:27], v123 offset:1568\n\t"
        "v_smfmac_f32_16x16x64_bf16 v[100:103], v[44:47], v[232:239], v122\n\t"
        "ds_read_b64_tr_b16 v[28:29], v123 offset:2304\n\t"
        "ds_read_b64_tr_b16 v[30:31], v123 offset:2336\n\t"
        "v_smfmac_f32_16x16x64_bf16 v[96:99], v[48:51], v[240:247], v122\n\t"
        "buffer_load_dwordx4 v[84:87], v120, %[srd], s66 offen sc1\n\t"
        "v_smfmac_f32_16x16x64_bf16 v[100:103], v[48:51], v[248:255], v122\n\t"
        "s_waitcnt lgkmcnt(9)\n\t"
        "s_cmp_eq_u32 s40, 63\n\t"
        "s_waitcnt lgkmcnt(6)\n\t"
        "v_add_f32_e32 v68, v68, v36\n\t"
        "v_add_f32_e32 v69, v69, v37\n\t"
        "v_pk_add_f32 v[70:71], v[70:71], v[38:39]\n\t"
        "s_waitcnt lgkmcnt(4)\n\t"
        "v_pk_add_f32 v[56:57], v[56:57], v[60:61]\n\t"
        "v_pk_add_f32 v[58:59], v[58:59], v[62:63]\n\t"
        "v_pk_add_f32 v[68:69], v[68:69], v[56:57]\n\t"
        "v_pk_add_f32 v[70:71], v[70:71], v[58:59]\n\t"
        "v_max3_f32 v2, v68, v69, v70\n\t"
        "v_max_f32_e32 v2, v2, v71\n\t"
        "v_pk_add_f32 v[96:97], v[96:97], v[98:99]\n\t"
        "v_pk_add_f32 v[100:101], v[100:101], v[102:103]\n\t"
        "v_max_f32_dpp v3, v2, v2 row_ror:8 row_mask:0xf bank_mask:0xf\n\t"
        "v_max_f32_dpp v1, v2, v2 row_ror:8 row_mask:0xf bank_mask:0xf\n\t"
        "v_cvt_pk_bf16_f32 v50, v96, v97\n\t"
        "v_cvt_pk_bf16_f32 v51, v100, v101\n\t"
        "v_permlane16_swap_b32_e32 v3, v1\n\t"
        "s_cbranch_scc1 .Lend%=\n\t"
        "v_max_f32_e32 v2, v3, v1\n\t"
        "v_max_f32_e32 v3, v3, v1\n\t"
        "v_mov_b32_e32 v9, 0\n\t"
        "v_mov_b32_e32 v10, 0\n\t"
        "v_permlane32_swap_b32_e32 v2, v3\n\t"
        "v_max_f32_e32 v0, v2, v3\n\t"
        "v_mul_f32_e32 v2, 0xbe0293ee, v0\n\t"
        "v_fmamk_f32 v68, v68, 0x3e0293ee, v2\n\t"
        "v_fmamk_f32 v69, v69, 0x3e0293ee, v2\n\t"
        "v_fmamk_f32 v70, v70, 0x3e0293ee, v2\n\t"
        "v_fmamk_f32 v71, v71, 0x3e0293ee, v2\n\t"
        "v_exp_f32_e32 v68, v68\n\t"
        "v_exp_f32_e32 v69, v69\n\t"
        "v_exp_f32_e32 v70, v70\n\t"
        "v_cvt_pk_bf16_f32 v36, v68, v69\n\t"
        "v_exp_f32_e32 v71, v71\n\t"
        "s_nop 0\n\t"
        "v_mov_b32_dpp v38, v36 row_ror:8 row_mask:0xf bank_mask:0xf\n\t"
        "v_cvt_pk_bf16_f32 v37, v70, v71\n\t"
        "s_nop 1\n\t"
        "v_mov_b32_dpp v39, v37 row_ror:8 row_mask:0xf bank_mask:0xf\n\t"
        "s_waitcnt vmcnt(4)\n\t"
        "s_nop 0\n\t"
        "v_mfma_f32_16x16x32_bf16 v[72:75], v[12:15], v[36:39], 0\n\t"
        "s_waitcnt lgkmcnt(0)\n\t"
        "s_waitcnt vmcnt(1)\n\t"
        "v_smfmac_f32_16x16x64_bf16 v[64:67], v[88:91], v[16:23], v122\n\t"
        "v_smfmac_f32_16x16x64_bf16 v[64:67], v[92:95], v[24:31], v122\n\t"
        "v_mfma_f32_16x16x32_bf16 v[40:43], v[112:115], v[36:39], 0\n\t"
        "v_mfma_f32_16x16x32_bf16 v[44:47], v[116:119], v[36:39], 0\n\t"
        ".Lrk%=:\n\t"
        "s_nop 1\n\t"
        "v_mov_b32_e32 v55, v55\n\t"
        "s_nop 1\n\t"
        "v_mov_b32_e32 v55, v55\n\t"
        "v_pk_add_f32 v[64:65], v[64:65], v[66:67]\n\t"
        "v_fmamk_f32 v66, v64, 0x3e0293ee, v2\n\t"
        "v_fmamk_f32 v67, v65, 0x3e0293ee, v2\n\t"
        "v_cmp_nge_f32_e32 vcc, 0x41a00000, v66\n\t"
        "v_exp_f32_e32 v66, v66\n\t"
        "v_cmp_nge_f32_e64 s[80:81], s38, v67\n\t"
        "v_exp_f32_e32 v67, v67\n\t"
        "s_or_b64 vcc, vcc, s[80:81]\n\t"
        "v_cvt_pk_bf16_f32 v8, v66, v67\n\t"
        "v_cvt_pk_bf16_f32 v11, v66, v67\n\t"
        "s_cbranch_scc1 .Lfb%=\n\t"
        ".Lrm%=:\n\t"
        "v_mfma_f32_16x16x16_bf16 v[72:75], v[12:13], v[8:9], v[72:75]\n\t"
        "v_mfma_f32_16x16x16_bf16 v[40:43], v[50:51], v[8:9], v[40:43]\n\t"
        "v_mfma_f32_16x16x16_bf16 v[44:47], v[50:51], v[10:11], v[44:47]\n\t"
        "s_nop 1\n\t"
        "v_mov_b32_e32 v55, v55\n\t"
        "s_nop 1\n\t"
        "v_mov_b32_e32 v55, v55\n\t"
        "v_rcp_f32_e32 v2, v72\n\t"
        "s_nop 0\n\t"
        "v_pk_mul_f32 v[40:41], v[40:41], v[2:3] op_sel_hi:[1,0]\n\t"
        "v_pk_mul_f32 v[42:43], v[42:43], v[2:3] op_sel_hi:[1,0]\n\t"
        "v_cvt_pk_bf16_f32 v76, v40, v41\n\t"
        "v_pk_mul_f32 v[44:45], v[44:45], v[2:3] op_sel_hi:[1,0]\n\t"
        "v_pk_mul_f32 v[46:47], v[46:47], v[2:3] op_sel_hi:[1,0]\n\t"
        "v_cvt_pk_bf16_f32 v77, v42, v43\n\t"
        "v_cvt_pk_bf16_f32 v78, v44, v45\n\t"
        "v_min_u32_e32 v76, -2, v76\n\t"
        "v_cvt_pk_bf16_f32 v79, v46, v47\n\t"
        "v_min_u32_e32 v78, -2, v78\n\t"
        "buffer_store_dwordx4 v[76:79], v33, %[srd], s63 offen sc1\n\t"
        "s_memrealtime s[76:77]\n\t"
        "v_mov_b32_e32 v8, -1\n\t"
        "v_mov_b32_e32 v9, -1\n\t"
        "v_mov_b32_e32 v10, -1\n\t"
        "v_mov_b32_e32 v11, -1\n\t"
        "v_permlane16_swap_b32_e32 v50, v51\n\t"
        "v_mov_b32_e32 v14, s40\n\t"
        "s_waitcnt lgkmcnt(0)\n\t"
        "s_waitcnt vmcnt(1)\n\t"
        "s_mov_b32 s97, 0\n\t"
        ".Lsr%=:\n\t"
        "s_mov_b32 exec_hi, 0xffff\n\t"
        "v_subrev_u32_e32 v0, s41, v84\n\t"
        "v_subrev_u32_e32 v1, s41, v86\n\t"
        "v_max_i32_e32 v0, v0, v1\n\t"
        "v_max_u32_e32 v1, v85, v87\n\t"
        "v_cmp_eq_u32_e32 vcc, -1, v1\n\t"
        "v_max_i32_dpp v0, v0, v0 quad_perm:[1,0,3,2] row_mask:0xf bank_mask:0xf\n\t"
        "v_or_b32_e32 v2, v85, v87\n\t"
        "s_nop 0\n\t"
        "v_max_i32_dpp v0, v0, v0 quad_perm:[2,3,0,1] row_mask:0xf bank_mask:0xf\n\t"
        "s_nop 1\n\t"
        "v_max_i32_dpp v0, v0, v0 row_ror:4 row_mask:0xf bank_mask:0xf\n\t"
        "s_nop 1\n\t"
        "v_max_i32_dpp v0, v0, v0 row_ror:8 row_mask:0xf bank_mask:0xf\n\t"
        "s_cbranch_vccnz .Lsf%=\n\t"
        "v_readlane_b32 s85, v0, 0\n\t"
        "v_readlane_b32 s81, v0, 16\n\t"
        "v_readlane_b32 s56, v0, 32\n\t"
        "v_and_b32_e32 v1, 1, v2\n\t"
        "v_cmp_ne_u32_e64 s[86:87], 0, v1\n\t"
        "v_and_b32_e32 v1, 2, v2\n\t"
        "v_cmp_ne_u32_e64 s[88:89], 0, v1\n\t"
        "v_and_b32_e32 v1, 4, v2\n\t"
        "v_cmp_ne_u32_e64 s[90:91], 0, v1\n\t"
        "s_max_i32 s56, s56, s81\n\t"
        "s_add_u32 s92, s85, s41\n\t"
        "s_sub_u32 s92, s92, s43\n\t"
        "s_add_i32 s93, s56, s47\n\t"
        "s_mov_b32 s44, s46\n\t"
        "s_mov_b32 s46, s45\n\t"
        "s_mov_b32 s94, s48\n\t"
        "s_mov_b32 s43, s41\n\t"
        "s_cmp_gt_i32 s56, s54\n\t"
        "s_cbranch_scc1 .Lmv%=\n\t"
        "s_mov_b32 s37, 0\n\t"
        "s_mov_b32 s48, 0\n\t"
        "s_mov_b32 s95, 0\n\t"
        ".Lsd%=:\n\t"
        "s_mov_b32 exec_hi, -1\n\t"
        "s_add_u32 s42, s43, s46\n\t"
        "s_sleep %c[HSLP]\n\t"
        "buffer_load_dwordx2 v[48:49], off, %[wsd], s64 sc1\n\t"
        "buffer_load_dwordx4 v[104:107], v120, %[wsd], s69 offen sc1\n\t"
        "buffer_load_dwordx4 v[108:111], v120, %[wsd], s70 offen sc1\n\t"
        "buffer_store_dwordx2 v[50:51], v34, %[wsd], s72 offen offset:2048 sc1\n\t"
        "s_cmp_eq_u32 %[w], 0\n\t"
        "s_cselect_b32 s80, s72, s36\n\t"
        "buffer_store_dwordx4 v[88:91], v120, %[wsd], s80 offen sc1\n\t"
        "s_cmp_eq_u32 %[w], 1\n\t"
        "s_cselect_b32 s80, s72, s36\n\t"
        "buffer_store_dwordx4 v[92:95], v120, %[wsd], s80 offen sc1\n\t"
        "s_lshl_b32 s80, %[w], 10\n\t"
        "s_add_u32 s80, s80, s61\n\t"
        "s_barrier\n\t"
        "buffer_store_dwordx2 v[8:9], v34, %[srd], s80 offen sc1\n\t"
        "s_cmp_eq_u32 %[w], 0\n\t"
        "s_cselect_b32 exec_hi, 0, -1\n\t"
        "buffer_store_dwordx4 v[8:11], v120, %[srd], s68 offen sc1\n\t"
        "s_mov_b32 exec_hi, -1\n\t"
        "s_mov_b64 exec, 1\n\t"
        "buffer_store_dword v14, off, %[wsd], s74 sc1\n\t"
        "s_mov_b64 exec, -1\n\t"
        "ds_read_b128 v[16:19], v121 offset:0\n\t"
        "ds_read_b128 v[20:23], v121 offset:1024\n\t"
        "ds_read_b128 v[24:27], v121 offset:2048\n\t"
        "ds_read_b128 v[28:31], v121 offset:3072\n\t"
        "ds_read_b128 v[32:35], v121 offset:4096\n\t"
        "ds_read_b128 v[36:39], v121 offset:5120\n\t"
        "ds_read_b128 v[40:43], v121 offset:6144\n\t"
        "ds_read_b128 v[44:47], v121 offset:7168\n\t"
        "ds_read_b128 v[56:59], v121 offset:8192\n\t"
        "ds_read_b128 v[60:63], v121 offset:9216\n\t"
        "ds_read_b128 v[64:67], v121 offset:10240\n\t"
        "ds_read_b128 v[68:71], v121 offset:11264\n\t"
        "ds_read_b128 v[72:75], v121 offset:12288\n\t"
        "ds_read_b128 v[76:79], v121 offset:13312\n\t"
        "ds_read_b128 v[80:83], v121 offset:14336\n\t"
        "ds_read_b128 v[84:87], v121 offset:15360\n\t"
        "s_cmp_ge_u32 s40, %c[iCT0]\n\t"
        "s_cselect_b32 s85, 1, 0\n\t"
        "s_cmp_lg_u64 s[86:87], 0\n\t"
        "s_cselect_b32 s86, s85, 0\n\t"
        "s_cmp_lg_u64 s[88:89], 0\n\t"
        "s_cselect_b32 s87, s85, 0\n\t"
        "s_cmp_lg_u64 s[90:91], 0\n\t"
        "s_cselect_b32 s88, s85, 0\n\t"
        "s_or_b32 s80, s94, s86\n\t"
        "s_or_b32 s80, s80, s87\n\t"
        "s_andn2_b32 s90, s85, s80\n\t"
        "s_cmp_le_i32 s92, s44\n\t"
        "s_cselect_b32 s80, s85, 0\n\t"
        "s_andn2_b32 s91, s80, s88\n\t"
        "s_and_b32 s80, s40, %c[LDEC]\n\t"
        "s_cselect_b32 s89, 0, 1\n\t"
        "s_andn2_b32 s80, s86, s94\n\t"
        "s_cmp_lt_i32 s51, %c[LA24]\n\t"
        "s_cselect_b32 s80, s80, 0\n\t"
        "s_mul_i32 s80, s80, %c[iBS]\n\t"
        "s_add_u32 s51, s80, s51\n\t"
        "s_sub_i32 s51, s51, s89\n\t"
        "s_max_i32 s51, s51, %c[LA]\n\t"
        "s_lshl_b32 s80, s93, 1\n\t"
        "s_sub_i32 s81, s80, s53\n\t"
        "s_ashr_i32 s82, s81, %c[UPSH]\n\t"
        "s_max_i32 s82, s82, %c[TRUP]\n\t"
        "s_add_i32 s82, s82, s53\n\t"
        "s_min_i32 s82, s82, s80\n\t"
        "s_ashr_i32 s83, s81, %c[DNSH]\n\t"
        "s_min_i32 s83, s83, -%c[TRDN]\n\t"
        "s_add_i32 s83, s83, s53\n\t"
        "s_max_i32 s83, s83, s80\n\t"
        "s_cmp_gt_i32 s81, 0\n\t"
        "s_cselect_b32 s82, s82, s83\n\t"
        "s_cmp_lg_u32 s91, 0\n\t"
        "s_cselect_b32 s53, s82, s53\n\t"
        "s_add_i32 s80, s53, 1\n\t"
        "s_ashr_i32 s80, s80, 1\n\t"
        "s_add_i32 s47, s80, s51\n\t"
        "s_add_u32 s81, s47, %c[AUP]\n\t"
        "s_min_i32 s80, s95, s81\n\t"
        "s_max_i32 s47, s47, s80\n\t"
        "s_add_u32 s41, s42, s47\n\t"
        "s_cmp_eq_u32 s40, 0\n\t"
        "s_cselect_b32 s51, %c[LA], s51\n\t"
        "v_mov_b32_e32 v88, 0\n\t"
        "v_mov_b32_e32 v89, 0\n\t"
        "v_mov_b32_e32 v90, 0\n\t"
        "v_mov_b32_e32 v91, 0\n\t"
        "v_mov_b32_e32 v92, 0\n\t"
        "v_mov_b32_e32 v93, 0\n\t"
        "v_mov_b32_e32 v94, 0\n\t"
        "v_mov_b32_e32 v95, 0\n\t"
        "v_mov_b32_e32 v96, 0\n\t"
        "v_mov_b32_e32 v97, 0\n\t"
        "v_mov_b32_e32 v98, 0\n\t"
        "v_mov_b32_e32 v99, 0\n\t"
        "v_mov_b32_e32 v100, 0\n\t"
        "v_mov_b32_e32 v101, 0\n\t"
        "v_mov_b32_e32 v102, 0\n\t"
        "v_mov_b32_e32 v103, 0\n\t"
        "s_add_u32 s40, s40, 1\n\t"
        "v_mov_b32_e32 v6, 30720\n\t"
        "ds_read_b64 v[6:7], v6\n\t"
        "s_waitcnt lgkmcnt(0)\n\t"
        "v_readfirstlane_b32 s80, v6\n\t"
        "v_readfirstlane_b32 s81, v7\n\t"
        "s_cmp_eq_u32 s80, s40\n\t"
        "s_cselect_b32 s84, 1, 0\n\t"
        "s_cmp_eq_u32 s81, s40\n\t"
        "s_cselect_b32 s81, 2, 0\n\t"
        "s_or_b32 s84, s84, s81\n\t"
        "s_mov_b32 s85, s79\n\t"
        "s_and_b32 s80, s40, 3\n\t"
        "s_lshl_b32 s80, s80, 17\n\t"
        "s_add_u32 s57, s80, %[nA]\n\t"
        "s_add_u32 s58, s57, 0x1000\n\t"
        "s_add_u32 s59, s57, 0x8000\n\t"
        "s_add_u32 s60, s57, 0x9000\n\t"
        "s_and_b32 s80, s40, 3\n\t"
        "s_lshl_b32 s80, s80, 17\n\t"
        "s_add_u32 s63, s80, %[nC]\n\t"
        "s_add_u32 s66, s80, %[nS]\n\t"
        "s_add_u32 s81, s80, 0x20000\n\t"
        "s_and_b32 s81, s81, 0x60000\n\t"
        "s_add_u32 s67, s81, %[nW]\n\t"
        "s_add_u32 s79, s81, %[nT]\n\t"
        "s_cmp_eq_u32 %[w], 2\n\t"
        "s_cselect_b32 s81, s80, s81\n\t"
        "s_xor_b32 s81, s81, 0x40000\n\t"
        "s_add_u32 s68, s81, %[pob]\n\t"
        "s_lshl_b32 s72, s40, 17\n\t"
        "s_add_u32 s72, s72, %[kvo]\n\t"
        "v_lshlrev_b32_e32 v51, 6, v120\n\t"
        "s_cmp_eq_u32 %[w], 0\n\t"
        "s_cselect_b32 s73, s85, s36\n\t"
        "s_cselect_b32 s74, %[pra], s36\n\t"
        "v_mov_b32_e32 v54, s40\n\t"
        "s_cmp_ge_u32 s40, 60\n\t"
        "s_cselect_b32 s65, s74, s36\n\t"
        "s_mov_b32 s80, s61\n\t"
        "s_mov_b32 s61, s62\n\t"
        "s_mov_b32 s62, s80\n\t"
        "s_waitcnt vmcnt(8)\n\t"
        "s_cmp_eq_u32 s64, s36\n\t"
        "s_cbranch_scc1 .Lk11%=\n\t"
        "s_mov_b32 s97, 0\n\t"
        ".Lk01%=:\n\t"
        "v_readfirstlane_b32 s82, v48\n\t"
        "v_readfirstlane_b32 s83, v49\n\t"
        "s_xor_b32 s82, s82, %[tag]\n\t"
        "s_xor_b32 s83, s83, %[tag]\n\t"
        "s_or_b32 s82, s82, s83\n\t"
        "s_cbranch_scc0 .Lk11%=\n\t"
        "s_sleep 3\n\t"
        "buffer_load_dwordx2 v[48:49], off, %[wsd], s64 sc1\n\t"
        "s_add_u32 s97, s97, 1\n\t"
        "s_cmp_lt_u32 s97, 0x200000\n\t"
        "s_cbranch_scc0 .Lgu%=\n\t"
        "s_waitcnt vmcnt(0)\n\t"
        "s_branch .Lk01%=\n\t"
        ".Lk11%=:\n\t"
        "v_mov_b32_e32 v48, s76\n\t"
        "v_mov_b32_e32 v49, s84\n\t"
        "s_branch .Lloop%=\n\t"
        ".Lmv%=:\n\t"
        ".Lhk0%=:\n\t"
        "s_cbranch_execz .Lhk1%=\n\t"
        "s_mov_b32 s48, 1\n\t"
        "s_cmp_eq_u32 s54, %c[iTOL]\n\t"
        "s_cbranch_scc1 .Lm1%=\n\t"
        "s_max_i32 s80, s85, s56\n\t"
        "s_add_u32 s43, s41, s80\n\t"
        "s_add_u32 s43, s43, %c[iPEN0]\n\t"
        ".Lhk1%=:\n\t"
        "s_cbranch_execz .Lhk2%=\n\t"
        "s_mov_b32 s54, %c[iTOL]\n\t"
        "s_add_u32 s46, s46, %c[iX0C]\n\t"
        "s_mov_b32 s95, 0\n\t"
        "s_add_u32 s51, s51, %c[iX0A]\n\t"
        "v_and_b32_e32 v0, 3, v84\n\t"
        "v_and_b32_e32 v1, 3, v86\n\t"
        ".Lhk2%=:\n\t"
        "s_cbranch_execz .Lhk3%=\n\t"
        "v_lshlrev_b32_e64 v0, v0, 1\n\t"
        "v_lshlrev_b32_e64 v1, v1, 1\n\t"
        "v_or_b32_e32 v0, v0, v1\n\t"
        "s_mov_b32 s80, 0\n\t"
        "v_and_b32_e32 v1, 1, v0\n\t"
        "v_cmp_ne_u32_e32 vcc, 0, v1\n\t"
        ".Lhk3%=:\n\t"
        "s_cbranch_execz .Lhk4%=\n\t"
        "s_nop 1\n\t"
        "s_cmp_lg_u64 vcc, 0\n\t"
        "s_cselect_b32 s81, 2, 0\n\t"
        "s_or_b32 s80, s80, s81\n\t"
        "v_and_b32_e32 v1, 2, v0\n\t"
        "v_cmp_ne_u32_e32 vcc, 0, v1\n\t"
        ".Lhk4%=:\n\t"
        "s_cbranch_execz .Lhk5%=\n\t"
        "s_nop 1\n\t"
        "s_cmp_lg_u64 vcc, 0\n\t"
        "s_cselect_b32 s81, 4, 0\n\t"
        "s_or_b32 s80, s80, s81\n\t"
        "v_and_b32_e32 v1, 4, v0\n\t"
        "v_cmp_ne_u32_e32 vcc, 0, v1\n\t"
        ".Lhk5%=:\n\t"
        "s_cbranch_execz .Lhk6%=\n\t"
        "s_nop 1\n\t"
        "s_cmp_lg_u64 vcc, 0\n\t"
        "s_cselect_b32 s81, 8, 0\n\t"
        "s_or_b32 s80, s80, s81\n\t"
        "v_and_b32_e32 v1, 8, v0\n\t"
        "v_cmp_ne_u32_e32 vcc, 0, v1\n\t"
        ".Lhk6%=:\n\t"
        "s_cbranch_execz .Lhk7%=\n\t"
        "s_nop 1\n\t"
        "s_cmp_lg_u64 vcc, 0\n\t"
        "s_cselect_b32 s81, 16, 0\n\t"
        "s_or_b32 s80, s80, s81\n\t"
        "s_lshr_b32 s80, 0x1adf1210, s80\n\t"
        "s_and_b32 s55, s80, 3\n\t"
        ".Lhk7%=:\n\t"
        "s_cbranch_execz .Lhk8%=\n\t"
        "s_branch .Lal%=\n\t"
        ".Lm1%=:\n\t"
        "s_add_u32 s80, s37, %c[iPEN2]\n\t"
        "s_min_i32 s80, s80, %c[iPENM]\n\t"
        "s_cmp_lg_u32 s37, 0\n\t"
        "s_cselect_b32 s37, s80, %c[iPEN]\n\t"
        "s_add_u32 s80, s56, s37\n\t"
        ".Lhk8%=:\n\t"
        "s_cbranch_execz .Lhk9%=\n\t"
        "s_max_i32 s80, s80, %c[iPMIN]\n\t"
        "s_add_u32 s43, s41, s80\n\t"
        "s_add_u32 s95, s93, %c[LA]\n\t"
        "s_add_u32 s46, s46, %c[iXR]\n\t"
        ".Lal%=:\n\t"
        "s_sub_u32 s80, s55, s43\n\t"
        "s_and_b32 s80, s80, %c[iPHM]\n\t"
        ".Lhk9%=:\n\t"
        "s_cbranch_execz .Lhk10%=\n\t"
        "s_add_u32 s43, s43, s80\n\t"
        "s_branch .Lsd%=\n\t"
        ".Lsf%=:\n\t"
        "s_mov_b32 exec_hi, -1\n\t"
        "s_sleep 2\n\t"
        "buffer_load_dwordx4 v[84:87], v120, %[srd], s66 offen sc1\n\t"
        "s_add_u32 s97, s97, 1\n\t"
        ".Lhk10%=:\n\t"
        "s_cbranch_execz .Lhk11%=\n\t"
        "s_cmp_lt_u32 s97, 0x100000\n\t"
        "s_cbranch_scc0 .Lgu%=\n\t"
        "s_waitcnt vmcnt(0)\n\t"
        "s_branch .Lsr%=\n\t"
        ".Lf1%=:\n\t"
        "v_max_u32_e32 v54, v0, v2\n\t"
        "v_max3_u32 v54, v54, v1, v3\n\t"
        ".Lhk11%=:\n\t"
        "s_cbranch_execz .Lhk12%=\n\t"
        "v_max3_u32 v54, v54, v4, v6\n\t"
        "v_max3_u32 v54, v54, v5, v7\n\t"
        "v_max3_u32 v54, v54, v8, v10\n\t"
        "v_max3_u32 v54, v54, v9, v11\n\t"
        "s_sleep %c[RSLP]\n\t"
        "buffer_load_dwordx4 v[0:3], v120, %[srd], s57 offen sc1\n\t"
        ".Lhk12%=:\n\t"
        "s_cbranch_execz .Lhk13%=\n\t"
        "buffer_load_dwordx4 v[4:7], v120, %[srd], s58 offen sc1\n\t"
        "buffer_load_dwordx4 v[8:11], v120, %[srd], s59 offen sc1\n\t"
        "buffer_load_dwordx4 v[12:15], v120, %[srd], s60 offen sc1\n\t"
        "buffer_store_dword v0, off, %[wsd], s36\n\t"
        "buffer_store_dword v0, off, %[wsd], s36\n\t"
        "buffer_store_dword v0, off, %[wsd], s36\n\t"
        ".Lhk13%=:\n\t"
        "s_cbranch_execz .Lhk14%=\n\t"
        "ds_read_b128 v[92:95], v120 offset:0\n\t"
        "ds_read_b128 v[68:71], v120 offset:4096\n\t"
        "v_max3_u32 v54, v54, v16, v18\n\t"
        "v_max3_u32 v54, v54, v17, v19\n\t"
        "v_max3_u32 v54, v54, v20, v22\n\t"
        "v_max3_u32 v54, v54, v21, v23\n\t"
        ".Lhk14%=:\n\t"
        "s_cbranch_execz .Lhk15%=\n\t"
        "v_max3_u32 v54, v54, v24, v26\n\t"
        "v_max3_u32 v54, v54, v25, v27\n\t"
        "v_max3_u32 v54, v54, v28, v30\n\t"
        "v_max3_u32 v54, v54, v29, v31\n\t"
        "v_max3_u32 v54, v54, v32, v34\n\t"
        "v_max3_u32 v54, v54, v33, v35\n\t"
        ".Lhk15%=:\n\t"
        "s_cbranch_execz .Lhk16%=\n\t"
        "v_max3_u32 v54, v54, v36, v38\n\t"
        "v_max3_u32 v54, v54, v37, v39\n\t"
        "v_max3_u32 v54, v54, v40, v42\n\t"
        "v_max3_u32 v54, v54, v41, v43\n\t"
        "v_max3_u32 v54, v54, v44, v46\n\t"
        "v_max3_u32 v54, v54, v45, v47\n\t"
        ".Lhk16%=:\n\t"
        "s_cbranch_execz .Lhk17%=\n\t"
        "v_max3_u32 v54, v54, v48, v50\n\t"
        "v_max3_u32 v54, v54, v49, v51\n\t"
        "s_waitcnt lgkmcnt(0)\n\t"
        "v_max3_u32 v54, v54, v92, v94\n\t"
        "v_max3_u32 v54, v54, v93, v95\n\t"
        "v_max3_u32 v54, v54, v68, v70\n\t"
        ".Lhk17%=:\n\t"
        "s_cbranch_execz .Lhk18%=\n\t"
        "v_max3_u32 v54, v54, v69, v71\n\t"
        "ds_read_b128 v[92:95], v120 offset:8192\n\t"
        "ds_read_b128 v[68:71], v120 offset:12288\n\t"
        "s_waitcnt lgkmcnt(0)\n\t"
        "v_max3_u32 v54, v54, v92, v94\n\t"
        "v_max3_u32 v54, v54, v93, v95\n\t"
        ".Lhk18%=:\n\t"
        "s_cbranch_execz .Lhk19%=\n\t"
        "v_max3_u32 v54, v54, v68, v70\n\t"
        "v_max3_u32 v54, v54, v69, v71\n\t"
        "v_cmp_eq_u32_e32 vcc, -1, v54\n\t"
        "s_nop 1\n\t"
        "v_mov_b32_e32 v55, v55\n\t"
        "s_nop 0\n\t"
        ".Lhk19%=:\n\t"
        "s_cbranch_execz .Lhk20%=\n\t"
        "s_cbranch_vccz .Lf2%=\n\t"
        "ds_read_b128 v[16:19], v121 offset:0\n\t"
        "ds_read_b128 v[20:23], v121 offset:1024\n\t"
        "ds_read_b128 v[24:27], v121 offset:2048\n\t"
        "ds_read_b128 v[28:31], v121 offset:3072\n\t"
        "ds_read_b128 v[32:35], v121 offset:4096\n\t"
        ".Lhk20%=:\n\t"
        "s_cbranch_execz .Lhk21%=\n\t"
        "ds_read_b128 v[36:39], v121 offset:5120\n\t"
        "ds_read_b128 v[40:43], v121 offset:6144\n\t"
        "ds_read_b128 v[44:47], v121 offset:7168\n\t"
        "ds_read_b128 v[56:59], v121 offset:8192\n\t"
        "ds_read_b128 v[60:63], v121 offset:9216\n\t"
        "ds_read_b128 v[64:67], v121 offset:10240\n\t"
        ".Lhk21%=:\n\t"
        "s_cbranch_execz .Lhk22%=\n\t"
        "ds_read_b128 v[68:71], v121 offset:11264\n\t"
        "ds_read_b128 v[72:75], v121 offset:12288\n\t"
        "ds_read_b128 v[76:79], v121 offset:13312\n\t"
        "ds_read_b128 v[80:83], v121 offset:14336\n\t"
        "ds_read_b128 v[84:87], v121 offset:15360\n\t"
        "s_add_u32 s80, s40, 1\n\t"
        ".Lhk22%=:\n\t"
        "s_cbranch_execz .Lhk23%=\n\t"
        "v_mov_b32_e32 v54, 30720\n\t"
        "v_mov_b32_e32 v55, s80\n\t"
        "ds_write_b32 v54, v55\n\t"
        "s_add_u32 s97, s97, 1\n\t"
        "s_cmp_lt_u32 s97, 0x100000\n\t"
        "s_cbranch_scc0 .Lgu%=\n\t"
        ".Lhk23%=:\n\t"
        "s_cbranch_execz .Lhk24%=\n\t"
        "s_mov_b32 s65, s36\n\t"
        "v_mov_b32_e32 v88, 0\n\t"
        "v_mov_b32_e32 v89, 0\n\t"
        "v_mov_b32_e32 v90, 0\n\t"
        "v_mov_b32_e32 v91, 0\n\t"
        "v_mov_b32_e32 v92, 0\n\t"
        ".Lhk24%=:\n\t"
        "s_cbranch_execz .Lhk25%=\n\t"
        "v_mov_b32_e32 v93, 0\n\t"
        "v_mov_b32_e32 v94, 0\n\t"
        "v_mov_b32_e32 v95, 0\n\t"
        "v_mov_b32_e32 v96, 0\n\t"
        "v_mov_b32_e32 v97, 0\n\t"
        "v_mov_b32_e32 v98, 0\n\t"
        ".Lhk25%=:\n\t"
        "s_cbranch_execz .Lhk26%=\n\t"
        "v_mov_b32_e32 v99, 0\n\t"
        "v_mov_b32_e32 v100, 0\n\t"
        "v_mov_b32_e32 v101, 0\n\t"
        "v_mov_b32_e32 v102, 0\n\t"
        "v_mov_b32_e32 v103, 0\n\t"
        "s_waitcnt lgkmcnt(0)\n\t"
        ".Lhk26%=:\n\t"
        "s_cbranch_execz .Lhk27%=\n\t"
        "s_barrier\n\t"
        "s_branch .Lgo%=\n\t"
        ".Lf2%=:\n\t"
        "s_waitcnt vmcnt(0)\n\t"
        "v_mov_b32_e32 v12, 0x3f803f80\n\t"
        "v_mov_b32_e32 v13, 0x3f803f80\n\t"
        "v_mov_b32_e32 v14, 0x3f803f80\n\t"
        ".Lhk27%=:\n\t"
        "s_cbranch_execz .Lhk28%=\n\t"
        "v_mov_b32_e32 v15, 0x3f803f80\n\t"
        "ds_read_b128 v[0:3], v124 offset:12288\n\t"
        "ds_read_b128 v[4:7], v125 offset:12288\n\t"
        "ds_read_b128 v[8:11], v126 offset:12288\n\t"
        "s_waitcnt lgkmcnt(0)\n\t"
        "s_branch .Lr1%=\n\t"
        ".Lfb%=:\n\t"
        ".Lhk28%=:\n\t"
        "s_cbranch_execz .Lhk29%=\n\t"
        "v_max_u32_e32 v54, v88, v90\n\t"
        "v_max3_u32 v54, v54, v89, v91\n\t"
        "v_max3_u32 v54, v54, v92, v94\n\t"
        "v_max3_u32 v54, v54, v93, v95\n\t"
        "v_cmp_eq_u32_e32 vcc, -1, v54\n\t"
        "s_nop 1\n\t"
        ".Lhk29%=:\n\t"
        "s_cbranch_execz .Lhk30%=\n\t"
        "v_mov_b32_e32 v55, v55\n\t"
        "s_nop 0\n\t"
        "s_cbranch_vccz .Lfm%=\n\t"
        "s_add_u32 s80, s40, 1\n\t"
        "v_mov_b32_e32 v54, 30724\n\t"
        "v_mov_b32_e32 v55, s80\n\t"
        ".Lhk30%=:\n\t"
        "s_cbranch_execz .Lhk31%=\n\t"
        "ds_write_b32 v54, v55\n\t"
        "s_sleep %c[ASLP]\n\t"
        "buffer_load_dwordx4 v[88:91], v120, %[srd], s61 offen offset:0 sc1\n\t"
        "buffer_load_dwordx4 v[92:95], v120, %[srd], s61 offen offset:1024 sc1\n\t"
        "s_add_u32 s97, s97, 1\n\t"
        "s_cmp_lt_u32 s97, 0x100000\n\t"
        ".Lhk31%=:\n\t"
        "s_cbranch_execz .Lhk32%=\n\t"
        "s_cbranch_scc0 .Lgu%=\n\t"
        "v_mov_b32_e32 v64, 0\n\t"
        "v_mov_b32_e32 v65, 0\n\t"
        "v_mov_b32_e32 v66, 0\n\t"
        "v_mov_b32_e32 v67, 0\n\t"
        "s_waitcnt vmcnt(0)\n\t"
        ".Lhk32%=:\n\t"
        "s_cbranch_execz .Lhk33%=\n\t"
        "s_nop 0\n\t"
        "v_smfmac_f32_16x16x64_bf16 v[64:67], v[88:91], v[16:23], v122\n\t"
        "v_smfmac_f32_16x16x64_bf16 v[64:67], v[92:95], v[24:31], v122\n\t"
        "s_nop 7\n\t"
        "s_branch .Lrk%=\n\t"
        ".Lfm%=:\n\t"
        "v_max3_f32 v3, v0, v64, v65\n\t"
        ".Lhk33%=:\n\t"
        "s_cbranch_execz .Lhk34%=\n\t"
        "v_max3_f32 v1, v0, v64, v65\n\t"
        "s_nop 1\n\t"
        "v_permlane16_swap_b32_e32 v3, v1\n\t"
        "v_max_f32_e32 v2, v3, v1\n\t"
        "v_max_f32_e32 v3, v3, v1\n\t"
        "s_nop 1\n\t"
        ".Lhk34%=:\n\t"
        "s_cbranch_execz .Lhk35%=\n\t"
        "v_permlane32_swap_b32_e32 v2, v3\n\t"
        "v_max_f32_e32 v2, v2, v3\n\t"
        "v_mul_f32_e32 v2, 0xbe0293ee, v2\n\t"
        "v_fmamk_f32 v6, v0, 0x3e0293ee, v2\n\t"
        "v_fmamk_f32 v66, v64, 0x3e0293ee, v2\n\t"
        "v_exp_f32_e32 v6, v6\n\t"
        ".Lhk35%=:\n\t"
        "s_cbranch_execz .Lhk36%=\n\t"
        "v_fmamk_f32 v67, v65, 0x3e0293ee, v2\n\t"
        "v_exp_f32_e32 v66, v66\n\t"
        "v_exp_f32_e32 v67, v67\n\t"
        "v_mul_f32_e32 v72, v72, v6\n\t"
        "v_pk_mul_f32 v[40:41], v[40:41], v[6:7] op_sel_hi:[1,0]\n\t"
        "v_pk_mul_f32 v[42:43], v[42:43], v[6:7] op_sel_hi:[1,0]\n\t"
        ".Lhk36%=:\n\t"
        "s_cbranch_execz .Lhk37%=\n\t"
        "v_pk_mul_f32 v[44:45], v[44:45], v[6:7] op_sel_hi:[1,0]\n\t"
        "v_pk_mul_f32 v[46:47], v[46:47], v[6:7] op_sel_hi:[1,0]\n\t"
        "v_cvt_pk_bf16_f32 v8, v66, v67\n\t"
        "v_cvt_pk_bf16_f32 v11, v66, v67\n\t"
        "s_nop 1\n\t"
        "s_branch .Lrm%=\n\t"
        ".Lta%=:\n\t"
        ".Lhk37%=:\n\t"
        "s_cbranch_execz .Lhk38%=\n\t"
        "s_waitcnt lgkmcnt(0)\n\t"
        "s_branch .Lxa%=\n\t"
        ".Lgu%=:\n\t"
        "s_mov_b64 exec, -1\n\t"
        "v_mov_b32_e32 v54, %c[ferr]\n\t"
        "v_mov_b32_e32 v55, 1\n\t"
        "buffer_store_dword v55, v54, %[wsd], 0 offen sc1\n\t"
        ".Lhk38%=:\n\t"
        "s_cbranch_execz .Lhk39%=\n\t"
        "s_endpgm\n\t"
        ".Lend%=:\n\t"
        "v_min_u32_e32 v50, -2, v50\n\t"
        "v_min_u32_e32 v51, -2, v51\n\t"
        "s_nop 1\n\t"
        "v_permlane16_swap_b32_e32 v50, v51\n\t"
        "buffer_store_dwordx2 v[50:51], v34, %[wsd], s72 offen offset:2048 sc1\n\t"
        ".Lhk39%=:\n\t"
        "s_cbranch_execz .Lhk40%=\n\t"
        "s_endpgm\n\t"
        ".Lhk40%=:\n\t"
        "s_branch .Lhr%="
        : [nA] "+s"(nA), [nS] "+s"(nS), [nT] "+s"(nT), [nW] "+s"(nW), [nC] "+s"(nC), [b0] "+s"(b0), [pob] "+s"(pob)
        : [srd] "s"(rhd), [wsd] "s"(wsd), [w] "s"(w), [tag] "s"(tag), [kx] "s"(kx), [vx] "s"(vx), [kf] "s"(kf), [kvo] "s"(kvo), [cal] "s"(cal), [sq] "s"(sq), [sk] "s"(sk), [m0b] "s"(m0b),
          [pra] "s"(pr_a), [ferr] "i"(F_ERR), [iD1] "i"(D1), [iD2] "i"(D2), [LC] "i"(LC0), [LA] "i"(LA0), [LC24] "i"(LC0 + 24), [LA24] "i"(LA0 + 24), [E1] "i"(2 * (D1 - LC0)), [E2] "i"(2 * (D2 - LA0)),
          [iPEN] "i"(PEN), [iPEN2] "i"(PEN2), [iPENM] "i"(PEN + 2 * PEN2), [iPEN0] "i"(PEN0), [iTOL] "i"(TOL), [iCT0] "i"(CT0), [iX0C] "i"(X0C), [iX0A] "i"(X0A), [iPHM] "i"(PH_M),
          [iFT] "i"(-3), [iFN] "i"(FINE_N), [AUP] "i"(A_UP), [ASLP] "i"(A_SLP), [LDEC] "i"(L_DEC), [UPSH] "i"(UP_SH), [TRUP] "i"(TR_UP), [TRDN] "i"(TR_DN), [HSLP] "i"(H_SLP), [iPMIN] "i"(PMIN), [VSLP] "i"(V_SLP), [RSLP] "i"(R_SLP), [iVD] "i"(V_DEL), [iXR] "i"(XR), [iBM] "i"(BM_), [iBS] "i"(BS_), [VASL] "i"(VA_SLP), [CSLP] "i"(C_SLP), [DNSH] "i"(DN_SH), [NX0] "i"(NEARX), [NX1] "i"(8192 - NEARX), [iCS1] "i"(CS1), [iCS2] "i"(CS2), [NY0] "i"(NEARY), [NY1] "i"(16384 - NEARY),
          "{v[120:127]}"(pc), "{v[112:113]}"(pd), "{v96}"(dv), "{v[98:105]}"(pa)
        : "memory", "vcc", "scc", "m0", "s36", "s37", "s38", "s39", "s40", "s41", "s42", "s43", "s44", "s45", "s46", "s47", "s48", "s49", "s50", "s51", "s52", "s53", "s54", "s55", "s56", "s57", "s58", "s59", "s60", "s61", "s62", "s63", "s64", "s65", "s66", "s67", "s68", "s69", "s70", "s71", "s72", "s73", "s74", "s75", "s76", "s77", "s78", "s79", "s80", "s81", "s82", "s83", "s84", "s85", "s86", "s87", "s88", "s89", "s90", "s91", "s92", "s93", "s94", "s95", "s96", "s97", "s98", "s99");
    } else {
        const int cb = i32;
        const v4u sv = mk_srd(p.Wk, 1024 * 4096 * 2), so = mk_srd(p.Wo, 1024 * 4096 * 2);
        const unsigned key = KEYM & ((cb & 1) * 2 + G), tu = cb >> 2, m0b = w * 32768, f = (g & 1) | ((c >> 1) << 1);
        const unsigned t0 = m0b + (16 * (g >> 1) + 4 * (g & 1) + (c & 1) + 8 * (c >> 1)) * 128 + (f << 5) + (n16 & 3) * 8, t1 = m0b + (16 * g + 4 * (c >> 1) + (c & 1)) * 64 + ((c >> 1) << 5) + (n16 & 3) * 8;
        v8u pa;
        pa[0] = (64 * w + (l >> 3)) * 8192 + vi * 128 + (((l & 7) ^ ((l >> 5) << 1)) << 4); pa[1] = pa[0] ^ 64u;
        pa[2] = (1024 * w + (l >> 2)) * 2048 + cb * 64 + (((l & 3) ^ ((g & 1) << 1)) << 4);
#pragma unroll
        for (int q = 0; q < 4; ++q) pa[3 + q] = t0 ^ (((w + q) & 3) << 5);
        pa[7] = t1;
        const v2u pb = {t1 ^ 32u, (unsigned)((w * 1024 + cb * 32) * 2048 + l * 1024) + (((3u - (so.x >> 7)) & 7u) << 7)};
        const v16u pc = {(unsigned)tid * 16, (n16 & 2) ? 0xEEEEu : 0x4444u, (unsigned)((vi & 1) * 1024 + w * 256 + ((n16 & 1) + 2 * (n16 >> 3) + 4 * g) * 16 + (n16 & 6) * 2), (unsigned)(4096 + tid * 16),
                   w < 3 ? (unsigned)((cb >> 4) * 32768 + ((cb >> 1) & 7) * 1024 + ((cb & 1) * 2 + (l >> 5)) * 256 + G * 128 + (l & 31) * 4) : OOB,
                   tid < 3 ? (unsigned)(l * 1024 + (32 + vi) * 8) : OOB, 0u, (unsigned)(L_V + 8192 + w * 512 + (g & 1) * 256 + (g >> 1) * 64 + n16 * 4),
                   (unsigned)(L_V + (((l >> 2) & 1) + ((l >> 3) & 2)) * 128 + (l >> 5) * 64 + (l & 8) * 4 + (l & 3) * 8), (l & 1) ? 0x03020706u : 0x05040100u,
                   (unsigned)(L_V + w * 2048 + l * 8), (unsigned)(L_V + ((w + 1) & 3) * 2048 + 512 + l * 8), (unsigned)(L_V + ((w + 2) & 3) * 2048 + 1024 + l * 8), (unsigned)(L_V + ((w + 3) & 3) * 2048 + 1536 + l * 8), 0u, 0u};
        if (tid < 2) ((lu32*)(size_t)(L_V + L_FL))[tid] = ~0u;
    asm volatile(
        "s_mov_b32 s82, 3\n\t"
        ".Ld0%=:\n\t"
        "s_xor_b32 s80, s82, %[key]\n\t"
        "s_mul_i32 s81, s80, 131072\n\t"
        "s_lshl_b32 s80, s80, 11\n\t"
        "s_add_u32 s80, s80, %[m0b]\n\t"
        "s_add_u32 m0, s80, 0\n\t"
        "s_add_u32 s81, s81, 0\n\t"
        "buffer_load_dwordx4 v96, %[sv], s81 offen lds\n\t"
        "s_add_u32 m0, m0, 0x400\n\t"
        "s_add_u32 s81, s81, 65536\n\t"
        "buffer_load_dwordx4 v97, %[sv], s81 offen lds\n\t"
        "s_sub_u32 s82, s82, 1\n\t"
        "s_cbranch_scc0 .Ld0%=\n\t"
        "s_mov_b32 s82, 3\n\t"
        ".Ld1%=:\n\t"
        "s_xor_b32 s80, s82, %[key]\n\t"
        "s_mul_i32 s81, s80, 131072\n\t"
        "s_lshl_b32 s80, s80, 11\n\t"
        "s_add_u32 s80, s80, %[m0b]\n\t"
        "s_add_u32 m0, s80, 8192\n\t"
        "s_add_u32 s81, s81, 2097152\n\t"
        "buffer_load_dwordx4 v96, %[sv], s81 offen lds\n\t"
        "s_add_u32 m0, m0, 0x400\n\t"
        "s_add_u32 s81, s81, 65536\n\t"
        "buffer_load_dwordx4 v97, %[sv], s81 offen lds\n\t"
        "s_sub_u32 s82, s82, 1\n\t"
        "s_cbranch_scc0 .Ld1%=\n\t"
        "s_cmp_eq_u32 %[tu], 0\n\t"
        "s_cselect_b32 s81, 0, 0x7fff0000\n\t"
        "buffer_load_dword v110, v105, %[so], s81 offen\n\t"
        "s_mov_b32 s82, 3\n\t"
        ".Ld2%=:\n\t"
        "s_xor_b32 s80, s82, %[key]\n\t"
        "s_mul_i32 s81, s80, 131072\n\t"
        "s_lshl_b32 s80, s80, 11\n\t"
        "s_add_u32 s80, s80, %[m0b]\n\t"
        "s_add_u32 m0, s80, 16384\n\t"
        "s_add_u32 s81, s81, 4194304\n\t"
        "buffer_load_dwordx4 v96, %[sv], s81 offen lds\n\t"
        "s_add_u32 m0, m0, 0x400\n\t"
        "s_add_u32 s81, s81, 65536\n\t"
        "buffer_load_dwordx4 v97, %[sv], s81 offen lds\n\t"
        "s_sub_u32 s82, s82, 1\n\t"
        "s_cbranch_scc0 .Ld2%=\n\t"
        "s_cmp_eq_u32 %[tu], 1\n\t"
        "s_cselect_b32 s81, 0, 0x7fff0000\n\t"
        "buffer_load_dword v110, v105, %[so], s81 offen\n\t"
        "s_mov_b32 s82, 3\n\t"
        ".Ld3%=:\n\t"
        "s_xor_b32 s80, s82, %[key]\n\t"
        "s_mul_i32 s81, s80, 131072\n\t"
        "s_lshl_b32 s80, s80, 11\n\t"
        "s_add_u32 s80, s80, %[m0b]\n\t"
        "s_add_u32 m0, s80, 24576\n\t"
        "s_add_u32 s81, s81, 6291456\n\t"
        "buffer_load_dwordx4 v96, %[sv], s81 offen lds\n\t"
        "s_add_u32 m0, m0, 0x400\n\t"
        "s_add_u32 s81, s81, 65536\n\t"
        "buffer_load_dwordx4 v97, %[sv], s81 offen lds\n\t"
        "s_sub_u32 s82, s82, 1\n\t"
        "s_cbranch_scc0 .Ld3%=\n\t"
        "s_waitcnt vmcnt(26)\n\t"
        "ds_read_b64_tr_b16 v[128:129], v99 offset:0\n\t"
        "ds_read_b64_tr_b16 v[130:131], v99 offset:256\n\t"
        "ds_read_b64_tr_b16 v[132:133], v99 offset:4096\n\t"
        "ds_read_b64_tr_b16 v[134:135], v99 offset:4352\n\t"
        "ds_read_b64_tr_b16 v[136:137], v100 offset:0\n\t"
        "ds_read_b64_tr_b16 v[138:139], v100 offset:256\n\t"
        "ds_read_b64_tr_b16 v[140:141], v100 offset:4096\n\t"
        "ds_read_b64_tr_b16 v[142:143], v100 offset:4352\n\t"
        "ds_read_b64_tr_b16 v[144:145], v101 offset:0\n\t"
        "ds_read_b64_tr_b16 v[146:147], v101 offset:256\n\t"
        "ds_read_b64_tr_b16 v[148:149], v101 offset:4096\n\t"
        "ds_read_b64_tr_b16 v[150:151], v101 offset:4352\n\t"
        "ds_read_b64_tr_b16 v[152:153], v102 offset:0\n\t"
        "ds_read_b64_tr_b16 v[154:155], v102 offset:256\n\t"
        "ds_read_b64_tr_b16 v[156:157], v102 offset:4096\n\t"
        "ds_read_b64_tr_b16 v[158:159], v102 offset:4352\n\t"
        "s_waitcnt lgkmcnt(0)\n\t"
        "s_cmp_eq_u32 %[tu], 2\n\t"
        "s_cselect_b32 s81, 0, 0x7fff0000\n\t"
        "buffer_load_dword v110, v105, %[so], s81 offen\n\t"
        "s_mov_b32 s82, 3\n\t"
        ".Ld4%=:\n\t"
        "s_xor_b32 s80, s82, %[key]\n\t"
        "s_mul_i32 s81, s80, 65536\n\t"
        "s_lshl_b32 s80, s80, 11\n\t"
        "s_add_u32 s80, s80, %[m0b]\n\t"
        "s_add_u32 m0, s80, 0\n\t"
        "s_add_u32 s81, s81, 0\n\t"
        "buffer_load_dwordx4 v98, %[so], s81 offen lds\n\t"
        "s_add_u32 m0, m0, 0x400\n\t"
        "s_add_u32 s81, s81, 32768\n\t"
        "buffer_load_dwordx4 v98, %[so], s81 offen lds\n\t"
        "s_sub_u32 s82, s82, 1\n\t"
        "s_cbranch_scc0 .Ld4%=\n\t"
        "s_waitcnt vmcnt(27)\n\t"
        "ds_read_b64_tr_b16 v[160:161], v99 offset:8192\n\t"
        "ds_read_b64_tr_b16 v[162:163], v99 offset:8448\n\t"
        "ds_read_b64_tr_b16 v[164:165], v99 offset:12288\n\t"
        "ds_read_b64_tr_b16 v[166:167], v99 offset:12544\n\t"
        "ds_read_b64_tr_b16 v[168:169], v100 offset:8192\n\t"
        "ds_read_b64_tr_b16 v[170:171], v100 offset:8448\n\t"
        "ds_read_b64_tr_b16 v[172:173], v100 offset:12288\n\t"
        "ds_read_b64_tr_b16 v[174:175], v100 offset:12544\n\t"
        "ds_read_b64_tr_b16 v[176:177], v101 offset:8192\n\t"
        "ds_read_b64_tr_b16 v[178:179], v101 offset:8448\n\t"
        "ds_read_b64_tr_b16 v[180:181], v101 offset:12288\n\t"
        "ds_read_b64_tr_b16 v[182:183], v101 offset:12544\n\t"
        "ds_read_b64_tr_b16 v[184:185], v102 offset:8192\n\t"
        "ds_read_b64_tr_b16 v[186:187], v102 offset:8448\n\t"
        "ds_read_b64_tr_b16 v[188:189], v102 offset:12288\n\t"
        "ds_read_b64_tr_b16 v[190:191], v102 offset:12544\n\t"
        "s_waitcnt lgkmcnt(0)\n\t"
        "s_cmp_eq_u32 %[tu], 3\n\t"
        "s_cselect_b32 s81, 0, 0x7fff0000\n\t"
        "buffer_load_dword v110, v105, %[so], s81 offen\n\t"
        "s_mov_b32 s82, 3\n\t"
        ".Ld5%=:\n\t"
        "s_xor_b32 s80, s82, %[key]\n\t"
        "s_mul_i32 s81, s80, 65536\n\t"
        "s_lshl_b32 s80, s80, 11\n\t"
        "s_add_u32 s80, s80, %[m0b]\n\t"
        "s_add_u32 m0, s80, 8192\n\t"
        "s_add_u32 s81, s81, 262144\n\t"
        "buffer_load_dwordx4 v98, %[so], s81 offen lds\n\t"
        "s_add_u32 m0, m0, 0x400\n\t"
        "s_add_u32 s81, s81, 32768\n\t"
        "buffer_load_dwordx4 v98, %[so], s81 offen lds\n\t"
        "s_sub_u32 s82, s82, 1\n\t"
        "s_cbranch_scc0 .Ld5%=\n\t"
        "s_waitcnt vmcnt(27)\n\t"
        "ds_read_b64_tr_b16 v[192:193], v99 offset:16384\n\t"
        "ds_read_b64_tr_b16 v[194:195], v99 offset:16640\n\t"
        "ds_read_b64_tr_b16 v[196:197], v99 offset:20480\n\t"
        "ds_read_b64_tr_b16 v[198:199], v99 offset:20736\n\t"
        "ds_read_b64_tr_b16 v[200:201], v100 offset:16384\n\t"
        "ds_read_b64_tr_b16 v[202:203], v100 offset:16640\n\t"
        "ds_read_b64_tr_b16 v[204:205], v100 offset:20480\n\t"
        "ds_read_b64_tr_b16 v[206:207], v100 offset:20736\n\t"
        "ds_read_b64_tr_b16 v[208:209], v101 offset:16384\n\t"
        "ds_read_b64_tr_b16 v[210:211], v101 offset:16640\n\t"
        "ds_read_b64_tr_b16 v[212:213], v101 offset:20480\n\t"
        "ds_read_b64_tr_b16 v[214:215], v101 offset:20736\n\t"
        "ds_read_b64_tr_b16 v[216:217], v102 offset:16384\n\t"
        "ds_read_b64_tr_b16 v[218:219], v102 offset:16640\n\t"
        "ds_read_b64_tr_b16 v[220:221], v102 offset:20480\n\t"
        "ds_read_b64_tr_b16 v[222:223], v102 offset:20736\n\t"
        "s_waitcnt lgkmcnt(0)\n\t"
        "s_cmp_eq_u32 %[tu], 4\n\t"
        "s_cselect_b32 s81, 0, 0x7fff0000\n\t"
        "buffer_load_dword v110, v105, %[so], s81 offen\n\t"
        "s_mov_b32 s82, 3\n\t"
        ".Ld6%=:\n\t"
        "s_xor_b32 s80, s82, %[key]\n\t"
        "s_mul_i32 s81, s80, 65536\n\t"
        "s_lshl_b32 s80, s80, 11\n\t"
        "s_add_u32 s80, s80, %[m0b]\n\t"
        "s_add_u32 m0, s80, 16384\n\t"
        "s_add_u32 s81, s81, 524288\n\t"
        "buffer_load_dwordx4 v98, %[so], s81 offen lds\n\t"
        "s_add_u32 m0, m0, 0x400\n\t"
        "s_add_u32 s81, s81, 32768\n\t"
        "buffer_load_dwordx4 v98, %[so], s81 offen lds\n\t"
        "s_sub_u32 s82, s82, 1\n\t"
        "s_cbranch_scc0 .Ld6%=\n\t"
        "s_waitcnt vmcnt(27)\n\t"
        "ds_read_b64_tr_b16 v[224:225], v99 offset:24576\n\t"
        "ds_read_b64_tr_b16 v[226:227], v99 offset:24832\n\t"
        "ds_read_b64_tr_b16 v[228:229], v99 offset:28672\n\t"
        "ds_read_b64_tr_b16 v[230:231], v99 offset:28928\n\t"
        "ds_read_b64_tr_b16 v[232:233], v100 offset:24576\n\t"
        "ds_read_b64_tr_b16 v[234:235], v100 offset:24832\n\t"
        "ds_read_b64_tr_b16 v[236:237], v100 offset:28672\n\t"
        "ds_read_b64_tr_b16 v[238:239], v100 offset:28928\n\t"
        "ds_read_b64_tr_b16 v[240:241], v101 offset:24576\n\t"
        "ds_read_b64_tr_b16 v[242:243], v101 offset:24832\n\t"
        "ds_read_b64_tr_b16 v[244:245], v101 offset:28672\n\t"
        "ds_read_b64_tr_b16 v[246:247], v101 offset:28928\n\t"
        "ds_read_b64_tr_b16 v[248:249], v102 offset:24576\n\t"
        "ds_read_b64_tr_b16 v[250:251], v102 offset:24832\n\t"
        "ds_read_b64_tr_b16 v[252:253], v102 offset:28672\n\t"
        "ds_read_b64_tr_b16 v[254:255], v102 offset:28928\n\t"
        "s_waitcnt lgkmcnt(0)\n\t"
        "s_cmp_eq_u32 %[tu], 5\n\t"
        "s_cselect_b32 s81, 0, 0x7fff0000\n\t"
        "buffer_load_dword v110, v105, %[so], s81 offen\n\t"
        "s_mov_b32 s82, 3\n\t"
        ".Ld7%=:\n\t"
        "s_xor_b32 s80, s82, %[key]\n\t"
        "s_mul_i32 s81, s80, 65536\n\t"
        "s_lshl_b32 s80, s80, 11\n\t"
        "s_add_u32 s80, s80, %[m0b]\n\t"
        "s_add_u32 m0, s80, 24576\n\t"
        "s_add_u32 s81, s81, 786432\n\t"
        "buffer_load_dwordx4 v98, %[so], s81 offen lds\n\t"
        "s_add_u32 m0, m0, 0x400\n\t"
        "s_add_u32 s81, s81, 32768\n\t"
        "buffer_load_dwordx4 v98, %[so], s81 offen lds\n\t"
        "s_sub_u32 s82, s82, 1\n\t"
        "s_cbranch_scc0 .Ld7%=\n\t"
        "s_mov_b64 exec, 0\n\t"
        "s_branch .Lhk0%=\n\t"
        ".Lhr%=:\n\t"
        "s_mov_b64 exec, -1\n\t"
        "s_waitcnt vmcnt(27)\n\t"
        "ds_read_b64_tr_b16 a[0:1], v103 offset:0\n\t"
        "ds_read_b64_tr_b16 a[2:3], v103 offset:128\n\t"
        "ds_read_b64_tr_b16 a[4:5], v103 offset:4096\n\t"
        "ds_read_b64_tr_b16 a[6:7], v103 offset:4224\n\t"
        "ds_read_b64_tr_b16 a[8:9], v104 offset:0\n\t"
        "ds_read_b64_tr_b16 a[10:11], v104 offset:128\n\t"
        "ds_read_b64_tr_b16 a[12:13], v104 offset:4096\n\t"
        "ds_read_b64_tr_b16 a[14:15], v104 offset:4224\n\t"
        "ds_read_b64_tr_b16 a[16:17], v103 offset:512\n\t"
        "ds_read_b64_tr_b16 a[18:19], v103 offset:640\n\t"
        "ds_read_b64_tr_b16 a[20:21], v103 offset:4608\n\t"
        "ds_read_b64_tr_b16 a[22:23], v103 offset:4736\n\t"
        "ds_read_b64_tr_b16 a[24:25], v104 offset:512\n\t"
        "ds_read_b64_tr_b16 a[26:27], v104 offset:640\n\t"
        "ds_read_b64_tr_b16 a[28:29], v104 offset:4608\n\t"
        "ds_read_b64_tr_b16 a[30:31], v104 offset:4736\n\t"
        "s_waitcnt lgkmcnt(0)\n\t"
        "s_cmp_eq_u32 %[tu], 6\n\t"
        "s_cselect_b32 s81, 0, 0x7fff0000\n\t"
        "buffer_load_dword v110, v105, %[so], s81 offen\n\t"
        "s_mov_b32 s82, 3\n\t"
        ".Ld8%=:\n\t"
        "s_xor_b32 s80, s82, %[key]\n\t"
        "s_mul_i32 s81, s80, 65536\n\t"
        "s_lshl_b32 s80, s80, 11\n\t"
        "s_add_u32 s80, s80, %[m0b]\n\t"
        "s_add_u32 m0, s80, 0\n\t"
        "s_add_u32 s81, s81, 1048576\n\t"
        "buffer_load_dwordx4 v98, %[so], s81 offen lds\n\t"
        "s_add_u32 m0, m0, 0x400\n\t"
        "s_add_u32 s81, s81, 32768\n\t"
        "buffer_load_dwordx4 v98, %[so], s81 offen lds\n\t"
        "s_sub_u32 s82, s82, 1\n\t"
        "s_cbranch_scc0 .Ld8%=\n\t"
        "s_waitcnt vmcnt(27)\n\t"
        "ds_read_b64_tr_b16 a[32:33], v103 offset:8192\n\t"
        "ds_read_b64_tr_b16 a[34:35], v103 offset:8320\n\t"
        "ds_read_b64_tr_b16 a[36:37], v103 offset:12288\n\t"
        "ds_read_b64_tr_b16 a[38:39], v103 offset:12416\n\t"
        "ds_read_b64_tr_b16 a[40:41], v104 offset:8192\n\t"
        "ds_read_b64_tr_b16 a[42:43], v104 offset:8320\n\t"
        "ds_read_b64_tr_b16 a[44:45], v104 offset:12288\n\t"
        "ds_read_b64_tr_b16 a[46:47], v104 offset:12416\n\t"
        "ds_read_b64_tr_b16 a[48:49], v103 offset:8704\n\t"
        "ds_read_b64_tr_b16 a[50:51], v103 offset:8832\n\t"
        "ds_read_b64_tr_b16 a[52:53], v103 offset:12800\n\t"
        "ds_read_b64_tr_b16 a[54:55], v103 offset:12928\n\t"
        "ds_read_b64_tr_b16 a[56:57], v104 offset:8704\n\t"
        "ds_read_b64_tr_b16 a[58:59], v104 offset:8832\n\t"
        "ds_read_b64_tr_b16 a[60:61], v104 offset:12800\n\t"
        "ds_read_b64_tr_b16 a[62:63], v104 offset:12928\n\t"
        "s_waitcnt lgkmcnt(0)\n\t"
        "s_cmp_eq_u32 %[tu], 7\n\t"
        "s_cselect_b32 s81, 0, 0x7fff0000\n\t"
        "buffer_load_dword v110, v105, %[so], s81 offen\n\t"
        "s_mov_b32 s82, 3\n\t"
        ".Ld9%=:\n\t"
        "s_xor_b32 s80, s82, %[key]\n\t"
        "s_mul_i32 s81, s80, 65536\n\t"
        "s_lshl_b32 s80, s80, 11\n\t"
        "s_add_u32 s80, s80, %[m0b]\n\t"
        "s_add_u32 m0, s80, 8192\n\t"
        "s_add_u32 s81, s81, 1310720\n\t"
        "buffer_load_dwordx4 v98, %[so], s81 offen lds\n\t"
        "s_add_u32 m0, m0, 0x400\n\t"
        "s_add_u32 s81, s81, 32768\n\t"
        "buffer_load_dwordx4 v98, %[so], s81 offen lds\n\t"
        "s_sub_u32 s82, s82, 1\n\t"
        "s_cbranch_scc0 .Ld9%=\n\t"
        "s_waitcnt vmcnt(27)\n\t"
        "ds_read_b64_tr_b16 a[64:65], v103 offset:16384\n\t"
        "ds_read_b64_tr_b16 a[66:67], v103 offset:16512\n\t"
        "ds_read_b64_tr_b16 a[68:69], v103 offset:20480\n\t"
        "ds_read_b64_tr_b16 a[70:71], v103 offset:20608\n\t"
        "ds_read_b64_tr_b16 a[72:73], v104 offset:16384\n\t"
        "ds_read_b64_tr_b16 a[74:75], v104 offset:16512\n\t"
        "ds_read_b64_tr_b16 a[76:77], v104 offset:20480\n\t"
        "ds_read_b64_tr_b16 a[78:79], v104 offset:20608\n\t"
        "ds_read_b64_tr_b16 a[80:81], v103 offset:16896\n\t"
        "ds_read_b64_tr_b16 a[82:83], v103 offset:17024\n\t"
        "ds_read_b64_tr_b16 a[84:85], v103 offset:20992\n\t"
        "ds_read_b64_tr_b16 a[86:87], v103 offset:21120\n\t"
        "ds_read_b64_tr_b16 a[88:89], v104 offset:16896\n\t"
        "ds_read_b64_tr_b16 a[90:91], v104 offset:17024\n\t"
        "ds_read_b64_tr_b16 a[92:93], v104 offset:20992\n\t"
        "ds_read_b64_tr_b16 a[94:95], v104 offset:21120\n\t"
        "buffer_load_dwordx4 v[0:3], v112, %[wsd], %[cal] offen sc1\n\t"
        "s_waitcnt lgkmcnt(0)\n\t"
        "s_mov_b32 s82, 3\n\t"
        ".Ld10%=:\n\t"
        "s_xor_b32 s80, s82, %[key]\n\t"
        "s_mul_i32 s81, s80, 65536\n\t"
        "s_lshl_b32 s80, s80, 11\n\t"
        "s_add_u32 s80, s80, %[m0b]\n\t"
        "s_add_u32 m0, s80, 16384\n\t"
        "s_add_u32 s81, s81, 1572864\n\t"
        "buffer_load_dwordx4 v98, %[so], s81 offen lds\n\t"
        "s_add_u32 m0, m0, 0x400\n\t"
        "s_add_u32 s81, s81, 32768\n\t"
        "buffer_load_dwordx4 v98, %[so], s81 offen lds\n\t"
        "s_sub_u32 s82, s82, 1\n\t"
        "s_cbranch_scc0 .Ld10%=\n\t"
        "s_waitcnt vmcnt(27)\n\t"
        "ds_read_b64_tr_b16 a[96:97], v103 offset:24576\n\t"
        "ds_read_b64_tr_b16 a[98:99], v103 offset:24704\n\t"
        "ds_read_b64_tr_b16 a[100:101], v103 offset:28672\n\t"
        "ds_read_b64_tr_b16 a[102:103], v103 offset:28800\n\t"
        "ds_read_b64_tr_b16 a[104:105], v104 offset:24576\n\t"
        "ds_read_b64_tr_b16 a[106:107], v104 offset:24704\n\t"
        "ds_read_b64_tr_b16 a[108:109], v104 offset:28672\n\t"
        "ds_read_b64_tr_b16 a[110:111], v104 offset:28800\n\t"
        "ds_read_b64_tr_b16 a[112:113], v103 offset:25088\n\t"
        "ds_read_b64_tr_b16 a[114:115], v103 offset:25216\n\t"
        "ds_read_b64_tr_b16 a[116:117], v103 offset:29184\n\t"
        "ds_read_b64_tr_b16 a[118:119], v103 offset:29312\n\t"
        "ds_read_b64_tr_b16 a[120:121], v104 offset:25088\n\t"
        "ds_read_b64_tr_b16 a[122:123], v104 offset:25216\n\t"
        "ds_read_b64_tr_b16 a[124:125], v104 offset:29184\n\t"
        "ds_read_b64_tr_b16 a[126:127], v104 offset:29312\n\t"
        "s_waitcnt lgkmcnt(0)\n\t"
        "s_mov_b32 s82, 3\n\t"
        ".Ld11%=:\n\t"
        "s_xor_b32 s80, s82, %[key]\n\t"
        "s_mul_i32 s81, s80, 65536\n\t"
        "s_lshl_b32 s80, s80, 11\n\t"
        "s_add_u32 s80, s80, %[m0b]\n\t"
        "s_add_u32 m0, s80, 24576\n\t"
        "s_add_u32 s81, s81, 1835008\n\t"
        "buffer_load_dwordx4 v98, %[so], s81 offen lds\n\t"
        "s_add_u32 m0, m0, 0x400\n\t"
        "s_add_u32 s81, s81, 32768\n\t"
        "buffer_load_dwordx4 v98, %[so], s81 offen lds\n\t"
        "s_sub_u32 s82, s82, 1\n\t"
        "s_cbranch_scc0 .Ld11%=\n\t"
        "s_waitcnt vmcnt(26)\n\t"
        "ds_read_b64_tr_b16 a[128:129], v103 offset:0\n\t"
        "ds_read_b64_tr_b16 a[130:131], v103 offset:128\n\t"
        "ds_read_b64_tr_b16 a[132:133], v103 offset:4096\n\t"
        "ds_read_b64_tr_b16 a[134:135], v103 offset:4224\n\t"
        "ds_read_b64_tr_b16 a[136:137], v104 offset:0\n\t"
        "ds_read_b64_tr_b16 a[138:139], v104 offset:128\n\t"
        "ds_read_b64_tr_b16 a[140:141], v104 offset:4096\n\t"
        "ds_read_b64_tr_b16 a[142:143], v104 offset:4224\n\t"
        "ds_read_b64_tr_b16 a[144:145], v103 offset:512\n\t"
        "ds_read_b64_tr_b16 a[146:147], v103 offset:640\n\t"
        "ds_read_b64_tr_b16 a[148:149], v103 offset:4608\n\t"
        "ds_read_b64_tr_b16 a[150:151], v103 offset:4736\n\t"
        "ds_read_b64_tr_b16 a[152:153], v104 offset:512\n\t"
        "ds_read_b64_tr_b16 a[154:155], v104 offset:640\n\t"
        "ds_read_b64_tr_b16 a[156:157], v104 offset:4608\n\t"
        "ds_read_b64_tr_b16 a[158:159], v104 offset:4736\n\t"
        "s_waitcnt lgkmcnt(0)\n\t"
        "s_waitcnt vmcnt(17)\n\t"
        "ds_read_b64_tr_b16 a[160:161], v103 offset:8192\n\t"
        "ds_read_b64_tr_b16 a[162:163], v103 offset:8320\n\t"
        "ds_read_b64_tr_b16 a[164:165], v103 offset:12288\n\t"
        "ds_read_b64_tr_b16 a[166:167], v103 offset:12416\n\t"
        "ds_read_b64_tr_b16 a[168:169], v104 offset:8192\n\t"
        "ds_read_b64_tr_b16 a[170:171], v104 offset:8320\n\t"
        "ds_read_b64_tr_b16 a[172:173], v104 offset:12288\n\t"
        "ds_read_b64_tr_b16 a[174:175], v104 offset:12416\n\t"
        "ds_read_b64_tr_b16 a[176:177], v103 offset:8704\n\t"
        "ds_read_b64_tr_b16 a[178:179], v103 offset:8832\n\t"
        "ds_read_b64_tr_b16 a[180:181], v103 offset:12800\n\t"
        "ds_read_b64_tr_b16 a[182:183], v103 offset:12928\n\t"
        "ds_read_b64_tr_b16 a[184:185], v104 offset:8704\n\t"
        "ds_read_b64_tr_b16 a[186:187], v104 offset:8832\n\t"
        "ds_read_b64_tr_b16 a[188:189], v104 offset:12800\n\t"
        "ds_read_b64_tr_b16 a[190:191], v104 offset:12928\n\t"
        "s_waitcnt lgkmcnt(0)\n\t"
        "s_waitcnt vmcnt(8)\n\t"
        "ds_read_b64_tr_b16 a[192:193], v103 offset:16384\n\t"
        "ds_read_b64_tr_b16 a[194:195], v103 offset:16512\n\t"
        "ds_read_b64_tr_b16 a[196:197], v103 offset:20480\n\t"
        "ds_read_b64_tr_b16 a[198:199], v103 offset:20608\n\t"
        "ds_read_b64_tr_b16 a[200:201], v104 offset:16384\n\t"
        "ds_read_b64_tr_b16 a[202:203], v104 offset:16512\n\t"
        "ds_read_b64_tr_b16 a[204:205], v104 offset:20480\n\t"
        "ds_read_b64_tr_b16 a[206:207], v104 offset:20608\n\t"
        "ds_read_b64_tr_b16 a[208:209], v103 offset:16896\n\t"
        "ds_read_b64_tr_b16 a[210:211], v103 offset:17024\n\t"
        "ds_read_b64_tr_b16 a[212:213], v103 offset:20992\n\t"
        "ds_read_b64_tr_b16 a[214:215], v103 offset:21120\n\t"
        "ds_read_b64_tr_b16 a[216:217], v104 offset:16896\n\t"
        "ds_read_b64_tr_b16 a[218:219], v104 offset:17024\n\t"
        "ds_read_b64_tr_b16 a[220:221], v104 offset:20992\n\t"
        "ds_read_b64_tr_b16 a[222:223], v104 offset:21120\n\t"
        "s_mov_b32 s97, 0\n\t"
        ".Lg0%=:\n\t"
        "v_cmp_ne_u32_e32 vcc, %[tag], v0\n\t"
        "v_cmp_ne_u32_e64 s[82:83], %[tag], v2\n\t"
        "s_nop 1\n\t"
        "s_or_b64 vcc, vcc, s[82:83]\n\t"
        "s_cbranch_scc0 .Lg1%=\n\t"
        "s_sleep 4\n\t"
        "buffer_load_dwordx4 v[0:3], v112, %[wsd], %[cal] offen sc1\n\t"
        "s_add_u32 s97, s97, 1\n\t"
        "s_cmp_lt_u32 s97, 0x100000\n\t"
        "s_cbranch_scc0 .Lgu%=\n\t"
        "s_waitcnt vmcnt(0)\n\t"
        "s_branch .Lg0%=\n\t"
        ".Lg1%=:\n\t"
        "s_mov_b32 s84, 0x55555555\n\t"
        "s_mov_b32 s85, s84\n\t"
        "v_and_b32_e32 v0, 1, v1\n\t"
        "v_and_b32_e32 v2, 1, v3\n\t"
        "v_cmp_ne_u32_e32 vcc, 0, v0\n\t"
        "v_cmp_ne_u32_e64 s[82:83], 0, v2\n\t"
        "s_nop 0\n\t"
        "s_and_b64 s[82:83], s[82:83], s[84:85]\n\t"
        "s_bcnt1_i32_b64 s87, s[82:83]\n\t"
        "s_and_b64 s[82:83], vcc, s[84:85]\n\t"
        "s_bcnt1_i32_b64 s86, s[82:83]\n\t"
        "s_andn2_b64 s[82:83], vcc, s[84:85]\n\t"
        "s_bcnt1_i32_b64 s88, s[82:83]\n\t"
        "v_and_b32_e32 v0, 2, v1\n\t"
        "v_and_b32_e32 v2, 2, v3\n\t"
        "v_cmp_ne_u32_e32 vcc, 0, v0\n\t"
        "v_cmp_ne_u32_e64 s[82:83], 0, v2\n\t"
        "s_nop 0\n\t"
        "s_and_b64 s[82:83], s[82:83], s[84:85]\n\t"
        "s_bcnt1_i32_b64 s90, s[82:83]\n\t"
        "s_and_b64 s[82:83], vcc, s[84:85]\n\t"
        "s_bcnt1_i32_b64 s89, s[82:83]\n\t"
        "s_andn2_b64 s[82:83], vcc, s[84:85]\n\t"
        "s_bcnt1_i32_b64 s91, s[82:83]\n\t"
        "s_add_u32 s80, s86, s87\n\t"
        "s_add_u32 s80, s80, s88\n\t"
        "s_cmp_ge_u32 s80, 48\n\t"
        "s_cselect_b32 s80, 8, 24\n\t"
        "s_cmp_gt_u32 s86, s80\n\t"
        "s_cselect_b32 s86, %c[NX1], %c[NX0]\n\t"
        "s_cmp_gt_u32 s89, 16\n\t"
        "s_cselect_b32 s81, %c[NY1], %c[NY0]\n\t"
        "s_or_b32 s86, s86, s81\n\t"
        "s_cmp_gt_u32 s87, s80\n\t"
        "s_cselect_b32 s87, %c[NX1], %c[NX0]\n\t"
        "s_cmp_gt_u32 s90, 16\n\t"
        "s_cselect_b32 s81, %c[NY1], %c[NY0]\n\t"
        "s_or_b32 s87, s87, s81\n\t"
        "s_cmp_gt_u32 s88, s80\n\t"
        "s_cselect_b32 s88, %c[NX1], %c[NX0]\n\t"
        "s_cmp_gt_u32 s91, 16\n\t"
        "s_cselect_b32 s81, %c[NY1], %c[NY0]\n\t"
        "s_or_b32 s88, s88, s81\n\t"
        "s_xor_b32 %[nA], %[nA], s86\n\t"
        "s_xor_b32 %[nW], %[nW], s86\n\t"
        "s_xor_b32 %[nC], %[nC], s87\n\t"
        "s_xor_b32 %[nS], %[nS], s88\n\t"
        "s_xor_b32 %[nT], %[nT], s88\n\t"
        "s_xor_b32 %[b0], %[b0], s88\n\t"
        "s_cmp_eq_u32 %[w], 1\n\t"
        "s_cselect_b32 s80, s87, s86\n\t"
        "s_cmp_eq_u32 %[w], 2\n\t"
        "s_cselect_b32 s80, s88, s80\n\t"
        "s_xor_b32 %[pob], %[pob], s80\n\t"
        "s_lshl_b32 s80, %[w], 10\n\t"
        "s_sub_u32 %[nS], %[nS], s80\n\t"
        "s_sub_u32 %[pob], %[pob], s80\n\t"
        "s_mov_b32 s36, 0xf0000000\n\t"
        "s_mov_b32 s38, 0x41a00000\n\t"
        "s_mov_b32 s40, 0\n\t"
        "s_mov_b32 s37, 0\n\t"
        "s_brev_b32 s54, 1\n\t"
        "s_mov_b32 s46, %c[iD1]\n\t"
        "s_mov_b32 s45, %c[iD1]\n\t"
        "s_mov_b32 s47, %c[iD2]\n\t"
        "s_mov_b32 s48, 1\n\t"
        "s_mov_b32 s50, %c[LC]\n\t"
        "s_mov_b32 s51, %c[LA]\n\t"
        "s_mov_b32 s52, %c[E1]\n\t"
        "s_mov_b32 s53, %c[E2]\n\t"
        "s_mov_b32 s43, 0\n\t"
        "s_mov_b32 s61, %[b0]\n\t"
        "s_add_u32 s62, %[b0], 0x40000\n\t"
        "s_mov_b32 s75, 162816\n\t"
        "s_waitcnt lgkmcnt(0)\n\t"
        "s_waitcnt vmcnt(0)\n\t"
        "ds_read_b64_tr_b16 a[224:225], v103 offset:24576\n\t"
        "ds_read_b64_tr_b16 a[226:227], v103 offset:24704\n\t"
        "ds_read_b64_tr_b16 a[228:229], v103 offset:28672\n\t"
        "ds_read_b64_tr_b16 a[230:231], v103 offset:28800\n\t"
        "ds_read_b64_tr_b16 a[232:233], v104 offset:24576\n\t"
        "ds_read_b64_tr_b16 a[234:235], v104 offset:24704\n\t"
        "ds_read_b64_tr_b16 a[236:237], v104 offset:28672\n\t"
        "ds_read_b64_tr_b16 a[238:239], v104 offset:28800\n\t"
        "ds_read_b64_tr_b16 a[240:241], v103 offset:25088\n\t"
        "ds_read_b64_tr_b16 a[242:243], v103 offset:25216\n\t"
        "ds_read_b64_tr_b16 a[244:245], v103 offset:29184\n\t"
        "ds_read_b64_tr_b16 a[246:247], v103 offset:29312\n\t"
        "ds_read_b64_tr_b16 a[248:249], v104 offset:25088\n\t"
        "ds_read_b64_tr_b16 a[250:251], v104 offset:25216\n\t"
        "ds_read_b64_tr_b16 a[252:253], v104 offset:29184\n\t"
        "ds_read_b64_tr_b16 a[254:255], v104 offset:29312\n\t"
        "s_waitcnt lgkmcnt(0)\n\t"
        "rec_v_code:\n\t"
        "s_mov_b32 s88, 0\n\t"
        "s_mov_b32 s89, 0\n\t"
        "s_mov_b32 s90, 0\n\t"
        "s_and_b32 s80, s40, 3\n\t"
        "s_lshl_b32 s80, s80, 17\n\t"
        "s_add_u32 s57, s80, %[nA]\n\t"
        "s_add_u32 s58, s57, 0x1000\n\t"
        "s_add_u32 s59, s57, 0x8000\n\t"
        "s_add_u32 s60, s57, 0x9000\n\t"
        "v_mov_b32_e32 v92, -1\n\t"
        "v_mov_b32_e32 v93, -1\n\t"
        "v_mov_b32_e32 v94, -1\n\t"
        "v_mov_b32_e32 v95, -1\n\t"
        "v_mov_b32_e32 v48, 0\n\t"
        "v_mov_b32_e32 v49, 0\n\t"
        "v_mov_b32_e32 v50, 0\n\t"
        "v_mov_b32_e32 v51, 0\n\t"
        "v_mov_b32_e32 v52, 0\n\t"
        "v_mov_b32_e32 v53, 0\n\t"
        "v_mov_b32_e32 v54, 0\n\t"
        "v_mov_b32_e32 v55, 0\n\t"
        "v_mov_b32_e32 v56, 0\n\t"
        "v_mov_b32_e32 v57, 0\n\t"
        "v_mov_b32_e32 v58, 0\n\t"
        "v_mov_b32_e32 v59, 0\n\t"
        "v_mov_b32_e32 v60, 0\n\t"
        "v_mov_b32_e32 v61, 0\n\t"
        "v_mov_b32_e32 v62, 0\n\t"
        "v_mov_b32_e32 v63, 0\n\t"
        "s_barrier\n\t"
        "s_memrealtime s[76:77]\n\t"
        "s_mov_b32 s79, %[nT]\n\t"
        "s_add_u32 s61, %[b0], 0x40000\n\t"
        "s_mov_b32 s62, %[b0]\n\t"
        "s_sub_u32 s75, s75, 16\n\t"
        "s_waitcnt lgkmcnt(0)\n\t"
        "s_mov_b32 s41, s76\n\t"
        "s_mov_b32 s69, s76\n\t"
        ".p2align 6\n\t"
        ".Lloop%=:\n\t"
        "s_mov_b32 s97, 0\n\t"
        "s_movk_i32 s96, 0x1000\n\t"
        ".Lwa%=:\n\t"
        "s_memrealtime s[98:99]\n\t"
        "s_addk_i32 s96, -1\n\t"
        "s_cmp_lt_i32 s96, 0\n\t"
        "s_cbranch_scc1 .Lta%=\n\t"
        "s_waitcnt lgkmcnt(0)\n\t"
        ".Lva%=:\n\t"
        "s_sub_i32 s98, s98, s69\n\t"
        "s_cmp_lt_i32 s98, %c[iFT]\n\t"
        "s_cbranch_scc1 .Lwa%=\n\t"
        "s_cmp_ge_i32 s98, 0\n\t"
        "s_cbranch_scc1 .Lxa%=\n\t"
        ".Lqa%=:\n\t"
        "s_nop %c[iFN]\n\t"
        "s_addk_i32 s98, 1\n\t"
        "s_cmp_lt_i32 s98, 0\n\t"
        "s_cbranch_scc1 .Lqa%=\n\t"
        ".Lxa%=:\n\t"
        "buffer_load_dwordx4 v[0:3], v112, %[srd], s57 offen sc1\n\t"
        "s_nop 7\n\t"
        "buffer_load_dwordx4 v[4:7], v112, %[srd], s58 offen sc1\n\t"
        "s_nop 7\n\t"
        "buffer_load_dwordx4 v[8:11], v112, %[srd], s59 offen sc1\n\t"
        "s_nop 7\n\t"
        "buffer_load_dwordx4 v[12:15], v112, %[srd], s60 offen sc1\n\t"
        "v_mov_b32_e32 v80, 161792\n\t"
        "ds_read_b64 v[80:81], v80\n\t"
        "s_waitcnt lgkmcnt(0)\n\t"
        "v_readfirstlane_b32 s80, v80\n\t"
        "v_readfirstlane_b32 s81, v81\n\t"
        "s_cmp_eq_u32 s80, s40\n\t"
        "s_cselect_b32 s84, 1, 0\n\t"
        "s_cmp_eq_u32 s81, s40\n\t"
        "s_cselect_b32 s81, 4, 0\n\t"
        "s_or_b32 s84, s84, s81\n\t"
        "v_mov_b32_e32 v106, s76\n\t"
        "v_mov_b32_e32 v107, s84\n\t"
        "buffer_store_dwordx2 v[106:107], v117, %[srd], s79 offen sc1\n\t"
        "s_and_b32 s80, s40, 3\n\t"
        "s_lshl_b32 s80, s80, 17\n\t"
        "s_add_u32 s63, s80, %[nC]\n\t"
        "s_add_u32 s66, s80, %[nS]\n\t"
        "s_add_u32 s81, s80, 0x20000\n\t"
        "s_and_b32 s81, s81, 0x60000\n\t"
        "s_add_u32 s67, s81, %[nW]\n\t"
        "s_add_u32 s79, s81, %[nT]\n\t"
        "s_cmp_eq_u32 %[w], 2\n\t"
        "s_cselect_b32 s81, s80, s81\n\t"
        "s_xor_b32 s81, s81, 0x40000\n\t"
        "s_add_u32 s68, s81, %[pob]\n\t"
        "s_lshl_b32 s72, s40, 17\n\t"
        "s_add_u32 s72, s72, %[kvo]\n\t"
        "s_mov_b32 s80, s61\n\t"
        "s_mov_b32 s61, s62\n\t"
        "s_mov_b32 s62, s80\n\t"
        ".Lgo%=:\n\t"
        "s_waitcnt vmcnt(4)\n\t"
        "v_smfmac_f32_16x16x64_bf16 v[52:55], v[0:3], v[136:143], v113\n\t"
        "v_smfmac_f32_16x16x64_bf16 v[56:59], v[0:3], v[144:151], v113\n\t"
        "v_smfmac_f32_16x16x64_bf16 v[60:63], v[0:3], v[152:159], v113\n\t"
        "v_smfmac_f32_16x16x64_bf16 v[48:51], v[0:3], v[128:135], v113\n\t"
        "s_waitcnt vmcnt(3)\n\t"
        "v_smfmac_f32_16x16x64_bf16 v[52:55], v[4:7], v[168:175], v113\n\t"
        "v_smfmac_f32_16x16x64_bf16 v[56:59], v[4:7], v[176:183], v113\n\t"
        "v_smfmac_f32_16x16x64_bf16 v[60:63], v[4:7], v[184:191], v113\n\t"
        "v_smfmac_f32_16x16x64_bf16 v[48:51], v[4:7], v[160:167], v113\n\t"
        "s_waitcnt vmcnt(2)\n\t"
        "v_smfmac_f32_16x16x64_bf16 v[52:55], v[8:11], v[200:207], v113\n\t"
        "v_smfmac_f32_16x16x64_bf16 v[56:59], v[8:11], v[208:215], v113\n\t"
        "v_smfmac_f32_16x16x64_bf16 v[60:63], v[8:11], v[216:223], v113\n\t"
        "v_smfmac_f32_16x16x64_bf16 v[48:51], v[8:11], v[192:199], v113\n\t"
        "s_waitcnt vmcnt(1)\n\t"
        "v_smfmac_f32_16x16x64_bf16 v[52:55], v[12:15], v[232:239], v113\n\t"
        "s_nop 2\n\t"
        "v_smfmac_f32_16x16x64_bf16 v[56:59], v[12:15], v[240:247], v113\n\t"
        "s_nop 2\n\t"
        "v_smfmac_f32_16x16x64_bf16 v[60:63], v[12:15], v[248:255], v113\n\t"
        "v_smfmac_f32_16x16x64_bf16 v[48:51], v[12:15], v[224:231], v113\n\t"
        "v_add_f32_e32 v98, v52, v54\n\t"
        "ds_write_b32 v123, v98 offset:0\n\t"
        "v_add_f32_e32 v99, v53, v55\n\t"
        "ds_write_b32 v123, v99 offset:4\n\t"
        "v_add_f32_e32 v100, v56, v58\n\t"
        "ds_write_b32 v124, v100 offset:0\n\t"
        "v_add_f32_e32 v101, v57, v59\n\t"
        "ds_write_b32 v124, v101 offset:4\n\t"
        "v_add_f32_e32 v102, v60, v62\n\t"
        "ds_write_b32 v125, v102 offset:0\n\t"
        "v_add_f32_e32 v103, v61, v63\n\t"
        "ds_write_b32 v125, v103 offset:4\n\t"
        "v_pk_add_f32 v[96:97], v[48:49], v[50:51]\n\t"
        "v_cmp_u_f32_e32 vcc, v96, v97\n\t"
        "s_nop 2\n\t"
        "v_mov_b32_e32 v104, v104\n\t"
        "s_cbranch_vccnz .Lfa%=\n\t"
        ".Lra%=:\n\t"
        "v_mov_b32_e32 v104, v104\n\t"
        "s_nop 2\n\t"
        "v_mov_b32_e32 v104, v104\n\t"
        "s_waitcnt lgkmcnt(0)\n\t"
        "s_barrier\n\t"
        "v_mov_b32_e32 v104, v104\n\t"
        "ds_read_b64 v[98:99], v122 offset:512\n\t"
        "v_mov_b32_e32 v104, v104\n\t"
        "ds_read_b64 v[100:101], v122 offset:1024\n\t"
        "v_mov_b32_e32 v104, v104\n\t"
        "ds_read_b64 v[102:103], v122 offset:1536\n\t"
        "v_mov_b32_e32 v104, v104\n\t"
        "s_nop 2\n\t"
        "v_mov_b32_e32 v104, v104\n\t"
        "s_waitcnt lgkmcnt(2)\n\t"
        "v_pk_add_f32 v[96:97], v[96:97], v[98:99]\n\t"
        "s_waitcnt lgkmcnt(0)\n\t"
        "v_pk_add_f32 v[100:101], v[100:101], v[102:103]\n\t"
        "v_pk_add_f32 v[96:97], v[96:97], v[100:101]\n\t"
        "v_cvt_pk_bf16_f32 v80, v96, v97\n\t"
        "s_nop 1\n\t"
        "v_mov_b32_dpp v81, v80 quad_perm:[1,0,3,2] row_mask:0xf bank_mask:0xf\n\t"
        "v_perm_b32 v80, v81, v80, v121\n\t"
        "v_min_u32_e32 v80, -2, v80\n\t"
        "buffer_store_dword v80, v114, %[srd], s61 offen sc1\n\t"
        "s_cmp_eq_u32 s40, 63\n\t"
        "s_cbranch_scc1 .Lend%=\n\t"
        "v_mov_b32_e32 v64, 0\n\t"
        "v_mov_b32_e32 v65, 0\n\t"
        "v_mov_b32_e32 v66, 0\n\t"
        "v_mov_b32_e32 v67, 0\n\t"
        "v_mov_b32_e32 v68, 0\n\t"
        "v_mov_b32_e32 v69, 0\n\t"
        "v_mov_b32_e32 v70, 0\n\t"
        "v_mov_b32_e32 v71, 0\n\t"
        "v_mov_b32_e32 v72, 0\n\t"
        "v_mov_b32_e32 v73, 0\n\t"
        "v_mov_b32_e32 v74, 0\n\t"
        "v_mov_b32_e32 v75, 0\n\t"
        "v_mov_b32_e32 v76, 0\n\t"
        "v_mov_b32_e32 v77, 0\n\t"
        "v_mov_b32_e32 v78, 0\n\t"
        "v_mov_b32_e32 v79, 0\n\t"
        "s_xor_b32 s64, s63, %c[iCS1]\n\t"
        "s_xor_b32 s65, s63, %c[iCS2]\n\t"
        "s_xor_b32 s73, s64, %c[iCS2]\n\t"
        "s_sleep %c[VSLP]\n\t"
        "buffer_load_dwordx4 v[88:91], v112, %[srd], s66 offen sc1\n\t"
        "s_cmp_eq_u32 %[w], 0\n\t"
        "s_cselect_b32 exec_hi, 0, -1\n\t"
        "buffer_store_dwordx4 v[92:95], v112, %[srd], s68 offen sc1\n\t"
        "s_mov_b32 exec_hi, -1\n\t"
        "s_waitcnt lgkmcnt(0)\n\t"
        "v_mov_b32_e32 v48, 0\n\t"
        "v_mov_b32_e32 v49, 0\n\t"
        "v_mov_b32_e32 v50, 0\n\t"
        "v_mov_b32_e32 v51, 0\n\t"
        "v_mov_b32_e32 v52, 0\n\t"
        "v_mov_b32_e32 v53, 0\n\t"
        "v_mov_b32_e32 v54, 0\n\t"
        "v_mov_b32_e32 v55, 0\n\t"
        "v_mov_b32_e32 v56, 0\n\t"
        "v_mov_b32_e32 v57, 0\n\t"
        "v_mov_b32_e32 v58, 0\n\t"
        "v_mov_b32_e32 v59, 0\n\t"
        "v_mov_b32_e32 v60, 0\n\t"
        "v_mov_b32_e32 v61, 0\n\t"
        "v_mov_b32_e32 v62, 0\n\t"
        "v_mov_b32_e32 v63, 0\n\t"
        "s_add_u32 s81, s40, 1\n\t"
        "s_and_b32 s80, s81, 3\n\t"
        "s_lshl_b32 s80, s80, 17\n\t"
        "s_add_u32 s57, s80, %[nA]\n\t"
        "s_add_u32 s58, s57, 0x1000\n\t"
        "s_add_u32 s59, s57, 0x8000\n\t"
        "s_add_u32 s60, s57, 0x9000\n\t"
        "s_mov_b32 s80, s88\n\t"
        "s_add_i32 s81, s92, s50\n\t"
        "s_sub_i32 s81, s81, %c[iBM]\n\t"
        "s_cmp_le_i32 s81, s44\n\t"
        "s_cselect_b32 s80, s80, 0\n\t"
        "s_cmp_lt_i32 s50, %c[LC24]\n\t"
        "s_cselect_b32 s80, s80, 0\n\t"
        "s_mul_i32 s80, s80, %c[iBS]\n\t"
        "s_add_u32 s50, s80, s50\n\t"
        "s_sub_i32 s50, s50, s89\n\t"
        "s_max_i32 s50, s50, %c[LC]\n\t"
        "s_lshl_b32 s80, s92, 1\n\t"
        "s_sub_i32 s81, s80, s52\n\t"
        "s_ashr_i32 s82, s81, %c[UPSH]\n\t"
        "s_max_i32 s82, s82, %c[TRUP]\n\t"
        "s_add_i32 s82, s82, s52\n\t"
        "s_min_i32 s82, s82, s80\n\t"
        "s_ashr_i32 s83, s81, %c[DNSH]\n\t"
        "s_min_i32 s83, s83, -%c[TRDN]\n\t"
        "s_add_i32 s83, s83, s52\n\t"
        "s_max_i32 s83, s83, s80\n\t"
        "s_cmp_gt_i32 s81, 0\n\t"
        "s_cselect_b32 s82, s82, s83\n\t"
        "s_cmp_lg_u32 s90, 0\n\t"
        "s_cselect_b32 s52, s82, s52\n\t"
        "s_add_i32 s80, s52, 1\n\t"
        "s_ashr_i32 s80, s80, 1\n\t"
        "s_add_i32 s45, s80, s50\n\t"
        "s_waitcnt vmcnt(1)\n\t"
        "s_mov_b32 s97, 0\n\t"
        ".Lsr%=:\n\t"
        "s_mov_b32 exec_hi, 0xffff\n\t"
        "v_subrev_u32_e32 v4, s41, v88\n\t"
        "v_subrev_u32_e32 v5, s41, v90\n\t"
        "v_max_i32_e32 v4, v4, v5\n\t"
        "v_max_u32_e32 v5, v89, v91\n\t"
        "v_cmp_eq_u32_e32 vcc, -1, v5\n\t"
        "v_max_i32_dpp v4, v4, v4 quad_perm:[1,0,3,2] row_mask:0xf bank_mask:0xf\n\t"
        "v_or_b32_e32 v6, v89, v91\n\t"
        "s_nop 0\n\t"
        "v_max_i32_dpp v4, v4, v4 quad_perm:[2,3,0,1] row_mask:0xf bank_mask:0xf\n\t"
        "s_nop 1\n\t"
        "v_max_i32_dpp v4, v4, v4 row_ror:4 row_mask:0xf bank_mask:0xf\n\t"
        "s_nop 1\n\t"
        "v_max_i32_dpp v4, v4, v4 row_ror:8 row_mask:0xf bank_mask:0xf\n\t"
        "s_cbranch_vccnz .Lsf%=\n\t"
        "v_readlane_b32 s85, v4, 0\n\t"
        "v_readlane_b32 s81, v4, 16\n\t"
        "v_readlane_b32 s56, v4, 32\n\t"
        "v_and_b32_e32 v5, 1, v6\n\t"
        "v_cmp_ne_u32_e64 s[86:87], 0, v5\n\t"
        "v_and_b32_e32 v5, 2, v6\n\t"
        "v_cmp_ne_u32_e64 s[88:89], 0, v5\n\t"
        "v_and_b32_e32 v5, 4, v6\n\t"
        "v_cmp_ne_u32_e64 s[90:91], 0, v5\n\t"
        "s_max_i32 s56, s56, s81\n\t"
        "s_add_u32 s92, s85, s41\n\t"
        "s_sub_u32 s92, s92, s43\n\t"
        "s_add_i32 s93, s56, s47\n\t"
        "s_mov_b32 s44, s46\n\t"
        "s_mov_b32 s46, s45\n\t"
        "s_mov_b32 s94, s48\n\t"
        "s_mov_b32 s43, s41\n\t"
        "s_cmp_gt_i32 s56, s54\n\t"
        "s_cbranch_scc1 .Lmv%=\n\t"
        "s_mov_b32 s37, 0\n\t"
        "s_mov_b32 s48, 0\n\t"
        "s_mov_b32 s95, 0\n\t"
        ".Lsd%=:\n\t"
        "s_mov_b32 exec_hi, -1\n\t"
        "s_add_u32 s42, s43, s46\n\t"
        "s_mov_b32 s97, 0\n\t"
        "s_movk_i32 s96, 0x1000\n\t"
        ".Lwc%=:\n\t"
        "s_memrealtime s[98:99]\n\t"
        "s_addk_i32 s96, -1\n\t"
        "s_cmp_lt_i32 s96, 0\n\t"
        "s_cbranch_scc1 .Ltc%=\n\t"
        "s_waitcnt lgkmcnt(0)\n\t"
        "s_sub_i32 s98, s98, s42\n\t"
        "s_cmp_lt_i32 s98, %c[iFT]\n\t"
        "s_cbranch_scc1 .Lwc%=\n\t"
        "s_cmp_ge_i32 s98, 0\n\t"
        "s_cbranch_scc1 .Lxc%=\n\t"
        ".Lqc%=:\n\t"
        "s_nop %c[iFN]\n\t"
        "s_addk_i32 s98, 1\n\t"
        "s_cmp_lt_i32 s98, 0\n\t"
        "s_cbranch_scc1 .Lqc%=\n\t"
        ".Lxc%=:\n\t"
        "buffer_load_dwordx4 v[16:19], v112, %[srd], s63 offen sc1\n\t"
        "s_cmp_ge_u32 s40, %c[iCT0]\n\t"
        "s_cselect_b32 s85, 1, 0\n\t"
        "s_cmp_lg_u64 s[86:87], 0\n\t"
        "s_cselect_b32 s86, s85, 0\n\t"
        "s_cmp_lg_u64 s[88:89], 0\n\t"
        "s_cselect_b32 s87, s85, 0\n\t"
        "s_cmp_lg_u64 s[90:91], 0\n\t"
        "s_cselect_b32 s88, s85, 0\n\t"
        "s_or_b32 s80, s94, s86\n\t"
        "s_or_b32 s80, s80, s87\n\t"
        "buffer_load_dwordx4 v[20:23], v115, %[srd], s63 offen sc1\n\t"
        "s_andn2_b32 s90, s85, s80\n\t"
        "s_cmp_le_i32 s92, s44\n\t"
        "s_cselect_b32 s80, s85, 0\n\t"
        "s_andn2_b32 s91, s80, s88\n\t"
        "s_and_b32 s80, s40, %c[LDEC]\n\t"
        "s_cselect_b32 s89, 0, 1\n\t"
        "s_andn2_b32 s80, s86, s94\n\t"
        "s_cmp_lt_i32 s51, %c[LA24]\n\t"
        "s_cselect_b32 s80, s80, 0\n\t"
        "s_mul_i32 s80, s80, %c[iBS]\n\t"
        "buffer_load_dwordx4 v[24:27], v112, %[srd], s64 offen sc1\n\t"
        "s_add_u32 s51, s80, s51\n\t"
        "s_sub_i32 s51, s51, s89\n\t"
        "s_max_i32 s51, s51, %c[LA]\n\t"
        "s_lshl_b32 s80, s93, 1\n\t"
        "s_sub_i32 s81, s80, s53\n\t"
        "s_ashr_i32 s82, s81, %c[UPSH]\n\t"
        "s_max_i32 s82, s82, %c[TRUP]\n\t"
        "s_add_i32 s82, s82, s53\n\t"
        "s_min_i32 s82, s82, s80\n\t"
        "s_ashr_i32 s83, s81, %c[DNSH]\n\t"
        "buffer_load_dwordx4 v[28:31], v115, %[srd], s64 offen sc1\n\t"
        "s_min_i32 s83, s83, -%c[TRDN]\n\t"
        "s_add_i32 s83, s83, s53\n\t"
        "s_max_i32 s83, s83, s80\n\t"
        "s_cmp_gt_i32 s81, 0\n\t"
        "s_cselect_b32 s82, s82, s83\n\t"
        "s_cmp_lg_u32 s91, 0\n\t"
        "s_cselect_b32 s53, s82, s53\n\t"
        "s_add_i32 s80, s53, 1\n\t"
        "s_ashr_i32 s80, s80, 1\n\t"
        "s_add_i32 s47, s80, s51\n\t"
        "buffer_load_dwordx4 v[32:35], v112, %[srd], s65 offen sc1\n\t"
        "s_add_u32 s81, s47, %c[AUP]\n\t"
        "s_min_i32 s80, s95, s81\n\t"
        "s_max_i32 s47, s47, s80\n\t"
        "s_add_u32 s41, s42, s47\n\t"
        "s_cmp_eq_u32 s40, 0\n\t"
        "s_cselect_b32 s51, %c[LA], s51\n\t"
        "s_nop 0\n\t"
        "s_nop 0\n\t"
        "s_nop 0\n\t"
        "s_nop 0\n\t"
        "buffer_load_dwordx4 v[36:39], v115, %[srd], s65 offen sc1\n\t"
        "s_nop 0\n\t"
        "s_nop 0\n\t"
        "s_nop 0\n\t"
        "s_nop 0\n\t"
        "s_nop 0\n\t"
        "s_nop 0\n\t"
        "s_nop 0\n\t"
        "s_nop 0\n\t"
        "s_nop 0\n\t"
        "s_nop 0\n\t"
        "buffer_load_dwordx4 v[40:43], v112, %[srd], s73 offen sc1\n\t"
        "s_nop 0\n\t"
        "s_nop 0\n\t"
        "s_nop 0\n\t"
        "s_nop 0\n\t"
        "s_nop 0\n\t"
        "s_nop 0\n\t"
        "s_nop 0\n\t"
        "s_nop 0\n\t"
        "s_nop 0\n\t"
        "s_nop 0\n\t"
        "buffer_load_dwordx4 v[44:47], v115, %[srd], s73 offen sc1\n\t"
        ".p2align 6\n\t"
        ".Lrc%=:\n\t"
        "s_waitcnt vmcnt(7)\n\t"
        "v_smfmac_f32_16x16x64_bf16 v[64:67], v[16:19], a[0:7], v113\n\t"
        "v_smfmac_f32_16x16x64_bf16 v[68:71], v[16:19], a[8:15], v113\n\t"
        "v_smfmac_f32_16x16x64_bf16 v[72:75], v[16:19], a[16:23], v113\n\t"
        "v_smfmac_f32_16x16x64_bf16 v[76:79], v[16:19], a[24:31], v113\n\t"
        "s_waitcnt vmcnt(6)\n\t"
        "v_smfmac_f32_16x16x64_bf16 v[64:67], v[20:23], a[32:39], v113\n\t"
        "v_smfmac_f32_16x16x64_bf16 v[68:71], v[20:23], a[40:47], v113\n\t"
        "v_smfmac_f32_16x16x64_bf16 v[72:75], v[20:23], a[48:55], v113\n\t"
        "v_smfmac_f32_16x16x64_bf16 v[76:79], v[20:23], a[56:63], v113\n\t"
        "s_waitcnt vmcnt(5)\n\t"
        "v_smfmac_f32_16x16x64_bf16 v[64:67], v[24:27], a[64:71], v113\n\t"
        "v_smfmac_f32_16x16x64_bf16 v[68:71], v[24:27], a[72:79], v113\n\t"
        "v_smfmac_f32_16x16x64_bf16 v[72:75], v[24:27], a[80:87], v113\n\t"
        "v_smfmac_f32_16x16x64_bf16 v[76:79], v[24:27], a[88:95], v113\n\t"
        "s_waitcnt vmcnt(4)\n\t"
        "v_smfmac_f32_16x16x64_bf16 v[64:67], v[28:31], a[96:103], v113\n\t"
        "v_smfmac_f32_16x16x64_bf16 v[68:71], v[28:31], a[104:111], v113\n\t"
        "v_smfmac_f32_16x16x64_bf16 v[72:75], v[28:31], a[112:119], v113\n\t"
        "v_smfmac_f32_16x16x64_bf16 v[76:79], v[28:31], a[120:127], v113\n\t"
        "s_waitcnt vmcnt(3)\n\t"
        "v_smfmac_f32_16x16x64_bf16 v[64:67], v[32:35], a[128:135], v113\n\t"
        "v_smfmac_f32_16x16x64_bf16 v[68:71], v[32:35], a[136:143], v113\n\t"
        "v_smfmac_f32_16x16x64_bf16 v[72:75], v[32:35], a[144:151], v113\n\t"
        "v_smfmac_f32_16x16x64_bf16 v[76:79], v[32:35], a[152:159], v113\n\t"
        "s_waitcnt vmcnt(2)\n\t"
        "v_smfmac_f32_16x16x64_bf16 v[64:67], v[36:39], a[160:167], v113\n\t"
        "v_smfmac_f32_16x16x64_bf16 v[68:71], v[36:39], a[168:175], v113\n\t"
        "v_smfmac_f32_16x16x64_bf16 v[72:75], v[36:39], a[176:183], v113\n\t"
        "v_smfmac_f32_16x16x64_bf16 v[76:79], v[36:39], a[184:191], v113\n\t"
        "s_waitcnt vmcnt(1)\n\t"
        "v_smfmac_f32_16x16x64_bf16 v[64:67], v[40:43], a[192:199], v113\n\t"
        "v_smfmac_f32_16x16x64_bf16 v[68:71], v[40:43], a[200:207], v113\n\t"
        "v_smfmac_f32_16x16x64_bf16 v[72:75], v[40:43], a[208:215], v113\n\t"
        "v_smfmac_f32_16x16x64_bf16 v[76:79], v[40:43], a[216:223], v113\n\t"
        "s_waitcnt vmcnt(0)\n\t"
        "v_smfmac_f32_16x16x64_bf16 v[64:67], v[44:47], a[224:231], v113\n\t"
        "s_nop 2\n\t"
        "v_smfmac_f32_16x16x64_bf16 v[68:71], v[44:47], a[232:239], v113\n\t"
        "s_cmp_gt_u32 %[w], 2\n\t"
        "s_nop 1\n\t"
        "v_smfmac_f32_16x16x64_bf16 v[72:75], v[44:47], a[240:247], v113\n\t"
        "v_add_f32_e32 v80, v64, v66\n\t"
        "s_nop 0\n\t"
        "v_smfmac_f32_16x16x64_bf16 v[76:79], v[44:47], a[248:255], v113\n\t"
        "v_add_f32_e32 v81, v65, v67\n\t"
        "v_add_f32_e32 v82, v68, v70\n\t"
        "v_add_f32_e32 v83, v69, v71\n\t"
        "s_mov_b32 exec_lo, 0\n\t"
        "s_nop 0\n\t"
        "v_add_f32_e32 v80, v72, v74\n\t"
        "v_add_f32_e32 v81, v73, v75\n\t"
        "s_nop 0\n\t"
        "v_pk_add_f32 v[82:83], v[76:77], v[78:79]\n\t"
        "s_mov_b32 exec_lo, -1\n\t"
        "s_nop 0\n\t"
        "v_permlane32_swap_b32_e32 v80, v82\n\t"
        "v_add_f32_e32 v80, v80, v82\n\t"
        "ds_write_b32 v119, v80\n\t"
        "v_permlane32_swap_b32_e32 v81, v83\n\t"
        "v_add_f32_e32 v81, v81, v83\n\t"
        "ds_write_b32 v119, v81 offset:128\n\t"
        "v_cmp_u_f32_e32 vcc, v80, v81\n\t"
        "s_nop 2\n\t"
        "v_mov_b32_e32 v104, v104\n\t"
        "s_cbranch_vccnz .Lfc%=\n\t"
        ".Lrd%=:\n\t"
        "v_mov_b32_e32 v104, v104\n\t"
        "s_cbranch_scc1 .Lno%=\n\t"
        "v_mov_b32_e32 v104, v104\n\t"
        "s_nop 2\n\t"
        "v_mov_b32_e32 v104, v104\n\t"
        "s_waitcnt lgkmcnt(0)\n\t"
        "s_barrier\n\t"
        "v_mov_b32_e32 v104, v104\n\t"
        "ds_read_b64 v[0:1], v120 offset:8192\n\t"
        "ds_read_b64 v[2:3], v120 offset:8704\n\t"
        "ds_read_b64 v[4:5], v120 offset:9216\n\t"
        "ds_read_b64 v[6:7], v120 offset:9728\n\t"
        "v_mov_b32_e32 v104, v104\n\t"
        "s_nop 2\n\t"
        "v_mov_b32_e32 v104, v104\n\t"
        "s_nop 2\n\t"
        "v_mov_b32_e32 v104, v104\n\t"
        "s_waitcnt lgkmcnt(2)\n\t"
        "v_pk_add_f32 v[0:1], v[0:1], v[2:3]\n\t"
        "s_waitcnt lgkmcnt(0)\n\t"
        "v_pk_add_f32 v[4:5], v[4:5], v[6:7]\n\t"
        "v_pk_add_f32 v[0:1], v[0:1], v[4:5]\n\t"
        "v_cvt_pk_bf16_f32 v0, v0, v1\n\t"
        "v_min_u32_e32 v0, -2, v0\n\t"
        "buffer_store_dword v0, v116, %[srd], s67 offen sc1\n\t"
        ".Lnp%=:\n\t"
        "s_memrealtime s[76:77]\n\t"
        "s_add_u32 s40, s40, 1\n\t"
        "s_waitcnt lgkmcnt(0)\n\t"
        "s_add_u32 s81, s76, %c[iVD]\n\t"
        "s_sub_u32 s80, s76, s41\n\t"
        "s_cmp_gt_i32 s80, %c[iTOL]\n\t"
        "s_cselect_b32 s69, s81, s41\n\t"
        "s_mov_b32 s97, 0\n\t"
        "s_movk_i32 s96, 0x1000\n\t"
        "s_mov_b32 s98, s76\n\t"
        "s_branch .Lva%=\n\t"
        ".Lmv%=:\n\t"
        ".Lhk0%=:\n\t"
        "s_cbranch_execz .Lhk1%=\n\t"
        "s_mov_b32 s48, 1\n\t"
        "s_cmp_eq_u32 s54, %c[iTOL]\n\t"
        "s_cbranch_scc1 .Lm1%=\n\t"
        "s_max_i32 s80, s85, s56\n\t"
        "s_add_u32 s43, s41, s80\n\t"
        "s_add_u32 s43, s43, %c[iPEN0]\n\t"
        ".Lhk1%=:\n\t"
        "s_cbranch_execz .Lhk2%=\n\t"
        "s_mov_b32 s54, %c[iTOL]\n\t"
        "s_add_u32 s46, s46, %c[iX0C]\n\t"
        "s_mov_b32 s95, 0\n\t"
        "s_add_u32 s51, s51, %c[iX0A]\n\t"
        "v_and_b32_e32 v4, 3, v88\n\t"
        "v_and_b32_e32 v5, 3, v90\n\t"
        ".Lhk2%=:\n\t"
        "s_cbranch_execz .Lhk3%=\n\t"
        "v_lshlrev_b32_e64 v4, v4, 1\n\t"
        "v_lshlrev_b32_e64 v5, v5, 1\n\t"
        "v_or_b32_e32 v4, v4, v5\n\t"
        "s_mov_b32 s80, 0\n\t"
        "v_and_b32_e32 v5, 1, v4\n\t"
        "v_cmp_ne_u32_e32 vcc, 0, v5\n\t"
        ".Lhk3%=:\n\t"
        "s_cbranch_execz .Lhk4%=\n\t"
        "s_nop 1\n\t"
        "s_cmp_lg_u64 vcc, 0\n\t"
        "s_cselect_b32 s81, 2, 0\n\t"
        "s_or_b32 s80, s80, s81\n\t"
        "v_and_b32_e32 v5, 2, v4\n\t"
        "v_cmp_ne_u32_e32 vcc, 0, v5\n\t"
        ".Lhk4%=:\n\t"
        "s_cbranch_execz .Lhk5%=\n\t"
        "s_nop 1\n\t"
        "s_cmp_lg_u64 vcc, 0\n\t"
        "s_cselect_b32 s81, 4, 0\n\t"
        "s_or_b32 s80, s80, s81\n\t"
        "v_and_b32_e32 v5, 4, v4\n\t"
        "v_cmp_ne_u32_e32 vcc, 0, v5\n\t"
        ".Lhk5%=:\n\t"
        "s_cbranch_execz .Lhk6%=\n\t"
        "s_nop 1\n\t"
        "s_cmp_lg_u64 vcc, 0\n\t"
        "s_cselect_b32 s81, 8, 0\n\t"
        "s_or_b32 s80, s80, s81\n\t"
        "v_and_b32_e32 v5, 8, v4\n\t"
        "v_cmp_ne_u32_e32 vcc, 0, v5\n\t"
        ".Lhk6%=:\n\t"
        "s_cbranch_execz .Lhk7%=\n\t"
        "s_nop 1\n\t"
        "s_cmp_lg_u64 vcc, 0\n\t"
        "s_cselect_b32 s81, 16, 0\n\t"
        "s_or_b32 s80, s80, s81\n\t"
        "s_lshr_b32 s80, 0x1adf1210, s80\n\t"
        "s_and_b32 s55, s80, 3\n\t"
        ".Lhk7%=:\n\t"
        "s_cbranch_execz .Lhk8%=\n\t"
        "s_branch .Lal%=\n\t"
        ".Lm1%=:\n\t"
        "s_add_u32 s80, s37, %c[iPEN2]\n\t"
        "s_min_i32 s80, s80, %c[iPENM]\n\t"
        "s_cmp_lg_u32 s37, 0\n\t"
        "s_cselect_b32 s37, s80, %c[iPEN]\n\t"
        "s_add_u32 s80, s56, s37\n\t"
        ".Lhk8%=:\n\t"
        "s_cbranch_execz .Lhk9%=\n\t"
        "s_max_i32 s80, s80, %c[iPMIN]\n\t"
        "s_add_u32 s43, s41, s80\n\t"
        "s_add_u32 s95, s93, %c[LA]\n\t"
        "s_add_u32 s46, s46, %c[iXR]\n\t"
        ".Lal%=:\n\t"
        "s_sub_u32 s80, s55, s43\n\t"
        "s_and_b32 s80, s80, %c[iPHM]\n\t"
        ".Lhk9%=:\n\t"
        "s_cbranch_execz .Lhk10%=\n\t"
        "s_add_u32 s43, s43, s80\n\t"
        "s_branch .Lsd%=\n\t"
        ".Lsf%=:\n\t"
        "s_mov_b32 exec_hi, -1\n\t"
        "s_sleep 2\n\t"
        "buffer_load_dwordx4 v[88:91], v112, %[srd], s66 offen sc1\n\t"
        "s_add_u32 s97, s97, 1\n\t"
        ".Lhk10%=:\n\t"
        "s_cbranch_execz .Lhk11%=\n\t"
        "s_cmp_lt_u32 s97, 0x100000\n\t"
        "s_cbranch_scc0 .Lgu%=\n\t"
        "s_waitcnt vmcnt(0)\n\t"
        "s_branch .Lsr%=\n\t"
        ".Lfa%=:\n\t"
        "v_max_u32_e32 v84, v0, v2\n\t"
        "v_max3_u32 v84, v84, v1, v3\n\t"
        ".Lhk11%=:\n\t"
        "s_cbranch_execz .Lhk12%=\n\t"
        "v_max3_u32 v84, v84, v4, v6\n\t"
        "v_max3_u32 v84, v84, v5, v7\n\t"
        "v_max3_u32 v84, v84, v8, v10\n\t"
        "v_max3_u32 v84, v84, v9, v11\n\t"
        "v_max3_u32 v84, v84, v12, v14\n\t"
        "v_max3_u32 v84, v84, v13, v15\n\t"
        ".Lhk12%=:\n\t"
        "s_cbranch_execz .Lhk13%=\n\t"
        "v_cmp_eq_u32_e32 vcc, -1, v84\n\t"
        "s_nop 1\n\t"
        "v_mov_b32_e32 v104, v104\n\t"
        "s_nop 0\n\t"
        "s_cbranch_vccz .Lra%=\n\t"
        "s_add_u32 s80, s40, 1\n\t"
        ".Lhk13%=:\n\t"
        "s_cbranch_execz .Lhk14%=\n\t"
        "v_mov_b32_e32 v80, 161792\n\t"
        "v_mov_b32_e32 v81, s80\n\t"
        "ds_write_b32 v80, v81\n\t"
        "v_mov_b32_e32 v48, 0\n\t"
        "v_mov_b32_e32 v49, 0\n\t"
        "v_mov_b32_e32 v50, 0\n\t"
        ".Lhk14%=:\n\t"
        "s_cbranch_execz .Lhk15%=\n\t"
        "v_mov_b32_e32 v51, 0\n\t"
        "v_mov_b32_e32 v52, 0\n\t"
        "v_mov_b32_e32 v53, 0\n\t"
        "v_mov_b32_e32 v54, 0\n\t"
        "v_mov_b32_e32 v55, 0\n\t"
        "v_mov_b32_e32 v56, 0\n\t"
        ".Lhk15%=:\n\t"
        "s_cbranch_execz .Lhk16%=\n\t"
        "v_mov_b32_e32 v57, 0\n\t"
        "v_mov_b32_e32 v58, 0\n\t"
        "v_mov_b32_e32 v59, 0\n\t"
        "v_mov_b32_e32 v60, 0\n\t"
        "v_mov_b32_e32 v61, 0\n\t"
        "v_mov_b32_e32 v62, 0\n\t"
        ".Lhk16%=:\n\t"
        "s_cbranch_execz .Lhk17%=\n\t"
        "v_mov_b32_e32 v63, 0\n\t"
        "s_sleep %c[VASL]\n\t"
        "buffer_load_dwordx4 v[0:3], v112, %[srd], s57 offen sc1\n\t"
        "buffer_load_dwordx4 v[4:7], v112, %[srd], s58 offen sc1\n\t"
        "buffer_load_dwordx4 v[8:11], v112, %[srd], s59 offen sc1\n\t"
        "buffer_load_dwordx4 v[12:15], v112, %[srd], s60 offen sc1\n\t"
        ".Lhk17%=:\n\t"
        "s_cbranch_execz .Lhk18%=\n\t"
        "buffer_store_dword v0, off, %[wsd], s36\n\t"
        "s_add_u32 s97, s97, 1\n\t"
        "s_cmp_lt_u32 s97, 0x100000\n\t"
        "s_cbranch_scc0 .Lgu%=\n\t"
        "s_branch .Lgo%=\n\t"
        ".Lfc%=:\n\t"
        "v_max_u32_e32 v84, v16, v18\n\t"
        ".Lhk18%=:\n\t"
        "s_cbranch_execz .Lhk19%=\n\t"
        "v_max3_u32 v84, v84, v20, v22\n\t"
        "v_max3_u32 v84, v84, v24, v26\n\t"
        "v_max3_u32 v84, v84, v28, v30\n\t"
        "v_max3_u32 v84, v84, v32, v34\n\t"
        "v_max3_u32 v84, v84, v36, v38\n\t"
        "v_max3_u32 v84, v84, v40, v42\n\t"
        ".Lhk19%=:\n\t"
        "s_cbranch_execz .Lhk20%=\n\t"
        "v_max3_u32 v84, v84, v44, v46\n\t"
        "v_cmp_eq_u32_e32 vcc, -1, v84\n\t"
        "s_nop 1\n\t"
        "v_mov_b32_e32 v104, v104\n\t"
        "s_nop 0\n\t"
        "s_cbranch_vccz .Lrd%=\n\t"
        ".Lhk20%=:\n\t"
        "s_cbranch_execz .Lhk21%=\n\t"
        "s_sleep %c[CSLP]\n\t"
        "buffer_load_dwordx4 v[16:19], v112, %[srd], s63 offen sc1\n\t"
        "v_mov_b32_e32 v64, 0\n\t"
        "v_mov_b32_e32 v65, 0\n\t"
        "buffer_load_dwordx4 v[20:23], v115, %[srd], s63 offen sc1\n\t"
        "v_mov_b32_e32 v66, 0\n\t"
        ".Lhk21%=:\n\t"
        "s_cbranch_execz .Lhk22%=\n\t"
        "v_mov_b32_e32 v67, 0\n\t"
        "buffer_load_dwordx4 v[24:27], v112, %[srd], s64 offen sc1\n\t"
        "v_mov_b32_e32 v68, 0\n\t"
        "v_mov_b32_e32 v69, 0\n\t"
        "buffer_load_dwordx4 v[28:31], v115, %[srd], s64 offen sc1\n\t"
        "v_mov_b32_e32 v70, 0\n\t"
        ".Lhk22%=:\n\t"
        "s_cbranch_execz .Lhk23%=\n\t"
        "v_mov_b32_e32 v71, 0\n\t"
        "buffer_load_dwordx4 v[32:35], v112, %[srd], s65 offen sc1\n\t"
        "v_mov_b32_e32 v72, 0\n\t"
        "v_mov_b32_e32 v73, 0\n\t"
        "buffer_load_dwordx4 v[36:39], v115, %[srd], s65 offen sc1\n\t"
        "v_mov_b32_e32 v74, 0\n\t"
        ".Lhk23%=:\n\t"
        "s_cbranch_execz .Lhk24%=\n\t"
        "v_mov_b32_e32 v75, 0\n\t"
        "buffer_load_dwordx4 v[40:43], v112, %[srd], s73 offen sc1\n\t"
        "v_mov_b32_e32 v76, 0\n\t"
        "v_mov_b32_e32 v77, 0\n\t"
        "buffer_load_dwordx4 v[44:47], v115, %[srd], s73 offen sc1\n\t"
        "v_mov_b32_e32 v78, 0\n\t"
        ".Lhk24%=:\n\t"
        "s_cbranch_execz .Lhk25%=\n\t"
        "v_mov_b32_e32 v79, 0\n\t"
        "s_add_u32 s80, s40, 1\n\t"
        "v_mov_b32_e32 v80, 161796\n\t"
        "v_mov_b32_e32 v81, s80\n\t"
        "ds_write_b32 v80, v81\n\t"
        "s_add_u32 s97, s97, 1\n\t"
        ".Lhk25%=:\n\t"
        "s_cbranch_execz .Lhk26%=\n\t"
        "s_cmp_lt_u32 s97, 0x100000\n\t"
        "s_cbranch_scc0 .Lgu%=\n\t"
        "s_branch .Lrc%=\n\t"
        ".Lno%=:\n\t"
        "s_waitcnt lgkmcnt(0)\n\t"
        "s_barrier\n\t"
        "s_branch .Lnp%=\n\t"
        ".Lta%=:\n\t"
        ".Lhk26%=:\n\t"
        "s_cbranch_execz .Lhk27%=\n\t"
        "s_waitcnt lgkmcnt(0)\n\t"
        "s_branch .Lxa%=\n\t"
        ".Ltc%=:\n\t"
        "s_waitcnt lgkmcnt(0)\n\t"
        "s_branch .Lxc%=\n\t"
        ".Lgu%=:\n\t"
        "s_mov_b64 exec, -1\n\t"
        "v_mov_b32_e32 v80, %c[ferr]\n\t"
        ".Lhk27%=:\n\t"
        "s_cbranch_execz .Lhk28%=\n\t"
        "v_mov_b32_e32 v81, 2\n\t"
        "buffer_store_dword v81, v80, %[wsd], 0 offen sc1\n\t"
        "s_endpgm\n\t"
        ".Lend%=:\n\t"
        "buffer_store_dword v80, v114, %[wsd], s72 offen sc1\n\t"
        "s_endpgm\n\t"
        ".Lhk28%=:\n\t"
        "s_branch .Lhr%="
        : [nA] "+s"(nA), [nS] "+s"(nS), [nT] "+s"(nT), [nW] "+s"(nW), [nC] "+s"(nC), [b0] "+s"(b0), [pob] "+s"(pob)
        : [srd] "s"(rhd), [wsd] "s"(wsd), [w] "s"(w), [tag] "s"(tag), [kvo] "s"(kvo), [cal] "s"(cal), [sv] "s"(sv), [so] "s"(so), [m0b] "s"(m0b), [key] "s"(key), [tu] "s"(tu),
          [ferr] "i"(F_ERR), [iD1] "i"(D1), [iD2] "i"(D2), [LC] "i"(LC0), [LA] "i"(LA0), [LC24] "i"(LC0 + 24), [LA24] "i"(LA0 + 24), [E1] "i"(2 * (D1 - LC0)), [E2] "i"(2 * (D2 - LA0)),
          [iPEN] "i"(PEN), [iPEN2] "i"(PEN2), [iPENM] "i"(PEN + 2 * PEN2), [iPEN0] "i"(PEN0), [iTOL] "i"(TOL), [iCT0] "i"(CT0), [iX0C] "i"(X0C), [iX0A] "i"(X0A), [iPHM] "i"(PH_M),
          [iFT] "i"(-3), [iFN] "i"(FINE_N), [AUP] "i"(A_UP), [ASLP] "i"(A_SLP), [LDEC] "i"(L_DEC), [UPSH] "i"(UP_SH), [TRUP] "i"(TR_UP), [TRDN] "i"(TR_DN), [HSLP] "i"(H_SLP), [iPMIN] "i"(PMIN), [VSLP] "i"(V_SLP), [RSLP] "i"(R_SLP), [iVD] "i"(V_DEL), [iXR] "i"(XR), [iBM] "i"(BM_), [iBS] "i"(BS_), [VASL] "i"(VA_SLP), [CSLP] "i"(C_SLP), [DNSH] "i"(DN_SH), [NX0] "i"(NEARX), [NX1] "i"(8192 - NEARX), [iCS1] "i"(CS1), [iCS2] "i"(CS2), [NY0] "i"(NEARY), [NY1] "i"(16384 - NEARY),
          "{v[112:127]}"(pc), "{v[96:103]}"(pa), "{v[104:105]}"(pb)
        : "memory", "vcc", "scc", "m0", "s36", "s37", "s38", "s39", "s40", "s41", "s42", "s43", "s44", "s45", "s46", "s47", "s48", "s49", "s50", "s51", "s52", "s53", "s54", "s55", "s56", "s57", "s58", "s59", "s60", "s61", "s62", "s63", "s64", "s65", "s66", "s67", "s68", "s69", "s70", "s71", "s72", "s73", "s74", "s75", "s76", "s77", "s78", "s79", "s80", "s81", "s82", "s83", "s84", "s85", "s86", "s87", "s88", "s89", "s90", "s91", "s92", "s93", "s94", "s95", "s96", "s97", "s98", "s99");
    }
}

using f32x16 = __attribute__((ext_vector_type(16))) float;
constexpr int CV_RP = 80;
constexpr int CV_U = 96 * CV_RP;
constexpr int CV_KP = 4416;
constexpr int CV_K0 = 4 * CV_U;
constexpr int CV_Y = CV_K0 + 16 * CV_KP;
__device__ __forceinline__ float lo16(unsigned x) { return __uint_as_float(x << 16); }
__device__ __forceinline__ float hi16(unsigned x) { return __uint_as_float(x & 0xFFFF0000u); }
__device__ __forceinline__ void prep_block(const Params& p, rsrc_t r, int k4, const int wv, const unsigned tag) {
    const int tid = otid(), w = wv, l = tid & 63;
    const int cu = (k4 & 3) * 32 + w * 8 + (l >> 3), oc0 = (k4 >> 2) * 8 + (l & 7);
    const rsrc_t rk = __builtin_amdgcn_make_buffer_rsrc((void*)p.Ak, 0, S * D * 2, 0x00020000), rv = __builtin_amdgcn_make_buffer_rsrc((void*)p.Av, 0, S * D * 2, 0x00020000);
    v4u sk[4], kn[11], vn[8], gw[2];
    auto fetch = [&](int oc) {
#pragma unroll
        for (int j = 0; j < 11; ++j) kn[j] = __builtin_amdgcn_raw_buffer_load_b128(rk, (unsigned)((oc * 8 + j - 3) * 2048 + cu * 16), 0, 2);
#pragma unroll
        for (int i = 0; i < 8; ++i) vn[i] = __builtin_amdgcn_raw_buffer_load_b128(rv, (unsigned)((oc * 8 + i) * 2048 + cu * 16), 0, 2); };
    fetch(oc0);
#pragma unroll
    for (int q = 0; q < 4; ++q) sk[q] = *(const v4u*)(p.shift_k + cu * 32 + q * 8);
#pragma unroll
    for (int q = 0; q < 2; ++q) gw[q] = *(const v4u*)(p.gate_w + q * 512 + l * 8);
    st4(r, tid < 128 ? F_RDY + k4 * 512 + tid * 4 : tid < 132 ? F_ATT + (k4 * 4 + tid - 128) * 64 : (tid == 192 && k4 == 0) ? F_CONV : 0xF0000000u, 0u);
#pragma unroll 1
    for (int it = 0; it < 4; ++it) {
        const int oc = oc0 + 64 * it, tk = (it * 32 + k4) * 16 + w;
        v4u kr[11], vr[8], xg[4][2];
#pragma unroll
        for (int j = 0; j < 11; ++j) kr[j] = kn[j];
#pragma unroll
        for (int i = 0; i < 8; ++i) vr[i] = vn[i];
        __builtin_amdgcn_sched_barrier(0);
#pragma unroll
        for (int i = 0; i < 4; ++i)
#pragma unroll
            for (int q = 0; q < 2; ++q) xg[i][q] = *(const v4u*)(p.Aq + (size_t)(tk + 4 * i) * D + q * 512 + l * 8);
        fetch(it < 3 ? oc + 64 : 4096);
        __builtin_amdgcn_sched_barrier(0);
#pragma unroll
        for (int c8 = 0; c8 < 8; ++c8) {
            const unsigned s01 = sk[c8 >> 1][(c8 & 1) * 2], s23 = sk[c8 >> 1][(c8 & 1) * 2 + 1];
            const float sv[4] = {hi16(s23), lo16(s23), hi16(s01), lo16(s01)};
            float kk[11], u[8];
#pragma unroll
            for (int j = 0; j < 11; ++j) kk[j] = (c8 & 1) ? hi16(kr[j][c8 >> 1]) : lo16(kr[j][c8 >> 1]);
            f32x2 acc[4];
#pragma unroll
            for (int j = 0; j < 4; ++j)
#pragma unroll
                for (int i = 0; i < 4; ++i) { const f32x2 t = rbf2((f32x2){kk[i + j], kk[i + j + 4]} * sv[j]); acc[i] = j == 0 ? t : rbf2(acc[i] + t); }
#pragma unroll
            for (int i = 0; i < 8; ++i) u[i] = acc[i & 3][i >> 2] * ((c8 & 1) ? hi16(vr[i][c8 >> 1]) : lo16(vr[i][c8 >> 1]));
            st16(r, W_U + (cu * 8 + c8) * 4096 + oc * 16, (v4u){pack2(u[7], u[6]), pack2(u[5], u[4]), pack2(u[3], u[2]), pack2(u[1], u[0])});
        }
        float ga[4];
#pragma unroll
        for (int i = 0; i < 4; ++i) { float acc = 0.f;
#pragma unroll
            for (int q = 0; q < 2; ++q)
#pragma unroll
                for (int e = 0; e < 4; ++e) { const unsigned xa = xg[i][q][e], gb = gw[q][e]; acc += rbfh(lo16(xa) * lo16(gb)); acc += rbfh(hi16(xa) * hi16(gb)); }
            ga[i] = acc; }
        for (int o = 32; o > 0; o >>= 1)
#pragma unroll
            for (int i = 0; i < 4; ++i) ga[i] += __shfl_xor(ga[i], o);
        const float z = rbfh(l == 0 ? ga[0] : l == 1 ? ga[1] : l == 2 ? ga[2] : ga[3]);
        st4(r, l < 4 ? W_GATE + (tk + 4 * l) * 4 : 0xF0000000u, __float_as_uint(rbfh(1.f / (1.f + expf(-z)))));
    }
    DRAIN(); __syncthreads();
    if (tid == 0) st4(r, F_PREP + k4 * 4, tag);
}
__device__ __forceinline__ bool conv_block(const Params& p, rsrc_t r, const int g0, const int n, unsigned char* lds, const int wv, const int j, const unsigned tag, const unsigned skew) {
    (void)j;
    const int tid = otid(), w = wv, l = tid & 63;
    const rsrc_t rs = __builtin_amdgcn_make_buffer_rsrc((void*)p.ssm_k, 0, S * D * 2, 0x00020000);
    v4u kr[4], uv[4]; v2u kx[4]; unsigned dn;
    auto fetch_k = [&](int ch) {
#pragma unroll
        for (int i = 0; i < 4; ++i) { const unsigned o = (unsigned)(ch * 4096 + (i * 64 + l) * 16);
            kr[i] = __builtin_amdgcn_raw_buffer_load_b128(rs, o, 0, 2); kx[i] = __builtin_amdgcn_raw_buffer_load_b64(rs, (i == 3 && l == 63) ? 0xF0000000u : o + 16, 0, 2); }
        dn = p.ssm_D[ch & 1023]; };
    auto fetch_u = [&](unsigned o) {
#pragma unroll
        for (int i = 0; i < 4; ++i) uv[i] = ld16(r, o + (i * 64 + l) * 16); };
    fetch_k(g0 * 4 + w);
    unsigned char* const uc = lds + w * CV_U; unsigned char* const kc = lds + CV_K0 + w * 4 * CV_KP;
    {
        const v4u z = {0u, 0u, 0u, 0u};
#pragma unroll
        for (int i = 0; i < 3; ++i) *(v4u*)(uc + min(i * 64 + l, 159) * 16) = z;
#pragma unroll
        for (int i = 0; i < 2; ++i) { const int q = min(i * 64 + l, 79), e = q % 20; *(v4u*)(kc + (q / 20) * CV_KP + e * 16 + (e < 8 ? 0 : 4096)) = z; }
    }
    if (!wait_prep(r, l, tag)) return false;
    wait_long(rtnow() + skew);
    fetch_u(W_U + (g0 * 4 + w) * 4096);
    const int b = l & 31, kg = l >> 5, cc = (b + 1) & 3;
#pragma unroll 1
    for (int it = 0; it < n; ++it) {
        const unsigned dv = dn;
#pragma unroll
        for (int i = 0; i < 4; ++i) { const v4u d = kr[i]; const v2u e = kx[i]; unsigned char* q = kc + 128 + (i * 64 + l) * 16;
            const unsigned s0 = __builtin_amdgcn_alignbit(d.y, d.x, 16), s1 = __builtin_amdgcn_alignbit(d.z, d.y, 16), s2 = __builtin_amdgcn_alignbit(d.w, d.z, 16), s3 = __builtin_amdgcn_alignbit(e.x, d.w, 16), s4 = __builtin_amdgcn_alignbit(e.y, e.x, 16);
            *(v4u*)q = d; *(v4u*)(q + CV_KP) = (v4u){s0, s1, s2, s3}; *(v4u*)(q + 2 * CV_KP) = (v4u){d.y, d.z, d.w, e.x}; *(v4u*)(q + 3 * CV_KP) = (v4u){s1, s2, s3, s4};
            if (i == 0 && l == 0) { *(unsigned*)(q + CV_KP - 4) = d.x << 16; *(unsigned*)(q + 2 * CV_KP - 4) = d.x; *(v2u*)(q + 3 * CV_KP - 8) = (v2u){d.x << 16, s0}; } }
#pragma unroll
        for (int i = 0; i < 4; ++i) *(v4u*)(uc + (32 + i * 16 + (l >> 2)) * CV_RP + (l & 3) * 16) = uv[i];
        __builtin_amdgcn_wave_barrier();
        __builtin_amdgcn_sched_barrier(0);
        fetch_k(it + 1 < n ? (g0 + it + 1) * 4 + w : 1024); fetch_u(it + 1 < n ? W_U + ((g0 + it + 1) * 4 + w) * 4096 : 0xF0000000u);
        __builtin_amdgcn_sched_barrier(0);
        f32x16 acc0, acc1, ad0, ad1;
#pragma unroll
        for (int i = 0; i < 16; ++i) { acc0[i] = 0.f; acc1[i] = 0.f; ad0[i] = 0.f; ad1[i] = 0.f; }
        typedef const __attribute__((address_space(3))) v2u* l2p;
        unsigned kb = (unsigned)(size_t)(kc + cc * CV_KP + (b - 8 * kg - 7 + 64 - cc) * 2) - 32, kb8 = kb + 8;
        asm volatile("" : "+v"(kb), "+v"(kb8));
        const unsigned char* ub = uc + (32 + b) * CV_RP + kg * 16;
        v2u ka[4][2], kz[4][2]; bf16x8 fa[4], fz[4];
        auto ld_k = [&](int set, int un, int hi) { const int dl = un >> 1, kh = un & 1; const unsigned o = (unsigned)((32 * dl - 16 * kh + 16) * 2 + hi * 2048);
            if (hi) { kz[set][0] = *(l2p)(size_t)(kb + o); kz[set][1] = *(l2p)(size_t)(kb8 + o); } else { ka[set][0] = *(l2p)(size_t)(kb + o); ka[set][1] = *(l2p)(size_t)(kb8 + o); } };
        auto ld_f = [&](int set, int un) { const int dl = un >> 1, kh = un & 1;
            fa[set] = *(const bf16x8*)(ub + (32 - dl) * CV_RP + kh * 32); fz[set] = *(const bf16x8*)(ub - dl * CV_RP + kh * 32); };
#pragma unroll
        for (int q = 0; q < 2; ++q) { ld_k(q, q, 0); ld_f(q, q); ld_k(q, q, 1); }
#pragma unroll
        for (int kh = 0; kh < 2; ++kh) { const int e = 7 - (b - 16 * kh - 8 * kg); const unsigned x = dv << ((e & 1) * 16); const int q = e >> 1;
            const v4u di = {q == 0 ? x : 0u, q == 1 ? x : 0u, q == 2 ? x : 0u, q == 3 ? x : 0u};
            asm volatile("s_nop 1\n\tv_mfma_f32_32x32x16_bf16 %0, %2, %4, %0\n\tv_mfma_f32_32x32x16_bf16 %1, %3, %4, %1" : "+v"(ad1), "+v"(ad0) : "v"(fa[kh]), "v"(fz[kh]), "v"(di)); }
#pragma unroll 1
        for (int u0 = 0; u0 < 64; u0 += 4) {
#pragma unroll
            for (int q = 0; q < 4; ++q) {
                const int un = u0 + q + 2, ns = (q + 2) & 3;
                const v4u kav = {ka[q][0].x, ka[q][0].y, ka[q][1].x, ka[q][1].y}, kzv = {kz[q][0].x, kz[q][0].y, kz[q][1].x, kz[q][1].y};
                __builtin_amdgcn_sched_barrier(0);
                asm volatile("s_nop 1\n\tv_mfma_f32_32x32x16_bf16 %0, %1, %2, %0" : "+v"(acc1) : "v"(fa[q]), "v"(kav));
                ld_k(ns, un, 0);
                __builtin_amdgcn_sched_barrier(0);
                asm volatile("v_mfma_f32_32x32x16_bf16 %0, %1, %2, %0" : "+v"(acc0) : "v"(fz[q]), "v"(kav));
                ld_f(ns, un);
                __builtin_amdgcn_sched_barrier(0);
                asm volatile("v_mfma_f32_32x32x16_bf16 %0, %1, %2, %0" : "+v"(acc1) : "v"(fz[q]), "v"(kzv));
                ld_k(ns, un, 1);
            }
        }
        asm volatile("s_nop 7\n\ts_nop 7" : "+v"(acc0), "+v"(acc1), "+v"(ad0), "+v"(ad1));
        unsigned char* const yb = lds + CV_Y + (it & 1) * 16384;
#pragma unroll
        for (int i = 0; i < 32; i += 2) { const int row = (i >> 4) * 32 + (i & 3) + 8 * ((i & 15) >> 2) + 4 * kg;
            const unsigned y = pack2(rbfh(i < 16 ? acc0[i] : acc1[i - 16]) + rbfh(i < 16 ? ad0[i] : ad1[i - 16]), rbfh(i < 16 ? acc0[i + 1] : acc1[i - 15]) + rbfh(i < 16 ? ad0[i + 1] : ad1[i - 15]));
            *(unsigned short*)(yb + (row * 32 + b) * 8 + w * 2) = (unsigned short)y; *(unsigned short*)(yb + (row * 32 + 32 + b) * 8 + w * 2) = (unsigned short)(y >> 16); }
        asm volatile("s_waitcnt lgkmcnt(0)\n\ts_barrier" ::: "memory");
#pragma unroll
        for (int i = 0; i < 4; ++i) { const int t = tid + 256 * i; st16(r, W_U + (g0 + it) * 16384 + t * 16, *(const v4u*)(yb + t * 16)); }
    }
    DRAIN(); __syncthreads();
    if (tid == 0) __hip_atomic_fetch_add((unsigned*)(p.ws + F_CONV), (unsigned)n, __ATOMIC_RELAXED, __HIP_MEMORY_SCOPE_AGENT);
    return true;
}

__device__ __forceinline__ void dma16(unsigned ldsdst, unsigned voff, const v4u& srd, unsigned soff) {
    asm volatile("s_mov_b32 m0, %0\n\ts_nop 0\n\tbuffer_load_dwordx4 %1, %2, %3 offen lds" :: "s"(ldsdst), "v"(voff), "s"(srd), "s"(soff) : "m0", "memory"); }
__device__ __forceinline__ v4u pack_acc(const f32x4& a, const f32x4& b) {
    float x0, x1, x2, x3, y0, y1, y2, y3;
    asm volatile("v_accvgpr_read_b32 %0, %8\n\tv_accvgpr_read_b32 %1, %9\n\tv_accvgpr_read_b32 %2, %10\n\tv_accvgpr_read_b32 %3, %11\n\t"
                 "v_accvgpr_read_b32 %4, %12\n\tv_accvgpr_read_b32 %5, %13\n\tv_accvgpr_read_b32 %6, %14\n\tv_accvgpr_read_b32 %7, %15"
                 : "=v"(x0), "=v"(x1), "=v"(x2), "=v"(x3), "=v"(y0), "=v"(y1), "=v"(y2), "=v"(y3)
                 : "a"(a[0]), "a"(a[1]), "a"(a[2]), "a"(a[3]), "a"(b[0]), "a"(b[1]), "a"(b[2]), "a"(b[3]));
    return (v4u){pack2(x0, x1), pack2(x2, x3), pack2(y0, y1), pack2(y2, y3)}; }
#define SB() __builtin_amdgcn_sched_barrier(0)
#define WO_REGS "a128", "a129", "a130", "a131", "a132", "a133", "a134", "a135", "a136", "a137", "a138", "a139", "a140", "a141", "a142", "a143", "a144", "a145", "a146", "a147", "a148", "a149", "a150", "a151", "a152", "a153", "a154", "a155", "a156", "a157", "a158", "a159", "a160", "a161", "a162", "a163", "a164", "a165", "a166", "a167", "a168", "a169", "a170", "a171", "a172", "a173", "a174", "a175", "a176", "a177", "a178", "a179", "a180", "a181", "a182", "a183", "a184", "a185", "a186", "a187", "a188", "a189", "a190", "a191", "a192", "a193", "a194", "a195", "a196", "a197", "a198", "a199", "a200", "a201", "a202", "a203", "a204", "a205", "a206", "a207", "a208", "a209", "a210", "a211", "a212", "a213", "a214", "a215", "a216", "a217", "a218", "a219", "a220", "a221", "a222", "a223", "a224", "a225", "a226", "a227", "a228", "a229", "a230", "a231", "a232", "a233", "a234", "a235", "a236", "a237", "a238", "a239", "a240", "a241", "a242", "a243", "a244", "a245", "a246", "a247", "a248", "a249", "a250", "a251", "a252", "a253", "a254", "a255", "v192", "v193", "v194", "v195", "v196", "v197", "v198", "v199", "v200", "v201", "v202", "v203", "v204", "v205", "v206", "v207", "v208", "v209", "v210", "v211", "v212", "v213", "v214", "v215", "v216", "v217", "v218", "v219", "v220", "v221", "v222", "v223", "v224", "v225", "v226", "v227", "v228", "v229", "v230", "v231", "v232", "v233", "v234", "v235", "v236", "v237", "v238", "v239", "v240", "v241", "v242", "v243", "v244", "v245", "v246", "v247", "v248", "v249", "v250", "v251", "v252", "v253", "v254", "v255"
struct NoHook { __device__ void operator()(int) const {} };
template <int MODE, int MI, int NJ, int NS, int HX = 0, class HOOK = NoHook, int MN = MI>
__device__ __forceinline__ void gemm(rsrc_t r, const bf16_t* __restrict__ A, const bf16_t* __restrict__ B, int m0, int n0, unsigned char* lds, unsigned coff, const int wv,
                                     const int rot, const void* tk = nullptr,
                                     const bool have = false, const bf16_t* Bn = nullptr, int m0n = 0, int n0n = 0, HOOK&& hook = NoHook()) {
    constexpr int AB = MN * 4096, BB = NJ * 2048, B0 = (NS / 2) * AB, NB = NJ / 2, NM = MI * NJ, NR = MI + NJ, BP = NJ * 64;
    static_assert(NS % 2 == 0 && 32 % NS == 0 && B0 <= 65536 && NS * BB <= 65536 && (NJ == 4 || NJ == 8), "ring");
    const int tid = otid();
    const int w = wv, l = tid & 63, n16 = l & 15, g = l >> 4;
    const int wm = w >> 1, wn = w & 1;
    const unsigned lb = __builtin_amdgcn_readfirstlane((unsigned)(size_t)lds);
    const v4u sa = mk_srd(A, 2048 * 1024 * 2); v4u sb = mk_srd(B, 1024 * 4096 * 2);
    f32x4 acc[MI][NJ];
#pragma unroll
    for (int i = 0; i < MI; ++i)
#pragma unroll
        for (int j = 0; j < NJ; ++j) acc[i][j] = (f32x4){0.f, 0.f, 0.f, 0.f};
    unsigned va = (unsigned)((m0 + 8 * MI * w + (l >> 3)) * 2048 + (((l & 7) ^ (l >> 3)) << 4));
    unsigned vb[2];
    if (NJ == 8) { vb[0] = (unsigned)((8 * w + (l >> 5)) * 8192 + n0 * 2) + (unsigned)(((l & 31) << 4) ^ ((l >> 5) << 5) ^ (wn << 7)); vb[1] = vb[0] ^ 64u; }
    else { vb[0] = (unsigned)((8 * w + g) * 8192 + n0 * 2) + (unsigned)((n16 << 4) ^ (g << 5) ^ (wn << 7)); vb[1] = vb[0]; }
    auto rk = [&](int kt) { return (kt & ~31) | ((kt + rot) & 31); };
    unsigned la = lb + w * MI * 1024;
    auto st_a = [&](int buf, int kt, int q) { dma16(la + buf * AB + q * 1024, va, sa, (unsigned)(rk(kt) * 64 + q * 16384)); };
    auto st_b = [&](int buf, int kt, int x) { dma16(lb + B0 + buf * BB + (w * NB + x) * 1024, vb[x & 1], sb, (unsigned)((rk(kt) * 32 + x * (8 / NB)) * 8192)); };
    const unsigned f = (unsigned)((n16 >> 2) | ((g & 1) << 2));
    constexpr int NQ = MODE == 1 ? NJ : NJ / 2;
    unsigned ra[2], rb[NQ];
    ra[0] = lb + (16 * MI * wm + n16) * 128 + ((g ^ (n16 & 7)) << 4); ra[1] = ra[0] ^ 64u;
    {   const unsigned row = lb + B0 + (8 * g + (n16 >> 2)) * BP + (NJ == 8 ? 256 * wn : 0), g0 = NJ == 8 ? 0u : 4u * wn;
#pragma unroll
        for (int j = 0; j < NQ; ++j) {
            if (MODE == 1) rb[j] = row + (((g0 + j) ^ f) << 5) + 8 * (n16 & 3);
            else rb[j] = row + (((g0 + 2 * j + ((n16 & 3) >> 1)) ^ f) << 5) + (n16 & 1) * 16;
            asm volatile("" : "+v"(rb[j])); } }
    bf16x8 af[2][MI], bfr[2][NJ];
    auto rd = [&](int set, int buf, int x) {
        if (x < NJ) { const unsigned a = rb[MODE == 1 ? x : (x >> 1)] + (MODE == 1 ? 0 : (x & 1) * 8) + buf * BB;
            const v2u lo = trb(a), hi = trb(a + 4 * BP); v4u v = {lo.x, lo.y, hi.x, hi.y}; bfr[set][x] = as_frag(v); }
        else af[set][x - NJ] = ldsa(ra[buf & 1] + (buf >> 1) * AB + (x - NJ) * 2048); };
    auto mf = [&](int set, int i, int j) {
        if (HX) asm volatile("v_mfma_f32_16x16x32_bf16 %0, %1, %2, %0" : "+a"(acc[i][j]) : "v"(MODE == 1 ? af[set][i] : bfr[set][j]), "v"(MODE == 1 ? bfr[set][j] : af[set][i]) : WO_REGS);
        else if (MODE == 1) asm volatile("v_mfma_f32_16x16x32_bf16 %0, %1, %2, %0" : "+a"(acc[i][j]) : "v"(af[set][i]), "v"(bfr[set][j]));
        else asm volatile("v_mfma_f32_16x16x32_bf16 %0, %1, %2, %0" : "+a"(acc[i][j]) : "v"(bfr[set][j]), "v"(af[set][i])); };
    if (!have) {
#pragma unroll
        for (int t = 0; t < NS; ++t) {
            if (t & 1) {
#pragma unroll
                for (int q = 0; q < MI; ++q) st_a(t >> 1, t - 1, q); }
#pragma unroll
            for (int q = 0; q < NB; ++q) st_b(t, t, q);
            if (HX) hook(t); }
    }
#pragma unroll
    for (int i = 0; i < MI; ++i)
#pragma unroll
        for (int j = 0; j < NJ; ++j) asm volatile("" : "+a"(acc[i][j]));
    asm volatile("s_nop 1" ::: "memory");
    if (!have) {
        asm volatile("s_waitcnt vmcnt(%c0)\n\ts_barrier" :: "i"((NS - 1) * (NB + HX) + (NS / 2 - 1) * MI) : "memory");
    }
#pragma unroll
    for (int x = 0; x < NR; ++x) rd(0, 0, x);
    constexpr int HEAD = NM / 8;
    const v4u st = mk_srd(tk, tk ? 32768 : 0); unsigned tdum = 0u;
#pragma unroll 1
    for (int k0 = 0; k0 < 32; k0 += NS) {
        int kb = k0 + NS;
        if (kb == 32) {
            kb = Bn ? 0 : 0x10100;
            if (Bn) { va += (unsigned)((m0n - m0 + 8 * (MN - MI) * w) * 2048); la = lb + w * MN * 1024; vb[0] += (unsigned)((n0n - n0) * 2); vb[1] += (unsigned)((n0n - n0) * 2); sb = mk_srd(Bn, 1024 * 4096 * 2); } }
        if (NS == 4) asm volatile("buffer_load_dword %0, %1, %2, %3 offen" : "+v"(tdum) : "v"(l < 32 ? (unsigned)l * 128u : 0xF0000000u), "s"(st), "s"((unsigned)k0 * 1024u) : "memory");
#pragma unroll
        for (int u = 0; u < NS; ++u) {
            SB();
#pragma unroll
            for (int q = 0; q < HEAD; ++q) mf(u & 1, q / NJ, q % NJ);
            SB();
            asm volatile("s_waitcnt vmcnt(%c0) lgkmcnt(0)\n\ts_barrier" :: "i"((NS - 2) * (NB + HX) + (NS / 2 - 1 - (u & 1)) * MI) : "memory");
            SB();
            constexpr int span = NM - HEAD;
#pragma unroll
            for (int q = HEAD; q < NM; ++q) {
                mf(u & 1, q / NJ, q % NJ);
                const int x = q - HEAD, nd = NB + ((u & 1) ? MI : 0);
#pragma unroll
                for (int y = 0; y < NR; ++y) if ((y * (span - HEAD)) / NR == x) rd((u & 1) ^ 1, (u + 1) % NS, y);
#pragma unroll
                for (int y = 0; y < nd; ++y) if ((y * span + span / 2) / nd == x) { if ((u & 1) && y < MI) st_a(u >> 1, kb + u - 1, y); else st_b(u, kb + u, y - ((u & 1) ? MI : 0)); }
                SB(); }
            if (MN > MI && (u & 1) && k0 == 32 - NS) {
#pragma unroll
                for (int y = MI; y < MN; ++y) st_a(u >> 1, kb + u - 1, y); }
            if (HX) hook(NS + k0 + u);
        }
    }
    asm volatile("s_nop 7\n\ts_nop 3" : "+v"(tdum) :: "memory");
    SB();
    const int col = n0 + 16 * NJ * wn, head = col >> 7, sub = (col & 127) >> 4, seg0 = (m0 >> 5) + (MI / 2) * wm;
#pragma unroll
    for (int sg = 0; sg < MI / 2; ++sg) {
        const unsigned base = coff + (unsigned)(((seg0 + sg) * 32 + head) * 8192 + l * 16);
#pragma unroll
        for (int q = 0; q < NJ; ++q) {
            if (HX) asm volatile("" ::: WO_REGS);
            if (MODE == 0) { const int tt = q / (NJ / 2), kk = q % (NJ / 2); st16(r, base + (tt * 4 + (sub >> 1) + kk) * 1024, pack_acc(acc[2 * sg + tt][2 * kk], acc[2 * sg + tt][2 * kk + 1])); }
            else st16(r, base + (sub + q) * 1024, pack_acc(acc[2 * sg][q], acc[2 * sg + 1][q])); }
    }
}

__device__ __forceinline__ void attn_block(const Params& p, const int wv, const int h, const unsigned tag) {
    const int l = otid() & 63, n16 = l & 15, g = l >> 4;
    const unsigned OOB = 0x80000000u;
    const v4u rd = mk_srd(p.ws, W_TOTAL);
    const unsigned kx = W_KX + h * 8192, qx = W_QX + h * 8192, vx = W_VF + h * 8192, km = HO_KVM + h * 4096;
    const v8u pc = {(unsigned)l * 16, (unsigned)(n16 * 16 + (g & 1) * 8), F_KVQ + ((l >> 2) * 32 + h) * 64 + min(l & 3, 2) * 4, F_PROG + h * 128, (unsigned)((g >> 1) * 256 + ((n16 & 1) + 2 * (g & 1) + 2 * (n16 & 6)) * 16),
                    (unsigned)(wv * 2048 + (8 * g + (n16 >> 2)) * 16 + (n16 & 1) * 8), (l & 31) ? OOB : F_ATT + (l >> 5) * 64, l < 32 ? F_RDY + l * 128 : OOB};
    const int dq = n16 - 4 * g;
    asm volatile(
        "s_mov_b32 s36, 0xf0000000\n\t"
        "s_mov_b32 s37, %[w]\n\t"
        "s_mov_b32 s38, s36\n\t"
        "s_mov_b32 s40, 0\n\t"
        "s_memrealtime s[98:99]\n\t"
        "s_waitcnt lgkmcnt(0)\n\t"
        "s_sub_u32 s41, s98, 0x100000\n\t"
        "s_mov_b64 s[42:43], 0\n\t"
        "s_mov_b32 s44, -1\n\t"
        "s_mov_b32 s45, -1\n\t"
        "s_mov_b32 s56, 0\n\t"
        "s_mov_b32 s57, -1\n\t"
        "s_lshl_b32 s58, %[w], 10\n\t"
        "s_add_u32 s58, s58, 16384\n\t"
        "v_cmp_le_i32_e64 s[48:49], 0, v252\n\t"
        "v_cmp_le_i32_e64 s[50:51], 1, v252\n\t"
        "v_cmp_le_i32_e64 s[52:53], 2, v252\n\t"
        "v_cmp_le_i32_e64 s[54:55], 3, v252\n\t"
        "v_mov_b32_e32 v253, 0xce6e6b28\n\t"
        "v_mov_b32_e32 v254, 0xff800000\n\t"
        "s_mov_b32 s97, 0\n\t"
        ".Lst%=:\n\t"
        "s_sleep 32\n\t"
        "buffer_load_dword v240, v246, %[rd], 0 offen sc1\n\t"
        "s_add_u32 s97, s97, 1\n\t"
        "s_cmp_lt_u32 s97, 0x200000\n\t"
        "s_cbranch_scc0 .Lgu%=\n\t"
        "s_waitcnt vmcnt(0)\n\t"
        "v_cmp_eq_u32_e32 vcc, %[tag], v240\n\t"
        "s_nop 1\n\t"
        "s_or_b64 s[42:43], s[42:43], vcc\n\t"
        "s_andn2_b32 s80, %c[stm], s43\n\t"
        "s_cbranch_scc1 .Lst%=\n\t"
        "s_sleep %c[sts]\n\t"
        ".Ltop%=:\n\t"
        "s_cmp_eq_u32 s38, 0\n\t"
        "s_cbranch_scc1 .La2%=\n\t"
        "s_mov_b32 s97, 0\n\t"
        ".La1%=:\n\t"
        "buffer_load_dword v240, v246, %[rd], 0 offen sc1\n\t"
        "buffer_load_dword v241, v247, %[rd], 0 offen sc1\n\t"
        "s_add_u32 s97, s97, 1\n\t"
        "s_cmp_lt_u32 s97, 0x200000\n\t"
        "s_cbranch_scc0 .Lgu%=\n\t"
        "s_memrealtime s[98:99]\n\t"
        "s_waitcnt vmcnt(0) lgkmcnt(0)\n\t"
        "v_cmp_eq_u32_e32 vcc, %[tag], v240\n\t"
        "s_nop 1\n\t"
        "s_or_b64 s[42:43], s[42:43], vcc\n\t"
        "v_readfirstlane_b32 s80, v241\n\t"
        "s_nop 0\n\t"
        "s_cmp_gt_u32 s80, s40\n\t"
        "s_cselect_b32 s41, s98, s41\n\t"
        "s_max_u32 s40, s40, s80\n\t"
        "s_and_b32 s80, s37, 60\n\t"
        "s_lshr_b64 s[80:81], s[42:43], s80\n\t"
        "s_andn2_b32 s80, 15, s80\n\t"
        "s_cbranch_scc0 .La1%=x\n\t"
        "s_sleep 16\n\t"
        "s_branch .La1%=\n\t"
        ".La1%=x:\n\t"
        "s_cmp_gt_u32 s40, s37\n\t"
        "s_cselect_b32 s39, 0, s36\n\t"
        "s_lshl_b32 s80, s37, 18\n\t"
        "s_or_b32 s80, s80, 0\n\t"
        "s_lshl_b32 s81, s37, 17\n\t"
        "s_add_u32 s81, s81, %[km]\n\t"
        "s_or_b32 s81, s81, s39\n\t"
        "s_add_u32 s82, s80, %[kx]\n\t"
        "s_add_u32 s83, s82, 0x1000\n\t"
        "buffer_load_dwordx4 a[0:3], v244, %[rd], s82 offen offset:0 sc1\n\t"
        "buffer_load_dwordx4 a[4:7], v244, %[rd], s82 offen offset:1024 sc1\n\t"
        "buffer_load_dwordx4 a[8:11], v244, %[rd], s82 offen offset:2048 sc1\n\t"
        "buffer_load_dwordx4 a[12:15], v244, %[rd], s82 offen offset:3072 sc1\n\t"
        "buffer_load_dwordx4 a[16:19], v244, %[rd], s83 offen offset:0 sc1\n\t"
        "buffer_load_dwordx4 a[20:23], v244, %[rd], s83 offen offset:1024 sc1\n\t"
        "buffer_load_dwordx4 a[24:27], v244, %[rd], s83 offen offset:2048 sc1\n\t"
        "buffer_load_dwordx4 a[28:31], v244, %[rd], s83 offen offset:3072 sc1\n\t"
        "s_cmp_eq_u64 s[42:43], -1\n\t"
        "s_cbranch_scc1 .La3%=\n\t"
        "s_sleep %c[aslp]\n\t"
        ".La3%=:\n\t"
        "s_sleep %c[bslp]\n\t"
        "s_add_u32 s82, s80, %[qx]\n\t"
        "s_add_u32 s83, s82, 0x1000\n\t"
        "buffer_load_dwordx4 a[32:35], v244, %[rd], s82 offen offset:0 sc1\n\t"
        "buffer_load_dwordx4 a[36:39], v244, %[rd], s82 offen offset:1024 sc1\n\t"
        "buffer_load_dwordx4 a[40:43], v244, %[rd], s82 offen offset:2048 sc1\n\t"
        "buffer_load_dwordx4 a[44:47], v244, %[rd], s82 offen offset:3072 sc1\n\t"
        "buffer_load_dwordx4 a[48:51], v244, %[rd], s83 offen offset:0 sc1\n\t"
        "buffer_load_dwordx4 a[52:55], v244, %[rd], s83 offen offset:1024 sc1\n\t"
        "buffer_load_dwordx4 a[56:59], v244, %[rd], s83 offen offset:2048 sc1\n\t"
        "buffer_load_dwordx4 a[60:63], v244, %[rd], s83 offen offset:3072 sc1\n\t"
        "s_cmp_eq_u64 s[42:43], -1\n\t"
        "s_cbranch_scc1 .La4%=\n\t"
        "s_sleep %c[aslp]\n\t"
        ".La4%=:\n\t"
        "s_sleep %c[bslp]\n\t"
        "buffer_load_dwordx4 a[96:99], v248, %[rd], s81 offen offset:0 sc1\n\t"
        "buffer_load_dwordx4 a[100:103], v248, %[rd], s81 offen offset:512 sc1\n\t"
        "buffer_load_dwordx4 a[104:107], v248, %[rd], s81 offen offset:1024 sc1\n\t"
        "buffer_load_dwordx4 a[108:111], v248, %[rd], s81 offen offset:1536 sc1\n\t"
        "buffer_load_dwordx2 a[112:113], v245, %[rd], s81 offen offset:2048 sc1\n\t"
        "buffer_load_dwordx2 a[114:115], v245, %[rd], s81 offen offset:2304 sc1\n\t"
        "buffer_load_dwordx2 a[116:117], v245, %[rd], s81 offen offset:2560 sc1\n\t"
        "buffer_load_dwordx2 a[118:119], v245, %[rd], s81 offen offset:2816 sc1\n\t"
        "buffer_load_dwordx2 a[120:121], v245, %[rd], s81 offen offset:3072 sc1\n\t"
        "buffer_load_dwordx2 a[122:123], v245, %[rd], s81 offen offset:3328 sc1\n\t"
        "buffer_load_dwordx2 a[124:125], v245, %[rd], s81 offen offset:3584 sc1\n\t"
        "buffer_load_dwordx2 a[126:127], v245, %[rd], s81 offen offset:3840 sc1\n\t"
        "s_add_u32 s82, s80, %[vx]\n\t"
        "s_add_u32 s83, s82, 0x1000\n\t"
        "buffer_load_dwordx4 a[64:67], v244, %[rd], s82 offen offset:0 sc1\n\t"
        "buffer_load_dwordx4 a[68:71], v244, %[rd], s82 offen offset:1024 sc1\n\t"
        "buffer_load_dwordx4 a[72:75], v244, %[rd], s82 offen offset:2048 sc1\n\t"
        "buffer_load_dwordx4 a[76:79], v244, %[rd], s82 offen offset:3072 sc1\n\t"
        "buffer_load_dwordx4 a[80:83], v244, %[rd], s83 offen offset:0 sc1\n\t"
        "buffer_load_dwordx4 a[84:87], v244, %[rd], s83 offen offset:1024 sc1\n\t"
        "buffer_load_dwordx4 a[88:91], v244, %[rd], s83 offen offset:2048 sc1\n\t"
        "buffer_load_dwordx4 a[92:95], v244, %[rd], s83 offen offset:3072 sc1\n\t"
        "s_cmp_eq_u64 s[42:43], -1\n\t"
        "s_cbranch_scc1 .La5%=\n\t"
        "s_sleep %c[aslp]\n\t"
        ".La5%=:\n\t"
        "s_sleep %c[bslp]\n\t"
        "buffer_store_dword v0, off, %[rd], s36\n\t"
        "buffer_store_dword v0, off, %[rd], s36\n\t"
        "buffer_store_dword v0, off, %[rd], s36\n\t"
        "buffer_store_dword v0, off, %[rd], s36\n\t"
        "buffer_store_dword v0, off, %[rd], s36\n\t"
        "buffer_store_dword v0, off, %[rd], s36\n\t"
        "buffer_store_dword v0, off, %[rd], s36\n\t"
        "buffer_store_dword v0, off, %[rd], s36\n\t"
        "buffer_store_dword v0, off, %[rd], s36\n\t"
        ".La2%=:\n\t"
        "s_cmp_gt_u32 s40, s37\n\t"
        "s_cselect_b32 s80, s39, 0\n\t"
        "s_cmp_eq_u32 s80, 0\n\t"
        "s_cbranch_scc1 .La6%=\n\t"
        "s_mov_b32 s39, 0\n\t"
        "s_lshl_b32 s81, s37, 17\n\t"
        "s_add_u32 s81, s81, %[km]\n\t"
        "buffer_load_dwordx4 a[96:99], v248, %[rd], s81 offen offset:0 sc1\n\t"
        "buffer_load_dwordx4 a[100:103], v248, %[rd], s81 offen offset:512 sc1\n\t"
        "buffer_load_dwordx4 a[104:107], v248, %[rd], s81 offen offset:1024 sc1\n\t"
        "buffer_load_dwordx4 a[108:111], v248, %[rd], s81 offen offset:1536 sc1\n\t"
        "buffer_load_dwordx2 a[112:113], v245, %[rd], s81 offen offset:2048 sc1\n\t"
        "buffer_load_dwordx2 a[114:115], v245, %[rd], s81 offen offset:2304 sc1\n\t"
        "buffer_load_dwordx2 a[116:117], v245, %[rd], s81 offen offset:2560 sc1\n\t"
        "buffer_load_dwordx2 a[118:119], v245, %[rd], s81 offen offset:2816 sc1\n\t"
        "buffer_load_dwordx2 a[120:121], v245, %[rd], s81 offen offset:3072 sc1\n\t"
        "buffer_load_dwordx2 a[122:123], v245, %[rd], s81 offen offset:3328 sc1\n\t"
        "buffer_load_dwordx2 a[124:125], v245, %[rd], s81 offen offset:3584 sc1\n\t"
        "buffer_load_dwordx2 a[126:127], v245, %[rd], s81 offen offset:3840 sc1\n\t"
        "buffer_store_dword v0, off, %[rd], s36\n\t"
        "buffer_store_dword v0, off, %[rd], s36\n\t"
        "buffer_store_dword v0, off, %[rd], s36\n\t"
        "buffer_store_dword v0, off, %[rd], s36\n\t"
        "buffer_store_dword v0, off, %[rd], s36\n\t"
        "buffer_store_dword v0, off, %[rd], s36\n\t"
        "buffer_store_dword v0, off, %[rd], s36\n\t"
        "buffer_store_dword v0, off, %[rd], s36\n\t"
        ".La6%=:\n\t"
        "s_add_u32 s86, s37, 4\n\t"
        "s_and_b32 s80, s86, 60\n\t"
        "s_lshr_b64 s[80:81], s[42:43], s80\n\t"
        "s_andn2_b32 s80, 15, s80\n\t"
        "s_cselect_b32 s46, s36, 0\n\t"
        "s_cmp_lt_u32 s86, 64\n\t"
        "s_cselect_b32 s46, s46, s36\n\t"
        "s_cmp_gt_u32 s40, s86\n\t"
        "s_cselect_b32 s47, s46, s36\n\t"
        "s_lshl_b32 s80, s86, 18\n\t"
        "s_or_b32 s80, s80, s46\n\t"
        "s_lshl_b32 s81, s86, 17\n\t"
        "s_add_u32 s81, s81, %[km]\n\t"
        "s_or_b32 s81, s81, s47\n\t"
        "s_add_u32 s82, s80, %[kx]\n\t"
        "s_add_u32 s83, s82, 0x1000\n\t"
        "buffer_load_dwordx4 v[0:3], v244, %[rd], s82 offen offset:0 sc1\n\t"
        "buffer_load_dwordx4 v[4:7], v244, %[rd], s82 offen offset:1024 sc1\n\t"
        "buffer_load_dwordx4 v[8:11], v244, %[rd], s82 offen offset:2048 sc1\n\t"
        "buffer_load_dwordx4 v[12:15], v244, %[rd], s82 offen offset:3072 sc1\n\t"
        "buffer_load_dwordx4 v[16:19], v244, %[rd], s83 offen offset:0 sc1\n\t"
        "buffer_load_dwordx4 v[20:23], v244, %[rd], s83 offen offset:1024 sc1\n\t"
        "buffer_load_dwordx4 v[24:27], v244, %[rd], s83 offen offset:2048 sc1\n\t"
        "buffer_load_dwordx4 v[28:31], v244, %[rd], s83 offen offset:3072 sc1\n\t"
        "s_cmp_eq_u64 s[42:43], -1\n\t"
        "s_cbranch_scc1 .La7%=\n\t"
        "s_sleep %c[aslp]\n\t"
        ".La7%=:\n\t"
        "s_sleep %c[bslp]\n\t"
        "s_add_u32 s82, s80, %[qx]\n\t"
        "s_add_u32 s83, s82, 0x1000\n\t"
        "buffer_load_dwordx4 v[32:35], v244, %[rd], s82 offen offset:0 sc1\n\t"
        "buffer_load_dwordx4 v[36:39], v244, %[rd], s82 offen offset:1024 sc1\n\t"
        "buffer_load_dwordx4 v[40:43], v244, %[rd], s82 offen offset:2048 sc1\n\t"
        "buffer_load_dwordx4 v[44:47], v244, %[rd], s82 offen offset:3072 sc1\n\t"
        "buffer_load_dwordx4 v[48:51], v244, %[rd], s83 offen offset:0 sc1\n\t"
        "buffer_load_dwordx4 v[52:55], v244, %[rd], s83 offen offset:1024 sc1\n\t"
        "buffer_load_dwordx4 v[56:59], v244, %[rd], s83 offen offset:2048 sc1\n\t"
        "buffer_load_dwordx4 v[60:63], v244, %[rd], s83 offen offset:3072 sc1\n\t"
        "s_cmp_eq_u64 s[42:43], -1\n\t"
        "s_cbranch_scc1 .La8%=\n\t"
        "s_sleep %c[aslp]\n\t"
        ".La8%=:\n\t"
        "s_sleep %c[bslp]\n\t"
        "buffer_load_dwordx4 v[96:99], v248, %[rd], s81 offen offset:0 sc1\n\t"
        "buffer_load_dwordx4 v[100:103], v248, %[rd], s81 offen offset:512 sc1\n\t"
        "buffer_load_dwordx4 v[104:107], v248, %[rd], s81 offen offset:1024 sc1\n\t"
        "buffer_load_dwordx4 v[108:111], v248, %[rd], s81 offen offset:1536 sc1\n\t"
        "buffer_load_dwordx2 v[112:113], v245, %[rd], s81 offen offset:2048 sc1\n\t"
        "buffer_load_dwordx2 v[114:115], v245, %[rd], s81 offen offset:2304 sc1\n\t"
        "buffer_load_dwordx2 v[116:117], v245, %[rd], s81 offen offset:2560 sc1\n\t"
        "buffer_load_dwordx2 v[118:119], v245, %[rd], s81 offen offset:2816 sc1\n\t"
        "buffer_load_dwordx2 v[120:121], v245, %[rd], s81 offen offset:3072 sc1\n\t"
        "buffer_load_dwordx2 v[122:123], v245, %[rd], s81 offen offset:3328 sc1\n\t"
        "buffer_load_dwordx2 v[124:125], v245, %[rd], s81 offen offset:3584 sc1\n\t"
        "buffer_load_dwordx2 v[126:127], v245, %[rd], s81 offen offset:3840 sc1\n\t"
        "s_add_u32 s82, s80, %[vx]\n\t"
        "s_add_u32 s83, s82, 0x1000\n\t"
        "buffer_load_dwordx4 v[64:67], v244, %[rd], s82 offen offset:0 sc1\n\t"
        "buffer_load_dwordx4 v[68:71], v244, %[rd], s82 offen offset:1024 sc1\n\t"
        "buffer_load_dwordx4 v[72:75], v244, %[rd], s82 offen offset:2048 sc1\n\t"
        "buffer_load_dwordx4 v[76:79], v244, %[rd], s82 offen offset:3072 sc1\n\t"
        "buffer_load_dwordx4 v[80:83], v244, %[rd], s83 offen offset:0 sc1\n\t"
        "buffer_load_dwordx4 v[84:87], v244, %[rd], s83 offen offset:1024 sc1\n\t"
        "buffer_load_dwordx4 v[88:91], v244, %[rd], s83 offen offset:2048 sc1\n\t"
        "buffer_load_dwordx4 v[92:95], v244, %[rd], s83 offen offset:3072 sc1\n\t"
        "s_cmp_eq_u64 s[42:43], -1\n\t"
        "s_cbranch_scc1 .La9%=\n\t"
        "s_sleep %c[aslp]\n\t"
        ".La9%=:\n\t"
        "s_sleep %c[bslp]\n\t"
        "s_waitcnt vmcnt(44)\n\t"
        "s_memrealtime s[98:99]\n\t"
        "v_mfma_f32_16x16x32_bf16 v[136:139], a[0:3], a[32:35], 0\n\t"
        "v_mfma_f32_16x16x32_bf16 v[148:151], a[0:3], a[48:51], 0\n\t"
        "v_mfma_f32_16x16x32_bf16 v[152:155], a[16:19], a[48:51], 0\n\t"
        "v_mfma_f32_16x16x32_bf16 v[136:139], a[4:7], a[36:39], v[136:139]\n\t"
        "v_mfma_f32_16x16x32_bf16 v[148:151], a[4:7], a[52:55], v[148:151]\n\t"
        "v_mfma_f32_16x16x32_bf16 v[152:155], a[20:23], a[52:55], v[152:155]\n\t"
        "v_mfma_f32_16x16x32_bf16 v[136:139], a[8:11], a[40:43], v[136:139]\n\t"
        "v_mfma_f32_16x16x32_bf16 v[148:151], a[8:11], a[56:59], v[148:151]\n\t"
        "v_mfma_f32_16x16x32_bf16 v[152:155], a[24:27], a[56:59], v[152:155]\n\t"
        "v_mfma_f32_16x16x32_bf16 v[136:139], a[12:15], a[44:47], v[136:139]\n\t"
        "v_mfma_f32_16x16x32_bf16 v[148:151], a[12:15], a[60:63], v[148:151]\n\t"
        "v_mfma_f32_16x16x32_bf16 v[152:155], a[28:31], a[60:63], v[152:155]\n\t"
        "s_waitcnt lgkmcnt(0)\n\t"
        "v_cmp_eq_u32_e32 vcc, %[tag], v240\n\t"
        "s_nop 1\n\t"
        "s_or_b64 s[42:43], s[42:43], vcc\n\t"
        "v_readfirstlane_b32 s80, v241\n\t"
        "s_nop 0\n\t"
        "s_cmp_gt_u32 s80, s40\n\t"
        "s_cselect_b32 s41, s98, s41\n\t"
        "s_max_u32 s40, s40, s80\n\t"
        "v_readlane_b32 s84, v242, 0\n\t"
        "v_readlane_b32 s85, v242, 32\n\t"
        "v_mov_b32_e32 v243, 32\n\t"
        "s_and_b32 s80, s45, 3\n\t"
        "s_lshl_b32 s80, s80, 12\n\t"
        "s_lshr_b32 s81, s45, 2\n\t"
        "s_lshl_b32 s81, s81, 3\n\t"
        "s_add_u32 s80, s80, s81\n\t"
        "s_lshr_b32 s82, s45, 1\n\t"
        "s_xnor_b32 s81, s45, s82\n\t"
        "s_and_b32 s81, s81, 1\n\t"
        "s_lshl_b32 s81, s81, 13\n\t"
        "s_lshl_b32 s82, s82, 2\n\t"
        "s_add_u32 s81, s81, s82\n\t"
        "s_cmp_lt_i32 s45, 60\n\t"
        "s_cselect_b32 s80, s80, s81\n\t"
        "s_cselect_b32 s81, 4, 0x1000\n\t"
        "s_add_u32 s81, s81, s80\n\t"
        "s_cmp_eq_u32 s84, 31\n\t"
        "s_cselect_b32 s80, s80, s36\n\t"
        "s_cmp_eq_u32 s85, 31\n\t"
        "s_cselect_b32 s81, s81, s36\n\t"
        "s_cmp_lt_i32 s45, 0\n\t"
        "s_cselect_b32 s80, s36, s80\n\t"
        "s_cselect_b32 s81, s36, s81\n\t"
        "buffer_store_dword v243, v251, %[rd], s80 offen sc1\n\t"
        "buffer_store_dword v243, v251, %[rd], s81 offen sc1\n\t"
        "s_add_u32 s86, s37, 8\n\t"
        "s_cmp_lg_u64 s[42:43], -1\n\t"
        "s_cselect_b32 s84, 0, s36\n\t"
        "s_cmp_gt_u32 s40, s86\n\t"
        "s_cselect_b32 s85, s36, s39\n\t"
        "buffer_load_dword v240, v246, %[rd], s84 offen sc1\n\t"
        "buffer_load_dword v241, v247, %[rd], s85 offen sc1\n\t"
        "v_cvt_pk_bf16_f32 v136, 0, v136\n\t"
        "v_cvt_pk_bf16_f32 v137, 0, v137\n\t"
        "v_cvt_pk_bf16_f32 v138, 0, v138\n\t"
        "v_cvt_pk_bf16_f32 v139, 0, v139\n\t"
        "v_mul_f32_e32 v136, 0x3db504f3, v136\n\t"
        "v_mul_f32_e32 v137, 0x3db504f3, v137\n\t"
        "v_mul_f32_e32 v138, 0x3db504f3, v138\n\t"
        "v_mul_f32_e32 v139, 0x3db504f3, v139\n\t"
        "v_cvt_pk_bf16_f32 v136, 0, v136\n\t"
        "v_cvt_pk_bf16_f32 v137, 0, v137\n\t"
        "v_cvt_pk_bf16_f32 v138, 0, v138\n\t"
        "v_cvt_pk_bf16_f32 v139, 0, v139\n\t"
        "v_cndmask_b32_e64 v136, v253, v136, s[48:49]\n\t"
        "v_cndmask_b32_e64 v137, v253, v137, s[50:51]\n\t"
        "v_cndmask_b32_e64 v138, v253, v138, s[52:53]\n\t"
        "v_cndmask_b32_e64 v139, v253, v139, s[54:55]\n\t"
        "v_cvt_pk_bf16_f32 v148, 0, v148\n\t"
        "v_cvt_pk_bf16_f32 v149, 0, v149\n\t"
        "v_cvt_pk_bf16_f32 v150, 0, v150\n\t"
        "v_cvt_pk_bf16_f32 v151, 0, v151\n\t"
        "v_mul_f32_e32 v148, 0x3db504f3, v148\n\t"
        "v_mul_f32_e32 v149, 0x3db504f3, v149\n\t"
        "v_mul_f32_e32 v150, 0x3db504f3, v150\n\t"
        "v_mul_f32_e32 v151, 0x3db504f3, v151\n\t"
        "v_cvt_pk_bf16_f32 v148, 0, v148\n\t"
        "v_cvt_pk_bf16_f32 v149, 0, v149\n\t"
        "v_cvt_pk_bf16_f32 v150, 0, v150\n\t"
        "v_cvt_pk_bf16_f32 v151, 0, v151\n\t"
        "v_cvt_pk_bf16_f32 v152, 0, v152\n\t"
        "v_cvt_pk_bf16_f32 v153, 0, v153\n\t"
        "v_cvt_pk_bf16_f32 v154, 0, v154\n\t"
        "v_cvt_pk_bf16_f32 v155, 0, v155\n\t"
        "v_mul_f32_e32 v152, 0x3db504f3, v152\n\t"
        "v_mul_f32_e32 v153, 0x3db504f3, v153\n\t"
        "v_mul_f32_e32 v154, 0x3db504f3, v154\n\t"
        "v_mul_f32_e32 v155, 0x3db504f3, v155\n\t"
        "v_cvt_pk_bf16_f32 v152, 0, v152\n\t"
        "v_cvt_pk_bf16_f32 v153, 0, v153\n\t"
        "v_cvt_pk_bf16_f32 v154, 0, v154\n\t"
        "v_cvt_pk_bf16_f32 v155, 0, v155\n\t"
        "v_cndmask_b32_e64 v152, v253, v152, s[48:49]\n\t"
        "v_cndmask_b32_e64 v153, v253, v153, s[50:51]\n\t"
        "v_cndmask_b32_e64 v154, v253, v154, s[52:53]\n\t"
        "v_cndmask_b32_e64 v155, v253, v155, s[54:55]\n\t"
        "v_max3_f32 v172, v136, v137, v253\n\t"
        "v_max3_f32 v172, v172, v138, v139\n\t"
        "v_mov_b32_e32 v140, v253\n\t"
        "v_max_f32_e32 v174, v148, v149\n\t"
        "v_max3_f32 v174, v174, v150, v151\n\t"
        "v_max3_f32 v174, v174, v152, v153\n\t"
        "v_max3_f32 v174, v174, v154, v155\n\t"
        "s_cmp_eq_u32 s39, 0\n\t"
        "s_cbranch_scc1 .La11%=\n\t"
        "s_waitcnt vmcnt(0)\n\t"
        "s_lshl_b32 s80, s44, 7\n\t"
        "s_cmp_lt_i32 s44, 0\n\t"
        "s_cselect_b32 s80, s36, s80\n\t"
        "v_mov_b32_e32 v242, 1\n\t"
        "buffer_atomic_add v242, v250, %[rd], s80 offen sc0 sc1\n\t"
        "s_waitcnt vmcnt(0)\n\t"
        "v_readlane_b32 s84, v242, 0\n\t"
        "v_readlane_b32 s85, v242, 32\n\t"
        "v_mov_b32_e32 v243, 32\n\t"
        "s_and_b32 s80, s44, 3\n\t"
        "s_lshl_b32 s80, s80, 12\n\t"
        "s_lshr_b32 s81, s44, 2\n\t"
        "s_lshl_b32 s81, s81, 3\n\t"
        "s_add_u32 s80, s80, s81\n\t"
        "s_lshr_b32 s82, s44, 1\n\t"
        "s_xnor_b32 s81, s44, s82\n\t"
        "s_and_b32 s81, s81, 1\n\t"
        "s_lshl_b32 s81, s81, 13\n\t"
        "s_lshl_b32 s82, s82, 2\n\t"
        "s_add_u32 s81, s81, s82\n\t"
        "s_cmp_lt_i32 s44, 60\n\t"
        "s_cselect_b32 s80, s80, s81\n\t"
        "s_cselect_b32 s81, 4, 0x1000\n\t"
        "s_add_u32 s81, s81, s80\n\t"
        "s_cmp_eq_u32 s84, 31\n\t"
        "s_cselect_b32 s80, s80, s36\n\t"
        "s_cmp_eq_u32 s85, 31\n\t"
        "s_cselect_b32 s81, s81, s36\n\t"
        "s_cmp_lt_i32 s44, 0\n\t"
        "s_cselect_b32 s80, s36, s80\n\t"
        "s_cselect_b32 s81, s36, s81\n\t"
        "buffer_store_dword v243, v251, %[rd], s80 offen sc1\n\t"
        "buffer_store_dword v243, v251, %[rd], s81 offen sc1\n\t"
        "s_mov_b32 s44, -1\n\t"
        "s_mov_b32 s97, 0\n\t"
        "s_min_u32 s87, s37, 62\n\t"
        ".La10%=:\n\t"
        "s_cmp_gt_u32 s40, s87\n\t"
        "s_cbranch_scc1 .La10%=x\n\t"
        "s_memrealtime s[98:99]\n\t"
        "s_add_u32 s97, s97, 1\n\t"
        "s_cmp_lt_u32 s97, 0x400000\n\t"
        "s_cbranch_scc0 .Lgu%=\n\t"
        "s_sub_u32 s80, s87, s40\n\t"
        "s_cmp_ge_u32 s87, %c[efrom]\n\t"
        "s_subb_u32 s80, s80, 0\n\t"
        "s_mul_i32 s80, s80, %c[smin]\n\t"
        "s_waitcnt lgkmcnt(0)\n\t"
        "s_sub_u32 s81, s98, s41\n\t"
        "s_cmp_ge_i32 s81, s80\n\t"
        "s_cbranch_scc1 .La10%=p\n\t"
        "s_sleep 16\n\t"
        "s_branch .La10%=\n\t"
        ".La10%=p:\n\t"
        "buffer_load_dword v241, v247, %[rd], 0 offen sc1\n\t"
        "s_waitcnt vmcnt(0)\n\t"
        "v_readfirstlane_b32 s80, v241\n\t"
        "s_nop 0\n\t"
        "s_cmp_gt_u32 s80, s40\n\t"
        "s_cselect_b32 s41, s98, s41\n\t"
        "s_max_u32 s40, s40, s80\n\t"
        "s_cmp_gt_u32 s40, s87\n\t"
        "s_cbranch_scc1 .La10%=x\n\t"
        "s_sleep %c[pslp]\n\t"
        "s_branch .La10%=\n\t"
        ".La10%=x:\n\t"
        "s_lshl_b32 s81, s37, 17\n\t"
        "s_add_u32 s81, s81, %[km]\n\t"
        "buffer_load_dwordx4 a[96:99], v248, %[rd], s81 offen offset:0 sc1\n\t"
        "buffer_load_dwordx4 a[100:103], v248, %[rd], s81 offen offset:512 sc1\n\t"
        "buffer_load_dwordx4 a[104:107], v248, %[rd], s81 offen offset:1024 sc1\n\t"
        "buffer_load_dwordx4 a[108:111], v248, %[rd], s81 offen offset:1536 sc1\n\t"
        "buffer_load_dwordx2 a[112:113], v245, %[rd], s81 offen offset:2048 sc1\n\t"
        "buffer_load_dwordx2 a[114:115], v245, %[rd], s81 offen offset:2304 sc1\n\t"
        "buffer_load_dwordx2 a[116:117], v245, %[rd], s81 offen offset:2560 sc1\n\t"
        "buffer_load_dwordx2 a[118:119], v245, %[rd], s81 offen offset:2816 sc1\n\t"
        "buffer_load_dwordx2 a[120:121], v245, %[rd], s81 offen offset:3072 sc1\n\t"
        "buffer_load_dwordx2 a[122:123], v245, %[rd], s81 offen offset:3328 sc1\n\t"
        "buffer_load_dwordx2 a[124:125], v245, %[rd], s81 offen offset:3584 sc1\n\t"
        "buffer_load_dwordx2 a[126:127], v245, %[rd], s81 offen offset:3840 sc1\n\t"
        "s_waitcnt vmcnt(0)\n\t"
        ".La11%=:\n\t"
        "v_mfma_f32_16x16x32_bf16 v[144:147], a[96:99], a[32:35], 0\n\t"
        "v_mfma_f32_16x16x32_bf16 v[156:159], a[96:99], a[48:51], 0\n\t"
        "v_mfma_f32_16x16x32_bf16 v[144:147], a[100:103], a[36:39], v[144:147]\n\t"
        "v_mfma_f32_16x16x32_bf16 v[156:159], a[100:103], a[52:55], v[156:159]\n\t"
        "v_mfma_f32_16x16x32_bf16 v[144:147], a[104:107], a[40:43], v[144:147]\n\t"
        "v_mfma_f32_16x16x32_bf16 v[156:159], a[104:107], a[56:59], v[156:159]\n\t"
        "v_mfma_f32_16x16x32_bf16 v[144:147], a[108:111], a[44:47], v[144:147]\n\t"
        "v_mfma_f32_16x16x32_bf16 v[156:159], a[108:111], a[60:63], v[156:159]\n\t"
        "s_nop 1\n\t"
        "v_mov_b32_e32 v252, v252\n\t"
        "s_nop 1\n\t"
        "v_mov_b32_e32 v252, v252\n\t"
        "s_nop 0\n\t"
        "v_cvt_pk_bf16_f32 v144, 0, v144\n\t"
        "v_cvt_pk_bf16_f32 v145, 0, v145\n\t"
        "v_cvt_pk_bf16_f32 v146, 0, v146\n\t"
        "v_cvt_pk_bf16_f32 v147, 0, v147\n\t"
        "v_mul_f32_e32 v144, 0x3db504f3, v144\n\t"
        "v_mul_f32_e32 v145, 0x3db504f3, v145\n\t"
        "v_mul_f32_e32 v146, 0x3db504f3, v146\n\t"
        "v_mul_f32_e32 v147, 0x3db504f3, v147\n\t"
        "v_cvt_pk_bf16_f32 v144, 0, v144\n\t"
        "v_cvt_pk_bf16_f32 v145, 0, v145\n\t"
        "v_cvt_pk_bf16_f32 v146, 0, v146\n\t"
        "v_cvt_pk_bf16_f32 v147, 0, v147\n\t"
        "v_cndmask_b32_e64 v144, v144, v254, s[56:57]\n\t"
        "v_cndmask_b32_e64 v145, v145, v254, s[56:57]\n\t"
        "v_cndmask_b32_e64 v146, v146, v254, s[56:57]\n\t"
        "v_cndmask_b32_e64 v147, v147, v254, s[56:57]\n\t"
        "v_max3_f32 v172, v172, v144, v145\n\t"
        "v_max3_f32 v173, v172, v146, v147\n\t"
        "v_max3_f32 v172, v172, v146, v147\n\t"
        "s_nop 1\n\t"
        "v_permlane16_swap_b32_e32 v172, v173\n\t"
        "v_max_f32_e32 v172, v172, v173\n\t"
        "v_mov_b32_e32 v173, v172\n\t"
        "s_nop 1\n\t"
        "v_permlane32_swap_b32_e32 v172, v173\n\t"
        "v_max_f32_e32 v172, v172, v173\n\t"
        "v_sub_f32_e32 v136, v136, v172\n\t"
        "v_sub_f32_e32 v137, v137, v172\n\t"
        "v_sub_f32_e32 v138, v138, v172\n\t"
        "v_sub_f32_e32 v139, v139, v172\n\t"
        "v_sub_f32_e32 v140, v140, v172\n\t"
        "v_sub_f32_e32 v144, v144, v172\n\t"
        "v_sub_f32_e32 v145, v145, v172\n\t"
        "v_sub_f32_e32 v146, v146, v172\n\t"
        "v_sub_f32_e32 v147, v147, v172\n\t"
        "v_mul_f32_e32 v136, 0x3fb8aa3b, v136\n\t"
        "v_mul_f32_e32 v137, 0x3fb8aa3b, v137\n\t"
        "v_mul_f32_e32 v138, 0x3fb8aa3b, v138\n\t"
        "v_mul_f32_e32 v139, 0x3fb8aa3b, v139\n\t"
        "v_mul_f32_e32 v140, 0x3fb8aa3b, v140\n\t"
        "v_mul_f32_e32 v144, 0x3fb8aa3b, v144\n\t"
        "v_mul_f32_e32 v145, 0x3fb8aa3b, v145\n\t"
        "v_mul_f32_e32 v146, 0x3fb8aa3b, v146\n\t"
        "v_mul_f32_e32 v147, 0x3fb8aa3b, v147\n\t"
        "v_exp_f32_e32 v136, v136\n\t"
        "v_exp_f32_e32 v137, v137\n\t"
        "v_exp_f32_e32 v138, v138\n\t"
        "v_exp_f32_e32 v139, v139\n\t"
        "v_exp_f32_e32 v140, v140\n\t"
        "v_exp_f32_e32 v144, v144\n\t"
        "v_exp_f32_e32 v145, v145\n\t"
        "v_exp_f32_e32 v146, v146\n\t"
        "v_exp_f32_e32 v147, v147\n\t"
        "v_mul_f32_e32 v172, 4.0, v140\n\t"
        "v_add_f32_e32 v172, v172, v136\n\t"
        "v_add_f32_e32 v172, v172, v137\n\t"
        "v_add_f32_e32 v172, v172, v138\n\t"
        "v_add_f32_e32 v172, v172, v139\n\t"
        "v_add_f32_e32 v172, v172, v144\n\t"
        "v_add_f32_e32 v172, v172, v145\n\t"
        "v_add_f32_e32 v172, v172, v146\n\t"
        "v_add_f32_e32 v172, v172, v147\n\t"
        "v_mov_b32_e32 v173, v172\n\t"
        "s_nop 1\n\t"
        "v_permlane16_swap_b32_e32 v172, v173\n\t"
        "v_add_f32_e32 v172, v172, v173\n\t"
        "v_mov_b32_e32 v173, v172\n\t"
        "s_nop 1\n\t"
        "v_permlane32_swap_b32_e32 v172, v173\n\t"
        "v_add_f32_e32 v172, v172, v173\n\t"
        "v_rcp_f32_e32 v172, v172\n\t"
        "s_nop 0\n\t"
        "v_mul_f32_e32 v136, v136, v172\n\t"
        "v_mul_f32_e32 v137, v137, v172\n\t"
        "v_mul_f32_e32 v138, v138, v172\n\t"
        "v_mul_f32_e32 v139, v139, v172\n\t"
        "v_mul_f32_e32 v140, v140, v172\n\t"
        "v_mul_f32_e32 v144, v144, v172\n\t"
        "v_mul_f32_e32 v145, v145, v172\n\t"
        "v_mul_f32_e32 v146, v146, v172\n\t"
        "v_mul_f32_e32 v147, v147, v172\n\t"
        "v_cvt_pk_bf16_f32 v160, v136, v137\n\t"
        "v_cvt_pk_bf16_f32 v161, v138, v139\n\t"
        "v_cvt_pk_bf16_f32 v162, v140, v140\n\t"
        "v_cvt_pk_bf16_f32 v163, v140, v140\n\t"
        "v_cvt_pk_bf16_f32 v164, v144, v145\n\t"
        "v_cvt_pk_bf16_f32 v165, v146, v147\n\t"
        "v_mfma_f32_16x16x32_bf16 v[176:179], a[64:67], v[160:163], 0\n\t"
        "v_cvt_pk_bf16_f32 v156, 0, v156\n\t"
        "v_cvt_pk_bf16_f32 v157, 0, v157\n\t"
        "v_cvt_pk_bf16_f32 v158, 0, v158\n\t"
        "v_mfma_f32_16x16x32_bf16 v[180:183], a[68:71], v[160:163], 0\n\t"
        "v_cvt_pk_bf16_f32 v159, 0, v159\n\t"
        "v_mul_f32_e32 v156, 0x3db504f3, v156\n\t"
        "v_mul_f32_e32 v157, 0x3db504f3, v157\n\t"
        "v_mfma_f32_16x16x32_bf16 v[184:187], a[72:75], v[160:163], 0\n\t"
        "v_mul_f32_e32 v158, 0x3db504f3, v158\n\t"
        "v_mul_f32_e32 v159, 0x3db504f3, v159\n\t"
        "v_cvt_pk_bf16_f32 v156, 0, v156\n\t"
        "v_mfma_f32_16x16x32_bf16 v[188:191], a[76:79], v[160:163], 0\n\t"
        "v_cvt_pk_bf16_f32 v157, 0, v157\n\t"
        "v_cvt_pk_bf16_f32 v158, 0, v158\n\t"
        "v_cvt_pk_bf16_f32 v159, 0, v159\n\t"
        "v_mfma_f32_16x16x32_bf16 v[192:195], a[80:83], v[160:163], 0\n\t"
        "v_cndmask_b32_e64 v156, v156, v254, s[56:57]\n\t"
        "v_cndmask_b32_e64 v157, v157, v254, s[56:57]\n\t"
        "v_cndmask_b32_e64 v158, v158, v254, s[56:57]\n\t"
        "v_mfma_f32_16x16x32_bf16 v[196:199], a[84:87], v[160:163], 0\n\t"
        "v_cndmask_b32_e64 v159, v159, v254, s[56:57]\n\t"
        "v_max3_f32 v174, v174, v156, v157\n\t"
        "v_max3_f32 v175, v174, v158, v159\n\t"
        "v_mfma_f32_16x16x32_bf16 v[200:203], a[88:91], v[160:163], 0\n\t"
        "v_max3_f32 v174, v174, v158, v159\n\t"
        "s_nop 1\n\t"
        "v_permlane16_swap_b32_e32 v174, v175\n\t"
        "v_max_f32_e32 v174, v174, v175\n\t"
        "v_mfma_f32_16x16x32_bf16 v[204:207], a[92:95], v[160:163], 0\n\t"
        "v_mov_b32_e32 v175, v174\n\t"
        "s_nop 1\n\t"
        "v_permlane32_swap_b32_e32 v174, v175\n\t"
        "v_max_f32_e32 v174, v174, v175\n\t"
        "v_mfma_f32_16x16x16_bf16 v[176:179], a[112:113], v[164:165], v[176:179]\n\t"
        "v_sub_f32_e32 v148, v148, v174\n\t"
        "v_sub_f32_e32 v149, v149, v174\n\t"
        "v_sub_f32_e32 v150, v150, v174\n\t"
        "v_mfma_f32_16x16x16_bf16 v[180:183], a[114:115], v[164:165], v[180:183]\n\t"
        "v_sub_f32_e32 v151, v151, v174\n\t"
        "v_sub_f32_e32 v152, v152, v174\n\t"
        "v_sub_f32_e32 v153, v153, v174\n\t"
        "v_mfma_f32_16x16x16_bf16 v[184:187], a[116:117], v[164:165], v[184:187]\n\t"
        "v_sub_f32_e32 v154, v154, v174\n\t"
        "v_sub_f32_e32 v155, v155, v174\n\t"
        "v_sub_f32_e32 v156, v156, v174\n\t"
        "v_mfma_f32_16x16x16_bf16 v[188:191], a[118:119], v[164:165], v[188:191]\n\t"
        "v_sub_f32_e32 v157, v157, v174\n\t"
        "v_sub_f32_e32 v158, v158, v174\n\t"
        "v_sub_f32_e32 v159, v159, v174\n\t"
        "v_mfma_f32_16x16x16_bf16 v[192:195], a[120:121], v[164:165], v[192:195]\n\t"
        "v_mul_f32_e32 v148, 0x3fb8aa3b, v148\n\t"
        "v_mul_f32_e32 v149, 0x3fb8aa3b, v149\n\t"
        "v_mul_f32_e32 v150, 0x3fb8aa3b, v150\n\t"
        "v_mfma_f32_16x16x16_bf16 v[196:199], a[122:123], v[164:165], v[196:199]\n\t"
        "v_mul_f32_e32 v151, 0x3fb8aa3b, v151\n\t"
        "v_mul_f32_e32 v152, 0x3fb8aa3b, v152\n\t"
        "v_mul_f32_e32 v153, 0x3fb8aa3b, v153\n\t"
        "v_mfma_f32_16x16x16_bf16 v[200:203], a[124:125], v[164:165], v[200:203]\n\t"
        "v_mul_f32_e32 v154, 0x3fb8aa3b, v154\n\t"
        "v_mul_f32_e32 v155, 0x3fb8aa3b, v155\n\t"
        "v_mul_f32_e32 v156, 0x3fb8aa3b, v156\n\t"
        "v_mfma_f32_16x16x16_bf16 v[204:207], a[126:127], v[164:165], v[204:207]\n\t"
        "v_mul_f32_e32 v157, 0x3fb8aa3b, v157\n\t"
        "v_mul_f32_e32 v158, 0x3fb8aa3b, v158\n\t"
        "v_mul_f32_e32 v159, 0x3fb8aa3b, v159\n\t"
        "v_exp_f32_e32 v148, v148\n\t"
        "v_exp_f32_e32 v149, v149\n\t"
        "v_exp_f32_e32 v150, v150\n\t"
        "v_exp_f32_e32 v151, v151\n\t"
        "v_exp_f32_e32 v152, v152\n\t"
        "v_exp_f32_e32 v153, v153\n\t"
        "v_exp_f32_e32 v154, v154\n\t"
        "v_exp_f32_e32 v155, v155\n\t"
        "v_exp_f32_e32 v156, v156\n\t"
        "v_exp_f32_e32 v157, v157\n\t"
        "v_exp_f32_e32 v158, v158\n\t"
        "v_exp_f32_e32 v159, v159\n\t"
        "v_add_f32_e32 v174, v148, v149\n\t"
        "v_add_f32_e32 v174, v174, v150\n\t"
        "v_add_f32_e32 v174, v174, v151\n\t"
        "v_add_f32_e32 v174, v174, v152\n\t"
        "v_add_f32_e32 v174, v174, v153\n\t"
        "v_add_f32_e32 v174, v174, v154\n\t"
        "v_add_f32_e32 v174, v174, v155\n\t"
        "v_add_f32_e32 v174, v174, v156\n\t"
        "v_add_f32_e32 v174, v174, v157\n\t"
        "v_add_f32_e32 v174, v174, v158\n\t"
        "v_add_f32_e32 v174, v174, v159\n\t"
        "v_mov_b32_e32 v175, v174\n\t"
        "s_nop 1\n\t"
        "v_permlane16_swap_b32_e32 v174, v175\n\t"
        "v_add_f32_e32 v174, v174, v175\n\t"
        "v_mov_b32_e32 v175, v174\n\t"
        "s_nop 1\n\t"
        "v_permlane32_swap_b32_e32 v174, v175\n\t"
        "v_add_f32_e32 v174, v174, v175\n\t"
        "v_rcp_f32_e32 v174, v174\n\t"
        "s_nop 0\n\t"
        "v_mul_f32_e32 v148, v148, v174\n\t"
        "v_mul_f32_e32 v149, v149, v174\n\t"
        "v_mul_f32_e32 v150, v150, v174\n\t"
        "v_mul_f32_e32 v151, v151, v174\n\t"
        "v_mul_f32_e32 v152, v152, v174\n\t"
        "v_mul_f32_e32 v153, v153, v174\n\t"
        "v_mul_f32_e32 v154, v154, v174\n\t"
        "v_mul_f32_e32 v155, v155, v174\n\t"
        "v_mul_f32_e32 v156, v156, v174\n\t"
        "v_mul_f32_e32 v157, v157, v174\n\t"
        "v_mul_f32_e32 v158, v158, v174\n\t"
        "v_mul_f32_e32 v159, v159, v174\n\t"
        "v_cvt_pk_bf16_f32 v166, v148, v149\n\t"
        "v_cvt_pk_bf16_f32 v167, v150, v151\n\t"
        "v_cvt_pk_bf16_f32 v168, v152, v153\n\t"
        "v_cvt_pk_bf16_f32 v169, v154, v155\n\t"
        "v_cvt_pk_bf16_f32 v170, v156, v157\n\t"
        "v_cvt_pk_bf16_f32 v171, v158, v159\n\t"
        "s_waitcnt vmcnt(40)\n\t"
        "s_lshl_b32 s80, s44, 7\n\t"
        "s_cmp_lt_i32 s44, 0\n\t"
        "s_cselect_b32 s80, s36, s80\n\t"
        "v_mov_b32_e32 v242, 1\n\t"
        "buffer_atomic_add v242, v250, %[rd], s80 offen sc0 sc1\n\t"
        "s_lshl_b32 s86, s37, 18\n\t"
        "s_add_u32 s86, s86, %[qx]\n\t"
        "s_add_u32 s87, s86, 0x1000\n\t"
        "v_mfma_f32_16x16x32_bf16 v[208:211], a[64:67], v[166:169], 0\n\t"
        "v_cvt_pk_bf16_f32 v176, v176, v177\n\t"
        "v_cvt_pk_bf16_f32 v177, v178, v179\n\t"
        "v_mfma_f32_16x16x32_bf16 v[212:215], a[68:71], v[166:169], 0\n\t"
        "v_cvt_pk_bf16_f32 v178, v180, v181\n\t"
        "v_cvt_pk_bf16_f32 v179, v182, v183\n\t"
        "v_mfma_f32_16x16x32_bf16 v[216:219], a[72:75], v[166:169], 0\n\t"
        "buffer_store_dwordx4 v[176:179], v244, %[rd], s86 offen offset:0 sc1\n\t"
        "v_cvt_pk_bf16_f32 v184, v184, v185\n\t"
        "v_mfma_f32_16x16x32_bf16 v[220:223], a[76:79], v[166:169], 0\n\t"
        "v_cvt_pk_bf16_f32 v185, v186, v187\n\t"
        "v_cvt_pk_bf16_f32 v186, v188, v189\n\t"
        "v_mfma_f32_16x16x32_bf16 v[224:227], a[80:83], v[166:169], 0\n\t"
        "v_cvt_pk_bf16_f32 v187, v190, v191\n\t"
        "buffer_store_dwordx4 v[184:187], v244, %[rd], s86 offen offset:1024 sc1\n\t"
        "v_mfma_f32_16x16x32_bf16 v[228:231], a[84:87], v[166:169], 0\n\t"
        "v_cvt_pk_bf16_f32 v192, v192, v193\n\t"
        "v_cvt_pk_bf16_f32 v193, v194, v195\n\t"
        "v_mfma_f32_16x16x32_bf16 v[232:235], a[88:91], v[166:169], 0\n\t"
        "v_cvt_pk_bf16_f32 v194, v196, v197\n\t"
        "v_cvt_pk_bf16_f32 v195, v198, v199\n\t"
        "v_mfma_f32_16x16x32_bf16 v[236:239], a[92:95], v[166:169], 0\n\t"
        "buffer_store_dwordx4 v[192:195], v244, %[rd], s86 offen offset:2048 sc1\n\t"
        "v_cvt_pk_bf16_f32 v200, v200, v201\n\t"
        "v_mfma_f32_16x16x16_bf16 v[208:211], a[112:113], v[170:171], v[208:211]\n\t"
        "v_cvt_pk_bf16_f32 v201, v202, v203\n\t"
        "v_cvt_pk_bf16_f32 v202, v204, v205\n\t"
        "v_mfma_f32_16x16x16_bf16 v[212:215], a[114:115], v[170:171], v[212:215]\n\t"
        "v_cvt_pk_bf16_f32 v203, v206, v207\n\t"
        "buffer_store_dwordx4 v[200:203], v244, %[rd], s86 offen offset:3072 sc1\n\t"
        "v_mfma_f32_16x16x16_bf16 v[216:219], a[116:117], v[170:171], v[216:219]\n\t"
        "v_mfma_f32_16x16x16_bf16 v[220:223], a[118:119], v[170:171], v[220:223]\n\t"
        "v_mfma_f32_16x16x16_bf16 v[224:227], a[120:121], v[170:171], v[224:227]\n\t"
        "v_mfma_f32_16x16x16_bf16 v[228:231], a[122:123], v[170:171], v[228:231]\n\t"
        "v_mfma_f32_16x16x16_bf16 v[232:235], a[124:125], v[170:171], v[232:235]\n\t"
        "v_mfma_f32_16x16x16_bf16 v[236:239], a[126:127], v[170:171], v[236:239]\n\t"
        "v_cvt_pk_bf16_f32 v208, v208, v209\n\t"
        "v_cvt_pk_bf16_f32 v209, v210, v211\n\t"
        "v_cvt_pk_bf16_f32 v210, v212, v213\n\t"
        "v_cvt_pk_bf16_f32 v211, v214, v215\n\t"
        "buffer_store_dwordx4 v[208:211], v244, %[rd], s87 offen offset:0 sc1\n\t"
        "v_cvt_pk_bf16_f32 v216, v216, v217\n\t"
        "v_cvt_pk_bf16_f32 v217, v218, v219\n\t"
        "v_cvt_pk_bf16_f32 v218, v220, v221\n\t"
        "v_cvt_pk_bf16_f32 v219, v222, v223\n\t"
        "buffer_store_dwordx4 v[216:219], v244, %[rd], s87 offen offset:1024 sc1\n\t"
        "v_cvt_pk_bf16_f32 v224, v224, v225\n\t"
        "v_cvt_pk_bf16_f32 v225, v226, v227\n\t"
        "v_cvt_pk_bf16_f32 v226, v228, v229\n\t"
        "v_cvt_pk_bf16_f32 v227, v230, v231\n\t"
        "buffer_store_dwordx4 v[224:227], v244, %[rd], s87 offen offset:2048 sc1\n\t"
        "v_cvt_pk_bf16_f32 v232, v232, v233\n\t"
        "v_cvt_pk_bf16_f32 v233, v234, v235\n\t"
        "v_cvt_pk_bf16_f32 v234, v236, v237\n\t"
        "v_cvt_pk_bf16_f32 v235, v238, v239\n\t"
        "buffer_store_dwordx4 v[232:235], v244, %[rd], s87 offen offset:3072 sc1\n\t"
        "s_mov_b32 s45, s44\n\t"
        "s_mov_b32 s44, s37\n\t"
        "s_add_u32 s80, s37, 4\n\t"
        "s_cmp_gt_u32 s40, s80\n\t"
        "s_cselect_b32 s80, s46, 1\n\t"
        "s_cmp_eq_u32 s80, 0\n\t"
        "s_cbranch_scc1 .La12%=\n\t"
        "s_waitcnt vmcnt(0)\n\t"
        "v_readlane_b32 s84, v242, 0\n\t"
        "v_readlane_b32 s85, v242, 32\n\t"
        "v_mov_b32_e32 v243, 32\n\t"
        "s_and_b32 s80, s45, 3\n\t"
        "s_lshl_b32 s80, s80, 12\n\t"
        "s_lshr_b32 s81, s45, 2\n\t"
        "s_lshl_b32 s81, s81, 3\n\t"
        "s_add_u32 s80, s80, s81\n\t"
        "s_lshr_b32 s82, s45, 1\n\t"
        "s_xnor_b32 s81, s45, s82\n\t"
        "s_and_b32 s81, s81, 1\n\t"
        "s_lshl_b32 s81, s81, 13\n\t"
        "s_lshl_b32 s82, s82, 2\n\t"
        "s_add_u32 s81, s81, s82\n\t"
        "s_cmp_lt_i32 s45, 60\n\t"
        "s_cselect_b32 s80, s80, s81\n\t"
        "s_cselect_b32 s81, 4, 0x1000\n\t"
        "s_add_u32 s81, s81, s80\n\t"
        "s_cmp_eq_u32 s84, 31\n\t"
        "s_cselect_b32 s80, s80, s36\n\t"
        "s_cmp_eq_u32 s85, 31\n\t"
        "s_cselect_b32 s81, s81, s36\n\t"
        "s_cmp_lt_i32 s45, 0\n\t"
        "s_cselect_b32 s80, s36, s80\n\t"
        "s_cselect_b32 s81, s36, s81\n\t"
        "buffer_store_dword v243, v251, %[rd], s80 offen sc1\n\t"
        "buffer_store_dword v243, v251, %[rd], s81 offen sc1\n\t"
        "s_lshl_b32 s80, s44, 7\n\t"
        "s_cmp_lt_i32 s44, 0\n\t"
        "s_cselect_b32 s80, s36, s80\n\t"
        "v_mov_b32_e32 v242, 1\n\t"
        "buffer_atomic_add v242, v250, %[rd], s80 offen sc0 sc1\n\t"
        "s_waitcnt vmcnt(0)\n\t"
        "v_readlane_b32 s84, v242, 0\n\t"
        "v_readlane_b32 s85, v242, 32\n\t"
        "v_mov_b32_e32 v243, 32\n\t"
        "s_and_b32 s80, s44, 3\n\t"
        "s_lshl_b32 s80, s80, 12\n\t"
        "s_lshr_b32 s81, s44, 2\n\t"
        "s_lshl_b32 s81, s81, 3\n\t"
        "s_add_u32 s80, s80, s81\n\t"
        "s_lshr_b32 s82, s44, 1\n\t"
        "s_xnor_b32 s81, s44, s82\n\t"
        "s_and_b32 s81, s81, 1\n\t"
        "s_lshl_b32 s81, s81, 13\n\t"
        "s_lshl_b32 s82, s82, 2\n\t"
        "s_add_u32 s81, s81, s82\n\t"
        "s_cmp_lt_i32 s44, 60\n\t"
        "s_cselect_b32 s80, s80, s81\n\t"
        "s_cselect_b32 s81, 4, 0x1000\n\t"
        "s_add_u32 s81, s81, s80\n\t"
        "s_cmp_eq_u32 s84, 31\n\t"
        "s_cselect_b32 s80, s80, s36\n\t"
        "s_cmp_eq_u32 s85, 31\n\t"
        "s_cselect_b32 s81, s81, s36\n\t"
        "s_cmp_lt_i32 s44, 0\n\t"
        "s_cselect_b32 s80, s36, s80\n\t"
        "s_cselect_b32 s81, s36, s81\n\t"
        "buffer_store_dword v243, v251, %[rd], s80 offen sc1\n\t"
        "buffer_store_dword v243, v251, %[rd], s81 offen sc1\n\t"
        "s_mov_b32 s44, -1\n\t"
        "s_mov_b32 s45, -1\n\t"
        ".La12%=:\n\t"
        "s_mov_b32 s38, s46\n\t"
        "s_mov_b32 s39, s47\n\t"
        "s_add_u32 s37, s37, 4\n\t"
        "s_cmp_eq_u32 s38, 0\n\t"
        "s_cbranch_scc1 .La14%=\n\t"
        "s_mov_b32 s97, 0\n\t"
        ".La13%=:\n\t"
        "buffer_load_dword v240, v246, %[rd], 0 offen sc1\n\t"
        "buffer_load_dword v241, v247, %[rd], 0 offen sc1\n\t"
        "s_add_u32 s97, s97, 1\n\t"
        "s_cmp_lt_u32 s97, 0x200000\n\t"
        "s_cbranch_scc0 .Lgu%=\n\t"
        "s_memrealtime s[98:99]\n\t"
        "s_waitcnt vmcnt(0) lgkmcnt(0)\n\t"
        "v_cmp_eq_u32_e32 vcc, %[tag], v240\n\t"
        "s_nop 1\n\t"
        "s_or_b64 s[42:43], s[42:43], vcc\n\t"
        "v_readfirstlane_b32 s80, v241\n\t"
        "s_nop 0\n\t"
        "s_cmp_gt_u32 s80, s40\n\t"
        "s_cselect_b32 s41, s98, s41\n\t"
        "s_max_u32 s40, s40, s80\n\t"
        "s_and_b32 s80, s37, 60\n\t"
        "s_lshr_b64 s[80:81], s[42:43], s80\n\t"
        "s_andn2_b32 s80, 15, s80\n\t"
        "s_cbranch_scc0 .La13%=x\n\t"
        "s_sleep 16\n\t"
        "s_branch .La13%=\n\t"
        ".La13%=x:\n\t"
        "s_cmp_gt_u32 s40, s37\n\t"
        "s_cselect_b32 s39, 0, s36\n\t"
        "s_lshl_b32 s80, s37, 18\n\t"
        "s_or_b32 s80, s80, 0\n\t"
        "s_lshl_b32 s81, s37, 17\n\t"
        "s_add_u32 s81, s81, %[km]\n\t"
        "s_or_b32 s81, s81, s39\n\t"
        "s_add_u32 s82, s80, %[kx]\n\t"
        "s_add_u32 s83, s82, 0x1000\n\t"
        "buffer_load_dwordx4 v[0:3], v244, %[rd], s82 offen offset:0 sc1\n\t"
        "buffer_load_dwordx4 v[4:7], v244, %[rd], s82 offen offset:1024 sc1\n\t"
        "buffer_load_dwordx4 v[8:11], v244, %[rd], s82 offen offset:2048 sc1\n\t"
        "buffer_load_dwordx4 v[12:15], v244, %[rd], s82 offen offset:3072 sc1\n\t"
        "buffer_load_dwordx4 v[16:19], v244, %[rd], s83 offen offset:0 sc1\n\t"
        "buffer_load_dwordx4 v[20:23], v244, %[rd], s83 offen offset:1024 sc1\n\t"
        "buffer_load_dwordx4 v[24:27], v244, %[rd], s83 offen offset:2048 sc1\n\t"
        "buffer_load_dwordx4 v[28:31], v244, %[rd], s83 offen offset:3072 sc1\n\t"
        "s_cmp_eq_u64 s[42:43], -1\n\t"
        "s_cbranch_scc1 .La15%=\n\t"
        "s_sleep %c[aslp]\n\t"
        ".La15%=:\n\t"
        "s_sleep %c[bslp]\n\t"
        "s_add_u32 s82, s80, %[qx]\n\t"
        "s_add_u32 s83, s82, 0x1000\n\t"
        "buffer_load_dwordx4 v[32:35], v244, %[rd], s82 offen offset:0 sc1\n\t"
        "buffer_load_dwordx4 v[36:39], v244, %[rd], s82 offen offset:1024 sc1\n\t"
        "buffer_load_dwordx4 v[40:43], v244, %[rd], s82 offen offset:2048 sc1\n\t"
        "buffer_load_dwordx4 v[44:47], v244, %[rd], s82 offen offset:3072 sc1\n\t"
        "buffer_load_dwordx4 v[48:51], v244, %[rd], s83 offen offset:0 sc1\n\t"
        "buffer_load_dwordx4 v[52:55], v244, %[rd], s83 offen offset:1024 sc1\n\t"
        "buffer_load_dwordx4 v[56:59], v244, %[rd], s83 offen offset:2048 sc1\n\t"
        "buffer_load_dwordx4 v[60:63], v244, %[rd], s83 offen offset:3072 sc1\n\t"
        "s_cmp_eq_u64 s[42:43], -1\n\t"
        "s_cbranch_scc1 .La16%=\n\t"
        "s_sleep %c[aslp]\n\t"
        ".La16%=:\n\t"
        "s_sleep %c[bslp]\n\t"
        "buffer_load_dwordx4 v[96:99], v248, %[rd], s81 offen offset:0 sc1\n\t"
        "buffer_load_dwordx4 v[100:103], v248, %[rd], s81 offen offset:512 sc1\n\t"
        "buffer_load_dwordx4 v[104:107], v248, %[rd], s81 offen offset:1024 sc1\n\t"
        "buffer_load_dwordx4 v[108:111], v248, %[rd], s81 offen offset:1536 sc1\n\t"
        "buffer_load_dwordx2 v[112:113], v245, %[rd], s81 offen offset:2048 sc1\n\t"
        "buffer_load_dwordx2 v[114:115], v245, %[rd], s81 offen offset:2304 sc1\n\t"
        "buffer_load_dwordx2 v[116:117], v245, %[rd], s81 offen offset:2560 sc1\n\t"
        "buffer_load_dwordx2 v[118:119], v245, %[rd], s81 offen offset:2816 sc1\n\t"
        "buffer_load_dwordx2 v[120:121], v245, %[rd], s81 offen offset:3072 sc1\n\t"
        "buffer_load_dwordx2 v[122:123], v245, %[rd], s81 offen offset:3328 sc1\n\t"
        "buffer_load_dwordx2 v[124:125], v245, %[rd], s81 offen offset:3584 sc1\n\t"
        "buffer_load_dwordx2 v[126:127], v245, %[rd], s81 offen offset:3840 sc1\n\t"
        "s_add_u32 s82, s80, %[vx]\n\t"
        "s_add_u32 s83, s82, 0x1000\n\t"
        "buffer_load_dwordx4 v[64:67], v244, %[rd], s82 offen offset:0 sc1\n\t"
        "buffer_load_dwordx4 v[68:71], v244, %[rd], s82 offen offset:1024 sc1\n\t"
        "buffer_load_dwordx4 v[72:75], v244, %[rd], s82 offen offset:2048 sc1\n\t"
        "buffer_load_dwordx4 v[76:79], v244, %[rd], s82 offen offset:3072 sc1\n\t"
        "buffer_load_dwordx4 v[80:83], v244, %[rd], s83 offen offset:0 sc1\n\t"
        "buffer_load_dwordx4 v[84:87], v244, %[rd], s83 offen offset:1024 sc1\n\t"
        "buffer_load_dwordx4 v[88:91], v244, %[rd], s83 offen offset:2048 sc1\n\t"
        "buffer_load_dwordx4 v[92:95], v244, %[rd], s83 offen offset:3072 sc1\n\t"
        "s_cmp_eq_u64 s[42:43], -1\n\t"
        "s_cbranch_scc1 .La17%=\n\t"
        "s_sleep %c[aslp]\n\t"
        ".La17%=:\n\t"
        "s_sleep %c[bslp]\n\t"
        "buffer_store_dword v0, off, %[rd], s36\n\t"
        "buffer_store_dword v0, off, %[rd], s36\n\t"
        "buffer_store_dword v0, off, %[rd], s36\n\t"
        "buffer_store_dword v0, off, %[rd], s36\n\t"
        "buffer_store_dword v0, off, %[rd], s36\n\t"
        "buffer_store_dword v0, off, %[rd], s36\n\t"
        "buffer_store_dword v0, off, %[rd], s36\n\t"
        "buffer_store_dword v0, off, %[rd], s36\n\t"
        "buffer_store_dword v0, off, %[rd], s36\n\t"
        ".La14%=:\n\t"
        "s_cmp_gt_u32 s40, s37\n\t"
        "s_cselect_b32 s80, s39, 0\n\t"
        "s_cmp_eq_u32 s80, 0\n\t"
        "s_cbranch_scc1 .La18%=\n\t"
        "s_mov_b32 s39, 0\n\t"
        "s_lshl_b32 s81, s37, 17\n\t"
        "s_add_u32 s81, s81, %[km]\n\t"
        "buffer_load_dwordx4 v[96:99], v248, %[rd], s81 offen offset:0 sc1\n\t"
        "buffer_load_dwordx4 v[100:103], v248, %[rd], s81 offen offset:512 sc1\n\t"
        "buffer_load_dwordx4 v[104:107], v248, %[rd], s81 offen offset:1024 sc1\n\t"
        "buffer_load_dwordx4 v[108:111], v248, %[rd], s81 offen offset:1536 sc1\n\t"
        "buffer_load_dwordx2 v[112:113], v245, %[rd], s81 offen offset:2048 sc1\n\t"
        "buffer_load_dwordx2 v[114:115], v245, %[rd], s81 offen offset:2304 sc1\n\t"
        "buffer_load_dwordx2 v[116:117], v245, %[rd], s81 offen offset:2560 sc1\n\t"
        "buffer_load_dwordx2 v[118:119], v245, %[rd], s81 offen offset:2816 sc1\n\t"
        "buffer_load_dwordx2 v[120:121], v245, %[rd], s81 offen offset:3072 sc1\n\t"
        "buffer_load_dwordx2 v[122:123], v245, %[rd], s81 offen offset:3328 sc1\n\t"
        "buffer_load_dwordx2 v[124:125], v245, %[rd], s81 offen offset:3584 sc1\n\t"
        "buffer_load_dwordx2 v[126:127], v245, %[rd], s81 offen offset:3840 sc1\n\t"
        "buffer_store_dword v0, off, %[rd], s36\n\t"
        "buffer_store_dword v0, off, %[rd], s36\n\t"
        "buffer_store_dword v0, off, %[rd], s36\n\t"
        "buffer_store_dword v0, off, %[rd], s36\n\t"
        "buffer_store_dword v0, off, %[rd], s36\n\t"
        "buffer_store_dword v0, off, %[rd], s36\n\t"
        "buffer_store_dword v0, off, %[rd], s36\n\t"
        "buffer_store_dword v0, off, %[rd], s36\n\t"
        ".La18%=:\n\t"
        "s_add_u32 s86, s37, 4\n\t"
        "s_and_b32 s80, s86, 60\n\t"
        "s_lshr_b64 s[80:81], s[42:43], s80\n\t"
        "s_andn2_b32 s80, 15, s80\n\t"
        "s_cselect_b32 s46, s36, 0\n\t"
        "s_cmp_lt_u32 s86, 64\n\t"
        "s_cselect_b32 s46, s46, s36\n\t"
        "s_cmp_gt_u32 s40, s86\n\t"
        "s_cselect_b32 s47, s46, s36\n\t"
        "s_lshl_b32 s80, s86, 18\n\t"
        "s_or_b32 s80, s80, s46\n\t"
        "s_lshl_b32 s81, s86, 17\n\t"
        "s_add_u32 s81, s81, %[km]\n\t"
        "s_or_b32 s81, s81, s47\n\t"
        "s_add_u32 s82, s80, %[kx]\n\t"
        "s_add_u32 s83, s82, 0x1000\n\t"
        "buffer_load_dwordx4 a[0:3], v244, %[rd], s82 offen offset:0 sc1\n\t"
        "buffer_load_dwordx4 a[4:7], v244, %[rd], s82 offen offset:1024 sc1\n\t"
        "buffer_load_dwordx4 a[8:11], v244, %[rd], s82 offen offset:2048 sc1\n\t"
        "buffer_load_dwordx4 a[12:15], v244, %[rd], s82 offen offset:3072 sc1\n\t"
        "buffer_load_dwordx4 a[16:19], v244, %[rd], s83 offen offset:0 sc1\n\t"
        "buffer_load_dwordx4 a[20:23], v244, %[rd], s83 offen offset:1024 sc1\n\t"
        "buffer_load_dwordx4 a[24:27], v244, %[rd], s83 offen offset:2048 sc1\n\t"
        "buffer_load_dwordx4 a[28:31], v244, %[rd], s83 offen offset:3072 sc1\n\t"
        "s_cmp_eq_u64 s[42:43], -1\n\t"
        "s_cbranch_scc1 .La19%=\n\t"
        "s_sleep %c[aslp]\n\t"
        ".La19%=:\n\t"
        "s_sleep %c[bslp]\n\t"
        "s_add_u32 s82, s80, %[qx]\n\t"
        "s_add_u32 s83, s82, 0x1000\n\t"
        "buffer_load_dwordx4 a[32:35], v244, %[rd], s82 offen offset:0 sc1\n\t"
        "buffer_load_dwordx4 a[36:39], v244, %[rd], s82 offen offset:1024 sc1\n\t"
        "buffer_load_dwordx4 a[40:43], v244, %[rd], s82 offen offset:2048 sc1\n\t"
        "buffer_load_dwordx4 a[44:47], v244, %[rd], s82 offen offset:3072 sc1\n\t"
        "buffer_load_dwordx4 a[48:51], v244, %[rd], s83 offen offset:0 sc1\n\t"
        "buffer_load_dwordx4 a[52:55], v244, %[rd], s83 offen offset:1024 sc1\n\t"
        "buffer_load_dwordx4 a[56:59], v244, %[rd], s83 offen offset:2048 sc1\n\t"
        "buffer_load_dwordx4 a[60:63], v244, %[rd], s83 offen offset:3072 sc1\n\t"
        "s_cmp_eq_u64 s[42:43], -1\n\t"
        "s_cbranch_scc1 .La20%=\n\t"
        "s_sleep %c[aslp]\n\t"
        ".La20%=:\n\t"
        "s_sleep %c[bslp]\n\t"
        "buffer_load_dwordx4 a[96:99], v248, %[rd], s81 offen offset:0 sc1\n\t"
        "buffer_load_dwordx4 a[100:103], v248, %[rd], s81 offen offset:512 sc1\n\t"
        "buffer_load_dwordx4 a[104:107], v248, %[rd], s81 offen offset:1024 sc1\n\t"
        "buffer_load_dwordx4 a[108:111], v248, %[rd], s81 offen offset:1536 sc1\n\t"
        "buffer_load_dwordx2 a[112:113], v245, %[rd], s81 offen offset:2048 sc1\n\t"
        "buffer_load_dwordx2 a[114:115], v245, %[rd], s81 offen offset:2304 sc1\n\t"
        "buffer_load_dwordx2 a[116:117], v245, %[rd], s81 offen offset:2560 sc1\n\t"
        "buffer_load_dwordx2 a[118:119], v245, %[rd], s81 offen offset:2816 sc1\n\t"
        "buffer_load_dwordx2 a[120:121], v245, %[rd], s81 offen offset:3072 sc1\n\t"
        "buffer_load_dwordx2 a[122:123], v245, %[rd], s81 offen offset:3328 sc1\n\t"
        "buffer_load_dwordx2 a[124:125], v245, %[rd], s81 offen offset:3584 sc1\n\t"
        "buffer_load_dwordx2 a[126:127], v245, %[rd], s81 offen offset:3840 sc1\n\t"
        "s_add_u32 s82, s80, %[vx]\n\t"
        "s_add_u32 s83, s82, 0x1000\n\t"
        "buffer_load_dwordx4 a[64:67], v244, %[rd], s82 offen offset:0 sc1\n\t"
        "buffer_load_dwordx4 a[68:71], v244, %[rd], s82 offen offset:1024 sc1\n\t"
        "buffer_load_dwordx4 a[72:75], v244, %[rd], s82 offen offset:2048 sc1\n\t"
        "buffer_load_dwordx4 a[76:79], v244, %[rd], s82 offen offset:3072 sc1\n\t"
        "buffer_load_dwordx4 a[80:83], v244, %[rd], s83 offen offset:0 sc1\n\t"
        "buffer_load_dwordx4 a[84:87], v244, %[rd], s83 offen offset:1024 sc1\n\t"
        "buffer_load_dwordx4 a[88:91], v244, %[rd], s83 offen offset:2048 sc1\n\t"
        "buffer_load_dwordx4 a[92:95], v244, %[rd], s83 offen offset:3072 sc1\n\t"
        "s_cmp_eq_u64 s[42:43], -1\n\t"
        "s_cbranch_scc1 .La21%=\n\t"
        "s_sleep %c[aslp]\n\t"
        ".La21%=:\n\t"
        "s_sleep %c[bslp]\n\t"
        "s_waitcnt vmcnt(44)\n\t"
        "s_memrealtime s[98:99]\n\t"
        "v_mfma_f32_16x16x32_bf16 v[136:139], v[0:3], v[32:35], 0\n\t"
        "v_mfma_f32_16x16x32_bf16 v[148:151], v[0:3], v[48:51], 0\n\t"
        "v_mfma_f32_16x16x32_bf16 v[152:155], v[16:19], v[48:51], 0\n\t"
        "v_mfma_f32_16x16x32_bf16 v[136:139], v[4:7], v[36:39], v[136:139]\n\t"
        "v_mfma_f32_16x16x32_bf16 v[148:151], v[4:7], v[52:55], v[148:151]\n\t"
        "v_mfma_f32_16x16x32_bf16 v[152:155], v[20:23], v[52:55], v[152:155]\n\t"
        "v_mfma_f32_16x16x32_bf16 v[136:139], v[8:11], v[40:43], v[136:139]\n\t"
        "v_mfma_f32_16x16x32_bf16 v[148:151], v[8:11], v[56:59], v[148:151]\n\t"
        "v_mfma_f32_16x16x32_bf16 v[152:155], v[24:27], v[56:59], v[152:155]\n\t"
        "v_mfma_f32_16x16x32_bf16 v[136:139], v[12:15], v[44:47], v[136:139]\n\t"
        "v_mfma_f32_16x16x32_bf16 v[148:151], v[12:15], v[60:63], v[148:151]\n\t"
        "v_mfma_f32_16x16x32_bf16 v[152:155], v[28:31], v[60:63], v[152:155]\n\t"
        "s_waitcnt lgkmcnt(0)\n\t"
        "v_cmp_eq_u32_e32 vcc, %[tag], v240\n\t"
        "s_nop 1\n\t"
        "s_or_b64 s[42:43], s[42:43], vcc\n\t"
        "v_readfirstlane_b32 s80, v241\n\t"
        "s_nop 0\n\t"
        "s_cmp_gt_u32 s80, s40\n\t"
        "s_cselect_b32 s41, s98, s41\n\t"
        "s_max_u32 s40, s40, s80\n\t"
        "v_readlane_b32 s84, v242, 0\n\t"
        "v_readlane_b32 s85, v242, 32\n\t"
        "v_mov_b32_e32 v243, 32\n\t"
        "s_and_b32 s80, s45, 3\n\t"
        "s_lshl_b32 s80, s80, 12\n\t"
        "s_lshr_b32 s81, s45, 2\n\t"
        "s_lshl_b32 s81, s81, 3\n\t"
        "s_add_u32 s80, s80, s81\n\t"
        "s_lshr_b32 s82, s45, 1\n\t"
        "s_xnor_b32 s81, s45, s82\n\t"
        "s_and_b32 s81, s81, 1\n\t"
        "s_lshl_b32 s81, s81, 13\n\t"
        "s_lshl_b32 s82, s82, 2\n\t"
        "s_add_u32 s81, s81, s82\n\t"
        "s_cmp_lt_i32 s45, 60\n\t"
        "s_cselect_b32 s80, s80, s81\n\t"
        "s_cselect_b32 s81, 4, 0x1000\n\t"
        "s_add_u32 s81, s81, s80\n\t"
        "s_cmp_eq_u32 s84, 31\n\t"
        "s_cselect_b32 s80, s80, s36\n\t"
        "s_cmp_eq_u32 s85, 31\n\t"
        "s_cselect_b32 s81, s81, s36\n\t"
        "s_cmp_lt_i32 s45, 0\n\t"
        "s_cselect_b32 s80, s36, s80\n\t"
        "s_cselect_b32 s81, s36, s81\n\t"
        "buffer_store_dword v243, v251, %[rd], s80 offen sc1\n\t"
        "buffer_store_dword v243, v251, %[rd], s81 offen sc1\n\t"
        "s_add_u32 s86, s37, 8\n\t"
        "s_cmp_lg_u64 s[42:43], -1\n\t"
        "s_cselect_b32 s84, 0, s36\n\t"
        "s_cmp_gt_u32 s40, s86\n\t"
        "s_cselect_b32 s85, s36, s39\n\t"
        "buffer_load_dword v240, v246, %[rd], s84 offen sc1\n\t"
        "buffer_load_dword v241, v247, %[rd], s85 offen sc1\n\t"
        "v_cvt_pk_bf16_f32 v136, 0, v136\n\t"
        "v_cvt_pk_bf16_f32 v137, 0, v137\n\t"
        "v_cvt_pk_bf16_f32 v138, 0, v138\n\t"
        "v_cvt_pk_bf16_f32 v139, 0, v139\n\t"
        "v_mul_f32_e32 v136, 0x3db504f3, v136\n\t"
        "v_mul_f32_e32 v137, 0x3db504f3, v137\n\t"
        "v_mul_f32_e32 v138, 0x3db504f3, v138\n\t"
        "v_mul_f32_e32 v139, 0x3db504f3, v139\n\t"
        "v_cvt_pk_bf16_f32 v136, 0, v136\n\t"
        "v_cvt_pk_bf16_f32 v137, 0, v137\n\t"
        "v_cvt_pk_bf16_f32 v138, 0, v138\n\t"
        "v_cvt_pk_bf16_f32 v139, 0, v139\n\t"
        "v_cndmask_b32_e64 v136, v253, v136, s[48:49]\n\t"
        "v_cndmask_b32_e64 v137, v253, v137, s[50:51]\n\t"
        "v_cndmask_b32_e64 v138, v253, v138, s[52:53]\n\t"
        "v_cndmask_b32_e64 v139, v253, v139, s[54:55]\n\t"
        "v_cvt_pk_bf16_f32 v148, 0, v148\n\t"
        "v_cvt_pk_bf16_f32 v149, 0, v149\n\t"
        "v_cvt_pk_bf16_f32 v150, 0, v150\n\t"
        "v_cvt_pk_bf16_f32 v151, 0, v151\n\t"
        "v_mul_f32_e32 v148, 0x3db504f3, v148\n\t"
        "v_mul_f32_e32 v149, 0x3db504f3, v149\n\t"
        "v_mul_f32_e32 v150, 0x3db504f3, v150\n\t"
        "v_mul_f32_e32 v151, 0x3db504f3, v151\n\t"
        "v_cvt_pk_bf16_f32 v148, 0, v148\n\t"
        "v_cvt_pk_bf16_f32 v149, 0, v149\n\t"
        "v_cvt_pk_bf16_f32 v150, 0, v150\n\t"
        "v_cvt_pk_bf16_f32 v151, 0, v151\n\t"
        "v_cvt_pk_bf16_f32 v152, 0, v152\n\t"
        "v_cvt_pk_bf16_f32 v153, 0, v153\n\t"
        "v_cvt_pk_bf16_f32 v154, 0, v154\n\t"
        "v_cvt_pk_bf16_f32 v155, 0, v155\n\t"
        "v_mul_f32_e32 v152, 0x3db504f3, v152\n\t"
        "v_mul_f32_e32 v153, 0x3db504f3, v153\n\t"
        "v_mul_f32_e32 v154, 0x3db504f3, v154\n\t"
        "v_mul_f32_e32 v155, 0x3db504f3, v155\n\t"
        "v_cvt_pk_bf16_f32 v152, 0, v152\n\t"
        "v_cvt_pk_bf16_f32 v153, 0, v153\n\t"
        "v_cvt_pk_bf16_f32 v154, 0, v154\n\t"
        "v_cvt_pk_bf16_f32 v155, 0, v155\n\t"
        "v_cndmask_b32_e64 v152, v253, v152, s[48:49]\n\t"
        "v_cndmask_b32_e64 v153, v253, v153, s[50:51]\n\t"
        "v_cndmask_b32_e64 v154, v253, v154, s[52:53]\n\t"
        "v_cndmask_b32_e64 v155, v253, v155, s[54:55]\n\t"
        "v_max3_f32 v172, v136, v137, v253\n\t"
        "v_max3_f32 v172, v172, v138, v139\n\t"
        "v_mov_b32_e32 v140, v253\n\t"
        "v_max_f32_e32 v174, v148, v149\n\t"
        "v_max3_f32 v174, v174, v150, v151\n\t"
        "v_max3_f32 v174, v174, v152, v153\n\t"
        "v_max3_f32 v174, v174, v154, v155\n\t"
        "s_cmp_eq_u32 s39, 0\n\t"
        "s_cbranch_scc1 .La23%=\n\t"
        "s_waitcnt vmcnt(0)\n\t"
        "s_lshl_b32 s80, s44, 7\n\t"
        "s_cmp_lt_i32 s44, 0\n\t"
        "s_cselect_b32 s80, s36, s80\n\t"
        "v_mov_b32_e32 v242, 1\n\t"
        "buffer_atomic_add v242, v250, %[rd], s80 offen sc0 sc1\n\t"
        "s_waitcnt vmcnt(0)\n\t"
        "v_readlane_b32 s84, v242, 0\n\t"
        "v_readlane_b32 s85, v242, 32\n\t"
        "v_mov_b32_e32 v243, 32\n\t"
        "s_and_b32 s80, s44, 3\n\t"
        "s_lshl_b32 s80, s80, 12\n\t"
        "s_lshr_b32 s81, s44, 2\n\t"
        "s_lshl_b32 s81, s81, 3\n\t"
        "s_add_u32 s80, s80, s81\n\t"
        "s_lshr_b32 s82, s44, 1\n\t"
        "s_xnor_b32 s81, s44, s82\n\t"
        "s_and_b32 s81, s81, 1\n\t"
        "s_lshl_b32 s81, s81, 13\n\t"
        "s_lshl_b32 s82, s82, 2\n\t"
        "s_add_u32 s81, s81, s82\n\t"
        "s_cmp_lt_i32 s44, 60\n\t"
        "s_cselect_b32 s80, s80, s81\n\t"
        "s_cselect_b32 s81, 4, 0x1000\n\t"
        "s_add_u32 s81, s81, s80\n\t"
        "s_cmp_eq_u32 s84, 31\n\t"
        "s_cselect_b32 s80, s80, s36\n\t"
        "s_cmp_eq_u32 s85, 31\n\t"
        "s_cselect_b32 s81, s81, s36\n\t"
        "s_cmp_lt_i32 s44, 0\n\t"
        "s_cselect_b32 s80, s36, s80\n\t"
        "s_cselect_b32 s81, s36, s81\n\t"
        "buffer_store_dword v243, v251, %[rd], s80 offen sc1\n\t"
        "buffer_store_dword v243, v251, %[rd], s81 offen sc1\n\t"
        "s_mov_b32 s44, -1\n\t"
        "s_mov_b32 s97, 0\n\t"
        "s_min_u32 s87, s37, 62\n\t"
        ".La22%=:\n\t"
        "s_cmp_gt_u32 s40, s87\n\t"
        "s_cbranch_scc1 .La22%=x\n\t"
        "s_memrealtime s[98:99]\n\t"
        "s_add_u32 s97, s97, 1\n\t"
        "s_cmp_lt_u32 s97, 0x400000\n\t"
        "s_cbranch_scc0 .Lgu%=\n\t"
        "s_sub_u32 s80, s87, s40\n\t"
        "s_cmp_ge_u32 s87, %c[efrom]\n\t"
        "s_subb_u32 s80, s80, 0\n\t"
        "s_mul_i32 s80, s80, %c[smin]\n\t"
        "s_waitcnt lgkmcnt(0)\n\t"
        "s_sub_u32 s81, s98, s41\n\t"
        "s_cmp_ge_i32 s81, s80\n\t"
        "s_cbranch_scc1 .La22%=p\n\t"
        "s_sleep 16\n\t"
        "s_branch .La22%=\n\t"
        ".La22%=p:\n\t"
        "buffer_load_dword v241, v247, %[rd], 0 offen sc1\n\t"
        "s_waitcnt vmcnt(0)\n\t"
        "v_readfirstlane_b32 s80, v241\n\t"
        "s_nop 0\n\t"
        "s_cmp_gt_u32 s80, s40\n\t"
        "s_cselect_b32 s41, s98, s41\n\t"
        "s_max_u32 s40, s40, s80\n\t"
        "s_cmp_gt_u32 s40, s87\n\t"
        "s_cbranch_scc1 .La22%=x\n\t"
        "s_sleep %c[pslp]\n\t"
        "s_branch .La22%=\n\t"
        ".La22%=x:\n\t"
        "s_lshl_b32 s81, s37, 17\n\t"
        "s_add_u32 s81, s81, %[km]\n\t"
        "s_cmp_lg_u32 s37, 63\n\t"
        "s_cbranch_scc1 .La22%=l\n\t"
        "s_sleep %c[lslp]\n\t"
        ".La22%=l:\n\t"
        "buffer_load_dwordx4 v[96:99], v248, %[rd], s81 offen offset:0 sc1\n\t"
        "buffer_load_dwordx4 v[100:103], v248, %[rd], s81 offen offset:512 sc1\n\t"
        "buffer_load_dwordx4 v[104:107], v248, %[rd], s81 offen offset:1024 sc1\n\t"
        "buffer_load_dwordx4 v[108:111], v248, %[rd], s81 offen offset:1536 sc1\n\t"
        "buffer_load_dwordx2 v[112:113], v245, %[rd], s81 offen offset:2048 sc1\n\t"
        "buffer_load_dwordx2 v[114:115], v245, %[rd], s81 offen offset:2304 sc1\n\t"
        "buffer_load_dwordx2 v[116:117], v245, %[rd], s81 offen offset:2560 sc1\n\t"
        "buffer_load_dwordx2 v[118:119], v245, %[rd], s81 offen offset:2816 sc1\n\t"
        "buffer_load_dwordx2 v[120:121], v245, %[rd], s81 offen offset:3072 sc1\n\t"
        "buffer_load_dwordx2 v[122:123], v245, %[rd], s81 offen offset:3328 sc1\n\t"
        "buffer_load_dwordx2 v[124:125], v245, %[rd], s81 offen offset:3584 sc1\n\t"
        "buffer_load_dwordx2 v[126:127], v245, %[rd], s81 offen offset:3840 sc1\n\t"
        "s_waitcnt vmcnt(0)\n\t"
        "s_cmp_lg_u32 s37, 63\n\t"
        "s_cbranch_scc1 .La23%=\n\t"
        "s_add_u32 s97, s97, 1\n\t"
        "s_cmp_lt_u32 s97, 0x400000\n\t"
        "s_cbranch_scc0 .Lgu%=\n\t"
        "v_max_u32_e32 v128, v96, v97\n\t"
        "v_max3_u32 v128, v128, v98, v99\n\t"
        "v_max3_u32 v128, v128, v100, v101\n\t"
        "v_max3_u32 v128, v128, v102, v103\n\t"
        "v_max3_u32 v128, v128, v104, v105\n\t"
        "v_max3_u32 v128, v128, v106, v107\n\t"
        "v_max3_u32 v128, v128, v108, v109\n\t"
        "v_max3_u32 v128, v128, v110, v111\n\t"
        "v_max3_u32 v128, v128, v112, v113\n\t"
        "v_max3_u32 v128, v128, v114, v115\n\t"
        "v_max3_u32 v128, v128, v116, v117\n\t"
        "v_max3_u32 v128, v128, v118, v119\n\t"
        "v_max3_u32 v128, v128, v120, v121\n\t"
        "v_max3_u32 v128, v128, v122, v123\n\t"
        "v_max3_u32 v128, v128, v124, v125\n\t"
        "v_max3_u32 v128, v128, v126, v127\n\t"
        "v_cmp_eq_u32_e32 vcc, -1, v128\n\t"
        "s_nop 1\n\t"
        "v_mov_b32_e32 v252, v252\n\t"
        "s_nop 0\n\t"
        "s_cbranch_vccnz .La22%=l\n\t"
        ".La23%=:\n\t"
        "v_mfma_f32_16x16x32_bf16 v[144:147], v[96:99], v[32:35], 0\n\t"
        "v_mfma_f32_16x16x32_bf16 v[156:159], v[96:99], v[48:51], 0\n\t"
        "v_mfma_f32_16x16x32_bf16 v[144:147], v[100:103], v[36:39], v[144:147]\n\t"
        "v_mfma_f32_16x16x32_bf16 v[156:159], v[100:103], v[52:55], v[156:159]\n\t"
        "v_mfma_f32_16x16x32_bf16 v[144:147], v[104:107], v[40:43], v[144:147]\n\t"
        "v_mfma_f32_16x16x32_bf16 v[156:159], v[104:107], v[56:59], v[156:159]\n\t"
        "v_mfma_f32_16x16x32_bf16 v[144:147], v[108:111], v[44:47], v[144:147]\n\t"
        "v_mfma_f32_16x16x32_bf16 v[156:159], v[108:111], v[60:63], v[156:159]\n\t"
        "s_nop 1\n\t"
        "v_mov_b32_e32 v252, v252\n\t"
        "s_nop 1\n\t"
        "v_mov_b32_e32 v252, v252\n\t"
        "s_nop 0\n\t"
        "v_cvt_pk_bf16_f32 v144, 0, v144\n\t"
        "v_cvt_pk_bf16_f32 v145, 0, v145\n\t"
        "v_cvt_pk_bf16_f32 v146, 0, v146\n\t"
        "v_cvt_pk_bf16_f32 v147, 0, v147\n\t"
        "v_mul_f32_e32 v144, 0x3db504f3, v144\n\t"
        "v_mul_f32_e32 v145, 0x3db504f3, v145\n\t"
        "v_mul_f32_e32 v146, 0x3db504f3, v146\n\t"
        "v_mul_f32_e32 v147, 0x3db504f3, v147\n\t"
        "v_cvt_pk_bf16_f32 v144, 0, v144\n\t"
        "v_cvt_pk_bf16_f32 v145, 0, v145\n\t"
        "v_cvt_pk_bf16_f32 v146, 0, v146\n\t"
        "v_cvt_pk_bf16_f32 v147, 0, v147\n\t"
        "v_cndmask_b32_e64 v144, v144, v254, s[56:57]\n\t"
        "v_cndmask_b32_e64 v145, v145, v254, s[56:57]\n\t"
        "v_cndmask_b32_e64 v146, v146, v254, s[56:57]\n\t"
        "v_cndmask_b32_e64 v147, v147, v254, s[56:57]\n\t"
        "v_max3_f32 v172, v172, v144, v145\n\t"
        "v_max3_f32 v173, v172, v146, v147\n\t"
        "v_max3_f32 v172, v172, v146, v147\n\t"
        "s_nop 1\n\t"
        "v_permlane16_swap_b32_e32 v172, v173\n\t"
        "v_max_f32_e32 v172, v172, v173\n\t"
        "v_mov_b32_e32 v173, v172\n\t"
        "s_nop 1\n\t"
        "v_permlane32_swap_b32_e32 v172, v173\n\t"
        "v_max_f32_e32 v172, v172, v173\n\t"
        "v_sub_f32_e32 v136, v136, v172\n\t"
        "v_sub_f32_e32 v137, v137, v172\n\t"
        "v_sub_f32_e32 v138, v138, v172\n\t"
        "v_sub_f32_e32 v139, v139, v172\n\t"
        "v_sub_f32_e32 v140, v140, v172\n\t"
        "v_sub_f32_e32 v144, v144, v172\n\t"
        "v_sub_f32_e32 v145, v145, v172\n\t"
        "v_sub_f32_e32 v146, v146, v172\n\t"
        "v_sub_f32_e32 v147, v147, v172\n\t"
        "v_mul_f32_e32 v136, 0x3fb8aa3b, v136\n\t"
        "v_mul_f32_e32 v137, 0x3fb8aa3b, v137\n\t"
        "v_mul_f32_e32 v138, 0x3fb8aa3b, v138\n\t"
        "v_mul_f32_e32 v139, 0x3fb8aa3b, v139\n\t"
        "v_mul_f32_e32 v140, 0x3fb8aa3b, v140\n\t"
        "v_mul_f32_e32 v144, 0x3fb8aa3b, v144\n\t"
        "v_mul_f32_e32 v145, 0x3fb8aa3b, v145\n\t"
        "v_mul_f32_e32 v146, 0x3fb8aa3b, v146\n\t"
        "v_mul_f32_e32 v147, 0x3fb8aa3b, v147\n\t"
        "v_exp_f32_e32 v136, v136\n\t"
        "v_exp_f32_e32 v137, v137\n\t"
        "v_exp_f32_e32 v138, v138\n\t"
        "v_exp_f32_e32 v139, v139\n\t"
        "v_exp_f32_e32 v140, v140\n\t"
        "v_exp_f32_e32 v144, v144\n\t"
        "v_exp_f32_e32 v145, v145\n\t"
        "v_exp_f32_e32 v146, v146\n\t"
        "v_exp_f32_e32 v147, v147\n\t"
        "v_mul_f32_e32 v172, 4.0, v140\n\t"
        "v_add_f32_e32 v172, v172, v136\n\t"
        "v_add_f32_e32 v172, v172, v137\n\t"
        "v_add_f32_e32 v172, v172, v138\n\t"
        "v_add_f32_e32 v172, v172, v139\n\t"
        "v_add_f32_e32 v172, v172, v144\n\t"
        "v_add_f32_e32 v172, v172, v145\n\t"
        "v_add_f32_e32 v172, v172, v146\n\t"
        "v_add_f32_e32 v172, v172, v147\n\t"
        "v_mov_b32_e32 v173, v172\n\t"
        "s_nop 1\n\t"
        "v_permlane16_swap_b32_e32 v172, v173\n\t"
        "v_add_f32_e32 v172, v172, v173\n\t"
        "v_mov_b32_e32 v173, v172\n\t"
        "s_nop 1\n\t"
        "v_permlane32_swap_b32_e32 v172, v173\n\t"
        "v_add_f32_e32 v172, v172, v173\n\t"
        "v_rcp_f32_e32 v172, v172\n\t"
        "s_nop 0\n\t"
        "v_mul_f32_e32 v136, v136, v172\n\t"
        "v_mul_f32_e32 v137, v137, v172\n\t"
        "v_mul_f32_e32 v138, v138, v172\n\t"
        "v_mul_f32_e32 v139, v139, v172\n\t"
        "v_mul_f32_e32 v140, v140, v172\n\t"
        "v_mul_f32_e32 v144, v144, v172\n\t"
        "v_mul_f32_e32 v145, v145, v172\n\t"
        "v_mul_f32_e32 v146, v146, v172\n\t"
        "v_mul_f32_e32 v147, v147, v172\n\t"
        "v_cvt_pk_bf16_f32 v160, v136, v137\n\t"
        "v_cvt_pk_bf16_f32 v161, v138, v139\n\t"
        "v_cvt_pk_bf16_f32 v162, v140, v140\n\t"
        "v_cvt_pk_bf16_f32 v163, v140, v140\n\t"
        "v_cvt_pk_bf16_f32 v164, v144, v145\n\t"
        "v_cvt_pk_bf16_f32 v165, v146, v147\n\t"
        "v_mfma_f32_16x16x32_bf16 v[176:179], v[64:67], v[160:163], 0\n\t"
        "v_cvt_pk_bf16_f32 v156, 0, v156\n\t"
        "v_cvt_pk_bf16_f32 v157, 0, v157\n\t"
        "v_cvt_pk_bf16_f32 v158, 0, v158\n\t"
        "v_mfma_f32_16x16x32_bf16 v[180:183], v[68:71], v[160:163], 0\n\t"
        "v_cvt_pk_bf16_f32 v159, 0, v159\n\t"
        "v_mul_f32_e32 v156, 0x3db504f3, v156\n\t"
        "v_mul_f32_e32 v157, 0x3db504f3, v157\n\t"
        "v_mfma_f32_16x16x32_bf16 v[184:187], v[72:75], v[160:163], 0\n\t"
        "v_mul_f32_e32 v158, 0x3db504f3, v158\n\t"
        "v_mul_f32_e32 v159, 0x3db504f3, v159\n\t"
        "v_cvt_pk_bf16_f32 v156, 0, v156\n\t"
        "v_mfma_f32_16x16x32_bf16 v[188:191], v[76:79], v[160:163], 0\n\t"
        "v_cvt_pk_bf16_f32 v157, 0, v157\n\t"
        "v_cvt_pk_bf16_f32 v158, 0, v158\n\t"
        "v_cvt_pk_bf16_f32 v159, 0, v159\n\t"
        "v_mfma_f32_16x16x32_bf16 v[192:195], v[80:83], v[160:163], 0\n\t"
        "v_cndmask_b32_e64 v156, v156, v254, s[56:57]\n\t"
        "v_cndmask_b32_e64 v157, v157, v254, s[56:57]\n\t"
        "v_cndmask_b32_e64 v158, v158, v254, s[56:57]\n\t"
        "v_mfma_f32_16x16x32_bf16 v[196:199], v[84:87], v[160:163], 0\n\t"
        "v_cndmask_b32_e64 v159, v159, v254, s[56:57]\n\t"
        "v_max3_f32 v174, v174, v156, v157\n\t"
        "v_max3_f32 v175, v174, v158, v159\n\t"
        "v_mfma_f32_16x16x32_bf16 v[200:203], v[88:91], v[160:163], 0\n\t"
        "v_max3_f32 v174, v174, v158, v159\n\t"
        "s_nop 1\n\t"
        "v_permlane16_swap_b32_e32 v174, v175\n\t"
        "v_max_f32_e32 v174, v174, v175\n\t"
        "v_mfma_f32_16x16x32_bf16 v[204:207], v[92:95], v[160:163], 0\n\t"
        "v_mov_b32_e32 v175, v174\n\t"
        "s_nop 1\n\t"
        "v_permlane32_swap_b32_e32 v174, v175\n\t"
        "v_max_f32_e32 v174, v174, v175\n\t"
        "v_mfma_f32_16x16x16_bf16 v[176:179], v[112:113], v[164:165], v[176:179]\n\t"
        "v_sub_f32_e32 v148, v148, v174\n\t"
        "v_sub_f32_e32 v149, v149, v174\n\t"
        "v_sub_f32_e32 v150, v150, v174\n\t"
        "v_mfma_f32_16x16x16_bf16 v[180:183], v[114:115], v[164:165], v[180:183]\n\t"
        "v_sub_f32_e32 v151, v151, v174\n\t"
        "v_sub_f32_e32 v152, v152, v174\n\t"
        "v_sub_f32_e32 v153, v153, v174\n\t"
        "v_mfma_f32_16x16x16_bf16 v[184:187], v[116:117], v[164:165], v[184:187]\n\t"
        "v_sub_f32_e32 v154, v154, v174\n\t"
        "v_sub_f32_e32 v155, v155, v174\n\t"
        "v_sub_f32_e32 v156, v156, v174\n\t"
        "v_mfma_f32_16x16x16_bf16 v[188:191], v[118:119], v[164:165], v[188:191]\n\t"
        "v_sub_f32_e32 v157, v157, v174\n\t"
        "v_sub_f32_e32 v158, v158, v174\n\t"
        "v_sub_f32_e32 v159, v159, v174\n\t"
        "v_mfma_f32_16x16x16_bf16 v[192:195], v[120:121], v[164:165], v[192:195]\n\t"
        "v_mul_f32_e32 v148, 0x3fb8aa3b, v148\n\t"
        "v_mul_f32_e32 v149, 0x3fb8aa3b, v149\n\t"
        "v_mul_f32_e32 v150, 0x3fb8aa3b, v150\n\t"
        "v_mfma_f32_16x16x16_bf16 v[196:199], v[122:123], v[164:165], v[196:199]\n\t"
        "v_mul_f32_e32 v151, 0x3fb8aa3b, v151\n\t"
        "v_mul_f32_e32 v152, 0x3fb8aa3b, v152\n\t"
        "v_mul_f32_e32 v153, 0x3fb8aa3b, v153\n\t"
        "v_mfma_f32_16x16x16_bf16 v[200:203], v[124:125], v[164:165], v[200:203]\n\t"
        "v_mul_f32_e32 v154, 0x3fb8aa3b, v154\n\t"
        "v_mul_f32_e32 v155, 0x3fb8aa3b, v155\n\t"
        "v_mul_f32_e32 v156, 0x3fb8aa3b, v156\n\t"
        "v_mfma_f32_16x16x16_bf16 v[204:207], v[126:127], v[164:165], v[204:207]\n\t"
        "v_mul_f32_e32 v157, 0x3fb8aa3b, v157\n\t"
        "v_mul_f32_e32 v158, 0x3fb8aa3b, v158\n\t"
        "v_mul_f32_e32 v159, 0x3fb8aa3b, v159\n\t"
        "v_exp_f32_e32 v148, v148\n\t"
        "v_exp_f32_e32 v149, v149\n\t"
        "v_exp_f32_e32 v150, v150\n\t"
        "v_exp_f32_e32 v151, v151\n\t"
        "v_exp_f32_e32 v152, v152\n\t"
        "v_exp_f32_e32 v153, v153\n\t"
        "v_exp_f32_e32 v154, v154\n\t"
        "v_exp_f32_e32 v155, v155\n\t"
        "v_exp_f32_e32 v156, v156\n\t"
        "v_exp_f32_e32 v157, v157\n\t"
        "v_exp_f32_e32 v158, v158\n\t"
        "v_exp_f32_e32 v159, v159\n\t"
        "v_add_f32_e32 v174, v148, v149\n\t"
        "v_add_f32_e32 v174, v174, v150\n\t"
        "v_add_f32_e32 v174, v174, v151\n\t"
        "v_add_f32_e32 v174, v174, v152\n\t"
        "v_add_f32_e32 v174, v174, v153\n\t"
        "v_add_f32_e32 v174, v174, v154\n\t"
        "v_add_f32_e32 v174, v174, v155\n\t"
        "v_add_f32_e32 v174, v174, v156\n\t"
        "v_add_f32_e32 v174, v174, v157\n\t"
        "v_add_f32_e32 v174, v174, v158\n\t"
        "v_add_f32_e32 v174, v174, v159\n\t"
        "v_mov_b32_e32 v175, v174\n\t"
        "s_nop 1\n\t"
        "v_permlane16_swap_b32_e32 v174, v175\n\t"
        "v_add_f32_e32 v174, v174, v175\n\t"
        "v_mov_b32_e32 v175, v174\n\t"
        "s_nop 1\n\t"
        "v_permlane32_swap_b32_e32 v174, v175\n\t"
        "v_add_f32_e32 v174, v174, v175\n\t"
        "v_rcp_f32_e32 v174, v174\n\t"
        "s_nop 0\n\t"
        "v_mul_f32_e32 v148, v148, v174\n\t"
        "v_mul_f32_e32 v149, v149, v174\n\t"
        "v_mul_f32_e32 v150, v150, v174\n\t"
        "v_mul_f32_e32 v151, v151, v174\n\t"
        "v_mul_f32_e32 v152, v152, v174\n\t"
        "v_mul_f32_e32 v153, v153, v174\n\t"
        "v_mul_f32_e32 v154, v154, v174\n\t"
        "v_mul_f32_e32 v155, v155, v174\n\t"
        "v_mul_f32_e32 v156, v156, v174\n\t"
        "v_mul_f32_e32 v157, v157, v174\n\t"
        "v_mul_f32_e32 v158, v158, v174\n\t"
        "v_mul_f32_e32 v159, v159, v174\n\t"
        "v_cvt_pk_bf16_f32 v166, v148, v149\n\t"
        "v_cvt_pk_bf16_f32 v167, v150, v151\n\t"
        "v_cvt_pk_bf16_f32 v168, v152, v153\n\t"
        "v_cvt_pk_bf16_f32 v169, v154, v155\n\t"
        "v_cvt_pk_bf16_f32 v170, v156, v157\n\t"
        "v_cvt_pk_bf16_f32 v171, v158, v159\n\t"
        "s_waitcnt vmcnt(40)\n\t"
        "s_lshl_b32 s80, s44, 7\n\t"
        "s_cmp_lt_i32 s44, 0\n\t"
        "s_cselect_b32 s80, s36, s80\n\t"
        "v_mov_b32_e32 v242, 1\n\t"
        "buffer_atomic_add v242, v250, %[rd], s80 offen sc0 sc1\n\t"
        "s_lshl_b32 s86, s37, 18\n\t"
        "s_add_u32 s86, s86, %[qx]\n\t"
        "s_add_u32 s87, s86, 0x1000\n\t"
        "v_mfma_f32_16x16x32_bf16 v[208:211], v[64:67], v[166:169], 0\n\t"
        "v_cvt_pk_bf16_f32 v176, v176, v177\n\t"
        "v_cvt_pk_bf16_f32 v177, v178, v179\n\t"
        "v_mfma_f32_16x16x32_bf16 v[212:215], v[68:71], v[166:169], 0\n\t"
        "v_cvt_pk_bf16_f32 v178, v180, v181\n\t"
        "v_cvt_pk_bf16_f32 v179, v182, v183\n\t"
        "v_mfma_f32_16x16x32_bf16 v[216:219], v[72:75], v[166:169], 0\n\t"
        "buffer_store_dwordx4 v[176:179], v244, %[rd], s86 offen offset:0 sc1\n\t"
        "v_cvt_pk_bf16_f32 v184, v184, v185\n\t"
        "v_mfma_f32_16x16x32_bf16 v[220:223], v[76:79], v[166:169], 0\n\t"
        "v_cvt_pk_bf16_f32 v185, v186, v187\n\t"
        "v_cvt_pk_bf16_f32 v186, v188, v189\n\t"
        "v_mfma_f32_16x16x32_bf16 v[224:227], v[80:83], v[166:169], 0\n\t"
        "v_cvt_pk_bf16_f32 v187, v190, v191\n\t"
        "buffer_store_dwordx4 v[184:187], v244, %[rd], s86 offen offset:1024 sc1\n\t"
        "v_mfma_f32_16x16x32_bf16 v[228:231], v[84:87], v[166:169], 0\n\t"
        "v_cvt_pk_bf16_f32 v192, v192, v193\n\t"
        "v_cvt_pk_bf16_f32 v193, v194, v195\n\t"
        "v_mfma_f32_16x16x32_bf16 v[232:235], v[88:91], v[166:169], 0\n\t"
        "v_cvt_pk_bf16_f32 v194, v196, v197\n\t"
        "v_cvt_pk_bf16_f32 v195, v198, v199\n\t"
        "v_mfma_f32_16x16x32_bf16 v[236:239], v[92:95], v[166:169], 0\n\t"
        "buffer_store_dwordx4 v[192:195], v244, %[rd], s86 offen offset:2048 sc1\n\t"
        "v_cvt_pk_bf16_f32 v200, v200, v201\n\t"
        "v_mfma_f32_16x16x16_bf16 v[208:211], v[112:113], v[170:171], v[208:211]\n\t"
        "v_cvt_pk_bf16_f32 v201, v202, v203\n\t"
        "v_cvt_pk_bf16_f32 v202, v204, v205\n\t"
        "v_mfma_f32_16x16x16_bf16 v[212:215], v[114:115], v[170:171], v[212:215]\n\t"
        "v_cvt_pk_bf16_f32 v203, v206, v207\n\t"
        "buffer_store_dwordx4 v[200:203], v244, %[rd], s86 offen offset:3072 sc1\n\t"
        "v_mfma_f32_16x16x16_bf16 v[216:219], v[116:117], v[170:171], v[216:219]\n\t"
        "v_mfma_f32_16x16x16_bf16 v[220:223], v[118:119], v[170:171], v[220:223]\n\t"
        "v_mfma_f32_16x16x16_bf16 v[224:227], v[120:121], v[170:171], v[224:227]\n\t"
        "v_mfma_f32_16x16x16_bf16 v[228:231], v[122:123], v[170:171], v[228:231]\n\t"
        "v_mfma_f32_16x16x16_bf16 v[232:235], v[124:125], v[170:171], v[232:235]\n\t"
        "v_mfma_f32_16x16x16_bf16 v[236:239], v[126:127], v[170:171], v[236:239]\n\t"
        "v_cvt_pk_bf16_f32 v208, v208, v209\n\t"
        "v_cvt_pk_bf16_f32 v209, v210, v211\n\t"
        "v_cvt_pk_bf16_f32 v210, v212, v213\n\t"
        "v_cvt_pk_bf16_f32 v211, v214, v215\n\t"
        "buffer_store_dwordx4 v[208:211], v244, %[rd], s87 offen offset:0 sc1\n\t"
        "v_cvt_pk_bf16_f32 v216, v216, v217\n\t"
        "v_cvt_pk_bf16_f32 v217, v218, v219\n\t"
        "v_cvt_pk_bf16_f32 v218, v220, v221\n\t"
        "v_cvt_pk_bf16_f32 v219, v222, v223\n\t"
        "buffer_store_dwordx4 v[216:219], v244, %[rd], s87 offen offset:1024 sc1\n\t"
        "v_cvt_pk_bf16_f32 v224, v224, v225\n\t"
        "v_cvt_pk_bf16_f32 v225, v226, v227\n\t"
        "v_cvt_pk_bf16_f32 v226, v228, v229\n\t"
        "v_cvt_pk_bf16_f32 v227, v230, v231\n\t"
        "buffer_store_dwordx4 v[224:227], v244, %[rd], s87 offen offset:2048 sc1\n\t"
        "v_cvt_pk_bf16_f32 v232, v232, v233\n\t"
        "v_cvt_pk_bf16_f32 v233, v234, v235\n\t"
        "v_cvt_pk_bf16_f32 v234, v236, v237\n\t"
        "v_cvt_pk_bf16_f32 v235, v238, v239\n\t"
        "buffer_store_dwordx4 v[232:235], v244, %[rd], s87 offen offset:3072 sc1\n\t"
        "s_mov_b32 s45, s44\n\t"
        "s_mov_b32 s44, s37\n\t"
        "s_add_u32 s80, s37, 4\n\t"
        "s_cmp_gt_u32 s40, s80\n\t"
        "s_cselect_b32 s80, s46, 1\n\t"
        "s_cmp_eq_u32 s80, 0\n\t"
        "s_cbranch_scc1 .La24%=\n\t"
        "s_waitcnt vmcnt(0)\n\t"
        "v_readlane_b32 s84, v242, 0\n\t"
        "v_readlane_b32 s85, v242, 32\n\t"
        "v_mov_b32_e32 v243, 32\n\t"
        "s_and_b32 s80, s45, 3\n\t"
        "s_lshl_b32 s80, s80, 12\n\t"
        "s_lshr_b32 s81, s45, 2\n\t"
        "s_lshl_b32 s81, s81, 3\n\t"
        "s_add_u32 s80, s80, s81\n\t"
        "s_lshr_b32 s82, s45, 1\n\t"
        "s_xnor_b32 s81, s45, s82\n\t"
        "s_and_b32 s81, s81, 1\n\t"
        "s_lshl_b32 s81, s81, 13\n\t"
        "s_lshl_b32 s82, s82, 2\n\t"
        "s_add_u32 s81, s81, s82\n\t"
        "s_cmp_lt_i32 s45, 60\n\t"
        "s_cselect_b32 s80, s80, s81\n\t"
        "s_cselect_b32 s81, 4, 0x1000\n\t"
        "s_add_u32 s81, s81, s80\n\t"
        "s_cmp_eq_u32 s84, 31\n\t"
        "s_cselect_b32 s80, s80, s36\n\t"
        "s_cmp_eq_u32 s85, 31\n\t"
        "s_cselect_b32 s81, s81, s36\n\t"
        "s_cmp_lt_i32 s45, 0\n\t"
        "s_cselect_b32 s80, s36, s80\n\t"
        "s_cselect_b32 s81, s36, s81\n\t"
        "buffer_store_dword v243, v251, %[rd], s80 offen sc1\n\t"
        "buffer_store_dword v243, v251, %[rd], s81 offen sc1\n\t"
        "s_lshl_b32 s80, s44, 7\n\t"
        "s_cmp_lt_i32 s44, 0\n\t"
        "s_cselect_b32 s80, s36, s80\n\t"
        "v_mov_b32_e32 v242, 1\n\t"
        "buffer_atomic_add v242, v250, %[rd], s80 offen sc0 sc1\n\t"
        "s_waitcnt vmcnt(0)\n\t"
        "v_readlane_b32 s84, v242, 0\n\t"
        "v_readlane_b32 s85, v242, 32\n\t"
        "v_mov_b32_e32 v243, 32\n\t"
        "s_and_b32 s80, s44, 3\n\t"
        "s_lshl_b32 s80, s80, 12\n\t"
        "s_lshr_b32 s81, s44, 2\n\t"
        "s_lshl_b32 s81, s81, 3\n\t"
        "s_add_u32 s80, s80, s81\n\t"
        "s_lshr_b32 s82, s44, 1\n\t"
        "s_xnor_b32 s81, s44, s82\n\t"
        "s_and_b32 s81, s81, 1\n\t"
        "s_lshl_b32 s81, s81, 13\n\t"
        "s_lshl_b32 s82, s82, 2\n\t"
        "s_add_u32 s81, s81, s82\n\t"
        "s_cmp_lt_i32 s44, 60\n\t"
        "s_cselect_b32 s80, s80, s81\n\t"
        "s_cselect_b32 s81, 4, 0x1000\n\t"
        "s_add_u32 s81, s81, s80\n\t"
        "s_cmp_eq_u32 s84, 31\n\t"
        "s_cselect_b32 s80, s80, s36\n\t"
        "s_cmp_eq_u32 s85, 31\n\t"
        "s_cselect_b32 s81, s81, s36\n\t"
        "s_cmp_lt_i32 s44, 0\n\t"
        "s_cselect_b32 s80, s36, s80\n\t"
        "s_cselect_b32 s81, s36, s81\n\t"
        "buffer_store_dword v243, v251, %[rd], s80 offen sc1\n\t"
        "buffer_store_dword v243, v251, %[rd], s81 offen sc1\n\t"
        "s_mov_b32 s44, -1\n\t"
        "s_mov_b32 s45, -1\n\t"
        ".La24%=:\n\t"
        "s_mov_b32 s38, s46\n\t"
        "s_mov_b32 s39, s47\n\t"
        "s_add_u32 s37, s37, 4\n\t"
        "s_cmp_lt_u32 s37, 64\n\t"
        "s_cbranch_scc1 .Ltop%=\n\t"
        "s_endpgm\n\t"
        ".Lgu%=:\n\t"
        "v_mov_b32_e32 v0, 64\n\t"
        "s_mov_b32 s80, %c[ferr]\n\t"
        "buffer_store_dword v0, off, %[rd], s80 sc1\n\t"
        "s_endpgm"
        :: [rd] "s"(rd), [w] "s"(wv), [tag] "s"(tag), [kx] "s"(kx), [qx] "s"(qx), [vx] "s"(vx), [km] "s"(km), [blk] "s"(h),
          [ferr] "i"(F_ERR), [dbg] "i"(W_DBG), [pslp] "i"(P_SLP), [stm] "i"(ST_M), [sts] "i"(ST_S), [lslp] "i"(L_SLP), [aslp] "i"(AT_SLP), [bslp] "i"(BT_SLP), [efrom] "i"(EARLY_FROM), [smin] "i"(STEP_MIN), "{v[244:251]}"(pc), "{v252}"(dq)
        : "memory", "vcc", "scc", "s36", "s37", "s38", "s39", "s40", "s41", "s42", "s43", "s44", "s45", "s46", "s47", "s48", "s49", "s50", "s51", "s52", "s53", "s54", "s55", "s56", "s57", "s58", "s59", "s80", "s81", "s82", "s83", "s84", "s85", "s86", "s87", "s88", "s89", "s90", "s91", "s92", "s93", "s94", "s95", "s96", "s97", "s98", "s99");
}

constexpr int OP_P = 144;
__device__ __forceinline__ void oproj_block(const Params& p, const int wv, const int nt, const int rho, const int j, const unsigned tag, const unsigned g_t0) {
    const int w = wv, l = otid() & 63, n16 = l & 15, g = l >> 4;
    const v4u rd = mk_srd(p.ws, W_TOTAL), ro = mk_srd(p.out, S * D * 2), rx = mk_srd(p.Aq, S * D * 2);
    const unsigned snd = (unsigned)((w >> 1) ^ nt) << 31, own = snd ^ 0x80000000u;
    const unsigned ep = (nt == 0 && rho == 0 && w == 0) ? F_EP : 0xF0000000u;
    const int n0 = (nt >> 1) * 64 + 4 * n16, row = 4 * w + g;
    const unsigned a_b = W_QX + ((nt & 1) * 16 + w * 4) * 8192 + l * 16, tr = (4 * g + (n16 >> 2)), tc = (n16 & 3) * 8;
    const v16u pc = {a_b, a_b + 4096, F_RDY + (rho * 32 + nt) * 128 + (l & 31) * 4, W_U + (n0 >> 2) * 16384 + row * 8, W_GATE + row * 4, (unsigned)((row * 1024 + n0) * 2),
                     W_KX + ((nt >> 1) * 4 + w) * 2048 + l * 16,
                     (unsigned)(w * 8320 + g * 256 + ((n16 + 4 * g) & 15) * 16), (unsigned)((n16 >> 2) * 1040 + (n16 & 3) * 256 + ((row + 4 * n16) & 15) * 16),
                     (unsigned)(w * 10240 + (l >> 3) * OP_P + (l & 7) * 16), w * 10240 + tr * OP_P + tc, WO_L + w * 16384 + tr * 128 + tc, 0u, 0u, 0u, 0u};
    asm volatile(
        "ds_write_b128 v185, v[192:195] offset:0\n\t"
        "ds_write_b128 v185, v[196:199] offset:1152\n\t"
        "ds_write_b128 v185, v[200:203] offset:2304\n\t"
        "ds_write_b128 v185, v[204:207] offset:3456\n\t"
        "ds_read_b64_tr_b16 a[0:1], v186 offset:0\n\t"
        "ds_read_b64_tr_b16 a[2:3], v186 offset:2304\n\t"
        "ds_read_b64_tr_b16 a[4:5], v186 offset:32\n\t"
        "ds_read_b64_tr_b16 a[6:7], v186 offset:2336\n\t"
        "ds_read_b64_tr_b16 a[8:9], v186 offset:64\n\t"
        "ds_read_b64_tr_b16 a[10:11], v186 offset:2368\n\t"
        "ds_read_b64_tr_b16 a[12:13], v186 offset:96\n\t"
        "ds_read_b64_tr_b16 a[14:15], v186 offset:2400\n\t"
        "ds_write_b128 v185, v[208:211] offset:5120\n\t"
        "ds_write_b128 v185, v[212:215] offset:6272\n\t"
        "ds_write_b128 v185, v[216:219] offset:7424\n\t"
        "ds_write_b128 v185, v[220:223] offset:8576\n\t"
        "ds_read_b64_tr_b16 a[16:17], v186 offset:5120\n\t"
        "ds_read_b64_tr_b16 a[18:19], v186 offset:7424\n\t"
        "ds_read_b64_tr_b16 a[20:21], v186 offset:5152\n\t"
        "ds_read_b64_tr_b16 a[22:23], v186 offset:7456\n\t"
        "ds_read_b64_tr_b16 a[24:25], v186 offset:5184\n\t"
        "ds_read_b64_tr_b16 a[26:27], v186 offset:7488\n\t"
        "ds_read_b64_tr_b16 a[28:29], v186 offset:5216\n\t"
        "ds_read_b64_tr_b16 a[30:31], v186 offset:7520\n\t"
        "ds_write_b128 v185, v[224:227] offset:0\n\t"
        "ds_write_b128 v185, v[228:231] offset:1152\n\t"
        "ds_write_b128 v185, v[232:235] offset:2304\n\t"
        "ds_write_b128 v185, v[236:239] offset:3456\n\t"
        "ds_read_b64_tr_b16 a[32:33], v186 offset:0\n\t"
        "ds_read_b64_tr_b16 a[34:35], v186 offset:2304\n\t"
        "ds_read_b64_tr_b16 a[36:37], v186 offset:32\n\t"
        "ds_read_b64_tr_b16 a[38:39], v186 offset:2336\n\t"
        "ds_read_b64_tr_b16 a[40:41], v186 offset:64\n\t"
        "ds_read_b64_tr_b16 a[42:43], v186 offset:2368\n\t"
        "ds_read_b64_tr_b16 a[44:45], v186 offset:96\n\t"
        "ds_read_b64_tr_b16 a[46:47], v186 offset:2400\n\t"
        "ds_write_b128 v185, v[240:243] offset:5120\n\t"
        "ds_write_b128 v185, v[244:247] offset:6272\n\t"
        "ds_write_b128 v185, v[248:251] offset:7424\n\t"
        "ds_write_b128 v185, v[252:255] offset:8576\n\t"
        "ds_read_b64_tr_b16 a[48:49], v186 offset:5120\n\t"
        "ds_read_b64_tr_b16 a[50:51], v186 offset:7424\n\t"
        "ds_read_b64_tr_b16 a[52:53], v186 offset:5152\n\t"
        "ds_read_b64_tr_b16 a[54:55], v186 offset:7456\n\t"
        "ds_read_b64_tr_b16 a[56:57], v186 offset:5184\n\t"
        "ds_read_b64_tr_b16 a[58:59], v186 offset:7488\n\t"
        "ds_read_b64_tr_b16 a[60:61], v186 offset:5216\n\t"
        "ds_read_b64_tr_b16 a[62:63], v186 offset:7520\n\t"
        "ds_read_b64_tr_b16 a[64:65], v187 offset:0\n\t"
        "ds_read_b64_tr_b16 a[66:67], v187 offset:2048\n\t"
        "ds_read_b64_tr_b16 a[68:69], v187 offset:32\n\t"
        "ds_read_b64_tr_b16 a[70:71], v187 offset:2080\n\t"
        "ds_read_b64_tr_b16 a[72:73], v187 offset:64\n\t"
        "ds_read_b64_tr_b16 a[74:75], v187 offset:2112\n\t"
        "ds_read_b64_tr_b16 a[76:77], v187 offset:96\n\t"
        "ds_read_b64_tr_b16 a[78:79], v187 offset:2144\n\t"
        "ds_read_b64_tr_b16 a[80:81], v187 offset:4096\n\t"
        "ds_read_b64_tr_b16 a[82:83], v187 offset:6144\n\t"
        "ds_read_b64_tr_b16 a[84:85], v187 offset:4128\n\t"
        "ds_read_b64_tr_b16 a[86:87], v187 offset:6176\n\t"
        "ds_read_b64_tr_b16 a[88:89], v187 offset:4160\n\t"
        "ds_read_b64_tr_b16 a[90:91], v187 offset:6208\n\t"
        "ds_read_b64_tr_b16 a[92:93], v187 offset:4192\n\t"
        "ds_read_b64_tr_b16 a[94:95], v187 offset:6240\n\t"
        "ds_read_b64_tr_b16 a[96:97], v187 offset:8192\n\t"
        "ds_read_b64_tr_b16 a[98:99], v187 offset:10240\n\t"
        "ds_read_b64_tr_b16 a[100:101], v187 offset:8224\n\t"
        "ds_read_b64_tr_b16 a[102:103], v187 offset:10272\n\t"
        "ds_read_b64_tr_b16 a[104:105], v187 offset:8256\n\t"
        "ds_read_b64_tr_b16 a[106:107], v187 offset:10304\n\t"
        "ds_read_b64_tr_b16 a[108:109], v187 offset:8288\n\t"
        "ds_read_b64_tr_b16 a[110:111], v187 offset:10336\n\t"
        "ds_read_b64_tr_b16 a[112:113], v187 offset:12288\n\t"
        "ds_read_b64_tr_b16 a[114:115], v187 offset:14336\n\t"
        "ds_read_b64_tr_b16 a[116:117], v187 offset:12320\n\t"
        "ds_read_b64_tr_b16 a[118:119], v187 offset:14368\n\t"
        "ds_read_b64_tr_b16 a[120:121], v187 offset:12352\n\t"
        "ds_read_b64_tr_b16 a[122:123], v187 offset:14400\n\t"
        "ds_read_b64_tr_b16 a[124:125], v187 offset:12384\n\t"
        "ds_read_b64_tr_b16 a[126:127], v187 offset:14432\n\t"
        "ds_write_b128 v185, a[128:131] offset:0\n\t"
        "ds_write_b128 v185, a[132:135] offset:1152\n\t"
        "ds_write_b128 v185, a[136:139] offset:2304\n\t"
        "ds_write_b128 v185, a[140:143] offset:3456\n\t"
        "s_waitcnt lgkmcnt(0)\n\t"
        "ds_read_b64_tr_b16 a[128:129], v186 offset:0\n\t"
        "ds_read_b64_tr_b16 a[130:131], v186 offset:2304\n\t"
        "ds_read_b64_tr_b16 a[132:133], v186 offset:32\n\t"
        "ds_read_b64_tr_b16 a[134:135], v186 offset:2336\n\t"
        "ds_read_b64_tr_b16 a[136:137], v186 offset:64\n\t"
        "ds_read_b64_tr_b16 a[138:139], v186 offset:2368\n\t"
        "ds_read_b64_tr_b16 a[140:141], v186 offset:96\n\t"
        "ds_read_b64_tr_b16 a[142:143], v186 offset:2400\n\t"
        "ds_write_b128 v185, a[144:147] offset:5120\n\t"
        "ds_write_b128 v185, a[148:151] offset:6272\n\t"
        "ds_write_b128 v185, a[152:155] offset:7424\n\t"
        "ds_write_b128 v185, a[156:159] offset:8576\n\t"
        "s_waitcnt lgkmcnt(0)\n\t"
        "ds_read_b64_tr_b16 a[144:145], v186 offset:5120\n\t"
        "ds_read_b64_tr_b16 a[146:147], v186 offset:7424\n\t"
        "ds_read_b64_tr_b16 a[148:149], v186 offset:5152\n\t"
        "ds_read_b64_tr_b16 a[150:151], v186 offset:7456\n\t"
        "ds_read_b64_tr_b16 a[152:153], v186 offset:5184\n\t"
        "ds_read_b64_tr_b16 a[154:155], v186 offset:7488\n\t"
        "ds_read_b64_tr_b16 a[156:157], v186 offset:5216\n\t"
        "ds_read_b64_tr_b16 a[158:159], v186 offset:7520\n\t"
        "ds_write_b128 v185, a[160:163] offset:0\n\t"
        "ds_write_b128 v185, a[164:167] offset:1152\n\t"
        "ds_write_b128 v185, a[168:171] offset:2304\n\t"
        "ds_write_b128 v185, a[172:175] offset:3456\n\t"
        "s_waitcnt lgkmcnt(0)\n\t"
        "ds_read_b64_tr_b16 a[160:161], v186 offset:0\n\t"
        "ds_read_b64_tr_b16 a[162:163], v186 offset:2304\n\t"
        "ds_read_b64_tr_b16 a[164:165], v186 offset:32\n\t"
        "ds_read_b64_tr_b16 a[166:167], v186 offset:2336\n\t"
        "ds_read_b64_tr_b16 a[168:169], v186 offset:64\n\t"
        "ds_read_b64_tr_b16 a[170:171], v186 offset:2368\n\t"
        "ds_read_b64_tr_b16 a[172:173], v186 offset:96\n\t"
        "ds_read_b64_tr_b16 a[174:175], v186 offset:2400\n\t"
        "ds_write_b128 v185, a[176:179] offset:5120\n\t"
        "ds_write_b128 v185, a[180:183] offset:6272\n\t"
        "ds_write_b128 v185, a[184:187] offset:7424\n\t"
        "ds_write_b128 v185, a[188:191] offset:8576\n\t"
        "s_waitcnt lgkmcnt(0)\n\t"
        "ds_read_b64_tr_b16 a[176:177], v186 offset:5120\n\t"
        "ds_read_b64_tr_b16 a[178:179], v186 offset:7424\n\t"
        "ds_read_b64_tr_b16 a[180:181], v186 offset:5152\n\t"
        "ds_read_b64_tr_b16 a[182:183], v186 offset:7456\n\t"
        "ds_read_b64_tr_b16 a[184:185], v186 offset:5184\n\t"
        "ds_read_b64_tr_b16 a[186:187], v186 offset:7488\n\t"
        "ds_read_b64_tr_b16 a[188:189], v186 offset:5216\n\t"
        "ds_read_b64_tr_b16 a[190:191], v186 offset:7520\n\t"
        "ds_write_b128 v185, a[192:195] offset:0\n\t"
        "ds_write_b128 v185, a[196:199] offset:1152\n\t"
        "ds_write_b128 v185, a[200:203] offset:2304\n\t"
        "ds_write_b128 v185, a[204:207] offset:3456\n\t"
        "s_waitcnt lgkmcnt(0)\n\t"
        "ds_read_b64_tr_b16 a[192:193], v186 offset:0\n\t"
        "ds_read_b64_tr_b16 a[194:195], v186 offset:2304\n\t"
        "ds_read_b64_tr_b16 a[196:197], v186 offset:32\n\t"
        "ds_read_b64_tr_b16 a[198:199], v186 offset:2336\n\t"
        "ds_read_b64_tr_b16 a[200:201], v186 offset:64\n\t"
        "ds_read_b64_tr_b16 a[202:203], v186 offset:2368\n\t"
        "ds_read_b64_tr_b16 a[204:205], v186 offset:96\n\t"
        "ds_read_b64_tr_b16 a[206:207], v186 offset:2400\n\t"
        "ds_write_b128 v185, a[208:211] offset:5120\n\t"
        "ds_write_b128 v185, a[212:215] offset:6272\n\t"
        "ds_write_b128 v185, a[216:219] offset:7424\n\t"
        "ds_write_b128 v185, a[220:223] offset:8576\n\t"
        "s_waitcnt lgkmcnt(0)\n\t"
        "ds_read_b64_tr_b16 a[208:209], v186 offset:5120\n\t"
        "ds_read_b64_tr_b16 a[210:211], v186 offset:7424\n\t"
        "ds_read_b64_tr_b16 a[212:213], v186 offset:5152\n\t"
        "ds_read_b64_tr_b16 a[214:215], v186 offset:7456\n\t"
        "ds_read_b64_tr_b16 a[216:217], v186 offset:5184\n\t"
        "ds_read_b64_tr_b16 a[218:219], v186 offset:7488\n\t"
        "ds_read_b64_tr_b16 a[220:221], v186 offset:5216\n\t"
        "ds_read_b64_tr_b16 a[222:223], v186 offset:7520\n\t"
        "ds_write_b128 v185, a[224:227] offset:0\n\t"
        "ds_write_b128 v185, a[228:231] offset:1152\n\t"
        "ds_write_b128 v185, a[232:235] offset:2304\n\t"
        "ds_write_b128 v185, a[236:239] offset:3456\n\t"
        "s_waitcnt lgkmcnt(0)\n\t"
        "ds_read_b64_tr_b16 a[224:225], v186 offset:0\n\t"
        "ds_read_b64_tr_b16 a[226:227], v186 offset:2304\n\t"
        "ds_read_b64_tr_b16 a[228:229], v186 offset:32\n\t"
        "ds_read_b64_tr_b16 a[230:231], v186 offset:2336\n\t"
        "ds_read_b64_tr_b16 a[232:233], v186 offset:64\n\t"
        "ds_read_b64_tr_b16 a[234:235], v186 offset:2368\n\t"
        "ds_read_b64_tr_b16 a[236:237], v186 offset:96\n\t"
        "ds_read_b64_tr_b16 a[238:239], v186 offset:2400\n\t"
        "ds_write_b128 v185, a[240:243] offset:5120\n\t"
        "ds_write_b128 v185, a[244:247] offset:6272\n\t"
        "ds_write_b128 v185, a[248:251] offset:7424\n\t"
        "ds_write_b128 v185, a[252:255] offset:8576\n\t"
        "s_waitcnt lgkmcnt(0)\n\t"
        "ds_read_b64_tr_b16 a[240:241], v186 offset:5120\n\t"
        "ds_read_b64_tr_b16 a[242:243], v186 offset:7424\n\t"
        "ds_read_b64_tr_b16 a[244:245], v186 offset:5152\n\t"
        "ds_read_b64_tr_b16 a[246:247], v186 offset:7456\n\t"
        "ds_read_b64_tr_b16 a[248:249], v186 offset:5184\n\t"
        "ds_read_b64_tr_b16 a[250:251], v186 offset:7488\n\t"
        "ds_read_b64_tr_b16 a[252:253], v186 offset:5216\n\t"
        "ds_read_b64_tr_b16 a[254:255], v186 offset:7520\n\t"
        "s_waitcnt lgkmcnt(0)\n\t"
        "v_mov_b32_e32 v240, v176\n\t"
        "v_mov_b32_e32 v241, v177\n\t"
        "v_mov_b32_e32 v242, v178\n\t"
        "v_mov_b32_e32 v243, v179\n\t"
        "v_mov_b32_e32 v244, v180\n\t"
        "v_mov_b32_e32 v245, v181\n\t"
        "v_mov_b32_e32 v246, v182\n\t"
        "v_mov_b32_e32 v247, v183\n\t"
        "v_mov_b32_e32 v248, v184\n\t"
        "v_mov_b32_e32 v205, %[tag]\n\t"
        "v_mov_b32_e32 v207, %[tag]\n\t"
        "v_mov_b32_e32 v209, %[tag]\n\t"
        "v_mov_b32_e32 v211, %[tag]\n\t"
        "v_mov_b32_e32 v213, %[tag]\n\t"
        "v_mov_b32_e32 v215, %[tag]\n\t"
        "v_mov_b32_e32 v217, %[tag]\n\t"
        "v_mov_b32_e32 v219, %[tag]\n\t"
        "s_mov_b32 s36, 0xf0000000\n\t"
        "s_mov_b32 s37, 0\n\t"
        "s_mov_b32 s38, -1\n\t"
        "s_mov_b32 s39, -1\n\t"
        "s_mov_b32 s40, 0\n\t"
        "s_mov_b32 s43, 0\n\t"
        "s_lshl_b32 s45, %[w], 10\n\t"
        "s_add_u32 s45, s45, 69632\n\t"
        "s_lshl_b32 s44, %[rho], 1\n\t"
        "s_mov_b64 exec, 0\n\t"
        "s_branch .Lhk0%=\n\t"
        ".Lhr%=:\n\t"
        "s_mov_b64 exec, -1\n\t"
        "s_barrier\n\t"
        "s_mov_b32 s97, 0\n\t"
        "v_and_b32_e32 v186, 0x7c, v242\n\t"
        "s_mov_b32 s80, %c[fprep]\n\t"
        "s_mov_b32 s81, %c[fconv]\n\t"
        ".Lo1%=:\n\t"
        "buffer_load_dword v187, v186, %[rd], s80 offen sc1\n\t"
        "s_add_u32 s97, s97, 1\n\t"
        "s_cmp_lt_u32 s97, 0x200000\n\t"
        "s_cbranch_scc0 .Lgu%=\n\t"
        "s_waitcnt vmcnt(0)\n\t"
        "v_cmp_ne_u32_e32 vcc, %[tag], v187\n\t"
        "s_nop 3\n\t"
        "s_cbranch_vccz .Lo2%=\n\t"
        "s_sleep 8\n\t"
        "s_branch .Lo1%=\n\t"
        ".Lo2%=:\n\t"
        "buffer_load_dword v187, off, %[rd], s81 sc1\n\t"
        "s_add_u32 s97, s97, 1\n\t"
        "s_cmp_lt_u32 s97, 0x200000\n\t"
        "s_cbranch_scc0 .Lgu%=\n\t"
        "s_waitcnt vmcnt(0)\n\t"
        "v_cmp_gt_u32_e32 vcc, 0x100, v187\n\t"
        "s_nop 3\n\t"
        "s_cbranch_vccz .Lo3%=\n\t"
        "s_sleep 8\n\t"
        "s_branch .Lo2%=\n\t"
        ".Lo3%=:\n\t"
        ".Lnew%=:\n\t"
        "s_lshl_b32 s85, s37, 1\n\t"
        "s_and_b32 s80, s40, 0x3fffffff\n\t"
        "s_lshr_b32 s80, s80, s85\n\t"
        "s_andn2_b32 s80, 3, s80\n\t"
        "s_cbranch_scc0 .Lgo%=\n\t"
        "s_cmp_lt_i32 s39, 0\n\t"
        "s_cbranch_scc1 .Lo4%=\n\t"
        "s_waitcnt vmcnt(0)\n\t"
        "s_add_i32 s80, s39, 0\n\t"
        "s_lshl_b32 s80, s80, 15\n\t"
        "s_or_b32 s81, s39, %[snd]\n\t"
        "s_cmp_lt_i32 s81, 0\n\t"
        "s_cselect_b32 s83, s36, s80\n\t"
        "s_cbranch_scc1 .Lo7%=\n\t"
        "s_mov_b32 s97, 0\n\t"
        ".Lo5%=:\n\t"
        "v_xor_b32_e32 v186, %[tag], v161\n\t"
        "v_xor_b32_e32 v187, %[tag], v163\n\t"
        "v_xor_b32_e32 v188, %[tag], v165\n\t"
        "v_or3_b32 v186, v186, v187, v188\n\t"
        "v_xor_b32_e32 v187, %[tag], v167\n\t"
        "v_or_b32_e32 v186, v186, v187\n\t"
        "v_cmp_ne_u32_e32 vcc, 0, v186\n\t"
        "v_sub_f32_e32 v198, 1.0, v184\n\t"
        "v_add_f32_e32 v190, v152, v160\n\t"
        "v_add_f32_e32 v191, v153, v162\n\t"
        "v_add_f32_e32 v192, v154, v164\n\t"
        "v_add_f32_e32 v193, v155, v166\n\t"
        "s_cbranch_vccz .Lo6%=\n\t"
        "s_sleep %c[xslp]\n\t"
        "s_lshl_b32 s82, s83, 2\n\t"
        "buffer_load_dwordx4 v[160:163], v246, %[rd], s82 offen offset:0 sc1\n\t"
        "buffer_load_dwordx4 v[164:167], v246, %[rd], s82 offen offset:1024 sc1\n\t"
        "s_add_u32 s97, s97, 1\n\t"
        "s_cmp_lt_u32 s97, 0x200000\n\t"
        "s_cbranch_scc0 .Lgu%=\n\t"
        "s_waitcnt vmcnt(0)\n\t"
        "s_branch .Lo5%=\n\t"
        ".Lo6%=:\n\t"
        "v_cvt_pk_bf16_f32 v198, 0, v198\n\t"
        "v_cvt_pk_bf16_f32 v190, 0, v190\n\t"
        "v_cvt_pk_bf16_f32 v191, 0, v191\n\t"
        "v_cvt_pk_bf16_f32 v192, 0, v192\n\t"
        "v_cvt_pk_bf16_f32 v193, 0, v193\n\t"
        "v_lshlrev_b32_e32 v186, 16, v180\n\t"
        "v_lshlrev_b32_e32 v194, 16, v176\n\t"
        "v_and_b32_e32 v187, 0xffff0000, v180\n\t"
        "v_and_b32_e32 v195, 0xffff0000, v176\n\t"
        "v_lshlrev_b32_e32 v188, 16, v181\n\t"
        "v_lshlrev_b32_e32 v196, 16, v177\n\t"
        "v_and_b32_e32 v189, 0xffff0000, v181\n\t"
        "v_and_b32_e32 v197, 0xffff0000, v177\n\t"
        "v_mul_f32_e32 v186, v186, v194\n\t"
        "v_mul_f32_e32 v187, v187, v195\n\t"
        "v_mul_f32_e32 v188, v188, v196\n\t"
        "v_mul_f32_e32 v189, v189, v197\n\t"
        "v_mul_f32_e32 v190, v198, v190\n\t"
        "v_mul_f32_e32 v191, v198, v191\n\t"
        "v_mul_f32_e32 v192, v198, v192\n\t"
        "v_mul_f32_e32 v193, v198, v193\n\t"
        "v_cvt_pk_bf16_f32 v186, 0, v186\n\t"
        "v_cvt_pk_bf16_f32 v187, 0, v187\n\t"
        "v_cvt_pk_bf16_f32 v188, 0, v188\n\t"
        "v_cvt_pk_bf16_f32 v189, 0, v189\n\t"
        "v_cvt_pk_bf16_f32 v190, 0, v190\n\t"
        "v_cvt_pk_bf16_f32 v191, 0, v191\n\t"
        "v_cvt_pk_bf16_f32 v192, 0, v192\n\t"
        "v_cvt_pk_bf16_f32 v193, 0, v193\n\t"
        "v_mul_f32_e32 v186, v184, v186\n\t"
        "v_mul_f32_e32 v187, v184, v187\n\t"
        "v_mul_f32_e32 v188, v184, v188\n\t"
        "v_mul_f32_e32 v189, v184, v189\n\t"
        "v_cvt_pk_bf16_f32 v186, 0, v186\n\t"
        "v_cvt_pk_bf16_f32 v187, 0, v187\n\t"
        "v_cvt_pk_bf16_f32 v188, 0, v188\n\t"
        "v_cvt_pk_bf16_f32 v189, 0, v189\n\t"
        "v_add_f32_e32 v186, v186, v190\n\t"
        "v_add_f32_e32 v187, v187, v191\n\t"
        "v_add_f32_e32 v188, v188, v192\n\t"
        "v_add_f32_e32 v189, v189, v193\n\t"
        "v_cvt_pk_bf16_f32 v200, v186, v187\n\t"
        "v_cvt_pk_bf16_f32 v201, v188, v189\n\t"
        ".Lo7%=:\n\t"
        "buffer_store_dwordx2 v[200:201], v245, %[ro], s83 offen\n\t"
        "s_add_i32 s80, s39, 1\n\t"
        "s_lshl_b32 s80, s80, 15\n\t"
        "s_or_b32 s81, s39, %[snd]\n\t"
        "s_cmp_lt_i32 s81, 0\n\t"
        "s_cselect_b32 s83, s36, s80\n\t"
        "s_cbranch_scc1 .Lo10%=\n\t"
        "s_mov_b32 s97, 0\n\t"
        ".Lo8%=:\n\t"
        "v_xor_b32_e32 v186, %[tag], v169\n\t"
        "v_xor_b32_e32 v187, %[tag], v171\n\t"
        "v_xor_b32_e32 v188, %[tag], v173\n\t"
        "v_or3_b32 v186, v186, v187, v188\n\t"
        "v_xor_b32_e32 v187, %[tag], v175\n\t"
        "v_or_b32_e32 v186, v186, v187\n\t"
        "v_cmp_ne_u32_e32 vcc, 0, v186\n\t"
        "v_sub_f32_e32 v198, 1.0, v185\n\t"
        "v_add_f32_e32 v190, v156, v168\n\t"
        "v_add_f32_e32 v191, v157, v170\n\t"
        "v_add_f32_e32 v192, v158, v172\n\t"
        "v_add_f32_e32 v193, v159, v174\n\t"
        "s_cbranch_vccz .Lo9%=\n\t"
        "s_sleep %c[xslp]\n\t"
        "s_lshl_b32 s82, s83, 2\n\t"
        "buffer_load_dwordx4 v[168:171], v246, %[rd], s82 offen offset:0 sc1\n\t"
        "buffer_load_dwordx4 v[172:175], v246, %[rd], s82 offen offset:1024 sc1\n\t"
        "s_add_u32 s97, s97, 1\n\t"
        "s_cmp_lt_u32 s97, 0x200000\n\t"
        "s_cbranch_scc0 .Lgu%=\n\t"
        "s_waitcnt vmcnt(0)\n\t"
        "s_branch .Lo8%=\n\t"
        ".Lo9%=:\n\t"
        "v_cvt_pk_bf16_f32 v198, 0, v198\n\t"
        "v_cvt_pk_bf16_f32 v190, 0, v190\n\t"
        "v_cvt_pk_bf16_f32 v191, 0, v191\n\t"
        "v_cvt_pk_bf16_f32 v192, 0, v192\n\t"
        "v_cvt_pk_bf16_f32 v193, 0, v193\n\t"
        "v_lshlrev_b32_e32 v186, 16, v182\n\t"
        "v_lshlrev_b32_e32 v194, 16, v178\n\t"
        "v_and_b32_e32 v187, 0xffff0000, v182\n\t"
        "v_and_b32_e32 v195, 0xffff0000, v178\n\t"
        "v_lshlrev_b32_e32 v188, 16, v183\n\t"
        "v_lshlrev_b32_e32 v196, 16, v179\n\t"
        "v_and_b32_e32 v189, 0xffff0000, v183\n\t"
        "v_and_b32_e32 v197, 0xffff0000, v179\n\t"
        "v_mul_f32_e32 v186, v186, v194\n\t"
        "v_mul_f32_e32 v187, v187, v195\n\t"
        "v_mul_f32_e32 v188, v188, v196\n\t"
        "v_mul_f32_e32 v189, v189, v197\n\t"
        "v_mul_f32_e32 v190, v198, v190\n\t"
        "v_mul_f32_e32 v191, v198, v191\n\t"
        "v_mul_f32_e32 v192, v198, v192\n\t"
        "v_mul_f32_e32 v193, v198, v193\n\t"
        "v_cvt_pk_bf16_f32 v186, 0, v186\n\t"
        "v_cvt_pk_bf16_f32 v187, 0, v187\n\t"
        "v_cvt_pk_bf16_f32 v188, 0, v188\n\t"
        "v_cvt_pk_bf16_f32 v189, 0, v189\n\t"
        "v_cvt_pk_bf16_f32 v190, 0, v190\n\t"
        "v_cvt_pk_bf16_f32 v191, 0, v191\n\t"
        "v_cvt_pk_bf16_f32 v192, 0, v192\n\t"
        "v_cvt_pk_bf16_f32 v193, 0, v193\n\t"
        "v_mul_f32_e32 v186, v185, v186\n\t"
        "v_mul_f32_e32 v187, v185, v187\n\t"
        "v_mul_f32_e32 v188, v185, v188\n\t"
        "v_mul_f32_e32 v189, v185, v189\n\t"
        "v_cvt_pk_bf16_f32 v186, 0, v186\n\t"
        "v_cvt_pk_bf16_f32 v187, 0, v187\n\t"
        "v_cvt_pk_bf16_f32 v188, 0, v188\n\t"
        "v_cvt_pk_bf16_f32 v189, 0, v189\n\t"
        "v_add_f32_e32 v186, v186, v190\n\t"
        "v_add_f32_e32 v187, v187, v191\n\t"
        "v_add_f32_e32 v188, v188, v192\n\t"
        "v_add_f32_e32 v189, v189, v193\n\t"
        "v_cvt_pk_bf16_f32 v200, v186, v187\n\t"
        "v_cvt_pk_bf16_f32 v201, v188, v189\n\t"
        ".Lo10%=:\n\t"
        "buffer_store_dwordx2 v[200:201], v245, %[ro], s83 offen\n\t"
        "s_mov_b32 s39, -1\n\t"
        ".Lo4%=:\n\t"
        "s_cmp_eq_u32 s37, 15\n\t"
        "s_cbranch_scc1 .Lsol%=\n\t"
        "s_mov_b32 s97, 0\n\t"
        ".Lo11%=:\n\t"
        "s_lshr_b32 s80, s40, s85\n\t"
        "s_andn2_b32 s80, 3, s80\n\t"
        "s_cbranch_scc0 .Lo12%=\n\t"
        "s_cmp_eq_u32 s97, 0\n\t"
        "s_cbranch_scc1 .Lo11%=x\n\t"
        "s_sleep %c[oslp]\n\t"
        ".Lo11%=x:\n\t"
        "buffer_load_dword v220, v242, %[rd], 0 offen sc1\n\t"
        "s_add_u32 s97, s97, 1\n\t"
        "s_cmp_lt_u32 s97, 0x200000\n\t"
        "s_cbranch_scc0 .Lgu%=\n\t"
        "s_waitcnt vmcnt(0)\n\t"
        "v_cmp_eq_u32_e32 vcc, 32, v220\n\t"
        "s_nop 1\n\t"
        "s_or_b32 s40, s40, vcc_lo\n\t"
        "s_branch .Lo11%=\n\t"
        ".Lo12%=:\n\t"
        ".Lgo%=:\n\t"
        "s_lshl_b32 s84, s44, 17\n\t"
        "buffer_load_dwordx4 v[0:3], v240, %[rd], s84 offen offset:0 sc1\n\t"
        "buffer_load_dwordx4 v[4:7], v240, %[rd], s84 offen offset:1024 sc1\n\t"
        "buffer_load_dwordx4 v[8:11], v240, %[rd], s84 offen offset:2048 sc1\n\t"
        "buffer_load_dwordx4 v[12:15], v240, %[rd], s84 offen offset:3072 sc1\n\t"
        "s_add_u32 s84, s84, 8192\n\t"
        "buffer_load_dwordx4 v[16:19], v240, %[rd], s84 offen offset:0 sc1\n\t"
        "buffer_load_dwordx4 v[20:23], v240, %[rd], s84 offen offset:1024 sc1\n\t"
        "buffer_load_dwordx4 v[24:27], v240, %[rd], s84 offen offset:2048 sc1\n\t"
        "buffer_load_dwordx4 v[28:31], v240, %[rd], s84 offen offset:3072 sc1\n\t"
        "s_add_u32 s84, s84, 8192\n\t"
        "buffer_load_dwordx4 v[32:35], v240, %[rd], s84 offen offset:0 sc1\n\t"
        "buffer_load_dwordx4 v[36:39], v240, %[rd], s84 offen offset:1024 sc1\n\t"
        "buffer_load_dwordx4 v[40:43], v240, %[rd], s84 offen offset:2048 sc1\n\t"
        "buffer_load_dwordx4 v[44:47], v240, %[rd], s84 offen offset:3072 sc1\n\t"
        "s_add_u32 s84, s84, 8192\n\t"
        "buffer_load_dwordx4 v[48:51], v240, %[rd], s84 offen offset:0 sc1\n\t"
        "buffer_load_dwordx4 v[52:55], v240, %[rd], s84 offen offset:1024 sc1\n\t"
        "buffer_load_dwordx4 v[56:59], v240, %[rd], s84 offen offset:2048 sc1\n\t"
        "buffer_load_dwordx4 v[60:63], v240, %[rd], s84 offen offset:3072 sc1\n\t"
        "s_sub_u32 s84, s84, 20480\n\t"
        "buffer_load_dwordx4 v[64:67], v240, %[rd], s84 offen offset:0 sc1\n\t"
        "buffer_load_dwordx4 v[68:71], v240, %[rd], s84 offen offset:1024 sc1\n\t"
        "buffer_load_dwordx4 v[72:75], v240, %[rd], s84 offen offset:2048 sc1\n\t"
        "buffer_load_dwordx4 v[76:79], v240, %[rd], s84 offen offset:3072 sc1\n\t"
        "s_add_u32 s84, s84, 8192\n\t"
        "buffer_load_dwordx4 v[80:83], v240, %[rd], s84 offen offset:0 sc1\n\t"
        "buffer_load_dwordx4 v[84:87], v240, %[rd], s84 offen offset:1024 sc1\n\t"
        "buffer_load_dwordx4 v[88:91], v240, %[rd], s84 offen offset:2048 sc1\n\t"
        "buffer_load_dwordx4 v[92:95], v240, %[rd], s84 offen offset:3072 sc1\n\t"
        "s_add_u32 s84, s84, 8192\n\t"
        "buffer_load_dwordx4 v[96:99], v240, %[rd], s84 offen offset:0 sc1\n\t"
        "buffer_load_dwordx4 v[100:103], v240, %[rd], s84 offen offset:1024 sc1\n\t"
        "buffer_load_dwordx4 v[104:107], v240, %[rd], s84 offen offset:2048 sc1\n\t"
        "buffer_load_dwordx4 v[108:111], v240, %[rd], s84 offen offset:3072 sc1\n\t"
        "s_add_u32 s84, s84, 8192\n\t"
        "buffer_load_dwordx4 v[112:115], v240, %[rd], s84 offen offset:0 sc1\n\t"
        "buffer_load_dwordx4 v[116:119], v240, %[rd], s84 offen offset:1024 sc1\n\t"
        "buffer_load_dwordx4 v[120:123], v240, %[rd], s84 offen offset:2048 sc1\n\t"
        "buffer_load_dwordx4 v[124:127], v240, %[rd], s84 offen offset:3072 sc1\n\t"
        "s_add_u32 s84, s84, 8192\n\t"
        "buffer_store_dword v0, off, %[ro], s36\n\t"
        "buffer_store_dword v0, off, %[ro], s36\n\t"
        "buffer_store_dword v0, off, %[ro], s36\n\t"
        "buffer_store_dword v0, off, %[ro], s36\n\t"
        "buffer_store_dword v0, off, %[ro], s36\n\t"
        "buffer_store_dword v0, off, %[ro], s36\n\t"
        "buffer_store_dword v0, off, %[ro], s36\n\t"
        "buffer_store_dword v0, off, %[ro], s36\n\t"
        "buffer_store_dword v0, off, %[ro], s36\n\t"
        "buffer_store_dword v0, off, %[ro], s36\n\t"
        "buffer_store_dword v0, off, %[ro], s36\n\t"
        "buffer_store_dword v0, off, %[ro], s36\n\t"
        "buffer_store_dword v0, off, %[ro], s36\n\t"
        "buffer_store_dword v0, off, %[ro], s36\n\t"
        "buffer_store_dword v0, off, %[ro], s36\n\t"
        "buffer_store_dword v0, off, %[ro], s36\n\t"
        "buffer_store_dword v0, off, %[ro], s36\n\t"
        ".Lrnd%=:\n\t"
        "s_lshl_b32 s85, s37, 1\n\t"
        "s_and_b32 s86, s40, 0x3fffffff\n\t"
        "s_lshr_b32 s86, s86, s85\n\t"
        "s_andn2_b32 s80, 12, s86\n\t"
        "s_cselect_b32 s41, 0, 1\n\t"
        "s_add_u32 s84, s44, 8\n\t"
        "s_lshl_b32 s84, s84, 17\n\t"
        "s_cmp_eq_u32 s41, 0\n\t"
        "s_cselect_b32 s84, s36, s84\n\t"
        "s_cselect_b32 s87, 0, s36\n\t"
        "s_andn2_b32 s80, 48, s86\n\t"
        "s_cselect_b32 s80, 0, s36\n\t"
        "v_add_u32_e32 v222, s43, v247\n\t"
        "s_sub_u32 s81, 33280, s43\n\t"
        "v_add_u32_e32 v223, s81, v248\n\t"
        "buffer_load_dword v220, v242, %[rd], s80 offen sc1\n\t"
        "s_waitcnt vmcnt(49)\n\t"
        "v_mfma_f32_16x16x32_bf16 v[128:131], a[128:131], v[0:3], 0\n\t"
        "ds_read_b128 v[186:189], v223 offset:0\n\t"
        "ds_read_b128 v[190:193], v223 offset:8320\n\t"
        "v_mfma_f32_16x16x32_bf16 v[132:135], a[132:135], v[0:3], 0\n\t"
        "ds_read_b128 v[194:197], v223 offset:16640\n\t"
        "ds_read_b128 v[198:201], v223 offset:24960\n\t"
        "v_mfma_f32_16x16x32_bf16 v[136:139], a[136:139], v[0:3], 0\n\t"
        "s_nop 0\n\t"
        "s_nop 0\n\t"
        "v_mfma_f32_16x16x32_bf16 v[140:143], a[140:143], v[0:3], 0\n\t"
        "s_nop 0\n\t"
        "s_nop 0\n\t"
        "buffer_load_dwordx4 v[0:3], v240, %[rd], s84 offen offset:0 sc1\n\t"
        "s_waitcnt vmcnt(49)\n\t"
        "v_mfma_f32_16x16x32_bf16 v[128:131], a[144:147], v[4:7], v[128:131]\n\t"
        "s_nop 0\n\t"
        "s_nop 0\n\t"
        "v_mfma_f32_16x16x32_bf16 v[132:135], a[148:151], v[4:7], v[132:135]\n\t"
        "s_waitcnt lgkmcnt(0)\n\t"
        "v_add_f32_e32 v186, v186, v190\n\t"
        "v_mfma_f32_16x16x32_bf16 v[136:139], a[152:155], v[4:7], v[136:139]\n\t"
        "v_add_f32_e32 v187, v187, v191\n\t"
        "v_add_f32_e32 v188, v188, v192\n\t"
        "v_mfma_f32_16x16x32_bf16 v[140:143], a[156:159], v[4:7], v[140:143]\n\t"
        "v_add_f32_e32 v189, v189, v193\n\t"
        "v_add_f32_e32 v194, v194, v198\n\t"
        "buffer_load_dwordx4 v[4:7], v240, %[rd], s84 offen offset:1024 sc1\n\t"
        "s_waitcnt vmcnt(49)\n\t"
        "v_mfma_f32_16x16x32_bf16 v[128:131], a[160:163], v[8:11], v[128:131]\n\t"
        "v_add_f32_e32 v195, v195, v199\n\t"
        "v_add_f32_e32 v196, v196, v200\n\t"
        "v_mfma_f32_16x16x32_bf16 v[132:135], a[164:167], v[8:11], v[132:135]\n\t"
        "v_add_f32_e32 v197, v197, v201\n\t"
        "v_add_f32_e32 v204, v186, v194\n\t"
        "v_mfma_f32_16x16x32_bf16 v[136:139], a[168:171], v[8:11], v[136:139]\n\t"
        "v_add_f32_e32 v206, v187, v195\n\t"
        "v_add_f32_e32 v208, v188, v196\n\t"
        "v_mfma_f32_16x16x32_bf16 v[140:143], a[172:175], v[8:11], v[140:143]\n\t"
        "v_add_f32_e32 v210, v189, v197\n\t"
        "s_add_i32 s80, s38, 0\n\t"
        "buffer_load_dwordx4 v[8:11], v240, %[rd], s84 offen offset:2048 sc1\n\t"
        "s_waitcnt vmcnt(47)\n\t"
        "v_mfma_f32_16x16x32_bf16 v[128:131], a[176:179], v[12:15], v[128:131]\n\t"
        "s_lshl_b32 s80, s80, 17\n\t"
        "s_or_b32 s81, s38, %[own]\n\t"
        "v_mfma_f32_16x16x32_bf16 v[132:135], a[180:183], v[12:15], v[132:135]\n\t"
        "s_cmp_lt_i32 s81, 0\n\t"
        "s_cselect_b32 s82, s36, s80\n\t"
        "buffer_store_dwordx4 v[204:207], v246, %[rd], s82 offen offset:0 sc1\n\t"
        "v_mfma_f32_16x16x32_bf16 v[136:139], a[184:187], v[12:15], v[136:139]\n\t"
        "buffer_store_dwordx4 v[208:211], v246, %[rd], s82 offen offset:1024 sc1\n\t"
        "v_mov_b32_e32 v144, v204\n\t"
        "v_mfma_f32_16x16x32_bf16 v[140:143], a[188:191], v[12:15], v[140:143]\n\t"
        "v_mov_b32_e32 v145, v206\n\t"
        "v_mov_b32_e32 v146, v208\n\t"
        "buffer_load_dwordx4 v[12:15], v240, %[rd], s84 offen offset:3072 sc1\n\t"
        "s_add_u32 s84, s84, 8192\n\t"
        "s_waitcnt vmcnt(49)\n\t"
        "v_mfma_f32_16x16x32_bf16 v[128:131], a[192:195], v[16:19], v[128:131]\n\t"
        "v_mov_b32_e32 v147, v210\n\t"
        "ds_read_b128 v[186:189], v223 offset:4160\n\t"
        "v_mfma_f32_16x16x32_bf16 v[132:135], a[196:199], v[16:19], v[132:135]\n\t"
        "ds_read_b128 v[190:193], v223 offset:12480\n\t"
        "ds_read_b128 v[194:197], v223 offset:20800\n\t"
        "v_mfma_f32_16x16x32_bf16 v[136:139], a[200:203], v[16:19], v[136:139]\n\t"
        "ds_read_b128 v[198:201], v223 offset:29120\n\t"
        "s_nop 0\n\t"
        "v_mfma_f32_16x16x32_bf16 v[140:143], a[204:207], v[16:19], v[140:143]\n\t"
        "s_nop 0\n\t"
        "s_nop 0\n\t"
        "buffer_load_dwordx4 v[16:19], v240, %[rd], s84 offen offset:0 sc1\n\t"
        "s_waitcnt vmcnt(49)\n\t"
        "v_mfma_f32_16x16x32_bf16 v[128:131], a[208:211], v[20:23], v[128:131]\n\t"
        "s_nop 0\n\t"
        "s_nop 0\n\t"
        "v_mfma_f32_16x16x32_bf16 v[132:135], a[212:215], v[20:23], v[132:135]\n\t"
        "s_nop 0\n\t"
        "s_waitcnt lgkmcnt(0)\n\t"
        "v_mfma_f32_16x16x32_bf16 v[136:139], a[216:219], v[20:23], v[136:139]\n\t"
        "v_add_f32_e32 v186, v186, v190\n\t"
        "v_add_f32_e32 v187, v187, v191\n\t"
        "v_mfma_f32_16x16x32_bf16 v[140:143], a[220:223], v[20:23], v[140:143]\n\t"
        "v_add_f32_e32 v188, v188, v192\n\t"
        "v_add_f32_e32 v189, v189, v193\n\t"
        "buffer_load_dwordx4 v[20:23], v240, %[rd], s84 offen offset:1024 sc1\n\t"
        "s_waitcnt vmcnt(49)\n\t"
        "v_mfma_f32_16x16x32_bf16 v[128:131], a[224:227], v[24:27], v[128:131]\n\t"
        "v_add_f32_e32 v194, v194, v198\n\t"
        "v_add_f32_e32 v195, v195, v199\n\t"
        "v_mfma_f32_16x16x32_bf16 v[132:135], a[228:231], v[24:27], v[132:135]\n\t"
        "v_add_f32_e32 v196, v196, v200\n\t"
        "v_add_f32_e32 v197, v197, v201\n\t"
        "v_mfma_f32_16x16x32_bf16 v[136:139], a[232:235], v[24:27], v[136:139]\n\t"
        "v_add_f32_e32 v212, v186, v194\n\t"
        "v_add_f32_e32 v214, v187, v195\n\t"
        "v_mfma_f32_16x16x32_bf16 v[140:143], a[236:239], v[24:27], v[140:143]\n\t"
        "v_add_f32_e32 v216, v188, v196\n\t"
        "v_add_f32_e32 v218, v189, v197\n\t"
        "buffer_load_dwordx4 v[24:27], v240, %[rd], s84 offen offset:2048 sc1\n\t"
        "s_waitcnt vmcnt(47)\n\t"
        "v_mfma_f32_16x16x32_bf16 v[128:131], a[240:243], v[28:31], v[128:131]\n\t"
        "s_add_i32 s80, s38, 1\n\t"
        "s_lshl_b32 s80, s80, 17\n\t"
        "v_mfma_f32_16x16x32_bf16 v[132:135], a[244:247], v[28:31], v[132:135]\n\t"
        "s_or_b32 s81, s38, %[own]\n\t"
        "s_cmp_lt_i32 s81, 0\n\t"
        "s_cselect_b32 s82, s36, s80\n\t"
        "v_mfma_f32_16x16x32_bf16 v[136:139], a[248:251], v[28:31], v[136:139]\n\t"
        "buffer_store_dwordx4 v[212:215], v246, %[rd], s82 offen offset:0 sc1\n\t"
        "buffer_store_dwordx4 v[216:219], v246, %[rd], s82 offen offset:1024 sc1\n\t"
        "v_mfma_f32_16x16x32_bf16 v[140:143], a[252:255], v[28:31], v[140:143]\n\t"
        "v_mov_b32_e32 v148, v212\n\t"
        "v_mov_b32_e32 v149, v214\n\t"
        "buffer_load_dwordx4 v[28:31], v240, %[rd], s84 offen offset:3072 sc1\n\t"
        "s_add_u32 s84, s84, 8192\n\t"
        "s_waitcnt vmcnt(49)\n\t"
        "v_mfma_f32_16x16x32_bf16 v[128:131], a[0:3], v[32:35], v[128:131]\n\t"
        "v_mov_b32_e32 v150, v216\n\t"
        "v_mov_b32_e32 v151, v218\n\t"
        "v_mfma_f32_16x16x32_bf16 v[132:135], a[4:7], v[32:35], v[132:135]\n\t"
        "s_waitcnt vmcnt(34)\n\t"
        "s_add_i32 s80, s39, 0\n\t"
        "v_mfma_f32_16x16x32_bf16 v[136:139], a[8:11], v[32:35], v[136:139]\n\t"
        "s_lshl_b32 s80, s80, 15\n\t"
        "s_or_b32 s81, s39, %[snd]\n\t"
        "v_mfma_f32_16x16x32_bf16 v[140:143], a[12:15], v[32:35], v[140:143]\n\t"
        "s_cmp_lt_i32 s81, 0\n\t"
        "s_cselect_b32 s83, s36, s80\n\t"
        "s_cselect_b64 s[88:89], 0, -1\n\t"
        "v_xor_b32_e32 v224, %[tag], v161\n\t"
        "buffer_load_dwordx4 v[32:35], v240, %[rd], s84 offen offset:0 sc1\n\t"
        "s_waitcnt vmcnt(49)\n\t"
        "v_mfma_f32_16x16x32_bf16 v[128:131], a[16:19], v[36:39], v[128:131]\n\t"
        "v_xor_b32_e32 v225, %[tag], v163\n\t"
        "v_xor_b32_e32 v226, %[tag], v165\n\t"
        "v_mfma_f32_16x16x32_bf16 v[132:135], a[20:23], v[36:39], v[132:135]\n\t"
        "v_or3_b32 v224, v224, v225, v226\n\t"
        "v_xor_b32_e32 v225, %[tag], v167\n\t"
        "v_mfma_f32_16x16x32_bf16 v[136:139], a[24:27], v[36:39], v[136:139]\n\t"
        "v_or_b32_e32 v224, v224, v225\n\t"
        "v_cmp_ne_u32_e32 vcc, 0, v224\n\t"
        "v_mfma_f32_16x16x32_bf16 v[140:143], a[28:31], v[36:39], v[140:143]\n\t"
        "v_sub_f32_e32 v236, 1.0, v184\n\t"
        "v_cvt_pk_bf16_f32 v236, 0, v236\n\t"
        "s_and_b64 vcc, vcc, s[88:89]\n\t"
        "s_cbranch_scc1 .Lo13%=\n\t"
        ".Lo14%=:\n\t"
        "buffer_load_dwordx4 v[36:39], v240, %[rd], s84 offen offset:1024 sc1\n\t"
        "s_waitcnt vmcnt(49)\n\t"
        "v_mfma_f32_16x16x32_bf16 v[128:131], a[32:35], v[40:43], v[128:131]\n\t"
        "v_add_f32_e32 v228, v152, v160\n\t"
        "v_add_f32_e32 v229, v153, v162\n\t"
        "v_mfma_f32_16x16x32_bf16 v[132:135], a[36:39], v[40:43], v[132:135]\n\t"
        "v_add_f32_e32 v230, v154, v164\n\t"
        "v_add_f32_e32 v231, v155, v166\n\t"
        "v_mfma_f32_16x16x32_bf16 v[136:139], a[40:43], v[40:43], v[136:139]\n\t"
        "v_cvt_pk_bf16_f32 v228, 0, v228\n\t"
        "v_cvt_pk_bf16_f32 v229, 0, v229\n\t"
        "v_mfma_f32_16x16x32_bf16 v[140:143], a[44:47], v[40:43], v[140:143]\n\t"
        "v_cvt_pk_bf16_f32 v230, 0, v230\n\t"
        "v_cvt_pk_bf16_f32 v231, 0, v231\n\t"
        "buffer_load_dwordx4 v[40:43], v240, %[rd], s84 offen offset:2048 sc1\n\t"
        "s_waitcnt vmcnt(49)\n\t"
        "v_mfma_f32_16x16x32_bf16 v[128:131], a[48:51], v[44:47], v[128:131]\n\t"
        "v_lshlrev_b32_e32 v224, 16, v180\n\t"
        "v_lshlrev_b32_e32 v232, 16, v176\n\t"
        "v_mfma_f32_16x16x32_bf16 v[132:135], a[52:55], v[44:47], v[132:135]\n\t"
        "v_and_b32_e32 v225, 0xffff0000, v180\n\t"
        "v_and_b32_e32 v233, 0xffff0000, v176\n\t"
        "v_mfma_f32_16x16x32_bf16 v[136:139], a[56:59], v[44:47], v[136:139]\n\t"
        "v_lshlrev_b32_e32 v226, 16, v181\n\t"
        "v_lshlrev_b32_e32 v234, 16, v177\n\t"
        "v_mfma_f32_16x16x32_bf16 v[140:143], a[60:63], v[44:47], v[140:143]\n\t"
        "v_and_b32_e32 v227, 0xffff0000, v181\n\t"
        "v_and_b32_e32 v235, 0xffff0000, v177\n\t"
        "buffer_load_dwordx4 v[44:47], v240, %[rd], s84 offen offset:3072 sc1\n\t"
        "s_add_u32 s84, s84, 8192\n\t"
        "s_waitcnt vmcnt(49)\n\t"
        "v_mfma_f32_16x16x32_bf16 v[128:131], a[64:67], v[48:51], v[128:131]\n\t"
        "v_mul_f32_e32 v224, v224, v232\n\t"
        "v_mul_f32_e32 v225, v225, v233\n\t"
        "v_mfma_f32_16x16x32_bf16 v[132:135], a[68:71], v[48:51], v[132:135]\n\t"
        "v_mul_f32_e32 v226, v226, v234\n\t"
        "v_mul_f32_e32 v227, v227, v235\n\t"
        "v_mfma_f32_16x16x32_bf16 v[136:139], a[72:75], v[48:51], v[136:139]\n\t"
        "v_mul_f32_e32 v228, v236, v228\n\t"
        "v_mul_f32_e32 v229, v236, v229\n\t"
        "v_mfma_f32_16x16x32_bf16 v[140:143], a[76:79], v[48:51], v[140:143]\n\t"
        "v_mul_f32_e32 v230, v236, v230\n\t"
        "v_mul_f32_e32 v231, v236, v231\n\t"
        "buffer_load_dwordx4 v[48:51], v240, %[rd], s84 offen offset:0 sc1\n\t"
        "s_waitcnt vmcnt(49)\n\t"
        "v_mfma_f32_16x16x32_bf16 v[128:131], a[80:83], v[52:55], v[128:131]\n\t"
        "v_cvt_pk_bf16_f32 v224, 0, v224\n\t"
        "v_cvt_pk_bf16_f32 v225, 0, v225\n\t"
        "v_mfma_f32_16x16x32_bf16 v[132:135], a[84:87], v[52:55], v[132:135]\n\t"
        "v_cvt_pk_bf16_f32 v226, 0, v226\n\t"
        "v_cvt_pk_bf16_f32 v227, 0, v227\n\t"
        "v_mfma_f32_16x16x32_bf16 v[136:139], a[88:91], v[52:55], v[136:139]\n\t"
        "v_cvt_pk_bf16_f32 v228, 0, v228\n\t"
        "v_cvt_pk_bf16_f32 v229, 0, v229\n\t"
        "v_mfma_f32_16x16x32_bf16 v[140:143], a[92:95], v[52:55], v[140:143]\n\t"
        "v_cvt_pk_bf16_f32 v230, 0, v230\n\t"
        "v_cvt_pk_bf16_f32 v231, 0, v231\n\t"
        "buffer_load_dwordx4 v[52:55], v240, %[rd], s84 offen offset:1024 sc1\n\t"
        "s_waitcnt vmcnt(49)\n\t"
        "v_mfma_f32_16x16x32_bf16 v[128:131], a[96:99], v[56:59], v[128:131]\n\t"
        "v_mul_f32_e32 v224, v184, v224\n\t"
        "v_mul_f32_e32 v225, v184, v225\n\t"
        "v_mfma_f32_16x16x32_bf16 v[132:135], a[100:103], v[56:59], v[132:135]\n\t"
        "v_mul_f32_e32 v226, v184, v226\n\t"
        "v_mul_f32_e32 v227, v184, v227\n\t"
        "v_mfma_f32_16x16x32_bf16 v[136:139], a[104:107], v[56:59], v[136:139]\n\t"
        "v_cvt_pk_bf16_f32 v224, 0, v224\n\t"
        "v_cvt_pk_bf16_f32 v225, 0, v225\n\t"
        "v_mfma_f32_16x16x32_bf16 v[140:143], a[108:111], v[56:59], v[140:143]\n\t"
        "v_cvt_pk_bf16_f32 v226, 0, v226\n\t"
        "v_cvt_pk_bf16_f32 v227, 0, v227\n\t"
        "buffer_load_dwordx4 v[56:59], v240, %[rd], s84 offen offset:2048 sc1\n\t"
        "s_waitcnt vmcnt(48)\n\t"
        "v_mfma_f32_16x16x32_bf16 v[128:131], a[112:115], v[60:63], v[128:131]\n\t"
        "v_add_f32_e32 v224, v224, v228\n\t"
        "v_add_f32_e32 v225, v225, v229\n\t"
        "v_mfma_f32_16x16x32_bf16 v[132:135], a[116:119], v[60:63], v[132:135]\n\t"
        "v_add_f32_e32 v226, v226, v230\n\t"
        "v_add_f32_e32 v227, v227, v231\n\t"
        "v_mfma_f32_16x16x32_bf16 v[136:139], a[120:123], v[60:63], v[136:139]\n\t"
        "v_cvt_pk_bf16_f32 v238, v224, v225\n\t"
        "v_cvt_pk_bf16_f32 v239, v226, v227\n\t"
        "v_mfma_f32_16x16x32_bf16 v[140:143], a[124:127], v[60:63], v[140:143]\n\t"
        "buffer_store_dwordx2 v[238:239], v245, %[ro], s83 offen\n\t"
        "s_add_i32 s80, s38, 0\n\t"
        "buffer_load_dwordx4 v[60:63], v240, %[rd], s84 offen offset:3072 sc1\n\t"
        "s_sub_u32 s84, s84, 20480\n\t"
        "ds_write_b128 v222, v[128:131] offset:0\n\t"
        "ds_write_b128 v222, v[132:135] offset:1040\n\t"
        "ds_write_b128 v222, v[136:139] offset:2080\n\t"
        "s_nop 0\n\t"
        "ds_write_b128 v222, v[140:143] offset:3120\n\t"
        "s_waitcnt vmcnt(47)\n\t"
        "v_mfma_f32_16x16x32_bf16 v[128:131], a[128:131], v[64:67], 0\n\t"
        "s_lshl_b32 s80, s80, 6\n\t"
        "s_or_b32 s81, s38, %[snd]\n\t"
        "v_mfma_f32_16x16x32_bf16 v[132:135], a[132:135], v[64:67], 0\n\t"
        "s_cmp_lt_i32 s81, 0\n\t"
        "s_cselect_b32 s82, s36, s80\n\t"
        "buffer_load_dword v184, v244, %[rd], s82 offen sc1\n\t"
        "v_mfma_f32_16x16x32_bf16 v[136:139], a[136:139], v[64:67], 0\n\t"
        "s_lshl_b32 s80, s80, 1\n\t"
        "s_cmp_lt_i32 s81, 0\n\t"
        "s_cselect_b32 s82, s36, s80\n\t"
        "v_mfma_f32_16x16x32_bf16 v[140:143], a[140:143], v[64:67], 0\n\t"
        "buffer_load_dwordx2 v[180:181], v243, %[rd], s82 offen sc1\n\t"
        "s_lshl_b32 s80, s80, 8\n\t"
        "buffer_load_dwordx4 v[64:67], v240, %[rd], s84 offen offset:0 sc1\n\t"
        "s_waitcnt vmcnt(46)\n\t"
        "v_mfma_f32_16x16x32_bf16 v[128:131], a[144:147], v[68:71], v[128:131]\n\t"
        "s_cmp_lt_i32 s81, 0\n\t"
        "s_cselect_b32 s82, s36, s80\n\t"
        "buffer_load_dwordx2 v[176:177], v245, %[rx], s82 offen\n\t"
        "v_mfma_f32_16x16x32_bf16 v[132:135], a[148:151], v[68:71], v[132:135]\n\t"
        "s_add_i32 s80, s38, 0\n\t"
        "s_lshl_b32 s80, s80, 17\n\t"
        "v_mfma_f32_16x16x32_bf16 v[136:139], a[152:155], v[68:71], v[136:139]\n\t"
        "s_or_b32 s81, s38, %[snd]\n\t"
        "s_cmp_lt_i32 s81, 0\n\t"
        "s_cselect_b32 s82, s36, s80\n\t"
        "v_mfma_f32_16x16x32_bf16 v[140:143], a[156:159], v[68:71], v[140:143]\n\t"
        "buffer_load_dwordx4 v[160:163], v246, %[rd], s82 offen offset:0 sc1\n\t"
        "buffer_load_dwordx4 v[164:167], v246, %[rd], s82 offen offset:1024 sc1\n\t"
        "buffer_load_dwordx4 v[68:71], v240, %[rd], s84 offen offset:1024 sc1\n\t"
        "s_waitcnt vmcnt(49)\n\t"
        "v_mfma_f32_16x16x32_bf16 v[128:131], a[160:163], v[72:75], v[128:131]\n\t"
        "s_waitcnt vmcnt(34)\n\t"
        "v_mfma_f32_16x16x32_bf16 v[132:135], a[164:167], v[72:75], v[132:135]\n\t"
        "s_add_i32 s80, s39, 1\n\t"
        "s_lshl_b32 s80, s80, 15\n\t"
        "v_mfma_f32_16x16x32_bf16 v[136:139], a[168:171], v[72:75], v[136:139]\n\t"
        "s_or_b32 s81, s39, %[snd]\n\t"
        "s_cmp_lt_i32 s81, 0\n\t"
        "s_cselect_b32 s83, s36, s80\n\t"
        "s_cselect_b64 s[88:89], 0, -1\n\t"
        "v_mfma_f32_16x16x32_bf16 v[140:143], a[172:175], v[72:75], v[140:143]\n\t"
        "v_xor_b32_e32 v224, %[tag], v169\n\t"
        "v_xor_b32_e32 v225, %[tag], v171\n\t"
        "buffer_load_dwordx4 v[72:75], v240, %[rd], s84 offen offset:2048 sc1\n\t"
        "s_waitcnt vmcnt(49)\n\t"
        "v_mfma_f32_16x16x32_bf16 v[128:131], a[176:179], v[76:79], v[128:131]\n\t"
        "v_xor_b32_e32 v226, %[tag], v173\n\t"
        "v_or3_b32 v224, v224, v225, v226\n\t"
        "v_mfma_f32_16x16x32_bf16 v[132:135], a[180:183], v[76:79], v[132:135]\n\t"
        "v_xor_b32_e32 v225, %[tag], v175\n\t"
        "v_or_b32_e32 v224, v224, v225\n\t"
        "v_mfma_f32_16x16x32_bf16 v[136:139], a[184:187], v[76:79], v[136:139]\n\t"
        "v_cmp_ne_u32_e32 vcc, 0, v224\n\t"
        "v_sub_f32_e32 v236, 1.0, v185\n\t"
        "v_mfma_f32_16x16x32_bf16 v[140:143], a[188:191], v[76:79], v[140:143]\n\t"
        "v_cvt_pk_bf16_f32 v236, 0, v236\n\t"
        "s_and_b64 vcc, vcc, s[88:89]\n\t"
        "s_cbranch_scc1 .Lo15%=\n\t"
        ".Lo16%=:\n\t"
        "v_add_f32_e32 v228, v156, v168\n\t"
        "buffer_load_dwordx4 v[76:79], v240, %[rd], s84 offen offset:3072 sc1\n\t"
        "s_add_u32 s84, s84, 8192\n\t"
        "s_waitcnt vmcnt(49)\n\t"
        "v_mfma_f32_16x16x32_bf16 v[128:131], a[192:195], v[80:83], v[128:131]\n\t"
        "v_add_f32_e32 v229, v157, v170\n\t"
        "v_add_f32_e32 v230, v158, v172\n\t"
        "v_mfma_f32_16x16x32_bf16 v[132:135], a[196:199], v[80:83], v[132:135]\n\t"
        "v_add_f32_e32 v231, v159, v174\n\t"
        "v_cvt_pk_bf16_f32 v228, 0, v228\n\t"
        "v_mfma_f32_16x16x32_bf16 v[136:139], a[200:203], v[80:83], v[136:139]\n\t"
        "v_cvt_pk_bf16_f32 v229, 0, v229\n\t"
        "v_cvt_pk_bf16_f32 v230, 0, v230\n\t"
        "v_mfma_f32_16x16x32_bf16 v[140:143], a[204:207], v[80:83], v[140:143]\n\t"
        "v_cvt_pk_bf16_f32 v231, 0, v231\n\t"
        "v_lshlrev_b32_e32 v224, 16, v182\n\t"
        "buffer_load_dwordx4 v[80:83], v240, %[rd], s84 offen offset:0 sc1\n\t"
        "s_waitcnt vmcnt(49)\n\t"
        "v_mfma_f32_16x16x32_bf16 v[128:131], a[208:211], v[84:87], v[128:131]\n\t"
        "v_lshlrev_b32_e32 v232, 16, v178\n\t"
        "v_and_b32_e32 v225, 0xffff0000, v182\n\t"
        "v_mfma_f32_16x16x32_bf16 v[132:135], a[212:215], v[84:87], v[132:135]\n\t"
        "v_and_b32_e32 v233, 0xffff0000, v178\n\t"
        "v_lshlrev_b32_e32 v226, 16, v183\n\t"
        "v_mfma_f32_16x16x32_bf16 v[136:139], a[216:219], v[84:87], v[136:139]\n\t"
        "v_lshlrev_b32_e32 v234, 16, v179\n\t"
        "v_and_b32_e32 v227, 0xffff0000, v183\n\t"
        "v_mfma_f32_16x16x32_bf16 v[140:143], a[220:223], v[84:87], v[140:143]\n\t"
        "v_and_b32_e32 v235, 0xffff0000, v179\n\t"
        "v_mul_f32_e32 v224, v224, v232\n\t"
        "buffer_load_dwordx4 v[84:87], v240, %[rd], s84 offen offset:1024 sc1\n\t"
        "s_waitcnt vmcnt(49)\n\t"
        "v_mfma_f32_16x16x32_bf16 v[128:131], a[224:227], v[88:91], v[128:131]\n\t"
        "v_mul_f32_e32 v225, v225, v233\n\t"
        "v_mul_f32_e32 v226, v226, v234\n\t"
        "v_mfma_f32_16x16x32_bf16 v[132:135], a[228:231], v[88:91], v[132:135]\n\t"
        "v_mul_f32_e32 v227, v227, v235\n\t"
        "v_mul_f32_e32 v228, v236, v228\n\t"
        "v_mfma_f32_16x16x32_bf16 v[136:139], a[232:235], v[88:91], v[136:139]\n\t"
        "v_mul_f32_e32 v229, v236, v229\n\t"
        "v_mul_f32_e32 v230, v236, v230\n\t"
        "v_mfma_f32_16x16x32_bf16 v[140:143], a[236:239], v[88:91], v[140:143]\n\t"
        "v_mul_f32_e32 v231, v236, v231\n\t"
        "v_cvt_pk_bf16_f32 v224, 0, v224\n\t"
        "buffer_load_dwordx4 v[88:91], v240, %[rd], s84 offen offset:2048 sc1\n\t"
        "s_waitcnt vmcnt(49)\n\t"
        "v_mfma_f32_16x16x32_bf16 v[128:131], a[240:243], v[92:95], v[128:131]\n\t"
        "v_cvt_pk_bf16_f32 v225, 0, v225\n\t"
        "v_cvt_pk_bf16_f32 v226, 0, v226\n\t"
        "v_mfma_f32_16x16x32_bf16 v[132:135], a[244:247], v[92:95], v[132:135]\n\t"
        "v_cvt_pk_bf16_f32 v227, 0, v227\n\t"
        "v_cvt_pk_bf16_f32 v228, 0, v228\n\t"
        "v_mfma_f32_16x16x32_bf16 v[136:139], a[248:251], v[92:95], v[136:139]\n\t"
        "v_cvt_pk_bf16_f32 v229, 0, v229\n\t"
        "v_cvt_pk_bf16_f32 v230, 0, v230\n\t"
        "v_mfma_f32_16x16x32_bf16 v[140:143], a[252:255], v[92:95], v[140:143]\n\t"
        "v_cvt_pk_bf16_f32 v231, 0, v231\n\t"
        "v_mul_f32_e32 v224, v185, v224\n\t"
        "buffer_load_dwordx4 v[92:95], v240, %[rd], s84 offen offset:3072 sc1\n\t"
        "s_add_u32 s84, s84, 8192\n\t"
        "buffer_load_dword v221, v242, %[rd], s87 offen sc1\n\t"
        "s_waitcnt vmcnt(49)\n\t"
        "v_mfma_f32_16x16x32_bf16 v[128:131], a[0:3], v[96:99], v[128:131]\n\t"
        "v_mul_f32_e32 v225, v185, v225\n\t"
        "v_mul_f32_e32 v226, v185, v226\n\t"
        "v_mfma_f32_16x16x32_bf16 v[132:135], a[4:7], v[96:99], v[132:135]\n\t"
        "v_mul_f32_e32 v227, v185, v227\n\t"
        "v_cvt_pk_bf16_f32 v224, 0, v224\n\t"
        "v_mfma_f32_16x16x32_bf16 v[136:139], a[8:11], v[96:99], v[136:139]\n\t"
        "v_cvt_pk_bf16_f32 v225, 0, v225\n\t"
        "v_cvt_pk_bf16_f32 v226, 0, v226\n\t"
        "v_mfma_f32_16x16x32_bf16 v[140:143], a[12:15], v[96:99], v[140:143]\n\t"
        "v_cvt_pk_bf16_f32 v227, 0, v227\n\t"
        "v_add_f32_e32 v224, v224, v228\n\t"
        "buffer_load_dwordx4 v[96:99], v240, %[rd], s84 offen offset:0 sc1\n\t"
        "s_waitcnt vmcnt(48)\n\t"
        "v_mfma_f32_16x16x32_bf16 v[128:131], a[16:19], v[100:103], v[128:131]\n\t"
        "v_add_f32_e32 v225, v225, v229\n\t"
        "v_add_f32_e32 v226, v226, v230\n\t"
        "v_mfma_f32_16x16x32_bf16 v[132:135], a[20:23], v[100:103], v[132:135]\n\t"
        "v_add_f32_e32 v227, v227, v231\n\t"
        "v_cvt_pk_bf16_f32 v238, v224, v225\n\t"
        "v_mfma_f32_16x16x32_bf16 v[136:139], a[24:27], v[100:103], v[136:139]\n\t"
        "v_cvt_pk_bf16_f32 v239, v226, v227\n\t"
        "buffer_store_dwordx2 v[238:239], v245, %[ro], s83 offen\n\t"
        "v_mfma_f32_16x16x32_bf16 v[140:143], a[28:31], v[100:103], v[140:143]\n\t"
        "s_add_i32 s80, s38, 1\n\t"
        "s_lshl_b32 s80, s80, 6\n\t"
        "buffer_load_dwordx4 v[100:103], v240, %[rd], s84 offen offset:1024 sc1\n\t"
        "s_waitcnt vmcnt(47)\n\t"
        "v_mfma_f32_16x16x32_bf16 v[128:131], a[32:35], v[104:107], v[128:131]\n\t"
        "s_or_b32 s81, s38, %[snd]\n\t"
        "s_cmp_lt_i32 s81, 0\n\t"
        "s_cselect_b32 s82, s36, s80\n\t"
        "v_mfma_f32_16x16x32_bf16 v[132:135], a[36:39], v[104:107], v[132:135]\n\t"
        "buffer_load_dword v185, v244, %[rd], s82 offen sc1\n\t"
        "s_lshl_b32 s80, s80, 1\n\t"
        "v_mfma_f32_16x16x32_bf16 v[136:139], a[40:43], v[104:107], v[136:139]\n\t"
        "s_cmp_lt_i32 s81, 0\n\t"
        "s_cselect_b32 s82, s36, s80\n\t"
        "buffer_load_dwordx2 v[182:183], v243, %[rd], s82 offen sc1\n\t"
        "v_mfma_f32_16x16x32_bf16 v[140:143], a[44:47], v[104:107], v[140:143]\n\t"
        "s_lshl_b32 s80, s80, 8\n\t"
        "s_cmp_lt_i32 s81, 0\n\t"
        "s_cselect_b32 s82, s36, s80\n\t"
        "buffer_load_dwordx4 v[104:107], v240, %[rd], s84 offen offset:2048 sc1\n\t"
        "s_waitcnt vmcnt(46)\n\t"
        "v_mfma_f32_16x16x32_bf16 v[128:131], a[48:51], v[108:111], v[128:131]\n\t"
        "buffer_load_dwordx2 v[178:179], v245, %[rx], s82 offen\n\t"
        "s_add_i32 s80, s38, 1\n\t"
        "v_mfma_f32_16x16x32_bf16 v[132:135], a[52:55], v[108:111], v[132:135]\n\t"
        "s_lshl_b32 s80, s80, 17\n\t"
        "s_or_b32 s81, s38, %[snd]\n\t"
        "v_mfma_f32_16x16x32_bf16 v[136:139], a[56:59], v[108:111], v[136:139]\n\t"
        "s_cmp_lt_i32 s81, 0\n\t"
        "s_cselect_b32 s82, s36, s80\n\t"
        "buffer_load_dwordx4 v[168:171], v246, %[rd], s82 offen offset:0 sc1\n\t"
        "v_mfma_f32_16x16x32_bf16 v[140:143], a[60:63], v[108:111], v[140:143]\n\t"
        "buffer_load_dwordx4 v[172:175], v246, %[rd], s82 offen offset:1024 sc1\n\t"
        "buffer_load_dwordx4 v[108:111], v240, %[rd], s84 offen offset:3072 sc1\n\t"
        "s_add_u32 s84, s84, 8192\n\t"
        "s_waitcnt vmcnt(49)\n\t"
        "v_mfma_f32_16x16x32_bf16 v[128:131], a[64:67], v[112:115], v[128:131]\n\t"
        "v_mfma_f32_16x16x32_bf16 v[132:135], a[68:71], v[112:115], v[132:135]\n\t"
        "v_mfma_f32_16x16x32_bf16 v[136:139], a[72:75], v[112:115], v[136:139]\n\t"
        "v_mfma_f32_16x16x32_bf16 v[140:143], a[76:79], v[112:115], v[140:143]\n\t"
        "buffer_load_dwordx4 v[112:115], v240, %[rd], s84 offen offset:0 sc1\n\t"
        "s_waitcnt vmcnt(49)\n\t"
        "v_mfma_f32_16x16x32_bf16 v[128:131], a[80:83], v[116:119], v[128:131]\n\t"
        "v_mfma_f32_16x16x32_bf16 v[132:135], a[84:87], v[116:119], v[132:135]\n\t"
        "v_mfma_f32_16x16x32_bf16 v[136:139], a[88:91], v[116:119], v[136:139]\n\t"
        "v_mfma_f32_16x16x32_bf16 v[140:143], a[92:95], v[116:119], v[140:143]\n\t"
        "buffer_load_dwordx4 v[116:119], v240, %[rd], s84 offen offset:1024 sc1\n\t"
        "s_waitcnt vmcnt(49)\n\t"
        "v_mfma_f32_16x16x32_bf16 v[128:131], a[96:99], v[120:123], v[128:131]\n\t"
        "v_mfma_f32_16x16x32_bf16 v[132:135], a[100:103], v[120:123], v[132:135]\n\t"
        "v_mfma_f32_16x16x32_bf16 v[136:139], a[104:107], v[120:123], v[136:139]\n\t"
        "v_mfma_f32_16x16x32_bf16 v[140:143], a[108:111], v[120:123], v[140:143]\n\t"
        "buffer_load_dwordx4 v[120:123], v240, %[rd], s84 offen offset:2048 sc1\n\t"
        "s_waitcnt vmcnt(49)\n\t"
        "v_mfma_f32_16x16x32_bf16 v[128:131], a[112:115], v[124:127], v[128:131]\n\t"
        "v_mfma_f32_16x16x32_bf16 v[132:135], a[116:119], v[124:127], v[132:135]\n\t"
        "v_mfma_f32_16x16x32_bf16 v[136:139], a[120:123], v[124:127], v[136:139]\n\t"
        "v_mfma_f32_16x16x32_bf16 v[140:143], a[124:127], v[124:127], v[140:143]\n\t"
        "buffer_load_dwordx4 v[124:127], v240, %[rd], s84 offen offset:3072 sc1\n\t"
        "s_add_u32 s84, s84, 8192\n\t"
        "s_nop 2\n\t"
        "ds_write_b128 v222, v[128:131] offset:4160\n\t"
        "ds_write_b128 v222, v[132:135] offset:5200\n\t"
        "ds_write_b128 v222, v[136:139] offset:6240\n\t"
        "ds_write_b128 v222, v[140:143] offset:7280\n\t"
        "s_waitcnt vmcnt(49)\n\t"
        "v_cmp_eq_u32_e32 vcc, 32, v220\n\t"
        "s_nop 1\n\t"
        "s_or_b32 s40, s40, vcc_lo\n\t"
        "s_waitcnt lgkmcnt(0)\n\t"
        "s_barrier\n\t"
        "s_cmp_eq_u32 s41, 0\n\t"
        "s_cbranch_scc1 .Lfl%=\n\t"
        "v_mov_b32_e32 v152, v144\n\t"
        "v_mov_b32_e32 v153, v145\n\t"
        "v_mov_b32_e32 v154, v146\n\t"
        "v_mov_b32_e32 v155, v147\n\t"
        "v_mov_b32_e32 v156, v148\n\t"
        "v_mov_b32_e32 v157, v149\n\t"
        "v_mov_b32_e32 v158, v150\n\t"
        "v_mov_b32_e32 v159, v151\n\t"
        "s_mov_b32 s39, s38\n\t"
        "s_mov_b32 s38, s44\n\t"
        "s_add_u32 s37, s37, 1\n\t"
        "s_add_u32 s44, s44, 8\n\t"
        "s_sub_u32 s43, 33280, s43\n\t"
        "s_branch .Lrnd%=\n\t"
        ".Lfl%=:\n\t"
        "v_add_u32_e32 v223, s43, v248\n\t"
        "buffer_load_dword v220, v242, %[rd], 0 offen sc1\n\t"
        "ds_read_b128 v[186:189], v223 offset:0\n\t"
        "ds_read_b128 v[190:193], v223 offset:8320\n\t"
        "ds_read_b128 v[194:197], v223 offset:16640\n\t"
        "ds_read_b128 v[198:201], v223 offset:24960\n\t"
        "s_waitcnt lgkmcnt(0)\n\t"
        "v_add_f32_e32 v186, v186, v190\n\t"
        "v_add_f32_e32 v187, v187, v191\n\t"
        "v_add_f32_e32 v188, v188, v192\n\t"
        "v_add_f32_e32 v189, v189, v193\n\t"
        "v_add_f32_e32 v194, v194, v198\n\t"
        "v_add_f32_e32 v195, v195, v199\n\t"
        "v_add_f32_e32 v196, v196, v200\n\t"
        "v_add_f32_e32 v197, v197, v201\n\t"
        "v_add_f32_e32 v204, v186, v194\n\t"
        "v_add_f32_e32 v206, v187, v195\n\t"
        "v_add_f32_e32 v208, v188, v196\n\t"
        "v_add_f32_e32 v210, v189, v197\n\t"
        "s_add_i32 s80, s44, 0\n\t"
        "s_lshl_b32 s80, s80, 17\n\t"
        "s_or_b32 s81, s44, %[own]\n\t"
        "s_cmp_lt_i32 s81, 0\n\t"
        "s_cselect_b32 s82, s36, s80\n\t"
        "buffer_store_dwordx4 v[204:207], v246, %[rd], s82 offen offset:0 sc1\n\t"
        "buffer_store_dwordx4 v[208:211], v246, %[rd], s82 offen offset:1024 sc1\n\t"
        "v_mov_b32_e32 v128, v204\n\t"
        "v_mov_b32_e32 v129, v206\n\t"
        "v_mov_b32_e32 v130, v208\n\t"
        "v_mov_b32_e32 v131, v210\n\t"
        "ds_read_b128 v[186:189], v223 offset:4160\n\t"
        "ds_read_b128 v[190:193], v223 offset:12480\n\t"
        "ds_read_b128 v[194:197], v223 offset:20800\n\t"
        "ds_read_b128 v[198:201], v223 offset:29120\n\t"
        "s_waitcnt lgkmcnt(0)\n\t"
        "v_add_f32_e32 v186, v186, v190\n\t"
        "v_add_f32_e32 v187, v187, v191\n\t"
        "v_add_f32_e32 v188, v188, v192\n\t"
        "v_add_f32_e32 v189, v189, v193\n\t"
        "v_add_f32_e32 v194, v194, v198\n\t"
        "v_add_f32_e32 v195, v195, v199\n\t"
        "v_add_f32_e32 v196, v196, v200\n\t"
        "v_add_f32_e32 v197, v197, v201\n\t"
        "v_add_f32_e32 v212, v186, v194\n\t"
        "v_add_f32_e32 v214, v187, v195\n\t"
        "v_add_f32_e32 v216, v188, v196\n\t"
        "v_add_f32_e32 v218, v189, v197\n\t"
        "s_add_i32 s80, s44, 1\n\t"
        "s_lshl_b32 s80, s80, 17\n\t"
        "s_or_b32 s81, s44, %[own]\n\t"
        "s_cmp_lt_i32 s81, 0\n\t"
        "s_cselect_b32 s82, s36, s80\n\t"
        "buffer_store_dwordx4 v[212:215], v246, %[rd], s82 offen offset:0 sc1\n\t"
        "buffer_store_dwordx4 v[216:219], v246, %[rd], s82 offen offset:1024 sc1\n\t"
        "v_mov_b32_e32 v132, v212\n\t"
        "v_mov_b32_e32 v133, v214\n\t"
        "v_mov_b32_e32 v134, v216\n\t"
        "v_mov_b32_e32 v135, v218\n\t"
        "s_waitcnt vmcnt(4)\n\t"
        "v_cmp_eq_u32_e32 vcc, 32, v220\n\t"
        "s_nop 1\n\t"
        "s_or_b32 s40, s40, vcc_lo\n\t"
        "s_add_i32 s80, s38, 0\n\t"
        "s_lshl_b32 s80, s80, 15\n\t"
        "s_or_b32 s81, s38, %[snd]\n\t"
        "s_cmp_lt_i32 s81, 0\n\t"
        "s_cselect_b32 s83, s36, s80\n\t"
        "s_cbranch_scc1 .Lo19%=\n\t"
        "s_mov_b32 s97, 0\n\t"
        ".Lo17%=:\n\t"
        "v_xor_b32_e32 v186, %[tag], v161\n\t"
        "v_xor_b32_e32 v187, %[tag], v163\n\t"
        "v_xor_b32_e32 v188, %[tag], v165\n\t"
        "v_or3_b32 v186, v186, v187, v188\n\t"
        "v_xor_b32_e32 v187, %[tag], v167\n\t"
        "v_or_b32_e32 v186, v186, v187\n\t"
        "v_cmp_ne_u32_e32 vcc, 0, v186\n\t"
        "v_sub_f32_e32 v198, 1.0, v184\n\t"
        "v_add_f32_e32 v190, v144, v160\n\t"
        "v_add_f32_e32 v191, v145, v162\n\t"
        "v_add_f32_e32 v192, v146, v164\n\t"
        "v_add_f32_e32 v193, v147, v166\n\t"
        "s_cbranch_vccz .Lo18%=\n\t"
        "s_sleep %c[xslp]\n\t"
        "s_lshl_b32 s82, s83, 2\n\t"
        "buffer_load_dwordx4 v[160:163], v246, %[rd], s82 offen offset:0 sc1\n\t"
        "buffer_load_dwordx4 v[164:167], v246, %[rd], s82 offen offset:1024 sc1\n\t"
        "s_add_u32 s97, s97, 1\n\t"
        "s_cmp_lt_u32 s97, 0x200000\n\t"
        "s_cbranch_scc0 .Lgu%=\n\t"
        "s_waitcnt vmcnt(0)\n\t"
        "s_branch .Lo17%=\n\t"
        ".Lo18%=:\n\t"
        "v_cvt_pk_bf16_f32 v198, 0, v198\n\t"
        "v_cvt_pk_bf16_f32 v190, 0, v190\n\t"
        "v_cvt_pk_bf16_f32 v191, 0, v191\n\t"
        "v_cvt_pk_bf16_f32 v192, 0, v192\n\t"
        "v_cvt_pk_bf16_f32 v193, 0, v193\n\t"
        "v_lshlrev_b32_e32 v186, 16, v180\n\t"
        "v_lshlrev_b32_e32 v194, 16, v176\n\t"
        "v_and_b32_e32 v187, 0xffff0000, v180\n\t"
        "v_and_b32_e32 v195, 0xffff0000, v176\n\t"
        "v_lshlrev_b32_e32 v188, 16, v181\n\t"
        "v_lshlrev_b32_e32 v196, 16, v177\n\t"
        "v_and_b32_e32 v189, 0xffff0000, v181\n\t"
        "v_and_b32_e32 v197, 0xffff0000, v177\n\t"
        "v_mul_f32_e32 v186, v186, v194\n\t"
        "v_mul_f32_e32 v187, v187, v195\n\t"
        "v_mul_f32_e32 v188, v188, v196\n\t"
        "v_mul_f32_e32 v189, v189, v197\n\t"
        "v_mul_f32_e32 v190, v198, v190\n\t"
        "v_mul_f32_e32 v191, v198, v191\n\t"
        "v_mul_f32_e32 v192, v198, v192\n\t"
        "v_mul_f32_e32 v193, v198, v193\n\t"
        "v_cvt_pk_bf16_f32 v186, 0, v186\n\t"
        "v_cvt_pk_bf16_f32 v187, 0, v187\n\t"
        "v_cvt_pk_bf16_f32 v188, 0, v188\n\t"
        "v_cvt_pk_bf16_f32 v189, 0, v189\n\t"
        "v_cvt_pk_bf16_f32 v190, 0, v190\n\t"
        "v_cvt_pk_bf16_f32 v191, 0, v191\n\t"
        "v_cvt_pk_bf16_f32 v192, 0, v192\n\t"
        "v_cvt_pk_bf16_f32 v193, 0, v193\n\t"
        "v_mul_f32_e32 v186, v184, v186\n\t"
        "v_mul_f32_e32 v187, v184, v187\n\t"
        "v_mul_f32_e32 v188, v184, v188\n\t"
        "v_mul_f32_e32 v189, v184, v189\n\t"
        "v_cvt_pk_bf16_f32 v186, 0, v186\n\t"
        "v_cvt_pk_bf16_f32 v187, 0, v187\n\t"
        "v_cvt_pk_bf16_f32 v188, 0, v188\n\t"
        "v_cvt_pk_bf16_f32 v189, 0, v189\n\t"
        "v_add_f32_e32 v186, v186, v190\n\t"
        "v_add_f32_e32 v187, v187, v191\n\t"
        "v_add_f32_e32 v188, v188, v192\n\t"
        "v_add_f32_e32 v189, v189, v193\n\t"
        "v_cvt_pk_bf16_f32 v200, v186, v187\n\t"
        "v_cvt_pk_bf16_f32 v201, v188, v189\n\t"
        ".Lo19%=:\n\t"
        "buffer_store_dwordx2 v[200:201], v245, %[ro], s83 offen\n\t"
        "s_add_i32 s80, s38, 1\n\t"
        "s_lshl_b32 s80, s80, 15\n\t"
        "s_or_b32 s81, s38, %[snd]\n\t"
        "s_cmp_lt_i32 s81, 0\n\t"
        "s_cselect_b32 s83, s36, s80\n\t"
        "s_cbranch_scc1 .Lo22%=\n\t"
        "s_mov_b32 s97, 0\n\t"
        ".Lo20%=:\n\t"
        "v_xor_b32_e32 v186, %[tag], v169\n\t"
        "v_xor_b32_e32 v187, %[tag], v171\n\t"
        "v_xor_b32_e32 v188, %[tag], v173\n\t"
        "v_or3_b32 v186, v186, v187, v188\n\t"
        "v_xor_b32_e32 v187, %[tag], v175\n\t"
        "v_or_b32_e32 v186, v186, v187\n\t"
        "v_cmp_ne_u32_e32 vcc, 0, v186\n\t"
        "v_sub_f32_e32 v198, 1.0, v185\n\t"
        "v_add_f32_e32 v190, v148, v168\n\t"
        "v_add_f32_e32 v191, v149, v170\n\t"
        "v_add_f32_e32 v192, v150, v172\n\t"
        "v_add_f32_e32 v193, v151, v174\n\t"
        "s_cbranch_vccz .Lo21%=\n\t"
        "s_sleep %c[xslp]\n\t"
        "s_lshl_b32 s82, s83, 2\n\t"
        "buffer_load_dwordx4 v[168:171], v246, %[rd], s82 offen offset:0 sc1\n\t"
        "buffer_load_dwordx4 v[172:175], v246, %[rd], s82 offen offset:1024 sc1\n\t"
        "s_add_u32 s97, s97, 1\n\t"
        "s_cmp_lt_u32 s97, 0x200000\n\t"
        "s_cbranch_scc0 .Lgu%=\n\t"
        "s_waitcnt vmcnt(0)\n\t"
        "s_branch .Lo20%=\n\t"
        ".Lo21%=:\n\t"
        "v_cvt_pk_bf16_f32 v198, 0, v198\n\t"
        "v_cvt_pk_bf16_f32 v190, 0, v190\n\t"
        "v_cvt_pk_bf16_f32 v191, 0, v191\n\t"
        "v_cvt_pk_bf16_f32 v192, 0, v192\n\t"
        "v_cvt_pk_bf16_f32 v193, 0, v193\n\t"
        "v_lshlrev_b32_e32 v186, 16, v182\n\t"
        "v_lshlrev_b32_e32 v194, 16, v178\n\t"
        "v_and_b32_e32 v187, 0xffff0000, v182\n\t"
        "v_and_b32_e32 v195, 0xffff0000, v178\n\t"
        "v_lshlrev_b32_e32 v188, 16, v183\n\t"
        "v_lshlrev_b32_e32 v196, 16, v179\n\t"
        "v_and_b32_e32 v189, 0xffff0000, v183\n\t"
        "v_and_b32_e32 v197, 0xffff0000, v179\n\t"
        "v_mul_f32_e32 v186, v186, v194\n\t"
        "v_mul_f32_e32 v187, v187, v195\n\t"
        "v_mul_f32_e32 v188, v188, v196\n\t"
        "v_mul_f32_e32 v189, v189, v197\n\t"
        "v_mul_f32_e32 v190, v198, v190\n\t"
        "v_mul_f32_e32 v191, v198, v191\n\t"
        "v_mul_f32_e32 v192, v198, v192\n\t"
        "v_mul_f32_e32 v193, v198, v193\n\t"
        "v_cvt_pk_bf16_f32 v186, 0, v186\n\t"
        "v_cvt_pk_bf16_f32 v187, 0, v187\n\t"
        "v_cvt_pk_bf16_f32 v188, 0, v188\n\t"
        "v_cvt_pk_bf16_f32 v189, 0, v189\n\t"
        "v_cvt_pk_bf16_f32 v190, 0, v190\n\t"
        "v_cvt_pk_bf16_f32 v191, 0, v191\n\t"
        "v_cvt_pk_bf16_f32 v192, 0, v192\n\t"
        "v_cvt_pk_bf16_f32 v193, 0, v193\n\t"
        "v_mul_f32_e32 v186, v185, v186\n\t"
        "v_mul_f32_e32 v187, v185, v187\n\t"
        "v_mul_f32_e32 v188, v185, v188\n\t"
        "v_mul_f32_e32 v189, v185, v189\n\t"
        "v_cvt_pk_bf16_f32 v186, 0, v186\n\t"
        "v_cvt_pk_bf16_f32 v187, 0, v187\n\t"
        "v_cvt_pk_bf16_f32 v188, 0, v188\n\t"
        "v_cvt_pk_bf16_f32 v189, 0, v189\n\t"
        "v_add_f32_e32 v186, v186, v190\n\t"
        "v_add_f32_e32 v187, v187, v191\n\t"
        "v_add_f32_e32 v188, v188, v192\n\t"
        "v_add_f32_e32 v189, v189, v193\n\t"
        "v_cvt_pk_bf16_f32 v200, v186, v187\n\t"
        "v_cvt_pk_bf16_f32 v201, v188, v189\n\t"
        ".Lo22%=:\n\t"
        "buffer_store_dwordx2 v[200:201], v245, %[ro], s83 offen\n\t"
        "s_add_i32 s80, s44, 0\n\t"
        "s_lshl_b32 s80, s80, 6\n\t"
        "s_or_b32 s81, s44, %[snd]\n\t"
        "s_cmp_lt_i32 s81, 0\n\t"
        "s_cselect_b32 s82, s36, s80\n\t"
        "buffer_load_dword v184, v244, %[rd], s82 offen sc1\n\t"
        "s_lshl_b32 s80, s80, 1\n\t"
        "s_cmp_lt_i32 s81, 0\n\t"
        "s_cselect_b32 s82, s36, s80\n\t"
        "buffer_load_dwordx2 v[180:181], v243, %[rd], s82 offen sc1\n\t"
        "s_lshl_b32 s80, s80, 8\n\t"
        "s_cmp_lt_i32 s81, 0\n\t"
        "s_cselect_b32 s82, s36, s80\n\t"
        "buffer_load_dwordx2 v[176:177], v245, %[rx], s82 offen\n\t"
        "s_add_i32 s80, s44, 1\n\t"
        "s_lshl_b32 s80, s80, 6\n\t"
        "s_or_b32 s81, s44, %[snd]\n\t"
        "s_cmp_lt_i32 s81, 0\n\t"
        "s_cselect_b32 s82, s36, s80\n\t"
        "buffer_load_dword v185, v244, %[rd], s82 offen sc1\n\t"
        "s_lshl_b32 s80, s80, 1\n\t"
        "s_cmp_lt_i32 s81, 0\n\t"
        "s_cselect_b32 s82, s36, s80\n\t"
        "buffer_load_dwordx2 v[182:183], v243, %[rd], s82 offen sc1\n\t"
        "s_lshl_b32 s80, s80, 8\n\t"
        "s_cmp_lt_i32 s81, 0\n\t"
        "s_cselect_b32 s82, s36, s80\n\t"
        "buffer_load_dwordx2 v[178:179], v245, %[rx], s82 offen\n\t"
        "s_sleep %c[ydel]\n\t"
        "s_add_i32 s80, s44, 0\n\t"
        "s_lshl_b32 s80, s80, 17\n\t"
        "s_or_b32 s81, s44, %[snd]\n\t"
        "s_cmp_lt_i32 s81, 0\n\t"
        "s_cselect_b32 s82, s36, s80\n\t"
        "buffer_load_dwordx4 v[160:163], v246, %[rd], s82 offen offset:0 sc1\n\t"
        "buffer_load_dwordx4 v[164:167], v246, %[rd], s82 offen offset:1024 sc1\n\t"
        "s_add_i32 s80, s44, 1\n\t"
        "s_lshl_b32 s80, s80, 17\n\t"
        "s_or_b32 s81, s44, %[snd]\n\t"
        "s_cmp_lt_i32 s81, 0\n\t"
        "s_cselect_b32 s82, s36, s80\n\t"
        "buffer_load_dwordx4 v[168:171], v246, %[rd], s82 offen offset:0 sc1\n\t"
        "buffer_load_dwordx4 v[172:175], v246, %[rd], s82 offen offset:1024 sc1\n\t"
        "s_mov_b32 s38, -1\n\t"
        "v_mov_b32_e32 v152, v128\n\t"
        "v_mov_b32_e32 v153, v129\n\t"
        "v_mov_b32_e32 v154, v130\n\t"
        "v_mov_b32_e32 v155, v131\n\t"
        "v_mov_b32_e32 v156, v132\n\t"
        "v_mov_b32_e32 v157, v133\n\t"
        "v_mov_b32_e32 v158, v134\n\t"
        "v_mov_b32_e32 v159, v135\n\t"
        "s_mov_b32 s39, s44\n\t"
        "s_add_u32 s37, s37, 1\n\t"
        "s_add_u32 s44, s44, 8\n\t"
        "s_sub_u32 s43, 33280, s43\n\t"
        "s_branch .Lnew%=\n\t"
        ".Lsol%=:\n\t"
        "s_xor_b32 s44, %[rho], 122\n\t"
        "s_mov_b32 s37, 30\n\t"
        ".Lone%=:\n\t"
        ".Lhk0%=:\n\t"
        "s_cbranch_execz .Lhk1%=\n\t"
        "s_add_i32 s80, s44, 0\n\t"
        "s_lshl_b32 s80, s80, 6\n\t"
        "s_or_b32 s81, s44, %[snd]\n\t"
        "s_cmp_lt_i32 s81, 0\n\t"
        "s_cselect_b32 s82, s36, s80\n\t"
        "buffer_load_dword v184, v244, %[rd], s82 offen sc1\n\t"
        "s_lshl_b32 s80, s80, 1\n\t"
        "s_cmp_lt_i32 s81, 0\n\t"
        ".Lhk1%=:\n\t"
        "s_cbranch_execz .Lhk2%=\n\t"
        "s_cselect_b32 s82, s36, s80\n\t"
        "buffer_load_dwordx2 v[180:181], v243, %[rd], s82 offen sc1\n\t"
        "s_lshl_b32 s80, s80, 8\n\t"
        "s_cmp_lt_i32 s81, 0\n\t"
        "s_cselect_b32 s82, s36, s80\n\t"
        "buffer_load_dwordx2 v[176:177], v245, %[rx], s82 offen\n\t"
        "v_add_u32_e32 v222, s43, v247\n\t"
        "v_add_u32_e32 v223, s43, v248\n\t"
        ".Lhk2%=:\n\t"
        "s_cbranch_execz .Lhk3%=\n\t"
        "s_mov_b32 s97, 0\n\t"
        ".Lo23%=:\n\t"
        "s_lshr_b32 s80, s40, s37\n\t"
        "s_andn2_b32 s80, 1, s80\n\t"
        "s_cbranch_scc0 .Lo24%=\n\t"
        "s_cmp_eq_u32 s97, 0\n\t"
        "s_cbranch_scc1 .Lo23%=x\n\t"
        "s_sleep %c[oslp]\n\t"
        ".Lo23%=x:\n\t"
        "buffer_load_dword v220, v242, %[rd], 0 offen sc1\n\t"
        ".Lhk3%=:\n\t"
        "s_cbranch_execz .Lhk4%=\n\t"
        "s_add_u32 s97, s97, 1\n\t"
        "s_cmp_lt_u32 s97, 0x200000\n\t"
        "s_cbranch_scc0 .Lgu%=\n\t"
        "s_waitcnt vmcnt(0)\n\t"
        "v_cmp_eq_u32_e32 vcc, 32, v220\n\t"
        "s_nop 1\n\t"
        "s_or_b32 s40, s40, vcc_lo\n\t"
        "s_branch .Lo23%=\n\t"
        ".Lo24%=:\n\t"
        ".Lhk4%=:\n\t"
        "s_cbranch_execz .Lhk5%=\n\t"
        "s_lshr_b32 s84, s44, 1\n\t"
        "s_lshl_b32 s84, s84, 18\n\t"
        "s_and_b32 s80, s44, 1\n\t"
        "s_lshl_b32 s80, s80, 12\n\t"
        "s_or_b32 s84, s84, s80\n\t"
        "buffer_load_dwordx4 v[0:3], v240, %[rd], s84 offen offset:0 sc1\n\t"
        "buffer_load_dwordx4 v[4:7], v240, %[rd], s84 offen offset:1024 sc1\n\t"
        "buffer_load_dwordx4 v[8:11], v240, %[rd], s84 offen offset:2048 sc1\n\t"
        ".Lhk5%=:\n\t"
        "s_cbranch_execz .Lhk6%=\n\t"
        "buffer_load_dwordx4 v[12:15], v240, %[rd], s84 offen offset:3072 sc1\n\t"
        "s_add_u32 s84, s84, 8192\n\t"
        "buffer_load_dwordx4 v[16:19], v240, %[rd], s84 offen offset:0 sc1\n\t"
        "buffer_load_dwordx4 v[20:23], v240, %[rd], s84 offen offset:1024 sc1\n\t"
        "buffer_load_dwordx4 v[24:27], v240, %[rd], s84 offen offset:2048 sc1\n\t"
        "buffer_load_dwordx4 v[28:31], v240, %[rd], s84 offen offset:3072 sc1\n\t"
        "s_add_u32 s84, s84, 8192\n\t"
        "buffer_load_dwordx4 v[32:35], v240, %[rd], s84 offen offset:0 sc1\n\t"
        ".Lhk6%=:\n\t"
        "s_cbranch_execz .Lhk7%=\n\t"
        "buffer_load_dwordx4 v[36:39], v240, %[rd], s84 offen offset:1024 sc1\n\t"
        "buffer_load_dwordx4 v[40:43], v240, %[rd], s84 offen offset:2048 sc1\n\t"
        "buffer_load_dwordx4 v[44:47], v240, %[rd], s84 offen offset:3072 sc1\n\t"
        "s_add_u32 s84, s84, 8192\n\t"
        "buffer_load_dwordx4 v[48:51], v240, %[rd], s84 offen offset:0 sc1\n\t"
        "buffer_load_dwordx4 v[52:55], v240, %[rd], s84 offen offset:1024 sc1\n\t"
        "buffer_load_dwordx4 v[56:59], v240, %[rd], s84 offen offset:2048 sc1\n\t"
        "buffer_load_dwordx4 v[60:63], v240, %[rd], s84 offen offset:3072 sc1\n\t"
        ".Lhk7%=:\n\t"
        "s_cbranch_execz .Lhk8%=\n\t"
        "s_sub_u32 s84, s84, 20480\n\t"
        "s_waitcnt vmcnt(15)\n\t"
        "v_mfma_f32_16x16x32_bf16 v[128:131], a[128:131], v[0:3], 0\n\t"
        "v_mfma_f32_16x16x32_bf16 v[132:135], a[132:135], v[0:3], 0\n\t"
        "v_mfma_f32_16x16x32_bf16 v[136:139], a[136:139], v[0:3], 0\n\t"
        "v_mfma_f32_16x16x32_bf16 v[140:143], a[140:143], v[0:3], 0\n\t"
        "s_waitcnt vmcnt(14)\n\t"
        "v_mfma_f32_16x16x32_bf16 v[128:131], a[144:147], v[4:7], v[128:131]\n\t"
        ".Lhk8%=:\n\t"
        "s_cbranch_execz .Lhk9%=\n\t"
        "v_mfma_f32_16x16x32_bf16 v[132:135], a[148:151], v[4:7], v[132:135]\n\t"
        "v_mfma_f32_16x16x32_bf16 v[136:139], a[152:155], v[4:7], v[136:139]\n\t"
        "v_mfma_f32_16x16x32_bf16 v[140:143], a[156:159], v[4:7], v[140:143]\n\t"
        "s_waitcnt vmcnt(13)\n\t"
        "v_mfma_f32_16x16x32_bf16 v[128:131], a[160:163], v[8:11], v[128:131]\n\t"
        "v_mfma_f32_16x16x32_bf16 v[132:135], a[164:167], v[8:11], v[132:135]\n\t"
        "v_mfma_f32_16x16x32_bf16 v[136:139], a[168:171], v[8:11], v[136:139]\n\t"
        "v_mfma_f32_16x16x32_bf16 v[140:143], a[172:175], v[8:11], v[140:143]\n\t"
        ".Lhk9%=:\n\t"
        "s_cbranch_execz .Lhk10%=\n\t"
        "s_waitcnt vmcnt(12)\n\t"
        "v_mfma_f32_16x16x32_bf16 v[128:131], a[176:179], v[12:15], v[128:131]\n\t"
        "v_mfma_f32_16x16x32_bf16 v[132:135], a[180:183], v[12:15], v[132:135]\n\t"
        "v_mfma_f32_16x16x32_bf16 v[136:139], a[184:187], v[12:15], v[136:139]\n\t"
        "v_mfma_f32_16x16x32_bf16 v[140:143], a[188:191], v[12:15], v[140:143]\n\t"
        "s_waitcnt vmcnt(11)\n\t"
        "v_mfma_f32_16x16x32_bf16 v[128:131], a[192:195], v[16:19], v[128:131]\n\t"
        "v_mfma_f32_16x16x32_bf16 v[132:135], a[196:199], v[16:19], v[132:135]\n\t"
        ".Lhk10%=:\n\t"
        "s_cbranch_execz .Lhk11%=\n\t"
        "v_mfma_f32_16x16x32_bf16 v[136:139], a[200:203], v[16:19], v[136:139]\n\t"
        "v_mfma_f32_16x16x32_bf16 v[140:143], a[204:207], v[16:19], v[140:143]\n\t"
        "s_waitcnt vmcnt(10)\n\t"
        "v_mfma_f32_16x16x32_bf16 v[128:131], a[208:211], v[20:23], v[128:131]\n\t"
        "v_mfma_f32_16x16x32_bf16 v[132:135], a[212:215], v[20:23], v[132:135]\n\t"
        "v_mfma_f32_16x16x32_bf16 v[136:139], a[216:219], v[20:23], v[136:139]\n\t"
        "v_mfma_f32_16x16x32_bf16 v[140:143], a[220:223], v[20:23], v[140:143]\n\t"
        "s_waitcnt vmcnt(9)\n\t"
        ".Lhk11%=:\n\t"
        "s_cbranch_execz .Lhk12%=\n\t"
        "v_mfma_f32_16x16x32_bf16 v[128:131], a[224:227], v[24:27], v[128:131]\n\t"
        "v_mfma_f32_16x16x32_bf16 v[132:135], a[228:231], v[24:27], v[132:135]\n\t"
        "v_mfma_f32_16x16x32_bf16 v[136:139], a[232:235], v[24:27], v[136:139]\n\t"
        "v_mfma_f32_16x16x32_bf16 v[140:143], a[236:239], v[24:27], v[140:143]\n\t"
        "s_waitcnt vmcnt(8)\n\t"
        "v_mfma_f32_16x16x32_bf16 v[128:131], a[240:243], v[28:31], v[128:131]\n\t"
        "v_mfma_f32_16x16x32_bf16 v[132:135], a[244:247], v[28:31], v[132:135]\n\t"
        "v_mfma_f32_16x16x32_bf16 v[136:139], a[248:251], v[28:31], v[136:139]\n\t"
        ".Lhk12%=:\n\t"
        "s_cbranch_execz .Lhk13%=\n\t"
        "v_mfma_f32_16x16x32_bf16 v[140:143], a[252:255], v[28:31], v[140:143]\n\t"
        "s_waitcnt vmcnt(7)\n\t"
        "v_mfma_f32_16x16x32_bf16 v[128:131], a[0:3], v[32:35], v[128:131]\n\t"
        "v_mfma_f32_16x16x32_bf16 v[132:135], a[4:7], v[32:35], v[132:135]\n\t"
        "v_mfma_f32_16x16x32_bf16 v[136:139], a[8:11], v[32:35], v[136:139]\n\t"
        "v_mfma_f32_16x16x32_bf16 v[140:143], a[12:15], v[32:35], v[140:143]\n\t"
        "s_waitcnt vmcnt(6)\n\t"
        "v_mfma_f32_16x16x32_bf16 v[128:131], a[16:19], v[36:39], v[128:131]\n\t"
        ".Lhk13%=:\n\t"
        "s_cbranch_execz .Lhk14%=\n\t"
        "v_mfma_f32_16x16x32_bf16 v[132:135], a[20:23], v[36:39], v[132:135]\n\t"
        "v_mfma_f32_16x16x32_bf16 v[136:139], a[24:27], v[36:39], v[136:139]\n\t"
        "v_mfma_f32_16x16x32_bf16 v[140:143], a[28:31], v[36:39], v[140:143]\n\t"
        "s_waitcnt vmcnt(5)\n\t"
        "v_mfma_f32_16x16x32_bf16 v[128:131], a[32:35], v[40:43], v[128:131]\n\t"
        "v_mfma_f32_16x16x32_bf16 v[132:135], a[36:39], v[40:43], v[132:135]\n\t"
        "v_mfma_f32_16x16x32_bf16 v[136:139], a[40:43], v[40:43], v[136:139]\n\t"
        "v_mfma_f32_16x16x32_bf16 v[140:143], a[44:47], v[40:43], v[140:143]\n\t"
        ".Lhk14%=:\n\t"
        "s_cbranch_execz .Lhk15%=\n\t"
        "s_waitcnt vmcnt(4)\n\t"
        "v_mfma_f32_16x16x32_bf16 v[128:131], a[48:51], v[44:47], v[128:131]\n\t"
        "v_mfma_f32_16x16x32_bf16 v[132:135], a[52:55], v[44:47], v[132:135]\n\t"
        "v_mfma_f32_16x16x32_bf16 v[136:139], a[56:59], v[44:47], v[136:139]\n\t"
        "v_mfma_f32_16x16x32_bf16 v[140:143], a[60:63], v[44:47], v[140:143]\n\t"
        "s_waitcnt vmcnt(3)\n\t"
        "v_mfma_f32_16x16x32_bf16 v[128:131], a[64:67], v[48:51], v[128:131]\n\t"
        "v_mfma_f32_16x16x32_bf16 v[132:135], a[68:71], v[48:51], v[132:135]\n\t"
        ".Lhk15%=:\n\t"
        "s_cbranch_execz .Lhk16%=\n\t"
        "v_mfma_f32_16x16x32_bf16 v[136:139], a[72:75], v[48:51], v[136:139]\n\t"
        "v_mfma_f32_16x16x32_bf16 v[140:143], a[76:79], v[48:51], v[140:143]\n\t"
        "s_waitcnt vmcnt(2)\n\t"
        "v_mfma_f32_16x16x32_bf16 v[128:131], a[80:83], v[52:55], v[128:131]\n\t"
        "v_mfma_f32_16x16x32_bf16 v[132:135], a[84:87], v[52:55], v[132:135]\n\t"
        "v_mfma_f32_16x16x32_bf16 v[136:139], a[88:91], v[52:55], v[136:139]\n\t"
        "v_mfma_f32_16x16x32_bf16 v[140:143], a[92:95], v[52:55], v[140:143]\n\t"
        "s_waitcnt vmcnt(1)\n\t"
        ".Lhk16%=:\n\t"
        "s_cbranch_execz .Lhk17%=\n\t"
        "v_mfma_f32_16x16x32_bf16 v[128:131], a[96:99], v[56:59], v[128:131]\n\t"
        "v_mfma_f32_16x16x32_bf16 v[132:135], a[100:103], v[56:59], v[132:135]\n\t"
        "v_mfma_f32_16x16x32_bf16 v[136:139], a[104:107], v[56:59], v[136:139]\n\t"
        "v_mfma_f32_16x16x32_bf16 v[140:143], a[108:111], v[56:59], v[140:143]\n\t"
        "s_waitcnt vmcnt(0)\n\t"
        "v_mfma_f32_16x16x32_bf16 v[128:131], a[112:115], v[60:63], v[128:131]\n\t"
        "v_mfma_f32_16x16x32_bf16 v[132:135], a[116:119], v[60:63], v[132:135]\n\t"
        "v_mfma_f32_16x16x32_bf16 v[136:139], a[120:123], v[60:63], v[136:139]\n\t"
        ".Lhk17%=:\n\t"
        "s_cbranch_execz .Lhk18%=\n\t"
        "v_mfma_f32_16x16x32_bf16 v[140:143], a[124:127], v[60:63], v[140:143]\n\t"
        "s_nop 4\n\t"
        "ds_write_b128 v222, v[128:131] offset:0\n\t"
        "ds_write_b128 v222, v[132:135] offset:1040\n\t"
        "ds_write_b128 v222, v[136:139] offset:2080\n\t"
        "ds_write_b128 v222, v[140:143] offset:3120\n\t"
        "s_waitcnt lgkmcnt(0)\n\t"
        "s_barrier\n\t"
        ".Lhk18%=:\n\t"
        "s_cbranch_execz .Lhk19%=\n\t"
        "ds_read_b128 v[186:189], v223 offset:0\n\t"
        "ds_read_b128 v[190:193], v223 offset:8320\n\t"
        "ds_read_b128 v[194:197], v223 offset:16640\n\t"
        "ds_read_b128 v[198:201], v223 offset:24960\n\t"
        "s_waitcnt lgkmcnt(0)\n\t"
        "v_add_f32_e32 v186, v186, v190\n\t"
        "v_add_f32_e32 v187, v187, v191\n\t"
        "v_add_f32_e32 v188, v188, v192\n\t"
        ".Lhk19%=:\n\t"
        "s_cbranch_execz .Lhk20%=\n\t"
        "v_add_f32_e32 v189, v189, v193\n\t"
        "v_add_f32_e32 v194, v194, v198\n\t"
        "v_add_f32_e32 v195, v195, v199\n\t"
        "v_add_f32_e32 v196, v196, v200\n\t"
        "v_add_f32_e32 v197, v197, v201\n\t"
        "v_add_f32_e32 v204, v186, v194\n\t"
        "v_add_f32_e32 v206, v187, v195\n\t"
        "v_add_f32_e32 v208, v188, v196\n\t"
        ".Lhk20%=:\n\t"
        "s_cbranch_execz .Lhk21%=\n\t"
        "v_add_f32_e32 v210, v189, v197\n\t"
        "s_add_i32 s80, s44, 0\n\t"
        "s_lshl_b32 s80, s80, 17\n\t"
        "s_or_b32 s81, s44, %[own]\n\t"
        "s_cmp_lt_i32 s81, 0\n\t"
        "s_cselect_b32 s82, s36, s80\n\t"
        "buffer_store_dwordx4 v[204:207], v246, %[rd], s82 offen offset:0 sc1\n\t"
        "buffer_store_dwordx4 v[208:211], v246, %[rd], s82 offen offset:1024 sc1\n\t"
        ".Lhk21%=:\n\t"
        "s_cbranch_execz .Lhk22%=\n\t"
        "v_mov_b32_e32 v144, v204\n\t"
        "v_mov_b32_e32 v145, v206\n\t"
        "v_mov_b32_e32 v146, v208\n\t"
        "v_mov_b32_e32 v147, v210\n\t"
        "s_sleep %c[xdel]\n\t"
        "s_add_i32 s80, s44, 0\n\t"
        "s_lshl_b32 s80, s80, 17\n\t"
        "s_or_b32 s81, s44, %[snd]\n\t"
        ".Lhk22%=:\n\t"
        "s_cbranch_execz .Lhk23%=\n\t"
        "s_cmp_lt_i32 s81, 0\n\t"
        "s_cselect_b32 s82, s36, s80\n\t"
        "buffer_load_dwordx4 v[160:163], v246, %[rd], s82 offen offset:0 sc1\n\t"
        "buffer_load_dwordx4 v[164:167], v246, %[rd], s82 offen offset:1024 sc1\n\t"
        "s_waitcnt vmcnt(0)\n\t"
        "s_add_i32 s80, s44, 0\n\t"
        "s_lshl_b32 s80, s80, 15\n\t"
        "s_or_b32 s81, s44, %[snd]\n\t"
        ".Lhk23%=:\n\t"
        "s_cbranch_execz .Lhk24%=\n\t"
        "s_cmp_lt_i32 s81, 0\n\t"
        "s_cselect_b32 s83, s36, s80\n\t"
        "s_cbranch_scc1 .Lo27%=\n\t"
        "s_mov_b32 s97, 0\n\t"
        ".Lo25%=:\n\t"
        "v_xor_b32_e32 v186, %[tag], v161\n\t"
        "v_xor_b32_e32 v187, %[tag], v163\n\t"
        "v_xor_b32_e32 v188, %[tag], v165\n\t"
        "v_or3_b32 v186, v186, v187, v188\n\t"
        ".Lhk24%=:\n\t"
        "s_cbranch_execz .Lhk25%=\n\t"
        "v_xor_b32_e32 v187, %[tag], v167\n\t"
        "v_or_b32_e32 v186, v186, v187\n\t"
        "v_cmp_ne_u32_e32 vcc, 0, v186\n\t"
        "v_sub_f32_e32 v198, 1.0, v184\n\t"
        "v_add_f32_e32 v190, v144, v160\n\t"
        "v_add_f32_e32 v191, v145, v162\n\t"
        "v_add_f32_e32 v192, v146, v164\n\t"
        "v_add_f32_e32 v193, v147, v166\n\t"
        ".Lhk25%=:\n\t"
        "s_cbranch_execz .Lhk26%=\n\t"
        "s_cbranch_vccz .Lo26%=\n\t"
        "s_sleep %c[xslp]\n\t"
        "s_lshl_b32 s82, s83, 2\n\t"
        "buffer_load_dwordx4 v[160:163], v246, %[rd], s82 offen offset:0 sc1\n\t"
        "buffer_load_dwordx4 v[164:167], v246, %[rd], s82 offen offset:1024 sc1\n\t"
        "s_add_u32 s97, s97, 1\n\t"
        "s_cmp_lt_u32 s97, 0x200000\n\t"
        "s_cbranch_scc0 .Lgu%=\n\t"
        ".Lhk26%=:\n\t"
        "s_cbranch_execz .Lhk27%=\n\t"
        "s_waitcnt vmcnt(0)\n\t"
        "s_branch .Lo25%=\n\t"
        ".Lo26%=:\n\t"
        "v_cvt_pk_bf16_f32 v198, 0, v198\n\t"
        "v_cvt_pk_bf16_f32 v190, 0, v190\n\t"
        "v_cvt_pk_bf16_f32 v191, 0, v191\n\t"
        "v_cvt_pk_bf16_f32 v192, 0, v192\n\t"
        "v_cvt_pk_bf16_f32 v193, 0, v193\n\t"
        "v_lshlrev_b32_e32 v186, 16, v180\n\t"
        ".Lhk27%=:\n\t"
        "s_cbranch_execz .Lhk28%=\n\t"
        "v_lshlrev_b32_e32 v194, 16, v176\n\t"
        "v_and_b32_e32 v187, 0xffff0000, v180\n\t"
        "v_and_b32_e32 v195, 0xffff0000, v176\n\t"
        "v_lshlrev_b32_e32 v188, 16, v181\n\t"
        "v_lshlrev_b32_e32 v196, 16, v177\n\t"
        "v_and_b32_e32 v189, 0xffff0000, v181\n\t"
        "v_and_b32_e32 v197, 0xffff0000, v177\n\t"
        "v_mul_f32_e32 v186, v186, v194\n\t"
        ".Lhk28%=:\n\t"
        "s_cbranch_execz .Lhk29%=\n\t"
        "v_mul_f32_e32 v187, v187, v195\n\t"
        "v_mul_f32_e32 v188, v188, v196\n\t"
        "v_mul_f32_e32 v189, v189, v197\n\t"
        "v_mul_f32_e32 v190, v198, v190\n\t"
        "v_mul_f32_e32 v191, v198, v191\n\t"
        "v_mul_f32_e32 v192, v198, v192\n\t"
        "v_mul_f32_e32 v193, v198, v193\n\t"
        "v_cvt_pk_bf16_f32 v186, 0, v186\n\t"
        ".Lhk29%=:\n\t"
        "s_cbranch_execz .Lhk30%=\n\t"
        "v_cvt_pk_bf16_f32 v187, 0, v187\n\t"
        "v_cvt_pk_bf16_f32 v188, 0, v188\n\t"
        "v_cvt_pk_bf16_f32 v189, 0, v189\n\t"
        "v_cvt_pk_bf16_f32 v190, 0, v190\n\t"
        "v_cvt_pk_bf16_f32 v191, 0, v191\n\t"
        "v_cvt_pk_bf16_f32 v192, 0, v192\n\t"
        "v_cvt_pk_bf16_f32 v193, 0, v193\n\t"
        "v_mul_f32_e32 v186, v184, v186\n\t"
        ".Lhk30%=:\n\t"
        "s_cbranch_execz .Lhk31%=\n\t"
        "v_mul_f32_e32 v187, v184, v187\n\t"
        "v_mul_f32_e32 v188, v184, v188\n\t"
        "v_mul_f32_e32 v189, v184, v189\n\t"
        "v_cvt_pk_bf16_f32 v186, 0, v186\n\t"
        "v_cvt_pk_bf16_f32 v187, 0, v187\n\t"
        "v_cvt_pk_bf16_f32 v188, 0, v188\n\t"
        "v_cvt_pk_bf16_f32 v189, 0, v189\n\t"
        "v_add_f32_e32 v186, v186, v190\n\t"
        ".Lhk31%=:\n\t"
        "s_cbranch_execz .Lhk32%=\n\t"
        "v_add_f32_e32 v187, v187, v191\n\t"
        "v_add_f32_e32 v188, v188, v192\n\t"
        "v_add_f32_e32 v189, v189, v193\n\t"
        "v_cvt_pk_bf16_f32 v200, v186, v187\n\t"
        "v_cvt_pk_bf16_f32 v201, v188, v189\n\t"
        ".Lo27%=:\n\t"
        "buffer_store_dwordx2 v[200:201], v245, %[ro], s83 offen\n\t"
        "s_add_u32 s37, s37, 1\n\t"
        "s_xor_b32 s44, s44, 6\n\t"
        ".Lhk32%=:\n\t"
        "s_cbranch_execz .Lhk33%=\n\t"
        "s_sub_u32 s43, 33280, s43\n\t"
        "s_cmp_lt_u32 s37, 32\n\t"
        "s_cbranch_scc1 .Lone%=\n\t"
        "buffer_store_dword v205, off, %[rd], %[ep] sc1\n\t"
        "s_endpgm\n\t"
        ".Lo13%=:\n\t"
        "s_mov_b32 s97, 0\n\t"
        "s_lshl_b32 s82, s83, 2\n\t"
        ".Lo13%=r:\n\t"
        "s_sleep %c[xslp]\n\t"
        "buffer_load_dwordx4 v[160:163], v246, %[rd], s82 offen offset:0 sc1\n\t"
        "buffer_load_dwordx4 v[164:167], v246, %[rd], s82 offen offset:1024 sc1\n\t"
        "s_add_u32 s97, s97, 1\n\t"
        "s_cmp_lt_u32 s97, 0x200000\n\t"
        "s_cbranch_scc0 .Lgu%=\n\t"
        "s_waitcnt vmcnt(0)\n\t"
        "v_xor_b32_e32 v224, %[tag], v161\n\t"
        "v_xor_b32_e32 v225, %[tag], v163\n\t"
        "v_xor_b32_e32 v226, %[tag], v165\n\t"
        "v_or3_b32 v224, v224, v225, v226\n\t"
        "v_xor_b32_e32 v225, %[tag], v167\n\t"
        "v_or_b32_e32 v224, v224, v225\n\t"
        "v_cmp_ne_u32_e32 vcc, 0, v224\n\t"
        "s_nop 3\n\t"
        "s_cbranch_vccnz .Lo13%=r\n\t"
        "s_branch .Lo14%=\n\t"
        ".Lo15%=:\n\t"
        "s_mov_b32 s97, 0\n\t"
        "s_lshl_b32 s82, s83, 2\n\t"
        ".Lo15%=r:\n\t"
        "s_sleep %c[xslp]\n\t"
        "buffer_load_dwordx4 v[168:171], v246, %[rd], s82 offen offset:0 sc1\n\t"
        "buffer_load_dwordx4 v[172:175], v246, %[rd], s82 offen offset:1024 sc1\n\t"
        "s_add_u32 s97, s97, 1\n\t"
        "s_cmp_lt_u32 s97, 0x200000\n\t"
        "s_cbranch_scc0 .Lgu%=\n\t"
        "s_waitcnt vmcnt(0)\n\t"
        "v_xor_b32_e32 v224, %[tag], v169\n\t"
        "v_xor_b32_e32 v225, %[tag], v171\n\t"
        "v_xor_b32_e32 v226, %[tag], v173\n\t"
        "v_or3_b32 v224, v224, v225, v226\n\t"
        "v_xor_b32_e32 v225, %[tag], v175\n\t"
        "v_or_b32_e32 v224, v224, v225\n\t"
        "v_cmp_ne_u32_e32 vcc, 0, v224\n\t"
        "s_nop 3\n\t"
        "s_cbranch_vccnz .Lo15%=r\n\t"
        "s_branch .Lo16%=\n\t"
        ".Lgu%=:\n\t"
        "v_mov_b32_e32 v0, 128\n\t"
        "s_mov_b32 s80, %c[ferr]\n\t"
        "buffer_store_dword v0, off, %[rd], s80 sc1\n\t"
        "s_endpgm\n\t"
        ".Lhk33%=:\n\t"
        "s_branch .Lhr%="
        :: [rd] "s"(rd), [ro] "s"(ro), [rx] "s"(rx), [w] "s"(w), [rho] "s"(rho), [tag] "s"(tag), [own] "s"(own), [snd] "s"(snd), [ep] "s"(ep), [blk] "s"(j), [t0] "s"(g_t0),
          [ferr] "i"(F_ERR), [fprep] "i"(F_PREP), [fconv] "i"(F_CONV), [fprog] "i"(F_PROG), [dbg] "i"(W_DBG), [oslp] "i"(O_SLP), [xslp] "i"(X_SLP), [xdel] "i"(X_DEL), [ydel] "i"(Y_DEL), "{v[176:191]}"(pc)
        : "memory", "vcc", "scc", "s36", "s37", "s38", "s39", "s40", "s41", "s42", "s43", "s44", "s45", "s46", "s47", "s80", "s81", "s82", "s83", "s84", "s85", "s86", "s87", "s88", "s89", "s90", "s91", "s92", "s93", "s94", "s95", "s96", "s97", "s98", "s99", WO_REGS);
}

__device__ __forceinline__ void bulk_block(const Params& p, rsrc_t r, unsigned char* lds, const int wv, const int j, const unsigned tag, const unsigned g_t0) {
    const int x = (j & 7) >> 1, i32 = (j >> 3) * 2 + (j & 1);
    auto flags = [&](int mt, int nmt, int h0, int nh, int mat) {
        DRAIN(); __syncthreads();
        const int tid = otid(); if (tid < nmt * nh) st4(r, F_KVQ + ((mt + tid / nh) * 32 + h0 + tid % nh) * 64 + mat * 4, tag); };
#pragma unroll 1
    for (int t = 0; t < 2; ++t) {
        const int mt = 2 * t + (i32 & 1), mat = (i32 >> 1) & 1, hd = 8 * x + (i32 >> 2), rot = ((i32 >> 1) & 7) * 2;
        const bf16_t* Bm = mat ? p.Wv : p.Wk;
        if (mat) gemm<1, 4, 4, 8>(r, p.Aq, Bm, mt * 128, hd * 128, lds, W_VF, wv, rot, nullptr, t == 1, t == 0 ? Bm : nullptr, (mt + 2) * 128, hd * 128);
        else gemm<0, 4, 4, 8>(r, p.Aq, Bm, mt * 128, hd * 128, lds, W_KX, wv, rot, nullptr, t == 1, t == 0 ? Bm : nullptr, (mt + 2) * 128, hd * 128);
        flags(mt, 1, hd, 1, mat);
    }
    const int hp = 4 * x + (i32 >> 3), q8 = i32 & 7, rot = ROT2 * (i32 >> 2);
    if (SKEW) wait_long(rtnow() + (unsigned)((q8 & 3) * 4 + x) * SKEW);
    {
        const int m = q8 < 4 ? 4 + (q8 & 1) : q8 - 4, mat = q8 < 4 ? q8 >> 1 : 2;
        const bf16_t* Bn = mat == 2 ? p.Wq : mat == 1 ? p.Wv : p.Wk;
        const int mt = 4 + (q8 & 3), mv = q8 >> 2;
        const bf16_t* tk = (wv == 1 && j < 32) ? p.Aq + (64 + j) * 16384 : nullptr;
        if (mv) gemm<1, 4, 8, 4, 0, NoHook, 8>(r, p.Aq, p.Wv, mt * 128, hp * 256, lds, W_VF, wv, rot, tk, false, Bn, m * 256, hp * 256);
        else gemm<0, 4, 8, 4, 0, NoHook, 8>(r, p.Aq, p.Wk, mt * 128, hp * 256, lds, W_KX, wv, rot, tk, false, Bn, m * 256, hp * 256);
        flags(mt, 1, 2 * hp, 2, mv);
        const bf16_t* tl = (wv == 1 && j < 32) ? p.Aq + (96 + j) * 16384 : nullptr;
        if (mat == 1) gemm<1, 8, 8, 4>(r, p.Aq, Bn, m * 256, hp * 256, lds, W_VF, wv, rot, tl, true);
        else gemm<0, 8, 8, 4>(r, p.Aq, Bn, m * 256, hp * 256, lds, mat == 2 ? W_QX : W_KX, wv, rot, tl, true);
        flags(2 * m, 2, 2 * hp, 2, mat);
    }
    {
        unsigned wi = 0u;
        const int l = otid() & 63; const v4u sw = mk_srd(p.Wo, 4096 * 1024 * 2); const unsigned wa = (unsigned)(((i32 & 1) * 2048 + wv * 512 + (l >> 3)) * 2048 + (i32 >> 1) * 128 + (l & 7) * 16);
        unsigned long long tab;
        asm volatile("s_getpc_b64 %0\n\ts_branch .Lwo%=\n\t"
                     "buffer_load_dwordx4 a[128:131], v191, s[92:95], s91 offen\n\ts_setpc_b64 s[96:97]\n\t" "buffer_load_dwordx4 a[132:135], v191, s[92:95], s91 offen\n\ts_setpc_b64 s[96:97]\n\t"
                     "buffer_load_dwordx4 a[136:139], v191, s[92:95], s91 offen\n\ts_setpc_b64 s[96:97]\n\t" "buffer_load_dwordx4 a[140:143], v191, s[92:95], s91 offen\n\ts_setpc_b64 s[96:97]\n\t"
                     "buffer_load_dwordx4 a[144:147], v191, s[92:95], s91 offen\n\ts_setpc_b64 s[96:97]\n\t" "buffer_load_dwordx4 a[148:151], v191, s[92:95], s91 offen\n\ts_setpc_b64 s[96:97]\n\t"
                     "buffer_load_dwordx4 a[152:155], v191, s[92:95], s91 offen\n\ts_setpc_b64 s[96:97]\n\t" "buffer_load_dwordx4 a[156:159], v191, s[92:95], s91 offen\n\ts_setpc_b64 s[96:97]\n\t"
                     "buffer_load_dwordx4 a[160:163], v191, s[92:95], s91 offen\n\ts_setpc_b64 s[96:97]\n\t" "buffer_load_dwordx4 a[164:167], v191, s[92:95], s91 offen\n\ts_setpc_b64 s[96:97]\n\t"
                     "buffer_load_dwordx4 a[168:171], v191, s[92:95], s91 offen\n\ts_setpc_b64 s[96:97]\n\t" "buffer_load_dwordx4 a[172:175], v191, s[92:95], s91 offen\n\ts_setpc_b64 s[96:97]\n\t"
                     "buffer_load_dwordx4 a[176:179], v191, s[92:95], s91 offen\n\ts_setpc_b64 s[96:97]\n\t" "buffer_load_dwordx4 a[180:183], v191, s[92:95], s91 offen\n\ts_setpc_b64 s[96:97]\n\t"
                     "buffer_load_dwordx4 a[184:187], v191, s[92:95], s91 offen\n\ts_setpc_b64 s[96:97]\n\t" "buffer_load_dwordx4 a[188:191], v191, s[92:95], s91 offen\n\ts_setpc_b64 s[96:97]\n\t"
                     "buffer_load_dwordx4 a[192:195], v191, s[92:95], s91 offen\n\ts_setpc_b64 s[96:97]\n\t" "buffer_load_dwordx4 a[196:199], v191, s[92:95], s91 offen\n\ts_setpc_b64 s[96:97]\n\t"
                     "buffer_load_dwordx4 a[200:203], v191, s[92:95], s91 offen\n\ts_setpc_b64 s[96:97]\n\t" "buffer_load_dwordx4 a[204:207], v191, s[92:95], s91 offen\n\ts_setpc_b64 s[96:97]\n\t"
                     "buffer_load_dwordx4 a[208:211], v191, s[92:95], s91 offen\n\ts_setpc_b64 s[96:97]\n\t" "buffer_load_dwordx4 a[212:215], v191, s[92:95], s91 offen\n\ts_setpc_b64 s[96:97]\n\t"
                     "buffer_load_dwordx4 a[216:219], v191, s[92:95], s91 offen\n\ts_setpc_b64 s[96:97]\n\t" "buffer_load_dwordx4 a[220:223], v191, s[92:95], s91 offen\n\ts_setpc_b64 s[96:97]\n\t"
                     "buffer_load_dwordx4 a[224:227], v191, s[92:95], s91 offen\n\ts_setpc_b64 s[96:97]\n\t" "buffer_load_dwordx4 a[228:231], v191, s[92:95], s91 offen\n\ts_setpc_b64 s[96:97]\n\t"
                     "buffer_load_dwordx4 a[232:235], v191, s[92:95], s91 offen\n\ts_setpc_b64 s[96:97]\n\t" "buffer_load_dwordx4 a[236:239], v191, s[92:95], s91 offen\n\ts_setpc_b64 s[96:97]\n\t"
                     "buffer_load_dwordx4 a[240:243], v191, s[92:95], s91 offen\n\ts_setpc_b64 s[96:97]\n\t" "buffer_load_dwordx4 a[244:247], v191, s[92:95], s91 offen\n\ts_setpc_b64 s[96:97]\n\t"
                     "buffer_load_dwordx4 a[248:251], v191, s[92:95], s91 offen\n\ts_setpc_b64 s[96:97]\n\t" "buffer_load_dwordx4 a[252:255], v191, s[92:95], s91 offen\n\ts_setpc_b64 s[96:97]\n\t"
                     "buffer_load_dwordx4 v[192:195], v191, s[92:95], s91 offen\n\ts_setpc_b64 s[96:97]\n\t" "buffer_load_dwordx4 v[196:199], v191, s[92:95], s91 offen\n\ts_setpc_b64 s[96:97]\n\t"
                     "buffer_load_dwordx4 v[200:203], v191, s[92:95], s91 offen\n\ts_setpc_b64 s[96:97]\n\t" "buffer_load_dwordx4 v[204:207], v191, s[92:95], s91 offen\n\ts_setpc_b64 s[96:97]\n\t"
                     "buffer_load_dwordx4 v[208:211], v191, s[92:95], s91 offen\n\ts_setpc_b64 s[96:97]\n\t" "buffer_load_dwordx4 v[212:215], v191, s[92:95], s91 offen\n\ts_setpc_b64 s[96:97]\n\t"
                     "buffer_load_dwordx4 v[216:219], v191, s[92:95], s91 offen\n\ts_setpc_b64 s[96:97]\n\t" "buffer_load_dwordx4 v[220:223], v191, s[92:95], s91 offen\n\ts_setpc_b64 s[96:97]\n\t"
                     "buffer_load_dwordx4 v[224:227], v191, s[92:95], s91 offen\n\ts_setpc_b64 s[96:97]\n\t" "buffer_load_dwordx4 v[228:231], v191, s[92:95], s91 offen\n\ts_setpc_b64 s[96:97]\n\t"
                     "buffer_load_dwordx4 v[232:235], v191, s[92:95], s91 offen\n\ts_setpc_b64 s[96:97]\n\t" "buffer_load_dwordx4 v[236:239], v191, s[92:95], s91 offen\n\ts_setpc_b64 s[96:97]\n\t"
                     "buffer_load_dwordx4 v[240:243], v191, s[92:95], s91 offen\n\ts_setpc_b64 s[96:97]\n\t" "buffer_load_dwordx4 v[244:247], v191, s[92:95], s91 offen\n\ts_setpc_b64 s[96:97]\n\t"
                     "buffer_load_dwordx4 v[248:251], v191, s[92:95], s91 offen\n\ts_setpc_b64 s[96:97]\n\t" "buffer_load_dwordx4 v[252:255], v191, s[92:95], s91 offen\n\ts_setpc_b64 s[96:97]\n\t"
                     "buffer_load_dwordx4 v191, s[92:95], s91 offen lds\n\ts_setpc_b64 s[96:97]\n.Lwo%=:" : "=s"(tab));
        auto hook = [&](int) {
            const unsigned q = min(wi, 63u);
            asm volatile("s_mov_b32 m0, %1\n\ts_swappc_b64 s[96:97], %0"
                         :: "s"(tab + (min(q, 48u) * 12u + 4u)), "s"(WO_L + wv * 16384 + (q - 48u) * 1024u), "{v191}"(wa), "{s[92:95]}"(sw), "{s91}"(q * 16384u) : "memory", "m0", "s96", "s97", WO_REGS);
            ++wi; };
#pragma unroll 1
        for (int t = 0; t < 2; ++t) {
            const int mt = t ? 8 + q8 : 12 + (q8 & 3), mv = t ? 2 : q8 >> 2;
            if (mv == 1) gemm<1, 4, 8, 4, 1>(r, p.Aq, p.Wv, mt * 128, hp * 256, lds, W_VF, wv, rot, nullptr, false, p.Wq, (8 + q8) * 128, hp * 256, hook);
            else gemm<0, 4, 8, 4, 1>(r, p.Aq, t ? p.Wq : p.Wk, mt * 128, hp * 256, lds, t ? W_QX : W_KX, wv, rot, nullptr, t == 1, t ? nullptr : p.Wq, (8 + q8) * 128, hp * 256, hook);
            flags(mt, 1, 2 * hp, 2, mv);
        }
        oproj_block(p, wv, i32, x, j, tag, g_t0);
    }
}

__device__ __forceinline__ void help_block(const Params& p, rsrc_t r, unsigned char* lds, const int wv, const int k, const unsigned tag, const unsigned far) {
    const int j = -32 + k; (void)j;
    {
        const unsigned th = rtnow() + HELP_D + (unsigned)k * SKEWP, OOB = 0xF0000000u;
        const int l = otid() & 63, n16 = l & 15;
        const rsrc_t rh = __builtin_amdgcn_make_buffer_rsrc(p.ws + hx_base(p.ws), 0, U_END * 16384, 0x00020000);
        const unsigned un = (unsigned)(wv * 32 + (k & 30)) * 16384u + (k & 1) * 4096u, ca = l == 0 ? un : OOB;
        v4u z[4]; unsigned best[4] = {~0u, ~0u, ~0u, ~0u};
#pragma unroll
        for (int i = 0; i < 4; ++i) z[i] = (v4u){~0u, ~0u, ~0u, ~0u};
        for (int i = 0; i < CAL_N; ++i)
#pragma unroll
            for (int q = 0; q < 4; ++q) {
                const unsigned t0 = (unsigned)__builtin_amdgcn_s_memtime(); __builtin_amdgcn_s_waitcnt(0);
                st16(rh, ca + q * 8192, z[0]); DRAIN();
                const unsigned t1 = (unsigned)__builtin_amdgcn_s_memtime(); __builtin_amdgcn_s_waitcnt(0);
                best[q] = min(best[q], t1 - t0); }
        const unsigned up = min(best[1], best[3]) < min(best[0], best[2]) ? 1u : 0u, vote = (up ^ far) | ((up ? best[2] < best[0] : best[3] < best[1]) ? 2u : 0u);
        if (wv == 0 && k < 8) {
#pragma unroll
            for (int i = 0; i < 4; ++i) z[i] = __builtin_elementwise_min(*(const v4u*)(p.mem0 + ((n16 & 1) + 2 * (n16 >> 2)) * 1024 + ((k & 3) * 4 + i) * 64 + (l >> 4) * 16 + (n16 & 2) * 4), (v4u){~1u, ~1u, ~1u, ~1u}); }
#pragma unroll
        for (int i = 0; i < 16; ++i) st16(rh, un + (i >> 2) * 8192 + (i & 3) * 1024 + l * 16, z[i & 3]);
        st4(r, (wv == 3 && l == 0) ? F_PROG + k * 128 : OOB, 0u);
        st16(r, HO_KVM + (63 * 32 + k) * 4096 + wv * 1024 + l * 16, (v4u){~0u, ~0u, ~0u, ~0u});
        DRAIN();
        st8(r, l == 0 ? F_CAL + k * 32 + wv * 8 : OOB, (v2u){tag, vote});
        wait_long(th);
    }
    prep_block(p, r, k, wv, tag);
    if (!conv_block(p, r, k * 8, 8, lds, wv, j, tag, (unsigned)k * SKEWH)) return;
    attn_block(p, wv, k, tag);
}

__global__ void __launch_bounds__(256, 1) k_main(Params p) {
    extern __shared__ __attribute__((aligned(16))) unsigned char lds[];
    const unsigned g_t0 = DIAG ? rtnow() : 0u;
    rsrc_t r = __builtin_amdgcn_make_buffer_rsrc(p.ws, 0, W_TOTAL, 0x00020000);
    const int wv = __builtin_amdgcn_readfirstlane(threadIdx.x >> 6);
    int role = blockIdx.x;
    unsigned xcc; asm volatile("s_getreg_b32 %0, hwreg(HW_REG_XCC_ID)" : "=s"(xcc)); xcc &= 7u;
    unsigned old = 0u, xr = xcc, rk = blockIdx.x >> 3;
    if (MIXH) { if (xcc == 3u) { xr = 4u + (rk >> 3); rk = 24u + (rk & 7u); } else if (xcc > 3u && rk >= 24u) { xr = 3u; rk = (xcc - 4u) * 8u + (rk & 7u); } }
    unsigned slot = (xr >> 2) * 128u + (rk >> 1) * 8u + (xr & 3u) * 2u + (rk & 1u);
    const unsigned tag = (__builtin_amdgcn_readfirstlane(ld4(r, (threadIdx.x & 63) == 1 ? F_CLM + slot * 64 + 4 : F_EP)) * 2654435761u + 0x9E3779B9u) | 1u;
    if (threadIdx.x == 0) old = __hip_atomic_exchange((unsigned*)(p.ws + F_CLM + slot * 64), tag, __ATOMIC_RELAXED, __HIP_MEMORY_SCOPE_AGENT);
    if (threadIdx.x == 0) {
        for (int i = 0; i < 256 && old == tag; ++i) { slot = (slot + 1u) & 255u; old = __hip_atomic_exchange((unsigned*)(p.ws + F_CLM + slot * 64), tag, __ATOMIC_RELAXED, __HIP_MEMORY_SCOPE_AGENT); }
        *(volatile __attribute__((address_space(3))) int*)(size_t)(unsigned)(size_t)lds = (int)slot;
    }
    __syncthreads();
    role = __builtin_amdgcn_readfirstlane(*(volatile __attribute__((address_space(3))) int*)(size_t)(unsigned)(size_t)lds) & 255;
    __syncthreads();
    if (DIAG >= 10 && blockIdx.x < 8 && threadIdx.x == 0) st4(r, F_PROG + 72 + xcc * 128, g_t0);
    if (role >= 128) bulk_block(p, r, lds, wv, role - 128, tag, g_t0);
    else if ((role & 6) == 6) help_block(p, r, lds, wv, (role >> 3) * 2 + (role & 1), tag, xcc >> 2);
    else rec_block(p, wv, role, tag);
}

extern "C" void kernel_launch(void* const* d_in, const int* in_sizes, int n_in, void* d_out, int out_size, void* d_ws, size_t ws_size, hipStream_t stream) {
    Params p;
    p.Aq = (const bf16_t*)d_in[0]; p.Ak = (const bf16_t*)d_in[1]; p.Av = (const bf16_t*)d_in[2];
    p.shift_k = (const bf16_t*)d_in[3]; p.ssm_k = (const bf16_t*)d_in[4]; p.ssm_D = (const bf16_t*)d_in[5];
    p.mem0 = (const bf16_t*)d_in[7]; p.Wq = (const bf16_t*)d_in[8]; p.Wk = (const bf16_t*)d_in[9];
    p.Wv = (const bf16_t*)d_in[10]; p.Wo = (const bf16_t*)d_in[11]; p.gate_w = (const bf16_t*)d_in[13];
    p.out = (bf16_t*)d_out; p.ws = (unsigned char*)d_ws;
    (void)hipFuncSetAttribute((const void*)k_main, hipFuncAttributeMaxDynamicSharedMemorySize, L_TOTAL);
    k_main<<<256, 256, L_TOTAL, stream>>>(p);
}
```

```cpp
#include <hip/hip_runtime.h>
#include <stdint.h>
#include <type_traits>
typedef unsigned short bf16_t;
using bf16x8 = __attribute__((ext_vector_type(8))) short;
using f32x4 = __attribute__((ext_vector_type(4))) float;
typedef unsigned v4u __attribute__((ext_vector_type(4)));
typedef unsigned v2u __attribute__((ext_vector_type(2)));
typedef __amdgpu_buffer_rsrc_t rsrc_t;

typedef __bf16 hbf2 __attribute__((ext_vector_type(2)));
typedef float f32x2 __attribute__((ext_vector_type(2)));
__device__ __forceinline__ unsigned pack2(float a, float b) { f32x2 v = {a, b}; return __builtin_bit_cast(unsigned, __builtin_convertvector(v, hbf2)); }
__device__ __forceinline__ float rbfh(float f) { return __uint_as_float(pack2(0.f, f)); }
__device__ __forceinline__ f32x2 rbf2(f32x2 f) { return (f32x2){rbfh(f.x), rbfh(f.y)}; }

constexpr int S = 2048, D = 1024;

constexpr unsigned W_FLG = 0;
constexpr unsigned F_KVQ = W_FLG;
constexpr unsigned F_EP = W_FLG + 32768;
constexpr unsigned F_ERR = W_FLG + 32768 + 256;
constexpr unsigned F_PREP = W_FLG + 32768 + 512;
constexpr unsigned F_CAL = W_FLG + 73728;
constexpr unsigned F_CLM = W_FLG + 77824;
constexpr unsigned F_CONV = W_FLG + 33792;
constexpr unsigned F_PROG = W_FLG + 36864;
constexpr unsigned F_ATT = W_FLG + 40960;
constexpr unsigned F_RDY = W_FLG + 49152;
constexpr unsigned FLG_BYTES = 94208;
constexpr unsigned HO_KVM = W_FLG + FLG_BYTES;
constexpr unsigned W_GATE = HO_KVM + 64 * 32 * 4096;
constexpr unsigned W_QX = W_GATE + 8192;
constexpr unsigned W_KX = W_QX + S * 4096 * 2;
constexpr unsigned W_VF = W_KX + S * 4096 * 2;
constexpr unsigned DBG_BYTES = 0;
constexpr unsigned W_DBG = W_VF + 64 * 32 * 8192;
constexpr unsigned W_U = W_DBG + DBG_BYTES;
constexpr unsigned W_TOTAL = W_U + S * D * 2;

__device__ __forceinline__ v4u ld16(rsrc_t r, unsigned off) { return __builtin_amdgcn_raw_buffer_load_b128(r, off, 0, 16); }
__device__ __forceinline__ unsigned ld4(rsrc_t r, unsigned off) { return __builtin_amdgcn_raw_buffer_load_b32(r, off, 0, 16); }
__device__ __forceinline__ void st16(rsrc_t r, unsigned off, v4u v) { __builtin_amdgcn_raw_buffer_store_b128(v, r, off, 0, 16); }
__device__ __forceinline__ void st8(rsrc_t r, unsigned off, v2u v) { __builtin_amdgcn_raw_buffer_store_b64(v, r, off, 0, 16); }
__device__ __forceinline__ void st4(rsrc_t r, unsigned off, unsigned v) { __builtin_amdgcn_raw_buffer_store_b32(v, r, off, 0, 16); }
__device__ __forceinline__ bf16x8 as_frag(v4u v) { return __builtin_bit_cast(bf16x8, v); }
#define D1 128
#define D2 82
#define LC0 7
#define LA0 8
#define TR_UP 4
#define TR_DN 2
#define L_DEC 15
#define UP_SH 2
#define EARLY_FROM 54
#define P_SLP 1
#define O_SLP 0
#define TOL 2
#define A_SLP 0
#define PEN2 20
#define A_UP 6
#define CT0 2
#define CPF 1
#define PH_M 3
#define SKEW 20
#define ROT2 2
#define HX_SH 0u
#define FINE_N 0
#define PEN0 32
#define X0C 24
#define X0A 20
#define PEN 40
#define MEMBAR() asm volatile("" ::: "memory")
#define ENDPGM() asm volatile("s_endpgm" ::: "memory")
#define DRAIN() asm volatile("s_waitcnt vmcnt(0)" ::: "memory")

typedef short v4i16_t __attribute__((ext_vector_type(4)));
__device__ __forceinline__ v2u trb(unsigned lds_addr) { return __builtin_bit_cast(v2u, __builtin_amdgcn_ds_read_tr16_b64_v4i16((__attribute__((address_space(3))) v4i16_t*)(size_t)lds_addr)); }
__device__ __forceinline__ int otid_(int wv) { int t; asm volatile("v_mbcnt_lo_u32_b32 %0, -1, 0\n\tv_mbcnt_hi_u32_b32 %0, -1, %0" : "=v"(t)); return wv * 64 + t; }
#define otid() otid_(wv)
__device__ __forceinline__ bool wait_prep(rsrc_t r, int l, unsigned tag) {
    for (unsigned spin = 0;; ++spin) { MEMBAR();
        const unsigned v = ld4(r, F_PREP + (l & 31) * 4);
        if (__all(v == tag)) return true;
        if (spin > (1u << 20)) { st4(r, F_ERR, 512u); return false; }
        __builtin_amdgcn_s_sleep(8); }
}
__device__ __forceinline__ unsigned rtnow() { return (unsigned)__builtin_amdgcn_s_memrealtime(); }
__device__ __forceinline__ void wait_long(unsigned target) { for (int i = 0; i < 4096; ++i) { if ((int)(rtnow() - target) >= 0) break; __builtin_amdgcn_s_sleep(8); } }
constexpr int L_TAB = 20480;
constexpr int L_QK = 24576;
__device__ __forceinline__ unsigned img(int f) { return (unsigned)(L_QK + f * 16 + (f >> 4) * 128); }
constexpr int L_FL = 30720, L_Z = L_FL + 128, L_LOG = 31744, L_W = 32768, L_V = 131072;
constexpr int L_TOTAL = 163840;
constexpr unsigned U_END = 96;
#define PMIN 82
#define V_SLP 1
#define R_SLP 0
constexpr unsigned WO_L = 98304;
#define HELP_D 800
#define V_DEL 24
#define XR 8
#define KEYM 3
#define DIAG 0
#define NEARX 0
#define MIXH 1
#define SKEWH 10
#define SKEWP 8
#define NEARY 0
#define CS1 0x8000u
#define CS2 0x10000u
#define X_SLP 2
#define X_DEL 8
#define DN_SH 1
#define AT_SLP 8
#define VA_SLP 0
#define C_SLP 0
#define BT_SLP 0
#define L_SLP 12
#define Y_DEL 6
#define ST_M 0
#define ST_S 1
#define BM_ 10
#define BS_ 3
#define FASTP 0
#define SKEWW 40
#define H_SLP 0
#define CAL_N 3
#define STEP_MIN 180u

struct Params {
    const bf16_t *Aq, *Ak, *Av, *shift_k, *ssm_k, *ssm_D, *mem0, *Wq, *Wk, *Wv, *Wo, *gate_w;
    bf16_t* out; unsigned char* ws;
};
__host__ __device__ __forceinline__ unsigned hx_base(const void* ws) { const size_t a = (size_t)ws; return (unsigned)((((a + W_TOTAL + 0x7FFFFFu) >> 23) << 23) - a) + HX_SH; }

typedef short v4s __attribute__((ext_vector_type(4)));
typedef unsigned v32u __attribute__((ext_vector_type(32)));
typedef unsigned v16u __attribute__((ext_vector_type(16)));
typedef unsigned v8u __attribute__((ext_vector_type(8)));
typedef unsigned v12u __attribute__((ext_vector_type(12)));
__device__ __forceinline__ v4u mk_srd(const void* p, unsigned bytes) { const size_t a = (size_t)p; v4u d;
    d.x = __builtin_amdgcn_readfirstlane((unsigned)a); d.y = __builtin_amdgcn_readfirstlane((unsigned)(a >> 32) & 0xFFFFu); d.z = __builtin_amdgcn_readfirstlane(bytes); d.w = 0x00020000u; return d; }
__device__ __forceinline__ bf16x8 ldsa(unsigned addr) { return *(const __attribute__((address_space(3))) bf16x8*)(size_t)addr; }
__device__ __forceinline__ void rec_block(const Params& p, const int wv, const int b, const unsigned tag) {
    const int w = wv, tid = otid(), l = tid & 63, n16 = l & 15, g = l >> 4, c = n16 >> 2;
    const int x = (b & 7) >> 1, i32 = (b >> 3) * 2 + (b & 1);
    const unsigned OOB = 0xF0000000u;
    const v4u rhd = mk_srd(p.ws + hx_base(p.ws), U_END * 16384), wsd = mk_srd(p.ws, W_TOTAL);
    const int G = x - 1, vi = G * 32 + i32, h = x == 0 ? i32 : vi >> 1;
    const unsigned pr_a = F_PROG + h * 128, cal = F_CAL - w * 1024, kvo = HO_KVM + h * 4096;
    if (CPF) {
        unsigned lo, hi, l2; asm volatile("s_getpc_b64 s[98:99]\n.Lpc%=:\n\ts_add_u32 %2, s98, rec_v_code-.Lpc%=\n\ts_add_u32 %0, s98, rec_s_code-.Lpc%=\n\ts_addc_u32 %1, s99, 0" : "=s"(lo), "=s"(hi), "=s"(l2) :: "s98", "s99", "scc");
        const size_t a = (((size_t)hi << 32) | lo) + (size_t)(long long)((x ? (int)(l2 - lo) : 0) + ((i32 * 4 + w) % 12) * 1024 - 4096 + l * 16);
        asm volatile("s_mov_b32 m0, %1\n\ts_nop 0\n\tglobal_load_lds_dwordx4 %0, off" :: "v"(a & ~(size_t)15), "s"(x ? L_V + 16384 : 0) : "memory", "m0");
    }
    typedef __attribute__((address_space(3))) volatile unsigned lu32;
    auto cpy = [](int k) -> unsigned { return k == 1 ? 0u : k * 32768u + 16384u; };
    auto cun = [](int u) -> unsigned { return ((u & 1) ? CS1 : 0u) ^ ((u & 2) ? CS2 : 0u); };
    unsigned nA = cpy(x), nW = cpy(w), nT = 64 * 16384 + (x ? 0 : h * 8), nS = 64 * 16384 + x * 1024, nC = 32 * 16384 + (x == 2 ? 16384 : 0), b0 = 65 * 16384 + (h >> 2) * 32768 + (h & 3) * 2048;
    unsigned pob = w == 0 ? nA + (i32 >> 4) * 32768 + (i32 & 15) * 512 : (w == 1 && x) ? (nC ^ cun(i32 >> 3)) + (i32 & 7) * 1024 : (w == 2 && i32 == 0) ? nS : OOB;
    if (x == 0) {
        const v4u sq = mk_srd(p.Wq, 1024 * 4096 * 2), sk = mk_srd(p.Wv, 1024 * 4096 * 2);
        const unsigned m0b = L_W + w * 1024, f = (c & 1) | ((g & 1) << 1) | ((c >> 1) << 2);
        const unsigned t0 = L_W + (16 * (g >> 1) + 4 * (g & 1) + (c & 1) + 8 * (c >> 1)) * 256 + (((2 * w) ^ f) << 5) + (n16 & 3) * 8;
        const unsigned dv = (4 * w + g) * 8192 + h * 256 + ((n16 ^ (((g & 1) | (w << 1)) << 1)) << 4);
        v8u pa;
#pragma unroll
        for (int q = 0; q < 8; ++q) pa[q] = (t0 + ((w + (q >> 1)) & 3) * 32768) ^ ((q & 1) << 5);
        const unsigned kx = W_KX + h * 8192, vx = W_VF + h * 8192 + w * 1024, kf = F_KVQ + h * 64;
        const unsigned aq = img(8 * g + (n16 >> 2)) + (n16 & 1) * 8;
        const v8u pc = {(unsigned)tid * 16, (unsigned)(L_W + w * 32768 + l * 16), (n16 & 2) ? 0xEEEEu : 0x4444u, img(16 * (g >> 1) + 4 * (g & 1) + (c & 1) + 8 * (c >> 1)) + (n16 & 1) * 8,
                  (unsigned)(((w + 1) & 3) * 1024 + l * 16), (unsigned)(((w + 2) & 3) * 1024 + l * 16), (unsigned)(((w + 3) & 3) * 1024 + l * 16), img(32 * w + n16) + g * 4};
        const v2u pd = {(n16 & 2) ? (unsigned)L_Z : aq + w * 768, (n16 & 2) ? aq + w * 768 : (unsigned)L_Z};
        *(__attribute__((address_space(3))) v4u*)(size_t)(L_TAB + tid * 16) = (v4u){(unsigned)(L_QK + 3072 + g * 384 + n16 * 8),
                  n16 < 8 ? (unsigned)((((n16 >> 2) * 16384) ^ cun((h >> 1) & 3)) + ((h & 1) * 4 + (h >> 3)) * 1024 + w * 256 + (g >> 1) * 128 + (n16 & 2) * 32 + (g & 1) * 32 + (n16 & 1) * 16) : OOB,
                  (unsigned)((32 * w + 16 * (g & 1) + n16) * 16 + (g >> 1) * 8), (unsigned)(L_QK + 3072 + w * 768 + ((n16 & 1) + 2 * (n16 >> 2)) * 32 + g * 8 + (n16 & 2) * 2)};
        if (tid < 64) ((lu32*)(size_t)L_FL)[tid] = 0u;
    asm volatile(
        "s_add_u32 s80, %[m0b], 0\n\t"
        "s_mov_b32 s81, 0\n\t"
        "s_mov_b32 s82, 4\n\t"
        ".Ld0%=:\n\t"
        "s_mov_b32 m0, s80\n\t"
        "s_add_u32 s80, s80, 0x8000\n\t"
        "buffer_load_dwordx4 v96, %[sq], s81 offen lds\n\t"
        "s_add_u32 m0, m0, 0x1000\n\t"
        "s_add_u32 s81, s81, 0x20000\n\t"
        "buffer_load_dwordx4 v96, %[sq], s81 offen lds\n\t"
        "s_add_u32 m0, m0, 0x1000\n\t"
        "s_add_u32 s81, s81, 0x20000\n\t"
        "buffer_load_dwordx4 v96, %[sq], s81 offen lds\n\t"
        "s_add_u32 m0, m0, 0x1000\n\t"
        "s_add_u32 s81, s81, 0x20000\n\t"
        "buffer_load_dwordx4 v96, %[sq], s81 offen lds\n\t"
        "s_add_u32 m0, m0, 0x1000\n\t"
        "s_add_u32 s81, s81, 0x20000\n\t"
        "s_sub_u32 s82, s82, 1\n\t"
        "s_cmp_lg_u32 s82, 0\n\t"
        "s_cbranch_scc1 .Ld0%=\n\t"
        "s_add_u32 s80, %[m0b], 16384\n\t"
        "s_mov_b32 s81, 0\n\t"
        "s_mov_b32 s82, 4\n\t"
        ".Ld1%=:\n\t"
        "s_mov_b32 m0, s80\n\t"
        "s_add_u32 s80, s80, 0x8000\n\t"
        "buffer_load_dwordx4 v96, %[sk], s81 offen lds\n\t"
        "s_add_u32 m0, m0, 0x1000\n\t"
        "s_add_u32 s81, s81, 0x20000\n\t"
        "buffer_load_dwordx4 v96, %[sk], s81 offen lds\n\t"
        "s_add_u32 m0, m0, 0x1000\n\t"
        "s_add_u32 s81, s81, 0x20000\n\t"
        "buffer_load_dwordx4 v96, %[sk], s81 offen lds\n\t"
        "s_add_u32 m0, m0, 0x1000\n\t"
        "s_add_u32 s81, s81, 0x20000\n\t"
        "buffer_load_dwordx4 v96, %[sk], s81 offen lds\n\t"
        "s_add_u32 m0, m0, 0x1000\n\t"
        "s_add_u32 s81, s81, 0x20000\n\t"
        "s_sub_u32 s82, s82, 1\n\t"
        "s_cmp_lg_u32 s82, 0\n\t"
        "s_cbranch_scc1 .Ld1%=\n\t"
        "s_waitcnt vmcnt(16)\n\t"
        "s_barrier\n\t"
        "ds_read_b64_tr_b16 v[0:1], v98 offset:0\n\t"
        "ds_read_b64_tr_b16 v[2:3], v98 offset:512\n\t"
        "ds_read_b64_tr_b16 v[4:5], v98 offset:8192\n\t"
        "ds_read_b64_tr_b16 v[6:7], v98 offset:8704\n\t"
        "ds_read_b64_tr_b16 v[8:9], v99 offset:0\n\t"
        "ds_read_b64_tr_b16 v[10:11], v99 offset:512\n\t"
        "ds_read_b64_tr_b16 v[12:13], v99 offset:8192\n\t"
        "ds_read_b64_tr_b16 v[14:15], v99 offset:8704\n\t"
        "ds_read_b64_tr_b16 a[16:17], v100 offset:0\n\t"
        "ds_read_b64_tr_b16 a[18:19], v100 offset:512\n\t"
        "ds_read_b64_tr_b16 a[20:21], v100 offset:8192\n\t"
        "ds_read_b64_tr_b16 a[22:23], v100 offset:8704\n\t"
        "ds_read_b64_tr_b16 a[24:25], v101 offset:0\n\t"
        "ds_read_b64_tr_b16 a[26:27], v101 offset:512\n\t"
        "ds_read_b64_tr_b16 a[28:29], v101 offset:8192\n\t"
        "ds_read_b64_tr_b16 a[30:31], v101 offset:8704\n\t"
        "ds_read_b64_tr_b16 a[32:33], v102 offset:0\n\t"
        "ds_read_b64_tr_b16 a[34:35], v102 offset:512\n\t"
        "ds_read_b64_tr_b16 a[36:37], v102 offset:8192\n\t"
        "ds_read_b64_tr_b16 a[38:39], v102 offset:8704\n\t"
        "ds_read_b64_tr_b16 a[40:41], v103 offset:0\n\t"
        "ds_read_b64_tr_b16 a[42:43], v103 offset:512\n\t"
        "ds_read_b64_tr_b16 a[44:45], v103 offset:8192\n\t"
        "ds_read_b64_tr_b16 a[46:47], v103 offset:8704\n\t"
        "ds_read_b64_tr_b16 a[48:49], v104 offset:0\n\t"
        "ds_read_b64_tr_b16 a[50:51], v104 offset:512\n\t"
        "ds_read_b64_tr_b16 a[52:53], v104 offset:8192\n\t"
        "ds_read_b64_tr_b16 a[54:55], v104 offset:8704\n\t"
        "ds_read_b64_tr_b16 a[56:57], v105 offset:0\n\t"
        "ds_read_b64_tr_b16 a[58:59], v105 offset:512\n\t"
        "ds_read_b64_tr_b16 a[60:61], v105 offset:8192\n\t"
        "ds_read_b64_tr_b16 a[62:63], v105 offset:8704\n\t"
        "s_waitcnt lgkmcnt(0)\n\t"
        "s_barrier\n\t"
        "s_add_u32 s80, %[m0b], 0\n\t"
        "s_mov_b32 s81, 2097152\n\t"
        "s_mov_b32 s82, 4\n\t"
        ".Ld2%=:\n\t"
        "s_mov_b32 m0, s80\n\t"
        "s_add_u32 s80, s80, 0x8000\n\t"
        "buffer_load_dwordx4 v96, %[sq], s81 offen lds\n\t"
        "s_add_u32 m0, m0, 0x1000\n\t"
        "s_add_u32 s81, s81, 0x20000\n\t"
        "buffer_load_dwordx4 v96, %[sq], s81 offen lds\n\t"
        "s_add_u32 m0, m0, 0x1000\n\t"
        "s_add_u32 s81, s81, 0x20000\n\t"
        "buffer_load_dwordx4 v96, %[sq], s81 offen lds\n\t"
        "s_add_u32 m0, m0, 0x1000\n\t"
        "s_add_u32 s81, s81, 0x20000\n\t"
        "buffer_load_dwordx4 v96, %[sq], s81 offen lds\n\t"
        "s_add_u32 m0, m0, 0x1000\n\t"
        "s_add_u32 s81, s81, 0x20000\n\t"
        "s_sub_u32 s82, s82, 1\n\t"
        "s_cmp_lg_u32 s82, 0\n\t"
        "s_cbranch_scc1 .Ld2%=\n\t"
        "s_waitcnt vmcnt(16)\n\t"
        "s_barrier\n\t"
        "ds_read_b64_tr_b16 v[16:17], v98 offset:16384\n\t"
        "ds_read_b64_tr_b16 v[18:19], v98 offset:16896\n\t"
        "ds_read_b64_tr_b16 v[20:21], v98 offset:24576\n\t"
        "ds_read_b64_tr_b16 v[22:23], v98 offset:25088\n\t"
        "ds_read_b64_tr_b16 v[24:25], v99 offset:16384\n\t"
        "ds_read_b64_tr_b16 v[26:27], v99 offset:16896\n\t"
        "ds_read_b64_tr_b16 v[28:29], v99 offset:24576\n\t"
        "ds_read_b64_tr_b16 v[30:31], v99 offset:25088\n\t"
        "ds_read_b64_tr_b16 a[80:81], v100 offset:16384\n\t"
        "ds_read_b64_tr_b16 a[82:83], v100 offset:16896\n\t"
        "ds_read_b64_tr_b16 a[84:85], v100 offset:24576\n\t"
        "ds_read_b64_tr_b16 a[86:87], v100 offset:25088\n\t"
        "ds_read_b64_tr_b16 a[88:89], v101 offset:16384\n\t"
        "ds_read_b64_tr_b16 a[90:91], v101 offset:16896\n\t"
        "ds_read_b64_tr_b16 a[92:93], v101 offset:24576\n\t"
        "ds_read_b64_tr_b16 a[94:95], v101 offset:25088\n\t"
        "ds_read_b64_tr_b16 a[96:97], v102 offset:16384\n\t"
        "ds_read_b64_tr_b16 a[98:99], v102 offset:16896\n\t"
        "ds_read_b64_tr_b16 a[100:101], v102 offset:24576\n\t"
        "ds_read_b64_tr_b16 a[102:103], v102 offset:25088\n\t"
        "ds_read_b64_tr_b16 a[104:105], v103 offset:16384\n\t"
        "ds_read_b64_tr_b16 a[106:107], v103 offset:16896\n\t"
        "ds_read_b64_tr_b16 a[108:109], v103 offset:24576\n\t"
        "ds_read_b64_tr_b16 a[110:111], v103 offset:25088\n\t"
        "ds_read_b64_tr_b16 a[112:113], v104 offset:16384\n\t"
        "ds_read_b64_tr_b16 a[114:115], v104 offset:16896\n\t"
        "ds_read_b64_tr_b16 a[116:117], v104 offset:24576\n\t"
        "ds_read_b64_tr_b16 a[118:119], v104 offset:25088\n\t"
        "ds_read_b64_tr_b16 a[120:121], v105 offset:16384\n\t"
        "ds_read_b64_tr_b16 a[122:123], v105 offset:16896\n\t"
        "ds_read_b64_tr_b16 a[124:125], v105 offset:24576\n\t"
        "ds_read_b64_tr_b16 a[126:127], v105 offset:25088\n\t"
        "s_waitcnt lgkmcnt(0)\n\t"
        "s_barrier\n\t"
        "s_add_u32 s80, %[m0b], 16384\n\t"
        "s_mov_b32 s81, 2097152\n\t"
        "s_mov_b32 s82, 4\n\t"
        ".Ld3%=:\n\t"
        "s_mov_b32 m0, s80\n\t"
        "s_add_u32 s80, s80, 0x8000\n\t"
        "buffer_load_dwordx4 v96, %[sk], s81 offen lds\n\t"
        "s_add_u32 m0, m0, 0x1000\n\t"
        "s_add_u32 s81, s81, 0x20000\n\t"
        "buffer_load_dwordx4 v96, %[sk], s81 offen lds\n\t"
        "s_add_u32 m0, m0, 0x1000\n\t"
        "s_add_u32 s81, s81, 0x20000\n\t"
        "buffer_load_dwordx4 v96, %[sk], s81 offen lds\n\t"
        "s_add_u32 m0, m0, 0x1000\n\t"
        "s_add_u32 s81, s81, 0x20000\n\t"
        "buffer_load_dwordx4 v96, %[sk], s81 offen lds\n\t"
        "s_add_u32 m0, m0, 0x1000\n\t"
        "s_add_u32 s81, s81, 0x20000\n\t"
        "s_sub_u32 s82, s82, 1\n\t"
        "s_cmp_lg_u32 s82, 0\n\t"
        "s_cbranch_scc1 .Ld3%=\n\t"
        "s_waitcnt vmcnt(16)\n\t"
        "s_barrier\n\t"
        "ds_read_b64_tr_b16 v[32:33], v98 offset:0\n\t"
        "ds_read_b64_tr_b16 v[34:35], v98 offset:512\n\t"
        "ds_read_b64_tr_b16 v[36:37], v98 offset:8192\n\t"
        "ds_read_b64_tr_b16 v[38:39], v98 offset:8704\n\t"
        "ds_read_b64_tr_b16 v[40:41], v99 offset:0\n\t"
        "ds_read_b64_tr_b16 v[42:43], v99 offset:512\n\t"
        "ds_read_b64_tr_b16 v[44:45], v99 offset:8192\n\t"
        "ds_read_b64_tr_b16 v[46:47], v99 offset:8704\n\t"
        "ds_read_b64_tr_b16 a[144:145], v100 offset:0\n\t"
        "ds_read_b64_tr_b16 a[146:147], v100 offset:512\n\t"
        "ds_read_b64_tr_b16 a[148:149], v100 offset:8192\n\t"
        "ds_read_b64_tr_b16 a[150:151], v100 offset:8704\n\t"
        "ds_read_b64_tr_b16 a[152:153], v101 offset:0\n\t"
        "ds_read_b64_tr_b16 a[154:155], v101 offset:512\n\t"
        "ds_read_b64_tr_b16 a[156:157], v101 offset:8192\n\t"
        "ds_read_b64_tr_b16 a[158:159], v101 offset:8704\n\t"
        "ds_read_b64_tr_b16 a[160:161], v102 offset:0\n\t"
        "ds_read_b64_tr_b16 a[162:163], v102 offset:512\n\t"
        "ds_read_b64_tr_b16 a[164:165], v102 offset:8192\n\t"
        "ds_read_b64_tr_b16 a[166:167], v102 offset:8704\n\t"
        "ds_read_b64_tr_b16 a[168:169], v103 offset:0\n\t"
        "ds_read_b64_tr_b16 a[170:171], v103 offset:512\n\t"
        "ds_read_b64_tr_b16 a[172:173], v103 offset:8192\n\t"
        "ds_read_b64_tr_b16 a[174:175], v103 offset:8704\n\t"
        "ds_read_b64_tr_b16 a[176:177], v104 offset:0\n\t"
        "ds_read_b64_tr_b16 a[178:179], v104 offset:512\n\t"
        "ds_read_b64_tr_b16 a[180:181], v104 offset:8192\n\t"
        "ds_read_b64_tr_b16 a[182:183], v104 offset:8704\n\t"
        "ds_read_b64_tr_b16 a[184:185], v105 offset:0\n\t"
        "ds_read_b64_tr_b16 a[186:187], v105 offset:512\n\t"
        "ds_read_b64_tr_b16 a[188:189], v105 offset:8192\n\t"
        "ds_read_b64_tr_b16 a[190:191], v105 offset:8704\n\t"
        "s_waitcnt lgkmcnt(0)\n\t"
        "s_barrier\n\t"
        "s_add_u32 s80, %[m0b], 0\n\t"
        "s_mov_b32 s81, 4194304\n\t"
        "s_mov_b32 s82, 4\n\t"
        ".Ld4%=:\n\t"
        "s_mov_b32 m0, s80\n\t"
        "s_add_u32 s80, s80, 0x8000\n\t"
        "buffer_load_dwordx4 v96, %[sq], s81 offen lds\n\t"
        "s_add_u32 m0, m0, 0x1000\n\t"
        "s_add_u32 s81, s81, 0x20000\n\t"
        "buffer_load_dwordx4 v96, %[sq], s81 offen lds\n\t"
        "s_add_u32 m0, m0, 0x1000\n\t"
        "s_add_u32 s81, s81, 0x20000\n\t"
        "buffer_load_dwordx4 v96, %[sq], s81 offen lds\n\t"
        "s_add_u32 m0, m0, 0x1000\n\t"
        "s_add_u32 s81, s81, 0x20000\n\t"
        "buffer_load_dwordx4 v96, %[sq], s81 offen lds\n\t"
        "s_add_u32 m0, m0, 0x1000\n\t"
        "s_add_u32 s81, s81, 0x20000\n\t"
        "s_sub_u32 s82, s82, 1\n\t"
        "s_cmp_lg_u32 s82, 0\n\t"
        "s_cbranch_scc1 .Ld4%=\n\t"
        "s_mov_b64 exec, 0\n\t"
        "s_branch .Lhk0%=\n\t"
        ".Lhr%=:\n\t"
        "s_mov_b64 exec, -1\n\t"
        "s_waitcnt vmcnt(16)\n\t"
        "s_barrier\n\t"
        "ds_read_b64_tr_b16 v[48:49], v98 offset:16384\n\t"
        "ds_read_b64_tr_b16 v[50:51], v98 offset:16896\n\t"
        "ds_read_b64_tr_b16 v[52:53], v98 offset:24576\n\t"
        "ds_read_b64_tr_b16 v[54:55], v98 offset:25088\n\t"
        "ds_read_b64_tr_b16 v[56:57], v99 offset:16384\n\t"
        "ds_read_b64_tr_b16 v[58:59], v99 offset:16896\n\t"
        "ds_read_b64_tr_b16 v[60:61], v99 offset:24576\n\t"
        "ds_read_b64_tr_b16 v[62:63], v99 offset:25088\n\t"
        "ds_read_b64_tr_b16 a[208:209], v100 offset:16384\n\t"
        "ds_read_b64_tr_b16 a[210:211], v100 offset:16896\n\t"
        "ds_read_b64_tr_b16 a[212:213], v100 offset:24576\n\t"
        "ds_read_b64_tr_b16 a[214:215], v100 offset:25088\n\t"
        "ds_read_b64_tr_b16 a[216:217], v101 offset:16384\n\t"
        "ds_read_b64_tr_b16 a[218:219], v101 offset:16896\n\t"
        "ds_read_b64_tr_b16 a[220:221], v101 offset:24576\n\t"
        "ds_read_b64_tr_b16 a[222:223], v101 offset:25088\n\t"
        "ds_read_b64_tr_b16 a[224:225], v102 offset:16384\n\t"
        "ds_read_b64_tr_b16 a[226:227], v102 offset:16896\n\t"
        "ds_read_b64_tr_b16 a[228:229], v102 offset:24576\n\t"
        "ds_read_b64_tr_b16 a[230:231], v102 offset:25088\n\t"
        "ds_read_b64_tr_b16 a[232:233], v103 offset:16384\n\t"
        "ds_read_b64_tr_b16 a[234:235], v103 offset:16896\n\t"
        "ds_read_b64_tr_b16 a[236:237], v103 offset:24576\n\t"
        "ds_read_b64_tr_b16 a[238:239], v103 offset:25088\n\t"
        "ds_read_b64_tr_b16 a[240:241], v104 offset:16384\n\t"
        "ds_read_b64_tr_b16 a[242:243], v104 offset:16896\n\t"
        "ds_read_b64_tr_b16 a[244:245], v104 offset:24576\n\t"
        "ds_read_b64_tr_b16 a[246:247], v104 offset:25088\n\t"
        "ds_read_b64_tr_b16 a[248:249], v105 offset:16384\n\t"
        "ds_read_b64_tr_b16 a[250:251], v105 offset:16896\n\t"
        "ds_read_b64_tr_b16 a[252:253], v105 offset:24576\n\t"
        "ds_read_b64_tr_b16 a[254:255], v105 offset:25088\n\t"
        "s_waitcnt lgkmcnt(0)\n\t"
        "s_barrier\n\t"
        "s_add_u32 s80, %[m0b], 16384\n\t"
        "s_mov_b32 s81, 4194304\n\t"
        "s_mov_b32 s82, 4\n\t"
        ".Ld5%=:\n\t"
        "s_mov_b32 m0, s80\n\t"
        "s_add_u32 s80, s80, 0x8000\n\t"
        "buffer_load_dwordx4 v96, %[sk], s81 offen lds\n\t"
        "s_add_u32 m0, m0, 0x1000\n\t"
        "s_add_u32 s81, s81, 0x20000\n\t"
        "buffer_load_dwordx4 v96, %[sk], s81 offen lds\n\t"
        "s_add_u32 m0, m0, 0x1000\n\t"
        "s_add_u32 s81, s81, 0x20000\n\t"
        "buffer_load_dwordx4 v96, %[sk], s81 offen lds\n\t"
        "s_add_u32 m0, m0, 0x1000\n\t"
        "s_add_u32 s81, s81, 0x20000\n\t"
        "buffer_load_dwordx4 v96, %[sk], s81 offen lds\n\t"
        "s_add_u32 m0, m0, 0x1000\n\t"
        "s_add_u32 s81, s81, 0x20000\n\t"
        "s_sub_u32 s82, s82, 1\n\t"
        "s_cmp_lg_u32 s82, 0\n\t"
        "s_cbranch_scc1 .Ld5%=\n\t"
        "s_waitcnt vmcnt(16)\n\t"
        "s_barrier\n\t"
        "ds_read_b64_tr_b16 v[128:129], v98 offset:0\n\t"
        "ds_read_b64_tr_b16 v[130:131], v98 offset:512\n\t"
        "ds_read_b64_tr_b16 v[132:133], v98 offset:8192\n\t"
        "ds_read_b64_tr_b16 v[134:135], v98 offset:8704\n\t"
        "ds_read_b64_tr_b16 v[136:137], v99 offset:0\n\t"
        "ds_read_b64_tr_b16 v[138:139], v99 offset:512\n\t"
        "ds_read_b64_tr_b16 v[140:141], v99 offset:8192\n\t"
        "ds_read_b64_tr_b16 v[142:143], v99 offset:8704\n\t"
        "ds_read_b64_tr_b16 v[144:145], v100 offset:0\n\t"
        "ds_read_b64_tr_b16 v[146:147], v100 offset:512\n\t"
        "ds_read_b64_tr_b16 v[148:149], v100 offset:8192\n\t"
        "ds_read_b64_tr_b16 v[150:151], v100 offset:8704\n\t"
        "ds_read_b64_tr_b16 v[152:153], v101 offset:0\n\t"
        "ds_read_b64_tr_b16 v[154:155], v101 offset:512\n\t"
        "ds_read_b64_tr_b16 v[156:157], v101 offset:8192\n\t"
        "ds_read_b64_tr_b16 v[158:159], v101 offset:8704\n\t"
        "ds_read_b64_tr_b16 v[160:161], v102 offset:0\n\t"
        "ds_read_b64_tr_b16 v[162:163], v102 offset:512\n\t"
        "ds_read_b64_tr_b16 v[164:165], v102 offset:8192\n\t"
        "ds_read_b64_tr_b16 v[166:167], v102 offset:8704\n\t"
        "ds_read_b64_tr_b16 v[168:169], v103 offset:0\n\t"
        "ds_read_b64_tr_b16 v[170:171], v103 offset:512\n\t"
        "ds_read_b64_tr_b16 v[172:173], v103 offset:8192\n\t"
        "ds_read_b64_tr_b16 v[174:175], v103 offset:8704\n\t"
        "ds_read_b64_tr_b16 v[176:177], v104 offset:0\n\t"
        "ds_read_b64_tr_b16 v[178:179], v104 offset:512\n\t"
        "ds_read_b64_tr_b16 v[180:181], v104 offset:8192\n\t"
        "ds_read_b64_tr_b16 v[182:183], v104 offset:8704\n\t"
        "ds_read_b64_tr_b16 v[184:185], v105 offset:0\n\t"
        "ds_read_b64_tr_b16 v[186:187], v105 offset:512\n\t"
        "ds_read_b64_tr_b16 v[188:189], v105 offset:8192\n\t"
        "ds_read_b64_tr_b16 v[190:191], v105 offset:8704\n\t"
        "s_waitcnt lgkmcnt(0)\n\t"
        "s_barrier\n\t"
        "s_add_u32 s80, %[m0b], 0\n\t"
        "s_mov_b32 s81, 6291456\n\t"
        "s_mov_b32 s82, 4\n\t"
        ".Ld6%=:\n\t"
        "s_mov_b32 m0, s80\n\t"
        "s_add_u32 s80, s80, 0x8000\n\t"
        "buffer_load_dwordx4 v96, %[sq], s81 offen lds\n\t"
        "s_add_u32 m0, m0, 0x1000\n\t"
        "s_add_u32 s81, s81, 0x20000\n\t"
        "buffer_load_dwordx4 v96, %[sq], s81 offen lds\n\t"
        "s_add_u32 m0, m0, 0x1000\n\t"
        "s_add_u32 s81, s81, 0x20000\n\t"
        "buffer_load_dwordx4 v96, %[sq], s81 offen lds\n\t"
        "s_add_u32 m0, m0, 0x1000\n\t"
        "s_add_u32 s81, s81, 0x20000\n\t"
        "buffer_load_dwordx4 v96, %[sq], s81 offen lds\n\t"
        "s_add_u32 m0, m0, 0x1000\n\t"
        "s_add_u32 s81, s81, 0x20000\n\t"
        "s_sub_u32 s82, s82, 1\n\t"
        "s_cmp_lg_u32 s82, 0\n\t"
        "s_cbranch_scc1 .Ld6%=\n\t"
        "s_waitcnt vmcnt(16)\n\t"
        "s_barrier\n\t"
        "ds_read_b64_tr_b16 v[192:193], v98 offset:16384\n\t"
        "ds_read_b64_tr_b16 v[194:195], v98 offset:16896\n\t"
        "ds_read_b64_tr_b16 v[196:197], v98 offset:24576\n\t"
        "ds_read_b64_tr_b16 v[198:199], v98 offset:25088\n\t"
        "ds_read_b64_tr_b16 v[200:201], v99 offset:16384\n\t"
        "ds_read_b64_tr_b16 v[202:203], v99 offset:16896\n\t"
        "ds_read_b64_tr_b16 v[204:205], v99 offset:24576\n\t"
        "ds_read_b64_tr_b16 v[206:207], v99 offset:25088\n\t"
        "ds_read_b64_tr_b16 v[208:209], v100 offset:16384\n\t"
        "ds_read_b64_tr_b16 v[210:211], v100 offset:16896\n\t"
        "ds_read_b64_tr_b16 v[212:213], v100 offset:24576\n\t"
        "ds_read_b64_tr_b16 v[214:215], v100 offset:25088\n\t"
        "ds_read_b64_tr_b16 v[216:217], v101 offset:16384\n\t"
        "ds_read_b64_tr_b16 v[218:219], v101 offset:16896\n\t"
        "ds_read_b64_tr_b16 v[220:221], v101 offset:24576\n\t"
        "ds_read_b64_tr_b16 v[222:223], v101 offset:25088\n\t"
        "ds_read_b64_tr_b16 v[224:225], v102 offset:16384\n\t"
        "ds_read_b64_tr_b16 v[226:227], v102 offset:16896\n\t"
        "ds_read_b64_tr_b16 v[228:229], v102 offset:24576\n\t"
        "ds_read_b64_tr_b16 v[230:231], v102 offset:25088\n\t"
        "ds_read_b64_tr_b16 v[232:233], v103 offset:16384\n\t"
        "ds_read_b64_tr_b16 v[234:235], v103 offset:16896\n\t"
        "ds_read_b64_tr_b16 v[236:237], v103 offset:24576\n\t"
        "ds_read_b64_tr_b16 v[238:239], v103 offset:25088\n\t"
        "ds_read_b64_tr_b16 v[240:241], v104 offset:16384\n\t"
        "ds_read_b64_tr_b16 v[242:243], v104 offset:16896\n\t"
        "ds_read_b64_tr_b16 v[244:245], v104 offset:24576\n\t"
        "ds_read_b64_tr_b16 v[246:247], v104 offset:25088\n\t"
        "ds_read_b64_tr_b16 v[248:249], v105 offset:16384\n\t"
        "ds_read_b64_tr_b16 v[250:251], v105 offset:16896\n\t"
        "ds_read_b64_tr_b16 v[252:253], v105 offset:24576\n\t"
        "ds_read_b64_tr_b16 v[254:255], v105 offset:25088\n\t"
        "s_waitcnt lgkmcnt(0)\n\t"
        "s_barrier\n\t"
        "s_add_u32 s80, %[m0b], 16384\n\t"
        "s_mov_b32 s81, 6291456\n\t"
        "s_mov_b32 s82, 4\n\t"
        ".Ld7%=:\n\t"
        "s_mov_b32 m0, s80\n\t"
        "s_add_u32 s80, s80, 0x8000\n\t"
        "buffer_load_dwordx4 v96, %[sk], s81 offen lds\n\t"
        "s_add_u32 m0, m0, 0x1000\n\t"
        "s_add_u32 s81, s81, 0x20000\n\t"
        "buffer_load_dwordx4 v96, %[sk], s81 offen lds\n\t"
        "s_add_u32 m0, m0, 0x1000\n\t"
        "s_add_u32 s81, s81, 0x20000\n\t"
        "buffer_load_dwordx4 v96, %[sk], s81 offen lds\n\t"
        "s_add_u32 m0, m0, 0x1000\n\t"
        "s_add_u32 s81, s81, 0x20000\n\t"
        "buffer_load_dwordx4 v96, %[sk], s81 offen lds\n\t"
        "s_add_u32 m0, m0, 0x1000\n\t"
        "s_add_u32 s81, s81, 0x20000\n\t"
        "s_sub_u32 s82, s82, 1\n\t"
        "s_cmp_lg_u32 s82, 0\n\t"
        "s_cbranch_scc1 .Ld7%=\n\t"
        "s_waitcnt vmcnt(16)\n\t"
        "s_barrier\n\t"
        "ds_read_b64_tr_b16 a[0:1], v98 offset:0\n\t"
        "ds_read_b64_tr_b16 a[2:3], v98 offset:512\n\t"
        "ds_read_b64_tr_b16 a[4:5], v98 offset:8192\n\t"
        "ds_read_b64_tr_b16 a[6:7], v98 offset:8704\n\t"
        "ds_read_b64_tr_b16 a[8:9], v99 offset:0\n\t"
        "ds_read_b64_tr_b16 a[10:11], v99 offset:512\n\t"
        "ds_read_b64_tr_b16 a[12:13], v99 offset:8192\n\t"
        "ds_read_b64_tr_b16 a[14:15], v99 offset:8704\n\t"
        "ds_read_b64_tr_b16 v[64:65], v100 offset:0\n\t"
        "ds_read_b64_tr_b16 v[66:67], v100 offset:512\n\t"
        "ds_read_b64_tr_b16 v[68:69], v100 offset:8192\n\t"
        "ds_read_b64_tr_b16 v[70:71], v100 offset:8704\n\t"
        "ds_read_b64_tr_b16 v[72:73], v101 offset:0\n\t"
        "ds_read_b64_tr_b16 v[74:75], v101 offset:512\n\t"
        "ds_read_b64_tr_b16 v[76:77], v101 offset:8192\n\t"
        "ds_read_b64_tr_b16 v[78:79], v101 offset:8704\n\t"
        "ds_read_b64_tr_b16 v[80:81], v102 offset:0\n\t"
        "ds_read_b64_tr_b16 v[82:83], v102 offset:512\n\t"
        "ds_read_b64_tr_b16 v[84:85], v102 offset:8192\n\t"
        "ds_read_b64_tr_b16 v[86:87], v102 offset:8704\n\t"
        "ds_read_b64_tr_b16 v[88:89], v103 offset:0\n\t"
        "ds_read_b64_tr_b16 v[90:91], v103 offset:512\n\t"
        "ds_read_b64_tr_b16 v[92:93], v103 offset:8192\n\t"
        "ds_read_b64_tr_b16 v[94:95], v103 offset:8704\n\t"
        "ds_read_b64_tr_b16 a[128:129], v104 offset:0\n\t"
        "ds_read_b64_tr_b16 a[130:131], v104 offset:512\n\t"
        "ds_read_b64_tr_b16 a[132:133], v104 offset:8192\n\t"
        "ds_read_b64_tr_b16 a[134:135], v104 offset:8704\n\t"
        "ds_read_b64_tr_b16 a[136:137], v105 offset:0\n\t"
        "ds_read_b64_tr_b16 a[138:139], v105 offset:512\n\t"
        "ds_read_b64_tr_b16 a[140:141], v105 offset:8192\n\t"
        "ds_read_b64_tr_b16 a[142:143], v105 offset:8704\n\t"
        "s_waitcnt lgkmcnt(0)\n\t"
        "s_barrier\n\t"
        "ds_write_b128 v121, v[0:3] offset:0\n\t"
        "ds_write_b128 v121, v[4:7] offset:1024\n\t"
        "ds_write_b128 v121, v[8:11] offset:2048\n\t"
        "ds_write_b128 v121, v[12:15] offset:3072\n\t"
        "ds_write_b128 v121, v[16:19] offset:4096\n\t"
        "ds_write_b128 v121, v[20:23] offset:5120\n\t"
        "ds_write_b128 v121, v[24:27] offset:6144\n\t"
        "ds_write_b128 v121, v[28:31] offset:7168\n\t"
        "ds_write_b128 v121, v[32:35] offset:8192\n\t"
        "ds_write_b128 v121, v[36:39] offset:9216\n\t"
        "ds_write_b128 v121, v[40:43] offset:10240\n\t"
        "ds_write_b128 v121, v[44:47] offset:11264\n\t"
        "ds_write_b128 v121, v[48:51] offset:12288\n\t"
        "ds_write_b128 v121, v[52:55] offset:13312\n\t"
        "ds_write_b128 v121, v[56:59] offset:14336\n\t"
        "ds_write_b128 v121, v[60:63] offset:15360\n\t"
        "s_waitcnt lgkmcnt(0)\n\t"
        "s_mov_b32 s64, %[kf]\n\t"
        "s_mov_b32 s36, 0xf0000000\n\t"
        "buffer_load_dwordx4 v[116:119], v120, %[wsd], %[cal] offen sc1\n\t"
        "buffer_load_dwordx2 v[114:115], off, %[wsd], s64 sc1\n\t"
        "s_waitcnt vmcnt(2)\n\t"
        "s_barrier\n\t"
        "ds_read_b64_tr_b16 a[64:65], v98 offset:16384\n\t"
        "ds_read_b64_tr_b16 a[66:67], v98 offset:16896\n\t"
        "ds_read_b64_tr_b16 a[68:69], v98 offset:24576\n\t"
        "ds_read_b64_tr_b16 a[70:71], v98 offset:25088\n\t"
        "ds_read_b64_tr_b16 a[72:73], v99 offset:16384\n\t"
        "ds_read_b64_tr_b16 a[74:75], v99 offset:16896\n\t"
        "ds_read_b64_tr_b16 a[76:77], v99 offset:24576\n\t"
        "ds_read_b64_tr_b16 a[78:79], v99 offset:25088\n\t"
        "ds_read_b64_tr_b16 v[0:1], v100 offset:16384\n\t"
        "ds_read_b64_tr_b16 v[2:3], v100 offset:16896\n\t"
        "ds_read_b64_tr_b16 v[4:5], v100 offset:24576\n\t"
        "ds_read_b64_tr_b16 v[6:7], v100 offset:25088\n\t"
        "ds_read_b64_tr_b16 v[8:9], v101 offset:16384\n\t"
        "ds_read_b64_tr_b16 v[10:11], v101 offset:16896\n\t"
        "ds_read_b64_tr_b16 v[12:13], v101 offset:24576\n\t"
        "ds_read_b64_tr_b16 v[14:15], v101 offset:25088\n\t"
        "ds_read_b64_tr_b16 v[16:17], v102 offset:16384\n\t"
        "ds_read_b64_tr_b16 v[18:19], v102 offset:16896\n\t"
        "ds_read_b64_tr_b16 v[20:21], v102 offset:24576\n\t"
        "ds_read_b64_tr_b16 v[22:23], v102 offset:25088\n\t"
        "ds_read_b64_tr_b16 v[24:25], v103 offset:16384\n\t"
        "ds_read_b64_tr_b16 v[26:27], v103 offset:16896\n\t"
        "ds_read_b64_tr_b16 v[28:29], v103 offset:24576\n\t"
        "ds_read_b64_tr_b16 v[30:31], v103 offset:25088\n\t"
        "ds_read_b64_tr_b16 a[192:193], v104 offset:16384\n\t"
        "ds_read_b64_tr_b16 a[194:195], v104 offset:16896\n\t"
        "ds_read_b64_tr_b16 a[196:197], v104 offset:24576\n\t"
        "ds_read_b64_tr_b16 a[198:199], v104 offset:25088\n\t"
        "ds_read_b64_tr_b16 a[200:201], v105 offset:16384\n\t"
        "ds_read_b64_tr_b16 a[202:203], v105 offset:16896\n\t"
        "ds_read_b64_tr_b16 a[204:205], v105 offset:24576\n\t"
        "ds_read_b64_tr_b16 a[206:207], v105 offset:25088\n\t"
        "s_waitcnt vmcnt(0)\n\t"
        "s_mov_b32 s97, 0\n\t"
        ".Lg0%=:\n\t"
        "v_cmp_ne_u32_e32 vcc, %[tag], v116\n\t"
        "v_cmp_ne_u32_e64 s[82:83], %[tag], v118\n\t"
        "s_nop 1\n\t"
        "s_or_b64 vcc, vcc, s[82:83]\n\t"
        "s_cbranch_scc0 .Lg1%=\n\t"
        "s_sleep 4\n\t"
        "buffer_load_dwordx4 v[116:119], v120, %[wsd], %[cal] offen sc1\n\t"
        "s_add_u32 s97, s97, 1\n\t"
        "s_cmp_lt_u32 s97, 0x100000\n\t"
        "s_cbranch_scc0 .Lgu%=\n\t"
        "s_waitcnt vmcnt(0)\n\t"
        "s_branch .Lg0%=\n\t"
        ".Lg1%=:\n\t"
        "s_mov_b32 s84, 0x55555555\n\t"
        "s_mov_b32 s85, s84\n\t"
        "v_and_b32_e32 v116, 1, v117\n\t"
        "v_and_b32_e32 v118, 1, v119\n\t"
        "v_cmp_ne_u32_e32 vcc, 0, v116\n\t"
        "v_cmp_ne_u32_e64 s[82:83], 0, v118\n\t"
        "s_nop 0\n\t"
        "s_and_b64 s[82:83], s[82:83], s[84:85]\n\t"
        "s_bcnt1_i32_b64 s87, s[82:83]\n\t"
        "s_and_b64 s[82:83], vcc, s[84:85]\n\t"
        "s_bcnt1_i32_b64 s86, s[82:83]\n\t"
        "s_andn2_b64 s[82:83], vcc, s[84:85]\n\t"
        "s_bcnt1_i32_b64 s88, s[82:83]\n\t"
        "v_and_b32_e32 v116, 2, v117\n\t"
        "v_and_b32_e32 v118, 2, v119\n\t"
        "v_cmp_ne_u32_e32 vcc, 0, v116\n\t"
        "v_cmp_ne_u32_e64 s[82:83], 0, v118\n\t"
        "s_nop 0\n\t"
        "s_and_b64 s[82:83], s[82:83], s[84:85]\n\t"
        "s_bcnt1_i32_b64 s90, s[82:83]\n\t"
        "s_and_b64 s[82:83], vcc, s[84:85]\n\t"
        "s_bcnt1_i32_b64 s89, s[82:83]\n\t"
        "s_andn2_b64 s[82:83], vcc, s[84:85]\n\t"
        "s_bcnt1_i32_b64 s91, s[82:83]\n\t"
        "s_add_u32 s80, s86, s87\n\t"
        "s_add_u32 s80, s80, s88\n\t"
        "s_cmp_ge_u32 s80, 48\n\t"
        "s_cselect_b32 s80, 8, 24\n\t"
        "s_add_u32 s82, s89, s91\n\t"
        "s_sub_u32 s82, s82, s90\n\t"
        "s_cmp_gt_i32 s82, 16\n\t"
        "s_cselect_b32 s82, 6, 25\n\t"
        "s_cselect_b32 s83, 25, 6\n\t"
        "s_cmp_gt_u32 s86, s80\n\t"
        "s_cselect_b32 s86, %c[NX1], %c[NX0]\n\t"
        "s_cmp_gt_u32 s89, s82\n\t"
        "s_cselect_b32 s81, %c[NY1], %c[NY0]\n\t"
        "s_or_b32 s86, s86, s81\n\t"
        "s_cmp_gt_u32 s87, s80\n\t"
        "s_cselect_b32 s87, %c[NX1], %c[NX0]\n\t"
        "s_cmp_gt_u32 s90, s83\n\t"
        "s_cselect_b32 s81, %c[NY1], %c[NY0]\n\t"
        "s_or_b32 s87, s87, s81\n\t"
        "s_cmp_gt_u32 s88, s80\n\t"
        "s_cselect_b32 s88, %c[NX1], %c[NX0]\n\t"
        "s_cmp_gt_u32 s91, s82\n\t"
        "s_cselect_b32 s81, %c[NY1], %c[NY0]\n\t"
        "s_or_b32 s88, s88, s81\n\t"
        "s_xor_b32 %[nA], %[nA], s86\n\t"
        "s_xor_b32 %[nW], %[nW], s86\n\t"
        "s_xor_b32 %[nC], %[nC], s87\n\t"
        "s_xor_b32 %[nS], %[nS], s88\n\t"
        "s_xor_b32 %[nT], %[nT], s88\n\t"
        "s_xor_b32 %[b0], %[b0], s88\n\t"
        "s_cmp_eq_u32 %[w], 1\n\t"
        "s_cselect_b32 s80, s87, s86\n\t"
        "s_cmp_eq_u32 %[w], 2\n\t"
        "s_cselect_b32 s80, s88, s80\n\t"
        "s_xor_b32 %[pob], %[pob], s80\n\t"
        "s_lshl_b32 s80, %[w], 10\n\t"
        "s_sub_u32 %[nS], %[nS], s80\n\t"
        "s_sub_u32 %[pob], %[pob], s80\n\t"
        "ds_read_b32 v116, v120 offset:20484\n\t"
        "s_and_b32 s80, s87, 0x4000\n\t"
        "s_xor_b32 %[nC], %[nC], s80\n\t"
        "s_lshl_b32 s81, %[w], 10\n\t"
        "s_sub_u32 %[b0], %[b0], s81\n\t"
        "s_waitcnt lgkmcnt(0)\n\t"
        "v_xor_b32_e32 v116, s80, v116\n\t"
        "ds_write_b32 v120, v116 offset:20484\n\t"
        "s_mov_b32 s36, 0xf0000000\n\t"
        "s_mov_b32 s38, 0x41a00000\n\t"
        "s_mov_b32 s40, 0\n\t"
        "s_mov_b32 s37, 0\n\t"
        "s_brev_b32 s54, 1\n\t"
        "s_mov_b32 s46, %c[iD1]\n\t"
        "s_mov_b32 s45, %c[iD1]\n\t"
        "s_mov_b32 s47, %c[iD2]\n\t"
        "s_mov_b32 s48, 1\n\t"
        "s_mov_b32 s50, %c[LC]\n\t"
        "s_mov_b32 s51, %c[LA]\n\t"
        "s_mov_b32 s52, %c[E1]\n\t"
        "s_mov_b32 s53, %c[E2]\n\t"
        "s_mov_b32 s43, 0\n\t"
        "s_mov_b32 s61, %[b0]\n\t"
        "s_add_u32 s62, %[b0], 0x40000\n\t"
        "s_mov_b32 s75, 31744\n\t"
        "s_cmp_eq_u32 s64, s36\n\t"
        "s_cbranch_scc1 .Lk10%=\n\t"
        "s_mov_b32 s97, 0\n\t"
        ".Lk00%=:\n\t"
        "v_readfirstlane_b32 s82, v114\n\t"
        "v_readfirstlane_b32 s83, v115\n\t"
        "s_xor_b32 s82, s82, %[tag]\n\t"
        "s_xor_b32 s83, s83, %[tag]\n\t"
        "s_or_b32 s82, s82, s83\n\t"
        "s_cbranch_scc0 .Lk10%=\n\t"
        "s_sleep 3\n\t"
        "buffer_load_dwordx2 v[114:115], off, %[wsd], s64 sc1\n\t"
        "s_add_u32 s97, s97, 1\n\t"
        "s_cmp_lt_u32 s97, 0x200000\n\t"
        "s_cbranch_scc0 .Lgu%=\n\t"
        "s_waitcnt vmcnt(0)\n\t"
        "s_branch .Lk00%=\n\t"
        ".Lk10%=:\n\t"
        "s_add_u32 s80, s40, 0\n\t"
        "s_min_u32 s80, s80, 63\n\t"
        "s_lshl_b32 s80, s80, 18\n\t"
        "s_add_u32 s69, s80, %[kx]\n\t"
        "s_add_u32 s70, s69, 0x1000\n\t"
        "s_lshl_b32 s80, s40, 18\n\t"
        "s_add_u32 s71, s80, %[vx]\n\t"
        "s_add_u32 s80, s40, 1\n\t"
        "s_lshl_b32 s81, s80, 9\n\t"
        "s_add_u32 s81, s81, %[kf]\n\t"
        "s_and_b32 s80, s80, 0x43\n\t"
        "s_cselect_b32 s64, s36, s81\n\t"
        "buffer_load_dwordx4 v[104:107], v120, %[wsd], s69 offen sc1\n\t"
        "buffer_load_dwordx4 v[108:111], v120, %[wsd], s70 offen sc1\n\t"
        "s_waitcnt lgkmcnt(0)\n\t"
        "s_barrier\n\t"
        "ds_write_b128 v121, v[64:67] offset:16384\n\t"
        "ds_write_b128 v121, v[68:71] offset:17408\n\t"
        "ds_write_b128 v121, v[72:75] offset:18432\n\t"
        "ds_write_b128 v121, v[76:79] offset:19456\n\t"
        "ds_write_b128 v121, v[80:83] offset:20480\n\t"
        "ds_write_b128 v121, v[84:87] offset:21504\n\t"
        "ds_write_b128 v121, v[88:91] offset:22528\n\t"
        "ds_write_b128 v121, v[92:95] offset:23552\n\t"
        "ds_write_b128 v121, v[0:3] offset:24576\n\t"
        "ds_write_b128 v121, v[4:7] offset:25600\n\t"
        "ds_write_b128 v121, v[8:11] offset:26624\n\t"
        "ds_write_b128 v121, v[12:15] offset:27648\n\t"
        "ds_write_b128 v121, v[16:19] offset:28672\n\t"
        "ds_write_b128 v121, v[20:23] offset:29696\n\t"
        "ds_write_b128 v121, v[24:27] offset:30720\n\t"
        "ds_write_b128 v121, v[28:31] offset:31744\n\t"
        "s_waitcnt lgkmcnt(0)\n\t"
        "rec_s_code:\n\t"
        "ds_read_b128 v[16:19], v121 offset:0\n\t"
        "ds_read_b128 v[20:23], v121 offset:1024\n\t"
        "ds_read_b128 v[24:27], v121 offset:2048\n\t"
        "ds_read_b128 v[28:31], v121 offset:3072\n\t"
        "ds_read_b128 v[32:35], v121 offset:4096\n\t"
        "ds_read_b128 v[36:39], v121 offset:5120\n\t"
        "ds_read_b128 v[40:43], v121 offset:6144\n\t"
        "ds_read_b128 v[44:47], v121 offset:7168\n\t"
        "ds_read_b128 v[56:59], v121 offset:8192\n\t"
        "ds_read_b128 v[60:63], v121 offset:9216\n\t"
        "ds_read_b128 v[64:67], v121 offset:10240\n\t"
        "ds_read_b128 v[68:71], v121 offset:11264\n\t"
        "ds_read_b128 v[72:75], v121 offset:12288\n\t"
        "ds_read_b128 v[76:79], v121 offset:13312\n\t"
        "ds_read_b128 v[80:83], v121 offset:14336\n\t"
        "ds_read_b128 v[84:87], v121 offset:15360\n\t"
        "s_and_b32 s80, s40, 3\n\t"
        "s_lshl_b32 s80, s80, 17\n\t"
        "s_add_u32 s57, s80, %[nA]\n\t"
        "s_add_u32 s58, s57, 0x1000\n\t"
        "s_add_u32 s59, s57, 0x8000\n\t"
        "s_add_u32 s60, s57, 0x9000\n\t"
        "s_and_b32 s80, s40, 3\n\t"
        "s_lshl_b32 s80, s80, 17\n\t"
        "s_add_u32 s63, s80, %[nC]\n\t"
        "s_add_u32 s66, s80, %[nS]\n\t"
        "s_add_u32 s81, s80, 0x20000\n\t"
        "s_and_b32 s81, s81, 0x60000\n\t"
        "s_add_u32 s67, s81, %[nW]\n\t"
        "s_add_u32 s79, s81, %[nT]\n\t"
        "s_cmp_eq_u32 %[w], 2\n\t"
        "s_cselect_b32 s81, s80, s81\n\t"
        "s_xor_b32 s81, s81, 0x40000\n\t"
        "s_add_u32 s68, s81, %[pob]\n\t"
        "s_lshl_b32 s72, s40, 17\n\t"
        "s_add_u32 s72, s72, %[kvo]\n\t"
        "s_mov_b32 s88, 0\n\t"
        "s_mov_b32 s89, 0\n\t"
        "s_mov_b32 s90, 0\n\t"
        "v_mov_b32_e32 v88, 0\n\t"
        "v_mov_b32_e32 v89, 0\n\t"
        "v_mov_b32_e32 v90, 0\n\t"
        "v_mov_b32_e32 v91, 0\n\t"
        "v_mov_b32_e32 v92, 0\n\t"
        "v_mov_b32_e32 v93, 0\n\t"
        "v_mov_b32_e32 v94, 0\n\t"
        "v_mov_b32_e32 v95, 0\n\t"
        "v_mov_b32_e32 v96, 0\n\t"
        "v_mov_b32_e32 v97, 0\n\t"
        "v_mov_b32_e32 v98, 0\n\t"
        "v_mov_b32_e32 v99, 0\n\t"
        "v_mov_b32_e32 v100, 0\n\t"
        "v_mov_b32_e32 v101, 0\n\t"
        "v_mov_b32_e32 v102, 0\n\t"
        "v_mov_b32_e32 v103, 0\n\t"
        "v_mov_b32_e32 v52, v112\n\t"
        "v_mov_b32_e32 v53, v113\n\t"
        "v_mov_b32_e32 v49, 0\n\t"
        "v_lshlrev_b32_e32 v51, 6, v120\n\t"
        "s_cmp_eq_u32 %[w], 0\n\t"
        "s_cselect_b32 s73, %[nT], s36\n\t"
        "s_cselect_b32 s74, %[pra], s36\n\t"
        "v_mov_b32_e32 v54, s40\n\t"
        "s_cmp_ge_u32 s40, 56\n\t"
        "s_cselect_b32 s65, s74, s36\n\t"
        "s_waitcnt lgkmcnt(0)\n\t"
        "s_barrier\n\t"
        "s_memrealtime s[98:99]\n\t"
        "s_waitcnt lgkmcnt(0)\n\t"
        "s_mov_b32 s41, s98\n\t"
        "v_mov_b32_e32 v48, s98\n\t"
        ".p2align 6\n\t"
        ".Lloop%=:\n\t"
        "s_mov_b32 s97, 0\n\t"
        "s_movk_i32 s96, 0x1000\n\t"
        ".Lwa%=:\n\t"
        "s_memrealtime s[98:99]\n\t"
        "s_addk_i32 s96, -1\n\t"
        "s_cmp_lt_i32 s96, 0\n\t"
        "s_cbranch_scc1 .Lta%=\n\t"
        "s_waitcnt lgkmcnt(0)\n\t"
        "s_sub_i32 s98, s98, s41\n\t"
        "s_cmp_lt_i32 s98, %c[iFT]\n\t"
        "s_cbranch_scc1 .Lwa%=\n\t"
        "s_cmp_ge_i32 s98, 0\n\t"
        "s_cbranch_scc1 .Lxa%=\n\t"
        ".Lqa%=:\n\t"
        "s_nop %c[iFN]\n\t"
        "s_addk_i32 s98, 1\n\t"
        "s_cmp_lt_i32 s98, 0\n\t"
        "s_cbranch_scc1 .Lqa%=\n\t"
        ".Lxa%=:\n\t"
        "buffer_load_dwordx4 v[0:3], v120, %[srd], s57 offen sc1\n\t"
        "s_mov_b32 s80, s88\n\t"
        "s_add_i32 s81, s92, s50\n\t"
        "s_sub_i32 s81, s81, %c[iBM]\n\t"
        "s_cmp_le_i32 s81, s44\n\t"
        "s_cselect_b32 s80, s80, 0\n\t"
        "s_cmp_lt_i32 s50, %c[LC24]\n\t"
        "s_cselect_b32 s80, s80, 0\n\t"
        "s_mul_i32 s80, s80, %c[iBS]\n\t"
        "buffer_load_dwordx4 v[4:7], v120, %[srd], s58 offen sc1\n\t"
        "s_add_u32 s50, s80, s50\n\t"
        "s_sub_i32 s50, s50, s89\n\t"
        "s_max_i32 s50, s50, %c[LC]\n\t"
        "s_lshl_b32 s80, s92, 1\n\t"
        "s_sub_i32 s81, s80, s52\n\t"
        "s_ashr_i32 s82, s81, %c[UPSH]\n\t"
        "s_max_i32 s82, s82, %c[TRUP]\n\t"
        "s_add_i32 s82, s82, s52\n\t"
        "buffer_load_dwordx4 v[8:11], v120, %[srd], s59 offen sc1\n\t"
        "s_min_i32 s82, s82, s80\n\t"
        "s_ashr_i32 s83, s81, %c[DNSH]\n\t"
        "s_min_i32 s83, s83, -%c[TRDN]\n\t"
        "s_add_i32 s83, s83, s52\n\t"
        "s_max_i32 s83, s83, s80\n\t"
        "s_cmp_gt_i32 s81, 0\n\t"
        "s_cselect_b32 s82, s82, s83\n\t"
        "s_cmp_lg_u32 s90, 0\n\t"
        "s_cselect_b32 s52, s82, s52\n\t"
        "buffer_load_dwordx4 v[12:15], v120, %[srd], s60 offen sc1\n\t"
        "s_add_i32 s80, s52, 1\n\t"
        "s_ashr_i32 s80, s80, 1\n\t"
        "s_add_i32 s45, s80, s50\n\t"
        "s_add_u32 s80, s40, 1\n\t"
        "s_min_u32 s80, s80, 63\n\t"
        "s_lshl_b32 s80, s80, 18\n\t"
        "s_add_u32 s69, s80, %[kx]\n\t"
        "s_add_u32 s70, s69, 0x1000\n\t"
        "s_lshl_b32 s80, s40, 18\n\t"
        "s_add_u32 s71, s80, %[vx]\n\t"
        "s_add_u32 s80, s40, 2\n\t"
        "s_lshl_b32 s81, s80, 9\n\t"
        "s_add_u32 s81, s81, %[kf]\n\t"
        "s_and_b32 s80, s80, 0x43\n\t"
        "s_cselect_b32 s64, s36, s81\n\t"
        "s_nop 15\n\t"
        "s_nop 7\n\t"
        "s_mov_b64 exec, 7\n\t"
        "buffer_store_dwordx2 v[48:49], v51, %[srd], s73 offen sc1\n\t"
        "s_mov_b64 exec, -1\n\t"
        "buffer_load_dwordx4 v[112:115], v120, %[wsd], s71 offen offset:0 sc1\n\t"
        "buffer_load_dwordx4 v[116:119], v120, %[wsd], s71 offen offset:1024 sc1\n\t"
        ".Lgo%=:\n\t"
        "s_waitcnt vmcnt(6)\n\t"
        "v_smfmac_f32_16x16x64_bf16 v[88:91], v[0:3], v[16:23], v122\n\t"
        "ds_write_b128 v120, v[0:3] offset:0\n\t"
        "v_smfmac_f32_16x16x64_bf16 v[92:95], v[0:3], v[24:31], v122\n\t"
        "v_smfmac_f32_16x16x64_bf16 v[96:99], v[0:3], v[32:39], v122\n\t"
        "v_smfmac_f32_16x16x64_bf16 v[100:103], v[0:3], v[40:47], v122\n\t"
        "s_waitcnt vmcnt(5)\n\t"
        "v_smfmac_f32_16x16x64_bf16 v[88:91], v[4:7], v[56:63], v122\n\t"
        "ds_write_b128 v120, v[4:7] offset:4096\n\t"
        "v_smfmac_f32_16x16x64_bf16 v[92:95], v[4:7], v[64:71], v122\n\t"
        "v_smfmac_f32_16x16x64_bf16 v[96:99], v[4:7], v[72:79], v122\n\t"
        "v_smfmac_f32_16x16x64_bf16 v[100:103], v[4:7], v[80:87], v122\n\t"
        "s_waitcnt vmcnt(4)\n\t"
        "v_smfmac_f32_16x16x64_bf16 v[88:91], v[8:11], v[128:135], v122\n\t"
        "ds_write_b128 v120, v[8:11] offset:8192\n\t"
        "v_smfmac_f32_16x16x64_bf16 v[92:95], v[8:11], v[136:143], v122\n\t"
        "s_waitcnt lgkmcnt(1)\n\t"
        "s_barrier\n\t"
        "v_smfmac_f32_16x16x64_bf16 v[96:99], v[8:11], v[192:199], v122\n\t"
        "ds_read_b128 v[16:19], v124 offset:0\n\t"
        "v_smfmac_f32_16x16x64_bf16 v[100:103], v[8:11], v[200:207], v122\n\t"
        "ds_read_b128 v[20:23], v125 offset:0\n\t"
        "s_waitcnt vmcnt(3)\n\t"
        "v_smfmac_f32_16x16x64_bf16 v[88:91], v[12:15], a[0:7], v122\n\t"
        "ds_write_b128 v120, v[12:15] offset:12288\n\t"
        "v_smfmac_f32_16x16x64_bf16 v[92:95], v[12:15], a[8:15], v122\n\t"
        "buffer_store_dword v54, off, %[wsd], s65 sc1\n\t"
        "v_smfmac_f32_16x16x64_bf16 v[96:99], v[12:15], a[64:71], v122\n\t"
        "ds_read_b128 v[24:27], v126 offset:0\n\t"
        "v_smfmac_f32_16x16x64_bf16 v[100:103], v[12:15], a[72:79], v122\n\t"
        "ds_read_b128 v[28:31], v124 offset:4096\n\t"
        "s_waitcnt lgkmcnt(4)\n\t"
        "v_smfmac_f32_16x16x64_bf16 v[88:91], v[16:19], a[16:23], v122\n\t"
        "ds_read_b128 v[32:35], v125 offset:4096\n\t"
        "v_smfmac_f32_16x16x64_bf16 v[92:95], v[16:19], a[24:31], v122\n\t"
        "ds_read_b128 v[36:39], v126 offset:4096\n\t"
        "s_waitcnt lgkmcnt(5)\n\t"
        "v_smfmac_f32_16x16x64_bf16 v[88:91], v[20:23], a[32:39], v122\n\t"
        "ds_read_b128 v[72:75], v121 offset:16384\n\t"
        "v_smfmac_f32_16x16x64_bf16 v[92:95], v[20:23], a[40:47], v122\n\t"
        "s_waitcnt lgkmcnt(5)\n\t"
        "s_barrier\n\t"
        "ds_read_b128 v[76:79], v121 offset:17408\n\t"
        "s_waitcnt lgkmcnt(5)\n\t"
        "v_smfmac_f32_16x16x64_bf16 v[88:91], v[24:27], a[48:55], v122\n\t"
        "ds_read_b128 v[0:3], v124 offset:12288\n\t"
        "v_smfmac_f32_16x16x64_bf16 v[92:95], v[24:27], a[56:63], v122\n\t"
        "ds_read_b128 v[80:83], v121 offset:18432\n\t"
        "s_waitcnt lgkmcnt(6)\n\t"
        "v_smfmac_f32_16x16x64_bf16 v[88:91], v[28:31], a[144:151], v122\n\t"
        "ds_read_b128 v[84:87], v121 offset:19456\n\t"
        "v_smfmac_f32_16x16x64_bf16 v[92:95], v[28:31], a[152:159], v122\n\t"
        "ds_read_b128 v[40:43], v124 offset:8192\n\t"
        "s_waitcnt lgkmcnt(7)\n\t"
        "v_smfmac_f32_16x16x64_bf16 v[88:91], v[32:35], a[160:167], v122\n\t"
        "ds_read_b128 v[44:47], v125 offset:8192\n\t"
        "v_mov_b32_e32 v12, 0x3f803f80\n\t"
        "v_smfmac_f32_16x16x64_bf16 v[92:95], v[32:35], a[168:175], v122\n\t"
        "ds_read_b128 v[56:59], v121 offset:20480\n\t"
        "v_mov_b32_e32 v13, 0x3f803f80\n\t"
        "s_waitcnt lgkmcnt(8)\n\t"
        "v_smfmac_f32_16x16x64_bf16 v[88:91], v[36:39], a[176:183], v122\n\t"
        "ds_read_b128 v[60:63], v121 offset:21504\n\t"
        "v_mov_b32_e32 v14, 0x3f803f80\n\t"
        "v_smfmac_f32_16x16x64_bf16 v[92:95], v[36:39], a[184:191], v122\n\t"
        "ds_read_b128 v[4:7], v125 offset:12288\n\t"
        "v_mov_b32_e32 v15, 0x3f803f80\n\t"
        "s_waitcnt lgkmcnt(8)\n\t"
        "s_waitcnt lgkmcnt(7)\n\t"
        "v_smfmac_f32_16x16x64_bf16 v[88:91], v[0:3], v[72:79], v122\n\t"
        "ds_read_b128 v[64:67], v121 offset:22528\n\t"
        "s_waitcnt lgkmcnt(6)\n\t"
        "v_smfmac_f32_16x16x64_bf16 v[92:95], v[0:3], v[80:87], v122\n\t"
        "ds_read_b128 v[68:71], v121 offset:23552\n\t"
        "s_waitcnt lgkmcnt(6)\n\t"
        "v_smfmac_f32_16x16x64_bf16 v[88:91], v[40:43], v[144:151], v122\n\t"
        "ds_read_b128 v[48:51], v126 offset:8192\n\t"
        "v_smfmac_f32_16x16x64_bf16 v[92:95], v[40:43], v[152:159], v122\n\t"
        "ds_read_b128 v[8:11], v126 offset:12288\n\t"
        "s_waitcnt lgkmcnt(7)\n\t"
        "v_smfmac_f32_16x16x64_bf16 v[88:91], v[44:47], v[160:167], v122\n\t"
        "ds_read_b128 v[72:75], v121 offset:24576\n\t"
        "v_smfmac_f32_16x16x64_bf16 v[92:95], v[44:47], v[168:175], v122\n\t"
        "ds_read_b128 v[76:79], v121 offset:25600\n\t"
        "s_waitcnt lgkmcnt(7)\n\t"
        "s_waitcnt lgkmcnt(6)\n\t"
        "v_smfmac_f32_16x16x64_bf16 v[88:91], v[4:7], v[56:63], v122\n\t"
        "ds_read_b128 v[80:83], v121 offset:26624\n\t"
        "s_waitcnt lgkmcnt(5)\n\t"
        "v_smfmac_f32_16x16x64_bf16 v[92:95], v[4:7], v[64:71], v122\n\t"
        "ds_read_b128 v[84:87], v121 offset:27648\n\t"
        "s_waitcnt lgkmcnt(5)\n\t"
        "v_smfmac_f32_16x16x64_bf16 v[88:91], v[48:51], v[176:183], v122\n\t"
        "v_mov_b32_e32 v64, 0\n\t"
        "v_smfmac_f32_16x16x64_bf16 v[92:95], v[48:51], v[184:191], v122\n\t"
        "v_mov_b32_e32 v65, 0\n\t"
        "s_waitcnt lgkmcnt(4)\n\t"
        "v_smfmac_f32_16x16x64_bf16 v[88:91], v[8:11], a[128:135], v122\n\t"
        "v_mov_b32_e32 v66, 0\n\t"
        "v_smfmac_f32_16x16x64_bf16 v[92:95], v[8:11], a[136:143], v122\n\t"
        "v_mov_b32_e32 v67, 0\n\t"
        "s_waitcnt lgkmcnt(2)\n\t"
        "v_smfmac_f32_16x16x64_bf16 v[96:99], v[0:3], v[72:79], v122\n\t"
        "ds_read_b128 v[72:75], v121 offset:28672\n\t"
        "s_waitcnt lgkmcnt(1)\n\t"
        "v_smfmac_f32_16x16x64_bf16 v[100:103], v[0:3], v[80:87], v122\n\t"
        "v_add_f32_e32 v88, v88, v90\n\t"
        "v_add_f32_e32 v89, v89, v91\n\t"
        "v_smfmac_f32_16x16x64_bf16 v[96:99], v[16:19], a[80:87], v122\n\t"
        "v_cvt_pk_bf16_f32 v90, v88, v89\n\t"
        "v_add_f32_e32 v92, v92, v94\n\t"
        "v_smfmac_f32_16x16x64_bf16 v[100:103], v[16:19], a[88:95], v122\n\t"
        "ds_write_b32 v127, v90\n\t"
        "v_add_f32_e32 v93, v93, v95\n\t"
        "v_cvt_pk_bf16_f32 v91, v92, v93\n\t"
        "v_smfmac_f32_16x16x64_bf16 v[96:99], v[20:23], a[96:103], v122\n\t"
        "ds_write_b32 v127, v91 offset:384\n\t"
        "v_cmp_u_f32_e32 vcc, v88, v89\n\t"
        "v_smfmac_f32_16x16x64_bf16 v[100:103], v[20:23], a[104:111], v122\n\t"
        "ds_read_b64_tr_b16 v[56:57], v52 offset:0\n\t"
        "ds_read_b64_tr_b16 v[58:59], v52 offset:64\n\t"
        "v_smfmac_f32_16x16x64_bf16 v[96:99], v[24:27], a[112:119], v122\n\t"
        "ds_read_b64_tr_b16 v[60:61], v53 offset:0\n\t"
        "ds_read_b64_tr_b16 v[62:63], v53 offset:64\n\t"
        "v_smfmac_f32_16x16x64_bf16 v[100:103], v[24:27], a[120:127], v122\n\t"
        "s_cbranch_vccnz .Lf1%=\n\t"
        ".Lr1%=:\n\t"
        "ds_read_b128 v[76:79], v121 offset:29696\n\t"
        "v_smfmac_f32_16x16x64_bf16 v[96:99], v[28:31], a[208:215], v122\n\t"
        "buffer_load_dwordx4 v[88:91], v120, %[srd], s61 offen offset:0 sc1\n\t"
        "v_smfmac_f32_16x16x64_bf16 v[100:103], v[28:31], a[216:223], v122\n\t"
        "buffer_load_dwordx4 v[92:95], v120, %[srd], s61 offen offset:1024 sc1\n\t"
        "v_smfmac_f32_16x16x64_bf16 v[96:99], v[32:35], a[224:231], v122\n\t"
        "s_waitcnt lgkmcnt(1)\n\t"
        "s_barrier\n\t"
        "v_mfma_f32_16x16x32_bf16 v[68:71], v[104:107], v[56:59], 0\n\t"
        "ds_read_b64_tr_b16 v[16:17], v123 offset:0\n\t"
        "v_mfma_f32_16x16x32_bf16 v[68:71], v[108:111], v[60:63], v[68:71]\n\t"
        "ds_read_b64_tr_b16 v[18:19], v123 offset:32\n\t"
        "v_smfmac_f32_16x16x64_bf16 v[100:103], v[32:35], a[232:239], v122\n\t"
        "ds_read_b128 v[80:83], v121 offset:30720\n\t"
        "v_smfmac_f32_16x16x64_bf16 v[96:99], v[36:39], a[240:247], v122\n\t"
        "ds_read_b128 v[84:87], v121 offset:31744\n\t"
        "v_smfmac_f32_16x16x64_bf16 v[100:103], v[36:39], a[248:255], v122\n\t"
        "s_nop 1\n\t"
        "ds_write_b128 v120, v[68:71] offset:16384\n\t"
        "v_smfmac_f32_16x16x64_bf16 v[96:99], v[8:11], a[192:199], v122\n\t"
        "ds_read_b128 v[32:35], v120 offset:20480\n\t"
        "v_smfmac_f32_16x16x64_bf16 v[100:103], v[8:11], a[200:207], v122\n\t"
        "ds_read_b64_tr_b16 v[20:21], v123 offset:768\n\t"
        "s_waitcnt lgkmcnt(7)\n\t"
        "v_smfmac_f32_16x16x64_bf16 v[96:99], v[4:7], v[72:79], v122\n\t"
        "ds_read_b64_tr_b16 v[22:23], v123 offset:800\n\t"
        "s_waitcnt lgkmcnt(4)\n\t"
        "v_smfmac_f32_16x16x64_bf16 v[100:103], v[4:7], v[80:87], v122\n\t"
        "s_waitcnt lgkmcnt(3)\n\t"
        "s_barrier\n\t"
        "ds_read_b128 v[36:39], v124 offset:16384\n\t"
        "v_smfmac_f32_16x16x64_bf16 v[96:99], v[40:43], v[208:215], v122\n\t"
        "ds_read_b128 v[56:59], v125 offset:16384\n\t"
        "v_smfmac_f32_16x16x64_bf16 v[100:103], v[40:43], v[216:223], v122\n\t"
        "ds_read_b128 v[60:63], v126 offset:16384\n\t"
        "v_smfmac_f32_16x16x64_bf16 v[96:99], v[44:47], v[224:231], v122\n\t"
        "ds_read_b64_tr_b16 v[24:25], v123 offset:1536\n\t"
        "ds_read_b64_tr_b16 v[26:27], v123 offset:1568\n\t"
        "v_smfmac_f32_16x16x64_bf16 v[100:103], v[44:47], v[232:239], v122\n\t"
        "ds_read_b64_tr_b16 v[28:29], v123 offset:2304\n\t"
        "ds_read_b64_tr_b16 v[30:31], v123 offset:2336\n\t"
        "v_smfmac_f32_16x16x64_bf16 v[96:99], v[48:51], v[240:247], v122\n\t"
        "buffer_load_dwordx4 v[84:87], v120, %[srd], s66 offen sc1\n\t"
        "v_smfmac_f32_16x16x64_bf16 v[100:103], v[48:51], v[248:255], v122\n\t"
        "s_waitcnt lgkmcnt(9)\n\t"
        "s_cmp_eq_u32 s40, 63\n\t"
        "s_waitcnt lgkmcnt(6)\n\t"
        "v_add_f32_e32 v68, v68, v36\n\t"
        "v_add_f32_e32 v69, v69, v37\n\t"
        "v_pk_add_f32 v[70:71], v[70:71], v[38:39]\n\t"
        "s_waitcnt lgkmcnt(4)\n\t"
        "v_pk_add_f32 v[56:57], v[56:57], v[60:61]\n\t"
        "v_pk_add_f32 v[58:59], v[58:59], v[62:63]\n\t"
        "v_pk_add_f32 v[68:69], v[68:69], v[56:57]\n\t"
        "v_pk_add_f32 v[70:71], v[70:71], v[58:59]\n\t"
        "v_max3_f32 v2, v68, v69, v70\n\t"
        "v_max_f32_e32 v2, v2, v71\n\t"
        "v_pk_add_f32 v[96:97], v[96:97], v[98:99]\n\t"
        "v_pk_add_f32 v[100:101], v[100:101], v[102:103]\n\t"
        "v_max_f32_dpp v3, v2, v2 row_ror:8 row_mask:0xf bank_mask:0xf\n\t"
        "v_max_f32_dpp v1, v2, v2 row_ror:8 row_mask:0xf bank_mask:0xf\n\t"
        "v_cvt_pk_bf16_f32 v50, v96, v97\n\t"
        "v_cvt_pk_bf16_f32 v51, v100, v101\n\t"
        "v_permlane16_swap_b32_e32 v3, v1\n\t"
        "s_cbranch_scc1 .Lend%=\n\t"
        "v_max_f32_e32 v2, v3, v1\n\t"
        "v_max_f32_e32 v3, v3, v1\n\t"
        "v_mov_b32_e32 v9, 0\n\t"
        "v_mov_b32_e32 v10, 0\n\t"
        "v_permlane32_swap_b32_e32 v2, v3\n\t"
        "v_max_f32_e32 v0, v2, v3\n\t"
        "v_mul_f32_e32 v2, 0xbe0293ee, v0\n\t"
        "v_fmamk_f32 v68, v68, 0x3e0293ee, v2\n\t"
        "v_fmamk_f32 v69, v69, 0x3e0293ee, v2\n\t"
        "v_fmamk_f32 v70, v70, 0x3e0293ee, v2\n\t"
        "v_fmamk_f32 v71, v71, 0x3e0293ee, v2\n\t"
        "v_exp_f32_e32 v68, v68\n\t"
        "v_exp_f32_e32 v69, v69\n\t"
        "v_exp_f32_e32 v70, v70\n\t"
        "v_cvt_pk_bf16_f32 v36, v68, v69\n\t"
        "v_exp_f32_e32 v71, v71\n\t"
        "s_nop 0\n\t"
        "v_mov_b32_dpp v38, v36 row_ror:8 row_mask:0xf bank_mask:0xf\n\t"
        "v_cvt_pk_bf16_f32 v37, v70, v71\n\t"
        "s_nop 1\n\t"
        "v_mov_b32_dpp v39, v37 row_ror:8 row_mask:0xf bank_mask:0xf\n\t"
        "s_waitcnt vmcnt(4)\n\t"
        "s_nop 0\n\t"
        "v_mfma_f32_16x16x32_bf16 v[72:75], v[12:15], v[36:39], 0\n\t"
        "s_waitcnt lgkmcnt(0)\n\t"
        "s_waitcnt vmcnt(1)\n\t"
        "v_smfmac_f32_16x16x64_bf16 v[64:67], v[88:91], v[16:23], v122\n\t"
        "v_smfmac_f32_16x16x64_bf16 v[64:67], v[92:95], v[24:31], v122\n\t"
        "v_mfma_f32_16x16x32_bf16 v[40:43], v[112:115], v[36:39], 0\n\t"
        "v_mfma_f32_16x16x32_bf16 v[44:47], v[116:119], v[36:39], 0\n\t"
        ".Lrk%=:\n\t"
        "s_nop 1\n\t"
        "v_mov_b32_e32 v55, v55\n\t"
        "s_nop 1\n\t"
        "v_mov_b32_e32 v55, v55\n\t"
        "v_pk_add_f32 v[64:65], v[64:65], v[66:67]\n\t"
        "v_fmamk_f32 v66, v64, 0x3e0293ee, v2\n\t"
        "v_fmamk_f32 v67, v65, 0x3e0293ee, v2\n\t"
        "v_cmp_nge_f32_e32 vcc, 0x41a00000, v66\n\t"
        "v_exp_f32_e32 v66, v66\n\t"
        "v_cmp_nge_f32_e64 s[80:81], s38, v67\n\t"
        "v_exp_f32_e32 v67, v67\n\t"
        "s_or_b64 vcc, vcc, s[80:81]\n\t"
        "v_cvt_pk_bf16_f32 v8, v66, v67\n\t"
        "v_cvt_pk_bf16_f32 v11, v66, v67\n\t"
        "s_cbranch_scc1 .Lfb%=\n\t"
        ".Lrm%=:\n\t"
        "v_mfma_f32_16x16x16_bf16 v[72:75], v[12:13], v[8:9], v[72:75]\n\t"
        "v_mfma_f32_16x16x16_bf16 v[40:43], v[50:51], v[8:9], v[40:43]\n\t"
        "v_mfma_f32_16x16x16_bf16 v[44:47], v[50:51], v[10:11], v[44:47]\n\t"
        "s_nop 1\n\t"
        "v_mov_b32_e32 v55, v55\n\t"
        "s_nop 1\n\t"
        "v_mov_b32_e32 v55, v55\n\t"
        "v_rcp_f32_e32 v2, v72\n\t"
        "s_nop 0\n\t"
        "v_pk_mul_f32 v[40:41], v[40:41], v[2:3] op_sel_hi:[1,0]\n\t"
        "v_pk_mul_f32 v[42:43], v[42:43], v[2:3] op_sel_hi:[1,0]\n\t"
        "v_cvt_pk_bf16_f32 v76, v40, v41\n\t"
        "v_pk_mul_f32 v[44:45], v[44:45], v[2:3] op_sel_hi:[1,0]\n\t"
        "v_pk_mul_f32 v[46:47], v[46:47], v[2:3] op_sel_hi:[1,0]\n\t"
        "v_cvt_pk_bf16_f32 v77, v42, v43\n\t"
        "v_cvt_pk_bf16_f32 v78, v44, v45\n\t"
        "v_min_u32_e32 v76, -2, v76\n\t"
        "v_cvt_pk_bf16_f32 v79, v46, v47\n\t"
        "v_min_u32_e32 v78, -2, v78\n\t"
        "buffer_store_dwordx4 v[76:79], v33, %[srd], s63 offen sc1\n\t"
        "s_memrealtime s[76:77]\n\t"
        "v_mov_b32_e32 v8, -1\n\t"
        "v_mov_b32_e32 v9, -1\n\t"
        "v_mov_b32_e32 v10, -1\n\t"
        "v_mov_b32_e32 v11, -1\n\t"
        "v_permlane16_swap_b32_e32 v50, v51\n\t"
        "v_mov_b32_e32 v14, s40\n\t"
        "s_waitcnt lgkmcnt(0)\n\t"
        "s_waitcnt vmcnt(1)\n\t"
        "s_mov_b32 s97, 0\n\t"
        ".Lsr%=:\n\t"
        "s_mov_b32 exec_hi, 0xffff\n\t"
        "v_subrev_u32_e32 v0, s41, v84\n\t"
        "v_subrev_u32_e32 v1, s41, v86\n\t"
        "v_max_i32_e32 v0, v0, v1\n\t"
        "v_max_u32_e32 v1, v85, v87\n\t"
        "v_cmp_eq_u32_e32 vcc, -1, v1\n\t"
        "v_max_i32_dpp v0, v0, v0 quad_perm:[1,0,3,2] row_mask:0xf bank_mask:0xf\n\t"
        "v_or_b32_e32 v2, v85, v87\n\t"
        "s_nop 0\n\t"
        "v_max_i32_dpp v0, v0, v0 quad_perm:[2,3,0,1] row_mask:0xf bank_mask:0xf\n\t"
        "s_nop 1\n\t"
        "v_max_i32_dpp v0, v0, v0 row_ror:4 row_mask:0xf bank_mask:0xf\n\t"
        "s_nop 1\n\t"
        "v_max_i32_dpp v0, v0, v0 row_ror:8 row_mask:0xf bank_mask:0xf\n\t"
        "s_cbranch_vccnz .Lsf%=\n\t"
        "v_readlane_b32 s85, v0, 0\n\t"
        "v_readlane_b32 s81, v0, 16\n\t"
        "v_readlane_b32 s56, v0, 32\n\t"
        "v_and_b32_e32 v1, 1, v2\n\t"
        "v_cmp_ne_u32_e64 s[86:87], 0, v1\n\t"
        "v_and_b32_e32 v1, 2, v2\n\t"
        "v_cmp_ne_u32_e64 s[88:89], 0, v1\n\t"
        "v_and_b32_e32 v1, 4, v2\n\t"
        "v_cmp_ne_u32_e64 s[90:91], 0, v1\n\t"
        "s_max_i32 s56, s56, s81\n\t"
        "s_add_u32 s92, s85, s41\n\t"
        "s_sub_u32 s92, s92, s43\n\t"
        "s_add_i32 s93, s56, s47\n\t"
        "s_mov_b32 s44, s46\n\t"
        "s_mov_b32 s46, s45\n\t"
        "s_mov_b32 s94, s48\n\t"
        "s_mov_b32 s43, s41\n\t"
        "s_cmp_gt_i32 s56, s54\n\t"
        "s_cbranch_scc1 .Lmv%=\n\t"
        "s_mov_b32 s37, 0\n\t"
        "s_mov_b32 s48, 0\n\t"
        "s_mov_b32 s95, 0\n\t"
        ".Lsd%=:\n\t"
        "s_mov_b32 exec_hi, -1\n\t"
        "s_add_u32 s42, s43, s46\n\t"
        "s_sleep %c[HSLP]\n\t"
        "buffer_load_dwordx2 v[48:49], off, %[wsd], s64 sc1\n\t"
        "buffer_load_dwordx4 v[104:107], v120, %[wsd], s69 offen sc1\n\t"
        "buffer_load_dwordx4 v[108:111], v120, %[wsd], s70 offen sc1\n\t"
        "buffer_store_dwordx2 v[50:51], v34, %[wsd], s72 offen offset:2048 sc1\n\t"
        "s_cmp_eq_u32 %[w], 0\n\t"
        "s_cselect_b32 s80, s72, s36\n\t"
        "buffer_store_dwordx4 v[88:91], v120, %[wsd], s80 offen sc1\n\t"
        "s_cmp_eq_u32 %[w], 1\n\t"
        "s_cselect_b32 s80, s72, s36\n\t"
        "buffer_store_dwordx4 v[92:95], v120, %[wsd], s80 offen sc1\n\t"
        "s_lshl_b32 s80, %[w], 10\n\t"
        "s_add_u32 s80, s80, s61\n\t"
        "s_barrier\n\t"
        "buffer_store_dwordx2 v[8:9], v34, %[srd], s80 offen sc1\n\t"
        "s_cmp_eq_u32 %[w], 0\n\t"
        "s_cselect_b32 exec_hi, 0, -1\n\t"
        "buffer_store_dwordx4 v[8:11], v120, %[srd], s68 offen sc1\n\t"
        "s_mov_b32 exec_hi, -1\n\t"
        "s_mov_b64 exec, 1\n\t"
        "buffer_store_dword v14, off, %[wsd], s74 sc1\n\t"
        "s_mov_b64 exec, -1\n\t"
        "ds_read_b128 v[16:19], v121 offset:0\n\t"
        "ds_read_b128 v[20:23], v121 offset:1024\n\t"
        "ds_read_b128 v[24:27], v121 offset:2048\n\t"
        "ds_read_b128 v[28:31], v121 offset:3072\n\t"
        "ds_read_b128 v[32:35], v121 offset:4096\n\t"
        "ds_read_b128 v[36:39], v121 offset:5120\n\t"
        "ds_read_b128 v[40:43], v121 offset:6144\n\t"
        "ds_read_b128 v[44:47], v121 offset:7168\n\t"
        "ds_read_b128 v[56:59], v121 offset:8192\n\t"
        "ds_read_b128 v[60:63], v121 offset:9216\n\t"
        "ds_read_b128 v[64:67], v121 offset:10240\n\t"
        "ds_read_b128 v[68:71], v121 offset:11264\n\t"
        "ds_read_b128 v[72:75], v121 offset:12288\n\t"
        "ds_read_b128 v[76:79], v121 offset:13312\n\t"
        "ds_read_b128 v[80:83], v121 offset:14336\n\t"
        "ds_read_b128 v[84:87], v121 offset:15360\n\t"
        "s_cmp_ge_u32 s40, %c[iCT0]\n\t"
        "s_cselect_b32 s85, 1, 0\n\t"
        "s_cmp_lg_u64 s[86:87], 0\n\t"
        "s_cselect_b32 s86, s85, 0\n\t"
        "s_cmp_lg_u64 s[88:89], 0\n\t"
        "s_cselect_b32 s87, s85, 0\n\t"
        "s_cmp_lg_u64 s[90:91], 0\n\t"
        "s_cselect_b32 s88, s85, 0\n\t"
        "s_or_b32 s80, s94, s86\n\t"
        "s_or_b32 s80, s80, s87\n\t"
        "s_andn2_b32 s90, s85, s80\n\t"
        "s_cmp_le_i32 s92, s44\n\t"
        "s_cselect_b32 s80, s85, 0\n\t"
        "s_andn2_b32 s91, s80, s88\n\t"
        "s_and_b32 s80, s40, %c[LDEC]\n\t"
        "s_cselect_b32 s89, 0, 1\n\t"
        "s_andn2_b32 s80, s86, s94\n\t"
        "s_cmp_lt_i32 s51, %c[LA24]\n\t"
        "s_cselect_b32 s80, s80, 0\n\t"
        "s_mul_i32 s80, s80, %c[iBS]\n\t"
        "s_add_u32 s51, s80, s51\n\t"
        "s_sub_i32 s51, s51, s89\n\t"
        "s_max_i32 s51, s51, %c[LA]\n\t"
        "s_lshl_b32 s80, s93, 1\n\t"
        "s_sub_i32 s81, s80, s53\n\t"
        "s_ashr_i32 s82, s81, %c[UPSH]\n\t"
        "s_max_i32 s82, s82, %c[TRUP]\n\t"
        "s_add_i32 s82, s82, s53\n\t"
        "s_min_i32 s82, s82, s80\n\t"
        "s_ashr_i32 s83, s81, %c[DNSH]\n\t"
        "s_min_i32 s83, s83, -%c[TRDN]\n\t"
        "s_add_i32 s83, s83, s53\n\t"
        "s_max_i32 s83, s83, s80\n\t"
        "s_cmp_gt_i32 s81, 0\n\t"
        "s_cselect_b32 s82, s82, s83\n\t"
        "s_cmp_lg_u32 s91, 0\n\t"
        "s_cselect_b32 s53, s82, s53\n\t"
        "s_add_i32 s80, s53, 1\n\t"
        "s_ashr_i32 s80, s80, 1\n\t"
        "s_add_i32 s47, s80, s51\n\t"
        "s_add_u32 s81, s47, %c[AUP]\n\t"
        "s_min_i32 s80, s95, s81\n\t"
        "s_max_i32 s47, s47, s80\n\t"
        "s_add_u32 s41, s42, s47\n\t"
        "s_cmp_eq_u32 s40, 0\n\t"
        "s_cselect_b32 s51, %c[LA], s51\n\t"
        "v_mov_b32_e32 v88, 0\n\t"
        "v_mov_b32_e32 v89, 0\n\t"
        "v_mov_b32_e32 v90, 0\n\t"
        "v_mov_b32_e32 v91, 0\n\t"
        "v_mov_b32_e32 v92, 0\n\t"
        "v_mov_b32_e32 v93, 0\n\t"
        "v_mov_b32_e32 v94, 0\n\t"
        "v_mov_b32_e32 v95, 0\n\t"
        "v_mov_b32_e32 v96, 0\n\t"
        "v_mov_b32_e32 v97, 0\n\t"
        "v_mov_b32_e32 v98, 0\n\t"
        "v_mov_b32_e32 v99, 0\n\t"
        "v_mov_b32_e32 v100, 0\n\t"
        "v_mov_b32_e32 v101, 0\n\t"
        "v_mov_b32_e32 v102, 0\n\t"
        "v_mov_b32_e32 v103, 0\n\t"
        "s_add_u32 s40, s40, 1\n\t"
        "v_mov_b32_e32 v6, 30720\n\t"
        "ds_read_b64 v[6:7], v6\n\t"
        "s_waitcnt lgkmcnt(0)\n\t"
        "v_readfirstlane_b32 s80, v6\n\t"
        "v_readfirstlane_b32 s81, v7\n\t"
        "s_cmp_eq_u32 s80, s40\n\t"
        "s_cselect_b32 s84, 1, 0\n\t"
        "s_cmp_eq_u32 s81, s40\n\t"
        "s_cselect_b32 s81, 2, 0\n\t"
        "s_or_b32 s84, s84, s81\n\t"
        "s_mov_b32 s85, s79\n\t"
        "s_and_b32 s80, s40, 3\n\t"
        "s_lshl_b32 s80, s80, 17\n\t"
        "s_add_u32 s57, s80, %[nA]\n\t"
        "s_add_u32 s58, s57, 0x1000\n\t"
        "s_add_u32 s59, s57, 0x8000\n\t"
        "s_add_u32 s60, s57, 0x9000\n\t"
        "s_and_b32 s80, s40, 3\n\t"
        "s_lshl_b32 s80, s80, 17\n\t"
        "s_add_u32 s63, s80, %[nC]\n\t"
        "s_add_u32 s66, s80, %[nS]\n\t"
        "s_add_u32 s81, s80, 0x20000\n\t"
        "s_and_b32 s81, s81, 0x60000\n\t"
        "s_add_u32 s67, s81, %[nW]\n\t"
        "s_add_u32 s79, s81, %[nT]\n\t"
        "s_cmp_eq_u32 %[w], 2\n\t"
        "s_cselect_b32 s81, s80, s81\n\t"
        "s_xor_b32 s81, s81, 0x40000\n\t"
        "s_add_u32 s68, s81, %[pob]\n\t"
        "s_lshl_b32 s72, s40, 17\n\t"
        "s_add_u32 s72, s72, %[kvo]\n\t"
        "v_lshlrev_b32_e32 v51, 6, v120\n\t"
        "s_cmp_eq_u32 %[w], 0\n\t"
        "s_cselect_b32 s73, s85, s36\n\t"
        "s_cselect_b32 s74, %[pra], s36\n\t"
        "v_mov_b32_e32 v54, s40\n\t"
        "s_cmp_ge_u32 s40, 56\n\t"
        "s_cselect_b32 s65, s74, s36\n\t"
        "s_mov_b32 s80, s61\n\t"
        "s_mov_b32 s61, s62\n\t"
        "s_mov_b32 s62, s80\n\t"
        "s_waitcnt vmcnt(8)\n\t"
        "s_cmp_eq_u32 s64, s36\n\t"
        "s_cbranch_scc1 .Lk11%=\n\t"
        "s_mov_b32 s97, 0\n\t"
        ".Lk01%=:\n\t"
        "v_readfirstlane_b32 s82, v48\n\t"
        "v_readfirstlane_b32 s83, v49\n\t"
        "s_xor_b32 s82, s82, %[tag]\n\t"
        "s_xor_b32 s83, s83, %[tag]\n\t"
        "s_or_b32 s82, s82, s83\n\t"
        "s_cbranch_scc0 .Lk11%=\n\t"
        "s_sleep 3\n\t"
        "buffer_load_dwordx2 v[48:49], off, %[wsd], s64 sc1\n\t"
        "s_add_u32 s97, s97, 1\n\t"
        "s_cmp_lt_u32 s97, 0x200000\n\t"
        "s_cbranch_scc0 .Lgu%=\n\t"
        "s_waitcnt vmcnt(0)\n\t"
        "s_branch .Lk01%=\n\t"
        ".Lk11%=:\n\t"
        "v_mov_b32_e32 v48, s76\n\t"
        "v_mov_b32_e32 v49, s84\n\t"
        "s_branch .Lloop%=\n\t"
        ".Lmv%=:\n\t"
        ".Lhk0%=:\n\t"
        "s_cbranch_execz .Lhk1%=\n\t"
        "s_mov_b32 s48, 1\n\t"
        "s_cmp_eq_u32 s54, %c[iTOL]\n\t"
        "s_cbranch_scc1 .Lm1%=\n\t"
        "s_max_i32 s80, s85, s56\n\t"
        "s_add_u32 s43, s41, s80\n\t"
        "s_add_u32 s43, s43, %c[iPEN0]\n\t"
        ".Lhk1%=:\n\t"
        "s_cbranch_execz .Lhk2%=\n\t"
        "s_mov_b32 s54, %c[iTOL]\n\t"
        "s_add_u32 s46, s46, %c[iX0C]\n\t"
        "s_mov_b32 s95, 0\n\t"
        "s_add_u32 s51, s51, %c[iX0A]\n\t"
        "v_and_b32_e32 v0, 3, v84\n\t"
        "v_and_b32_e32 v1, 3, v86\n\t"
        ".Lhk2%=:\n\t"
        "s_cbranch_execz .Lhk3%=\n\t"
        "v_lshlrev_b32_e64 v0, v0, 1\n\t"
        "v_lshlrev_b32_e64 v1, v1, 1\n\t"
        "v_or_b32_e32 v0, v0, v1\n\t"
        "s_mov_b32 s80, 0\n\t"
        "v_and_b32_e32 v1, 1, v0\n\t"
        "v_cmp_ne_u32_e32 vcc, 0, v1\n\t"
        ".Lhk3%=:\n\t"
        "s_cbranch_execz .Lhk4%=\n\t"
        "s_nop 1\n\t"
        "s_cmp_lg_u64 vcc, 0\n\t"
        "s_cselect_b32 s81, 2, 0\n\t"
        "s_or_b32 s80, s80, s81\n\t"
        "v_and_b32_e32 v1, 2, v0\n\t"
        "v_cmp_ne_u32_e32 vcc, 0, v1\n\t"
        ".Lhk4%=:\n\t"
        "s_cbranch_execz .Lhk5%=\n\t"
        "s_nop 1\n\t"
        "s_cmp_lg_u64 vcc, 0\n\t"
        "s_cselect_b32 s81, 4, 0\n\t"
        "s_or_b32 s80, s80, s81\n\t"
        "v_and_b32_e32 v1, 4, v0\n\t"
        "v_cmp_ne_u32_e32 vcc, 0, v1\n\t"
        ".Lhk5%=:\n\t"
        "s_cbranch_execz .Lhk6%=\n\t"
        "s_nop 1\n\t"
        "s_cmp_lg_u64 vcc, 0\n\t"
        "s_cselect_b32 s81, 8, 0\n\t"
        "s_or_b32 s80, s80, s81\n\t"
        "v_and_b32_e32 v1, 8, v0\n\t"
        "v_cmp_ne_u32_e32 vcc, 0, v1\n\t"
        ".Lhk6%=:\n\t"
        "s_cbranch_execz .Lhk7%=\n\t"
        "s_nop 1\n\t"
        "s_cmp_lg_u64 vcc, 0\n\t"
        "s_cselect_b32 s81, 16, 0\n\t"
        "s_or_b32 s80, s80, s81\n\t"
        "s_lshr_b32 s80, 0x1adf1210, s80\n\t"
        "s_and_b32 s55, s80, 3\n\t"
        ".Lhk7%=:\n\t"
        "s_cbranch_execz .Lhk8%=\n\t"
        "s_branch .Lal%=\n\t"
        ".Lm1%=:\n\t"
        "s_add_u32 s80, s37, %c[iPEN2]\n\t"
        "s_min_i32 s80, s80, %c[iPENM]\n\t"
        "s_cmp_lg_u32 s37, 0\n\t"
        "s_cselect_b32 s37, s80, %c[iPEN]\n\t"
        "s_add_u32 s80, s56, s37\n\t"
        ".Lhk8%=:\n\t"
        "s_cbranch_execz .Lhk9%=\n\t"
        "s_max_i32 s80, s80, %c[iPMIN]\n\t"
        "s_add_u32 s43, s41, s80\n\t"
        "s_add_u32 s95, s93, %c[LA]\n\t"
        "s_add_u32 s46, s46, %c[iXR]\n\t"
        ".Lal%=:\n\t"
        "s_sub_u32 s80, s55, s43\n\t"
        "s_and_b32 s80, s80, %c[iPHM]\n\t"
        ".Lhk9%=:\n\t"
        "s_cbranch_execz .Lhk10%=\n\t"
        "s_add_u32 s43, s43, s80\n\t"
        "s_branch .Lsd%=\n\t"
        ".Lsf%=:\n\t"
        "s_mov_b32 exec_hi, -1\n\t"
        "s_sleep 2\n\t"
        "buffer_load_dwordx4 v[84:87], v120, %[srd], s66 offen sc1\n\t"
        "s_add_u32 s97, s97, 1\n\t"
        ".Lhk10%=:\n\t"
        "s_cbranch_execz .Lhk11%=\n\t"
        "s_cmp_lt_u32 s97, 0x100000\n\t"
        "s_cbranch_scc0 .Lgu%=\n\t"
        "s_waitcnt vmcnt(0)\n\t"
        "s_branch .Lsr%=\n\t"
        ".Lf1%=:\n\t"
        "v_max_u32_e32 v54, v0, v2\n\t"
        "v_max3_u32 v54, v54, v1, v3\n\t"
        ".Lhk11%=:\n\t"
        "s_cbranch_execz .Lhk12%=\n\t"
        "v_max3_u32 v54, v54, v4, v6\n\t"
        "v_max3_u32 v54, v54, v5, v7\n\t"
        "v_max3_u32 v54, v54, v8, v10\n\t"
        "v_max3_u32 v54, v54, v9, v11\n\t"
        "s_sleep %c[RSLP]\n\t"
        "buffer_load_dwordx4 v[0:3], v120, %[srd], s57 offen sc1\n\t"
        ".Lhk12%=:\n\t"
        "s_cbranch_execz .Lhk13%=\n\t"
        "buffer_load_dwordx4 v[4:7], v120, %[srd], s58 offen sc1\n\t"
        "buffer_load_dwordx4 v[8:11], v120, %[srd], s59 offen sc1\n\t"
        "buffer_load_dwordx4 v[12:15], v120, %[srd], s60 offen sc1\n\t"
        "buffer_store_dword v0, off, %[wsd], s36\n\t"
        "buffer_store_dword v0, off, %[wsd], s36\n\t"
        "buffer_store_dword v0, off, %[wsd], s36\n\t"
        ".Lhk13%=:\n\t"
        "s_cbranch_execz .Lhk14%=\n\t"
        "ds_read_b128 v[92:95], v120 offset:0\n\t"
        "ds_read_b128 v[68:71], v120 offset:4096\n\t"
        "v_max3_u32 v54, v54, v16, v18\n\t"
        "v_max3_u32 v54, v54, v17, v19\n\t"
        "v_max3_u32 v54, v54, v20, v22\n\t"
        "v_max3_u32 v54, v54, v21, v23\n\t"
        ".Lhk14%=:\n\t"
        "s_cbranch_execz .Lhk15%=\n\t"
        "v_max3_u32 v54, v54, v24, v26\n\t"
        "v_max3_u32 v54, v54, v25, v27\n\t"
        "v_max3_u32 v54, v54, v28, v30\n\t"
        "v_max3_u32 v54, v54, v29, v31\n\t"
        "v_max3_u32 v54, v54, v32, v34\n\t"
        "v_max3_u32 v54, v54, v33, v35\n\t"
        ".Lhk15%=:\n\t"
        "s_cbranch_execz .Lhk16%=\n\t"
        "v_max3_u32 v54, v54, v36, v38\n\t"
        "v_max3_u32 v54, v54, v37, v39\n\t"
        "v_max3_u32 v54, v54, v40, v42\n\t"
        "v_max3_u32 v54, v54, v41, v43\n\t"
        "v_max3_u32 v54, v54, v44, v46\n\t"
        "v_max3_u32 v54, v54, v45, v47\n\t"
        ".Lhk16%=:\n\t"
        "s_cbranch_execz .Lhk17%=\n\t"
        "v_max3_u32 v54, v54, v48, v50\n\t"
        "v_max3_u32 v54, v54, v49, v51\n\t"
        "s_waitcnt lgkmcnt(0)\n\t"
        "v_max3_u32 v54, v54, v92, v94\n\t"
        "v_max3_u32 v54, v54, v93, v95\n\t"
        "v_max3_u32 v54, v54, v68, v70\n\t"
        ".Lhk17%=:\n\t"
        "s_cbranch_execz .Lhk18%=\n\t"
        "v_max3_u32 v54, v54, v69, v71\n\t"
        "ds_read_b128 v[92:95], v120 offset:8192\n\t"
        "ds_read_b128 v[68:71], v120 offset:12288\n\t"
        "s_waitcnt lgkmcnt(0)\n\t"
        "v_max3_u32 v54, v54, v92, v94\n\t"
        "v_max3_u32 v54, v54, v93, v95\n\t"
        ".Lhk18%=:\n\t"
        "s_cbranch_execz .Lhk19%=\n\t"
        "v_max3_u32 v54, v54, v68, v70\n\t"
        "v_max3_u32 v54, v54, v69, v71\n\t"
        "v_cmp_eq_u32_e32 vcc, -1, v54\n\t"
        "s_nop 1\n\t"
        "v_mov_b32_e32 v55, v55\n\t"
        "s_nop 0\n\t"
        ".Lhk19%=:\n\t"
        "s_cbranch_execz .Lhk20%=\n\t"
        "s_cbranch_vccz .Lf2%=\n\t"
        "ds_read_b128 v[16:19], v121 offset:0\n\t"
        "ds_read_b128 v[20:23], v121 offset:1024\n\t"
        "ds_read_b128 v[24:27], v121 offset:2048\n\t"
        "ds_read_b128 v[28:31], v121 offset:3072\n\t"
        "ds_read_b128 v[32:35], v121 offset:4096\n\t"
        ".Lhk20%=:\n\t"
        "s_cbranch_execz .Lhk21%=\n\t"
        "ds_read_b128 v[36:39], v121 offset:5120\n\t"
        "ds_read_b128 v[40:43], v121 offset:6144\n\t"
        "ds_read_b128 v[44:47], v121 offset:7168\n\t"
        "ds_read_b128 v[56:59], v121 offset:8192\n\t"
        "ds_read_b128 v[60:63], v121 offset:9216\n\t"
        "ds_read_b128 v[64:67], v121 offset:10240\n\t"
        ".Lhk21%=:\n\t"
        "s_cbranch_execz .Lhk22%=\n\t"
        "ds_read_b128 v[68:71], v121 offset:11264\n\t"
        "ds_read_b128 v[72:75], v121 offset:12288\n\t"
        "ds_read_b128 v[76:79], v121 offset:13312\n\t"
        "ds_read_b128 v[80:83], v121 offset:14336\n\t"
        "ds_read_b128 v[84:87], v121 offset:15360\n\t"
        "s_add_u32 s80, s40, 1\n\t"
        ".Lhk22%=:\n\t"
        "s_cbranch_execz .Lhk23%=\n\t"
        "v_mov_b32_e32 v54, 30720\n\t"
        "v_mov_b32_e32 v55, s80\n\t"
        "ds_write_b32 v54, v55\n\t"
        "s_add_u32 s97, s97, 1\n\t"
        "s_cmp_lt_u32 s97, 0x100000\n\t"
        "s_cbranch_scc0 .Lgu%=\n\t"
        ".Lhk23%=:\n\t"
        "s_cbranch_execz .Lhk24%=\n\t"
        "s_mov_b32 s65, s36\n\t"
        "v_mov_b32_e32 v88, 0\n\t"
        "v_mov_b32_e32 v89, 0\n\t"
        "v_mov_b32_e32 v90, 0\n\t"
        "v_mov_b32_e32 v91, 0\n\t"
        "v_mov_b32_e32 v92, 0\n\t"
        ".Lhk24%=:\n\t"
        "s_cbranch_execz .Lhk25%=\n\t"
        "v_mov_b32_e32 v93, 0\n\t"
        "v_mov_b32_e32 v94, 0\n\t"
        "v_mov_b32_e32 v95, 0\n\t"
        "v_mov_b32_e32 v96, 0\n\t"
        "v_mov_b32_e32 v97, 0\n\t"
        "v_mov_b32_e32 v98, 0\n\t"
        ".Lhk25%=:\n\t"
        "s_cbranch_execz .Lhk26%=\n\t"
        "v_mov_b32_e32 v99, 0\n\t"
        "v_mov_b32_e32 v100, 0\n\t"
        "v_mov_b32_e32 v101, 0\n\t"
        "v_mov_b32_e32 v102, 0\n\t"
        "v_mov_b32_e32 v103, 0\n\t"
        "s_waitcnt lgkmcnt(0)\n\t"
        ".Lhk26%=:\n\t"
        "s_cbranch_execz .Lhk27%=\n\t"
        "s_barrier\n\t"
        "s_branch .Lgo%=\n\t"
        ".Lf2%=:\n\t"
        "s_waitcnt vmcnt(0)\n\t"
        "v_mov_b32_e32 v12, 0x3f803f80\n\t"
        "v_mov_b32_e32 v13, 0x3f803f80\n\t"
        "v_mov_b32_e32 v14, 0x3f803f80\n\t"
        ".Lhk27%=:\n\t"
        "s_cbranch_execz .Lhk28%=\n\t"
        "v_mov_b32_e32 v15, 0x3f803f80\n\t"
        "ds_read_b128 v[0:3], v124 offset:12288\n\t"
        "ds_read_b128 v[4:7], v125 offset:12288\n\t"
        "ds_read_b128 v[8:11], v126 offset:12288\n\t"
        "s_waitcnt lgkmcnt(0)\n\t"
        "s_branch .Lr1%=\n\t"
        ".Lfb%=:\n\t"
        ".Lhk28%=:\n\t"
        "s_cbranch_execz .Lhk29%=\n\t"
        "v_max_u32_e32 v54, v88, v90\n\t"
        "v_max3_u32 v54, v54, v89, v91\n\t"
        "v_max3_u32 v54, v54, v92, v94\n\t"
        "v_max3_u32 v54, v54, v93, v95\n\t"
        "v_cmp_eq_u32_e32 vcc, -1, v54\n\t"
        "s_nop 1\n\t"
        ".Lhk29%=:\n\t"
        "s_cbranch_execz .Lhk30%=\n\t"
        "v_mov_b32_e32 v55, v55\n\t"
        "s_nop 0\n\t"
        "s_cbranch_vccz .Lfm%=\n\t"
        "s_add_u32 s80, s40, 1\n\t"
        "v_mov_b32_e32 v54, 30724\n\t"
        "v_mov_b32_e32 v55, s80\n\t"
        ".Lhk30%=:\n\t"
        "s_cbranch_execz .Lhk31%=\n\t"
        "ds_write_b32 v54, v55\n\t"
        "s_sleep %c[ASLP]\n\t"
        "buffer_load_dwordx4 v[88:91], v120, %[srd], s61 offen offset:0 sc1\n\t"
        "buffer_load_dwordx4 v[92:95], v120, %[srd], s61 offen offset:1024 sc1\n\t"
        "s_add_u32 s97, s97, 1\n\t"
        "s_cmp_lt_u32 s97, 0x100000\n\t"
        ".Lhk31%=:\n\t"
        "s_cbranch_execz .Lhk32%=\n\t"
        "s_cbranch_scc0 .Lgu%=\n\t"
        "v_mov_b32_e32 v64, 0\n\t"
        "v_mov_b32_e32 v65, 0\n\t"
        "v_mov_b32_e32 v66, 0\n\t"
        "v_mov_b32_e32 v67, 0\n\t"
        "s_waitcnt vmcnt(0)\n\t"
        ".Lhk32%=:\n\t"
        "s_cbranch_execz .Lhk33%=\n\t"
        "s_nop 0\n\t"
        "v_smfmac_f32_16x16x64_bf16 v[64:67], v[88:91], v[16:23], v122\n\t"
        "v_smfmac_f32_16x16x64_bf16 v[64:67], v[92:95], v[24:31], v122\n\t"
        "s_nop 7\n\t"
        "s_branch .Lrk%=\n\t"
        ".Lfm%=:\n\t"
        "v_max3_f32 v3, v0, v64, v65\n\t"
        ".Lhk33%=:\n\t"
        "s_cbranch_execz .Lhk34%=\n\t"
        "v_max3_f32 v1, v0, v64, v65\n\t"
        "s_nop 1\n\t"
        "v_permlane16_swap_b32_e32 v3, v1\n\t"
        "v_max_f32_e32 v2, v3, v1\n\t"
        "v_max_f32_e32 v3, v3, v1\n\t"
        "s_nop 1\n\t"
        ".Lhk34%=:\n\t"
        "s_cbranch_execz .Lhk35%=\n\t"
        "v_permlane32_swap_b32_e32 v2, v3\n\t"
        "v_max_f32_e32 v2, v2, v3\n\t"
        "v_mul_f32_e32 v2, 0xbe0293ee, v2\n\t"
        "v_fmamk_f32 v6, v0, 0x3e0293ee, v2\n\t"
        "v_fmamk_f32 v66, v64, 0x3e0293ee, v2\n\t"
        "v_exp_f32_e32 v6, v6\n\t"
        ".Lhk35%=:\n\t"
        "s_cbranch_execz .Lhk36%=\n\t"
        "v_fmamk_f32 v67, v65, 0x3e0293ee, v2\n\t"
        "v_exp_f32_e32 v66, v66\n\t"
        "v_exp_f32_e32 v67, v67\n\t"
        "v_mul_f32_e32 v72, v72, v6\n\t"
        "v_pk_mul_f32 v[40:41], v[40:41], v[6:7] op_sel_hi:[1,0]\n\t"
        "v_pk_mul_f32 v[42:43], v[42:43], v[6:7] op_sel_hi:[1,0]\n\t"
        ".Lhk36%=:\n\t"
        "s_cbranch_execz .Lhk37%=\n\t"
        "v_pk_mul_f32 v[44:45], v[44:45], v[6:7] op_sel_hi:[1,0]\n\t"
        "v_pk_mul_f32 v[46:47], v[46:47], v[6:7] op_sel_hi:[1,0]\n\t"
        "v_cvt_pk_bf16_f32 v8, v66, v67\n\t"
        "v_cvt_pk_bf16_f32 v11, v66, v67\n\t"
        "s_nop 1\n\t"
        "s_branch .Lrm%=\n\t"
        ".Lta%=:\n\t"
        ".Lhk37%=:\n\t"
        "s_cbranch_execz .Lhk38%=\n\t"
        "s_waitcnt lgkmcnt(0)\n\t"
        "s_branch .Lxa%=\n\t"
        ".Lgu%=:\n\t"
        "s_mov_b64 exec, -1\n\t"
        "v_mov_b32_e32 v54, %c[ferr]\n\t"
        "v_mov_b32_e32 v55, 1\n\t"
        "buffer_store_dword v55, v54, %[wsd], 0 offen sc1\n\t"
        ".Lhk38%=:\n\t"
        "s_cbranch_execz .Lhk39%=\n\t"
        "s_endpgm\n\t"
        ".Lend%=:\n\t"
        "v_min_u32_e32 v50, -2, v50\n\t"
        "v_min_u32_e32 v51, -2, v51\n\t"
        "s_nop 1\n\t"
        "v_permlane16_swap_b32_e32 v50, v51\n\t"
        "buffer_store_dwordx2 v[50:51], v34, %[wsd], s72 offen offset:2048 sc1\n\t"
        ".Lhk39%=:\n\t"
        "s_cbranch_execz .Lhk40%=\n\t"
        "s_endpgm\n\t"
        ".Lhk40%=:\n\t"
        "s_branch .Lhr%="
        : [nA] "+s"(nA), [nS] "+s"(nS), [nT] "+s"(nT), [nW] "+s"(nW), [nC] "+s"(nC), [b0] "+s"(b0), [pob] "+s"(pob)
        : [srd] "s"(rhd), [wsd] "s"(wsd), [w] "s"(w), [tag] "s"(tag), [kx] "s"(kx), [vx] "s"(vx), [kf] "s"(kf), [kvo] "s"(kvo), [cal] "s"(cal), [sq] "s"(sq), [sk] "s"(sk), [m0b] "s"(m0b),
          [pra] "s"(pr_a), [ferr] "i"(F_ERR), [iD1] "i"(D1), [iD2] "i"(D2), [LC] "i"(LC0), [LA] "i"(LA0), [LC24] "i"(LC0 + 24), [LA24] "i"(LA0 + 24), [E1] "i"(2 * (D1 - LC0)), [E2] "i"(2 * (D2 - LA0)),
          [iPEN] "i"(PEN), [iPEN2] "i"(PEN2), [iPENM] "i"(PEN + 2 * PEN2), [iPEN0] "i"(PEN0), [iTOL] "i"(TOL), [iCT0] "i"(CT0), [iX0C] "i"(X0C), [iX0A] "i"(X0A), [iPHM] "i"(PH_M),
          [iFT] "i"(-3), [iFN] "i"(FINE_N), [AUP] "i"(A_UP), [ASLP] "i"(A_SLP), [LDEC] "i"(L_DEC), [UPSH] "i"(UP_SH), [TRUP] "i"(TR_UP), [TRDN] "i"(TR_DN), [HSLP] "i"(H_SLP), [iPMIN] "i"(PMIN), [VSLP] "i"(V_SLP), [RSLP] "i"(R_SLP), [iVD] "i"(V_DEL), [iXR] "i"(XR), [iBM] "i"(BM_), [iBS] "i"(BS_), [VASL] "i"(VA_SLP), [CSLP] "i"(C_SLP), [DNSH] "i"(DN_SH), [NX0] "i"(NEARX), [NX1] "i"(8192 - NEARX), [iCS1] "i"(CS1), [iCS2] "i"(CS2), [NY0] "i"(NEARY), [NY1] "i"(16384 - NEARY),
          "{v[120:127]}"(pc), "{v[112:113]}"(pd), "{v96}"(dv), "{v[98:105]}"(pa)
        : "memory", "vcc", "scc", "m0", "s36", "s37", "s38", "s39", "s40", "s41", "s42", "s43", "s44", "s45", "s46", "s47", "s48", "s49", "s50", "s51", "s52", "s53", "s54", "s55", "s56", "s57", "s58", "s59", "s60", "s61", "s62", "s63", "s64", "s65", "s66", "s67", "s68", "s69", "s70", "s71", "s72", "s73", "s74", "s75", "s76", "s77", "s78", "s79", "s80", "s81", "s82", "s83", "s84", "s85", "s86", "s87", "s88", "s89", "s90", "s91", "s92", "s93", "s94", "s95", "s96", "s97", "s98", "s99");
    } else {
        const int cb = i32;
        const v4u sv = mk_srd(p.Wk, 1024 * 4096 * 2), so = mk_srd(p.Wo, 1024 * 4096 * 2);
        const unsigned key = KEYM & ((cb & 1) * 2 + G), tu = cb >> 2, m0b = w * 32768, f = (g & 1) | ((c >> 1) << 1);
        const unsigned t0 = m0b + (16 * (g >> 1) + 4 * (g & 1) + (c & 1) + 8 * (c >> 1)) * 128 + (f << 5) + (n16 & 3) * 8, t1 = m0b + (16 * g + 4 * (c >> 1) + (c & 1)) * 64 + ((c >> 1) << 5) + (n16 & 3) * 8;
        v8u pa;
        pa[0] = (64 * w + (l >> 3)) * 8192 + vi * 128 + (((l & 7) ^ ((l >> 5) << 1)) << 4); pa[1] = pa[0] ^ 64u;
        pa[2] = (1024 * w + (l >> 2)) * 2048 + cb * 64 + (((l & 3) ^ ((g & 1) << 1)) << 4);
#pragma unroll
        for (int q = 0; q < 4; ++q) pa[3 + q] = t0 ^ (((w + q) & 3) << 5);
        pa[7] = t1;
        const v2u pb = {t1 ^ 32u, (unsigned)((w * 1024 + cb * 32) * 2048 + l * 1024) + (((3u - (so.x >> 7)) & 7u) << 7)};
        const v16u pc = {(unsigned)tid * 16, (n16 & 2) ? 0xEEEEu : 0x4444u, (unsigned)((vi & 1) * 1024 + w * 256 + ((n16 & 1) + 2 * (n16 >> 3) + 4 * g) * 16 + (n16 & 6) * 2), (unsigned)(4096 + tid * 16),
                   w < 3 ? (unsigned)((cb >> 4) * 32768 + ((cb >> 1) & 7) * 1024 + ((cb & 1) * 2 + (l >> 5)) * 256 + G * 128 + (l & 31) * 4) : OOB,
                   tid < 3 ? (unsigned)(l * 1024 + (32 + vi) * 8) : OOB, 0u, (unsigned)(L_V + 8192 + w * 512 + (g & 1) * 256 + (g >> 1) * 64 + n16 * 4),
                   (unsigned)(L_V + (((l >> 2) & 1) + ((l >> 3) & 2)) * 128 + (l >> 5) * 64 + (l & 8) * 4 + (l & 3) * 8), (l & 1) ? 0x03020706u : 0x05040100u,
                   (unsigned)(L_V + w * 2048 + l * 8), (unsigned)(L_V + ((w + 1) & 3) * 2048 + 512 + l * 8), (unsigned)(L_V + ((w + 2) & 3) * 2048 + 1024 + l * 8), (unsigned)(L_V + ((w + 3) & 3) * 2048 + 1536 + l * 8), 0u, 0u};
        if (tid < 2) ((lu32*)(size_t)(L_V + L_FL))[tid] = ~0u;
    asm volatile(
        "s_mov_b32 s82, 3\n\t"
        ".Ld0%=:\n\t"
        "s_xor_b32 s80, s82, %[key]\n\t"
        "s_mul_i32 s81, s80, 131072\n\t"
        "s_lshl_b32 s80, s80, 11\n\t"
        "s_add_u32 s80, s80, %[m0b]\n\t"
        "s_add_u32 m0, s80, 0\n\t"
        "s_add_u32 s81, s81, 0\n\t"
        "buffer_load_dwordx4 v96, %[sv], s81 offen lds\n\t"
        "s_add_u32 m0, m0, 0x400\n\t"
        "s_add_u32 s81, s81, 65536\n\t"
        "buffer_load_dwordx4 v97, %[sv], s81 offen lds\n\t"
        "s_sub_u32 s82, s82, 1\n\t"
        "s_cbranch_scc0 .Ld0%=\n\t"
        "s_mov_b32 s82, 3\n\t"
        ".Ld1%=:\n\t"
        "s_xor_b32 s80, s82, %[key]\n\t"
        "s_mul_i32 s81, s80, 131072\n\t"
        "s_lshl_b32 s80, s80, 11\n\t"
        "s_add_u32 s80, s80, %[m0b]\n\t"
        "s_add_u32 m0, s80, 8192\n\t"
        "s_add_u32 s81, s81, 2097152\n\t"
        "buffer_load_dwordx4 v96, %[sv], s81 offen lds\n\t"
        "s_add_u32 m0, m0, 0x400\n\t"
        "s_add_u32 s81, s81, 65536\n\t"
        "buffer_load_dwordx4 v97, %[sv], s81 offen lds\n\t"
        "s_sub_u32 s82, s82, 1\n\t"
        "s_cbranch_scc0 .Ld1%=\n\t"
        "s_cmp_eq_u32 %[tu], 0\n\t"
        "s_cselect_b32 s81, 0, 0x7fff0000\n\t"
        "buffer_load_dword v110, v105, %[so], s81 offen\n\t"
        "s_mov_b32 s82, 3\n\t"
        ".Ld2%=:\n\t"
        "s_xor_b32 s80, s82, %[key]\n\t"
        "s_mul_i32 s81, s80, 131072\n\t"
        "s_lshl_b32 s80, s80, 11\n\t"
        "s_add_u32 s80, s80, %[m0b]\n\t"
        "s_add_u32 m0, s80, 16384\n\t"
        "s_add_u32 s81, s81, 4194304\n\t"
        "buffer_load_dwordx4 v96, %[sv], s81 offen lds\n\t"
        "s_add_u32 m0, m0, 0x400\n\t"
        "s_add_u32 s81, s81, 65536\n\t"
        "buffer_load_dwordx4 v97, %[sv], s81 offen lds\n\t"
        "s_sub_u32 s82, s82, 1\n\t"
        "s_cbranch_scc0 .Ld2%=\n\t"
        "s_cmp_eq_u32 %[tu], 1\n\t"
        "s_cselect_b32 s81, 0, 0x7fff0000\n\t"
        "buffer_load_dword v110, v105, %[so], s81 offen\n\t"
        "s_mov_b32 s82, 3\n\t"
        ".Ld3%=:\n\t"
        "s_xor_b32 s80, s82, %[key]\n\t"
        "s_mul_i32 s81, s80, 131072\n\t"
        "s_lshl_b32 s80, s80, 11\n\t"
        "s_add_u32 s80, s80, %[m0b]\n\t"
        "s_add_u32 m0, s80, 24576\n\t"
        "s_add_u32 s81, s81, 6291456\n\t"
        "buffer_load_dwordx4 v96, %[sv], s81 offen lds\n\t"
        "s_add_u32 m0, m0, 0x400\n\t"
        "s_add_u32 s81, s81, 65536\n\t"
        "buffer_load_dwordx4 v97, %[sv], s81 offen lds\n\t"
        "s_sub_u32 s82, s82, 1\n\t"
        "s_cbranch_scc0 .Ld3%=\n\t"
        "s_waitcnt vmcnt(26)\n\t"
        "ds_read_b64_tr_b16 v[128:129], v99 offset:0\n\t"
        "ds_read_b64_tr_b16 v[130:131], v99 offset:256\n\t"
        "ds_read_b64_tr_b16 v[132:133], v99 offset:4096\n\t"
        "ds_read_b64_tr_b16 v[134:135], v99 offset:4352\n\t"
        "ds_read_b64_tr_b16 v[136:137], v100 offset:0\n\t"
        "ds_read_b64_tr_b16 v[138:139], v100 offset:256\n\t"
        "ds_read_b64_tr_b16 v[140:141], v100 offset:4096\n\t"
        "ds_read_b64_tr_b16 v[142:143], v100 offset:4352\n\t"
        "ds_read_b64_tr_b16 v[144:145], v101 offset:0\n\t"
        "ds_read_b64_tr_b16 v[146:147], v101 offset:256\n\t"
        "ds_read_b64_tr_b16 v[148:149], v101 offset:4096\n\t"
        "ds_read_b64_tr_b16 v[150:151], v101 offset:4352\n\t"
        "ds_read_b64_tr_b16 v[152:153], v102 offset:0\n\t"
        "ds_read_b64_tr_b16 v[154:155], v102 offset:256\n\t"
        "ds_read_b64_tr_b16 v[156:157], v102 offset:4096\n\t"
        "ds_read_b64_tr_b16 v[158:159], v102 offset:4352\n\t"
        "s_waitcnt lgkmcnt(0)\n\t"
        "s_cmp_eq_u32 %[tu], 2\n\t"
        "s_cselect_b32 s81, 0, 0x7fff0000\n\t"
        "buffer_load_dword v110, v105, %[so], s81 offen\n\t"
        "s_mov_b32 s82, 3\n\t"
        ".Ld4%=:\n\t"
        "s_xor_b32 s80, s82, %[key]\n\t"
        "s_mul_i32 s81, s80, 65536\n\t"
        "s_lshl_b32 s80, s80, 11\n\t"
        "s_add_u32 s80, s80, %[m0b]\n\t"
        "s_add_u32 m0, s80, 0\n\t"
        "s_add_u32 s81, s81, 0\n\t"
        "buffer_load_dwordx4 v98, %[so], s81 offen lds\n\t"
        "s_add_u32 m0, m0, 0x400\n\t"
        "s_add_u32 s81, s81, 32768\n\t"
        "buffer_load_dwordx4 v98, %[so], s81 offen lds\n\t"
        "s_sub_u32 s82, s82, 1\n\t"
        "s_cbranch_scc0 .Ld4%=\n\t"
        "s_waitcnt vmcnt(27)\n\t"
        "ds_read_b64_tr_b16 v[160:161], v99 offset:8192\n\t"
        "ds_read_b64_tr_b16 v[162:163], v99 offset:8448\n\t"
        "ds_read_b64_tr_b16 v[164:165], v99 offset:12288\n\t"
        "ds_read_b64_tr_b16 v[166:167], v99 offset:12544\n\t"
        "ds_read_b64_tr_b16 v[168:169], v100 offset:8192\n\t"
        "ds_read_b64_tr_b16 v[170:171], v100 offset:8448\n\t"
        "ds_read_b64_tr_b16 v[172:173], v100 offset:12288\n\t"
        "ds_read_b64_tr_b16 v[174:175], v100 offset:12544\n\t"
        "ds_read_b64_tr_b16 v[176:177], v101 offset:8192\n\t"
        "ds_read_b64_tr_b16 v[178:179], v101 offset:8448\n\t"
        "ds_read_b64_tr_b16 v[180:181], v101 offset:12288\n\t"
        "ds_read_b64_tr_b16 v[182:183], v101 offset:12544\n\t"
        "ds_read_b64_tr_b16 v[184:185], v102 offset:8192\n\t"
        "ds_read_b64_tr_b16 v[186:187], v102 offset:8448\n\t"
        "ds_read_b64_tr_b16 v[188:189], v102 offset:12288\n\t"
        "ds_read_b64_tr_b16 v[190:191], v102 offset:12544\n\t"
        "s_waitcnt lgkmcnt(0)\n\t"
        "s_cmp_eq_u32 %[tu], 3\n\t"
        "s_cselect_b32 s81, 0, 0x7fff0000\n\t"
        "buffer_load_dword v110, v105, %[so], s81 offen\n\t"
        "s_mov_b32 s82, 3\n\t"
        ".Ld5%=:\n\t"
        "s_xor_b32 s80, s82, %[key]\n\t"
        "s_mul_i32 s81, s80, 65536\n\t"
        "s_lshl_b32 s80, s80, 11\n\t"
        "s_add_u32 s80, s80, %[m0b]\n\t"
        "s_add_u32 m0, s80, 8192\n\t"
        "s_add_u32 s81, s81, 262144\n\t"
        "buffer_load_dwordx4 v98, %[so], s81 offen lds\n\t"
        "s_add_u32 m0, m0, 0x400\n\t"
        "s_add_u32 s81, s81, 32768\n\t"
        "buffer_load_dwordx4 v98, %[so], s81 offen lds\n\t"
        "s_sub_u32 s82, s82, 1\n\t"
        "s_cbranch_scc0 .Ld5%=\n\t"
        "s_waitcnt vmcnt(27)\n\t"
        "ds_read_b64_tr_b16 v[192:193], v99 offset:16384\n\t"
        "ds_read_b64_tr_b16 v[194:195], v99 offset:16640\n\t"
        "ds_read_b64_tr_b16 v[196:197], v99 offset:20480\n\t"
        "ds_read_b64_tr_b16 v[198:199], v99 offset:20736\n\t"
        "ds_read_b64_tr_b16 v[200:201], v100 offset:16384\n\t"
        "ds_read_b64_tr_b16 v[202:203], v100 offset:16640\n\t"
        "ds_read_b64_tr_b16 v[204:205], v100 offset:20480\n\t"
        "ds_read_b64_tr_b16 v[206:207], v100 offset:20736\n\t"
        "ds_read_b64_tr_b16 v[208:209], v101 offset:16384\n\t"
        "ds_read_b64_tr_b16 v[210:211], v101 offset:16640\n\t"
        "ds_read_b64_tr_b16 v[212:213], v101 offset:20480\n\t"
        "ds_read_b64_tr_b16 v[214:215], v101 offset:20736\n\t"
        "ds_read_b64_tr_b16 v[216:217], v102 offset:16384\n\t"
        "ds_read_b64_tr_b16 v[218:219], v102 offset:16640\n\t"
        "ds_read_b64_tr_b16 v[220:221], v102 offset:20480\n\t"
        "ds_read_b64_tr_b16 v[222:223], v102 offset:20736\n\t"
        "s_waitcnt lgkmcnt(0)\n\t"
        "s_cmp_eq_u32 %[tu], 4\n\t"
        "s_cselect_b32 s81, 0, 0x7fff0000\n\t"
        "buffer_load_dword v110, v105, %[so], s81 offen\n\t"
        "s_mov_b32 s82, 3\n\t"
        ".Ld6%=:\n\t"
        "s_xor_b32 s80, s82, %[key]\n\t"
        "s_mul_i32 s81, s80, 65536\n\t"
        "s_lshl_b32 s80, s80, 11\n\t"
        "s_add_u32 s80, s80, %[m0b]\n\t"
        "s_add_u32 m0, s80, 16384\n\t"
        "s_add_u32 s81, s81, 524288\n\t"
        "buffer_load_dwordx4 v98, %[so], s81 offen lds\n\t"
        "s_add_u32 m0, m0, 0x400\n\t"
        "s_add_u32 s81, s81, 32768\n\t"
        "buffer_load_dwordx4 v98, %[so], s81 offen lds\n\t"
        "s_sub_u32 s82, s82, 1\n\t"
        "s_cbranch_scc0 .Ld6%=\n\t"
        "s_waitcnt vmcnt(27)\n\t"
        "ds_read_b64_tr_b16 v[224:225], v99 offset:24576\n\t"
        "ds_read_b64_tr_b16 v[226:227], v99 offset:24832\n\t"
        "ds_read_b64_tr_b16 v[228:229], v99 offset:28672\n\t"
        "ds_read_b64_tr_b16 v[230:231], v99 offset:28928\n\t"
        "ds_read_b64_tr_b16 v[232:233], v100 offset:24576\n\t"
        "ds_read_b64_tr_b16 v[234:235], v100 offset:24832\n\t"
        "ds_read_b64_tr_b16 v[236:237], v100 offset:28672\n\t"
        "ds_read_b64_tr_b16 v[238:239], v100 offset:28928\n\t"
        "ds_read_b64_tr_b16 v[240:241], v101 offset:24576\n\t"
        "ds_read_b64_tr_b16 v[242:243], v101 offset:24832\n\t"
        "ds_read_b64_tr_b16 v[244:245], v101 offset:28672\n\t"
        "ds_read_b64_tr_b16 v[246:247], v101 offset:28928\n\t"
        "ds_read_b64_tr_b16 v[248:249], v102 offset:24576\n\t"
        "ds_read_b64_tr_b16 v[250:251], v102 offset:24832\n\t"
        "ds_read_b64_tr_b16 v[252:253], v102 offset:28672\n\t"
        "ds_read_b64_tr_b16 v[254:255], v102 offset:28928\n\t"
        "s_waitcnt lgkmcnt(0)\n\t"
        "s_cmp_eq_u32 %[tu], 5\n\t"
        "s_cselect_b32 s81, 0, 0x7fff0000\n\t"
        "buffer_load_dword v110, v105, %[so], s81 offen\n\t"
        "s_mov_b32 s82, 3\n\t"
        ".Ld7%=:\n\t"
        "s_xor_b32 s80, s82, %[key]\n\t"
        "s_mul_i32 s81, s80, 65536\n\t"
        "s_lshl_b32 s80, s80, 11\n\t"
        "s_add_u32 s80, s80, %[m0b]\n\t"
        "s_add_u32 m0, s80, 24576\n\t"
        "s_add_u32 s81, s81, 786432\n\t"
        "buffer_load_dwordx4 v98, %[so], s81 offen lds\n\t"
        "s_add_u32 m0, m0, 0x400\n\t"
        "s_add_u32 s81, s81, 32768\n\t"
        "buffer_load_dwordx4 v98, %[so], s81 offen lds\n\t"
        "s_sub_u32 s82, s82, 1\n\t"
        "s_cbranch_scc0 .Ld7%=\n\t"
        "s_mov_b64 exec, 0\n\t"
        "s_branch .Lhk0%=\n\t"
        ".Lhr%=:\n\t"
        "s_mov_b64 exec, -1\n\t"
        "s_waitcnt vmcnt(27)\n\t"
        "ds_read_b64_tr_b16 a[0:1], v103 offset:0\n\t"
        "ds_read_b64_tr_b16 a[2:3], v103 offset:128\n\t"
        "ds_read_b64_tr_b16 a[4:5], v103 offset:4096\n\t"
        "ds_read_b64_tr_b16 a[6:7], v103 offset:4224\n\t"
        "ds_read_b64_tr_b16 a[8:9], v104 offset:0\n\t"
        "ds_read_b64_tr_b16 a[10:11], v104 offset:128\n\t"
        "ds_read_b64_tr_b16 a[12:13], v104 offset:4096\n\t"
        "ds_read_b64_tr_b16 a[14:15], v104 offset:4224\n\t"
        "ds_read_b64_tr_b16 a[16:17], v103 offset:512\n\t"
        "ds_read_b64_tr_b16 a[18:19], v103 offset:640\n\t"
        "ds_read_b64_tr_b16 a[20:21], v103 offset:4608\n\t"
        "ds_read_b64_tr_b16 a[22:23], v103 offset:4736\n\t"
        "ds_read_b64_tr_b16 a[24:25], v104 offset:512\n\t"
        "ds_read_b64_tr_b16 a[26:27], v104 offset:640\n\t"
        "ds_read_b64_tr_b16 a[28:29], v104 offset:4608\n\t"
        "ds_read_b64_tr_b16 a[30:31], v104 offset:4736\n\t"
        "s_waitcnt lgkmcnt(0)\n\t"
        "s_cmp_eq_u32 %[tu], 6\n\t"
        "s_cselect_b32 s81, 0, 0x7fff0000\n\t"
        "buffer_load_dword v110, v105, %[so], s81 offen\n\t"
        "s_mov_b32 s82, 3\n\t"
        ".Ld8%=:\n\t"
        "s_xor_b32 s80, s82, %[key]\n\t"
        "s_mul_i32 s81, s80, 65536\n\t"
        "s_lshl_b32 s80, s80, 11\n\t"
        "s_add_u32 s80, s80, %[m0b]\n\t"
        "s_add_u32 m0, s80, 0\n\t"
        "s_add_u32 s81, s81, 1048576\n\t"
        "buffer_load_dwordx4 v98, %[so], s81 offen lds\n\t"
        "s_add_u32 m0, m0, 0x400\n\t"
        "s_add_u32 s81, s81, 32768\n\t"
        "buffer_load_dwordx4 v98, %[so], s81 offen lds\n\t"
        "s_sub_u32 s82, s82, 1\n\t"
        "s_cbranch_scc0 .Ld8%=\n\t"
        "s_waitcnt vmcnt(27)\n\t"
        "ds_read_b64_tr_b16 a[32:33], v103 offset:8192\n\t"
        "ds_read_b64_tr_b16 a[34:35], v103 offset:8320\n\t"
        "ds_read_b64_tr_b16 a[36:37], v103 offset:12288\n\t"
        "ds_read_b64_tr_b16 a[38:39], v103 offset:12416\n\t"
        "ds_read_b64_tr_b16 a[40:41], v104 offset:8192\n\t"
        "ds_read_b64_tr_b16 a[42:43], v104 offset:8320\n\t"
        "ds_read_b64_tr_b16 a[44:45], v104 offset:12288\n\t"
        "ds_read_b64_tr_b16 a[46:47], v104 offset:12416\n\t"
        "ds_read_b64_tr_b16 a[48:49], v103 offset:8704\n\t"
        "ds_read_b64_tr_b16 a[50:51], v103 offset:8832\n\t"
        "ds_read_b64_tr_b16 a[52:53], v103 offset:12800\n\t"
        "ds_read_b64_tr_b16 a[54:55], v103 offset:12928\n\t"
        "ds_read_b64_tr_b16 a[56:57], v104 offset:8704\n\t"
        "ds_read_b64_tr_b16 a[58:59], v104 offset:8832\n\t"
        "ds_read_b64_tr_b16 a[60:61], v104 offset:12800\n\t"
        "ds_read_b64_tr_b16 a[62:63], v104 offset:12928\n\t"
        "s_waitcnt lgkmcnt(0)\n\t"
        "s_cmp_eq_u32 %[tu], 7\n\t"
        "s_cselect_b32 s81, 0, 0x7fff0000\n\t"
        "buffer_load_dword v110, v105, %[so], s81 offen\n\t"
        "s_mov_b32 s82, 3\n\t"
        ".Ld9%=:\n\t"
        "s_xor_b32 s80, s82, %[key]\n\t"
        "s_mul_i32 s81, s80, 65536\n\t"
        "s_lshl_b32 s80, s80, 11\n\t"
        "s_add_u32 s80, s80, %[m0b]\n\t"
        "s_add_u32 m0, s80, 8192\n\t"
        "s_add_u32 s81, s81, 1310720\n\t"
        "buffer_load_dwordx4 v98, %[so], s81 offen lds\n\t"
        "s_add_u32 m0, m0, 0x400\n\t"
        "s_add_u32 s81, s81, 32768\n\t"
        "buffer_load_dwordx4 v98, %[so], s81 offen lds\n\t"
        "s_sub_u32 s82, s82, 1\n\t"
        "s_cbranch_scc0 .Ld9%=\n\t"
        "s_waitcnt vmcnt(27)\n\t"
        "ds_read_b64_tr_b16 a[64:65], v103 offset:16384\n\t"
        "ds_read_b64_tr_b16 a[66:67], v103 offset:16512\n\t"
        "ds_read_b64_tr_b16 a[68:69], v103 offset:20480\n\t"
        "ds_read_b64_tr_b16 a[70:71], v103 offset:20608\n\t"
        "ds_read_b64_tr_b16 a[72:73], v104 offset:16384\n\t"
        "ds_read_b64_tr_b16 a[74:75], v104 offset:16512\n\t"
        "ds_read_b64_tr_b16 a[76:77], v104 offset:20480\n\t"
        "ds_read_b64_tr_b16 a[78:79], v104 offset:20608\n\t"
        "ds_read_b64_tr_b16 a[80:81], v103 offset:16896\n\t"
        "ds_read_b64_tr_b16 a[82:83], v103 offset:17024\n\t"
        "ds_read_b64_tr_b16 a[84:85], v103 offset:20992\n\t"
        "ds_read_b64_tr_b16 a[86:87], v103 offset:21120\n\t"
        "ds_read_b64_tr_b16 a[88:89], v104 offset:16896\n\t"
        "ds_read_b64_tr_b16 a[90:91], v104 offset:17024\n\t"
        "ds_read_b64_tr_b16 a[92:93], v104 offset:20992\n\t"
        "ds_read_b64_tr_b16 a[94:95], v104 offset:21120\n\t"
        "buffer_load_dwordx4 v[0:3], v112, %[wsd], %[cal] offen sc1\n\t"
        "s_waitcnt lgkmcnt(0)\n\t"
        "s_mov_b32 s82, 3\n\t"
        ".Ld10%=:\n\t"
        "s_xor_b32 s80, s82, %[key]\n\t"
        "s_mul_i32 s81, s80, 65536\n\t"
        "s_lshl_b32 s80, s80, 11\n\t"
        "s_add_u32 s80, s80, %[m0b]\n\t"
        "s_add_u32 m0, s80, 16384\n\t"
        "s_add_u32 s81, s81, 1572864\n\t"
        "buffer_load_dwordx4 v98, %[so], s81 offen lds\n\t"
        "s_add_u32 m0, m0, 0x400\n\t"
        "s_add_u32 s81, s81, 32768\n\t"
        "buffer_load_dwordx4 v98, %[so], s81 offen lds\n\t"
        "s_sub_u32 s82, s82, 1\n\t"
        "s_cbranch_scc0 .Ld10%=\n\t"
        "s_waitcnt vmcnt(27)\n\t"
        "ds_read_b64_tr_b16 a[96:97], v103 offset:24576\n\t"
        "ds_read_b64_tr_b16 a[98:99], v103 offset:24704\n\t"
        "ds_read_b64_tr_b16 a[100:101], v103 offset:28672\n\t"
        "ds_read_b64_tr_b16 a[102:103], v103 offset:28800\n\t"
        "ds_read_b64_tr_b16 a[104:105], v104 offset:24576\n\t"
        "ds_read_b64_tr_b16 a[106:107], v104 offset:24704\n\t"
        "ds_read_b64_tr_b16 a[108:109], v104 offset:28672\n\t"
        "ds_read_b64_tr_b16 a[110:111], v104 offset:28800\n\t"
        "ds_read_b64_tr_b16 a[112:113], v103 offset:25088\n\t"
        "ds_read_b64_tr_b16 a[114:115], v103 offset:25216\n\t"
        "ds_read_b64_tr_b16 a[116:117], v103 offset:29184\n\t"
        "ds_read_b64_tr_b16 a[118:119], v103 offset:29312\n\t"
        "ds_read_b64_tr_b16 a[120:121], v104 offset:25088\n\t"
        "ds_read_b64_tr_b16 a[122:123], v104 offset:25216\n\t"
        "ds_read_b64_tr_b16 a[124:125], v104 offset:29184\n\t"
        "ds_read_b64_tr_b16 a[126:127], v104 offset:29312\n\t"
        "s_waitcnt lgkmcnt(0)\n\t"
        "s_mov_b32 s82, 3\n\t"
        ".Ld11%=:\n\t"
        "s_xor_b32 s80, s82, %[key]\n\t"
        "s_mul_i32 s81, s80, 65536\n\t"
        "s_lshl_b32 s80, s80, 11\n\t"
        "s_add_u32 s80, s80, %[m0b]\n\t"
        "s_add_u32 m0, s80, 24576\n\t"
        "s_add_u32 s81, s81, 1835008\n\t"
        "buffer_load_dwordx4 v98, %[so], s81 offen lds\n\t"
        "s_add_u32 m0, m0, 0x400\n\t"
        "s_add_u32 s81, s81, 32768\n\t"
        "buffer_load_dwordx4 v98, %[so], s81 offen lds\n\t"
        "s_sub_u32 s82, s82, 1\n\t"
        "s_cbranch_scc0 .Ld11%=\n\t"
        "s_waitcnt vmcnt(26)\n\t"
        "ds_read_b64_tr_b16 a[128:129], v103 offset:0\n\t"
        "ds_read_b64_tr_b16 a[130:131], v103 offset:128\n\t"
        "ds_read_b64_tr_b16 a[132:133], v103 offset:4096\n\t"
        "ds_read_b64_tr_b16 a[134:135], v103 offset:4224\n\t"
        "ds_read_b64_tr_b16 a[136:137], v104 offset:0\n\t"
        "ds_read_b64_tr_b16 a[138:139], v104 offset:128\n\t"
        "ds_read_b64_tr_b16 a[140:141], v104 offset:4096\n\t"
        "ds_read_b64_tr_b16 a[142:143], v104 offset:4224\n\t"
        "ds_read_b64_tr_b16 a[144:145], v103 offset:512\n\t"
        "ds_read_b64_tr_b16 a[146:147], v103 offset:640\n\t"
        "ds_read_b64_tr_b16 a[148:149], v103 offset:4608\n\t"
        "ds_read_b64_tr_b16 a[150:151], v103 offset:4736\n\t"
        "ds_read_b64_tr_b16 a[152:153], v104 offset:512\n\t"
        "ds_read_b64_tr_b16 a[154:155], v104 offset:640\n\t"
        "ds_read_b64_tr_b16 a[156:157], v104 offset:4608\n\t"
        "ds_read_b64_tr_b16 a[158:159], v104 offset:4736\n\t"
        "s_waitcnt lgkmcnt(0)\n\t"
        "s_waitcnt vmcnt(17)\n\t"
        "ds_read_b64_tr_b16 a[160:161], v103 offset:8192\n\t"
        "ds_read_b64_tr_b16 a[162:163], v103 offset:8320\n\t"
        "ds_read_b64_tr_b16 a[164:165], v103 offset:12288\n\t"
        "ds_read_b64_tr_b16 a[166:167], v103 offset:12416\n\t"
        "ds_read_b64_tr_b16 a[168:169], v104 offset:8192\n\t"
        "ds_read_b64_tr_b16 a[170:171], v104 offset:8320\n\t"
        "ds_read_b64_tr_b16 a[172:173], v104 offset:12288\n\t"
        "ds_read_b64_tr_b16 a[174:175], v104 offset:12416\n\t"
        "ds_read_b64_tr_b16 a[176:177], v103 offset:8704\n\t"
        "ds_read_b64_tr_b16 a[178:179], v103 offset:8832\n\t"
        "ds_read_b64_tr_b16 a[180:181], v103 offset:12800\n\t"
        "ds_read_b64_tr_b16 a[182:183], v103 offset:12928\n\t"
        "ds_read_b64_tr_b16 a[184:185], v104 offset:8704\n\t"
        "ds_read_b64_tr_b16 a[186:187], v104 offset:8832\n\t"
        "ds_read_b64_tr_b16 a[188:189], v104 offset:12800\n\t"
        "ds_read_b64_tr_b16 a[190:191], v104 offset:12928\n\t"
        "s_waitcnt lgkmcnt(0)\n\t"
        "s_waitcnt vmcnt(8)\n\t"
        "ds_read_b64_tr_b16 a[192:193], v103 offset:16384\n\t"
        "ds_read_b64_tr_b16 a[194:195], v103 offset:16512\n\t"
        "ds_read_b64_tr_b16 a[196:197], v103 offset:20480\n\t"
        "ds_read_b64_tr_b16 a[198:199], v103 offset:20608\n\t"
        "ds_read_b64_tr_b16 a[200:201], v104 offset:16384\n\t"
        "ds_read_b64_tr_b16 a[202:203], v104 offset:16512\n\t"
        "ds_read_b64_tr_b16 a[204:205], v104 offset:20480\n\t"
        "ds_read_b64_tr_b16 a[206:207], v104 offset:20608\n\t"
        "ds_read_b64_tr_b16 a[208:209], v103 offset:16896\n\t"
        "ds_read_b64_tr_b16 a[210:211], v103 offset:17024\n\t"
        "ds_read_b64_tr_b16 a[212:213], v103 offset:20992\n\t"
        "ds_read_b64_tr_b16 a[214:215], v103 offset:21120\n\t"
        "ds_read_b64_tr_b16 a[216:217], v104 offset:16896\n\t"
        "ds_read_b64_tr_b16 a[218:219], v104 offset:17024\n\t"
        "ds_read_b64_tr_b16 a[220:221], v104 offset:20992\n\t"
        "ds_read_b64_tr_b16 a[222:223], v104 offset:21120\n\t"
        "s_mov_b32 s97, 0\n\t"
        ".Lg0%=:\n\t"
        "v_cmp_ne_u32_e32 vcc, %[tag], v0\n\t"
        "v_cmp_ne_u32_e64 s[82:83], %[tag], v2\n\t"
        "s_nop 1\n\t"
        "s_or_b64 vcc, vcc, s[82:83]\n\t"
        "s_cbranch_scc0 .Lg1%=\n\t"
        "s_sleep 4\n\t"
        "buffer_load_dwordx4 v[0:3], v112, %[wsd], %[cal] offen sc1\n\t"
        "s_add_u32 s97, s97, 1\n\t"
        "s_cmp_lt_u32 s97, 0x100000\n\t"
        "s_cbranch_scc0 .Lgu%=\n\t"
        "s_waitcnt vmcnt(0)\n\t"
        "s_branch .Lg0%=\n\t"
        ".Lg1%=:\n\t"
        "s_mov_b32 s84, 0x55555555\n\t"
        "s_mov_b32 s85, s84\n\t"
        "v_and_b32_e32 v0, 1, v1\n\t"
        "v_and_b32_e32 v2, 1, v3\n\t"
        "v_cmp_ne_u32_e32 vcc, 0, v0\n\t"
        "v_cmp_ne_u32_e64 s[82:83], 0, v2\n\t"
        "s_nop 0\n\t"
        "s_and_b64 s[82:83], s[82:83], s[84:85]\n\t"
        "s_bcnt1_i32_b64 s87, s[82:83]\n\t"
        "s_and_b64 s[82:83], vcc, s[84:85]\n\t"
        "s_bcnt1_i32_b64 s86, s[82:83]\n\t"
        "s_andn2_b64 s[82:83], vcc, s[84:85]\n\t"
        "s_bcnt1_i32_b64 s88, s[82:83]\n\t"
        "v_and_b32_e32 v0, 2, v1\n\t"
        "v_and_b32_e32 v2, 2, v3\n\t"
        "v_cmp_ne_u32_e32 vcc, 0, v0\n\t"
        "v_cmp_ne_u32_e64 s[82:83], 0, v2\n\t"
        "s_nop 0\n\t"
        "s_and_b64 s[82:83], s[82:83], s[84:85]\n\t"
        "s_bcnt1_i32_b64 s90, s[82:83]\n\t"
        "s_and_b64 s[82:83], vcc, s[84:85]\n\t"
        "s_bcnt1_i32_b64 s89, s[82:83]\n\t"
        "s_andn2_b64 s[82:83], vcc, s[84:85]\n\t"
        "s_bcnt1_i32_b64 s91, s[82:83]\n\t"
        "s_add_u32 s80, s86, s87\n\t"
        "s_add_u32 s80, s80, s88\n\t"
        "s_cmp_ge_u32 s80, 48\n\t"
        "s_cselect_b32 s80, 8, 24\n\t"
        "s_add_u32 s82, s89, s91\n\t"
        "s_sub_u32 s82, s82, s90\n\t"
        "s_cmp_gt_i32 s82, 16\n\t"
        "s_cselect_b32 s82, 6, 25\n\t"
        "s_cselect_b32 s83, 25, 6\n\t"
        "s_cmp_gt_u32 s86, s80\n\t"
        "s_cselect_b32 s86, %c[NX1], %c[NX0]\n\t"
        "s_cmp_gt_u32 s89, s82\n\t"
        "s_cselect_b32 s81, %c[NY1], %c[NY0]\n\t"
        "s_or_b32 s86, s86, s81\n\t"
        "s_cmp_gt_u32 s87, s80\n\t"
        "s_cselect_b32 s87, %c[NX1], %c[NX0]\n\t"
        "s_cmp_gt_u32 s90, s83\n\t"
        "s_cselect_b32 s81, %c[NY1], %c[NY0]\n\t"
        "s_or_b32 s87, s87, s81\n\t"
        "s_cmp_gt_u32 s88, s80\n\t"
        "s_cselect_b32 s88, %c[NX1], %c[NX0]\n\t"
        "s_cmp_gt_u32 s91, s82\n\t"
        "s_cselect_b32 s81, %c[NY1], %c[NY0]\n\t"
        "s_or_b32 s88, s88, s81\n\t"
        "s_xor_b32 %[nA], %[nA], s86\n\t"
        "s_xor_b32 %[nW], %[nW], s86\n\t"
        "s_xor_b32 %[nC], %[nC], s87\n\t"
        "s_xor_b32 %[nS], %[nS], s88\n\t"
        "s_xor_b32 %[nT], %[nT], s88\n\t"
        "s_xor_b32 %[b0], %[b0], s88\n\t"
        "s_cmp_eq_u32 %[w], 1\n\t"
        "s_cselect_b32 s80, s87, s86\n\t"
        "s_cmp_eq_u32 %[w], 2\n\t"
        "s_cselect_b32 s80, s88, s80\n\t"
        "s_xor_b32 %[pob], %[pob], s80\n\t"
        "s_lshl_b32 s80, %[w], 10\n\t"
        "s_sub_u32 %[nS], %[nS], s80\n\t"
        "s_sub_u32 %[pob], %[pob], s80\n\t"
        "s_mov_b32 s36, 0xf0000000\n\t"
        "s_mov_b32 s38, 0x41a00000\n\t"
        "s_mov_b32 s40, 0\n\t"
        "s_mov_b32 s37, 0\n\t"
        "s_brev_b32 s54, 1\n\t"
        "s_mov_b32 s46, %c[iD1]\n\t"
        "s_mov_b32 s45, %c[iD1]\n\t"
        "s_mov_b32 s47, %c[iD2]\n\t"
        "s_mov_b32 s48, 1\n\t"
        "s_mov_b32 s50, %c[LC]\n\t"
        "s_mov_b32 s51, %c[LA]\n\t"
        "s_mov_b32 s52, %c[E1]\n\t"
        "s_mov_b32 s53, %c[E2]\n\t"
        "s_mov_b32 s43, 0\n\t"
        "s_mov_b32 s61, %[b0]\n\t"
        "s_add_u32 s62, %[b0], 0x40000\n\t"
        "s_mov_b32 s75, 162816\n\t"
        "s_waitcnt lgkmcnt(0)\n\t"
        "s_waitcnt vmcnt(0)\n\t"
        "ds_read_b64_tr_b16 a[224:225], v103 offset:24576\n\t"
        "ds_read_b64_tr_b16 a[226:227], v103 offset:24704\n\t"
        "ds_read_b64_tr_b16 a[228:229], v103 offset:28672\n\t"
        "ds_read_b64_tr_b16 a[230:231], v103 offset:28800\n\t"
        "ds_read_b64_tr_b16 a[232:233], v104 offset:24576\n\t"
        "ds_read_b64_tr_b16 a[234:235], v104 offset:24704\n\t"
        "ds_read_b64_tr_b16 a[236:237], v104 offset:28672\n\t"
        "ds_read_b64_tr_b16 a[238:239], v104 offset:28800\n\t"
        "ds_read_b64_tr_b16 a[240:241], v103 offset:25088\n\t"
        "ds_read_b64_tr_b16 a[242:243], v103 offset:25216\n\t"
        "ds_read_b64_tr_b16 a[244:245], v103 offset:29184\n\t"
        "ds_read_b64_tr_b16 a[246:247], v103 offset:29312\n\t"
        "ds_read_b64_tr_b16 a[248:249], v104 offset:25088\n\t"
        "ds_read_b64_tr_b16 a[250:251], v104 offset:25216\n\t"
        "ds_read_b64_tr_b16 a[252:253], v104 offset:29184\n\t"
        "ds_read_b64_tr_b16 a[254:255], v104 offset:29312\n\t"
        "s_waitcnt lgkmcnt(0)\n\t"
        "rec_v_code:\n\t"
        "s_mov_b32 s88, 0\n\t"
        "s_mov_b32 s89, 0\n\t"
        "s_mov_b32 s90, 0\n\t"
        "s_and_b32 s80, s40, 3\n\t"
        "s_lshl_b32 s80, s80, 17\n\t"
        "s_add_u32 s57, s80, %[nA]\n\t"
        "s_add_u32 s58, s57, 0x1000\n\t"
        "s_add_u32 s59, s57, 0x8000\n\t"
        "s_add_u32 s60, s57, 0x9000\n\t"
        "v_mov_b32_e32 v92, -1\n\t"
        "v_mov_b32_e32 v93, -1\n\t"
        "v_mov_b32_e32 v94, -1\n\t"
        "v_mov_b32_e32 v95, -1\n\t"
        "v_mov_b32_e32 v48, 0\n\t"
        "v_mov_b32_e32 v49, 0\n\t"
        "v_mov_b32_e32 v50, 0\n\t"
        "v_mov_b32_e32 v51, 0\n\t"
        "v_mov_b32_e32 v52, 0\n\t"
        "v_mov_b32_e32 v53, 0\n\t"
        "v_mov_b32_e32 v54, 0\n\t"
        "v_mov_b32_e32 v55, 0\n\t"
        "v_mov_b32_e32 v56, 0\n\t"
        "v_mov_b32_e32 v57, 0\n\t"
        "v_mov_b32_e32 v58, 0\n\t"
        "v_mov_b32_e32 v59, 0\n\t"
        "v_mov_b32_e32 v60, 0\n\t"
        "v_mov_b32_e32 v61, 0\n\t"
        "v_mov_b32_e32 v62, 0\n\t"
        "v_mov_b32_e32 v63, 0\n\t"
        "s_barrier\n\t"
        "s_memrealtime s[76:77]\n\t"
        "s_mov_b32 s79, %[nT]\n\t"
        "s_add_u32 s61, %[b0], 0x40000\n\t"
        "s_mov_b32 s62, %[b0]\n\t"
        "s_sub_u32 s75, s75, 16\n\t"
        "s_waitcnt lgkmcnt(0)\n\t"
        "s_mov_b32 s41, s76\n\t"
        "s_mov_b32 s69, s76\n\t"
        ".p2align 6\n\t"
        ".Lloop%=:\n\t"
        "s_mov_b32 s97, 0\n\t"
        "s_movk_i32 s96, 0x1000\n\t"
        ".Lwa%=:\n\t"
        "s_memrealtime s[98:99]\n\t"
        "s_addk_i32 s96, -1\n\t"
        "s_cmp_lt_i32 s96, 0\n\t"
        "s_cbranch_scc1 .Lta%=\n\t"
        "s_waitcnt lgkmcnt(0)\n\t"
        ".Lva%=:\n\t"
        "s_sub_i32 s98, s98, s69\n\t"
        "s_cmp_lt_i32 s98, %c[iFT]\n\t"
        "s_cbranch_scc1 .Lwa%=\n\t"
        "s_cmp_ge_i32 s98, 0\n\t"
        "s_cbranch_scc1 .Lxa%=\n\t"
        ".Lqa%=:\n\t"
        "s_nop %c[iFN]\n\t"
        "s_addk_i32 s98, 1\n\t"
        "s_cmp_lt_i32 s98, 0\n\t"
        "s_cbranch_scc1 .Lqa%=\n\t"
        ".Lxa%=:\n\t"
        "buffer_load_dwordx4 v[0:3], v112, %[srd], s57 offen sc1\n\t"
        "s_nop 7\n\t"
        "buffer_load_dwordx4 v[4:7], v112, %[srd], s58 offen sc1\n\t"
        "s_nop 7\n\t"
        "buffer_load_dwordx4 v[8:11], v112, %[srd], s59 offen sc1\n\t"
        "s_nop 7\n\t"
        "buffer_load_dwordx4 v[12:15], v112, %[srd], s60 offen sc1\n\t"
        "v_mov_b32_e32 v80, 161792\n\t"
        "ds_read_b64 v[80:81], v80\n\t"
        "s_waitcnt lgkmcnt(0)\n\t"
        "v_readfirstlane_b32 s80, v80\n\t"
        "v_readfirstlane_b32 s81, v81\n\t"
        "s_cmp_eq_u32 s80, s40\n\t"
        "s_cselect_b32 s84, 1, 0\n\t"
        "s_cmp_eq_u32 s81, s40\n\t"
        "s_cselect_b32 s81, 4, 0\n\t"
        "s_or_b32 s84, s84, s81\n\t"
        "v_mov_b32_e32 v106, s76\n\t"
        "v_mov_b32_e32 v107, s84\n\t"
        "buffer_store_dwordx2 v[106:107], v117, %[srd], s79 offen sc1\n\t"
        "s_and_b32 s80, s40, 3\n\t"
        "s_lshl_b32 s80, s80, 17\n\t"
        "s_add_u32 s63, s80, %[nC]\n\t"
        "s_add_u32 s66, s80, %[nS]\n\t"
        "s_add_u32 s81, s80, 0x20000\n\t"
        "s_and_b32 s81, s81, 0x60000\n\t"
        "s_add_u32 s67, s81, %[nW]\n\t"
        "s_add_u32 s79, s81, %[nT]\n\t"
        "s_cmp_eq_u32 %[w], 2\n\t"
        "s_cselect_b32 s81, s80, s81\n\t"
        "s_xor_b32 s81, s81, 0x40000\n\t"
        "s_add_u32 s68, s81, %[pob]\n\t"
        "s_lshl_b32 s72, s40, 17\n\t"
        "s_add_u32 s72, s72, %[kvo]\n\t"
        "s_mov_b32 s80, s61\n\t"
        "s_mov_b32 s61, s62\n\t"
        "s_mov_b32 s62, s80\n\t"
        ".Lgo%=:\n\t"
        "s_waitcnt vmcnt(4)\n\t"
        "v_smfmac_f32_16x16x64_bf16 v[52:55], v[0:3], v[136:143], v113\n\t"
        "v_smfmac_f32_16x16x64_bf16 v[56:59], v[0:3], v[144:151], v113\n\t"
        "v_smfmac_f32_16x16x64_bf16 v[60:63], v[0:3], v[152:159], v113\n\t"
        "v_smfmac_f32_16x16x64_bf16 v[48:51], v[0:3], v[128:135], v113\n\t"
        "s_waitcnt vmcnt(3)\n\t"
        "v_smfmac_f32_16x16x64_bf16 v[52:55], v[4:7], v[168:175], v113\n\t"
        "v_smfmac_f32_16x16x64_bf16 v[56:59], v[4:7], v[176:183], v113\n\t"
        "v_smfmac_f32_16x16x64_bf16 v[60:63], v[4:7], v[184:191], v113\n\t"
        "v_smfmac_f32_16x16x64_bf16 v[48:51], v[4:7], v[160:167], v113\n\t"
        "s_waitcnt vmcnt(2)\n\t"
        "v_smfmac_f32_16x16x64_bf16 v[52:55], v[8:11], v[200:207], v113\n\t"
        "v_smfmac_f32_16x16x64_bf16 v[56:59], v[8:11], v[208:215], v113\n\t"
        "v_smfmac_f32_16x16x64_bf16 v[60:63], v[8:11], v[216:223], v113\n\t"
        "v_smfmac_f32_16x16x64_bf16 v[48:51], v[8:11], v[192:199], v113\n\t"
        "s_waitcnt vmcnt(1)\n\t"
        "v_smfmac_f32_16x16x64_bf16 v[52:55], v[12:15], v[232:239], v113\n\t"
        "s_nop 2\n\t"
        "v_smfmac_f32_16x16x64_bf16 v[56:59], v[12:15], v[240:247], v113\n\t"
        "s_nop 2\n\t"
        "v_smfmac_f32_16x16x64_bf16 v[60:63], v[12:15], v[248:255], v113\n\t"
        "v_smfmac_f32_16x16x64_bf16 v[48:51], v[12:15], v[224:231], v113\n\t"
        "v_add_f32_e32 v98, v52, v54\n\t"
        "ds_write_b32 v123, v98 offset:0\n\t"
        "v_add_f32_e32 v99, v53, v55\n\t"
        "ds_write_b32 v123, v99 offset:4\n\t"
        "v_add_f32_e32 v100, v56, v58\n\t"
        "ds_write_b32 v124, v100 offset:0\n\t"
        "v_add_f32_e32 v101, v57, v59\n\t"
        "ds_write_b32 v124, v101 offset:4\n\t"
        "v_add_f32_e32 v102, v60, v62\n\t"
        "ds_write_b32 v125, v102 offset:0\n\t"
        "v_add_f32_e32 v103, v61, v63\n\t"
        "ds_write_b32 v125, v103 offset:4\n\t"
        "v_pk_add_f32 v[96:97], v[48:49], v[50:51]\n\t"
        "v_cmp_u_f32_e32 vcc, v96, v97\n\t"
        "s_nop 2\n\t"
        "v_mov_b32_e32 v104, v104\n\t"
        "s_cbranch_vccnz .Lfa%=\n\t"
        ".Lra%=:\n\t"
        "v_mov_b32_e32 v104, v104\n\t"
        "s_nop 2\n\t"
        "v_mov_b32_e32 v104, v104\n\t"
        "s_waitcnt lgkmcnt(0)\n\t"
        "s_barrier\n\t"
        "v_mov_b32_e32 v104, v104\n\t"
        "ds_read_b64 v[98:99], v122 offset:512\n\t"
        "v_mov_b32_e32 v104, v104\n\t"
        "ds_read_b64 v[100:101], v122 offset:1024\n\t"
        "v_mov_b32_e32 v104, v104\n\t"
        "ds_read_b64 v[102:103], v122 offset:1536\n\t"
        "v_mov_b32_e32 v104, v104\n\t"
        "s_nop 2\n\t"
        "v_mov_b32_e32 v104, v104\n\t"
        "s_waitcnt lgkmcnt(2)\n\t"
        "v_pk_add_f32 v[96:97], v[96:97], v[98:99]\n\t"
        "s_waitcnt lgkmcnt(0)\n\t"
        "v_pk_add_f32 v[100:101], v[100:101], v[102:103]\n\t"
        "v_pk_add_f32 v[96:97], v[96:97], v[100:101]\n\t"
        "v_cvt_pk_bf16_f32 v80, v96, v97\n\t"
        "s_nop 1\n\t"
        "v_mov_b32_dpp v81, v80 quad_perm:[1,0,3,2] row_mask:0xf bank_mask:0xf\n\t"
        "v_perm_b32 v80, v81, v80, v121\n\t"
        "v_min_u32_e32 v80, -2, v80\n\t"
        "buffer_store_dword v80, v114, %[srd], s61 offen sc1\n\t"
        "s_cmp_eq_u32 s40, 63\n\t"
        "s_cbranch_scc1 .Lend%=\n\t"
        "v_mov_b32_e32 v64, 0\n\t"
        "v_mov_b32_e32 v65, 0\n\t"
        "v_mov_b32_e32 v66, 0\n\t"
        "v_mov_b32_e32 v67, 0\n\t"
        "v_mov_b32_e32 v68, 0\n\t"
        "v_mov_b32_e32 v69, 0\n\t"
        "v_mov_b32_e32 v70, 0\n\t"
        "v_mov_b32_e32 v71, 0\n\t"
        "v_mov_b32_e32 v72, 0\n\t"
        "v_mov_b32_e32 v73, 0\n\t"
        "v_mov_b32_e32 v74, 0\n\t"
        "v_mov_b32_e32 v75, 0\n\t"
        "v_mov_b32_e32 v76, 0\n\t"
        "v_mov_b32_e32 v77, 0\n\t"
        "v_mov_b32_e32 v78, 0\n\t"
        "v_mov_b32_e32 v79, 0\n\t"
        "s_xor_b32 s64, s63, %c[iCS1]\n\t"
        "s_xor_b32 s65, s63, %c[iCS2]\n\t"
        "s_xor_b32 s73, s64, %c[iCS2]\n\t"
        "s_sleep %c[VSLP]\n\t"
        "buffer_load_dwordx4 v[88:91], v112, %[srd], s66 offen sc1\n\t"
        "s_cmp_eq_u32 %[w], 0\n\t"
        "s_cselect_b32 exec_hi, 0, -1\n\t"
        "buffer_store_dwordx4 v[92:95], v112, %[srd], s68 offen sc1\n\t"
        "s_mov_b32 exec_hi, -1\n\t"
        "s_waitcnt lgkmcnt(0)\n\t"
        "v_mov_b32_e32 v48, 0\n\t"
        "v_mov_b32_e32 v49, 0\n\t"
        "v_mov_b32_e32 v50, 0\n\t"
        "v_mov_b32_e32 v51, 0\n\t"
        "v_mov_b32_e32 v52, 0\n\t"
        "v_mov_b32_e32 v53, 0\n\t"
        "v_mov_b32_e32 v54, 0\n\t"
        "v_mov_b32_e32 v55, 0\n\t"
        "v_mov_b32_e32 v56, 0\n\t"
        "v_mov_b32_e32 v57, 0\n\t"
        "v_mov_b32_e32 v58, 0\n\t"
        "v_mov_b32_e32 v59, 0\n\t"
        "v_mov_b32_e32 v60, 0\n\t"
        "v_mov_b32_e32 v61, 0\n\t"
        "v_mov_b32_e32 v62, 0\n\t"
        "v_mov_b32_e32 v63, 0\n\t"
        "s_add_u32 s81, s40, 1\n\t"
        "s_and_b32 s80, s81, 3\n\t"
        "s_lshl_b32 s80, s80, 17\n\t"
        "s_add_u32 s57, s80, %[nA]\n\t"
        "s_add_u32 s58, s57, 0x1000\n\t"
        "s_add_u32 s59, s57, 0x8000\n\t"
        "s_add_u32 s60, s57, 0x9000\n\t"
        "s_mov_b32 s80, s88\n\t"
        "s_add_i32 s81, s92, s50\n\t"
        "s_sub_i32 s81, s81, %c[iBM]\n\t"
        "s_cmp_le_i32 s81, s44\n\t"
        "s_cselect_b32 s80, s80, 0\n\t"
        "s_cmp_lt_i32 s50, %c[LC24]\n\t"
        "s_cselect_b32 s80, s80, 0\n\t"
        "s_mul_i32 s80, s80, %c[iBS]\n\t"
        "s_add_u32 s50, s80, s50\n\t"
        "s_sub_i32 s50, s50, s89\n\t"
        "s_max_i32 s50, s50, %c[LC]\n\t"
        "s_lshl_b32 s80, s92, 1\n\t"
        "s_sub_i32 s81, s80, s52\n\t"
        "s_ashr_i32 s82, s81, %c[UPSH]\n\t"
        "s_max_i32 s82, s82, %c[TRUP]\n\t"
        "s_add_i32 s82, s82, s52\n\t"
        "s_min_i32 s82, s82, s80\n\t"
        "s_ashr_i32 s83, s81, %c[DNSH]\n\t"
        "s_min_i32 s83, s83, -%c[TRDN]\n\t"
        "s_add_i32 s83, s83, s52\n\t"
        "s_max_i32 s83, s83, s80\n\t"
        "s_cmp_gt_i32 s81, 0\n\t"
        "s_cselect_b32 s82, s82, s83\n\t"
        "s_cmp_lg_u32 s90, 0\n\t"
        "s_cselect_b32 s52, s82, s52\n\t"
        "s_add_i32 s80, s52, 1\n\t"
        "s_ashr_i32 s80, s80, 1\n\t"
        "s_add_i32 s45, s80, s50\n\t"
        "s_waitcnt vmcnt(1)\n\t"
        "s_mov_b32 s97, 0\n\t"
        ".Lsr%=:\n\t"
        "s_mov_b32 exec_hi, 0xffff\n\t"
        "v_subrev_u32_e32 v4, s41, v88\n\t"
        "v_subrev_u32_e32 v5, s41, v90\n\t"
        "v_max_i32_e32 v4, v4, v5\n\t"
        "v_max_u32_e32 v5, v89, v91\n\t"
        "v_cmp_eq_u32_e32 vcc, -1, v5\n\t"
        "v_max_i32_dpp v4, v4, v4 quad_perm:[1,0,3,2] row_mask:0xf bank_mask:0xf\n\t"
        "v_or_b32_e32 v6, v89, v91\n\t"
        "s_nop 0\n\t"
        "v_max_i32_dpp v4, v4, v4 quad_perm:[2,3,0,1] row_mask:0xf bank_mask:0xf\n\t"
        "s_nop 1\n\t"
        "v_max_i32_dpp v4, v4, v4 row_ror:4 row_mask:0xf bank_mask:0xf\n\t"
        "s_nop 1\n\t"
        "v_max_i32_dpp v4, v4, v4 row_ror:8 row_mask:0xf bank_mask:0xf\n\t"
        "s_cbranch_vccnz .Lsf%=\n\t"
        "v_readlane_b32 s85, v4, 0\n\t"
        "v_readlane_b32 s81, v4, 16\n\t"
        "v_readlane_b32 s56, v4, 32\n\t"
        "v_and_b32_e32 v5, 1, v6\n\t"
        "v_cmp_ne_u32_e64 s[86:87], 0, v5\n\t"
        "v_and_b32_e32 v5, 2, v6\n\t"
        "v_cmp_ne_u32_e64 s[88:89], 0, v5\n\t"
        "v_and_b32_e32 v5, 4, v6\n\t"
        "v_cmp_ne_u32_e64 s[90:91], 0, v5\n\t"
        "s_max_i32 s56, s56, s81\n\t"
        "s_add_u32 s92, s85, s41\n\t"
        "s_sub_u32 s92, s92, s43\n\t"
        "s_add_i32 s93, s56, s47\n\t"
        "s_mov_b32 s44, s46\n\t"
        "s_mov_b32 s46, s45\n\t"
        "s_mov_b32 s94, s48\n\t"
        "s_mov_b32 s43, s41\n\t"
        "s_cmp_gt_i32 s56, s54\n\t"
        "s_cbranch_scc1 .Lmv%=\n\t"
        "s_mov_b32 s37, 0\n\t"
        "s_mov_b32 s48, 0\n\t"
        "s_mov_b32 s95, 0\n\t"
        ".Lsd%=:\n\t"
        "s_mov_b32 exec_hi, -1\n\t"
        "s_add_u32 s42, s43, s46\n\t"
        "s_mov_b32 s97, 0\n\t"
        "s_movk_i32 s96, 0x1000\n\t"
        ".Lwc%=:\n\t"
        "s_memrealtime s[98:99]\n\t"
        "s_addk_i32 s96, -1\n\t"
        "s_cmp_lt_i32 s96, 0\n\t"
        "s_cbranch_scc1 .Ltc%=\n\t"
        "s_waitcnt lgkmcnt(0)\n\t"
        "s_sub_i32 s98, s98, s42\n\t"
        "s_cmp_lt_i32 s98, %c[iFT]\n\t"
        "s_cbranch_scc1 .Lwc%=\n\t"
        "s_cmp_ge_i32 s98, 0\n\t"
        "s_cbranch_scc1 .Lxc%=\n\t"
        ".Lqc%=:\n\t"
        "s_nop %c[iFN]\n\t"
        "s_addk_i32 s98, 1\n\t"
        "s_cmp_lt_i32 s98, 0\n\t"
        "s_cbranch_scc1 .Lqc%=\n\t"
        ".Lxc%=:\n\t"
        "buffer_load_dwordx4 v[16:19], v112, %[srd], s63 offen sc1\n\t"
        "s_cmp_ge_u32 s40, %c[iCT0]\n\t"
        "s_cselect_b32 s85, 1, 0\n\t"
        "s_cmp_lg_u64 s[86:87], 0\n\t"
        "s_cselect_b32 s86, s85, 0\n\t"
        "s_cmp_lg_u64 s[88:89], 0\n\t"
        "s_cselect_b32 s87, s85, 0\n\t"
        "s_cmp_lg_u64 s[90:91], 0\n\t"
        "s_cselect_b32 s88, s85, 0\n\t"
        "s_or_b32 s80, s94, s86\n\t"
        "s_or_b32 s80, s80, s87\n\t"
        "buffer_load_dwordx4 v[20:23], v115, %[srd], s63 offen sc1\n\t"
        "s_andn2_b32 s90, s85, s80\n\t"
        "s_cmp_le_i32 s92, s44\n\t"
        "s_cselect_b32 s80, s85, 0\n\t"
        "s_andn2_b32 s91, s80, s88\n\t"
        "s_and_b32 s80, s40, %c[LDEC]\n\t"
        "s_cselect_b32 s89, 0, 1\n\t"
        "s_andn2_b32 s80, s86, s94\n\t"
        "s_cmp_lt_i32 s51, %c[LA24]\n\t"
        "s_cselect_b32 s80, s80, 0\n\t"
        "s_mul_i32 s80, s80, %c[iBS]\n\t"
        "buffer_load_dwordx4 v[24:27], v112, %[srd], s64 offen sc1\n\t"
        "s_add_u32 s51, s80, s51\n\t"
        "s_sub_i32 s51, s51, s89\n\t"
        "s_max_i32 s51, s51, %c[LA]\n\t"
        "s_lshl_b32 s80, s93, 1\n\t"
        "s_sub_i32 s81, s80, s53\n\t"
        "s_ashr_i32 s82, s81, %c[UPSH]\n\t"
        "s_max_i32 s82, s82, %c[TRUP]\n\t"
        "s_add_i32 s82, s82, s53\n\t"
        "s_min_i32 s82, s82, s80\n\t"
        "s_ashr_i32 s83, s81, %c[DNSH]\n\t"
        "buffer_load_dwordx4 v[28:31], v115, %[srd], s64 offen sc1\n\t"
        "s_min_i32 s83, s83, -%c[TRDN]\n\t"
        "s_add_i32 s83, s83, s53\n\t"
        "s_max_i32 s83, s83, s80\n\t"
        "s_cmp_gt_i32 s81, 0\n\t"
        "s_cselect_b32 s82, s82, s83\n\t"
        "s_cmp_lg_u32 s91, 0\n\t"
        "s_cselect_b32 s53, s82, s53\n\t"
        "s_add_i32 s80, s53, 1\n\t"
        "s_ashr_i32 s80, s80, 1\n\t"
        "s_add_i32 s47, s80, s51\n\t"
        "buffer_load_dwordx4 v[32:35], v112, %[srd], s65 offen sc1\n\t"
        "s_add_u32 s81, s47, %c[AUP]\n\t"
        "s_min_i32 s80, s95, s81\n\t"
        "s_max_i32 s47, s47, s80\n\t"
        "s_add_u32 s41, s42, s47\n\t"
        "s_cmp_eq_u32 s40, 0\n\t"
        "s_cselect_b32 s51, %c[LA], s51\n\t"
        "s_nop 0\n\t"
        "s_nop 0\n\t"
        "s_nop 0\n\t"
        "s_nop 0\n\t"
        "buffer_load_dwordx4 v[36:39], v115, %[srd], s65 offen sc1\n\t"
        "s_nop 0\n\t"
        "s_nop 0\n\t"
        "s_nop 0\n\t"
        "s_nop 0\n\t"
        "s_nop 0\n\t"
        "s_nop 0\n\t"
        "s_nop 0\n\t"
        "s_nop 0\n\t"
        "s_nop 0\n\t"
        "s_nop 0\n\t"
        "buffer_load_dwordx4 v[40:43], v112, %[srd], s73 offen sc1\n\t"
        "s_nop 0\n\t"
        "s_nop 0\n\t"
        "s_nop 0\n\t"
        "s_nop 0\n\t"
        "s_nop 0\n\t"
        "s_nop 0\n\t"
        "s_nop 0\n\t"
        "s_nop 0\n\t"
        "s_nop 0\n\t"
        "s_nop 0\n\t"
        "buffer_load_dwordx4 v[44:47], v115, %[srd], s73 offen sc1\n\t"
        ".p2align 6\n\t"
        ".Lrc%=:\n\t"
        "s_waitcnt vmcnt(7)\n\t"
        "v_smfmac_f32_16x16x64_bf16 v[64:67], v[16:19], a[0:7], v113\n\t"
        "v_smfmac_f32_16x16x64_bf16 v[68:71], v[16:19], a[8:15], v113\n\t"
        "v_smfmac_f32_16x16x64_bf16 v[72:75], v[16:19], a[16:23], v113\n\t"
        "v_smfmac_f32_16x16x64_bf16 v[76:79], v[16:19], a[24:31], v113\n\t"
        "s_waitcnt vmcnt(6)\n\t"
        "v_smfmac_f32_16x16x64_bf16 v[64:67], v[20:23], a[32:39], v113\n\t"
        "v_smfmac_f32_16x16x64_bf16 v[68:71], v[20:23], a[40:47], v113\n\t"
        "v_smfmac_f32_16x16x64_bf16 v[72:75], v[20:23], a[48:55], v113\n\t"
        "v_smfmac_f32_16x16x64_bf16 v[76:79], v[20:23], a[56:63], v113\n\t"
        "s_waitcnt vmcnt(5)\n\t"
        "v_smfmac_f32_16x16x64_bf16 v[64:67], v[24:27], a[64:71], v113\n\t"
        "v_smfmac_f32_16x16x64_bf16 v[68:71], v[24:27], a[72:79], v113\n\t"
        "v_smfmac_f32_16x16x64_bf16 v[72:75], v[24:27], a[80:87], v113\n\t"
        "v_smfmac_f32_16x16x64_bf16 v[76:79], v[24:27], a[88:95], v113\n\t"
        "s_waitcnt vmcnt(4)\n\t"
        "v_smfmac_f32_16x16x64_bf16 v[64:67], v[28:31], a[96:103], v113\n\t"
        "v_smfmac_f32_16x16x64_bf16 v[68:71], v[28:31], a[104:111], v113\n\t"
        "v_smfmac_f32_16x16x64_bf16 v[72:75], v[28:31], a[112:119], v113\n\t"
        "v_smfmac_f32_16x16x64_bf16 v[76:79], v[28:31], a[120:127], v113\n\t"
        "s_waitcnt vmcnt(3)\n\t"
        "v_smfmac_f32_16x16x64_bf16 v[64:67], v[32:35], a[128:135], v113\n\t"
        "v_smfmac_f32_16x16x64_bf16 v[68:71], v[32:35], a[136:143], v113\n\t"
        "v_smfmac_f32_16x16x64_bf16 v[72:75], v[32:35], a[144:151], v113\n\t"
        "v_smfmac_f32_16x16x64_bf16 v[76:79], v[32:35], a[152:159], v113\n\t"
        "s_waitcnt vmcnt(2)\n\t"
        "v_smfmac_f32_16x16x64_bf16 v[64:67], v[36:39], a[160:167], v113\n\t"
        "v_smfmac_f32_16x16x64_bf16 v[68:71], v[36:39], a[168:175], v113\n\t"
        "v_smfmac_f32_16x16x64_bf16 v[72:75], v[36:39], a[176:183], v113\n\t"
        "v_smfmac_f32_16x16x64_bf16 v[76:79], v[36:39], a[184:191], v113\n\t"
        "s_waitcnt vmcnt(1)\n\t"
        "v_smfmac_f32_16x16x64_bf16 v[64:67], v[40:43], a[192:199], v113\n\t"
        "v_smfmac_f32_16x16x64_bf16 v[68:71], v[40:43], a[200:207], v113\n\t"
        "v_smfmac_f32_16x16x64_bf16 v[72:75], v[40:43], a[208:215], v113\n\t"
        "v_smfmac_f32_16x16x64_bf16 v[76:79], v[40:43], a[216:223], v113\n\t"
        "s_waitcnt vmcnt(0)\n\t"
        "v_smfmac_f32_16x16x64_bf16 v[64:67], v[44:47], a[224:231], v113\n\t"
        "s_nop 2\n\t"
        "v_smfmac_f32_16x16x64_bf16 v[68:71], v[44:47], a[232:239], v113\n\t"
        "s_cmp_gt_u32 %[w], 2\n\t"
        "s_nop 1\n\t"
        "v_smfmac_f32_16x16x64_bf16 v[72:75], v[44:47], a[240:247], v113\n\t"
        "v_add_f32_e32 v80, v64, v66\n\t"
        "s_nop 0\n\t"
        "v_smfmac_f32_16x16x64_bf16 v[76:79], v[44:47], a[248:255], v113\n\t"
        "v_add_f32_e32 v81, v65, v67\n\t"
        "v_add_f32_e32 v82, v68, v70\n\t"
        "v_add_f32_e32 v83, v69, v71\n\t"
        "s_mov_b32 exec_lo, 0\n\t"
        "s_nop 0\n\t"
        "v_add_f32_e32 v80, v72, v74\n\t"
        "v_add_f32_e32 v81, v73, v75\n\t"
        "s_nop 0\n\t"
        "v_pk_add_f32 v[82:83], v[76:77], v[78:79]\n\t"
        "s_mov_b32 exec_lo, -1\n\t"
        "s_nop 0\n\t"
        "v_permlane32_swap_b32_e32 v80, v82\n\t"
        "v_add_f32_e32 v80, v80, v82\n\t"
        "ds_write_b32 v119, v80\n\t"
        "v_permlane32_swap_b32_e32 v81, v83\n\t"
        "v_add_f32_e32 v81, v81, v83\n\t"
        "ds_write_b32 v119, v81 offset:128\n\t"
        "v_cmp_u_f32_e32 vcc, v80, v81\n\t"
        "s_nop 2\n\t"
        "v_mov_b32_e32 v104, v104\n\t"
        "s_cbranch_vccnz .Lfc%=\n\t"
        ".Lrd%=:\n\t"
        "v_mov_b32_e32 v104, v104\n\t"
        "s_cbranch_scc1 .Lno%=\n\t"
        "v_mov_b32_e32 v104, v104\n\t"
        "s_nop 2\n\t"
        "v_mov_b32_e32 v104, v104\n\t"
        "s_waitcnt lgkmcnt(0)\n\t"
        "s_barrier\n\t"
        "v_mov_b32_e32 v104, v104\n\t"
        "ds_read_b64 v[0:1], v120 offset:8192\n\t"
        "ds_read_b64 v[2:3], v120 offset:8704\n\t"
        "ds_read_b64 v[4:5], v120 offset:9216\n\t"
        "ds_read_b64 v[6:7], v120 offset:9728\n\t"
        "v_mov_b32_e32 v104, v104\n\t"
        "s_nop 2\n\t"
        "v_mov_b32_e32 v104, v104\n\t"
        "s_nop 2\n\t"
        "v_mov_b32_e32 v104, v104\n\t"
        "s_waitcnt lgkmcnt(2)\n\t"
        "v_pk_add_f32 v[0:1], v[0:1], v[2:3]\n\t"
        "s_waitcnt lgkmcnt(0)\n\t"
        "v_pk_add_f32 v[4:5], v[4:5], v[6:7]\n\t"
        "v_pk_add_f32 v[0:1], v[0:1], v[4:5]\n\t"
        "v_cvt_pk_bf16_f32 v0, v0, v1\n\t"
        "v_min_u32_e32 v0, -2, v0\n\t"
        "buffer_store_dword v0, v116, %[srd], s67 offen sc1\n\t"
        ".Lnp%=:\n\t"
        "s_memrealtime s[76:77]\n\t"
        "s_add_u32 s40, s40, 1\n\t"
        "s_waitcnt lgkmcnt(0)\n\t"
        "s_add_u32 s81, s76, %c[iVD]\n\t"
        "s_sub_u32 s80, s76, s41\n\t"
        "s_cmp_gt_i32 s80, %c[iTOL]\n\t"
        "s_cselect_b32 s69, s81, s41\n\t"
        "s_mov_b32 s97, 0\n\t"
        "s_movk_i32 s96, 0x1000\n\t"
        "s_mov_b32 s98, s76\n\t"
        "s_branch .Lva%=\n\t"
        ".Lmv%=:\n\t"
        ".Lhk0%=:\n\t"
        "s_cbranch_execz .Lhk1%=\n\t"
        "s_mov_b32 s48, 1\n\t"
        "s_cmp_eq_u32 s54, %c[iTOL]\n\t"
        "s_cbranch_scc1 .Lm1%=\n\t"
        "s_max_i32 s80, s85, s56\n\t"
        "s_add_u32 s43, s41, s80\n\t"
        "s_add_u32 s43, s43, %c[iPEN0]\n\t"
        ".Lhk1%=:\n\t"
        "s_cbranch_execz .Lhk2%=\n\t"
        "s_mov_b32 s54, %c[iTOL]\n\t"
        "s_add_u32 s46, s46, %c[iX0C]\n\t"
        "s_mov_b32 s95, 0\n\t"
        "s_add_u32 s51, s51, %c[iX0A]\n\t"
        "v_and_b32_e32 v4, 3, v88\n\t"
        "v_and_b32_e32 v5, 3, v90\n\t"
        ".Lhk2%=:\n\t"
        "s_cbranch_execz .Lhk3%=\n\t"
        "v_lshlrev_b32_e64 v4, v4, 1\n\t"
        "v_lshlrev_b32_e64 v5, v5, 1\n\t"
        "v_or_b32_e32 v4, v4, v5\n\t"
        "s_mov_b32 s80, 0\n\t"
        "v_and_b32_e32 v5, 1, v4\n\t"
        "v_cmp_ne_u32_e32 vcc, 0, v5\n\t"
        ".Lhk3%=:\n\t"
        "s_cbranch_execz .Lhk4%=\n\t"
        "s_nop 1\n\t"
        "s_cmp_lg_u64 vcc, 0\n\t"
        "s_cselect_b32 s81, 2, 0\n\t"
        "s_or_b32 s80, s80, s81\n\t"
        "v_and_b32_e32 v5, 2, v4\n\t"
        "v_cmp_ne_u32_e32 vcc, 0, v5\n\t"
        ".Lhk4%=:\n\t"
        "s_cbranch_execz .Lhk5%=\n\t"
        "s_nop 1\n\t"
        "s_cmp_lg_u64 vcc, 0\n\t"
        "s_cselect_b32 s81, 4, 0\n\t"
        "s_or_b32 s80, s80, s81\n\t"
        "v_and_b32_e32 v5, 4, v4\n\t"
        "v_cmp_ne_u32_e32 vcc, 0, v5\n\t"
        ".Lhk5%=:\n\t"
        "s_cbranch_execz .Lhk6%=\n\t"
        "s_nop 1\n\t"
        "s_cmp_lg_u64 vcc, 0\n\t"
        "s_cselect_b32 s81, 8, 0\n\t"
        "s_or_b32 s80, s80, s81\n\t"
        "v_and_b32_e32 v5, 8, v4\n\t"
        "v_cmp_ne_u32_e32 vcc, 0, v5\n\t"
        ".Lhk6%=:\n\t"
        "s_cbranch_execz .Lhk7%=\n\t"
        "s_nop 1\n\t"
        "s_cmp_lg_u64 vcc, 0\n\t"
        "s_cselect_b32 s81, 16, 0\n\t"
        "s_or_b32 s80, s80, s81\n\t"
        "s_lshr_b32 s80, 0x1adf1210, s80\n\t"
        "s_and_b32 s55, s80, 3\n\t"
        ".Lhk7%=:\n\t"
        "s_cbranch_execz .Lhk8%=\n\t"
        "s_branch .Lal%=\n\t"
        ".Lm1%=:\n\t"
        "s_add_u32 s80, s37, %c[iPEN2]\n\t"
        "s_min_i32 s80, s80, %c[iPENM]\n\t"
        "s_cmp_lg_u32 s37, 0\n\t"
        "s_cselect_b32 s37, s80, %c[iPEN]\n\t"
        "s_add_u32 s80, s56, s37\n\t"
        ".Lhk8%=:\n\t"
        "s_cbranch_execz .Lhk9%=\n\t"
        "s_max_i32 s80, s80, %c[iPMIN]\n\t"
        "s_add_u32 s43, s41, s80\n\t"
        "s_add_u32 s95, s93, %c[LA]\n\t"
        "s_add_u32 s46, s46, %c[iXR]\n\t"
        ".Lal%=:\n\t"
        "s_sub_u32 s80, s55, s43\n\t"
        "s_and_b32 s80, s80, %c[iPHM]\n\t"
        ".Lhk9%=:\n\t"
        "s_cbranch_execz .Lhk10%=\n\t"
        "s_add_u32 s43, s43, s80\n\t"
        "s_branch .Lsd%=\n\t"
        ".Lsf%=:\n\t"
        "s_mov_b32 exec_hi, -1\n\t"
        "s_sleep 2\n\t"
        "buffer_load_dwordx4 v[88:91], v112, %[srd], s66 offen sc1\n\t"
        "s_add_u32 s97, s97, 1\n\t"
        ".Lhk10%=:\n\t"
        "s_cbranch_execz .Lhk11%=\n\t"
        "s_cmp_lt_u32 s97, 0x100000\n\t"
        "s_cbranch_scc0 .Lgu%=\n\t"
        "s_waitcnt vmcnt(0)\n\t"
        "s_branch .Lsr%=\n\t"
        ".Lfa%=:\n\t"
        "v_max_u32_e32 v84, v0, v2\n\t"
        "v_max3_u32 v84, v84, v1, v3\n\t"
        ".Lhk11%=:\n\t"
        "s_cbranch_execz .Lhk12%=\n\t"
        "v_max3_u32 v84, v84, v4, v6\n\t"
        "v_max3_u32 v84, v84, v5, v7\n\t"
        "v_max3_u32 v84, v84, v8, v10\n\t"
        "v_max3_u32 v84, v84, v9, v11\n\t"
        "v_max3_u32 v84, v84, v12, v14\n\t"
        "v_max3_u32 v84, v84, v13, v15\n\t"
        ".Lhk12%=:\n\t"
        "s_cbranch_execz .Lhk13%=\n\t"
        "v_cmp_eq_u32_e32 vcc, -1, v84\n\t"
        "s_nop 1\n\t"
        "v_mov_b32_e32 v104, v104\n\t"
        "s_nop 0\n\t"
        "s_cbranch_vccz .Lra%=\n\t"
        "s_add_u32 s80, s40, 1\n\t"
        ".Lhk13%=:\n\t"
        "s_cbranch_execz .Lhk14%=\n\t"
        "v_mov_b32_e32 v80, 161792\n\t"
        "v_mov_b32_e32 v81, s80\n\t"
        "ds_write_b32 v80, v81\n\t"
        "v_mov_b32_e32 v48, 0\n\t"
        "v_mov_b32_e32 v49, 0\n\t"
        "v_mov_b32_e32 v50, 0\n\t"
        ".Lhk14%=:\n\t"
        "s_cbranch_execz .Lhk15%=\n\t"
        "v_mov_b32_e32 v51, 0\n\t"
        "v_mov_b32_e32 v52, 0\n\t"
        "v_mov_b32_e32 v53, 0\n\t"
        "v_mov_b32_e32 v54, 0\n\t"
        "v_mov_b32_e32 v55, 0\n\t"
        "v_mov_b32_e32 v56, 0\n\t"
        ".Lhk15%=:\n\t"
        "s_cbranch_execz .Lhk16%=\n\t"
        "v_mov_b32_e32 v57, 0\n\t"
        "v_mov_b32_e32 v58, 0\n\t"
        "v_mov_b32_e32 v59, 0\n\t"
        "v_mov_b32_e32 v60, 0\n\t"
        "v_mov_b32_e32 v61, 0\n\t"
        "v_mov_b32_e32 v62, 0\n\t"
        ".Lhk16%=:\n\t"
        "s_cbranch_execz .Lhk17%=\n\t"
        "v_mov_b32_e32 v63, 0\n\t"
        "s_sleep %c[VASL]\n\t"
        "buffer_load_dwordx4 v[0:3], v112, %[srd], s57 offen sc1\n\t"
        "buffer_load_dwordx4 v[4:7], v112, %[srd], s58 offen sc1\n\t"
        "buffer_load_dwordx4 v[8:11], v112, %[srd], s59 offen sc1\n\t"
        "buffer_load_dwordx4 v[12:15], v112, %[srd], s60 offen sc1\n\t"
        ".Lhk17%=:\n\t"
        "s_cbranch_execz .Lhk18%=\n\t"
        "buffer_store_dword v0, off, %[wsd], s36\n\t"
        "s_add_u32 s97, s97, 1\n\t"
        "s_cmp_lt_u32 s97, 0x100000\n\t"
        "s_cbranch_scc0 .Lgu%=\n\t"
        "s_branch .Lgo%=\n\t"
        ".Lfc%=:\n\t"
        "v_max_u32_e32 v84, v16, v18\n\t"
        ".Lhk18%=:\n\t"
        "s_cbranch_execz .Lhk19%=\n\t"
        "v_max3_u32 v84, v84, v20, v22\n\t"
        "v_max3_u32 v84, v84, v24, v26\n\t"
        "v_max3_u32 v84, v84, v28, v30\n\t"
        "v_max3_u32 v84, v84, v32, v34\n\t"
        "v_max3_u32 v84, v84, v36, v38\n\t"
        "v_max3_u32 v84, v84, v40, v42\n\t"
        ".Lhk19%=:\n\t"
        "s_cbranch_execz .Lhk20%=\n\t"
        "v_max3_u32 v84, v84, v44, v46\n\t"
        "v_cmp_eq_u32_e32 vcc, -1, v84\n\t"
        "s_nop 1\n\t"
        "v_mov_b32_e32 v104, v104\n\t"
        "s_nop 0\n\t"
        "s_cbranch_vccz .Lrd%=\n\t"
        ".Lhk20%=:\n\t"
        "s_cbranch_execz .Lhk21%=\n\t"
        "s_sleep %c[CSLP]\n\t"
        "buffer_load_dwordx4 v[16:19], v112, %[srd], s63 offen sc1\n\t"
        "v_mov_b32_e32 v64, 0\n\t"
        "v_mov_b32_e32 v65, 0\n\t"
        "buffer_load_dwordx4 v[20:23], v115, %[srd], s63 offen sc1\n\t"
        "v_mov_b32_e32 v66, 0\n\t"
        ".Lhk21%=:\n\t"
        "s_cbranch_execz .Lhk22%=\n\t"
        "v_mov_b32_e32 v67, 0\n\t"
        "buffer_load_dwordx4 v[24:27], v112, %[srd], s64 offen sc1\n\t"
        "v_mov_b32_e32 v68, 0\n\t"
        "v_mov_b32_e32 v69, 0\n\t"
        "buffer_load_dwordx4 v[28:31], v115, %[srd], s64 offen sc1\n\t"
        "v_mov_b32_e32 v70, 0\n\t"
        ".Lhk22%=:\n\t"
        "s_cbranch_execz .Lhk23%=\n\t"
        "v_mov_b32_e32 v71, 0\n\t"
        "buffer_load_dwordx4 v[32:35], v112, %[srd], s65 offen sc1\n\t"
        "v_mov_b32_e32 v72, 0\n\t"
        "v_mov_b32_e32 v73, 0\n\t"
        "buffer_load_dwordx4 v[36:39], v115, %[srd], s65 offen sc1\n\t"
        "v_mov_b32_e32 v74, 0\n\t"
        ".Lhk23%=:\n\t"
        "s_cbranch_execz .Lhk24%=\n\t"
        "v_mov_b32_e32 v75, 0\n\t"
        "buffer_load_dwordx4 v[40:43], v112, %[srd], s73 offen sc1\n\t"
        "v_mov_b32_e32 v76, 0\n\t"
        "v_mov_b32_e32 v77, 0\n\t"
        "buffer_load_dwordx4 v[44:47], v115, %[srd], s73 offen sc1\n\t"
        "v_mov_b32_e32 v78, 0\n\t"
        ".Lhk24%=:\n\t"
        "s_cbranch_execz .Lhk25%=\n\t"
        "v_mov_b32_e32 v79, 0\n\t"
        "s_add_u32 s80, s40, 1\n\t"
        "v_mov_b32_e32 v80, 161796\n\t"
        "v_mov_b32_e32 v81, s80\n\t"
        "ds_write_b32 v80, v81\n\t"
        "s_add_u32 s97, s97, 1\n\t"
        ".Lhk25%=:\n\t"
        "s_cbranch_execz .Lhk26%=\n\t"
        "s_cmp_lt_u32 s97, 0x100000\n\t"
        "s_cbranch_scc0 .Lgu%=\n\t"
        "s_branch .Lrc%=\n\t"
        ".Lno%=:\n\t"
        "s_waitcnt lgkmcnt(0)\n\t"
        "s_barrier\n\t"
        "s_branch .Lnp%=\n\t"
        ".Lta%=:\n\t"
        ".Lhk26%=:\n\t"
        "s_cbranch_execz .Lhk27%=\n\t"
        "s_waitcnt lgkmcnt(0)\n\t"
        "s_branch .Lxa%=\n\t"
        ".Ltc%=:\n\t"
        "s_waitcnt lgkmcnt(0)\n\t"
        "s_branch .Lxc%=\n\t"
        ".Lgu%=:\n\t"
        "s_mov_b64 exec, -1\n\t"
        "v_mov_b32_e32 v80, %c[ferr]\n\t"
        ".Lhk27%=:\n\t"
        "s_cbranch_execz .Lhk28%=\n\t"
        "v_mov_b32_e32 v81, 2\n\t"
        "buffer_store_dword v81, v80, %[wsd], 0 offen sc1\n\t"
        "s_endpgm\n\t"
        ".Lend%=:\n\t"
        "buffer_store_dword v80, v114, %[wsd], s72 offen sc1\n\t"
        "s_endpgm\n\t"
        ".Lhk28%=:\n\t"
        "s_branch .Lhr%="
        : [nA] "+s"(nA), [nS] "+s"(nS), [nT] "+s"(nT), [nW] "+s"(nW), [nC] "+s"(nC), [b0] "+s"(b0), [pob] "+s"(pob)
        : [srd] "s"(rhd), [wsd] "s"(wsd), [w] "s"(w), [tag] "s"(tag), [kvo] "s"(kvo), [cal] "s"(cal), [sv] "s"(sv), [so] "s"(so), [m0b] "s"(m0b), [key] "s"(key), [tu] "s"(tu),
          [ferr] "i"(F_ERR), [iD1] "i"(D1), [iD2] "i"(D2), [LC] "i"(LC0), [LA] "i"(LA0), [LC24] "i"(LC0 + 24), [LA24] "i"(LA0 + 24), [E1] "i"(2 * (D1 - LC0)), [E2] "i"(2 * (D2 - LA0)),
          [iPEN] "i"(PEN), [iPEN2] "i"(PEN2), [iPENM] "i"(PEN + 2 * PEN2), [iPEN0] "i"(PEN0), [iTOL] "i"(TOL), [iCT0] "i"(CT0), [iX0C] "i"(X0C), [iX0A] "i"(X0A), [iPHM] "i"(PH_M),
          [iFT] "i"(-3), [iFN] "i"(FINE_N), [AUP] "i"(A_UP), [ASLP] "i"(A_SLP), [LDEC] "i"(L_DEC), [UPSH] "i"(UP_SH), [TRUP] "i"(TR_UP), [TRDN] "i"(TR_DN), [HSLP] "i"(H_SLP), [iPMIN] "i"(PMIN), [VSLP] "i"(V_SLP), [RSLP] "i"(R_SLP), [iVD] "i"(V_DEL), [iXR] "i"(XR), [iBM] "i"(BM_), [iBS] "i"(BS_), [VASL] "i"(VA_SLP), [CSLP] "i"(C_SLP), [DNSH] "i"(DN_SH), [NX0] "i"(NEARX), [NX1] "i"(8192 - NEARX), [iCS1] "i"(CS1), [iCS2] "i"(CS2), [NY0] "i"(NEARY), [NY1] "i"(16384 - NEARY),
          "{v[112:127]}"(pc), "{v[96:103]}"(pa), "{v[104:105]}"(pb)
        : "memory", "vcc", "scc", "m0", "s36", "s37", "s38", "s39", "s40", "s41", "s42", "s43", "s44", "s45", "s46", "s47", "s48", "s49", "s50", "s51", "s52", "s53", "s54", "s55", "s56", "s57", "s58", "s59", "s60", "s61", "s62", "s63", "s64", "s65", "s66", "s67", "s68", "s69", "s70", "s71", "s72", "s73", "s74", "s75", "s76", "s77", "s78", "s79", "s80", "s81", "s82", "s83", "s84", "s85", "s86", "s87", "s88", "s89", "s90", "s91", "s92", "s93", "s94", "s95", "s96", "s97", "s98", "s99");
    }
}

using f32x16 = __attribute__((ext_vector_type(16))) float;
constexpr int CV_RP = 80;
constexpr int CV_U = 96 * CV_RP;
constexpr int CV_KP = 4416;
constexpr int CV_K0 = 4 * CV_U;
__device__ __forceinline__ float lo16(unsigned x) { return __uint_as_float(x << 16); }
__device__ __forceinline__ float hi16(unsigned x) { return __uint_as_float(x & 0xFFFF0000u); }
__device__ __forceinline__ void prep_block(const Params& p, rsrc_t r, int k4, const int wv, const unsigned tag) {
    const int tid = otid(), w = wv, l = tid & 63;
    const int cu = (k4 & 3) * 32 + w * 8 + (l >> 3), oc0 = (k4 >> 2) * 8 + (l & 7);
    const rsrc_t rk = __builtin_amdgcn_make_buffer_rsrc((void*)p.Ak, 0, S * D * 2, 0x00020000), rv = __builtin_amdgcn_make_buffer_rsrc((void*)p.Av, 0, S * D * 2, 0x00020000);
    v4u sk[4], kn[11], vn[8], gw[2];
    auto fetch = [&](int oc) {
#pragma unroll
        for (int j = 0; j < 11; ++j) kn[j] = __builtin_amdgcn_raw_buffer_load_b128(rk, (unsigned)((oc * 8 + j - 3) * 2048 + cu * 16), 0, 2);
#pragma unroll
        for (int i = 0; i < 8; ++i) vn[i] = __builtin_amdgcn_raw_buffer_load_b128(rv, (unsigned)((oc * 8 + i) * 2048 + cu * 16), 0, 2); };
    fetch(oc0);
#pragma unroll
    for (int q = 0; q < 4; ++q) sk[q] = *(const v4u*)(p.shift_k + cu * 32 + q * 8);
#pragma unroll
    for (int q = 0; q < 2; ++q) gw[q] = *(const v4u*)(p.gate_w + q * 512 + l * 8);
    st4(r, tid < 128 ? F_RDY + k4 * 512 + tid * 4 : tid < 132 ? F_ATT + (k4 * 4 + tid - 128) * 64 : (tid == 192 && k4 == 0) ? F_CONV : 0xF0000000u, 0u);
#pragma unroll 1
    for (int it = 0; it < 4; ++it) {
        const int oc = oc0 + 64 * it, tk = (it * 32 + k4) * 16 + w;
        v4u kr[11], vr[8], xg[4][2];
#pragma unroll
        for (int j = 0; j < 11; ++j) kr[j] = kn[j];
#pragma unroll
        for (int i = 0; i < 8; ++i) vr[i] = vn[i];
        __builtin_amdgcn_sched_barrier(0);
#pragma unroll
        for (int i = 0; i < 4; ++i)
#pragma unroll
            for (int q = 0; q < 2; ++q) xg[i][q] = *(const v4u*)(p.Aq + (size_t)(tk + 4 * i) * D + q * 512 + l * 8);
        fetch(it < 3 ? oc + 64 : 4096);
        __builtin_amdgcn_sched_barrier(0);
#pragma unroll
        for (int c8 = 0; c8 < 8; ++c8) {
            const unsigned s01 = sk[c8 >> 1][(c8 & 1) * 2], s23 = sk[c8 >> 1][(c8 & 1) * 2 + 1];
            const float sv[4] = {hi16(s23), lo16(s23), hi16(s01), lo16(s01)};
            float kk[11], u[8];
#pragma unroll
            for (int j = 0; j < 11; ++j) kk[j] = (c8 & 1) ? hi16(kr[j][c8 >> 1]) : lo16(kr[j][c8 >> 1]);
            f32x2 acc[4];
#pragma unroll
            for (int j = 0; j < 4; ++j)
#pragma unroll
                for (int i = 0; i < 4; ++i) { if (FASTP) { const f32x2 t = (f32x2){kk[i + j], kk[i + j + 4]} * sv[j]; acc[i] = j == 0 ? t : j == 3 ? rbf2(acc[i] + t) : acc[i] + t; }
                    else { const f32x2 t = rbf2((f32x2){kk[i + j], kk[i + j + 4]} * sv[j]); acc[i] = j == 0 ? t : rbf2(acc[i] + t); } }
#pragma unroll
            for (int i = 0; i < 8; ++i) u[i] = acc[i & 3][i >> 2] * ((c8 & 1) ? hi16(vr[i][c8 >> 1]) : lo16(vr[i][c8 >> 1]));
            st16(r, W_U + (cu * 8 + c8) * 4096 + oc * 16, (v4u){pack2(u[7], u[6]), pack2(u[5], u[4]), pack2(u[3], u[2]), pack2(u[1], u[0])});
        }
        float ga[4];
#pragma unroll
        for (int i = 0; i < 4; ++i) { float acc = 0.f;
#pragma unroll
            for (int q = 0; q < 2; ++q)
#pragma unroll
                for (int e = 0; e < 4; ++e) { const unsigned xa = xg[i][q][e], gb = gw[q][e]; acc += rbfh(lo16(xa) * lo16(gb)); acc += rbfh(hi16(xa) * hi16(gb)); }
            ga[i] = acc; }
        for (int o = 32; o > 0; o >>= 1)
#pragma unroll
            for (int i = 0; i < 4; ++i) ga[i] += __shfl_xor(ga[i], o);
        const float z = rbfh(l == 0 ? ga[0] : l == 1 ? ga[1] : l == 2 ? ga[2] : ga[3]);
        st4(r, l < 4 ? W_GATE + (tk + 4 * l) * 4 : 0xF0000000u, __float_as_uint(rbfh(1.f / (1.f + expf(-z)))));
    }
    DRAIN(); __syncthreads();
    if (tid == 0) st4(r, F_PREP + k4 * 4, tag);
}
__device__ __forceinline__ bool conv_block(const Params& p, rsrc_t r, const int k8, unsigned char* lds, const int wv, const int j, const unsigned tag, const unsigned skew) {
    (void)j;
    const int tid = otid(), w = wv, l = tid & 63;
    const rsrc_t rs = __builtin_amdgcn_make_buffer_rsrc((void*)p.ssm_k, 0, S * D * 2, 0x00020000);
    v4u kr[4], uv[4]; v2u kx[4]; unsigned dn;
    auto fetch_k = [&](int ch) {
#pragma unroll
        for (int i = 0; i < 4; ++i) { const unsigned o = (unsigned)(ch * 4096 + (i * 64 + l) * 16);
            kr[i] = __builtin_amdgcn_raw_buffer_load_b128(rs, o, 0, 2); kx[i] = __builtin_amdgcn_raw_buffer_load_b64(rs, (i == 3 && l == 63) ? 0xF0000000u : o + 16, 0, 2); }
        dn = p.ssm_D[ch & 1023]; };
    auto fetch_u = [&](unsigned o) {
#pragma unroll
        for (int i = 0; i < 4; ++i) uv[i] = ld16(r, o + (i * 64 + l) * 16); };
    const int c0 = k8 * 32 + w * 8;
    fetch_k(c0);
    unsigned char* const uc = lds + w * CV_U; unsigned char* const kc = lds + CV_K0 + w * 4 * CV_KP;
    {
        const v4u z = {0u, 0u, 0u, 0u};
#pragma unroll
        for (int i = 0; i < 3; ++i) *(v4u*)(uc + min(i * 64 + l, 159) * 16) = z;
#pragma unroll
        for (int i = 0; i < 2; ++i) { const int q = min(i * 64 + l, 79), e = q % 20; *(v4u*)(kc + (q / 20) * CV_KP + e * 16 + (e < 8 ? 0 : 4096)) = z; }
    }
    if (!wait_prep(r, l, tag)) return false;
    wait_long(rtnow() + skew);
    fetch_u(W_U + c0 * 4096);
    const int b = l & 31, kg = l >> 5, cc = (b + 1) & 3;
    unsigned ya[32], yb[32];
#pragma unroll
    for (int i = 0; i < 32; ++i) { ya[i] = 0u; yb[i] = 0u; }
#pragma unroll 1
    for (int it = 0; it < 8; ++it) {
        const unsigned dv = dn;
#pragma unroll
        for (int i = 0; i < 4; ++i) { const v4u d = kr[i]; const v2u e = kx[i]; unsigned char* q = kc + 128 + (i * 64 + l) * 16;
            const unsigned s0 = __builtin_amdgcn_alignbit(d.y, d.x, 16), s1 = __builtin_amdgcn_alignbit(d.z, d.y, 16), s2 = __builtin_amdgcn_alignbit(d.w, d.z, 16), s3 = __builtin_amdgcn_alignbit(e.x, d.w, 16), s4 = __builtin_amdgcn_alignbit(e.y, e.x, 16);
            *(v4u*)q = d; *(v4u*)(q + CV_KP) = (v4u){s0, s1, s2, s3}; *(v4u*)(q + 2 * CV_KP) = (v4u){d.y, d.z, d.w, e.x}; *(v4u*)(q + 3 * CV_KP) = (v4u){s1, s2, s3, s4};
            if (i == 0 && l == 0) { *(unsigned*)(q + CV_KP - 4) = d.x << 16; *(unsigned*)(q + 2 * CV_KP - 4) = d.x; *(v2u*)(q + 3 * CV_KP - 8) = (v2u){d.x << 16, s0}; } }
#pragma unroll
        for (int i = 0; i < 4; ++i) *(v4u*)(uc + (32 + i * 16 + (l >> 2)) * CV_RP + (l & 3) * 16) = uv[i];
        __builtin_amdgcn_wave_barrier();
        __builtin_amdgcn_sched_barrier(0);
        fetch_k(it < 7 ? c0 + it + 1 : 1024); fetch_u(it < 7 ? W_U + (c0 + it + 1) * 4096 : 0xF0000000u);
        __builtin_amdgcn_sched_barrier(0);
        f32x16 acc0, acc1, ad0, ad1;
#pragma unroll
        for (int i = 0; i < 16; ++i) { acc0[i] = 0.f; acc1[i] = 0.f; ad0[i] = 0.f; ad1[i] = 0.f; }
        typedef const __attribute__((address_space(3))) v2u* l2p;
        unsigned kb = (unsigned)(size_t)(kc + cc * CV_KP + (b - 8 * kg - 7 + 64 - cc) * 2) - 32, kb8 = kb + 8;
        asm volatile("" : "+v"(kb), "+v"(kb8));
        const unsigned char* ub = uc + (32 + b) * CV_RP + kg * 16;
        v2u ka[4][2], kz[4][2]; bf16x8 fa[4], fz[4];
        auto ld_k = [&](int set, int un, int hi) { const int dl = un >> 1, kh = un & 1; const unsigned o = (unsigned)((32 * dl - 16 * kh + 16) * 2 + hi * 2048);
            if (hi) { kz[set][0] = *(l2p)(size_t)(kb + o); kz[set][1] = *(l2p)(size_t)(kb8 + o); } else { ka[set][0] = *(l2p)(size_t)(kb + o); ka[set][1] = *(l2p)(size_t)(kb8 + o); } };
        auto ld_f = [&](int set, int un) { const int dl = un >> 1, kh = un & 1;
            fa[set] = *(const bf16x8*)(ub + (32 - dl) * CV_RP + kh * 32); fz[set] = *(const bf16x8*)(ub - dl * CV_RP + kh * 32); };
#pragma unroll
        for (int q = 0; q < 2; ++q) { ld_k(q, q, 0); ld_f(q, q); ld_k(q, q, 1); }
#pragma unroll
        for (int kh = 0; kh < 2; ++kh) { const int e = 7 - (b - 16 * kh - 8 * kg); const unsigned x = dv << ((e & 1) * 16); const int q = e >> 1;
            const v4u di = {q == 0 ? x : 0u, q == 1 ? x : 0u, q == 2 ? x : 0u, q == 3 ? x : 0u};
            asm volatile("s_nop 1\n\tv_mfma_f32_32x32x16_bf16 %0, %2, %4, %0\n\tv_mfma_f32_32x32x16_bf16 %1, %3, %4, %1" : "+v"(ad1), "+v"(ad0) : "v"(fa[kh]), "v"(fz[kh]), "v"(di)); }
#pragma unroll 1
        for (int u0 = 0; u0 < 64; u0 += 4) {
#pragma unroll
            for (int q = 0; q < 4; ++q) {
                const int un = u0 + q + 2, ns = (q + 2) & 3;
                const v4u kav = {ka[q][0].x, ka[q][0].y, ka[q][1].x, ka[q][1].y}, kzv = {kz[q][0].x, kz[q][0].y, kz[q][1].x, kz[q][1].y};
                __builtin_amdgcn_sched_barrier(0);
                asm volatile("s_nop 1\n\tv_mfma_f32_32x32x16_bf16 %0, %1, %2, %0" : "+v"(acc1) : "v"(fa[q]), "v"(kav));
                ld_k(ns, un, 0);
                __builtin_amdgcn_sched_barrier(0);
                asm volatile("v_mfma_f32_32x32x16_bf16 %0, %1, %2, %0" : "+v"(acc0) : "v"(fz[q]), "v"(kav));
                ld_f(ns, un);
                __builtin_amdgcn_sched_barrier(0);
                asm volatile("v_mfma_f32_32x32x16_bf16 %0, %1, %2, %0" : "+v"(acc1) : "v"(fz[q]), "v"(kzv));
                ld_k(ns, un, 1);
            }
        }
        asm volatile("s_nop 7\n\ts_nop 7" : "+v"(acc0), "+v"(acc1), "+v"(ad0), "+v"(ad1));
#pragma unroll
        for (int i = 0; i < 32; ++i) { const unsigned y = pack2(rbfh(i < 16 ? acc0[i] : acc1[i - 16]) + rbfh(i < 16 ? ad0[i] : ad1[i - 16]), 0.f);
            ya[i] = __builtin_amdgcn_alignbit(yb[i], ya[i], 16); yb[i] = __builtin_amdgcn_alignbit(y, yb[i], 16); }
        if ((it & 3) == 3) {
#pragma unroll
            for (int i = 0; i < 32; ++i) { const int row = (i >> 4) * 32 + (i & 3) + 8 * ((i & 15) >> 2) + 4 * kg; st8(r, W_U + (c0 + it - 3) * 4096 + (row * 32 + b) * 8, (v2u){ya[i], yb[i]}); } }
    }
    DRAIN(); __syncthreads();
    if (tid == 0) __hip_atomic_fetch_add((unsigned*)(p.ws + F_CONV), 8u, __ATOMIC_RELAXED, __HIP_MEMORY_SCOPE_AGENT);
    return true;
}

__device__ __forceinline__ void dma16(unsigned ldsdst, unsigned voff, const v4u& srd, unsigned soff) {
    asm volatile("s_mov_b32 m0, %0\n\ts_nop 0\n\tbuffer_load_dwordx4 %1, %2, %3 offen lds" :: "s"(ldsdst), "v"(voff), "s"(srd), "s"(soff) : "m0", "memory"); }
__device__ __forceinline__ v4u pack_acc(const f32x4& a, const f32x4& b) {
    float x0, x1, x2, x3, y0, y1, y2, y3;
    asm volatile("v_accvgpr_read_b32 %0, %8\n\tv_accvgpr_read_b32 %1, %9\n\tv_accvgpr_read_b32 %2, %10\n\tv_accvgpr_read_b32 %3, %11\n\t"
                 "v_accvgpr_read_b32 %4, %12\n\tv_accvgpr_read_b32 %5, %13\n\tv_accvgpr_read_b32 %6, %14\n\tv_accvgpr_read_b32 %7, %15"
                 : "=v"(x0), "=v"(x1), "=v"(x2), "=v"(x3), "=v"(y0), "=v"(y1), "=v"(y2), "=v"(y3)
                 : "a"(a[0]), "a"(a[1]), "a"(a[2]), "a"(a[3]), "a"(b[0]), "a"(b[1]), "a"(b[2]), "a"(b[3]));
    return (v4u){pack2(x0, x1), pack2(x2, x3), pack2(y0, y1), pack2(y2, y3)}; }
#define SB() __builtin_amdgcn_sched_barrier(0)
#define WO_REGS "a128", "a129", "a130", "a131", "a132", "a133", "a134", "a135", "a136", "a137", "a138", "a139", "a140", "a141", "a142", "a143", "a144", "a145", "a146", "a147", "a148", "a149", "a150", "a151", "a152", "a153", "a154", "a155", "a156", "a157", "a158", "a159", "a160", "a161", "a162", "a163", "a164", "a165", "a166", "a167", "a168", "a169", "a170", "a171", "a172", "a173", "a174", "a175", "a176", "a177", "a178", "a179", "a180", "a181", "a182", "a183", "a184", "a185", "a186", "a187", "a188", "a189", "a190", "a191", "a192", "a193", "a194", "a195", "a196", "a197", "a198", "a199", "a200", "a201", "a202", "a203", "a204", "a205", "a206", "a207", "a208", "a209", "a210", "a211", "a212", "a213", "a214", "a215", "a216", "a217", "a218", "a219", "a220", "a221", "a222", "a223", "a224", "a225", "a226", "a227", "a228", "a229", "a230", "a231", "a232", "a233", "a234", "a235", "a236", "a237", "a238", "a239", "a240", "a241", "a242", "a243", "a244", "a245", "a246", "a247", "a248", "a249", "a250", "a251", "a252", "a253", "a254", "a255", "v192", "v193", "v194", "v195", "v196", "v197", "v198", "v199", "v200", "v201", "v202", "v203", "v204", "v205", "v206", "v207", "v208", "v209", "v210", "v211", "v212", "v213", "v214", "v215", "v216", "v217", "v218", "v219", "v220", "v221", "v222", "v223", "v224", "v225", "v226", "v227", "v228", "v229", "v230", "v231", "v232", "v233", "v234", "v235", "v236", "v237", "v238", "v239", "v240", "v241", "v242", "v243", "v244", "v245", "v246", "v247", "v248", "v249", "v250", "v251", "v252", "v253", "v254", "v255"
struct NoHook { __device__ void operator()(int) const {} };
template <int MODE, int MI, int NJ, int NS, int HX = 0, class HOOK = NoHook, int MN = MI>
__device__ __forceinline__ void gemm(rsrc_t r, const bf16_t* __restrict__ A, const bf16_t* __restrict__ B, int m0, int n0, unsigned char* lds, unsigned coff, const int wv,
                                     const int rot, const void* tk = nullptr,
                                     const bool have = false, const bf16_t* Bn = nullptr, int m0n = 0, int n0n = 0, HOOK&& hook = NoHook()) {
    constexpr int AB = MN * 4096, BB = NJ * 2048, B0 = (NS / 2) * AB, NB = NJ / 2, NM = MI * NJ, NR = MI + NJ, BP = NJ * 64;
    static_assert(NS % 2 == 0 && 32 % NS == 0 && B0 <= 65536 && NS * BB <= 65536 && (NJ == 4 || NJ == 8), "ring");
    const int tid = otid();
    const int w = wv, l = tid & 63, n16 = l & 15, g = l >> 4;
    const int wm = w >> 1, wn = w & 1;
    const unsigned lb = __builtin_amdgcn_readfirstlane((unsigned)(size_t)lds);
    const v4u sa = mk_srd(A, 2048 * 1024 * 2); v4u sb = mk_srd(B, 1024 * 4096 * 2);
    f32x4 acc[MI][NJ];
#pragma unroll
    for (int i = 0; i < MI; ++i)
#pragma unroll
        for (int j = 0; j < NJ; ++j) acc[i][j] = (f32x4){0.f, 0.f, 0.f, 0.f};
    unsigned va = (unsigned)((m0 + 8 * MI * w + (l >> 3)) * 2048 + (((l & 7) ^ (l >> 3)) << 4));
    unsigned vb[2];
    if (NJ == 8) { vb[0] = (unsigned)((8 * w + (l >> 5)) * 8192 + n0 * 2) + (unsigned)(((l & 31) << 4) ^ ((l >> 5) << 5) ^ (wn << 7)); vb[1] = vb[0] ^ 64u; }
    else { vb[0] = (unsigned)((8 * w + g) * 8192 + n0 * 2) + (unsigned)((n16 << 4) ^ (g << 5) ^ (wn << 7)); vb[1] = vb[0]; }
    auto rk = [&](int kt) { return (kt & ~31) | ((kt + rot) & 31); };
    unsigned la = lb + w * MI * 1024;
    auto st_a = [&](int buf, int kt, int q) { dma16(la + buf * AB + q * 1024, va, sa, (unsigned)(rk(kt) * 64 + q * 16384)); };
    auto st_b = [&](int buf, int kt, int x) { dma16(lb + B0 + buf * BB + (w * NB + x) * 1024, vb[x & 1], sb, (unsigned)((rk(kt) * 32 + x * (8 / NB)) * 8192)); };
    const unsigned f = (unsigned)((n16 >> 2) | ((g & 1) << 2));
    constexpr int NQ = MODE == 1 ? NJ : NJ / 2;
    unsigned ra[2], rb[NQ];
    ra[0] = lb + (16 * MI * wm + n16) * 128 + ((g ^ (n16 & 7)) << 4); ra[1] = ra[0] ^ 64u;
    {   const unsigned row = lb + B0 + (8 * g + (n16 >> 2)) * BP + (NJ == 8 ? 256 * wn : 0), g0 = NJ == 8 ? 0u : 4u * wn;
#pragma unroll
        for (int j = 0; j < NQ; ++j) {
            if (MODE == 1) rb[j] = row + (((g0 + j) ^ f) << 5) + 8 * (n16 & 3);
            else rb[j] = row + (((g0 + 2 * j + ((n16 & 3) >> 1)) ^ f) << 5) + (n16 & 1) * 16;
            asm volatile("" : "+v"(rb[j])); } }
    bf16x8 af[2][MI], bfr[2][NJ];
    auto rd = [&](int set, int buf, int x) {
        if (x < NJ) { const unsigned a = rb[MODE == 1 ? x : (x >> 1)] + (MODE == 1 ? 0 : (x & 1) * 8) + buf * BB;
            const v2u lo = trb(a), hi = trb(a + 4 * BP); v4u v = {lo.x, lo.y, hi.x, hi.y}; bfr[set][x] = as_frag(v); }
        else af[set][x - NJ] = ldsa(ra[buf & 1] + (buf >> 1) * AB + (x - NJ) * 2048); };
    auto mf = [&](int set, int i, int j) {
        if (HX) asm volatile("v_mfma_f32_16x16x32_bf16 %0, %1, %2, %0" : "+a"(acc[i][j]) : "v"(MODE == 1 ? af[set][i] : bfr[set][j]), "v"(MODE == 1 ? bfr[set][j] : af[set][i]) : WO_REGS);
        else if (MODE == 1) asm volatile("v_mfma_f32_16x16x32_bf16 %0, %1, %2, %0" : "+a"(acc[i][j]) : "v"(af[set][i]), "v"(bfr[set][j]));
        else asm volatile("v_mfma_f32_16x16x32_bf16 %0, %1, %2, %0" : "+a"(acc[i][j]) : "v"(bfr[set][j]), "v"(af[set][i])); };
    if (!have) {
#pragma unroll
        for (int t = 0; t < NS; ++t) {
            if (t & 1) {
#pragma unroll
                for (int q = 0; q < MI; ++q) st_a(t >> 1, t - 1, q); }
#pragma unroll
            for (int q = 0; q < NB; ++q) st_b(t, t, q);
            if (HX) hook(t); }
    }
#pragma unroll
    for (int i = 0; i < MI; ++i)
#pragma unroll
        for (int j = 0; j < NJ; ++j) asm volatile("" : "+a"(acc[i][j]));
    asm volatile("s_nop 1" ::: "memory");
    if (!have) {
        asm volatile("s_waitcnt vmcnt(%c0)\n\ts_barrier" :: "i"((NS - 1) * (NB + HX) + (NS / 2 - 1) * MI) : "memory");
    }
#pragma unroll
    for (int x = 0; x < NR; ++x) rd(0, 0, x);
    constexpr int HEAD = NM / 8;
    const v4u st = mk_srd(tk, tk ? 32768 : 0); unsigned tdum = 0u;
#pragma unroll 1
    for (int k0 = 0; k0 < 32; k0 += NS) {
        int kb = k0 + NS;
        if (kb == 32) {
            kb = Bn ? 0 : 0x10100;
            if (Bn) { va += (unsigned)((m0n - m0 + 8 * (MN - MI) * w) * 2048); la = lb + w * MN * 1024; vb[0] += (unsigned)((n0n - n0) * 2); vb[1] += (unsigned)((n0n - n0) * 2); sb = mk_srd(Bn, 1024 * 4096 * 2); } }
        if (NS == 4) asm volatile("buffer_load_dword %0, %1, %2, %3 offen" : "+v"(tdum) : "v"(l < 32 ? (unsigned)l * 128u : 0xF0000000u), "s"(st), "s"((unsigned)k0 * 1024u) : "memory");
#pragma unroll
        for (int u = 0; u < NS; ++u) {
            SB();
#pragma unroll
            for (int q = 0; q < HEAD; ++q) mf(u & 1, q / NJ, q % NJ);
            SB();
            asm volatile("s_waitcnt vmcnt(%c0) lgkmcnt(0)\n\ts_barrier" :: "i"((NS - 2) * (NB + HX) + (NS / 2 - 1 - (u & 1)) * MI) : "memory");
            SB();
            constexpr int span = NM - HEAD;
#pragma unroll
            for (int q = HEAD; q < NM; ++q) {
                mf(u & 1, q / NJ, q % NJ);
                const int x = q - HEAD, nd = NB + ((u & 1) ? MI : 0);
#pragma unroll
                for (int y = 0; y < NR; ++y) if ((y * (span - HEAD)) / NR == x) rd((u & 1) ^ 1, (u + 1) % NS, y);
#pragma unroll
                for (int y = 0; y < nd; ++y) if ((y * span + span / 2) / nd == x) { if ((u & 1) && y < MI) st_a(u >> 1, kb + u - 1, y); else st_b(u, kb + u, y - ((u & 1) ? MI : 0)); }
                SB(); }
            if (MN > MI && (u & 1) && k0 == 32 - NS) {
#pragma unroll
                for (int y = MI; y < MN; ++y) st_a(u >> 1, kb + u - 1, y); }
            if (HX) hook(NS + k0 + u);
        }
    }
    asm volatile("s_nop 7\n\ts_nop 3" : "+v"(tdum) :: "memory");
    SB();
    const int col = n0 + 16 * NJ * wn, head = col >> 7, sub = (col & 127) >> 4, seg0 = (m0 >> 5) + (MI / 2) * wm;
#pragma unroll
    for (int sg = 0; sg < MI / 2; ++sg) {
        const unsigned base = coff + (unsigned)(((seg0 + sg) * 32 + head) * 8192 + l * 16);
#pragma unroll
        for (int q = 0; q < NJ; ++q) {
            if (HX) asm volatile("" ::: WO_REGS);
            if (MODE == 0) { const int tt = q / (NJ / 2), kk = q % (NJ / 2); st16(r, base + (tt * 4 + (sub >> 1) + kk) * 1024, pack_acc(acc[2 * sg + tt][2 * kk], acc[2 * sg + tt][2 * kk + 1])); }
            else st16(r, base + (sub + q) * 1024, pack_acc(acc[2 * sg][q], acc[2 * sg + 1][q])); }
    }
}

__device__ __forceinline__ void attn_block(const Params& p, const int wv, const int h, const unsigned tag) {
    const int l = otid() & 63, n16 = l & 15, g = l >> 4;
    const unsigned OOB = 0x80000000u;
    const v4u rd = mk_srd(p.ws, W_TOTAL);
    const unsigned kx = W_KX + h * 8192, qx = W_QX + h * 8192, vx = W_VF + h * 8192, km = HO_KVM + h * 4096;
    const v8u pc = {(unsigned)l * 16, (unsigned)(n16 * 16 + (g & 1) * 8), F_KVQ + ((l >> 2) * 32 + h) * 64 + min(l & 3, 2) * 4, F_PROG + h * 128, (unsigned)((g >> 1) * 256 + ((n16 & 1) + 2 * (g & 1) + 2 * (n16 & 6)) * 16),
                    (unsigned)(wv * 2048 + (8 * g + (n16 >> 2)) * 16 + (n16 & 1) * 8), (l & 31) ? OOB : F_ATT + (l >> 5) * 64, l < 32 ? F_RDY + l * 128 : OOB};
    const int dq = n16 - 4 * g;
    asm volatile(
        "s_mov_b32 s36, 0xf0000000\n\t"
        "s_mov_b32 s37, %[w]\n\t"
        "s_mov_b32 s38, s36\n\t"
        "s_mov_b32 s40, 0\n\t"
        "s_memrealtime s[98:99]\n\t"
        "s_waitcnt lgkmcnt(0)\n\t"
        "s_sub_u32 s41, s98, 0x100000\n\t"
        "s_mov_b64 s[42:43], 0\n\t"
        "s_mov_b32 s44, -1\n\t"
        "s_mov_b32 s45, -1\n\t"
        "s_mov_b32 s56, 0\n\t"
        "s_mov_b32 s57, -1\n\t"
        "s_lshl_b32 s58, %[w], 10\n\t"
        "s_add_u32 s58, s58, 16384\n\t"
        "v_cmp_le_i32_e64 s[48:49], 0, v252\n\t"
        "v_cmp_le_i32_e64 s[50:51], 1, v252\n\t"
        "v_cmp_le_i32_e64 s[52:53], 2, v252\n\t"
        "v_cmp_le_i32_e64 s[54:55], 3, v252\n\t"
        "v_mov_b32_e32 v253, 0xce6e6b28\n\t"
        "v_mov_b32_e32 v254, 0xff800000\n\t"
        "s_mov_b32 s97, 0\n\t"
        ".Lst%=:\n\t"
        "s_sleep 32\n\t"
        "buffer_load_dword v240, v246, %[rd], 0 offen sc1\n\t"
        "s_add_u32 s97, s97, 1\n\t"
        "s_cmp_lt_u32 s97, 0x200000\n\t"
        "s_cbranch_scc0 .Lgu%=\n\t"
        "s_waitcnt vmcnt(0)\n\t"
        "v_cmp_eq_u32_e32 vcc, %[tag], v240\n\t"
        "s_nop 1\n\t"
        "s_or_b64 s[42:43], s[42:43], vcc\n\t"
        "s_andn2_b32 s80, %c[stm], s43\n\t"
        "s_cbranch_scc1 .Lst%=\n\t"
        "s_lshl_b32 s80, %[w], 5\n\t"
        "s_add_u32 s80, s80, %[blk]\n\t"
        ".Lsu%=:\n\t"
        "s_sleep %c[sts]\n\t"
        "s_sub_u32 s80, s80, 1\n\t"
        "s_cbranch_scc0 .Lsu%=\n\t"
        ".Ltop%=:\n\t"
        "s_cmp_eq_u32 s38, 0\n\t"
        "s_cbranch_scc1 .La2%=\n\t"
        "s_mov_b32 s97, 0\n\t"
        ".La1%=:\n\t"
        "buffer_load_dword v240, v246, %[rd], 0 offen sc1\n\t"
        "buffer_load_dword v241, v247, %[rd], 0 offen sc1\n\t"
        "s_add_u32 s97, s97, 1\n\t"
        "s_cmp_lt_u32 s97, 0x200000\n\t"
        "s_cbranch_scc0 .Lgu%=\n\t"
        "s_memrealtime s[98:99]\n\t"
        "s_waitcnt vmcnt(0) lgkmcnt(0)\n\t"
        "v_cmp_eq_u32_e32 vcc, %[tag], v240\n\t"
        "s_nop 1\n\t"
        "s_or_b64 s[42:43], s[42:43], vcc\n\t"
        "v_readfirstlane_b32 s80, v241\n\t"
        "s_nop 0\n\t"
        "s_cmp_gt_u32 s80, s40\n\t"
        "s_cselect_b32 s41, s98, s41\n\t"
        "s_max_u32 s40, s40, s80\n\t"
        "s_and_b32 s80, s37, 60\n\t"
        "s_lshr_b64 s[80:81], s[42:43], s80\n\t"
        "s_andn2_b32 s80, 15, s80\n\t"
        "s_cbranch_scc0 .La1%=x\n\t"
        "s_sleep 16\n\t"
        "s_branch .La1%=\n\t"
        ".La1%=x:\n\t"
        "s_cmp_gt_u32 s40, s37\n\t"
        "s_cselect_b32 s39, 0, s36\n\t"
        "s_lshl_b32 s80, s37, 18\n\t"
        "s_or_b32 s80, s80, 0\n\t"
        "s_lshl_b32 s81, s37, 17\n\t"
        "s_add_u32 s81, s81, %[km]\n\t"
        "s_or_b32 s81, s81, s39\n\t"
        "s_add_u32 s82, s80, %[kx]\n\t"
        "s_add_u32 s83, s82, 0x1000\n\t"
        "buffer_load_dwordx4 a[0:3], v244, %[rd], s82 offen offset:0 sc1\n\t"
        "buffer_load_dwordx4 a[4:7], v244, %[rd], s82 offen offset:1024 sc1\n\t"
        "buffer_load_dwordx4 a[8:11], v244, %[rd], s82 offen offset:2048 sc1\n\t"
        "buffer_load_dwordx4 a[12:15], v244, %[rd], s82 offen offset:3072 sc1\n\t"
        "buffer_load_dwordx4 a[16:19], v244, %[rd], s83 offen offset:0 sc1\n\t"
        "buffer_load_dwordx4 a[20:23], v244, %[rd], s83 offen offset:1024 sc1\n\t"
        "buffer_load_dwordx4 a[24:27], v244, %[rd], s83 offen offset:2048 sc1\n\t"
        "buffer_load_dwordx4 a[28:31], v244, %[rd], s83 offen offset:3072 sc1\n\t"
        "s_cmp_eq_u64 s[42:43], -1\n\t"
        "s_cbranch_scc1 .La3%=\n\t"
        "s_sleep %c[aslp]\n\t"
        ".La3%=:\n\t"
        "s_sleep %c[bslp]\n\t"
        "s_add_u32 s82, s80, %[qx]\n\t"
        "s_add_u32 s83, s82, 0x1000\n\t"
        "buffer_load_dwordx4 a[32:35], v244, %[rd], s82 offen offset:0 sc1\n\t"
        "buffer_load_dwordx4 a[36:39], v244, %[rd], s82 offen offset:1024 sc1\n\t"
        "buffer_load_dwordx4 a[40:43], v244, %[rd], s82 offen offset:2048 sc1\n\t"
        "buffer_load_dwordx4 a[44:47], v244, %[rd], s82 offen offset:3072 sc1\n\t"
        "buffer_load_dwordx4 a[48:51], v244, %[rd], s83 offen offset:0 sc1\n\t"
        "buffer_load_dwordx4 a[52:55], v244, %[rd], s83 offen offset:1024 sc1\n\t"
        "buffer_load_dwordx4 a[56:59], v244, %[rd], s83 offen offset:2048 sc1\n\t"
        "buffer_load_dwordx4 a[60:63], v244, %[rd], s83 offen offset:3072 sc1\n\t"
        "s_cmp_eq_u64 s[42:43], -1\n\t"
        "s_cbranch_scc1 .La4%=\n\t"
        "s_sleep %c[aslp]\n\t"
        ".La4%=:\n\t"
        "s_sleep %c[bslp]\n\t"
        "buffer_load_dwordx4 a[96:99], v248, %[rd], s81 offen offset:0 sc1\n\t"
        "buffer_load_dwordx4 a[100:103], v248, %[rd], s81 offen offset:512 sc1\n\t"
        "buffer_load_dwordx4 a[104:107], v248, %[rd], s81 offen offset:1024 sc1\n\t"
        "buffer_load_dwordx4 a[108:111], v248, %[rd], s81 offen offset:1536 sc1\n\t"
        "buffer_load_dwordx2 a[112:113], v245, %[rd], s81 offen offset:2048 sc1\n\t"
        "buffer_load_dwordx2 a[114:115], v245, %[rd], s81 offen offset:2304 sc1\n\t"
        "buffer_load_dwordx2 a[116:117], v245, %[rd], s81 offen offset:2560 sc1\n\t"
        "buffer_load_dwordx2 a[118:119], v245, %[rd], s81 offen offset:2816 sc1\n\t"
        "buffer_load_dwordx2 a[120:121], v245, %[rd], s81 offen offset:3072 sc1\n\t"
        "buffer_load_dwordx2 a[122:123], v245, %[rd], s81 offen offset:3328 sc1\n\t"
        "buffer_load_dwordx2 a[124:125], v245, %[rd], s81 offen offset:3584 sc1\n\t"
        "buffer_load_dwordx2 a[126:127], v245, %[rd], s81 offen offset:3840 sc1\n\t"
        "s_add_u32 s82, s80, %[vx]\n\t"
        "s_add_u32 s83, s82, 0x1000\n\t"
        "buffer_load_dwordx4 a[64:67], v244, %[rd], s82 offen offset:0 sc1\n\t"
        "buffer_load_dwordx4 a[68:71], v244, %[rd], s82 offen offset:1024 sc1\n\t"
        "buffer_load_dwordx4 a[72:75], v244, %[rd], s82 offen offset:2048 sc1\n\t"
        "buffer_load_dwordx4 a[76:79], v244, %[rd], s82 offen offset:3072 sc1\n\t"
        "buffer_load_dwordx4 a[80:83], v244, %[rd], s83 offen offset:0 sc1\n\t"
        "buffer_load_dwordx4 a[84:87], v244, %[rd], s83 offen offset:1024 sc1\n\t"
        "buffer_load_dwordx4 a[88:91], v244, %[rd], s83 offen offset:2048 sc1\n\t"
        "buffer_load_dwordx4 a[92:95], v244, %[rd], s83 offen offset:3072 sc1\n\t"
        "s_cmp_eq_u64 s[42:43], -1\n\t"
        "s_cbranch_scc1 .La5%=\n\t"
        "s_sleep %c[aslp]\n\t"
        ".La5%=:\n\t"
        "s_sleep %c[bslp]\n\t"
        "buffer_store_dword v0, off, %[rd], s36\n\t"
        "buffer_store_dword v0, off, %[rd], s36\n\t"
        "buffer_store_dword v0, off, %[rd], s36\n\t"
        "buffer_store_dword v0, off, %[rd], s36\n\t"
        "buffer_store_dword v0, off, %[rd], s36\n\t"
        "buffer_store_dword v0, off, %[rd], s36\n\t"
        "buffer_store_dword v0, off, %[rd], s36\n\t"
        "buffer_store_dword v0, off, %[rd], s36\n\t"
        "buffer_store_dword v0, off, %[rd], s36\n\t"
        ".La2%=:\n\t"
        "s_cmp_gt_u32 s40, s37\n\t"
        "s_cselect_b32 s80, s39, 0\n\t"
        "s_cmp_eq_u32 s80, 0\n\t"
        "s_cbranch_scc1 .La6%=\n\t"
        "s_mov_b32 s39, 0\n\t"
        "s_lshl_b32 s81, s37, 17\n\t"
        "s_add_u32 s81, s81, %[km]\n\t"
        "buffer_load_dwordx4 a[96:99], v248, %[rd], s81 offen offset:0 sc1\n\t"
        "buffer_load_dwordx4 a[100:103], v248, %[rd], s81 offen offset:512 sc1\n\t"
        "buffer_load_dwordx4 a[104:107], v248, %[rd], s81 offen offset:1024 sc1\n\t"
        "buffer_load_dwordx4 a[108:111], v248, %[rd], s81 offen offset:1536 sc1\n\t"
        "buffer_load_dwordx2 a[112:113], v245, %[rd], s81 offen offset:2048 sc1\n\t"
        "buffer_load_dwordx2 a[114:115], v245, %[rd], s81 offen offset:2304 sc1\n\t"
        "buffer_load_dwordx2 a[116:117], v245, %[rd], s81 offen offset:2560 sc1\n\t"
        "buffer_load_dwordx2 a[118:119], v245, %[rd], s81 offen offset:2816 sc1\n\t"
        "buffer_load_dwordx2 a[120:121], v245, %[rd], s81 offen offset:3072 sc1\n\t"
        "buffer_load_dwordx2 a[122:123], v245, %[rd], s81 offen offset:3328 sc1\n\t"
        "buffer_load_dwordx2 a[124:125], v245, %[rd], s81 offen offset:3584 sc1\n\t"
        "buffer_load_dwordx2 a[126:127], v245, %[rd], s81 offen offset:3840 sc1\n\t"
        "buffer_store_dword v0, off, %[rd], s36\n\t"
        "buffer_store_dword v0, off, %[rd], s36\n\t"
        "buffer_store_dword v0, off, %[rd], s36\n\t"
        "buffer_store_dword v0, off, %[rd], s36\n\t"
        "buffer_store_dword v0, off, %[rd], s36\n\t"
        "buffer_store_dword v0, off, %[rd], s36\n\t"
        "buffer_store_dword v0, off, %[rd], s36\n\t"
        "buffer_store_dword v0, off, %[rd], s36\n\t"
        ".La6%=:\n\t"
        "s_add_u32 s86, s37, 4\n\t"
        "s_and_b32 s80, s86, 60\n\t"
        "s_lshr_b64 s[80:81], s[42:43], s80\n\t"
        "s_andn2_b32 s80, 15, s80\n\t"
        "s_cselect_b32 s46, s36, 0\n\t"
        "s_cmp_lt_u32 s86, 64\n\t"
        "s_cselect_b32 s46, s46, s36\n\t"
        "s_cmp_gt_u32 s40, s86\n\t"
        "s_cselect_b32 s47, s46, s36\n\t"
        "s_lshl_b32 s80, s86, 18\n\t"
        "s_or_b32 s80, s80, s46\n\t"
        "s_lshl_b32 s81, s86, 17\n\t"
        "s_add_u32 s81, s81, %[km]\n\t"
        "s_or_b32 s81, s81, s47\n\t"
        "s_add_u32 s82, s80, %[kx]\n\t"
        "s_add_u32 s83, s82, 0x1000\n\t"
        "buffer_load_dwordx4 v[0:3], v244, %[rd], s82 offen offset:0 sc1\n\t"
        "buffer_load_dwordx4 v[4:7], v244, %[rd], s82 offen offset:1024 sc1\n\t"
        "buffer_load_dwordx4 v[8:11], v244, %[rd], s82 offen offset:2048 sc1\n\t"
        "buffer_load_dwordx4 v[12:15], v244, %[rd], s82 offen offset:3072 sc1\n\t"
        "buffer_load_dwordx4 v[16:19], v244, %[rd], s83 offen offset:0 sc1\n\t"
        "buffer_load_dwordx4 v[20:23], v244, %[rd], s83 offen offset:1024 sc1\n\t"
        "buffer_load_dwordx4 v[24:27], v244, %[rd], s83 offen offset:2048 sc1\n\t"
        "buffer_load_dwordx4 v[28:31], v244, %[rd], s83 offen offset:3072 sc1\n\t"
        "s_cmp_eq_u64 s[42:43], -1\n\t"
        "s_cbranch_scc1 .La7%=\n\t"
        "s_sleep %c[aslp]\n\t"
        ".La7%=:\n\t"
        "s_sleep %c[bslp]\n\t"
        "s_add_u32 s82, s80, %[qx]\n\t"
        "s_add_u32 s83, s82, 0x1000\n\t"
        "buffer_load_dwordx4 v[32:35], v244, %[rd], s82 offen offset:0 sc1\n\t"
        "buffer_load_dwordx4 v[36:39], v244, %[rd], s82 offen offset:1024 sc1\n\t"
        "buffer_load_dwordx4 v[40:43], v244, %[rd], s82 offen offset:2048 sc1\n\t"
        "buffer_load_dwordx4 v[44:47], v244, %[rd], s82 offen offset:3072 sc1\n\t"
        "buffer_load_dwordx4 v[48:51], v244, %[rd], s83 offen offset:0 sc1\n\t"
        "buffer_load_dwordx4 v[52:55], v244, %[rd], s83 offen offset:1024 sc1\n\t"
        "buffer_load_dwordx4 v[56:59], v244, %[rd], s83 offen offset:2048 sc1\n\t"
        "buffer_load_dwordx4 v[60:63], v244, %[rd], s83 offen offset:3072 sc1\n\t"
        "s_cmp_eq_u64 s[42:43], -1\n\t"
        "s_cbranch_scc1 .La8%=\n\t"
        "s_sleep %c[aslp]\n\t"
        ".La8%=:\n\t"
        "s_sleep %c[bslp]\n\t"
        "buffer_load_dwordx4 v[96:99], v248, %[rd], s81 offen offset:0 sc1\n\t"
        "buffer_load_dwordx4 v[100:103], v248, %[rd], s81 offen offset:512 sc1\n\t"
        "buffer_load_dwordx4 v[104:107], v248, %[rd], s81 offen offset:1024 sc1\n\t"
        "buffer_load_dwordx4 v[108:111], v248, %[rd], s81 offen offset:1536 sc1\n\t"
        "buffer_load_dwordx2 v[112:113], v245, %[rd], s81 offen offset:2048 sc1\n\t"
        "buffer_load_dwordx2 v[114:115], v245, %[rd], s81 offen offset:2304 sc1\n\t"
        "buffer_load_dwordx2 v[116:117], v245, %[rd], s81 offen offset:2560 sc1\n\t"
        "buffer_load_dwordx2 v[118:119], v245, %[rd], s81 offen offset:2816 sc1\n\t"
        "buffer_load_dwordx2 v[120:121], v245, %[rd], s81 offen offset:3072 sc1\n\t"
        "buffer_load_dwordx2 v[122:123], v245, %[rd], s81 offen offset:3328 sc1\n\t"
        "buffer_load_dwordx2 v[124:125], v245, %[rd], s81 offen offset:3584 sc1\n\t"
        "buffer_load_dwordx2 v[126:127], v245, %[rd], s81 offen offset:3840 sc1\n\t"
        "s_add_u32 s82, s80, %[vx]\n\t"
        "s_add_u32 s83, s82, 0x1000\n\t"
        "buffer_load_dwordx4 v[64:67], v244, %[rd], s82 offen offset:0 sc1\n\t"
        "buffer_load_dwordx4 v[68:71], v244, %[rd], s82 offen offset:1024 sc1\n\t"
        "buffer_load_dwordx4 v[72:75], v244, %[rd], s82 offen offset:2048 sc1\n\t"
        "buffer_load_dwordx4 v[76:79], v244, %[rd], s82 offen offset:3072 sc1\n\t"
        "buffer_load_dwordx4 v[80:83], v244, %[rd], s83 offen offset:0 sc1\n\t"
        "buffer_load_dwordx4 v[84:87], v244, %[rd], s83 offen offset:1024 sc1\n\t"
        "buffer_load_dwordx4 v[88:91], v244, %[rd], s83 offen offset:2048 sc1\n\t"
        "buffer_load_dwordx4 v[92:95], v244, %[rd], s83 offen offset:3072 sc1\n\t"
        "s_cmp_eq_u64 s[42:43], -1\n\t"
        "s_cbranch_scc1 .La9%=\n\t"
        "s_sleep %c[aslp]\n\t"
        ".La9%=:\n\t"
        "s_sleep %c[bslp]\n\t"
        "s_waitcnt vmcnt(44)\n\t"
        "s_memrealtime s[98:99]\n\t"
        "v_mfma_f32_16x16x32_bf16 v[136:139], a[0:3], a[32:35], 0\n\t"
        "v_mfma_f32_16x16x32_bf16 v[148:151], a[0:3], a[48:51], 0\n\t"
        "v_mfma_f32_16x16x32_bf16 v[152:155], a[16:19], a[48:51], 0\n\t"
        "v_mfma_f32_16x16x32_bf16 v[136:139], a[4:7], a[36:39], v[136:139]\n\t"
        "v_mfma_f32_16x16x32_bf16 v[148:151], a[4:7], a[52:55], v[148:151]\n\t"
        "v_mfma_f32_16x16x32_bf16 v[152:155], a[20:23], a[52:55], v[152:155]\n\t"
        "v_mfma_f32_16x16x32_bf16 v[136:139], a[8:11], a[40:43], v[136:139]\n\t"
        "v_mfma_f32_16x16x32_bf16 v[148:151], a[8:11], a[56:59], v[148:151]\n\t"
        "v_mfma_f32_16x16x32_bf16 v[152:155], a[24:27], a[56:59], v[152:155]\n\t"
        "v_mfma_f32_16x16x32_bf16 v[136:139], a[12:15], a[44:47], v[136:139]\n\t"
        "v_mfma_f32_16x16x32_bf16 v[148:151], a[12:15], a[60:63], v[148:151]\n\t"
        "v_mfma_f32_16x16x32_bf16 v[152:155], a[28:31], a[60:63], v[152:155]\n\t"
        "s_waitcnt lgkmcnt(0)\n\t"
        "v_cmp_eq_u32_e32 vcc, %[tag], v240\n\t"
        "s_nop 1\n\t"
        "s_or_b64 s[42:43], s[42:43], vcc\n\t"
        "v_readfirstlane_b32 s80, v241\n\t"
        "s_nop 0\n\t"
        "s_cmp_gt_u32 s80, s40\n\t"
        "s_cselect_b32 s41, s98, s41\n\t"
        "s_max_u32 s40, s40, s80\n\t"
        "v_readlane_b32 s84, v242, 0\n\t"
        "v_readlane_b32 s85, v242, 32\n\t"
        "v_mov_b32_e32 v243, 32\n\t"
        "s_and_b32 s80, s45, 3\n\t"
        "s_lshl_b32 s80, s80, 12\n\t"
        "s_lshr_b32 s81, s45, 2\n\t"
        "s_lshl_b32 s81, s81, 3\n\t"
        "s_add_u32 s80, s80, s81\n\t"
        "s_lshr_b32 s82, s45, 1\n\t"
        "s_cmp_eq_u32 s82, 30\n\t"
        "s_addc_u32 s81, s45, 0\n\t"
        "s_and_b32 s81, s81, 1\n\t"
        "s_lshl_b32 s81, s81, 13\n\t"
        "s_lshl_b32 s82, s82, 2\n\t"
        "s_add_u32 s81, s81, s82\n\t"
        "s_cmp_lt_i32 s45, 56\n\t"
        "s_cselect_b32 s80, s80, s81\n\t"
        "s_cselect_b32 s81, 4, 0x1000\n\t"
        "s_add_u32 s81, s81, s80\n\t"
        "s_cmp_eq_u32 s84, 31\n\t"
        "s_cselect_b32 s80, s80, s36\n\t"
        "s_cmp_eq_u32 s85, 31\n\t"
        "s_cselect_b32 s81, s81, s36\n\t"
        "s_cmp_lt_i32 s45, 0\n\t"
        "s_cselect_b32 s80, s36, s80\n\t"
        "s_cselect_b32 s81, s36, s81\n\t"
        "buffer_store_dword v243, v251, %[rd], s80 offen sc1\n\t"
        "buffer_store_dword v243, v251, %[rd], s81 offen sc1\n\t"
        "s_add_u32 s86, s37, 8\n\t"
        "s_cmp_lg_u64 s[42:43], -1\n\t"
        "s_cselect_b32 s84, 0, s36\n\t"
        "s_cmp_gt_u32 s40, s86\n\t"
        "s_cselect_b32 s85, s36, s39\n\t"
        "buffer_load_dword v240, v246, %[rd], s84 offen sc1\n\t"
        "buffer_load_dword v241, v247, %[rd], s85 offen sc1\n\t"
        "v_cvt_pk_bf16_f32 v136, 0, v136\n\t"
        "v_cvt_pk_bf16_f32 v137, 0, v137\n\t"
        "v_cvt_pk_bf16_f32 v138, 0, v138\n\t"
        "v_cvt_pk_bf16_f32 v139, 0, v139\n\t"
        "v_mul_f32_e32 v136, 0x3db504f3, v136\n\t"
        "v_mul_f32_e32 v137, 0x3db504f3, v137\n\t"
        "v_mul_f32_e32 v138, 0x3db504f3, v138\n\t"
        "v_mul_f32_e32 v139, 0x3db504f3, v139\n\t"
        "v_cvt_pk_bf16_f32 v136, 0, v136\n\t"
        "v_cvt_pk_bf16_f32 v137, 0, v137\n\t"
        "v_cvt_pk_bf16_f32 v138, 0, v138\n\t"
        "v_cvt_pk_bf16_f32 v139, 0, v139\n\t"
        "v_cndmask_b32_e64 v136, v253, v136, s[48:49]\n\t"
        "v_cndmask_b32_e64 v137, v253, v137, s[50:51]\n\t"
        "v_cndmask_b32_e64 v138, v253, v138, s[52:53]\n\t"
        "v_cndmask_b32_e64 v139, v253, v139, s[54:55]\n\t"
        "v_cvt_pk_bf16_f32 v148, 0, v148\n\t"
        "v_cvt_pk_bf16_f32 v149, 0, v149\n\t"
        "v_cvt_pk_bf16_f32 v150, 0, v150\n\t"
        "v_cvt_pk_bf16_f32 v151, 0, v151\n\t"
        "v_mul_f32_e32 v148, 0x3db504f3, v148\n\t"
        "v_mul_f32_e32 v149, 0x3db504f3, v149\n\t"
        "v_mul_f32_e32 v150, 0x3db504f3, v150\n\t"
        "v_mul_f32_e32 v151, 0x3db504f3, v151\n\t"
        "v_cvt_pk_bf16_f32 v148, 0, v148\n\t"
        "v_cvt_pk_bf16_f32 v149, 0, v149\n\t"
        "v_cvt_pk_bf16_f32 v150, 0, v150\n\t"
        "v_cvt_pk_bf16_f32 v151, 0, v151\n\t"
        "v_cvt_pk_bf16_f32 v152, 0, v152\n\t"
        "v_cvt_pk_bf16_f32 v153, 0, v153\n\t"
        "v_cvt_pk_bf16_f32 v154, 0, v154\n\t"
        "v_cvt_pk_bf16_f32 v155, 0, v155\n\t"
        "v_mul_f32_e32 v152, 0x3db504f3, v152\n\t"
        "v_mul_f32_e32 v153, 0x3db504f3, v153\n\t"
        "v_mul_f32_e32 v154, 0x3db504f3, v154\n\t"
        "v_mul_f32_e32 v155, 0x3db504f3, v155\n\t"
        "v_cvt_pk_bf16_f32 v152, 0, v152\n\t"
        "v_cvt_pk_bf16_f32 v153, 0, v153\n\t"
        "v_cvt_pk_bf16_f32 v154, 0, v154\n\t"
        "v_cvt_pk_bf16_f32 v155, 0, v155\n\t"
        "v_cndmask_b32_e64 v152, v253, v152, s[48:49]\n\t"
        "v_cndmask_b32_e64 v153, v253, v153, s[50:51]\n\t"
        "v_cndmask_b32_e64 v154, v253, v154, s[52:53]\n\t"
        "v_cndmask_b32_e64 v155, v253, v155, s[54:55]\n\t"
        "v_max3_f32 v172, v136, v137, v253\n\t"
        "v_max3_f32 v172, v172, v138, v139\n\t"
        "v_mov_b32_e32 v140, v253\n\t"
        "v_max_f32_e32 v174, v148, v149\n\t"
        "v_max3_f32 v174, v174, v150, v151\n\t"
        "v_max3_f32 v174, v174, v152, v153\n\t"
        "v_max3_f32 v174, v174, v154, v155\n\t"
        "s_cmp_eq_u32 s39, 0\n\t"
        "s_cbranch_scc1 .La11%=\n\t"
        "s_waitcnt vmcnt(0)\n\t"
        "s_lshl_b32 s80, s44, 7\n\t"
        "s_cmp_lt_i32 s44, 0\n\t"
        "s_cselect_b32 s80, s36, s80\n\t"
        "v_mov_b32_e32 v242, 1\n\t"
        "buffer_atomic_add v242, v250, %[rd], s80 offen sc0 sc1\n\t"
        "s_waitcnt vmcnt(0)\n\t"
        "v_readlane_b32 s84, v242, 0\n\t"
        "v_readlane_b32 s85, v242, 32\n\t"
        "v_mov_b32_e32 v243, 32\n\t"
        "s_and_b32 s80, s44, 3\n\t"
        "s_lshl_b32 s80, s80, 12\n\t"
        "s_lshr_b32 s81, s44, 2\n\t"
        "s_lshl_b32 s81, s81, 3\n\t"
        "s_add_u32 s80, s80, s81\n\t"
        "s_lshr_b32 s82, s44, 1\n\t"
        "s_cmp_eq_u32 s82, 30\n\t"
        "s_addc_u32 s81, s44, 0\n\t"
        "s_and_b32 s81, s81, 1\n\t"
        "s_lshl_b32 s81, s81, 13\n\t"
        "s_lshl_b32 s82, s82, 2\n\t"
        "s_add_u32 s81, s81, s82\n\t"
        "s_cmp_lt_i32 s44, 56\n\t"
        "s_cselect_b32 s80, s80, s81\n\t"
        "s_cselect_b32 s81, 4, 0x1000\n\t"
        "s_add_u32 s81, s81, s80\n\t"
        "s_cmp_eq_u32 s84, 31\n\t"
        "s_cselect_b32 s80, s80, s36\n\t"
        "s_cmp_eq_u32 s85, 31\n\t"
        "s_cselect_b32 s81, s81, s36\n\t"
        "s_cmp_lt_i32 s44, 0\n\t"
        "s_cselect_b32 s80, s36, s80\n\t"
        "s_cselect_b32 s81, s36, s81\n\t"
        "buffer_store_dword v243, v251, %[rd], s80 offen sc1\n\t"
        "buffer_store_dword v243, v251, %[rd], s81 offen sc1\n\t"
        "s_mov_b32 s44, -1\n\t"
        "s_mov_b32 s97, 0\n\t"
        "s_min_u32 s87, s37, 62\n\t"
        ".La10%=:\n\t"
        "s_cmp_gt_u32 s40, s87\n\t"
        "s_cbranch_scc1 .La10%=x\n\t"
        "s_memrealtime s[98:99]\n\t"
        "s_add_u32 s97, s97, 1\n\t"
        "s_cmp_lt_u32 s97, 0x400000\n\t"
        "s_cbranch_scc0 .Lgu%=\n\t"
        "s_sub_u32 s80, s87, s40\n\t"
        "s_cmp_ge_u32 s87, %c[efrom]\n\t"
        "s_subb_u32 s80, s80, 0\n\t"
        "s_mul_i32 s80, s80, %c[smin]\n\t"
        "s_waitcnt lgkmcnt(0)\n\t"
        "s_sub_u32 s81, s98, s41\n\t"
        "s_cmp_ge_i32 s81, s80\n\t"
        "s_cbranch_scc1 .La10%=p\n\t"
        "s_sleep 16\n\t"
        "s_branch .La10%=\n\t"
        ".La10%=p:\n\t"
        "buffer_load_dword v241, v247, %[rd], 0 offen sc1\n\t"
        "s_waitcnt vmcnt(0)\n\t"
        "v_readfirstlane_b32 s80, v241\n\t"
        "s_nop 0\n\t"
        "s_cmp_gt_u32 s80, s40\n\t"
        "s_cselect_b32 s41, s98, s41\n\t"
        "s_max_u32 s40, s40, s80\n\t"
        "s_cmp_gt_u32 s40, s87\n\t"
        "s_cbranch_scc1 .La10%=x\n\t"
        "s_sleep %c[pslp]\n\t"
        "s_branch .La10%=\n\t"
        ".La10%=x:\n\t"
        "s_lshl_b32 s81, s37, 17\n\t"
        "s_add_u32 s81, s81, %[km]\n\t"
        "buffer_load_dwordx4 a[96:99], v248, %[rd], s81 offen offset:0 sc1\n\t"
        "buffer_load_dwordx4 a[100:103], v248, %[rd], s81 offen offset:512 sc1\n\t"
        "buffer_load_dwordx4 a[104:107], v248, %[rd], s81 offen offset:1024 sc1\n\t"
        "buffer_load_dwordx4 a[108:111], v248, %[rd], s81 offen offset:1536 sc1\n\t"
        "buffer_load_dwordx2 a[112:113], v245, %[rd], s81 offen offset:2048 sc1\n\t"
        "buffer_load_dwordx2 a[114:115], v245, %[rd], s81 offen offset:2304 sc1\n\t"
        "buffer_load_dwordx2 a[116:117], v245, %[rd], s81 offen offset:2560 sc1\n\t"
        "buffer_load_dwordx2 a[118:119], v245, %[rd], s81 offen offset:2816 sc1\n\t"
        "buffer_load_dwordx2 a[120:121], v245, %[rd], s81 offen offset:3072 sc1\n\t"
        "buffer_load_dwordx2 a[122:123], v245, %[rd], s81 offen offset:3328 sc1\n\t"
        "buffer_load_dwordx2 a[124:125], v245, %[rd], s81 offen offset:3584 sc1\n\t"
        "buffer_load_dwordx2 a[126:127], v245, %[rd], s81 offen offset:3840 sc1\n\t"
        "s_waitcnt vmcnt(0)\n\t"
        ".La11%=:\n\t"
        "v_mfma_f32_16x16x32_bf16 v[144:147], a[96:99], a[32:35], 0\n\t"
        "v_mfma_f32_16x16x32_bf16 v[156:159], a[96:99], a[48:51], 0\n\t"
        "v_mfma_f32_16x16x32_bf16 v[144:147], a[100:103], a[36:39], v[144:147]\n\t"
        "v_mfma_f32_16x16x32_bf16 v[156:159], a[100:103], a[52:55], v[156:159]\n\t"
        "v_mfma_f32_16x16x32_bf16 v[144:147], a[104:107], a[40:43], v[144:147]\n\t"
        "v_mfma_f32_16x16x32_bf16 v[156:159], a[104:107], a[56:59], v[156:159]\n\t"
        "v_mfma_f32_16x16x32_bf16 v[144:147], a[108:111], a[44:47], v[144:147]\n\t"
        "v_mfma_f32_16x16x32_bf16 v[156:159], a[108:111], a[60:63], v[156:159]\n\t"
        "s_nop 1\n\t"
        "v_mov_b32_e32 v252, v252\n\t"
        "s_nop 1\n\t"
        "v_mov_b32_e32 v252, v252\n\t"
        "s_nop 0\n\t"
        "v_cvt_pk_bf16_f32 v144, 0, v144\n\t"
        "v_cvt_pk_bf16_f32 v145, 0, v145\n\t"
        "v_cvt_pk_bf16_f32 v146, 0, v146\n\t"
        "v_cvt_pk_bf16_f32 v147, 0, v147\n\t"
        "v_mul_f32_e32 v144, 0x3db504f3, v144\n\t"
        "v_mul_f32_e32 v145, 0x3db504f3, v145\n\t"
        "v_mul_f32_e32 v146, 0x3db504f3, v146\n\t"
        "v_mul_f32_e32 v147, 0x3db504f3, v147\n\t"
        "v_cvt_pk_bf16_f32 v144, 0, v144\n\t"
        "v_cvt_pk_bf16_f32 v145, 0, v145\n\t"
        "v_cvt_pk_bf16_f32 v146, 0, v146\n\t"
        "v_cvt_pk_bf16_f32 v147, 0, v147\n\t"
        "v_cndmask_b32_e64 v144, v144, v254, s[56:57]\n\t"
        "v_cndmask_b32_e64 v145, v145, v254, s[56:57]\n\t"
        "v_cndmask_b32_e64 v146, v146, v254, s[56:57]\n\t"
        "v_cndmask_b32_e64 v147, v147, v254, s[56:57]\n\t"
        "v_max3_f32 v172, v172, v144, v145\n\t"
        "v_max3_f32 v173, v172, v146, v147\n\t"
        "v_max3_f32 v172, v172, v146, v147\n\t"
        "s_nop 1\n\t"
        "v_permlane16_swap_b32_e32 v172, v173\n\t"
        "v_max_f32_e32 v172, v172, v173\n\t"
        "v_mov_b32_e32 v173, v172\n\t"
        "s_nop 1\n\t"
        "v_permlane32_swap_b32_e32 v172, v173\n\t"
        "v_max_f32_e32 v172, v172, v173\n\t"
        "v_sub_f32_e32 v136, v136, v172\n\t"
        "v_sub_f32_e32 v137, v137, v172\n\t"
        "v_sub_f32_e32 v138, v138, v172\n\t"
        "v_sub_f32_e32 v139, v139, v172\n\t"
        "v_sub_f32_e32 v140, v140, v172\n\t"
        "v_sub_f32_e32 v144, v144, v172\n\t"
        "v_sub_f32_e32 v145, v145, v172\n\t"
        "v_sub_f32_e32 v146, v146, v172\n\t"
        "v_sub_f32_e32 v147, v147, v172\n\t"
        "v_mul_f32_e32 v136, 0x3fb8aa3b, v136\n\t"
        "v_mul_f32_e32 v137, 0x3fb8aa3b, v137\n\t"
        "v_mul_f32_e32 v138, 0x3fb8aa3b, v138\n\t"
        "v_mul_f32_e32 v139, 0x3fb8aa3b, v139\n\t"
        "v_mul_f32_e32 v140, 0x3fb8aa3b, v140\n\t"
        "v_mul_f32_e32 v144, 0x3fb8aa3b, v144\n\t"
        "v_mul_f32_e32 v145, 0x3fb8aa3b, v145\n\t"
        "v_mul_f32_e32 v146, 0x3fb8aa3b, v146\n\t"
        "v_mul_f32_e32 v147, 0x3fb8aa3b, v147\n\t"
        "v_exp_f32_e32 v136, v136\n\t"
        "v_exp_f32_e32 v137, v137\n\t"
        "v_exp_f32_e32 v138, v138\n\t"
        "v_exp_f32_e32 v139, v139\n\t"
        "v_exp_f32_e32 v140, v140\n\t"
        "v_exp_f32_e32 v144, v144\n\t"
        "v_exp_f32_e32 v145, v145\n\t"
        "v_exp_f32_e32 v146, v146\n\t"
        "v_exp_f32_e32 v147, v147\n\t"
        "v_mul_f32_e32 v172, 4.0, v140\n\t"
        "v_add_f32_e32 v172, v172, v136\n\t"
        "v_add_f32_e32 v172, v172, v137\n\t"
        "v_add_f32_e32 v172, v172, v138\n\t"
        "v_add_f32_e32 v172, v172, v139\n\t"
        "v_add_f32_e32 v172, v172, v144\n\t"
        "v_add_f32_e32 v172, v172, v145\n\t"
        "v_add_f32_e32 v172, v172, v146\n\t"
        "v_add_f32_e32 v172, v172, v147\n\t"
        "v_mov_b32_e32 v173, v172\n\t"
        "s_nop 1\n\t"
        "v_permlane16_swap_b32_e32 v172, v173\n\t"
        "v_add_f32_e32 v172, v172, v173\n\t"
        "v_mov_b32_e32 v173, v172\n\t"
        "s_nop 1\n\t"
        "v_permlane32_swap_b32_e32 v172, v173\n\t"
        "v_add_f32_e32 v172, v172, v173\n\t"
        "v_rcp_f32_e32 v172, v172\n\t"
        "s_nop 0\n\t"
        "v_mul_f32_e32 v136, v136, v172\n\t"
        "v_mul_f32_e32 v137, v137, v172\n\t"
        "v_mul_f32_e32 v138, v138, v172\n\t"
        "v_mul_f32_e32 v139, v139, v172\n\t"
        "v_mul_f32_e32 v140, v140, v172\n\t"
        "v_mul_f32_e32 v144, v144, v172\n\t"
        "v_mul_f32_e32 v145, v145, v172\n\t"
        "v_mul_f32_e32 v146, v146, v172\n\t"
        "v_mul_f32_e32 v147, v147, v172\n\t"
        "v_cvt_pk_bf16_f32 v160, v136, v137\n\t"
        "v_cvt_pk_bf16_f32 v161, v138, v139\n\t"
        "v_cvt_pk_bf16_f32 v162, v140, v140\n\t"
        "v_cvt_pk_bf16_f32 v163, v140, v140\n\t"
        "v_cvt_pk_bf16_f32 v164, v144, v145\n\t"
        "v_cvt_pk_bf16_f32 v165, v146, v147\n\t"
        "v_mfma_f32_16x16x32_bf16 v[176:179], a[64:67], v[160:163], 0\n\t"
        "v_cvt_pk_bf16_f32 v156, 0, v156\n\t"
        "v_cvt_pk_bf16_f32 v157, 0, v157\n\t"
        "v_cvt_pk_bf16_f32 v158, 0, v158\n\t"
        "v_mfma_f32_16x16x32_bf16 v[180:183], a[68:71], v[160:163], 0\n\t"
        "v_cvt_pk_bf16_f32 v159, 0, v159\n\t"
        "v_mul_f32_e32 v156, 0x3db504f3, v156\n\t"
        "v_mul_f32_e32 v157, 0x3db504f3, v157\n\t"
        "v_mfma_f32_16x16x32_bf16 v[184:187], a[72:75], v[160:163], 0\n\t"
        "v_mul_f32_e32 v158, 0x3db504f3, v158\n\t"
        "v_mul_f32_e32 v159, 0x3db504f3, v159\n\t"
        "v_cvt_pk_bf16_f32 v156, 0, v156\n\t"
        "v_mfma_f32_16x16x32_bf16 v[188:191], a[76:79], v[160:163], 0\n\t"
        "v_cvt_pk_bf16_f32 v157, 0, v157\n\t"
        "v_cvt_pk_bf16_f32 v158, 0, v158\n\t"
        "v_cvt_pk_bf16_f32 v159, 0, v159\n\t"
        "v_mfma_f32_16x16x32_bf16 v[192:195], a[80:83], v[160:163], 0\n\t"
        "v_cndmask_b32_e64 v156, v156, v254, s[56:57]\n\t"
        "v_cndmask_b32_e64 v157, v157, v254, s[56:57]\n\t"
        "v_cndmask_b32_e64 v158, v158, v254, s[56:57]\n\t"
        "v_mfma_f32_16x16x32_bf16 v[196:199], a[84:87], v[160:163], 0\n\t"
        "v_cndmask_b32_e64 v159, v159, v254, s[56:57]\n\t"
        "v_max3_f32 v174, v174, v156, v157\n\t"
        "v_max3_f32 v175, v174, v158, v159\n\t"
        "v_mfma_f32_16x16x32_bf16 v[200:203], a[88:91], v[160:163], 0\n\t"
        "v_max3_f32 v174, v174, v158, v159\n\t"
        "s_nop 1\n\t"
        "v_permlane16_swap_b32_e32 v174, v175\n\t"
        "v_max_f32_e32 v174, v174, v175\n\t"
        "v_mfma_f32_16x16x32_bf16 v[204:207], a[92:95], v[160:163], 0\n\t"
        "v_mov_b32_e32 v175, v174\n\t"
        "s_nop 1\n\t"
        "v_permlane32_swap_b32_e32 v174, v175\n\t"
        "v_max_f32_e32 v174, v174, v175\n\t"
        "v_mfma_f32_16x16x16_bf16 v[176:179], a[112:113], v[164:165], v[176:179]\n\t"
        "v_sub_f32_e32 v148, v148, v174\n\t"
        "v_sub_f32_e32 v149, v149, v174\n\t"
        "v_sub_f32_e32 v150, v150, v174\n\t"
        "v_mfma_f32_16x16x16_bf16 v[180:183], a[114:115], v[164:165], v[180:183]\n\t"
        "v_sub_f32_e32 v151, v151, v174\n\t"
        "v_sub_f32_e32 v152, v152, v174\n\t"
        "v_sub_f32_e32 v153, v153, v174\n\t"
        "v_mfma_f32_16x16x16_bf16 v[184:187], a[116:117], v[164:165], v[184:187]\n\t"
        "v_sub_f32_e32 v154, v154, v174\n\t"
        "v_sub_f32_e32 v155, v155, v174\n\t"
        "v_sub_f32_e32 v156, v156, v174\n\t"
        "v_mfma_f32_16x16x16_bf16 v[188:191], a[118:119], v[164:165], v[188:191]\n\t"
        "v_sub_f32_e32 v157, v157, v174\n\t"
        "v_sub_f32_e32 v158, v158, v174\n\t"
        "v_sub_f32_e32 v159, v159, v174\n\t"
        "v_mfma_f32_16x16x16_bf16 v[192:195], a[120:121], v[164:165], v[192:195]\n\t"
        "v_mul_f32_e32 v148, 0x3fb8aa3b, v148\n\t"
        "v_mul_f32_e32 v149, 0x3fb8aa3b, v149\n\t"
        "v_mul_f32_e32 v150, 0x3fb8aa3b, v150\n\t"
        "v_mfma_f32_16x16x16_bf16 v[196:199], a[122:123], v[164:165], v[196:199]\n\t"
        "v_mul_f32_e32 v151, 0x3fb8aa3b, v151\n\t"
        "v_mul_f32_e32 v152, 0x3fb8aa3b, v152\n\t"
        "v_mul_f32_e32 v153, 0x3fb8aa3b, v153\n\t"
        "v_mfma_f32_16x16x16_bf16 v[200:203], a[124:125], v[164:165], v[200:203]\n\t"
        "v_mul_f32_e32 v154, 0x3fb8aa3b, v154\n\t"
        "v_mul_f32_e32 v155, 0x3fb8aa3b, v155\n\t"
        "v_mul_f32_e32 v156, 0x3fb8aa3b, v156\n\t"
        "v_mfma_f32_16x16x16_bf16 v[204:207], a[126:127], v[164:165], v[204:207]\n\t"
        "v_mul_f32_e32 v157, 0x3fb8aa3b, v157\n\t"
        "v_mul_f32_e32 v158, 0x3fb8aa3b, v158\n\t"
        "v_mul_f32_e32 v159, 0x3fb8aa3b, v159\n\t"
        "v_exp_f32_e32 v148, v148\n\t"
        "v_exp_f32_e32 v149, v149\n\t"
        "v_exp_f32_e32 v150, v150\n\t"
        "v_exp_f32_e32 v151, v151\n\t"
        "v_exp_f32_e32 v152, v152\n\t"
        "v_exp_f32_e32 v153, v153\n\t"
        "v_exp_f32_e32 v154, v154\n\t"
        "v_exp_f32_e32 v155, v155\n\t"
        "v_exp_f32_e32 v156, v156\n\t"
        "v_exp_f32_e32 v157, v157\n\t"
        "v_exp_f32_e32 v158, v158\n\t"
        "v_exp_f32_e32 v159, v159\n\t"
        "v_add_f32_e32 v174, v148, v149\n\t"
        "v_add_f32_e32 v174, v174, v150\n\t"
        "v_add_f32_e32 v174, v174, v151\n\t"
        "v_add_f32_e32 v174, v174, v152\n\t"
        "v_add_f32_e32 v174, v174, v153\n\t"
        "v_add_f32_e32 v174, v174, v154\n\t"
        "v_add_f32_e32 v174, v174, v155\n\t"
        "v_add_f32_e32 v174, v174, v156\n\t"
        "v_add_f32_e32 v174, v174, v157\n\t"
        "v_add_f32_e32 v174, v174, v158\n\t"
        "v_add_f32_e32 v174, v174, v159\n\t"
        "v_mov_b32_e32 v175, v174\n\t"
        "s_nop 1\n\t"
        "v_permlane16_swap_b32_e32 v174, v175\n\t"
        "v_add_f32_e32 v174, v174, v175\n\t"
        "v_mov_b32_e32 v175, v174\n\t"
        "s_nop 1\n\t"
        "v_permlane32_swap_b32_e32 v174, v175\n\t"
        "v_add_f32_e32 v174, v174, v175\n\t"
        "v_rcp_f32_e32 v174, v174\n\t"
        "s_nop 0\n\t"
        "v_mul_f32_e32 v148, v148, v174\n\t"
        "v_mul_f32_e32 v149, v149, v174\n\t"
        "v_mul_f32_e32 v150, v150, v174\n\t"
        "v_mul_f32_e32 v151, v151, v174\n\t"
        "v_mul_f32_e32 v152, v152, v174\n\t"
        "v_mul_f32_e32 v153, v153, v174\n\t"
        "v_mul_f32_e32 v154, v154, v174\n\t"
        "v_mul_f32_e32 v155, v155, v174\n\t"
        "v_mul_f32_e32 v156, v156, v174\n\t"
        "v_mul_f32_e32 v157, v157, v174\n\t"
        "v_mul_f32_e32 v158, v158, v174\n\t"
        "v_mul_f32_e32 v159, v159, v174\n\t"
        "v_cvt_pk_bf16_f32 v166, v148, v149\n\t"
        "v_cvt_pk_bf16_f32 v167, v150, v151\n\t"
        "v_cvt_pk_bf16_f32 v168, v152, v153\n\t"
        "v_cvt_pk_bf16_f32 v169, v154, v155\n\t"
        "v_cvt_pk_bf16_f32 v170, v156, v157\n\t"
        "v_cvt_pk_bf16_f32 v171, v158, v159\n\t"
        "s_waitcnt vmcnt(40)\n\t"
        "s_lshl_b32 s80, s44, 7\n\t"
        "s_cmp_lt_i32 s44, 0\n\t"
        "s_cselect_b32 s80, s36, s80\n\t"
        "v_mov_b32_e32 v242, 1\n\t"
        "buffer_atomic_add v242, v250, %[rd], s80 offen sc0 sc1\n\t"
        "s_lshl_b32 s86, s37, 18\n\t"
        "s_add_u32 s86, s86, %[qx]\n\t"
        "s_add_u32 s87, s86, 0x1000\n\t"
        "v_mfma_f32_16x16x32_bf16 v[208:211], a[64:67], v[166:169], 0\n\t"
        "v_cvt_pk_bf16_f32 v176, v176, v177\n\t"
        "v_cvt_pk_bf16_f32 v177, v178, v179\n\t"
        "v_mfma_f32_16x16x32_bf16 v[212:215], a[68:71], v[166:169], 0\n\t"
        "v_cvt_pk_bf16_f32 v178, v180, v181\n\t"
        "v_cvt_pk_bf16_f32 v179, v182, v183\n\t"
        "v_mfma_f32_16x16x32_bf16 v[216:219], a[72:75], v[166:169], 0\n\t"
        "buffer_store_dwordx4 v[176:179], v244, %[rd], s86 offen offset:0 sc1\n\t"
        "v_cvt_pk_bf16_f32 v184, v184, v185\n\t"
        "v_mfma_f32_16x16x32_bf16 v[220:223], a[76:79], v[166:169], 0\n\t"
        "v_cvt_pk_bf16_f32 v185, v186, v187\n\t"
        "v_cvt_pk_bf16_f32 v186, v188, v189\n\t"
        "v_mfma_f32_16x16x32_bf16 v[224:227], a[80:83], v[166:169], 0\n\t"
        "v_cvt_pk_bf16_f32 v187, v190, v191\n\t"
        "buffer_store_dwordx4 v[184:187], v244, %[rd], s86 offen offset:1024 sc1\n\t"
        "v_mfma_f32_16x16x32_bf16 v[228:231], a[84:87], v[166:169], 0\n\t"
        "v_cvt_pk_bf16_f32 v192, v192, v193\n\t"
        "v_cvt_pk_bf16_f32 v193, v194, v195\n\t"
        "v_mfma_f32_16x16x32_bf16 v[232:235], a[88:91], v[166:169], 0\n\t"
        "v_cvt_pk_bf16_f32 v194, v196, v197\n\t"
        "v_cvt_pk_bf16_f32 v195, v198, v199\n\t"
        "v_mfma_f32_16x16x32_bf16 v[236:239], a[92:95], v[166:169], 0\n\t"
        "buffer_store_dwordx4 v[192:195], v244, %[rd], s86 offen offset:2048 sc1\n\t"
        "v_cvt_pk_bf16_f32 v200, v200, v201\n\t"
        "v_mfma_f32_16x16x16_bf16 v[208:211], a[112:113], v[170:171], v[208:211]\n\t"
        "v_cvt_pk_bf16_f32 v201, v202, v203\n\t"
        "v_cvt_pk_bf16_f32 v202, v204, v205\n\t"
        "v_mfma_f32_16x16x16_bf16 v[212:215], a[114:115], v[170:171], v[212:215]\n\t"
        "v_cvt_pk_bf16_f32 v203, v206, v207\n\t"
        "buffer_store_dwordx4 v[200:203], v244, %[rd], s86 offen offset:3072 sc1\n\t"
        "v_mfma_f32_16x16x16_bf16 v[216:219], a[116:117], v[170:171], v[216:219]\n\t"
        "v_mfma_f32_16x16x16_bf16 v[220:223], a[118:119], v[170:171], v[220:223]\n\t"
        "v_mfma_f32_16x16x16_bf16 v[224:227], a[120:121], v[170:171], v[224:227]\n\t"
        "v_mfma_f32_16x16x16_bf16 v[228:231], a[122:123], v[170:171], v[228:231]\n\t"
        "v_mfma_f32_16x16x16_bf16 v[232:235], a[124:125], v[170:171], v[232:235]\n\t"
        "v_mfma_f32_16x16x16_bf16 v[236:239], a[126:127], v[170:171], v[236:239]\n\t"
        "v_cvt_pk_bf16_f32 v208, v208, v209\n\t"
        "v_cvt_pk_bf16_f32 v209, v210, v211\n\t"
        "v_cvt_pk_bf16_f32 v210, v212, v213\n\t"
        "v_cvt_pk_bf16_f32 v211, v214, v215\n\t"
        "buffer_store_dwordx4 v[208:211], v244, %[rd], s87 offen offset:0 sc1\n\t"
        "v_cvt_pk_bf16_f32 v216, v216, v217\n\t"
        "v_cvt_pk_bf16_f32 v217, v218, v219\n\t"
        "v_cvt_pk_bf16_f32 v218, v220, v221\n\t"
        "v_cvt_pk_bf16_f32 v219, v222, v223\n\t"
        "buffer_store_dwordx4 v[216:219], v244, %[rd], s87 offen offset:1024 sc1\n\t"
        "v_cvt_pk_bf16_f32 v224, v224, v225\n\t"
        "v_cvt_pk_bf16_f32 v225, v226, v227\n\t"
        "v_cvt_pk_bf16_f32 v226, v228, v229\n\t"
        "v_cvt_pk_bf16_f32 v227, v230, v231\n\t"
        "buffer_store_dwordx4 v[224:227], v244, %[rd], s87 offen offset:2048 sc1\n\t"
        "v_cvt_pk_bf16_f32 v232, v232, v233\n\t"
        "v_cvt_pk_bf16_f32 v233, v234, v235\n\t"
        "v_cvt_pk_bf16_f32 v234, v236, v237\n\t"
        "v_cvt_pk_bf16_f32 v235, v238, v239\n\t"
        "buffer_store_dwordx4 v[232:235], v244, %[rd], s87 offen offset:3072 sc1\n\t"
        "s_mov_b32 s45, s44\n\t"
        "s_mov_b32 s44, s37\n\t"
        "s_add_u32 s80, s37, 4\n\t"
        "s_cmp_gt_u32 s40, s80\n\t"
        "s_cselect_b32 s80, s46, 1\n\t"
        "s_cmp_eq_u32 s80, 0\n\t"
        "s_cbranch_scc1 .La12%=\n\t"
        "s_waitcnt vmcnt(0)\n\t"
        "v_readlane_b32 s84, v242, 0\n\t"
        "v_readlane_b32 s85, v242, 32\n\t"
        "v_mov_b32_e32 v243, 32\n\t"
        "s_and_b32 s80, s45, 3\n\t"
        "s_lshl_b32 s80, s80, 12\n\t"
        "s_lshr_b32 s81, s45, 2\n\t"
        "s_lshl_b32 s81, s81, 3\n\t"
        "s_add_u32 s80, s80, s81\n\t"
        "s_lshr_b32 s82, s45, 1\n\t"
        "s_cmp_eq_u32 s82, 30\n\t"
        "s_addc_u32 s81, s45, 0\n\t"
        "s_and_b32 s81, s81, 1\n\t"
        "s_lshl_b32 s81, s81, 13\n\t"
        "s_lshl_b32 s82, s82, 2\n\t"
        "s_add_u32 s81, s81, s82\n\t"
        "s_cmp_lt_i32 s45, 56\n\t"
        "s_cselect_b32 s80, s80, s81\n\t"
        "s_cselect_b32 s81, 4, 0x1000\n\t"
        "s_add_u32 s81, s81, s80\n\t"
        "s_cmp_eq_u32 s84, 31\n\t"
        "s_cselect_b32 s80, s80, s36\n\t"
        "s_cmp_eq_u32 s85, 31\n\t"
        "s_cselect_b32 s81, s81, s36\n\t"
        "s_cmp_lt_i32 s45, 0\n\t"
        "s_cselect_b32 s80, s36, s80\n\t"
        "s_cselect_b32 s81, s36, s81\n\t"
        "buffer_store_dword v243, v251, %[rd], s80 offen sc1\n\t"
        "buffer_store_dword v243, v251, %[rd], s81 offen sc1\n\t"
        "s_lshl_b32 s80, s44, 7\n\t"
        "s_cmp_lt_i32 s44, 0\n\t"
        "s_cselect_b32 s80, s36, s80\n\t"
        "v_mov_b32_e32 v242, 1\n\t"
        "buffer_atomic_add v242, v250, %[rd], s80 offen sc0 sc1\n\t"
        "s_waitcnt vmcnt(0)\n\t"
        "v_readlane_b32 s84, v242, 0\n\t"
        "v_readlane_b32 s85, v242, 32\n\t"
        "v_mov_b32_e32 v243, 32\n\t"
        "s_and_b32 s80, s44, 3\n\t"
        "s_lshl_b32 s80, s80, 12\n\t"
        "s_lshr_b32 s81, s44, 2\n\t"
        "s_lshl_b32 s81, s81, 3\n\t"
        "s_add_u32 s80, s80, s81\n\t"
        "s_lshr_b32 s82, s44, 1\n\t"
        "s_cmp_eq_u32 s82, 30\n\t"
        "s_addc_u32 s81, s44, 0\n\t"
        "s_and_b32 s81, s81, 1\n\t"
        "s_lshl_b32 s81, s81, 13\n\t"
        "s_lshl_b32 s82, s82, 2\n\t"
        "s_add_u32 s81, s81, s82\n\t"
        "s_cmp_lt_i32 s44, 56\n\t"
        "s_cselect_b32 s80, s80, s81\n\t"
        "s_cselect_b32 s81, 4, 0x1000\n\t"
        "s_add_u32 s81, s81, s80\n\t"
        "s_cmp_eq_u32 s84, 31\n\t"
        "s_cselect_b32 s80, s80, s36\n\t"
        "s_cmp_eq_u32 s85, 31\n\t"
        "s_cselect_b32 s81, s81, s36\n\t"
        "s_cmp_lt_i32 s44, 0\n\t"
        "s_cselect_b32 s80, s36, s80\n\t"
        "s_cselect_b32 s81, s36, s81\n\t"
        "buffer_store_dword v243, v251, %[rd], s80 offen sc1\n\t"
        "buffer_store_dword v243, v251, %[rd], s81 offen sc1\n\t"
        "s_mov_b32 s44, -1\n\t"
        "s_mov_b32 s45, -1\n\t"
        ".La12%=:\n\t"
        "s_mov_b32 s38, s46\n\t"
        "s_mov_b32 s39, s47\n\t"
        "s_add_u32 s37, s37, 4\n\t"
        "s_cmp_eq_u32 s38, 0\n\t"
        "s_cbranch_scc1 .La14%=\n\t"
        "s_mov_b32 s97, 0\n\t"
        ".La13%=:\n\t"
        "buffer_load_dword v240, v246, %[rd], 0 offen sc1\n\t"
        "buffer_load_dword v241, v247, %[rd], 0 offen sc1\n\t"
        "s_add_u32 s97, s97, 1\n\t"
        "s_cmp_lt_u32 s97, 0x200000\n\t"
        "s_cbranch_scc0 .Lgu%=\n\t"
        "s_memrealtime s[98:99]\n\t"
        "s_waitcnt vmcnt(0) lgkmcnt(0)\n\t"
        "v_cmp_eq_u32_e32 vcc, %[tag], v240\n\t"
        "s_nop 1\n\t"
        "s_or_b64 s[42:43], s[42:43], vcc\n\t"
        "v_readfirstlane_b32 s80, v241\n\t"
        "s_nop 0\n\t"
        "s_cmp_gt_u32 s80, s40\n\t"
        "s_cselect_b32 s41, s98, s41\n\t"
        "s_max_u32 s40, s40, s80\n\t"
        "s_and_b32 s80, s37, 60\n\t"
        "s_lshr_b64 s[80:81], s[42:43], s80\n\t"
        "s_andn2_b32 s80, 15, s80\n\t"
        "s_cbranch_scc0 .La13%=x\n\t"
        "s_sleep 16\n\t"
        "s_branch .La13%=\n\t"
        ".La13%=x:\n\t"
        "s_cmp_gt_u32 s40, s37\n\t"
        "s_cselect_b32 s39, 0, s36\n\t"
        "s_lshl_b32 s80, s37, 18\n\t"
        "s_or_b32 s80, s80, 0\n\t"
        "s_lshl_b32 s81, s37, 17\n\t"
        "s_add_u32 s81, s81, %[km]\n\t"
        "s_or_b32 s81, s81, s39\n\t"
        "s_add_u32 s82, s80, %[kx]\n\t"
        "s_add_u32 s83, s82, 0x1000\n\t"
        "buffer_load_dwordx4 v[0:3], v244, %[rd], s82 offen offset:0 sc1\n\t"
        "buffer_load_dwordx4 v[4:7], v244, %[rd], s82 offen offset:1024 sc1\n\t"
        "buffer_load_dwordx4 v[8:11], v244, %[rd], s82 offen offset:2048 sc1\n\t"
        "buffer_load_dwordx4 v[12:15], v244, %[rd], s82 offen offset:3072 sc1\n\t"
        "buffer_load_dwordx4 v[16:19], v244, %[rd], s83 offen offset:0 sc1\n\t"
        "buffer_load_dwordx4 v[20:23], v244, %[rd], s83 offen offset:1024 sc1\n\t"
        "buffer_load_dwordx4 v[24:27], v244, %[rd], s83 offen offset:2048 sc1\n\t"
        "buffer_load_dwordx4 v[28:31], v244, %[rd], s83 offen offset:3072 sc1\n\t"
        "s_cmp_eq_u64 s[42:43], -1\n\t"
        "s_cbranch_scc1 .La15%=\n\t"
        "s_sleep %c[aslp]\n\t"
        ".La15%=:\n\t"
        "s_sleep %c[bslp]\n\t"
        "s_add_u32 s82, s80, %[qx]\n\t"
        "s_add_u32 s83, s82, 0x1000\n\t"
        "buffer_load_dwordx4 v[32:35], v244, %[rd], s82 offen offset:0 sc1\n\t"
        "buffer_load_dwordx4 v[36:39], v244, %[rd], s82 offen offset:1024 sc1\n\t"
        "buffer_load_dwordx4 v[40:43], v244, %[rd], s82 offen offset:2048 sc1\n\t"
        "buffer_load_dwordx4 v[44:47], v244, %[rd], s82 offen offset:3072 sc1\n\t"
        "buffer_load_dwordx4 v[48:51], v244, %[rd], s83 offen offset:0 sc1\n\t"
        "buffer_load_dwordx4 v[52:55], v244, %[rd], s83 offen offset:1024 sc1\n\t"
        "buffer_load_dwordx4 v[56:59], v244, %[rd], s83 offen offset:2048 sc1\n\t"
        "buffer_load_dwordx4 v[60:63], v244, %[rd], s83 offen offset:3072 sc1\n\t"
        "s_cmp_eq_u64 s[42:43], -1\n\t"
        "s_cbranch_scc1 .La16%=\n\t"
        "s_sleep %c[aslp]\n\t"
        ".La16%=:\n\t"
        "s_sleep %c[bslp]\n\t"
        "buffer_load_dwordx4 v[96:99], v248, %[rd], s81 offen offset:0 sc1\n\t"
        "buffer_load_dwordx4 v[100:103], v248, %[rd], s81 offen offset:512 sc1\n\t"
        "buffer_load_dwordx4 v[104:107], v248, %[rd], s81 offen offset:1024 sc1\n\t"
        "buffer_load_dwordx4 v[108:111], v248, %[rd], s81 offen offset:1536 sc1\n\t"
        "buffer_load_dwordx2 v[112:113], v245, %[rd], s81 offen offset:2048 sc1\n\t"
        "buffer_load_dwordx2 v[114:115], v245, %[rd], s81 offen offset:2304 sc1\n\t"
        "buffer_load_dwordx2 v[116:117], v245, %[rd], s81 offen offset:2560 sc1\n\t"
        "buffer_load_dwordx2 v[118:119], v245, %[rd], s81 offen offset:2816 sc1\n\t"
        "buffer_load_dwordx2 v[120:121], v245, %[rd], s81 offen offset:3072 sc1\n\t"
        "buffer_load_dwordx2 v[122:123], v245, %[rd], s81 offen offset:3328 sc1\n\t"
        "buffer_load_dwordx2 v[124:125], v245, %[rd], s81 offen offset:3584 sc1\n\t"
        "buffer_load_dwordx2 v[126:127], v245, %[rd], s81 offen offset:3840 sc1\n\t"
        "s_add_u32 s82, s80, %[vx]\n\t"
        "s_add_u32 s83, s82, 0x1000\n\t"
        "buffer_load_dwordx4 v[64:67], v244, %[rd], s82 offen offset:0 sc1\n\t"
        "buffer_load_dwordx4 v[68:71], v244, %[rd], s82 offen offset:1024 sc1\n\t"
        "buffer_load_dwordx4 v[72:75], v244, %[rd], s82 offen offset:2048 sc1\n\t"
        "buffer_load_dwordx4 v[76:79], v244, %[rd], s82 offen offset:3072 sc1\n\t"
        "buffer_load_dwordx4 v[80:83], v244, %[rd], s83 offen offset:0 sc1\n\t"
        "buffer_load_dwordx4 v[84:87], v244, %[rd], s83 offen offset:1024 sc1\n\t"
        "buffer_load_dwordx4 v[88:91], v244, %[rd], s83 offen offset:2048 sc1\n\t"
        "buffer_load_dwordx4 v[92:95], v244, %[rd], s83 offen offset:3072 sc1\n\t"
        "s_cmp_eq_u64 s[42:43], -1\n\t"
        "s_cbranch_scc1 .La17%=\n\t"
        "s_sleep %c[aslp]\n\t"
        ".La17%=:\n\t"
        "s_sleep %c[bslp]\n\t"
        "buffer_store_dword v0, off, %[rd], s36\n\t"
        "buffer_store_dword v0, off, %[rd], s36\n\t"
        "buffer_store_dword v0, off, %[rd], s36\n\t"
        "buffer_store_dword v0, off, %[rd], s36\n\t"
        "buffer_store_dword v0, off, %[rd], s36\n\t"
        "buffer_store_dword v0, off, %[rd], s36\n\t"
        "buffer_store_dword v0, off, %[rd], s36\n\t"
        "buffer_store_dword v0, off, %[rd], s36\n\t"
        "buffer_store_dword v0, off, %[rd], s36\n\t"
        ".La14%=:\n\t"
        "s_cmp_gt_u32 s40, s37\n\t"
        "s_cselect_b32 s80, s39, 0\n\t"
        "s_cmp_eq_u32 s80, 0\n\t"
        "s_cbranch_scc1 .La18%=\n\t"
        "s_mov_b32 s39, 0\n\t"
        "s_lshl_b32 s81, s37, 17\n\t"
        "s_add_u32 s81, s81, %[km]\n\t"
        "buffer_load_dwordx4 v[96:99], v248, %[rd], s81 offen offset:0 sc1\n\t"
        "buffer_load_dwordx4 v[100:103], v248, %[rd], s81 offen offset:512 sc1\n\t"
        "buffer_load_dwordx4 v[104:107], v248, %[rd], s81 offen offset:1024 sc1\n\t"
        "buffer_load_dwordx4 v[108:111], v248, %[rd], s81 offen offset:1536 sc1\n\t"
        "buffer_load_dwordx2 v[112:113], v245, %[rd], s81 offen offset:2048 sc1\n\t"
        "buffer_load_dwordx2 v[114:115], v245, %[rd], s81 offen offset:2304 sc1\n\t"
        "buffer_load_dwordx2 v[116:117], v245, %[rd], s81 offen offset:2560 sc1\n\t"
        "buffer_load_dwordx2 v[118:119], v245, %[rd], s81 offen offset:2816 sc1\n\t"
        "buffer_load_dwordx2 v[120:121], v245, %[rd], s81 offen offset:3072 sc1\n\t"
        "buffer_load_dwordx2 v[122:123], v245, %[rd], s81 offen offset:3328 sc1\n\t"
        "buffer_load_dwordx2 v[124:125], v245, %[rd], s81 offen offset:3584 sc1\n\t"
        "buffer_load_dwordx2 v[126:127], v245, %[rd], s81 offen offset:3840 sc1\n\t"
        "buffer_store_dword v0, off, %[rd], s36\n\t"
        "buffer_store_dword v0, off, %[rd], s36\n\t"
        "buffer_store_dword v0, off, %[rd], s36\n\t"
        "buffer_store_dword v0, off, %[rd], s36\n\t"
        "buffer_store_dword v0, off, %[rd], s36\n\t"
        "buffer_store_dword v0, off, %[rd], s36\n\t"
        "buffer_store_dword v0, off, %[rd], s36\n\t"
        "buffer_store_dword v0, off, %[rd], s36\n\t"
        ".La18%=:\n\t"
        "s_add_u32 s86, s37, 4\n\t"
        "s_and_b32 s80, s86, 60\n\t"
        "s_lshr_b64 s[80:81], s[42:43], s80\n\t"
        "s_andn2_b32 s80, 15, s80\n\t"
        "s_cselect_b32 s46, s36, 0\n\t"
        "s_cmp_lt_u32 s86, 64\n\t"
        "s_cselect_b32 s46, s46, s36\n\t"
        "s_cmp_gt_u32 s40, s86\n\t"
        "s_cselect_b32 s47, s46, s36\n\t"
        "s_lshl_b32 s80, s86, 18\n\t"
        "s_or_b32 s80, s80, s46\n\t"
        "s_lshl_b32 s81, s86, 17\n\t"
        "s_add_u32 s81, s81, %[km]\n\t"
        "s_or_b32 s81, s81, s47\n\t"
        "s_add_u32 s82, s80, %[kx]\n\t"
        "s_add_u32 s83, s82, 0x1000\n\t"
        "buffer_load_dwordx4 a[0:3], v244, %[rd], s82 offen offset:0 sc1\n\t"
        "buffer_load_dwordx4 a[4:7], v244, %[rd], s82 offen offset:1024 sc1\n\t"
        "buffer_load_dwordx4 a[8:11], v244, %[rd], s82 offen offset:2048 sc1\n\t"
        "buffer_load_dwordx4 a[12:15], v244, %[rd], s82 offen offset:3072 sc1\n\t"
        "buffer_load_dwordx4 a[16:19], v244, %[rd], s83 offen offset:0 sc1\n\t"
        "buffer_load_dwordx4 a[20:23], v244, %[rd], s83 offen offset:1024 sc1\n\t"
        "buffer_load_dwordx4 a[24:27], v244, %[rd], s83 offen offset:2048 sc1\n\t"
        "buffer_load_dwordx4 a[28:31], v244, %[rd], s83 offen offset:3072 sc1\n\t"
        "s_cmp_eq_u64 s[42:43], -1\n\t"
        "s_cbranch_scc1 .La19%=\n\t"
        "s_sleep %c[aslp]\n\t"
        ".La19%=:\n\t"
        "s_sleep %c[bslp]\n\t"
        "s_add_u32 s82, s80, %[qx]\n\t"
        "s_add_u32 s83, s82, 0x1000\n\t"
        "buffer_load_dwordx4 a[32:35], v244, %[rd], s82 offen offset:0 sc1\n\t"
        "buffer_load_dwordx4 a[36:39], v244, %[rd], s82 offen offset:1024 sc1\n\t"
        "buffer_load_dwordx4 a[40:43], v244, %[rd], s82 offen offset:2048 sc1\n\t"
        "buffer_load_dwordx4 a[44:47], v244, %[rd], s82 offen offset:3072 sc1\n\t"
        "buffer_load_dwordx4 a[48:51], v244, %[rd], s83 offen offset:0 sc1\n\t"
        "buffer_load_dwordx4 a[52:55], v244, %[rd], s83 offen offset:1024 sc1\n\t"
        "buffer_load_dwordx4 a[56:59], v244, %[rd], s83 offen offset:2048 sc1\n\t"
        "buffer_load_dwordx4 a[60:63], v244, %[rd], s83 offen offset:3072 sc1\n\t"
        "s_cmp_eq_u64 s[42:43], -1\n\t"
        "s_cbranch_scc1 .La20%=\n\t"
        "s_sleep %c[aslp]\n\t"
        ".La20%=:\n\t"
        "s_sleep %c[bslp]\n\t"
        "buffer_load_dwordx4 a[96:99], v248, %[rd], s81 offen offset:0 sc1\n\t"
        "buffer_load_dwordx4 a[100:103], v248, %[rd], s81 offen offset:512 sc1\n\t"
        "buffer_load_dwordx4 a[104:107], v248, %[rd], s81 offen offset:1024 sc1\n\t"
        "buffer_load_dwordx4 a[108:111], v248, %[rd], s81 offen offset:1536 sc1\n\t"
        "buffer_load_dwordx2 a[112:113], v245, %[rd], s81 offen offset:2048 sc1\n\t"
        "buffer_load_dwordx2 a[114:115], v245, %[rd], s81 offen offset:2304 sc1\n\t"
        "buffer_load_dwordx2 a[116:117], v245, %[rd], s81 offen offset:2560 sc1\n\t"
        "buffer_load_dwordx2 a[118:119], v245, %[rd], s81 offen offset:2816 sc1\n\t"
        "buffer_load_dwordx2 a[120:121], v245, %[rd], s81 offen offset:3072 sc1\n\t"
        "buffer_load_dwordx2 a[122:123], v245, %[rd], s81 offen offset:3328 sc1\n\t"
        "buffer_load_dwordx2 a[124:125], v245, %[rd], s81 offen offset:3584 sc1\n\t"
        "buffer_load_dwordx2 a[126:127], v245, %[rd], s81 offen offset:3840 sc1\n\t"
        "s_add_u32 s82, s80, %[vx]\n\t"
        "s_add_u32 s83, s82, 0x1000\n\t"
        "buffer_load_dwordx4 a[64:67], v244, %[rd], s82 offen offset:0 sc1\n\t"
        "buffer_load_dwordx4 a[68:71], v244, %[rd], s82 offen offset:1024 sc1\n\t"
        "buffer_load_dwordx4 a[72:75], v244, %[rd], s82 offen offset:2048 sc1\n\t"
        "buffer_load_dwordx4 a[76:79], v244, %[rd], s82 offen offset:3072 sc1\n\t"
        "buffer_load_dwordx4 a[80:83], v244, %[rd], s83 offen offset:0 sc1\n\t"
        "buffer_load_dwordx4 a[84:87], v244, %[rd], s83 offen offset:1024 sc1\n\t"
        "buffer_load_dwordx4 a[88:91], v244, %[rd], s83 offen offset:2048 sc1\n\t"
        "buffer_load_dwordx4 a[92:95], v244, %[rd], s83 offen offset:3072 sc1\n\t"
        "s_cmp_eq_u64 s[42:43], -1\n\t"
        "s_cbranch_scc1 .La21%=\n\t"
        "s_sleep %c[aslp]\n\t"
        ".La21%=:\n\t"
        "s_sleep %c[bslp]\n\t"
        "s_waitcnt vmcnt(44)\n\t"
        "s_memrealtime s[98:99]\n\t"
        "v_mfma_f32_16x16x32_bf16 v[136:139], v[0:3], v[32:35], 0\n\t"
        "v_mfma_f32_16x16x32_bf16 v[148:151], v[0:3], v[48:51], 0\n\t"
        "v_mfma_f32_16x16x32_bf16 v[152:155], v[16:19], v[48:51], 0\n\t"
        "v_mfma_f32_16x16x32_bf16 v[136:139], v[4:7], v[36:39], v[136:139]\n\t"
        "v_mfma_f32_16x16x32_bf16 v[148:151], v[4:7], v[52:55], v[148:151]\n\t"
        "v_mfma_f32_16x16x32_bf16 v[152:155], v[20:23], v[52:55], v[152:155]\n\t"
        "v_mfma_f32_16x16x32_bf16 v[136:139], v[8:11], v[40:43], v[136:139]\n\t"
        "v_mfma_f32_16x16x32_bf16 v[148:151], v[8:11], v[56:59], v[148:151]\n\t"
        "v_mfma_f32_16x16x32_bf16 v[152:155], v[24:27], v[56:59], v[152:155]\n\t"
        "v_mfma_f32_16x16x32_bf16 v[136:139], v[12:15], v[44:47], v[136:139]\n\t"
        "v_mfma_f32_16x16x32_bf16 v[148:151], v[12:15], v[60:63], v[148:151]\n\t"
        "v_mfma_f32_16x16x32_bf16 v[152:155], v[28:31], v[60:63], v[152:155]\n\t"
        "s_waitcnt lgkmcnt(0)\n\t"
        "v_cmp_eq_u32_e32 vcc, %[tag], v240\n\t"
        "s_nop 1\n\t"
        "s_or_b64 s[42:43], s[42:43], vcc\n\t"
        "v_readfirstlane_b32 s80, v241\n\t"
        "s_nop 0\n\t"
        "s_cmp_gt_u32 s80, s40\n\t"
        "s_cselect_b32 s41, s98, s41\n\t"
        "s_max_u32 s40, s40, s80\n\t"
        "v_readlane_b32 s84, v242, 0\n\t"
        "v_readlane_b32 s85, v242, 32\n\t"
        "v_mov_b32_e32 v243, 32\n\t"
        "s_and_b32 s80, s45, 3\n\t"
        "s_lshl_b32 s80, s80, 12\n\t"
        "s_lshr_b32 s81, s45, 2\n\t"
        "s_lshl_b32 s81, s81, 3\n\t"
        "s_add_u32 s80, s80, s81\n\t"
        "s_lshr_b32 s82, s45, 1\n\t"
        "s_cmp_eq_u32 s82, 30\n\t"
        "s_addc_u32 s81, s45, 0\n\t"
        "s_and_b32 s81, s81, 1\n\t"
        "s_lshl_b32 s81, s81, 13\n\t"
        "s_lshl_b32 s82, s82, 2\n\t"
        "s_add_u32 s81, s81, s82\n\t"
        "s_cmp_lt_i32 s45, 56\n\t"
        "s_cselect_b32 s80, s80, s81\n\t"
        "s_cselect_b32 s81, 4, 0x1000\n\t"
        "s_add_u32 s81, s81, s80\n\t"
        "s_cmp_eq_u32 s84, 31\n\t"
        "s_cselect_b32 s80, s80, s36\n\t"
        "s_cmp_eq_u32 s85, 31\n\t"
        "s_cselect_b32 s81, s81, s36\n\t"
        "s_cmp_lt_i32 s45, 0\n\t"
        "s_cselect_b32 s80, s36, s80\n\t"
        "s_cselect_b32 s81, s36, s81\n\t"
        "buffer_store_dword v243, v251, %[rd], s80 offen sc1\n\t"
        "buffer_store_dword v243, v251, %[rd], s81 offen sc1\n\t"
        "s_add_u32 s86, s37, 8\n\t"
        "s_cmp_lg_u64 s[42:43], -1\n\t"
        "s_cselect_b32 s84, 0, s36\n\t"
        "s_cmp_gt_u32 s40, s86\n\t"
        "s_cselect_b32 s85, s36, s39\n\t"
        "buffer_load_dword v240, v246, %[rd], s84 offen sc1\n\t"
        "buffer_load_dword v241, v247, %[rd], s85 offen sc1\n\t"
        "v_cvt_pk_bf16_f32 v136, 0, v136\n\t"
        "v_cvt_pk_bf16_f32 v137, 0, v137\n\t"
        "v_cvt_pk_bf16_f32 v138, 0, v138\n\t"
        "v_cvt_pk_bf16_f32 v139, 0, v139\n\t"
        "v_mul_f32_e32 v136, 0x3db504f3, v136\n\t"
        "v_mul_f32_e32 v137, 0x3db504f3, v137\n\t"
        "v_mul_f32_e32 v138, 0x3db504f3, v138\n\t"
        "v_mul_f32_e32 v139, 0x3db504f3, v139\n\t"
        "v_cvt_pk_bf16_f32 v136, 0, v136\n\t"
        "v_cvt_pk_bf16_f32 v137, 0, v137\n\t"
        "v_cvt_pk_bf16_f32 v138, 0, v138\n\t"
        "v_cvt_pk_bf16_f32 v139, 0, v139\n\t"
        "v_cndmask_b32_e64 v136, v253, v136, s[48:49]\n\t"
        "v_cndmask_b32_e64 v137, v253, v137, s[50:51]\n\t"
        "v_cndmask_b32_e64 v138, v253, v138, s[52:53]\n\t"
        "v_cndmask_b32_e64 v139, v253, v139, s[54:55]\n\t"
        "v_cvt_pk_bf16_f32 v148, 0, v148\n\t"
        "v_cvt_pk_bf16_f32 v149, 0, v149\n\t"
        "v_cvt_pk_bf16_f32 v150, 0, v150\n\t"
        "v_cvt_pk_bf16_f32 v151, 0, v151\n\t"
        "v_mul_f32_e32 v148, 0x3db504f3, v148\n\t"
        "v_mul_f32_e32 v149, 0x3db504f3, v149\n\t"
        "v_mul_f32_e32 v150, 0x3db504f3, v150\n\t"
        "v_mul_f32_e32 v151, 0x3db504f3, v151\n\t"
        "v_cvt_pk_bf16_f32 v148, 0, v148\n\t"
        "v_cvt_pk_bf16_f32 v149, 0, v149\n\t"
        "v_cvt_pk_bf16_f32 v150, 0, v150\n\t"
        "v_cvt_pk_bf16_f32 v151, 0, v151\n\t"
        "v_cvt_pk_bf16_f32 v152, 0, v152\n\t"
        "v_cvt_pk_bf16_f32 v153, 0, v153\n\t"
        "v_cvt_pk_bf16_f32 v154, 0, v154\n\t"
        "v_cvt_pk_bf16_f32 v155, 0, v155\n\t"
        "v_mul_f32_e32 v152, 0x3db504f3, v152\n\t"
        "v_mul_f32_e32 v153, 0x3db504f3, v153\n\t"
        "v_mul_f32_e32 v154, 0x3db504f3, v154\n\t"
        "v_mul_f32_e32 v155, 0x3db504f3, v155\n\t"
        "v_cvt_pk_bf16_f32 v152, 0, v152\n\t"
        "v_cvt_pk_bf16_f32 v153, 0, v153\n\t"
        "v_cvt_pk_bf16_f32 v154, 0, v154\n\t"
        "v_cvt_pk_bf16_f32 v155, 0, v155\n\t"
        "v_cndmask_b32_e64 v152, v253, v152, s[48:49]\n\t"
        "v_cndmask_b32_e64 v153, v253, v153, s[50:51]\n\t"
        "v_cndmask_b32_e64 v154, v253, v154, s[52:53]\n\t"
        "v_cndmask_b32_e64 v155, v253, v155, s[54:55]\n\t"
        "v_max3_f32 v172, v136, v137, v253\n\t"
        "v_max3_f32 v172, v172, v138, v139\n\t"
        "v_mov_b32_e32 v140, v253\n\t"
        "v_max_f32_e32 v174, v148, v149\n\t"
        "v_max3_f32 v174, v174, v150, v151\n\t"
        "v_max3_f32 v174, v174, v152, v153\n\t"
        "v_max3_f32 v174, v174, v154, v155\n\t"
        "s_cmp_eq_u32 s39, 0\n\t"
        "s_cbranch_scc1 .La23%=\n\t"
        "s_waitcnt vmcnt(0)\n\t"
        "s_lshl_b32 s80, s44, 7\n\t"
        "s_cmp_lt_i32 s44, 0\n\t"
        "s_cselect_b32 s80, s36, s80\n\t"
        "v_mov_b32_e32 v242, 1\n\t"
        "buffer_atomic_add v242, v250, %[rd], s80 offen sc0 sc1\n\t"
        "s_waitcnt vmcnt(0)\n\t"
        "v_readlane_b32 s84, v242, 0\n\t"
        "v_readlane_b32 s85, v242, 32\n\t"
        "v_mov_b32_e32 v243, 32\n\t"
        "s_and_b32 s80, s44, 3\n\t"
        "s_lshl_b32 s80, s80, 12\n\t"
        "s_lshr_b32 s81, s44, 2\n\t"
        "s_lshl_b32 s81, s81, 3\n\t"
        "s_add_u32 s80, s80, s81\n\t"
        "s_lshr_b32 s82, s44, 1\n\t"
        "s_cmp_eq_u32 s82, 30\n\t"
        "s_addc_u32 s81, s44, 0\n\t"
        "s_and_b32 s81, s81, 1\n\t"
        "s_lshl_b32 s81, s81, 13\n\t"
        "s_lshl_b32 s82, s82, 2\n\t"
        "s_add_u32 s81, s81, s82\n\t"
        "s_cmp_lt_i32 s44, 56\n\t"
        "s_cselect_b32 s80, s80, s81\n\t"
        "s_cselect_b32 s81, 4, 0x1000\n\t"
        "s_add_u32 s81, s81, s80\n\t"
        "s_cmp_eq_u32 s84, 31\n\t"
        "s_cselect_b32 s80, s80, s36\n\t"
        "s_cmp_eq_u32 s85, 31\n\t"
        "s_cselect_b32 s81, s81, s36\n\t"
        "s_cmp_lt_i32 s44, 0\n\t"
        "s_cselect_b32 s80, s36, s80\n\t"
        "s_cselect_b32 s81, s36, s81\n\t"
        "buffer_store_dword v243, v251, %[rd], s80 offen sc1\n\t"
        "buffer_store_dword v243, v251, %[rd], s81 offen sc1\n\t"
        "s_mov_b32 s44, -1\n\t"
        "s_mov_b32 s97, 0\n\t"
        "s_min_u32 s87, s37, 62\n\t"
        ".La22%=:\n\t"
        "s_cmp_gt_u32 s40, s87\n\t"
        "s_cbranch_scc1 .La22%=x\n\t"
        "s_memrealtime s[98:99]\n\t"
        "s_add_u32 s97, s97, 1\n\t"
        "s_cmp_lt_u32 s97, 0x400000\n\t"
        "s_cbranch_scc0 .Lgu%=\n\t"
        "s_sub_u32 s80, s87, s40\n\t"
        "s_cmp_ge_u32 s87, %c[efrom]\n\t"
        "s_subb_u32 s80, s80, 0\n\t"
        "s_mul_i32 s80, s80, %c[smin]\n\t"
        "s_waitcnt lgkmcnt(0)\n\t"
        "s_sub_u32 s81, s98, s41\n\t"
        "s_cmp_ge_i32 s81, s80\n\t"
        "s_cbranch_scc1 .La22%=p\n\t"
        "s_sleep 16\n\t"
        "s_branch .La22%=\n\t"
        ".La22%=p:\n\t"
        "buffer_load_dword v241, v247, %[rd], 0 offen sc1\n\t"
        "s_waitcnt vmcnt(0)\n\t"
        "v_readfirstlane_b32 s80, v241\n\t"
        "s_nop 0\n\t"
        "s_cmp_gt_u32 s80, s40\n\t"
        "s_cselect_b32 s41, s98, s41\n\t"
        "s_max_u32 s40, s40, s80\n\t"
        "s_cmp_gt_u32 s40, s87\n\t"
        "s_cbranch_scc1 .La22%=x\n\t"
        "s_sleep %c[pslp]\n\t"
        "s_branch .La22%=\n\t"
        ".La22%=x:\n\t"
        "s_lshl_b32 s81, s37, 17\n\t"
        "s_add_u32 s81, s81, %[km]\n\t"
        "s_cmp_lg_u32 s37, 63\n\t"
        "s_cbranch_scc1 .La22%=l\n\t"
        "s_sleep %c[lslp]\n\t"
        ".La22%=l:\n\t"
        "buffer_load_dwordx4 v[96:99], v248, %[rd], s81 offen offset:0 sc1\n\t"
        "buffer_load_dwordx4 v[100:103], v248, %[rd], s81 offen offset:512 sc1\n\t"
        "buffer_load_dwordx4 v[104:107], v248, %[rd], s81 offen offset:1024 sc1\n\t"
        "buffer_load_dwordx4 v[108:111], v248, %[rd], s81 offen offset:1536 sc1\n\t"
        "buffer_load_dwordx2 v[112:113], v245, %[rd], s81 offen offset:2048 sc1\n\t"
        "buffer_load_dwordx2 v[114:115], v245, %[rd], s81 offen offset:2304 sc1\n\t"
        "buffer_load_dwordx2 v[116:117], v245, %[rd], s81 offen offset:2560 sc1\n\t"
        "buffer_load_dwordx2 v[118:119], v245, %[rd], s81 offen offset:2816 sc1\n\t"
        "buffer_load_dwordx2 v[120:121], v245, %[rd], s81 offen offset:3072 sc1\n\t"
        "buffer_load_dwordx2 v[122:123], v245, %[rd], s81 offen offset:3328 sc1\n\t"
        "buffer_load_dwordx2 v[124:125], v245, %[rd], s81 offen offset:3584 sc1\n\t"
        "buffer_load_dwordx2 v[126:127], v245, %[rd], s81 offen offset:3840 sc1\n\t"
        "s_waitcnt vmcnt(0)\n\t"
        "s_cmp_lg_u32 s37, 63\n\t"
        "s_cbranch_scc1 .La23%=\n\t"
        "s_add_u32 s97, s97, 1\n\t"
        "s_cmp_lt_u32 s97, 0x400000\n\t"
        "s_cbranch_scc0 .Lgu%=\n\t"
        "v_max_u32_e32 v128, v96, v97\n\t"
        "v_max3_u32 v128, v128, v98, v99\n\t"
        "v_max3_u32 v128, v128, v100, v101\n\t"
        "v_max3_u32 v128, v128, v102, v103\n\t"
        "v_max3_u32 v128, v128, v104, v105\n\t"
        "v_max3_u32 v128, v128, v106, v107\n\t"
        "v_max3_u32 v128, v128, v108, v109\n\t"
        "v_max3_u32 v128, v128, v110, v111\n\t"
        "v_max3_u32 v128, v128, v112, v113\n\t"
        "v_max3_u32 v128, v128, v114, v115\n\t"
        "v_max3_u32 v128, v128, v116, v117\n\t"
        "v_max3_u32 v128, v128, v118, v119\n\t"
        "v_max3_u32 v128, v128, v120, v121\n\t"
        "v_max3_u32 v128, v128, v122, v123\n\t"
        "v_max3_u32 v128, v128, v124, v125\n\t"
        "v_max3_u32 v128, v128, v126, v127\n\t"
        "v_cmp_eq_u32_e32 vcc, -1, v128\n\t"
        "s_nop 1\n\t"
        "v_mov_b32_e32 v252, v252\n\t"
        "s_nop 0\n\t"
        "s_cbranch_vccnz .La22%=l\n\t"
        ".La23%=:\n\t"
        "v_mfma_f32_16x16x32_bf16 v[144:147], v[96:99], v[32:35], 0\n\t"
        "v_mfma_f32_16x16x32_bf16 v[156:159], v[96:99], v[48:51], 0\n\t"
        "v_mfma_f32_16x16x32_bf16 v[144:147], v[100:103], v[36:39], v[144:147]\n\t"
        "v_mfma_f32_16x16x32_bf16 v[156:159], v[100:103], v[52:55], v[156:159]\n\t"
        "v_mfma_f32_16x16x32_bf16 v[144:147], v[104:107], v[40:43], v[144:147]\n\t"
        "v_mfma_f32_16x16x32_bf16 v[156:159], v[104:107], v[56:59], v[156:159]\n\t"
        "v_mfma_f32_16x16x32_bf16 v[144:147], v[108:111], v[44:47], v[144:147]\n\t"
        "v_mfma_f32_16x16x32_bf16 v[156:159], v[108:111], v[60:63], v[156:159]\n\t"
        "s_nop 1\n\t"
        "v_mov_b32_e32 v252, v252\n\t"
        "s_nop 1\n\t"
        "v_mov_b32_e32 v252, v252\n\t"
        "s_nop 0\n\t"
        "v_cvt_pk_bf16_f32 v144, 0, v144\n\t"
        "v_cvt_pk_bf16_f32 v145, 0, v145\n\t"
        "v_cvt_pk_bf16_f32 v146, 0, v146\n\t"
        "v_cvt_pk_bf16_f32 v147, 0, v147\n\t"
        "v_mul_f32_e32 v144, 0x3db504f3, v144\n\t"
        "v_mul_f32_e32 v145, 0x3db504f3, v145\n\t"
        "v_mul_f32_e32 v146, 0x3db504f3, v146\n\t"
        "v_mul_f32_e32 v147, 0x3db504f3, v147\n\t"
        "v_cvt_pk_bf16_f32 v144, 0, v144\n\t"
        "v_cvt_pk_bf16_f32 v145, 0, v145\n\t"
        "v_cvt_pk_bf16_f32 v146, 0, v146\n\t"
        "v_cvt_pk_bf16_f32 v147, 0, v147\n\t"
        "v_cndmask_b32_e64 v144, v144, v254, s[56:57]\n\t"
        "v_cndmask_b32_e64 v145, v145, v254, s[56:57]\n\t"
        "v_cndmask_b32_e64 v146, v146, v254, s[56:57]\n\t"
        "v_cndmask_b32_e64 v147, v147, v254, s[56:57]\n\t"
        "v_max3_f32 v172, v172, v144, v145\n\t"
        "v_max3_f32 v173, v172, v146, v147\n\t"
        "v_max3_f32 v172, v172, v146, v147\n\t"
        "s_nop 1\n\t"
        "v_permlane16_swap_b32_e32 v172, v173\n\t"
        "v_max_f32_e32 v172, v172, v173\n\t"
        "v_mov_b32_e32 v173, v172\n\t"
        "s_nop 1\n\t"
        "v_permlane32_swap_b32_e32 v172, v173\n\t"
        "v_max_f32_e32 v172, v172, v173\n\t"
        "v_sub_f32_e32 v136, v136, v172\n\t"
        "v_sub_f32_e32 v137, v137, v172\n\t"
        "v_sub_f32_e32 v138, v138, v172\n\t"
        "v_sub_f32_e32 v139, v139, v172\n\t"
        "v_sub_f32_e32 v140, v140, v172\n\t"
        "v_sub_f32_e32 v144, v144, v172\n\t"
        "v_sub_f32_e32 v145, v145, v172\n\t"
        "v_sub_f32_e32 v146, v146, v172\n\t"
        "v_sub_f32_e32 v147, v147, v172\n\t"
        "v_mul_f32_e32 v136, 0x3fb8aa3b, v136\n\t"
        "v_mul_f32_e32 v137, 0x3fb8aa3b, v137\n\t"
        "v_mul_f32_e32 v138, 0x3fb8aa3b, v138\n\t"
        "v_mul_f32_e32 v139, 0x3fb8aa3b, v139\n\t"
        "v_mul_f32_e32 v140, 0x3fb8aa3b, v140\n\t"
        "v_mul_f32_e32 v144, 0x3fb8aa3b, v144\n\t"
        "v_mul_f32_e32 v145, 0x3fb8aa3b, v145\n\t"
        "v_mul_f32_e32 v146, 0x3fb8aa3b, v146\n\t"
        "v_mul_f32_e32 v147, 0x3fb8aa3b, v147\n\t"
        "v_exp_f32_e32 v136, v136\n\t"
        "v_exp_f32_e32 v137, v137\n\t"
        "v_exp_f32_e32 v138, v138\n\t"
        "v_exp_f32_e32 v139, v139\n\t"
        "v_exp_f32_e32 v140, v140\n\t"
        "v_exp_f32_e32 v144, v144\n\t"
        "v_exp_f32_e32 v145, v145\n\t"
        "v_exp_f32_e32 v146, v146\n\t"
        "v_exp_f32_e32 v147, v147\n\t"
        "v_mul_f32_e32 v172, 4.0, v140\n\t"
        "v_add_f32_e32 v172, v172, v136\n\t"
        "v_add_f32_e32 v172, v172, v137\n\t"
        "v_add_f32_e32 v172, v172, v138\n\t"
        "v_add_f32_e32 v172, v172, v139\n\t"
        "v_add_f32_e32 v172, v172, v144\n\t"
        "v_add_f32_e32 v172, v172, v145\n\t"
        "v_add_f32_e32 v172, v172, v146\n\t"
        "v_add_f32_e32 v172, v172, v147\n\t"
        "v_mov_b32_e32 v173, v172\n\t"
        "s_nop 1\n\t"
        "v_permlane16_swap_b32_e32 v172, v173\n\t"
        "v_add_f32_e32 v172, v172, v173\n\t"
        "v_mov_b32_e32 v173, v172\n\t"
        "s_nop 1\n\t"
        "v_permlane32_swap_b32_e32 v172, v173\n\t"
        "v_add_f32_e32 v172, v172, v173\n\t"
        "v_rcp_f32_e32 v172, v172\n\t"
        "s_nop 0\n\t"
        "v_mul_f32_e32 v136, v136, v172\n\t"
        "v_mul_f32_e32 v137, v137, v172\n\t"
        "v_mul_f32_e32 v138, v138, v172\n\t"
        "v_mul_f32_e32 v139, v139, v172\n\t"
        "v_mul_f32_e32 v140, v140, v172\n\t"
        "v_mul_f32_e32 v144, v144, v172\n\t"
        "v_mul_f32_e32 v145, v145, v172\n\t"
        "v_mul_f32_e32 v146, v146, v172\n\t"
        "v_mul_f32_e32 v147, v147, v172\n\t"
        "v_cvt_pk_bf16_f32 v160, v136, v137\n\t"
        "v_cvt_pk_bf16_f32 v161, v138, v139\n\t"
        "v_cvt_pk_bf16_f32 v162, v140, v140\n\t"
        "v_cvt_pk_bf16_f32 v163, v140, v140\n\t"
        "v_cvt_pk_bf16_f32 v164, v144, v145\n\t"
        "v_cvt_pk_bf16_f32 v165, v146, v147\n\t"
        "v_mfma_f32_16x16x32_bf16 v[176:179], v[64:67], v[160:163], 0\n\t"
        "v_cvt_pk_bf16_f32 v156, 0, v156\n\t"
        "v_cvt_pk_bf16_f32 v157, 0, v157\n\t"
        "v_cvt_pk_bf16_f32 v158, 0, v158\n\t"
        "v_mfma_f32_16x16x32_bf16 v[180:183], v[68:71], v[160:163], 0\n\t"
        "v_cvt_pk_bf16_f32 v159, 0, v159\n\t"
        "v_mul_f32_e32 v156, 0x3db504f3, v156\n\t"
        "v_mul_f32_e32 v157, 0x3db504f3, v157\n\t"
        "v_mfma_f32_16x16x32_bf16 v[184:187], v[72:75], v[160:163], 0\n\t"
        "v_mul_f32_e32 v158, 0x3db504f3, v158\n\t"
        "v_mul_f32_e32 v159, 0x3db504f3, v159\n\t"
        "v_cvt_pk_bf16_f32 v156, 0, v156\n\t"
        "v_mfma_f32_16x16x32_bf16 v[188:191], v[76:79], v[160:163], 0\n\t"
        "v_cvt_pk_bf16_f32 v157, 0, v157\n\t"
        "v_cvt_pk_bf16_f32 v158, 0, v158\n\t"
        "v_cvt_pk_bf16_f32 v159, 0, v159\n\t"
        "v_mfma_f32_16x16x32_bf16 v[192:195], v[80:83], v[160:163], 0\n\t"
        "v_cndmask_b32_e64 v156, v156, v254, s[56:57]\n\t"
        "v_cndmask_b32_e64 v157, v157, v254, s[56:57]\n\t"
        "v_cndmask_b32_e64 v158, v158, v254, s[56:57]\n\t"
        "v_mfma_f32_16x16x32_bf16 v[196:199], v[84:87], v[160:163], 0\n\t"
        "v_cndmask_b32_e64 v159, v159, v254, s[56:57]\n\t"
        "v_max3_f32 v174, v174, v156, v157\n\t"
        "v_max3_f32 v175, v174, v158, v159\n\t"
        "v_mfma_f32_16x16x32_bf16 v[200:203], v[88:91], v[160:163], 0\n\t"
        "v_max3_f32 v174, v174, v158, v159\n\t"
        "s_nop 1\n\t"
        "v_permlane16_swap_b32_e32 v174, v175\n\t"
        "v_max_f32_e32 v174, v174, v175\n\t"
        "v_mfma_f32_16x16x32_bf16 v[204:207], v[92:95], v[160:163], 0\n\t"
        "v_mov_b32_e32 v175, v174\n\t"
        "s_nop 1\n\t"
        "v_permlane32_swap_b32_e32 v174, v175\n\t"
        "v_max_f32_e32 v174, v174, v175\n\t"
        "v_mfma_f32_16x16x16_bf16 v[176:179], v[112:113], v[164:165], v[176:179]\n\t"
        "v_sub_f32_e32 v148, v148, v174\n\t"
        "v_sub_f32_e32 v149, v149, v174\n\t"
        "v_sub_f32_e32 v150, v150, v174\n\t"
        "v_mfma_f32_16x16x16_bf16 v[180:183], v[114:115], v[164:165], v[180:183]\n\t"
        "v_sub_f32_e32 v151, v151, v174\n\t"
        "v_sub_f32_e32 v152, v152, v174\n\t"
        "v_sub_f32_e32 v153, v153, v174\n\t"
        "v_mfma_f32_16x16x16_bf16 v[184:187], v[116:117], v[164:165], v[184:187]\n\t"
        "v_sub_f32_e32 v154, v154, v174\n\t"
        "v_sub_f32_e32 v155, v155, v174\n\t"
        "v_sub_f32_e32 v156, v156, v174\n\t"
        "v_mfma_f32_16x16x16_bf16 v[188:191], v[118:119], v[164:165], v[188:191]\n\t"
        "v_sub_f32_e32 v157, v157, v174\n\t"
        "v_sub_f32_e32 v158, v158, v174\n\t"
        "v_sub_f32_e32 v159, v159, v174\n\t"
        "v_mfma_f32_16x16x16_bf16 v[192:195], v[120:121], v[164:165], v[192:195]\n\t"
        "v_mul_f32_e32 v148, 0x3fb8aa3b, v148\n\t"
        "v_mul_f32_e32 v149, 0x3fb8aa3b, v149\n\t"
        "v_mul_f32_e32 v150, 0x3fb8aa3b, v150\n\t"
        "v_mfma_f32_16x16x16_bf16 v[196:199], v[122:123], v[164:165], v[196:199]\n\t"
        "v_mul_f32_e32 v151, 0x3fb8aa3b, v151\n\t"
        "v_mul_f32_e32 v152, 0x3fb8aa3b, v152\n\t"
        "v_mul_f32_e32 v153, 0x3fb8aa3b, v153\n\t"
        "v_mfma_f32_16x16x16_bf16 v[200:203], v[124:125], v[164:165], v[200:203]\n\t"
        "v_mul_f32_e32 v154, 0x3fb8aa3b, v154\n\t"
        "v_mul_f32_e32 v155, 0x3fb8aa3b, v155\n\t"
        "v_mul_f32_e32 v156, 0x3fb8aa3b, v156\n\t"
        "v_mfma_f32_16x16x16_bf16 v[204:207], v[126:127], v[164:165], v[204:207]\n\t"
        "v_mul_f32_e32 v157, 0x3fb8aa3b, v157\n\t"
        "v_mul_f32_e32 v158, 0x3fb8aa3b, v158\n\t"
        "v_mul_f32_e32 v159, 0x3fb8aa3b, v159\n\t"
        "v_exp_f32_e32 v148, v148\n\t"
        "v_exp_f32_e32 v149, v149\n\t"
        "v_exp_f32_e32 v150, v150\n\t"
        "v_exp_f32_e32 v151, v151\n\t"
        "v_exp_f32_e32 v152, v152\n\t"
        "v_exp_f32_e32 v153, v153\n\t"
        "v_exp_f32_e32 v154, v154\n\t"
        "v_exp_f32_e32 v155, v155\n\t"
        "v_exp_f32_e32 v156, v156\n\t"
        "v_exp_f32_e32 v157, v157\n\t"
        "v_exp_f32_e32 v158, v158\n\t"
        "v_exp_f32_e32 v159, v159\n\t"
        "v_add_f32_e32 v174, v148, v149\n\t"
        "v_add_f32_e32 v174, v174, v150\n\t"
        "v_add_f32_e32 v174, v174, v151\n\t"
        "v_add_f32_e32 v174, v174, v152\n\t"
        "v_add_f32_e32 v174, v174, v153\n\t"
        "v_add_f32_e32 v174, v174, v154\n\t"
        "v_add_f32_e32 v174, v174, v155\n\t"
        "v_add_f32_e32 v174, v174, v156\n\t"
        "v_add_f32_e32 v174, v174, v157\n\t"
        "v_add_f32_e32 v174, v174, v158\n\t"
        "v_add_f32_e32 v174, v174, v159\n\t"
        "v_mov_b32_e32 v175, v174\n\t"
        "s_nop 1\n\t"
        "v_permlane16_swap_b32_e32 v174, v175\n\t"
        "v_add_f32_e32 v174, v174, v175\n\t"
        "v_mov_b32_e32 v175, v174\n\t"
        "s_nop 1\n\t"
        "v_permlane32_swap_b32_e32 v174, v175\n\t"
        "v_add_f32_e32 v174, v174, v175\n\t"
        "v_rcp_f32_e32 v174, v174\n\t"
        "s_nop 0\n\t"
        "v_mul_f32_e32 v148, v148, v174\n\t"
        "v_mul_f32_e32 v149, v149, v174\n\t"
        "v_mul_f32_e32 v150, v150, v174\n\t"
        "v_mul_f32_e32 v151, v151, v174\n\t"
        "v_mul_f32_e32 v152, v152, v174\n\t"
        "v_mul_f32_e32 v153, v153, v174\n\t"
        "v_mul_f32_e32 v154, v154, v174\n\t"
        "v_mul_f32_e32 v155, v155, v174\n\t"
        "v_mul_f32_e32 v156, v156, v174\n\t"
        "v_mul_f32_e32 v157, v157, v174\n\t"
        "v_mul_f32_e32 v158, v158, v174\n\t"
        "v_mul_f32_e32 v159, v159, v174\n\t"
        "v_cvt_pk_bf16_f32 v166, v148, v149\n\t"
        "v_cvt_pk_bf16_f32 v167, v150, v151\n\t"
        "v_cvt_pk_bf16_f32 v168, v152, v153\n\t"
        "v_cvt_pk_bf16_f32 v169, v154, v155\n\t"
        "v_cvt_pk_bf16_f32 v170, v156, v157\n\t"
        "v_cvt_pk_bf16_f32 v171, v158, v159\n\t"
        "s_waitcnt vmcnt(40)\n\t"
        "s_lshl_b32 s80, s44, 7\n\t"
        "s_cmp_lt_i32 s44, 0\n\t"
        "s_cselect_b32 s80, s36, s80\n\t"
        "v_mov_b32_e32 v242, 1\n\t"
        "buffer_atomic_add v242, v250, %[rd], s80 offen sc0 sc1\n\t"
        "s_lshl_b32 s86, s37, 18\n\t"
        "s_add_u32 s86, s86, %[qx]\n\t"
        "s_add_u32 s87, s86, 0x1000\n\t"
        "v_mfma_f32_16x16x32_bf16 v[208:211], v[64:67], v[166:169], 0\n\t"
        "v_cvt_pk_bf16_f32 v176, v176, v177\n\t"
        "v_cvt_pk_bf16_f32 v177, v178, v179\n\t"
        "v_mfma_f32_16x16x32_bf16 v[212:215], v[68:71], v[166:169], 0\n\t"
        "v_cvt_pk_bf16_f32 v178, v180, v181\n\t"
        "v_cvt_pk_bf16_f32 v179, v182, v183\n\t"
        "v_mfma_f32_16x16x32_bf16 v[216:219], v[72:75], v[166:169], 0\n\t"
        "buffer_store_dwordx4 v[176:179], v244, %[rd], s86 offen offset:0 sc1\n\t"
        "v_cvt_pk_bf16_f32 v184, v184, v185\n\t"
        "v_mfma_f32_16x16x32_bf16 v[220:223], v[76:79], v[166:169], 0\n\t"
        "v_cvt_pk_bf16_f32 v185, v186, v187\n\t"
        "v_cvt_pk_bf16_f32 v186, v188, v189\n\t"
        "v_mfma_f32_16x16x32_bf16 v[224:227], v[80:83], v[166:169], 0\n\t"
        "v_cvt_pk_bf16_f32 v187, v190, v191\n\t"
        "buffer_store_dwordx4 v[184:187], v244, %[rd], s86 offen offset:1024 sc1\n\t"
        "v_mfma_f32_16x16x32_bf16 v[228:231], v[84:87], v[166:169], 0\n\t"
        "v_cvt_pk_bf16_f32 v192, v192, v193\n\t"
        "v_cvt_pk_bf16_f32 v193, v194, v195\n\t"
        "v_mfma_f32_16x16x32_bf16 v[232:235], v[88:91], v[166:169], 0\n\t"
        "v_cvt_pk_bf16_f32 v194, v196, v197\n\t"
        "v_cvt_pk_bf16_f32 v195, v198, v199\n\t"
        "v_mfma_f32_16x16x32_bf16 v[236:239], v[92:95], v[166:169], 0\n\t"
        "buffer_store_dwordx4 v[192:195], v244, %[rd], s86 offen offset:2048 sc1\n\t"
        "v_cvt_pk_bf16_f32 v200, v200, v201\n\t"
        "v_mfma_f32_16x16x16_bf16 v[208:211], v[112:113], v[170:171], v[208:211]\n\t"
        "v_cvt_pk_bf16_f32 v201, v202, v203\n\t"
        "v_cvt_pk_bf16_f32 v202, v204, v205\n\t"
        "v_mfma_f32_16x16x16_bf16 v[212:215], v[114:115], v[170:171], v[212:215]\n\t"
        "v_cvt_pk_bf16_f32 v203, v206, v207\n\t"
        "buffer_store_dwordx4 v[200:203], v244, %[rd], s86 offen offset:3072 sc1\n\t"
        "v_mfma_f32_16x16x16_bf16 v[216:219], v[116:117], v[170:171], v[216:219]\n\t"
        "v_mfma_f32_16x16x16_bf16 v[220:223], v[118:119], v[170:171], v[220:223]\n\t"
        "v_mfma_f32_16x16x16_bf16 v[224:227], v[120:121], v[170:171], v[224:227]\n\t"
        "v_mfma_f32_16x16x16_bf16 v[228:231], v[122:123], v[170:171], v[228:231]\n\t"
        "v_mfma_f32_16x16x16_bf16 v[232:235], v[124:125], v[170:171], v[232:235]\n\t"
        "v_mfma_f32_16x16x16_bf16 v[236:239], v[126:127], v[170:171], v[236:239]\n\t"
        "v_cvt_pk_bf16_f32 v208, v208, v209\n\t"
        "v_cvt_pk_bf16_f32 v209, v210, v211\n\t"
        "v_cvt_pk_bf16_f32 v210, v212, v213\n\t"
        "v_cvt_pk_bf16_f32 v211, v214, v215\n\t"
        "buffer_store_dwordx4 v[208:211], v244, %[rd], s87 offen offset:0 sc1\n\t"
        "v_cvt_pk_bf16_f32 v216, v216, v217\n\t"
        "v_cvt_pk_bf16_f32 v217, v218, v219\n\t"
        "v_cvt_pk_bf16_f32 v218, v220, v221\n\t"
        "v_cvt_pk_bf16_f32 v219, v222, v223\n\t"
        "buffer_store_dwordx4 v[216:219], v244, %[rd], s87 offen offset:1024 sc1\n\t"
        "v_cvt_pk_bf16_f32 v224, v224, v225\n\t"
        "v_cvt_pk_bf16_f32 v225, v226, v227\n\t"
        "v_cvt_pk_bf16_f32 v226, v228, v229\n\t"
        "v_cvt_pk_bf16_f32 v227, v230, v231\n\t"
        "buffer_store_dwordx4 v[224:227], v244, %[rd], s87 offen offset:2048 sc1\n\t"
        "v_cvt_pk_bf16_f32 v232, v232, v233\n\t"
        "v_cvt_pk_bf16_f32 v233, v234, v235\n\t"
        "v_cvt_pk_bf16_f32 v234, v236, v237\n\t"
        "v_cvt_pk_bf16_f32 v235, v238, v239\n\t"
        "buffer_store_dwordx4 v[232:235], v244, %[rd], s87 offen offset:3072 sc1\n\t"
        "s_mov_b32 s45, s44\n\t"
        "s_mov_b32 s44, s37\n\t"
        "s_add_u32 s80, s37, 4\n\t"
        "s_cmp_gt_u32 s40, s80\n\t"
        "s_cselect_b32 s80, s46, 1\n\t"
        "s_cmp_eq_u32 s80, 0\n\t"
        "s_cbranch_scc1 .La24%=\n\t"
        "s_waitcnt vmcnt(0)\n\t"
        "v_readlane_b32 s84, v242, 0\n\t"
        "v_readlane_b32 s85, v242, 32\n\t"
        "v_mov_b32_e32 v243, 32\n\t"
        "s_and_b32 s80, s45, 3\n\t"
        "s_lshl_b32 s80, s80, 12\n\t"
        "s_lshr_b32 s81, s45, 2\n\t"
        "s_lshl_b32 s81, s81, 3\n\t"
        "s_add_u32 s80, s80, s81\n\t"
        "s_lshr_b32 s82, s45, 1\n\t"
        "s_cmp_eq_u32 s82, 30\n\t"
        "s_addc_u32 s81, s45, 0\n\t"
        "s_and_b32 s81, s81, 1\n\t"
        "s_lshl_b32 s81, s81, 13\n\t"
        "s_lshl_b32 s82, s82, 2\n\t"
        "s_add_u32 s81, s81, s82\n\t"
        "s_cmp_lt_i32 s45, 56\n\t"
        "s_cselect_b32 s80, s80, s81\n\t"
        "s_cselect_b32 s81, 4, 0x1000\n\t"
        "s_add_u32 s81, s81, s80\n\t"
        "s_cmp_eq_u32 s84, 31\n\t"
        "s_cselect_b32 s80, s80, s36\n\t"
        "s_cmp_eq_u32 s85, 31\n\t"
        "s_cselect_b32 s81, s81, s36\n\t"
        "s_cmp_lt_i32 s45, 0\n\t"
        "s_cselect_b32 s80, s36, s80\n\t"
        "s_cselect_b32 s81, s36, s81\n\t"
        "buffer_store_dword v243, v251, %[rd], s80 offen sc1\n\t"
        "buffer_store_dword v243, v251, %[rd], s81 offen sc1\n\t"
        "s_lshl_b32 s80, s44, 7\n\t"
        "s_cmp_lt_i32 s44, 0\n\t"
        "s_cselect_b32 s80, s36, s80\n\t"
        "v_mov_b32_e32 v242, 1\n\t"
        "buffer_atomic_add v242, v250, %[rd], s80 offen sc0 sc1\n\t"
        "s_waitcnt vmcnt(0)\n\t"
        "v_readlane_b32 s84, v242, 0\n\t"
        "v_readlane_b32 s85, v242, 32\n\t"
        "v_mov_b32_e32 v243, 32\n\t"
        "s_and_b32 s80, s44, 3\n\t"
        "s_lshl_b32 s80, s80, 12\n\t"
        "s_lshr_b32 s81, s44, 2\n\t"
        "s_lshl_b32 s81, s81, 3\n\t"
        "s_add_u32 s80, s80, s81\n\t"
        "s_lshr_b32 s82, s44, 1\n\t"
        "s_cmp_eq_u32 s82, 30\n\t"
        "s_addc_u32 s81, s44, 0\n\t"
        "s_and_b32 s81, s81, 1\n\t"
        "s_lshl_b32 s81, s81, 13\n\t"
        "s_lshl_b32 s82, s82, 2\n\t"
        "s_add_u32 s81, s81, s82\n\t"
        "s_cmp_lt_i32 s44, 56\n\t"
        "s_cselect_b32 s80, s80, s81\n\t"
        "s_cselect_b32 s81, 4, 0x1000\n\t"
        "s_add_u32 s81, s81, s80\n\t"
        "s_cmp_eq_u32 s84, 31\n\t"
        "s_cselect_b32 s80, s80, s36\n\t"
        "s_cmp_eq_u32 s85, 31\n\t"
        "s_cselect_b32 s81, s81, s36\n\t"
        "s_cmp_lt_i32 s44, 0\n\t"
        "s_cselect_b32 s80, s36, s80\n\t"
        "s_cselect_b32 s81, s36, s81\n\t"
        "buffer_store_dword v243, v251, %[rd], s80 offen sc1\n\t"
        "buffer_store_dword v243, v251, %[rd], s81 offen sc1\n\t"
        "s_mov_b32 s44, -1\n\t"
        "s_mov_b32 s45, -1\n\t"
        ".La24%=:\n\t"
        "s_mov_b32 s38, s46\n\t"
        "s_mov_b32 s39, s47\n\t"
        "s_add_u32 s37, s37, 4\n\t"
        "s_cmp_lt_u32 s37, 64\n\t"
        "s_cbranch_scc1 .Ltop%=\n\t"
        "s_endpgm\n\t"
        ".Lgu%=:\n\t"
        "v_mov_b32_e32 v0, 64\n\t"
        "s_mov_b32 s80, %c[ferr]\n\t"
        "buffer_store_dword v0, off, %[rd], s80 sc1\n\t"
        "s_endpgm"
        :: [rd] "s"(rd), [w] "s"(wv), [tag] "s"(tag), [kx] "s"(kx), [qx] "s"(qx), [vx] "s"(vx), [km] "s"(km), [blk] "s"(h),
          [ferr] "i"(F_ERR), [dbg] "i"(W_DBG), [pslp] "i"(P_SLP), [stm] "i"(ST_M), [sts] "i"(ST_S), [lslp] "i"(L_SLP), [aslp] "i"(AT_SLP), [bslp] "i"(BT_SLP), [efrom] "i"(EARLY_FROM), [smin] "i"(STEP_MIN), "{v[244:251]}"(pc), "{v252}"(dq)
        : "memory", "vcc", "scc", "s36", "s37", "s38", "s39", "s40", "s41", "s42", "s43", "s44", "s45", "s46", "s47", "s48", "s49", "s50", "s51", "s52", "s53", "s54", "s55", "s56", "s57", "s58", "s59", "s80", "s81", "s82", "s83", "s84", "s85", "s86", "s87", "s88", "s89", "s90", "s91", "s92", "s93", "s94", "s95", "s96", "s97", "s98", "s99");
}

constexpr int OP_P = 144;
__device__ __forceinline__ void oproj_block(const Params& p, const int wv, const int nt, const int rho, const int j, const unsigned tag, const unsigned g_t0) {
    const int w = wv, l = otid() & 63, n16 = l & 15, g = l >> 4;
    const v4u rd = mk_srd(p.ws, W_TOTAL), ro = mk_srd(p.out, S * D * 2), rx = mk_srd(p.Aq, S * D * 2);
    const unsigned snd = (unsigned)((w >> 1) ^ nt) << 31, own = snd ^ 0x80000000u;
    const unsigned ep = (nt == 0 && rho == 0 && w == 0) ? F_EP : 0xF0000000u;
    const int n0 = (nt >> 1) * 64 + 4 * n16, row = 4 * w + g;
    const unsigned a_b = W_QX + ((nt & 1) * 16 + w * 4) * 8192 + l * 16, tr = (4 * g + (n16 >> 2)), tc = (n16 & 3) * 8;
    const v16u pc = {a_b, a_b + 4096, F_RDY + (rho * 32 + nt) * 128 + (l & 31) * 4, W_U + (n0 >> 2) * 16384 + row * 8, W_GATE + row * 4, (unsigned)((row * 1024 + n0) * 2),
                     W_KX + ((nt >> 1) * 4 + w) * 2048 + l * 16,
                     (unsigned)(w * 8320 + g * 256 + ((n16 + 4 * g) & 15) * 16), (unsigned)((n16 >> 2) * 1040 + (n16 & 3) * 256 + ((row + 4 * n16) & 15) * 16),
                     (unsigned)(w * 10240 + (l >> 3) * OP_P + (l & 7) * 16), w * 10240 + tr * OP_P + tc, WO_L + w * 16384 + tr * 128 + tc, 0u, 0u, 0u, 0u};
    asm volatile(
        "ds_write_b128 v185, v[192:195] offset:0\n\t"
        "ds_write_b128 v185, v[196:199] offset:1152\n\t"
        "ds_write_b128 v185, v[200:203] offset:2304\n\t"
        "ds_write_b128 v185, v[204:207] offset:3456\n\t"
        "ds_read_b64_tr_b16 a[0:1], v186 offset:0\n\t"
        "ds_read_b64_tr_b16 a[2:3], v186 offset:2304\n\t"
        "ds_read_b64_tr_b16 a[4:5], v186 offset:32\n\t"
        "ds_read_b64_tr_b16 a[6:7], v186 offset:2336\n\t"
        "ds_read_b64_tr_b16 a[8:9], v186 offset:64\n\t"
        "ds_read_b64_tr_b16 a[10:11], v186 offset:2368\n\t"
        "ds_read_b64_tr_b16 a[12:13], v186 offset:96\n\t"
        "ds_read_b64_tr_b16 a[14:15], v186 offset:2400\n\t"
        "ds_write_b128 v185, v[208:211] offset:5120\n\t"
        "ds_write_b128 v185, v[212:215] offset:6272\n\t"
        "ds_write_b128 v185, v[216:219] offset:7424\n\t"
        "ds_write_b128 v185, v[220:223] offset:8576\n\t"
        "ds_read_b64_tr_b16 a[16:17], v186 offset:5120\n\t"
        "ds_read_b64_tr_b16 a[18:19], v186 offset:7424\n\t"
        "ds_read_b64_tr_b16 a[20:21], v186 offset:5152\n\t"
        "ds_read_b64_tr_b16 a[22:23], v186 offset:7456\n\t"
        "ds_read_b64_tr_b16 a[24:25], v186 offset:5184\n\t"
        "ds_read_b64_tr_b16 a[26:27], v186 offset:7488\n\t"
        "ds_read_b64_tr_b16 a[28:29], v186 offset:5216\n\t"
        "ds_read_b64_tr_b16 a[30:31], v186 offset:7520\n\t"
        "ds_write_b128 v185, v[224:227] offset:0\n\t"
        "ds_write_b128 v185, v[228:231] offset:1152\n\t"
        "ds_write_b128 v185, v[232:235] offset:2304\n\t"
        "ds_write_b128 v185, v[236:239] offset:3456\n\t"
        "ds_read_b64_tr_b16 a[32:33], v186 offset:0\n\t"
        "ds_read_b64_tr_b16 a[34:35], v186 offset:2304\n\t"
        "ds_read_b64_tr_b16 a[36:37], v186 offset:32\n\t"
        "ds_read_b64_tr_b16 a[38:39], v186 offset:2336\n\t"
        "ds_read_b64_tr_b16 a[40:41], v186 offset:64\n\t"
        "ds_read_b64_tr_b16 a[42:43], v186 offset:2368\n\t"
        "ds_read_b64_tr_b16 a[44:45], v186 offset:96\n\t"
        "ds_read_b64_tr_b16 a[46:47], v186 offset:2400\n\t"
        "ds_write_b128 v185, v[240:243] offset:5120\n\t"
        "ds_write_b128 v185, v[244:247] offset:6272\n\t"
        "ds_write_b128 v185, v[248:251] offset:7424\n\t"
        "ds_write_b128 v185, v[252:255] offset:8576\n\t"
        "ds_read_b64_tr_b16 a[48:49], v186 offset:5120\n\t"
        "ds_read_b64_tr_b16 a[50:51], v186 offset:7424\n\t"
        "ds_read_b64_tr_b16 a[52:53], v186 offset:5152\n\t"
        "ds_read_b64_tr_b16 a[54:55], v186 offset:7456\n\t"
        "ds_read_b64_tr_b16 a[56:57], v186 offset:5184\n\t"
        "ds_read_b64_tr_b16 a[58:59], v186 offset:7488\n\t"
        "ds_read_b64_tr_b16 a[60:61], v186 offset:5216\n\t"
        "ds_read_b64_tr_b16 a[62:63], v186 offset:7520\n\t"
        "ds_read_b64_tr_b16 a[64:65], v187 offset:0\n\t"
        "ds_read_b64_tr_b16 a[66:67], v187 offset:2048\n\t"
        "ds_read_b64_tr_b16 a[68:69], v187 offset:32\n\t"
        "ds_read_b64_tr_b16 a[70:71], v187 offset:2080\n\t"
        "ds_read_b64_tr_b16 a[72:73], v187 offset:64\n\t"
        "ds_read_b64_tr_b16 a[74:75], v187 offset:2112\n\t"
        "ds_read_b64_tr_b16 a[76:77], v187 offset:96\n\t"
        "ds_read_b64_tr_b16 a[78:79], v187 offset:2144\n\t"
        "ds_read_b64_tr_b16 a[80:81], v187 offset:4096\n\t"
        "ds_read_b64_tr_b16 a[82:83], v187 offset:6144\n\t"
        "ds_read_b64_tr_b16 a[84:85], v187 offset:4128\n\t"
        "ds_read_b64_tr_b16 a[86:87], v187 offset:6176\n\t"
        "ds_read_b64_tr_b16 a[88:89], v187 offset:4160\n\t"
        "ds_read_b64_tr_b16 a[90:91], v187 offset:6208\n\t"
        "ds_read_b64_tr_b16 a[92:93], v187 offset:4192\n\t"
        "ds_read_b64_tr_b16 a[94:95], v187 offset:6240\n\t"
        "ds_read_b64_tr_b16 a[96:97], v187 offset:8192\n\t"
        "ds_read_b64_tr_b16 a[98:99], v187 offset:10240\n\t"
        "ds_read_b64_tr_b16 a[100:101], v187 offset:8224\n\t"
        "ds_read_b64_tr_b16 a[102:103], v187 offset:10272\n\t"
        "ds_read_b64_tr_b16 a[104:105], v187 offset:8256\n\t"
        "ds_read_b64_tr_b16 a[106:107], v187 offset:10304\n\t"
        "ds_read_b64_tr_b16 a[108:109], v187 offset:8288\n\t"
        "ds_read_b64_tr_b16 a[110:111], v187 offset:10336\n\t"
        "ds_read_b64_tr_b16 a[112:113], v187 offset:12288\n\t"
        "ds_read_b64_tr_b16 a[114:115], v187 offset:14336\n\t"
        "ds_read_b64_tr_b16 a[116:117], v187 offset:12320\n\t"
        "ds_read_b64_tr_b16 a[118:119], v187 offset:14368\n\t"
        "ds_read_b64_tr_b16 a[120:121], v187 offset:12352\n\t"
        "ds_read_b64_tr_b16 a[122:123], v187 offset:14400\n\t"
        "ds_read_b64_tr_b16 a[124:125], v187 offset:12384\n\t"
        "ds_read_b64_tr_b16 a[126:127], v187 offset:14432\n\t"
        "ds_write_b128 v185, a[128:131] offset:0\n\t"
        "ds_write_b128 v185, a[132:135] offset:1152\n\t"
        "ds_write_b128 v185, a[136:139] offset:2304\n\t"
        "ds_write_b128 v185, a[140:143] offset:3456\n\t"
        "s_waitcnt lgkmcnt(0)\n\t"
        "ds_read_b64_tr_b16 a[128:129], v186 offset:0\n\t"
        "ds_read_b64_tr_b16 a[130:131], v186 offset:2304\n\t"
        "ds_read_b64_tr_b16 a[132:133], v186 offset:32\n\t"
        "ds_read_b64_tr_b16 a[134:135], v186 offset:2336\n\t"
        "ds_read_b64_tr_b16 a[136:137], v186 offset:64\n\t"
        "ds_read_b64_tr_b16 a[138:139], v186 offset:2368\n\t"
        "ds_read_b64_tr_b16 a[140:141], v186 offset:96\n\t"
        "ds_read_b64_tr_b16 a[142:143], v186 offset:2400\n\t"
        "ds_write_b128 v185, a[144:147] offset:5120\n\t"
        "ds_write_b128 v185, a[148:151] offset:6272\n\t"
        "ds_write_b128 v185, a[152:155] offset:7424\n\t"
        "ds_write_b128 v185, a[156:159] offset:8576\n\t"
        "s_waitcnt lgkmcnt(0)\n\t"
        "ds_read_b64_tr_b16 a[144:145], v186 offset:5120\n\t"
        "ds_read_b64_tr_b16 a[146:147], v186 offset:7424\n\t"
        "ds_read_b64_tr_b16 a[148:149], v186 offset:5152\n\t"
        "ds_read_b64_tr_b16 a[150:151], v186 offset:7456\n\t"
        "ds_read_b64_tr_b16 a[152:153], v186 offset:5184\n\t"
        "ds_read_b64_tr_b16 a[154:155], v186 offset:7488\n\t"
        "ds_read_b64_tr_b16 a[156:157], v186 offset:5216\n\t"
        "ds_read_b64_tr_b16 a[158:159], v186 offset:7520\n\t"
        "ds_write_b128 v185, a[160:163] offset:0\n\t"
        "ds_write_b128 v185, a[164:167] offset:1152\n\t"
        "ds_write_b128 v185, a[168:171] offset:2304\n\t"
        "ds_write_b128 v185, a[172:175] offset:3456\n\t"
        "s_waitcnt lgkmcnt(0)\n\t"
        "ds_read_b64_tr_b16 a[160:161], v186 offset:0\n\t"
        "ds_read_b64_tr_b16 a[162:163], v186 offset:2304\n\t"
        "ds_read_b64_tr_b16 a[164:165], v186 offset:32\n\t"
        "ds_read_b64_tr_b16 a[166:167], v186 offset:2336\n\t"
        "ds_read_b64_tr_b16 a[168:169], v186 offset:64\n\t"
        "ds_read_b64_tr_b16 a[170:171], v186 offset:2368\n\t"
        "ds_read_b64_tr_b16 a[172:173], v186 offset:96\n\t"
        "ds_read_b64_tr_b16 a[174:175], v186 offset:2400\n\t"
        "ds_write_b128 v185, a[176:179] offset:5120\n\t"
        "ds_write_b128 v185, a[180:183] offset:6272\n\t"
        "ds_write_b128 v185, a[184:187] offset:7424\n\t"
        "ds_write_b128 v185, a[188:191] offset:8576\n\t"
        "s_waitcnt lgkmcnt(0)\n\t"
        "ds_read_b64_tr_b16 a[176:177], v186 offset:5120\n\t"
        "ds_read_b64_tr_b16 a[178:179], v186 offset:7424\n\t"
        "ds_read_b64_tr_b16 a[180:181], v186 offset:5152\n\t"
        "ds_read_b64_tr_b16 a[182:183], v186 offset:7456\n\t"
        "ds_read_b64_tr_b16 a[184:185], v186 offset:5184\n\t"
        "ds_read_b64_tr_b16 a[186:187], v186 offset:7488\n\t"
        "ds_read_b64_tr_b16 a[188:189], v186 offset:5216\n\t"
        "ds_read_b64_tr_b16 a[190:191], v186 offset:7520\n\t"
        "ds_write_b128 v185, a[192:195] offset:0\n\t"
        "ds_write_b128 v185, a[196:199] offset:1152\n\t"
        "ds_write_b128 v185, a[200:203] offset:2304\n\t"
        "ds_write_b128 v185, a[204:207] offset:3456\n\t"
        "s_waitcnt lgkmcnt(0)\n\t"
        "ds_read_b64_tr_b16 a[192:193], v186 offset:0\n\t"
        "ds_read_b64_tr_b16 a[194:195], v186 offset:2304\n\t"
        "ds_read_b64_tr_b16 a[196:197], v186 offset:32\n\t"
        "ds_read_b64_tr_b16 a[198:199], v186 offset:2336\n\t"
        "ds_read_b64_tr_b16 a[200:201], v186 offset:64\n\t"
        "ds_read_b64_tr_b16 a[202:203], v186 offset:2368\n\t"
        "ds_read_b64_tr_b16 a[204:205], v186 offset:96\n\t"
        "ds_read_b64_tr_b16 a[206:207], v186 offset:2400\n\t"
        "ds_write_b128 v185, a[208:211] offset:5120\n\t"
        "ds_write_b128 v185, a[212:215] offset:6272\n\t"
        "ds_write_b128 v185, a[216:219] offset:7424\n\t"
        "ds_write_b128 v185, a[220:223] offset:8576\n\t"
        "s_waitcnt lgkmcnt(0)\n\t"
        "ds_read_b64_tr_b16 a[208:209], v186 offset:5120\n\t"
        "ds_read_b64_tr_b16 a[210:211], v186 offset:7424\n\t"
        "ds_read_b64_tr_b16 a[212:213], v186 offset:5152\n\t"
        "ds_read_b64_tr_b16 a[214:215], v186 offset:7456\n\t"
        "ds_read_b64_tr_b16 a[216:217], v186 offset:5184\n\t"
        "ds_read_b64_tr_b16 a[218:219], v186 offset:7488\n\t"
        "ds_read_b64_tr_b16 a[220:221], v186 offset:5216\n\t"
        "ds_read_b64_tr_b16 a[222:223], v186 offset:7520\n\t"
        "ds_write_b128 v185, a[224:227] offset:0\n\t"
        "ds_write_b128 v185, a[228:231] offset:1152\n\t"
        "ds_write_b128 v185, a[232:235] offset:2304\n\t"
        "ds_write_b128 v185, a[236:239] offset:3456\n\t"
        "s_waitcnt lgkmcnt(0)\n\t"
        "ds_read_b64_tr_b16 a[224:225], v186 offset:0\n\t"
        "ds_read_b64_tr_b16 a[226:227], v186 offset:2304\n\t"
        "ds_read_b64_tr_b16 a[228:229], v186 offset:32\n\t"
        "ds_read_b64_tr_b16 a[230:231], v186 offset:2336\n\t"
        "ds_read_b64_tr_b16 a[232:233], v186 offset:64\n\t"
        "ds_read_b64_tr_b16 a[234:235], v186 offset:2368\n\t"
        "ds_read_b64_tr_b16 a[236:237], v186 offset:96\n\t"
        "ds_read_b64_tr_b16 a[238:239], v186 offset:2400\n\t"
        "ds_write_b128 v185, a[240:243] offset:5120\n\t"
        "ds_write_b128 v185, a[244:247] offset:6272\n\t"
        "ds_write_b128 v185, a[248:251] offset:7424\n\t"
        "ds_write_b128 v185, a[252:255] offset:8576\n\t"
        "s_waitcnt lgkmcnt(0)\n\t"
        "ds_read_b64_tr_b16 a[240:241], v186 offset:5120\n\t"
        "ds_read_b64_tr_b16 a[242:243], v186 offset:7424\n\t"
        "ds_read_b64_tr_b16 a[244:245], v186 offset:5152\n\t"
        "ds_read_b64_tr_b16 a[246:247], v186 offset:7456\n\t"
        "ds_read_b64_tr_b16 a[248:249], v186 offset:5184\n\t"
        "ds_read_b64_tr_b16 a[250:251], v186 offset:7488\n\t"
        "ds_read_b64_tr_b16 a[252:253], v186 offset:5216\n\t"
        "ds_read_b64_tr_b16 a[254:255], v186 offset:7520\n\t"
        "s_waitcnt lgkmcnt(0)\n\t"
        "v_mov_b32_e32 v240, v176\n\t"
        "v_mov_b32_e32 v241, v177\n\t"
        "v_mov_b32_e32 v242, v178\n\t"
        "v_mov_b32_e32 v243, v179\n\t"
        "v_mov_b32_e32 v244, v180\n\t"
        "v_mov_b32_e32 v245, v181\n\t"
        "v_mov_b32_e32 v246, v182\n\t"
        "v_mov_b32_e32 v247, v183\n\t"
        "v_mov_b32_e32 v248, v184\n\t"
        "v_mov_b32_e32 v205, %[tag]\n\t"
        "v_mov_b32_e32 v207, %[tag]\n\t"
        "v_mov_b32_e32 v209, %[tag]\n\t"
        "v_mov_b32_e32 v211, %[tag]\n\t"
        "v_mov_b32_e32 v213, %[tag]\n\t"
        "v_mov_b32_e32 v215, %[tag]\n\t"
        "v_mov_b32_e32 v217, %[tag]\n\t"
        "v_mov_b32_e32 v219, %[tag]\n\t"
        "s_mov_b32 s36, 0xf0000000\n\t"
        "s_mov_b32 s37, 0\n\t"
        "s_mov_b32 s38, -1\n\t"
        "s_mov_b32 s39, -1\n\t"
        "s_mov_b32 s40, 0\n\t"
        "s_mov_b32 s43, 0\n\t"
        "s_lshl_b32 s45, %[w], 10\n\t"
        "s_add_u32 s45, s45, 69632\n\t"
        "s_lshl_b32 s44, %[rho], 1\n\t"
        "s_mov_b64 exec, 0\n\t"
        "s_branch .Lhk0%=\n\t"
        ".Lhr%=:\n\t"
        "s_mov_b64 exec, -1\n\t"
        "s_barrier\n\t"
        "s_mov_b32 s97, 0\n\t"
        "v_and_b32_e32 v186, 0x7c, v242\n\t"
        "s_mov_b32 s80, %c[fprep]\n\t"
        "s_mov_b32 s81, %c[fconv]\n\t"
        ".Lo1%=:\n\t"
        "buffer_load_dword v187, v186, %[rd], s80 offen sc1\n\t"
        "s_add_u32 s97, s97, 1\n\t"
        "s_cmp_lt_u32 s97, 0x200000\n\t"
        "s_cbranch_scc0 .Lgu%=\n\t"
        "s_waitcnt vmcnt(0)\n\t"
        "v_cmp_ne_u32_e32 vcc, %[tag], v187\n\t"
        "s_nop 3\n\t"
        "s_cbranch_vccz .Lo2%=\n\t"
        "s_sleep 8\n\t"
        "s_branch .Lo1%=\n\t"
        ".Lo2%=:\n\t"
        "buffer_load_dword v187, off, %[rd], s81 sc1\n\t"
        "s_add_u32 s97, s97, 1\n\t"
        "s_cmp_lt_u32 s97, 0x200000\n\t"
        "s_cbranch_scc0 .Lgu%=\n\t"
        "s_waitcnt vmcnt(0)\n\t"
        "v_cmp_gt_u32_e32 vcc, 0x100, v187\n\t"
        "s_nop 3\n\t"
        "s_cbranch_vccz .Lo3%=\n\t"
        "s_sleep 8\n\t"
        "s_branch .Lo2%=\n\t"
        ".Lo3%=:\n\t"
        ".Lnew%=:\n\t"
        "s_lshl_b32 s85, s37, 1\n\t"
        "s_and_b32 s80, s40, 0x0fffffff\n\t"
        "s_lshr_b32 s80, s80, s85\n\t"
        "s_andn2_b32 s80, 3, s80\n\t"
        "s_cbranch_scc0 .Lgo%=\n\t"
        "s_cmp_lt_i32 s39, 0\n\t"
        "s_cbranch_scc1 .Lo4%=\n\t"
        "s_waitcnt vmcnt(0)\n\t"
        "s_add_i32 s80, s39, 0\n\t"
        "s_lshl_b32 s80, s80, 15\n\t"
        "s_or_b32 s81, s39, %[snd]\n\t"
        "s_cmp_lt_i32 s81, 0\n\t"
        "s_cselect_b32 s83, s36, s80\n\t"
        "s_cbranch_scc1 .Lo7%=\n\t"
        "s_mov_b32 s97, 0\n\t"
        ".Lo5%=:\n\t"
        "v_xor_b32_e32 v186, %[tag], v161\n\t"
        "v_xor_b32_e32 v187, %[tag], v163\n\t"
        "v_xor_b32_e32 v188, %[tag], v165\n\t"
        "v_or3_b32 v186, v186, v187, v188\n\t"
        "v_xor_b32_e32 v187, %[tag], v167\n\t"
        "v_or_b32_e32 v186, v186, v187\n\t"
        "v_cmp_ne_u32_e32 vcc, 0, v186\n\t"
        "v_sub_f32_e32 v198, 1.0, v184\n\t"
        "v_add_f32_e32 v190, v152, v160\n\t"
        "v_add_f32_e32 v191, v153, v162\n\t"
        "v_add_f32_e32 v192, v154, v164\n\t"
        "v_add_f32_e32 v193, v155, v166\n\t"
        "s_cbranch_vccz .Lo6%=\n\t"
        "s_sleep %c[xslp]\n\t"
        "s_lshl_b32 s82, s83, 2\n\t"
        "buffer_load_dwordx4 v[160:163], v246, %[rd], s82 offen offset:0 sc1\n\t"
        "buffer_load_dwordx4 v[164:167], v246, %[rd], s82 offen offset:1024 sc1\n\t"
        "s_add_u32 s97, s97, 1\n\t"
        "s_cmp_lt_u32 s97, 0x200000\n\t"
        "s_cbranch_scc0 .Lgu%=\n\t"
        "s_waitcnt vmcnt(0)\n\t"
        "s_branch .Lo5%=\n\t"
        ".Lo6%=:\n\t"
        "v_cvt_pk_bf16_f32 v198, 0, v198\n\t"
        "v_cvt_pk_bf16_f32 v190, 0, v190\n\t"
        "v_cvt_pk_bf16_f32 v191, 0, v191\n\t"
        "v_cvt_pk_bf16_f32 v192, 0, v192\n\t"
        "v_cvt_pk_bf16_f32 v193, 0, v193\n\t"
        "v_lshlrev_b32_e32 v186, 16, v180\n\t"
        "v_lshlrev_b32_e32 v194, 16, v176\n\t"
        "v_and_b32_e32 v187, 0xffff0000, v180\n\t"
        "v_and_b32_e32 v195, 0xffff0000, v176\n\t"
        "v_lshlrev_b32_e32 v188, 16, v181\n\t"
        "v_lshlrev_b32_e32 v196, 16, v177\n\t"
        "v_and_b32_e32 v189, 0xffff0000, v181\n\t"
        "v_and_b32_e32 v197, 0xffff0000, v177\n\t"
        "v_mul_f32_e32 v186, v186, v194\n\t"
        "v_mul_f32_e32 v187, v187, v195\n\t"
        "v_mul_f32_e32 v188, v188, v196\n\t"
        "v_mul_f32_e32 v189, v189, v197\n\t"
        "v_mul_f32_e32 v190, v198, v190\n\t"
        "v_mul_f32_e32 v191, v198, v191\n\t"
        "v_mul_f32_e32 v192, v198, v192\n\t"
        "v_mul_f32_e32 v193, v198, v193\n\t"
        "v_cvt_pk_bf16_f32 v186, 0, v186\n\t"
        "v_cvt_pk_bf16_f32 v187, 0, v187\n\t"
        "v_cvt_pk_bf16_f32 v188, 0, v188\n\t"
        "v_cvt_pk_bf16_f32 v189, 0, v189\n\t"
        "v_cvt_pk_bf16_f32 v190, 0, v190\n\t"
        "v_cvt_pk_bf16_f32 v191, 0, v191\n\t"
        "v_cvt_pk_bf16_f32 v192, 0, v192\n\t"
        "v_cvt_pk_bf16_f32 v193, 0, v193\n\t"
        "v_mul_f32_e32 v186, v184, v186\n\t"
        "v_mul_f32_e32 v187, v184, v187\n\t"
        "v_mul_f32_e32 v188, v184, v188\n\t"
        "v_mul_f32_e32 v189, v184, v189\n\t"
        "v_cvt_pk_bf16_f32 v186, 0, v186\n\t"
        "v_cvt_pk_bf16_f32 v187, 0, v187\n\t"
        "v_cvt_pk_bf16_f32 v188, 0, v188\n\t"
        "v_cvt_pk_bf16_f32 v189, 0, v189\n\t"
        "v_add_f32_e32 v186, v186, v190\n\t"
        "v_add_f32_e32 v187, v187, v191\n\t"
        "v_add_f32_e32 v188, v188, v192\n\t"
        "v_add_f32_e32 v189, v189, v193\n\t"
        "v_cvt_pk_bf16_f32 v200, v186, v187\n\t"
        "v_cvt_pk_bf16_f32 v201, v188, v189\n\t"
        ".Lo7%=:\n\t"
        "buffer_store_dwordx2 v[200:201], v245, %[ro], s83 offen\n\t"
        "s_add_i32 s80, s39, 1\n\t"
        "s_lshl_b32 s80, s80, 15\n\t"
        "s_or_b32 s81, s39, %[snd]\n\t"
        "s_cmp_lt_i32 s81, 0\n\t"
        "s_cselect_b32 s83, s36, s80\n\t"
        "s_cbranch_scc1 .Lo10%=\n\t"
        "s_mov_b32 s97, 0\n\t"
        ".Lo8%=:\n\t"
        "v_xor_b32_e32 v186, %[tag], v169\n\t"
        "v_xor_b32_e32 v187, %[tag], v171\n\t"
        "v_xor_b32_e32 v188, %[tag], v173\n\t"
        "v_or3_b32 v186, v186, v187, v188\n\t"
        "v_xor_b32_e32 v187, %[tag], v175\n\t"
        "v_or_b32_e32 v186, v186, v187\n\t"
        "v_cmp_ne_u32_e32 vcc, 0, v186\n\t"
        "v_sub_f32_e32 v198, 1.0, v185\n\t"
        "v_add_f32_e32 v190, v156, v168\n\t"
        "v_add_f32_e32 v191, v157, v170\n\t"
        "v_add_f32_e32 v192, v158, v172\n\t"
        "v_add_f32_e32 v193, v159, v174\n\t"
        "s_cbranch_vccz .Lo9%=\n\t"
        "s_sleep %c[xslp]\n\t"
        "s_lshl_b32 s82, s83, 2\n\t"
        "buffer_load_dwordx4 v[168:171], v246, %[rd], s82 offen offset:0 sc1\n\t"
        "buffer_load_dwordx4 v[172:175], v246, %[rd], s82 offen offset:1024 sc1\n\t"
        "s_add_u32 s97, s97, 1\n\t"
        "s_cmp_lt_u32 s97, 0x200000\n\t"
        "s_cbranch_scc0 .Lgu%=\n\t"
        "s_waitcnt vmcnt(0)\n\t"
        "s_branch .Lo8%=\n\t"
        ".Lo9%=:\n\t"
        "v_cvt_pk_bf16_f32 v198, 0, v198\n\t"
        "v_cvt_pk_bf16_f32 v190, 0, v190\n\t"
        "v_cvt_pk_bf16_f32 v191, 0, v191\n\t"
        "v_cvt_pk_bf16_f32 v192, 0, v192\n\t"
        "v_cvt_pk_bf16_f32 v193, 0, v193\n\t"
        "v_lshlrev_b32_e32 v186, 16, v182\n\t"
        "v_lshlrev_b32_e32 v194, 16, v178\n\t"
        "v_and_b32_e32 v187, 0xffff0000, v182\n\t"
        "v_and_b32_e32 v195, 0xffff0000, v178\n\t"
        "v_lshlrev_b32_e32 v188, 16, v183\n\t"
        "v_lshlrev_b32_e32 v196, 16, v179\n\t"
        "v_and_b32_e32 v189, 0xffff0000, v183\n\t"
        "v_and_b32_e32 v197, 0xffff0000, v179\n\t"
        "v_mul_f32_e32 v186, v186, v194\n\t"
        "v_mul_f32_e32 v187, v187, v195\n\t"
        "v_mul_f32_e32 v188, v188, v196\n\t"
        "v_mul_f32_e32 v189, v189, v197\n\t"
        "v_mul_f32_e32 v190, v198, v190\n\t"
        "v_mul_f32_e32 v191, v198, v191\n\t"
        "v_mul_f32_e32 v192, v198, v192\n\t"
        "v_mul_f32_e32 v193, v198, v193\n\t"
        "v_cvt_pk_bf16_f32 v186, 0, v186\n\t"
        "v_cvt_pk_bf16_f32 v187, 0, v187\n\t"
        "v_cvt_pk_bf16_f32 v188, 0, v188\n\t"
        "v_cvt_pk_bf16_f32 v189, 0, v189\n\t"
        "v_cvt_pk_bf16_f32 v190, 0, v190\n\t"
        "v_cvt_pk_bf16_f32 v191, 0, v191\n\t"
        "v_cvt_pk_bf16_f32 v192, 0, v192\n\t"
        "v_cvt_pk_bf16_f32 v193, 0, v193\n\t"
        "v_mul_f32_e32 v186, v185, v186\n\t"
        "v_mul_f32_e32 v187, v185, v187\n\t"
        "v_mul_f32_e32 v188, v185, v188\n\t"
        "v_mul_f32_e32 v189, v185, v189\n\t"
        "v_cvt_pk_bf16_f32 v186, 0, v186\n\t"
        "v_cvt_pk_bf16_f32 v187, 0, v187\n\t"
        "v_cvt_pk_bf16_f32 v188, 0, v188\n\t"
        "v_cvt_pk_bf16_f32 v189, 0, v189\n\t"
        "v_add_f32_e32 v186, v186, v190\n\t"
        "v_add_f32_e32 v187, v187, v191\n\t"
        "v_add_f32_e32 v188, v188, v192\n\t"
        "v_add_f32_e32 v189, v189, v193\n\t"
        "v_cvt_pk_bf16_f32 v200, v186, v187\n\t"
        "v_cvt_pk_bf16_f32 v201, v188, v189\n\t"
        ".Lo10%=:\n\t"
        "buffer_store_dwordx2 v[200:201], v245, %[ro], s83 offen\n\t"
        "s_mov_b32 s39, -1\n\t"
        ".Lo4%=:\n\t"
        "s_cmp_eq_u32 s37, 14\n\t"
        "s_cbranch_scc1 .Lsol%=\n\t"
        "s_mov_b32 s97, 0\n\t"
        ".Lo11%=:\n\t"
        "s_lshr_b32 s80, s40, s85\n\t"
        "s_andn2_b32 s80, 3, s80\n\t"
        "s_cbranch_scc0 .Lo12%=\n\t"
        "s_cmp_eq_u32 s97, 0\n\t"
        "s_cbranch_scc1 .Lo11%=x\n\t"
        "s_sleep %c[oslp]\n\t"
        ".Lo11%=x:\n\t"
        "buffer_load_dword v220, v242, %[rd], 0 offen sc1\n\t"
        "s_add_u32 s97, s97, 1\n\t"
        "s_cmp_lt_u32 s97, 0x200000\n\t"
        "s_cbranch_scc0 .Lgu%=\n\t"
        "s_waitcnt vmcnt(0)\n\t"
        "v_cmp_eq_u32_e32 vcc, 32, v220\n\t"
        "s_nop 1\n\t"
        "s_or_b32 s40, s40, vcc_lo\n\t"
        "s_branch .Lo11%=\n\t"
        ".Lo12%=:\n\t"
        ".Lgo%=:\n\t"
        "s_lshl_b32 s84, s44, 17\n\t"
        "buffer_load_dwordx4 v[0:3], v240, %[rd], s84 offen offset:0 sc1\n\t"
        "buffer_load_dwordx4 v[4:7], v240, %[rd], s84 offen offset:1024 sc1\n\t"
        "buffer_load_dwordx4 v[8:11], v240, %[rd], s84 offen offset:2048 sc1\n\t"
        "buffer_load_dwordx4 v[12:15], v240, %[rd], s84 offen offset:3072 sc1\n\t"
        "s_add_u32 s84, s84, 8192\n\t"
        "buffer_load_dwordx4 v[16:19], v240, %[rd], s84 offen offset:0 sc1\n\t"
        "buffer_load_dwordx4 v[20:23], v240, %[rd], s84 offen offset:1024 sc1\n\t"
        "buffer_load_dwordx4 v[24:27], v240, %[rd], s84 offen offset:2048 sc1\n\t"
        "buffer_load_dwordx4 v[28:31], v240, %[rd], s84 offen offset:3072 sc1\n\t"
        "s_add_u32 s84, s84, 8192\n\t"
        "buffer_load_dwordx4 v[32:35], v240, %[rd], s84 offen offset:0 sc1\n\t"
        "buffer_load_dwordx4 v[36:39], v240, %[rd], s84 offen offset:1024 sc1\n\t"
        "buffer_load_dwordx4 v[40:43], v240, %[rd], s84 offen offset:2048 sc1\n\t"
        "buffer_load_dwordx4 v[44:47], v240, %[rd], s84 offen offset:3072 sc1\n\t"
        "s_add_u32 s84, s84, 8192\n\t"
        "buffer_load_dwordx4 v[48:51], v240, %[rd], s84 offen offset:0 sc1\n\t"
        "buffer_load_dwordx4 v[52:55], v240, %[rd], s84 offen offset:1024 sc1\n\t"
        "buffer_load_dwordx4 v[56:59], v240, %[rd], s84 offen offset:2048 sc1\n\t"
        "buffer_load_dwordx4 v[60:63], v240, %[rd], s84 offen offset:3072 sc1\n\t"
        "s_sub_u32 s84, s84, 20480\n\t"
        "buffer_load_dwordx4 v[64:67], v240, %[rd], s84 offen offset:0 sc1\n\t"
        "buffer_load_dwordx4 v[68:71], v240, %[rd], s84 offen offset:1024 sc1\n\t"
        "buffer_load_dwordx4 v[72:75], v240, %[rd], s84 offen offset:2048 sc1\n\t"
        "buffer_load_dwordx4 v[76:79], v240, %[rd], s84 offen offset:3072 sc1\n\t"
        "s_add_u32 s84, s84, 8192\n\t"
        "buffer_load_dwordx4 v[80:83], v240, %[rd], s84 offen offset:0 sc1\n\t"
        "buffer_load_dwordx4 v[84:87], v240, %[rd], s84 offen offset:1024 sc1\n\t"
        "buffer_load_dwordx4 v[88:91], v240, %[rd], s84 offen offset:2048 sc1\n\t"
        "buffer_load_dwordx4 v[92:95], v240, %[rd], s84 offen offset:3072 sc1\n\t"
        "s_add_u32 s84, s84, 8192\n\t"
        "buffer_load_dwordx4 v[96:99], v240, %[rd], s84 offen offset:0 sc1\n\t"
        "buffer_load_dwordx4 v[100:103], v240, %[rd], s84 offen offset:1024 sc1\n\t"
        "buffer_load_dwordx4 v[104:107], v240, %[rd], s84 offen offset:2048 sc1\n\t"
        "buffer_load_dwordx4 v[108:111], v240, %[rd], s84 offen offset:3072 sc1\n\t"
        "s_add_u32 s84, s84, 8192\n\t"
        "buffer_load_dwordx4 v[112:115], v240, %[rd], s84 offen offset:0 sc1\n\t"
        "buffer_load_dwordx4 v[116:119], v240, %[rd], s84 offen offset:1024 sc1\n\t"
        "buffer_load_dwordx4 v[120:123], v240, %[rd], s84 offen offset:2048 sc1\n\t"
        "buffer_load_dwordx4 v[124:127], v240, %[rd], s84 offen offset:3072 sc1\n\t"
        "s_add_u32 s84, s84, 8192\n\t"
        "buffer_store_dword v0, off, %[ro], s36\n\t"
        "buffer_store_dword v0, off, %[ro], s36\n\t"
        "buffer_store_dword v0, off, %[ro], s36\n\t"
        "buffer_store_dword v0, off, %[ro], s36\n\t"
        "buffer_store_dword v0, off, %[ro], s36\n\t"
        "buffer_store_dword v0, off, %[ro], s36\n\t"
        "buffer_store_dword v0, off, %[ro], s36\n\t"
        "buffer_store_dword v0, off, %[ro], s36\n\t"
        "buffer_store_dword v0, off, %[ro], s36\n\t"
        "buffer_store_dword v0, off, %[ro], s36\n\t"
        "buffer_store_dword v0, off, %[ro], s36\n\t"
        "buffer_store_dword v0, off, %[ro], s36\n\t"
        "buffer_store_dword v0, off, %[ro], s36\n\t"
        "buffer_store_dword v0, off, %[ro], s36\n\t"
        "buffer_store_dword v0, off, %[ro], s36\n\t"
        "buffer_store_dword v0, off, %[ro], s36\n\t"
        "buffer_store_dword v0, off, %[ro], s36\n\t"
        ".Lrnd%=:\n\t"
        "s_lshl_b32 s85, s37, 1\n\t"
        "s_and_b32 s86, s40, 0x0fffffff\n\t"
        "s_lshr_b32 s86, s86, s85\n\t"
        "s_andn2_b32 s80, 12, s86\n\t"
        "s_cselect_b32 s41, 0, 1\n\t"
        "s_add_u32 s84, s44, 8\n\t"
        "s_lshl_b32 s84, s84, 17\n\t"
        "s_cmp_eq_u32 s41, 0\n\t"
        "s_cselect_b32 s84, s36, s84\n\t"
        "s_cselect_b32 s87, 0, s36\n\t"
        "s_andn2_b32 s80, 48, s86\n\t"
        "s_cselect_b32 s80, 0, s36\n\t"
        "v_add_u32_e32 v222, s43, v247\n\t"
        "s_sub_u32 s81, 33280, s43\n\t"
        "v_add_u32_e32 v223, s81, v248\n\t"
        "buffer_load_dword v220, v242, %[rd], s80 offen sc1\n\t"
        "s_waitcnt vmcnt(49)\n\t"
        "v_mfma_f32_16x16x32_bf16 v[128:131], a[128:131], v[0:3], 0\n\t"
        "ds_read_b128 v[186:189], v223 offset:0\n\t"
        "ds_read_b128 v[190:193], v223 offset:8320\n\t"
        "v_mfma_f32_16x16x32_bf16 v[132:135], a[132:135], v[0:3], 0\n\t"
        "ds_read_b128 v[194:197], v223 offset:16640\n\t"
        "ds_read_b128 v[198:201], v223 offset:24960\n\t"
        "v_mfma_f32_16x16x32_bf16 v[136:139], a[136:139], v[0:3], 0\n\t"
        "s_nop 0\n\t"
        "s_nop 0\n\t"
        "v_mfma_f32_16x16x32_bf16 v[140:143], a[140:143], v[0:3], 0\n\t"
        "s_nop 0\n\t"
        "s_nop 0\n\t"
        "buffer_load_dwordx4 v[0:3], v240, %[rd], s84 offen offset:0 sc1\n\t"
        "s_waitcnt vmcnt(49)\n\t"
        "v_mfma_f32_16x16x32_bf16 v[128:131], a[144:147], v[4:7], v[128:131]\n\t"
        "s_nop 0\n\t"
        "s_nop 0\n\t"
        "v_mfma_f32_16x16x32_bf16 v[132:135], a[148:151], v[4:7], v[132:135]\n\t"
        "s_waitcnt lgkmcnt(0)\n\t"
        "v_add_f32_e32 v186, v186, v190\n\t"
        "v_mfma_f32_16x16x32_bf16 v[136:139], a[152:155], v[4:7], v[136:139]\n\t"
        "v_add_f32_e32 v187, v187, v191\n\t"
        "v_add_f32_e32 v188, v188, v192\n\t"
        "v_mfma_f32_16x16x32_bf16 v[140:143], a[156:159], v[4:7], v[140:143]\n\t"
        "v_add_f32_e32 v189, v189, v193\n\t"
        "v_add_f32_e32 v194, v194, v198\n\t"
        "buffer_load_dwordx4 v[4:7], v240, %[rd], s84 offen offset:1024 sc1\n\t"
        "s_waitcnt vmcnt(49)\n\t"
        "v_mfma_f32_16x16x32_bf16 v[128:131], a[160:163], v[8:11], v[128:131]\n\t"
        "v_add_f32_e32 v195, v195, v199\n\t"
        "v_add_f32_e32 v196, v196, v200\n\t"
        "v_mfma_f32_16x16x32_bf16 v[132:135], a[164:167], v[8:11], v[132:135]\n\t"
        "v_add_f32_e32 v197, v197, v201\n\t"
        "v_add_f32_e32 v204, v186, v194\n\t"
        "v_mfma_f32_16x16x32_bf16 v[136:139], a[168:171], v[8:11], v[136:139]\n\t"
        "v_add_f32_e32 v206, v187, v195\n\t"
        "v_add_f32_e32 v208, v188, v196\n\t"
        "v_mfma_f32_16x16x32_bf16 v[140:143], a[172:175], v[8:11], v[140:143]\n\t"
        "v_add_f32_e32 v210, v189, v197\n\t"
        "s_add_i32 s80, s38, 0\n\t"
        "buffer_load_dwordx4 v[8:11], v240, %[rd], s84 offen offset:2048 sc1\n\t"
        "s_waitcnt vmcnt(47)\n\t"
        "v_mfma_f32_16x16x32_bf16 v[128:131], a[176:179], v[12:15], v[128:131]\n\t"
        "s_lshl_b32 s80, s80, 17\n\t"
        "s_or_b32 s81, s38, %[own]\n\t"
        "v_mfma_f32_16x16x32_bf16 v[132:135], a[180:183], v[12:15], v[132:135]\n\t"
        "s_cmp_lt_i32 s81, 0\n\t"
        "s_cselect_b32 s82, s36, s80\n\t"
        "buffer_store_dwordx4 v[204:207], v246, %[rd], s82 offen offset:0 sc1\n\t"
        "v_mfma_f32_16x16x32_bf16 v[136:139], a[184:187], v[12:15], v[136:139]\n\t"
        "buffer_store_dwordx4 v[208:211], v246, %[rd], s82 offen offset:1024 sc1\n\t"
        "v_mov_b32_e32 v144, v204\n\t"
        "v_mfma_f32_16x16x32_bf16 v[140:143], a[188:191], v[12:15], v[140:143]\n\t"
        "v_mov_b32_e32 v145, v206\n\t"
        "v_mov_b32_e32 v146, v208\n\t"
        "buffer_load_dwordx4 v[12:15], v240, %[rd], s84 offen offset:3072 sc1\n\t"
        "s_add_u32 s84, s84, 8192\n\t"
        "s_waitcnt vmcnt(49)\n\t"
        "v_mfma_f32_16x16x32_bf16 v[128:131], a[192:195], v[16:19], v[128:131]\n\t"
        "v_mov_b32_e32 v147, v210\n\t"
        "ds_read_b128 v[186:189], v223 offset:4160\n\t"
        "v_mfma_f32_16x16x32_bf16 v[132:135], a[196:199], v[16:19], v[132:135]\n\t"
        "ds_read_b128 v[190:193], v223 offset:12480\n\t"
        "ds_read_b128 v[194:197], v223 offset:20800\n\t"
        "v_mfma_f32_16x16x32_bf16 v[136:139], a[200:203], v[16:19], v[136:139]\n\t"
        "ds_read_b128 v[198:201], v223 offset:29120\n\t"
        "s_nop 0\n\t"
        "v_mfma_f32_16x16x32_bf16 v[140:143], a[204:207], v[16:19], v[140:143]\n\t"
        "s_nop 0\n\t"
        "s_nop 0\n\t"
        "buffer_load_dwordx4 v[16:19], v240, %[rd], s84 offen offset:0 sc1\n\t"
        "s_waitcnt vmcnt(49)\n\t"
        "v_mfma_f32_16x16x32_bf16 v[128:131], a[208:211], v[20:23], v[128:131]\n\t"
        "s_nop 0\n\t"
        "s_nop 0\n\t"
        "v_mfma_f32_16x16x32_bf16 v[132:135], a[212:215], v[20:23], v[132:135]\n\t"
        "s_nop 0\n\t"
        "s_waitcnt lgkmcnt(0)\n\t"
        "v_mfma_f32_16x16x32_bf16 v[136:139], a[216:219], v[20:23], v[136:139]\n\t"
        "v_add_f32_e32 v186, v186, v190\n\t"
        "v_add_f32_e32 v187, v187, v191\n\t"
        "v_mfma_f32_16x16x32_bf16 v[140:143], a[220:223], v[20:23], v[140:143]\n\t"
        "v_add_f32_e32 v188, v188, v192\n\t"
        "v_add_f32_e32 v189, v189, v193\n\t"
        "buffer_load_dwordx4 v[20:23], v240, %[rd], s84 offen offset:1024 sc1\n\t"
        "s_waitcnt vmcnt(49)\n\t"
        "v_mfma_f32_16x16x32_bf16 v[128:131], a[224:227], v[24:27], v[128:131]\n\t"
        "v_add_f32_e32 v194, v194, v198\n\t"
        "v_add_f32_e32 v195, v195, v199\n\t"
        "v_mfma_f32_16x16x32_bf16 v[132:135], a[228:231], v[24:27], v[132:135]\n\t"
        "v_add_f32_e32 v196, v196, v200\n\t"
        "v_add_f32_e32 v197, v197, v201\n\t"
        "v_mfma_f32_16x16x32_bf16 v[136:139], a[232:235], v[24:27], v[136:139]\n\t"
        "v_add_f32_e32 v212, v186, v194\n\t"
        "v_add_f32_e32 v214, v187, v195\n\t"
        "v_mfma_f32_16x16x32_bf16 v[140:143], a[236:239], v[24:27], v[140:143]\n\t"
        "v_add_f32_e32 v216, v188, v196\n\t"
        "v_add_f32_e32 v218, v189, v197\n\t"
        "buffer_load_dwordx4 v[24:27], v240, %[rd], s84 offen offset:2048 sc1\n\t"
        "s_waitcnt vmcnt(47)\n\t"
        "v_mfma_f32_16x16x32_bf16 v[128:131], a[240:243], v[28:31], v[128:131]\n\t"
        "s_add_i32 s80, s38, 1\n\t"
        "s_lshl_b32 s80, s80, 17\n\t"
        "v_mfma_f32_16x16x32_bf16 v[132:135], a[244:247], v[28:31], v[132:135]\n\t"
        "s_or_b32 s81, s38, %[own]\n\t"
        "s_cmp_lt_i32 s81, 0\n\t"
        "s_cselect_b32 s82, s36, s80\n\t"
        "v_mfma_f32_16x16x32_bf16 v[136:139], a[248:251], v[28:31], v[136:139]\n\t"
        "buffer_store_dwordx4 v[212:215], v246, %[rd], s82 offen offset:0 sc1\n\t"
        "buffer_store_dwordx4 v[216:219], v246, %[rd], s82 offen offset:1024 sc1\n\t"
        "v_mfma_f32_16x16x32_bf16 v[140:143], a[252:255], v[28:31], v[140:143]\n\t"
        "v_mov_b32_e32 v148, v212\n\t"
        "v_mov_b32_e32 v149, v214\n\t"
        "buffer_load_dwordx4 v[28:31], v240, %[rd], s84 offen offset:3072 sc1\n\t"
        "s_add_u32 s84, s84, 8192\n\t"
        "s_waitcnt vmcnt(49)\n\t"
        "v_mfma_f32_16x16x32_bf16 v[128:131], a[0:3], v[32:35], v[128:131]\n\t"
        "v_mov_b32_e32 v150, v216\n\t"
        "v_mov_b32_e32 v151, v218\n\t"
        "v_mfma_f32_16x16x32_bf16 v[132:135], a[4:7], v[32:35], v[132:135]\n\t"
        "s_waitcnt vmcnt(34)\n\t"
        "s_add_i32 s80, s39, 0\n\t"
        "v_mfma_f32_16x16x32_bf16 v[136:139], a[8:11], v[32:35], v[136:139]\n\t"
        "s_lshl_b32 s80, s80, 15\n\t"
        "s_or_b32 s81, s39, %[snd]\n\t"
        "v_mfma_f32_16x16x32_bf16 v[140:143], a[12:15], v[32:35], v[140:143]\n\t"
        "s_cmp_lt_i32 s81, 0\n\t"
        "s_cselect_b32 s83, s36, s80\n\t"
        "s_cselect_b64 s[88:89], 0, -1\n\t"
        "v_xor_b32_e32 v224, %[tag], v161\n\t"
        "buffer_load_dwordx4 v[32:35], v240, %[rd], s84 offen offset:0 sc1\n\t"
        "s_waitcnt vmcnt(49)\n\t"
        "v_mfma_f32_16x16x32_bf16 v[128:131], a[16:19], v[36:39], v[128:131]\n\t"
        "v_xor_b32_e32 v225, %[tag], v163\n\t"
        "v_xor_b32_e32 v226, %[tag], v165\n\t"
        "v_mfma_f32_16x16x32_bf16 v[132:135], a[20:23], v[36:39], v[132:135]\n\t"
        "v_or3_b32 v224, v224, v225, v226\n\t"
        "v_xor_b32_e32 v225, %[tag], v167\n\t"
        "v_mfma_f32_16x16x32_bf16 v[136:139], a[24:27], v[36:39], v[136:139]\n\t"
        "v_or_b32_e32 v224, v224, v225\n\t"
        "v_cmp_ne_u32_e32 vcc, 0, v224\n\t"
        "v_mfma_f32_16x16x32_bf16 v[140:143], a[28:31], v[36:39], v[140:143]\n\t"
        "v_sub_f32_e32 v236, 1.0, v184\n\t"
        "v_cvt_pk_bf16_f32 v236, 0, v236\n\t"
        "s_and_b64 vcc, vcc, s[88:89]\n\t"
        "s_cbranch_scc1 .Lo13%=\n\t"
        ".Lo14%=:\n\t"
        "buffer_load_dwordx4 v[36:39], v240, %[rd], s84 offen offset:1024 sc1\n\t"
        "s_waitcnt vmcnt(49)\n\t"
        "v_mfma_f32_16x16x32_bf16 v[128:131], a[32:35], v[40:43], v[128:131]\n\t"
        "v_add_f32_e32 v228, v152, v160\n\t"
        "v_add_f32_e32 v229, v153, v162\n\t"
        "v_mfma_f32_16x16x32_bf16 v[132:135], a[36:39], v[40:43], v[132:135]\n\t"
        "v_add_f32_e32 v230, v154, v164\n\t"
        "v_add_f32_e32 v231, v155, v166\n\t"
        "v_mfma_f32_16x16x32_bf16 v[136:139], a[40:43], v[40:43], v[136:139]\n\t"
        "v_cvt_pk_bf16_f32 v228, 0, v228\n\t"
        "v_cvt_pk_bf16_f32 v229, 0, v229\n\t"
        "v_mfma_f32_16x16x32_bf16 v[140:143], a[44:47], v[40:43], v[140:143]\n\t"
        "v_cvt_pk_bf16_f32 v230, 0, v230\n\t"
        "v_cvt_pk_bf16_f32 v231, 0, v231\n\t"
        "buffer_load_dwordx4 v[40:43], v240, %[rd], s84 offen offset:2048 sc1\n\t"
        "s_waitcnt vmcnt(49)\n\t"
        "v_mfma_f32_16x16x32_bf16 v[128:131], a[48:51], v[44:47], v[128:131]\n\t"
        "v_lshlrev_b32_e32 v224, 16, v180\n\t"
        "v_lshlrev_b32_e32 v232, 16, v176\n\t"
        "v_mfma_f32_16x16x32_bf16 v[132:135], a[52:55], v[44:47], v[132:135]\n\t"
        "v_and_b32_e32 v225, 0xffff0000, v180\n\t"
        "v_and_b32_e32 v233, 0xffff0000, v176\n\t"
        "v_mfma_f32_16x16x32_bf16 v[136:139], a[56:59], v[44:47], v[136:139]\n\t"
        "v_lshlrev_b32_e32 v226, 16, v181\n\t"
        "v_lshlrev_b32_e32 v234, 16, v177\n\t"
        "v_mfma_f32_16x16x32_bf16 v[140:143], a[60:63], v[44:47], v[140:143]\n\t"
        "v_and_b32_e32 v227, 0xffff0000, v181\n\t"
        "v_and_b32_e32 v235, 0xffff0000, v177\n\t"
        "buffer_load_dwordx4 v[44:47], v240, %[rd], s84 offen offset:3072 sc1\n\t"
        "s_add_u32 s84, s84, 8192\n\t"
        "s_waitcnt vmcnt(49)\n\t"
        "v_mfma_f32_16x16x32_bf16 v[128:131], a[64:67], v[48:51], v[128:131]\n\t"
        "v_mul_f32_e32 v224, v224, v232\n\t"
        "v_mul_f32_e32 v225, v225, v233\n\t"
        "v_mfma_f32_16x16x32_bf16 v[132:135], a[68:71], v[48:51], v[132:135]\n\t"
        "v_mul_f32_e32 v226, v226, v234\n\t"
        "v_mul_f32_e32 v227, v227, v235\n\t"
        "v_mfma_f32_16x16x32_bf16 v[136:139], a[72:75], v[48:51], v[136:139]\n\t"
        "v_mul_f32_e32 v228, v236, v228\n\t"
        "v_mul_f32_e32 v229, v236, v229\n\t"
        "v_mfma_f32_16x16x32_bf16 v[140:143], a[76:79], v[48:51], v[140:143]\n\t"
        "v_mul_f32_e32 v230, v236, v230\n\t"
        "v_mul_f32_e32 v231, v236, v231\n\t"
        "buffer_load_dwordx4 v[48:51], v240, %[rd], s84 offen offset:0 sc1\n\t"
        "s_waitcnt vmcnt(49)\n\t"
        "v_mfma_f32_16x16x32_bf16 v[128:131], a[80:83], v[52:55], v[128:131]\n\t"
        "v_cvt_pk_bf16_f32 v224, 0, v224\n\t"
        "v_cvt_pk_bf16_f32 v225, 0, v225\n\t"
        "v_mfma_f32_16x16x32_bf16 v[132:135], a[84:87], v[52:55], v[132:135]\n\t"
        "v_cvt_pk_bf16_f32 v226, 0, v226\n\t"
        "v_cvt_pk_bf16_f32 v227, 0, v227\n\t"
        "v_mfma_f32_16x16x32_bf16 v[136:139], a[88:91], v[52:55], v[136:139]\n\t"
        "v_cvt_pk_bf16_f32 v228, 0, v228\n\t"
        "v_cvt_pk_bf16_f32 v229, 0, v229\n\t"
        "v_mfma_f32_16x16x32_bf16 v[140:143], a[92:95], v[52:55], v[140:143]\n\t"
        "v_cvt_pk_bf16_f32 v230, 0, v230\n\t"
        "v_cvt_pk_bf16_f32 v231, 0, v231\n\t"
        "buffer_load_dwordx4 v[52:55], v240, %[rd], s84 offen offset:1024 sc1\n\t"
        "s_waitcnt vmcnt(49)\n\t"
        "v_mfma_f32_16x16x32_bf16 v[128:131], a[96:99], v[56:59], v[128:131]\n\t"
        "v_mul_f32_e32 v224, v184, v224\n\t"
        "v_mul_f32_e32 v225, v184, v225\n\t"
        "v_mfma_f32_16x16x32_bf16 v[132:135], a[100:103], v[56:59], v[132:135]\n\t"
        "v_mul_f32_e32 v226, v184, v226\n\t"
        "v_mul_f32_e32 v227, v184, v227\n\t"
        "v_mfma_f32_16x16x32_bf16 v[136:139], a[104:107], v[56:59], v[136:139]\n\t"
        "v_cvt_pk_bf16_f32 v224, 0, v224\n\t"
        "v_cvt_pk_bf16_f32 v225, 0, v225\n\t"
        "v_mfma_f32_16x16x32_bf16 v[140:143], a[108:111], v[56:59], v[140:143]\n\t"
        "v_cvt_pk_bf16_f32 v226, 0, v226\n\t"
        "v_cvt_pk_bf16_f32 v227, 0, v227\n\t"
        "buffer_load_dwordx4 v[56:59], v240, %[rd], s84 offen offset:2048 sc1\n\t"
        "s_waitcnt vmcnt(48)\n\t"
        "v_mfma_f32_16x16x32_bf16 v[128:131], a[112:115], v[60:63], v[128:131]\n\t"
        "v_add_f32_e32 v224, v224, v228\n\t"
        "v_add_f32_e32 v225, v225, v229\n\t"
        "v_mfma_f32_16x16x32_bf16 v[132:135], a[116:119], v[60:63], v[132:135]\n\t"
        "v_add_f32_e32 v226, v226, v230\n\t"
        "v_add_f32_e32 v227, v227, v231\n\t"
        "v_mfma_f32_16x16x32_bf16 v[136:139], a[120:123], v[60:63], v[136:139]\n\t"
        "v_cvt_pk_bf16_f32 v238, v224, v225\n\t"
        "v_cvt_pk_bf16_f32 v239, v226, v227\n\t"
        "v_mfma_f32_16x16x32_bf16 v[140:143], a[124:127], v[60:63], v[140:143]\n\t"
        "buffer_store_dwordx2 v[238:239], v245, %[ro], s83 offen\n\t"
        "s_add_i32 s80, s38, 0\n\t"
        "buffer_load_dwordx4 v[60:63], v240, %[rd], s84 offen offset:3072 sc1\n\t"
        "s_sub_u32 s84, s84, 20480\n\t"
        "ds_write_b128 v222, v[128:131] offset:0\n\t"
        "ds_write_b128 v222, v[132:135] offset:1040\n\t"
        "ds_write_b128 v222, v[136:139] offset:2080\n\t"
        "s_nop 0\n\t"
        "ds_write_b128 v222, v[140:143] offset:3120\n\t"
        "s_waitcnt vmcnt(47)\n\t"
        "v_mfma_f32_16x16x32_bf16 v[128:131], a[128:131], v[64:67], 0\n\t"
        "s_lshl_b32 s80, s80, 6\n\t"
        "s_or_b32 s81, s38, %[snd]\n\t"
        "v_mfma_f32_16x16x32_bf16 v[132:135], a[132:135], v[64:67], 0\n\t"
        "s_cmp_lt_i32 s81, 0\n\t"
        "s_cselect_b32 s82, s36, s80\n\t"
        "buffer_load_dword v184, v244, %[rd], s82 offen sc1\n\t"
        "v_mfma_f32_16x16x32_bf16 v[136:139], a[136:139], v[64:67], 0\n\t"
        "s_lshl_b32 s80, s80, 1\n\t"
        "s_cmp_lt_i32 s81, 0\n\t"
        "s_cselect_b32 s82, s36, s80\n\t"
        "v_mfma_f32_16x16x32_bf16 v[140:143], a[140:143], v[64:67], 0\n\t"
        "buffer_load_dwordx2 v[180:181], v243, %[rd], s82 offen sc1\n\t"
        "s_lshl_b32 s80, s80, 8\n\t"
        "buffer_load_dwordx4 v[64:67], v240, %[rd], s84 offen offset:0 sc1\n\t"
        "s_waitcnt vmcnt(46)\n\t"
        "v_mfma_f32_16x16x32_bf16 v[128:131], a[144:147], v[68:71], v[128:131]\n\t"
        "s_cmp_lt_i32 s81, 0\n\t"
        "s_cselect_b32 s82, s36, s80\n\t"
        "buffer_load_dwordx2 v[176:177], v245, %[rx], s82 offen\n\t"
        "v_mfma_f32_16x16x32_bf16 v[132:135], a[148:151], v[68:71], v[132:135]\n\t"
        "s_add_i32 s80, s38, 0\n\t"
        "s_lshl_b32 s80, s80, 17\n\t"
        "v_mfma_f32_16x16x32_bf16 v[136:139], a[152:155], v[68:71], v[136:139]\n\t"
        "s_or_b32 s81, s38, %[snd]\n\t"
        "s_cmp_lt_i32 s81, 0\n\t"
        "s_cselect_b32 s82, s36, s80\n\t"
        "v_mfma_f32_16x16x32_bf16 v[140:143], a[156:159], v[68:71], v[140:143]\n\t"
        "buffer_load_dwordx4 v[160:163], v246, %[rd], s82 offen offset:0 sc1\n\t"
        "buffer_load_dwordx4 v[164:167], v246, %[rd], s82 offen offset:1024 sc1\n\t"
        "buffer_load_dwordx4 v[68:71], v240, %[rd], s84 offen offset:1024 sc1\n\t"
        "s_waitcnt vmcnt(49)\n\t"
        "v_mfma_f32_16x16x32_bf16 v[128:131], a[160:163], v[72:75], v[128:131]\n\t"
        "s_waitcnt vmcnt(34)\n\t"
        "v_mfma_f32_16x16x32_bf16 v[132:135], a[164:167], v[72:75], v[132:135]\n\t"
        "s_add_i32 s80, s39, 1\n\t"
        "s_lshl_b32 s80, s80, 15\n\t"
        "v_mfma_f32_16x16x32_bf16 v[136:139], a[168:171], v[72:75], v[136:139]\n\t"
        "s_or_b32 s81, s39, %[snd]\n\t"
        "s_cmp_lt_i32 s81, 0\n\t"
        "s_cselect_b32 s83, s36, s80\n\t"
        "s_cselect_b64 s[88:89], 0, -1\n\t"
        "v_mfma_f32_16x16x32_bf16 v[140:143], a[172:175], v[72:75], v[140:143]\n\t"
        "v_xor_b32_e32 v224, %[tag], v169\n\t"
        "v_xor_b32_e32 v225, %[tag], v171\n\t"
        "buffer_load_dwordx4 v[72:75], v240, %[rd], s84 offen offset:2048 sc1\n\t"
        "s_waitcnt vmcnt(49)\n\t"
        "v_mfma_f32_16x16x32_bf16 v[128:131], a[176:179], v[76:79], v[128:131]\n\t"
        "v_xor_b32_e32 v226, %[tag], v173\n\t"
        "v_or3_b32 v224, v224, v225, v226\n\t"
        "v_mfma_f32_16x16x32_bf16 v[132:135], a[180:183], v[76:79], v[132:135]\n\t"
        "v_xor_b32_e32 v225, %[tag], v175\n\t"
        "v_or_b32_e32 v224, v224, v225\n\t"
        "v_mfma_f32_16x16x32_bf16 v[136:139], a[184:187], v[76:79], v[136:139]\n\t"
        "v_cmp_ne_u32_e32 vcc, 0, v224\n\t"
        "v_sub_f32_e32 v236, 1.0, v185\n\t"
        "v_mfma_f32_16x16x32_bf16 v[140:143], a[188:191], v[76:79], v[140:143]\n\t"
        "v_cvt_pk_bf16_f32 v236, 0, v236\n\t"
        "s_and_b64 vcc, vcc, s[88:89]\n\t"
        "s_cbranch_scc1 .Lo15%=\n\t"
        ".Lo16%=:\n\t"
        "v_add_f32_e32 v228, v156, v168\n\t"
        "buffer_load_dwordx4 v[76:79], v240, %[rd], s84 offen offset:3072 sc1\n\t"
        "s_add_u32 s84, s84, 8192\n\t"
        "s_waitcnt vmcnt(49)\n\t"
        "v_mfma_f32_16x16x32_bf16 v[128:131], a[192:195], v[80:83], v[128:131]\n\t"
        "v_add_f32_e32 v229, v157, v170\n\t"
        "v_add_f32_e32 v230, v158, v172\n\t"
        "v_mfma_f32_16x16x32_bf16 v[132:135], a[196:199], v[80:83], v[132:135]\n\t"
        "v_add_f32_e32 v231, v159, v174\n\t"
        "v_cvt_pk_bf16_f32 v228, 0, v228\n\t"
        "v_mfma_f32_16x16x32_bf16 v[136:139], a[200:203], v[80:83], v[136:139]\n\t"
        "v_cvt_pk_bf16_f32 v229, 0, v229\n\t"
        "v_cvt_pk_bf16_f32 v230, 0, v230\n\t"
        "v_mfma_f32_16x16x32_bf16 v[140:143], a[204:207], v[80:83], v[140:143]\n\t"
        "v_cvt_pk_bf16_f32 v231, 0, v231\n\t"
        "v_lshlrev_b32_e32 v224, 16, v182\n\t"
        "buffer_load_dwordx4 v[80:83], v240, %[rd], s84 offen offset:0 sc1\n\t"
        "s_waitcnt vmcnt(49)\n\t"
        "v_mfma_f32_16x16x32_bf16 v[128:131], a[208:211], v[84:87], v[128:131]\n\t"
        "v_lshlrev_b32_e32 v232, 16, v178\n\t"
        "v_and_b32_e32 v225, 0xffff0000, v182\n\t"
        "v_mfma_f32_16x16x32_bf16 v[132:135], a[212:215], v[84:87], v[132:135]\n\t"
        "v_and_b32_e32 v233, 0xffff0000, v178\n\t"
        "v_lshlrev_b32_e32 v226, 16, v183\n\t"
        "v_mfma_f32_16x16x32_bf16 v[136:139], a[216:219], v[84:87], v[136:139]\n\t"
        "v_lshlrev_b32_e32 v234, 16, v179\n\t"
        "v_and_b32_e32 v227, 0xffff0000, v183\n\t"
        "v_mfma_f32_16x16x32_bf16 v[140:143], a[220:223], v[84:87], v[140:143]\n\t"
        "v_and_b32_e32 v235, 0xffff0000, v179\n\t"
        "v_mul_f32_e32 v224, v224, v232\n\t"
        "buffer_load_dwordx4 v[84:87], v240, %[rd], s84 offen offset:1024 sc1\n\t"
        "s_waitcnt vmcnt(49)\n\t"
        "v_mfma_f32_16x16x32_bf16 v[128:131], a[224:227], v[88:91], v[128:131]\n\t"
        "v_mul_f32_e32 v225, v225, v233\n\t"
        "v_mul_f32_e32 v226, v226, v234\n\t"
        "v_mfma_f32_16x16x32_bf16 v[132:135], a[228:231], v[88:91], v[132:135]\n\t"
        "v_mul_f32_e32 v227, v227, v235\n\t"
        "v_mul_f32_e32 v228, v236, v228\n\t"
        "v_mfma_f32_16x16x32_bf16 v[136:139], a[232:235], v[88:91], v[136:139]\n\t"
        "v_mul_f32_e32 v229, v236, v229\n\t"
        "v_mul_f32_e32 v230, v236, v230\n\t"
        "v_mfma_f32_16x16x32_bf16 v[140:143], a[236:239], v[88:91], v[140:143]\n\t"
        "v_mul_f32_e32 v231, v236, v231\n\t"
        "v_cvt_pk_bf16_f32 v224, 0, v224\n\t"
        "buffer_load_dwordx4 v[88:91], v240, %[rd], s84 offen offset:2048 sc1\n\t"
        "s_waitcnt vmcnt(49)\n\t"
        "v_mfma_f32_16x16x32_bf16 v[128:131], a[240:243], v[92:95], v[128:131]\n\t"
        "v_cvt_pk_bf16_f32 v225, 0, v225\n\t"
        "v_cvt_pk_bf16_f32 v226, 0, v226\n\t"
        "v_mfma_f32_16x16x32_bf16 v[132:135], a[244:247], v[92:95], v[132:135]\n\t"
        "v_cvt_pk_bf16_f32 v227, 0, v227\n\t"
        "v_cvt_pk_bf16_f32 v228, 0, v228\n\t"
        "v_mfma_f32_16x16x32_bf16 v[136:139], a[248:251], v[92:95], v[136:139]\n\t"
        "v_cvt_pk_bf16_f32 v229, 0, v229\n\t"
        "v_cvt_pk_bf16_f32 v230, 0, v230\n\t"
        "v_mfma_f32_16x16x32_bf16 v[140:143], a[252:255], v[92:95], v[140:143]\n\t"
        "v_cvt_pk_bf16_f32 v231, 0, v231\n\t"
        "v_mul_f32_e32 v224, v185, v224\n\t"
        "buffer_load_dwordx4 v[92:95], v240, %[rd], s84 offen offset:3072 sc1\n\t"
        "s_add_u32 s84, s84, 8192\n\t"
        "buffer_load_dword v221, v242, %[rd], s87 offen sc1\n\t"
        "s_waitcnt vmcnt(49)\n\t"
        "v_mfma_f32_16x16x32_bf16 v[128:131], a[0:3], v[96:99], v[128:131]\n\t"
        "v_mul_f32_e32 v225, v185, v225\n\t"
        "v_mul_f32_e32 v226, v185, v226\n\t"
        "v_mfma_f32_16x16x32_bf16 v[132:135], a[4:7], v[96:99], v[132:135]\n\t"
        "v_mul_f32_e32 v227, v185, v227\n\t"
        "v_cvt_pk_bf16_f32 v224, 0, v224\n\t"
        "v_mfma_f32_16x16x32_bf16 v[136:139], a[8:11], v[96:99], v[136:139]\n\t"
        "v_cvt_pk_bf16_f32 v225, 0, v225\n\t"
        "v_cvt_pk_bf16_f32 v226, 0, v226\n\t"
        "v_mfma_f32_16x16x32_bf16 v[140:143], a[12:15], v[96:99], v[140:143]\n\t"
        "v_cvt_pk_bf16_f32 v227, 0, v227\n\t"
        "v_add_f32_e32 v224, v224, v228\n\t"
        "buffer_load_dwordx4 v[96:99], v240, %[rd], s84 offen offset:0 sc1\n\t"
        "s_waitcnt vmcnt(48)\n\t"
        "v_mfma_f32_16x16x32_bf16 v[128:131], a[16:19], v[100:103], v[128:131]\n\t"
        "v_add_f32_e32 v225, v225, v229\n\t"
        "v_add_f32_e32 v226, v226, v230\n\t"
        "v_mfma_f32_16x16x32_bf16 v[132:135], a[20:23], v[100:103], v[132:135]\n\t"
        "v_add_f32_e32 v227, v227, v231\n\t"
        "v_cvt_pk_bf16_f32 v238, v224, v225\n\t"
        "v_mfma_f32_16x16x32_bf16 v[136:139], a[24:27], v[100:103], v[136:139]\n\t"
        "v_cvt_pk_bf16_f32 v239, v226, v227\n\t"
        "buffer_store_dwordx2 v[238:239], v245, %[ro], s83 offen\n\t"
        "v_mfma_f32_16x16x32_bf16 v[140:143], a[28:31], v[100:103], v[140:143]\n\t"
        "s_add_i32 s80, s38, 1\n\t"
        "s_lshl_b32 s80, s80, 6\n\t"
        "buffer_load_dwordx4 v[100:103], v240, %[rd], s84 offen offset:1024 sc1\n\t"
        "s_waitcnt vmcnt(47)\n\t"
        "v_mfma_f32_16x16x32_bf16 v[128:131], a[32:35], v[104:107], v[128:131]\n\t"
        "s_or_b32 s81, s38, %[snd]\n\t"
        "s_cmp_lt_i32 s81, 0\n\t"
        "s_cselect_b32 s82, s36, s80\n\t"
        "v_mfma_f32_16x16x32_bf16 v[132:135], a[36:39], v[104:107], v[132:135]\n\t"
        "buffer_load_dword v185, v244, %[rd], s82 offen sc1\n\t"
        "s_lshl_b32 s80, s80, 1\n\t"
        "v_mfma_f32_16x16x32_bf16 v[136:139], a[40:43], v[104:107], v[136:139]\n\t"
        "s_cmp_lt_i32 s81, 0\n\t"
        "s_cselect_b32 s82, s36, s80\n\t"
        "buffer_load_dwordx2 v[182:183], v243, %[rd], s82 offen sc1\n\t"
        "v_mfma_f32_16x16x32_bf16 v[140:143], a[44:47], v[104:107], v[140:143]\n\t"
        "s_lshl_b32 s80, s80, 8\n\t"
        "s_cmp_lt_i32 s81, 0\n\t"
        "s_cselect_b32 s82, s36, s80\n\t"
        "buffer_load_dwordx4 v[104:107], v240, %[rd], s84 offen offset:2048 sc1\n\t"
        "s_waitcnt vmcnt(46)\n\t"
        "v_mfma_f32_16x16x32_bf16 v[128:131], a[48:51], v[108:111], v[128:131]\n\t"
        "buffer_load_dwordx2 v[178:179], v245, %[rx], s82 offen\n\t"
        "s_add_i32 s80, s38, 1\n\t"
        "v_mfma_f32_16x16x32_bf16 v[132:135], a[52:55], v[108:111], v[132:135]\n\t"
        "s_lshl_b32 s80, s80, 17\n\t"
        "s_or_b32 s81, s38, %[snd]\n\t"
        "v_mfma_f32_16x16x32_bf16 v[136:139], a[56:59], v[108:111], v[136:139]\n\t"
        "s_cmp_lt_i32 s81, 0\n\t"
        "s_cselect_b32 s82, s36, s80\n\t"
        "buffer_load_dwordx4 v[168:171], v246, %[rd], s82 offen offset:0 sc1\n\t"
        "v_mfma_f32_16x16x32_bf16 v[140:143], a[60:63], v[108:111], v[140:143]\n\t"
        "buffer_load_dwordx4 v[172:175], v246, %[rd], s82 offen offset:1024 sc1\n\t"
        "buffer_load_dwordx4 v[108:111], v240, %[rd], s84 offen offset:3072 sc1\n\t"
        "s_add_u32 s84, s84, 8192\n\t"
        "s_waitcnt vmcnt(49)\n\t"
        "v_mfma_f32_16x16x32_bf16 v[128:131], a[64:67], v[112:115], v[128:131]\n\t"
        "v_mfma_f32_16x16x32_bf16 v[132:135], a[68:71], v[112:115], v[132:135]\n\t"
        "v_mfma_f32_16x16x32_bf16 v[136:139], a[72:75], v[112:115], v[136:139]\n\t"
        "v_mfma_f32_16x16x32_bf16 v[140:143], a[76:79], v[112:115], v[140:143]\n\t"
        "buffer_load_dwordx4 v[112:115], v240, %[rd], s84 offen offset:0 sc1\n\t"
        "s_waitcnt vmcnt(49)\n\t"
        "v_mfma_f32_16x16x32_bf16 v[128:131], a[80:83], v[116:119], v[128:131]\n\t"
        "v_mfma_f32_16x16x32_bf16 v[132:135], a[84:87], v[116:119], v[132:135]\n\t"
        "v_mfma_f32_16x16x32_bf16 v[136:139], a[88:91], v[116:119], v[136:139]\n\t"
        "v_mfma_f32_16x16x32_bf16 v[140:143], a[92:95], v[116:119], v[140:143]\n\t"
        "buffer_load_dwordx4 v[116:119], v240, %[rd], s84 offen offset:1024 sc1\n\t"
        "s_waitcnt vmcnt(49)\n\t"
        "v_mfma_f32_16x16x32_bf16 v[128:131], a[96:99], v[120:123], v[128:131]\n\t"
        "v_mfma_f32_16x16x32_bf16 v[132:135], a[100:103], v[120:123], v[132:135]\n\t"
        "v_mfma_f32_16x16x32_bf16 v[136:139], a[104:107], v[120:123], v[136:139]\n\t"
        "v_mfma_f32_16x16x32_bf16 v[140:143], a[108:111], v[120:123], v[140:143]\n\t"
        "buffer_load_dwordx4 v[120:123], v240, %[rd], s84 offen offset:2048 sc1\n\t"
        "s_waitcnt vmcnt(49)\n\t"
        "v_mfma_f32_16x16x32_bf16 v[128:131], a[112:115], v[124:127], v[128:131]\n\t"
        "v_mfma_f32_16x16x32_bf16 v[132:135], a[116:119], v[124:127], v[132:135]\n\t"
        "v_mfma_f32_16x16x32_bf16 v[136:139], a[120:123], v[124:127], v[136:139]\n\t"
        "v_mfma_f32_16x16x32_bf16 v[140:143], a[124:127], v[124:127], v[140:143]\n\t"
        "buffer_load_dwordx4 v[124:127], v240, %[rd], s84 offen offset:3072 sc1\n\t"
        "s_add_u32 s84, s84, 8192\n\t"
        "s_nop 2\n\t"
        "ds_write_b128 v222, v[128:131] offset:4160\n\t"
        "ds_write_b128 v222, v[132:135] offset:5200\n\t"
        "ds_write_b128 v222, v[136:139] offset:6240\n\t"
        "ds_write_b128 v222, v[140:143] offset:7280\n\t"
        "s_waitcnt vmcnt(49)\n\t"
        "v_cmp_eq_u32_e32 vcc, 32, v220\n\t"
        "s_nop 1\n\t"
        "s_or_b32 s40, s40, vcc_lo\n\t"
        "s_waitcnt lgkmcnt(0)\n\t"
        "s_barrier\n\t"
        "s_cmp_eq_u32 s41, 0\n\t"
        "s_cbranch_scc1 .Lfl%=\n\t"
        "v_mov_b32_e32 v152, v144\n\t"
        "v_mov_b32_e32 v153, v145\n\t"
        "v_mov_b32_e32 v154, v146\n\t"
        "v_mov_b32_e32 v155, v147\n\t"
        "v_mov_b32_e32 v156, v148\n\t"
        "v_mov_b32_e32 v157, v149\n\t"
        "v_mov_b32_e32 v158, v150\n\t"
        "v_mov_b32_e32 v159, v151\n\t"
        "s_mov_b32 s39, s38\n\t"
        "s_mov_b32 s38, s44\n\t"
        "s_add_u32 s37, s37, 1\n\t"
        "s_add_u32 s44, s44, 8\n\t"
        "s_sub_u32 s43, 33280, s43\n\t"
        "s_branch .Lrnd%=\n\t"
        ".Lfl%=:\n\t"
        "v_add_u32_e32 v223, s43, v248\n\t"
        "buffer_load_dword v220, v242, %[rd], 0 offen sc1\n\t"
        "ds_read_b128 v[186:189], v223 offset:0\n\t"
        "ds_read_b128 v[190:193], v223 offset:8320\n\t"
        "ds_read_b128 v[194:197], v223 offset:16640\n\t"
        "ds_read_b128 v[198:201], v223 offset:24960\n\t"
        "s_waitcnt lgkmcnt(0)\n\t"
        "v_add_f32_e32 v186, v186, v190\n\t"
        "v_add_f32_e32 v187, v187, v191\n\t"
        "v_add_f32_e32 v188, v188, v192\n\t"
        "v_add_f32_e32 v189, v189, v193\n\t"
        "v_add_f32_e32 v194, v194, v198\n\t"
        "v_add_f32_e32 v195, v195, v199\n\t"
        "v_add_f32_e32 v196, v196, v200\n\t"
        "v_add_f32_e32 v197, v197, v201\n\t"
        "v_add_f32_e32 v204, v186, v194\n\t"
        "v_add_f32_e32 v206, v187, v195\n\t"
        "v_add_f32_e32 v208, v188, v196\n\t"
        "v_add_f32_e32 v210, v189, v197\n\t"
        "s_add_i32 s80, s44, 0\n\t"
        "s_lshl_b32 s80, s80, 17\n\t"
        "s_or_b32 s81, s44, %[own]\n\t"
        "s_cmp_lt_i32 s81, 0\n\t"
        "s_cselect_b32 s82, s36, s80\n\t"
        "buffer_store_dwordx4 v[204:207], v246, %[rd], s82 offen offset:0 sc1\n\t"
        "buffer_store_dwordx4 v[208:211], v246, %[rd], s82 offen offset:1024 sc1\n\t"
        "v_mov_b32_e32 v128, v204\n\t"
        "v_mov_b32_e32 v129, v206\n\t"
        "v_mov_b32_e32 v130, v208\n\t"
        "v_mov_b32_e32 v131, v210\n\t"
        "ds_read_b128 v[186:189], v223 offset:4160\n\t"
        "ds_read_b128 v[190:193], v223 offset:12480\n\t"
        "ds_read_b128 v[194:197], v223 offset:20800\n\t"
        "ds_read_b128 v[198:201], v223 offset:29120\n\t"
        "s_waitcnt lgkmcnt(0)\n\t"
        "v_add_f32_e32 v186, v186, v190\n\t"
        "v_add_f32_e32 v187, v187, v191\n\t"
        "v_add_f32_e32 v188, v188, v192\n\t"
        "v_add_f32_e32 v189, v189, v193\n\t"
        "v_add_f32_e32 v194, v194, v198\n\t"
        "v_add_f32_e32 v195, v195, v199\n\t"
        "v_add_f32_e32 v196, v196, v200\n\t"
        "v_add_f32_e32 v197, v197, v201\n\t"
        "v_add_f32_e32 v212, v186, v194\n\t"
        "v_add_f32_e32 v214, v187, v195\n\t"
        "v_add_f32_e32 v216, v188, v196\n\t"
        "v_add_f32_e32 v218, v189, v197\n\t"
        "s_add_i32 s80, s44, 1\n\t"
        "s_lshl_b32 s80, s80, 17\n\t"
        "s_or_b32 s81, s44, %[own]\n\t"
        "s_cmp_lt_i32 s81, 0\n\t"
        "s_cselect_b32 s82, s36, s80\n\t"
        "buffer_store_dwordx4 v[212:215], v246, %[rd], s82 offen offset:0 sc1\n\t"
        "buffer_store_dwordx4 v[216:219], v246, %[rd], s82 offen offset:1024 sc1\n\t"
        "v_mov_b32_e32 v132, v212\n\t"
        "v_mov_b32_e32 v133, v214\n\t"
        "v_mov_b32_e32 v134, v216\n\t"
        "v_mov_b32_e32 v135, v218\n\t"
        "s_waitcnt vmcnt(4)\n\t"
        "v_cmp_eq_u32_e32 vcc, 32, v220\n\t"
        "s_nop 1\n\t"
        "s_or_b32 s40, s40, vcc_lo\n\t"
        "s_add_i32 s80, s38, 0\n\t"
        "s_lshl_b32 s80, s80, 15\n\t"
        "s_or_b32 s81, s38, %[snd]\n\t"
        "s_cmp_lt_i32 s81, 0\n\t"
        "s_cselect_b32 s83, s36, s80\n\t"
        "s_cbranch_scc1 .Lo19%=\n\t"
        "s_mov_b32 s97, 0\n\t"
        ".Lo17%=:\n\t"
        "v_xor_b32_e32 v186, %[tag], v161\n\t"
        "v_xor_b32_e32 v187, %[tag], v163\n\t"
        "v_xor_b32_e32 v188, %[tag], v165\n\t"
        "v_or3_b32 v186, v186, v187, v188\n\t"
        "v_xor_b32_e32 v187, %[tag], v167\n\t"
        "v_or_b32_e32 v186, v186, v187\n\t"
        "v_cmp_ne_u32_e32 vcc, 0, v186\n\t"
        "v_sub_f32_e32 v198, 1.0, v184\n\t"
        "v_add_f32_e32 v190, v144, v160\n\t"
        "v_add_f32_e32 v191, v145, v162\n\t"
        "v_add_f32_e32 v192, v146, v164\n\t"
        "v_add_f32_e32 v193, v147, v166\n\t"
        "s_cbranch_vccz .Lo18%=\n\t"
        "s_sleep %c[xslp]\n\t"
        "s_lshl_b32 s82, s83, 2\n\t"
        "buffer_load_dwordx4 v[160:163], v246, %[rd], s82 offen offset:0 sc1\n\t"
        "buffer_load_dwordx4 v[164:167], v246, %[rd], s82 offen offset:1024 sc1\n\t"
        "s_add_u32 s97, s97, 1\n\t"
        "s_cmp_lt_u32 s97, 0x200000\n\t"
        "s_cbranch_scc0 .Lgu%=\n\t"
        "s_waitcnt vmcnt(0)\n\t"
        "s_branch .Lo17%=\n\t"
        ".Lo18%=:\n\t"
        "v_cvt_pk_bf16_f32 v198, 0, v198\n\t"
        "v_cvt_pk_bf16_f32 v190, 0, v190\n\t"
        "v_cvt_pk_bf16_f32 v191, 0, v191\n\t"
        "v_cvt_pk_bf16_f32 v192, 0, v192\n\t"
        "v_cvt_pk_bf16_f32 v193, 0, v193\n\t"
        "v_lshlrev_b32_e32 v186, 16, v180\n\t"
        "v_lshlrev_b32_e32 v194, 16, v176\n\t"
        "v_and_b32_e32 v187, 0xffff0000, v180\n\t"
        "v_and_b32_e32 v195, 0xffff0000, v176\n\t"
        "v_lshlrev_b32_e32 v188, 16, v181\n\t"
        "v_lshlrev_b32_e32 v196, 16, v177\n\t"
        "v_and_b32_e32 v189, 0xffff0000, v181\n\t"
        "v_and_b32_e32 v197, 0xffff0000, v177\n\t"
        "v_mul_f32_e32 v186, v186, v194\n\t"
        "v_mul_f32_e32 v187, v187, v195\n\t"
        "v_mul_f32_e32 v188, v188, v196\n\t"
        "v_mul_f32_e32 v189, v189, v197\n\t"
        "v_mul_f32_e32 v190, v198, v190\n\t"
        "v_mul_f32_e32 v191, v198, v191\n\t"
        "v_mul_f32_e32 v192, v198, v192\n\t"
        "v_mul_f32_e32 v193, v198, v193\n\t"
        "v_cvt_pk_bf16_f32 v186, 0, v186\n\t"
        "v_cvt_pk_bf16_f32 v187, 0, v187\n\t"
        "v_cvt_pk_bf16_f32 v188, 0, v188\n\t"
        "v_cvt_pk_bf16_f32 v189, 0, v189\n\t"
        "v_cvt_pk_bf16_f32 v190, 0, v190\n\t"
        "v_cvt_pk_bf16_f32 v191, 0, v191\n\t"
        "v_cvt_pk_bf16_f32 v192, 0, v192\n\t"
        "v_cvt_pk_bf16_f32 v193, 0, v193\n\t"
        "v_mul_f32_e32 v186, v184, v186\n\t"
        "v_mul_f32_e32 v187, v184, v187\n\t"
        "v_mul_f32_e32 v188, v184, v188\n\t"
        "v_mul_f32_e32 v189, v184, v189\n\t"
        "v_cvt_pk_bf16_f32 v186, 0, v186\n\t"
        "v_cvt_pk_bf16_f32 v187, 0, v187\n\t"
        "v_cvt_pk_bf16_f32 v188, 0, v188\n\t"
        "v_cvt_pk_bf16_f32 v189, 0, v189\n\t"
        "v_add_f32_e32 v186, v186, v190\n\t"
        "v_add_f32_e32 v187, v187, v191\n\t"
        "v_add_f32_e32 v188, v188, v192\n\t"
        "v_add_f32_e32 v189, v189, v193\n\t"
        "v_cvt_pk_bf16_f32 v200, v186, v187\n\t"
        "v_cvt_pk_bf16_f32 v201, v188, v189\n\t"
        ".Lo19%=:\n\t"
        "buffer_store_dwordx2 v[200:201], v245, %[ro], s83 offen\n\t"
        "s_add_i32 s80, s38, 1\n\t"
        "s_lshl_b32 s80, s80, 15\n\t"
        "s_or_b32 s81, s38, %[snd]\n\t"
        "s_cmp_lt_i32 s81, 0\n\t"
        "s_cselect_b32 s83, s36, s80\n\t"
        "s_cbranch_scc1 .Lo22%=\n\t"
        "s_mov_b32 s97, 0\n\t"
        ".Lo20%=:\n\t"
        "v_xor_b32_e32 v186, %[tag], v169\n\t"
        "v_xor_b32_e32 v187, %[tag], v171\n\t"
        "v_xor_b32_e32 v188, %[tag], v173\n\t"
        "v_or3_b32 v186, v186, v187, v188\n\t"
        "v_xor_b32_e32 v187, %[tag], v175\n\t"
        "v_or_b32_e32 v186, v186, v187\n\t"
        "v_cmp_ne_u32_e32 vcc, 0, v186\n\t"
        "v_sub_f32_e32 v198, 1.0, v185\n\t"
        "v_add_f32_e32 v190, v148, v168\n\t"
        "v_add_f32_e32 v191, v149, v170\n\t"
        "v_add_f32_e32 v192, v150, v172\n\t"
        "v_add_f32_e32 v193, v151, v174\n\t"
        "s_cbranch_vccz .Lo21%=\n\t"
        "s_sleep %c[xslp]\n\t"
        "s_lshl_b32 s82, s83, 2\n\t"
        "buffer_load_dwordx4 v[168:171], v246, %[rd], s82 offen offset:0 sc1\n\t"
        "buffer_load_dwordx4 v[172:175], v246, %[rd], s82 offen offset:1024 sc1\n\t"
        "s_add_u32 s97, s97, 1\n\t"
        "s_cmp_lt_u32 s97, 0x200000\n\t"
        "s_cbranch_scc0 .Lgu%=\n\t"
        "s_waitcnt vmcnt(0)\n\t"
        "s_branch .Lo20%=\n\t"
        ".Lo21%=:\n\t"
        "v_cvt_pk_bf16_f32 v198, 0, v198\n\t"
        "v_cvt_pk_bf16_f32 v190, 0, v190\n\t"
        "v_cvt_pk_bf16_f32 v191, 0, v191\n\t"
        "v_cvt_pk_bf16_f32 v192, 0, v192\n\t"
        "v_cvt_pk_bf16_f32 v193, 0, v193\n\t"
        "v_lshlrev_b32_e32 v186, 16, v182\n\t"
        "v_lshlrev_b32_e32 v194, 16, v178\n\t"
        "v_and_b32_e32 v187, 0xffff0000, v182\n\t"
        "v_and_b32_e32 v195, 0xffff0000, v178\n\t"
        "v_lshlrev_b32_e32 v188, 16, v183\n\t"
        "v_lshlrev_b32_e32 v196, 16, v179\n\t"
        "v_and_b32_e32 v189, 0xffff0000, v183\n\t"
        "v_and_b32_e32 v197, 0xffff0000, v179\n\t"
        "v_mul_f32_e32 v186, v186, v194\n\t"
        "v_mul_f32_e32 v187, v187, v195\n\t"
        "v_mul_f32_e32 v188, v188, v196\n\t"
        "v_mul_f32_e32 v189, v189, v197\n\t"
        "v_mul_f32_e32 v190, v198, v190\n\t"
        "v_mul_f32_e32 v191, v198, v191\n\t"
        "v_mul_f32_e32 v192, v198, v192\n\t"
        "v_mul_f32_e32 v193, v198, v193\n\t"
        "v_cvt_pk_bf16_f32 v186, 0, v186\n\t"
        "v_cvt_pk_bf16_f32 v187, 0, v187\n\t"
        "v_cvt_pk_bf16_f32 v188, 0, v188\n\t"
        "v_cvt_pk_bf16_f32 v189, 0, v189\n\t"
        "v_cvt_pk_bf16_f32 v190, 0, v190\n\t"
        "v_cvt_pk_bf16_f32 v191, 0, v191\n\t"
        "v_cvt_pk_bf16_f32 v192, 0, v192\n\t"
        "v_cvt_pk_bf16_f32 v193, 0, v193\n\t"
        "v_mul_f32_e32 v186, v185, v186\n\t"
        "v_mul_f32_e32 v187, v185, v187\n\t"
        "v_mul_f32_e32 v188, v185, v188\n\t"
        "v_mul_f32_e32 v189, v185, v189\n\t"
        "v_cvt_pk_bf16_f32 v186, 0, v186\n\t"
        "v_cvt_pk_bf16_f32 v187, 0, v187\n\t"
        "v_cvt_pk_bf16_f32 v188, 0, v188\n\t"
        "v_cvt_pk_bf16_f32 v189, 0, v189\n\t"
        "v_add_f32_e32 v186, v186, v190\n\t"
        "v_add_f32_e32 v187, v187, v191\n\t"
        "v_add_f32_e32 v188, v188, v192\n\t"
        "v_add_f32_e32 v189, v189, v193\n\t"
        "v_cvt_pk_bf16_f32 v200, v186, v187\n\t"
        "v_cvt_pk_bf16_f32 v201, v188, v189\n\t"
        ".Lo22%=:\n\t"
        "buffer_store_dwordx2 v[200:201], v245, %[ro], s83 offen\n\t"
        "s_add_i32 s80, s44, 0\n\t"
        "s_lshl_b32 s80, s80, 6\n\t"
        "s_or_b32 s81, s44, %[snd]\n\t"
        "s_cmp_lt_i32 s81, 0\n\t"
        "s_cselect_b32 s82, s36, s80\n\t"
        "buffer_load_dword v184, v244, %[rd], s82 offen sc1\n\t"
        "s_lshl_b32 s80, s80, 1\n\t"
        "s_cmp_lt_i32 s81, 0\n\t"
        "s_cselect_b32 s82, s36, s80\n\t"
        "buffer_load_dwordx2 v[180:181], v243, %[rd], s82 offen sc1\n\t"
        "s_lshl_b32 s80, s80, 8\n\t"
        "s_cmp_lt_i32 s81, 0\n\t"
        "s_cselect_b32 s82, s36, s80\n\t"
        "buffer_load_dwordx2 v[176:177], v245, %[rx], s82 offen\n\t"
        "s_add_i32 s80, s44, 1\n\t"
        "s_lshl_b32 s80, s80, 6\n\t"
        "s_or_b32 s81, s44, %[snd]\n\t"
        "s_cmp_lt_i32 s81, 0\n\t"
        "s_cselect_b32 s82, s36, s80\n\t"
        "buffer_load_dword v185, v244, %[rd], s82 offen sc1\n\t"
        "s_lshl_b32 s80, s80, 1\n\t"
        "s_cmp_lt_i32 s81, 0\n\t"
        "s_cselect_b32 s82, s36, s80\n\t"
        "buffer_load_dwordx2 v[182:183], v243, %[rd], s82 offen sc1\n\t"
        "s_lshl_b32 s80, s80, 8\n\t"
        "s_cmp_lt_i32 s81, 0\n\t"
        "s_cselect_b32 s82, s36, s80\n\t"
        "buffer_load_dwordx2 v[178:179], v245, %[rx], s82 offen\n\t"
        "s_sleep %c[ydel]\n\t"
        "s_add_i32 s80, s44, 0\n\t"
        "s_lshl_b32 s80, s80, 17\n\t"
        "s_or_b32 s81, s44, %[snd]\n\t"
        "s_cmp_lt_i32 s81, 0\n\t"
        "s_cselect_b32 s82, s36, s80\n\t"
        "buffer_load_dwordx4 v[160:163], v246, %[rd], s82 offen offset:0 sc1\n\t"
        "buffer_load_dwordx4 v[164:167], v246, %[rd], s82 offen offset:1024 sc1\n\t"
        "s_add_i32 s80, s44, 1\n\t"
        "s_lshl_b32 s80, s80, 17\n\t"
        "s_or_b32 s81, s44, %[snd]\n\t"
        "s_cmp_lt_i32 s81, 0\n\t"
        "s_cselect_b32 s82, s36, s80\n\t"
        "buffer_load_dwordx4 v[168:171], v246, %[rd], s82 offen offset:0 sc1\n\t"
        "buffer_load_dwordx4 v[172:175], v246, %[rd], s82 offen offset:1024 sc1\n\t"
        "s_mov_b32 s38, -1\n\t"
        "v_mov_b32_e32 v152, v128\n\t"
        "v_mov_b32_e32 v153, v129\n\t"
        "v_mov_b32_e32 v154, v130\n\t"
        "v_mov_b32_e32 v155, v131\n\t"
        "v_mov_b32_e32 v156, v132\n\t"
        "v_mov_b32_e32 v157, v133\n\t"
        "v_mov_b32_e32 v158, v134\n\t"
        "v_mov_b32_e32 v159, v135\n\t"
        "s_mov_b32 s39, s44\n\t"
        "s_add_u32 s37, s37, 1\n\t"
        "s_add_u32 s44, s44, 8\n\t"
        "s_sub_u32 s43, 33280, s43\n\t"
        "s_branch .Lnew%=\n\t"
        ".Lsol%=:\n\t"
        "s_mov_b32 s37, 28\n\t"
        ".Lone%=:\n\t"
        ".Lhk0%=:\n\t"
        "s_cbranch_execz .Lhk1%=\n\t"
        "s_lshl2_add_u32 s44, s37, %[rho]\n\t"
        "s_cmp_eq_u32 s37, 30\n\t"
        "s_cselect_b32 s80, 2, 0\n\t"
        "s_xor_b32 s44, s44, s80\n\t"
        "s_add_i32 s80, s44, 0\n\t"
        "s_lshl_b32 s80, s80, 6\n\t"
        "s_or_b32 s81, s44, %[snd]\n\t"
        "s_cmp_lt_i32 s81, 0\n\t"
        ".Lhk1%=:\n\t"
        "s_cbranch_execz .Lhk2%=\n\t"
        "s_cselect_b32 s82, s36, s80\n\t"
        "buffer_load_dword v184, v244, %[rd], s82 offen sc1\n\t"
        "s_lshl_b32 s80, s80, 1\n\t"
        "s_cmp_lt_i32 s81, 0\n\t"
        "s_cselect_b32 s82, s36, s80\n\t"
        "buffer_load_dwordx2 v[180:181], v243, %[rd], s82 offen sc1\n\t"
        "s_lshl_b32 s80, s80, 8\n\t"
        "s_cmp_lt_i32 s81, 0\n\t"
        ".Lhk2%=:\n\t"
        "s_cbranch_execz .Lhk3%=\n\t"
        "s_cselect_b32 s82, s36, s80\n\t"
        "buffer_load_dwordx2 v[176:177], v245, %[rx], s82 offen\n\t"
        "v_add_u32_e32 v222, s43, v247\n\t"
        "v_add_u32_e32 v223, s43, v248\n\t"
        "s_mov_b32 s97, 0\n\t"
        ".Lo23%=:\n\t"
        "s_lshr_b32 s80, s40, s37\n\t"
        "s_andn2_b32 s80, 1, s80\n\t"
        "s_cbranch_scc0 .Lo24%=\n\t"
        ".Lhk3%=:\n\t"
        "s_cbranch_execz .Lhk4%=\n\t"
        "s_cmp_eq_u32 s97, 0\n\t"
        "s_cbranch_scc1 .Lo23%=x\n\t"
        "s_sleep %c[oslp]\n\t"
        ".Lo23%=x:\n\t"
        "buffer_load_dword v220, v242, %[rd], 0 offen sc1\n\t"
        "s_add_u32 s97, s97, 1\n\t"
        "s_cmp_lt_u32 s97, 0x200000\n\t"
        "s_cbranch_scc0 .Lgu%=\n\t"
        "s_waitcnt vmcnt(0)\n\t"
        ".Lhk4%=:\n\t"
        "s_cbranch_execz .Lhk5%=\n\t"
        "v_cmp_eq_u32_e32 vcc, 32, v220\n\t"
        "s_nop 1\n\t"
        "s_or_b32 s40, s40, vcc_lo\n\t"
        "s_branch .Lo23%=\n\t"
        ".Lo24%=:\n\t"
        "s_lshr_b32 s84, s44, 1\n\t"
        "s_lshl_b32 s84, s84, 18\n\t"
        "s_and_b32 s80, s44, 1\n\t"
        "s_lshl_b32 s80, s80, 12\n\t"
        ".Lhk5%=:\n\t"
        "s_cbranch_execz .Lhk6%=\n\t"
        "s_or_b32 s84, s84, s80\n\t"
        "buffer_load_dwordx4 v[0:3], v240, %[rd], s84 offen offset:0 sc1\n\t"
        "buffer_load_dwordx4 v[4:7], v240, %[rd], s84 offen offset:1024 sc1\n\t"
        "buffer_load_dwordx4 v[8:11], v240, %[rd], s84 offen offset:2048 sc1\n\t"
        "buffer_load_dwordx4 v[12:15], v240, %[rd], s84 offen offset:3072 sc1\n\t"
        "s_add_u32 s84, s84, 8192\n\t"
        "buffer_load_dwordx4 v[16:19], v240, %[rd], s84 offen offset:0 sc1\n\t"
        "buffer_load_dwordx4 v[20:23], v240, %[rd], s84 offen offset:1024 sc1\n\t"
        ".Lhk6%=:\n\t"
        "s_cbranch_execz .Lhk7%=\n\t"
        "buffer_load_dwordx4 v[24:27], v240, %[rd], s84 offen offset:2048 sc1\n\t"
        "buffer_load_dwordx4 v[28:31], v240, %[rd], s84 offen offset:3072 sc1\n\t"
        "s_add_u32 s84, s84, 8192\n\t"
        "buffer_load_dwordx4 v[32:35], v240, %[rd], s84 offen offset:0 sc1\n\t"
        "buffer_load_dwordx4 v[36:39], v240, %[rd], s84 offen offset:1024 sc1\n\t"
        "buffer_load_dwordx4 v[40:43], v240, %[rd], s84 offen offset:2048 sc1\n\t"
        "buffer_load_dwordx4 v[44:47], v240, %[rd], s84 offen offset:3072 sc1\n\t"
        "s_add_u32 s84, s84, 8192\n\t"
        ".Lhk7%=:\n\t"
        "s_cbranch_execz .Lhk8%=\n\t"
        "buffer_load_dwordx4 v[48:51], v240, %[rd], s84 offen offset:0 sc1\n\t"
        "buffer_load_dwordx4 v[52:55], v240, %[rd], s84 offen offset:1024 sc1\n\t"
        "buffer_load_dwordx4 v[56:59], v240, %[rd], s84 offen offset:2048 sc1\n\t"
        "buffer_load_dwordx4 v[60:63], v240, %[rd], s84 offen offset:3072 sc1\n\t"
        "s_sub_u32 s84, s84, 20480\n\t"
        "s_waitcnt vmcnt(15)\n\t"
        "v_mfma_f32_16x16x32_bf16 v[128:131], a[128:131], v[0:3], 0\n\t"
        "v_mfma_f32_16x16x32_bf16 v[132:135], a[132:135], v[0:3], 0\n\t"
        ".Lhk8%=:\n\t"
        "s_cbranch_execz .Lhk9%=\n\t"
        "v_mfma_f32_16x16x32_bf16 v[136:139], a[136:139], v[0:3], 0\n\t"
        "v_mfma_f32_16x16x32_bf16 v[140:143], a[140:143], v[0:3], 0\n\t"
        "s_waitcnt vmcnt(14)\n\t"
        "v_mfma_f32_16x16x32_bf16 v[128:131], a[144:147], v[4:7], v[128:131]\n\t"
        "v_mfma_f32_16x16x32_bf16 v[132:135], a[148:151], v[4:7], v[132:135]\n\t"
        "v_mfma_f32_16x16x32_bf16 v[136:139], a[152:155], v[4:7], v[136:139]\n\t"
        "v_mfma_f32_16x16x32_bf16 v[140:143], a[156:159], v[4:7], v[140:143]\n\t"
        "s_waitcnt vmcnt(13)\n\t"
        ".Lhk9%=:\n\t"
        "s_cbranch_execz .Lhk10%=\n\t"
        "v_mfma_f32_16x16x32_bf16 v[128:131], a[160:163], v[8:11], v[128:131]\n\t"
        "v_mfma_f32_16x16x32_bf16 v[132:135], a[164:167], v[8:11], v[132:135]\n\t"
        "v_mfma_f32_16x16x32_bf16 v[136:139], a[168:171], v[8:11], v[136:139]\n\t"
        "v_mfma_f32_16x16x32_bf16 v[140:143], a[172:175], v[8:11], v[140:143]\n\t"
        "s_waitcnt vmcnt(12)\n\t"
        "v_mfma_f32_16x16x32_bf16 v[128:131], a[176:179], v[12:15], v[128:131]\n\t"
        "v_mfma_f32_16x16x32_bf16 v[132:135], a[180:183], v[12:15], v[132:135]\n\t"
        "v_mfma_f32_16x16x32_bf16 v[136:139], a[184:187], v[12:15], v[136:139]\n\t"
        ".Lhk10%=:\n\t"
        "s_cbranch_execz .Lhk11%=\n\t"
        "v_mfma_f32_16x16x32_bf16 v[140:143], a[188:191], v[12:15], v[140:143]\n\t"
        "s_waitcnt vmcnt(11)\n\t"
        "v_mfma_f32_16x16x32_bf16 v[128:131], a[192:195], v[16:19], v[128:131]\n\t"
        "v_mfma_f32_16x16x32_bf16 v[132:135], a[196:199], v[16:19], v[132:135]\n\t"
        "v_mfma_f32_16x16x32_bf16 v[136:139], a[200:203], v[16:19], v[136:139]\n\t"
        "v_mfma_f32_16x16x32_bf16 v[140:143], a[204:207], v[16:19], v[140:143]\n\t"
        "s_waitcnt vmcnt(10)\n\t"
        "v_mfma_f32_16x16x32_bf16 v[128:131], a[208:211], v[20:23], v[128:131]\n\t"
        ".Lhk11%=:\n\t"
        "s_cbranch_execz .Lhk12%=\n\t"
        "v_mfma_f32_16x16x32_bf16 v[132:135], a[212:215], v[20:23], v[132:135]\n\t"
        "v_mfma_f32_16x16x32_bf16 v[136:139], a[216:219], v[20:23], v[136:139]\n\t"
        "v_mfma_f32_16x16x32_bf16 v[140:143], a[220:223], v[20:23], v[140:143]\n\t"
        "s_waitcnt vmcnt(9)\n\t"
        "v_mfma_f32_16x16x32_bf16 v[128:131], a[224:227], v[24:27], v[128:131]\n\t"
        "v_mfma_f32_16x16x32_bf16 v[132:135], a[228:231], v[24:27], v[132:135]\n\t"
        "v_mfma_f32_16x16x32_bf16 v[136:139], a[232:235], v[24:27], v[136:139]\n\t"
        "v_mfma_f32_16x16x32_bf16 v[140:143], a[236:239], v[24:27], v[140:143]\n\t"
        ".Lhk12%=:\n\t"
        "s_cbranch_execz .Lhk13%=\n\t"
        "s_waitcnt vmcnt(8)\n\t"
        "v_mfma_f32_16x16x32_bf16 v[128:131], a[240:243], v[28:31], v[128:131]\n\t"
        "v_mfma_f32_16x16x32_bf16 v[132:135], a[244:247], v[28:31], v[132:135]\n\t"
        "v_mfma_f32_16x16x32_bf16 v[136:139], a[248:251], v[28:31], v[136:139]\n\t"
        "v_mfma_f32_16x16x32_bf16 v[140:143], a[252:255], v[28:31], v[140:143]\n\t"
        "s_waitcnt vmcnt(7)\n\t"
        "v_mfma_f32_16x16x32_bf16 v[128:131], a[0:3], v[32:35], v[128:131]\n\t"
        "v_mfma_f32_16x16x32_bf16 v[132:135], a[4:7], v[32:35], v[132:135]\n\t"
        ".Lhk13%=:\n\t"
        "s_cbranch_execz .Lhk14%=\n\t"
        "v_mfma_f32_16x16x32_bf16 v[136:139], a[8:11], v[32:35], v[136:139]\n\t"
        "v_mfma_f32_16x16x32_bf16 v[140:143], a[12:15], v[32:35], v[140:143]\n\t"
        "s_waitcnt vmcnt(6)\n\t"
        "v_mfma_f32_16x16x32_bf16 v[128:131], a[16:19], v[36:39], v[128:131]\n\t"
        "v_mfma_f32_16x16x32_bf16 v[132:135], a[20:23], v[36:39], v[132:135]\n\t"
        "v_mfma_f32_16x16x32_bf16 v[136:139], a[24:27], v[36:39], v[136:139]\n\t"
        "v_mfma_f32_16x16x32_bf16 v[140:143], a[28:31], v[36:39], v[140:143]\n\t"
        "s_waitcnt vmcnt(5)\n\t"
        ".Lhk14%=:\n\t"
        "s_cbranch_execz .Lhk15%=\n\t"
        "v_mfma_f32_16x16x32_bf16 v[128:131], a[32:35], v[40:43], v[128:131]\n\t"
        "v_mfma_f32_16x16x32_bf16 v[132:135], a[36:39], v[40:43], v[132:135]\n\t"
        "v_mfma_f32_16x16x32_bf16 v[136:139], a[40:43], v[40:43], v[136:139]\n\t"
        "v_mfma_f32_16x16x32_bf16 v[140:143], a[44:47], v[40:43], v[140:143]\n\t"
        "s_waitcnt vmcnt(4)\n\t"
        "v_mfma_f32_16x16x32_bf16 v[128:131], a[48:51], v[44:47], v[128:131]\n\t"
        "v_mfma_f32_16x16x32_bf16 v[132:135], a[52:55], v[44:47], v[132:135]\n\t"
        "v_mfma_f32_16x16x32_bf16 v[136:139], a[56:59], v[44:47], v[136:139]\n\t"
        ".Lhk15%=:\n\t"
        "s_cbranch_execz .Lhk16%=\n\t"
        "v_mfma_f32_16x16x32_bf16 v[140:143], a[60:63], v[44:47], v[140:143]\n\t"
        "s_waitcnt vmcnt(3)\n\t"
        "v_mfma_f32_16x16x32_bf16 v[128:131], a[64:67], v[48:51], v[128:131]\n\t"
        "v_mfma_f32_16x16x32_bf16 v[132:135], a[68:71], v[48:51], v[132:135]\n\t"
        "v_mfma_f32_16x16x32_bf16 v[136:139], a[72:75], v[48:51], v[136:139]\n\t"
        "v_mfma_f32_16x16x32_bf16 v[140:143], a[76:79], v[48:51], v[140:143]\n\t"
        "s_waitcnt vmcnt(2)\n\t"
        "v_mfma_f32_16x16x32_bf16 v[128:131], a[80:83], v[52:55], v[128:131]\n\t"
        ".Lhk16%=:\n\t"
        "s_cbranch_execz .Lhk17%=\n\t"
        "v_mfma_f32_16x16x32_bf16 v[132:135], a[84:87], v[52:55], v[132:135]\n\t"
        "v_mfma_f32_16x16x32_bf16 v[136:139], a[88:91], v[52:55], v[136:139]\n\t"
        "v_mfma_f32_16x16x32_bf16 v[140:143], a[92:95], v[52:55], v[140:143]\n\t"
        "s_waitcnt vmcnt(1)\n\t"
        "v_mfma_f32_16x16x32_bf16 v[128:131], a[96:99], v[56:59], v[128:131]\n\t"
        "v_mfma_f32_16x16x32_bf16 v[132:135], a[100:103], v[56:59], v[132:135]\n\t"
        "v_mfma_f32_16x16x32_bf16 v[136:139], a[104:107], v[56:59], v[136:139]\n\t"
        "v_mfma_f32_16x16x32_bf16 v[140:143], a[108:111], v[56:59], v[140:143]\n\t"
        ".Lhk17%=:\n\t"
        "s_cbranch_execz .Lhk18%=\n\t"
        "s_waitcnt vmcnt(0)\n\t"
        "v_mfma_f32_16x16x32_bf16 v[128:131], a[112:115], v[60:63], v[128:131]\n\t"
        "v_mfma_f32_16x16x32_bf16 v[132:135], a[116:119], v[60:63], v[132:135]\n\t"
        "v_mfma_f32_16x16x32_bf16 v[136:139], a[120:123], v[60:63], v[136:139]\n\t"
        "v_mfma_f32_16x16x32_bf16 v[140:143], a[124:127], v[60:63], v[140:143]\n\t"
        "s_nop 4\n\t"
        "ds_write_b128 v222, v[128:131] offset:0\n\t"
        "ds_write_b128 v222, v[132:135] offset:1040\n\t"
        ".Lhk18%=:\n\t"
        "s_cbranch_execz .Lhk19%=\n\t"
        "ds_write_b128 v222, v[136:139] offset:2080\n\t"
        "ds_write_b128 v222, v[140:143] offset:3120\n\t"
        "s_waitcnt lgkmcnt(0)\n\t"
        "s_barrier\n\t"
        "ds_read_b128 v[186:189], v223 offset:0\n\t"
        "ds_read_b128 v[190:193], v223 offset:8320\n\t"
        "ds_read_b128 v[194:197], v223 offset:16640\n\t"
        "ds_read_b128 v[198:201], v223 offset:24960\n\t"
        ".Lhk19%=:\n\t"
        "s_cbranch_execz .Lhk20%=\n\t"
        "s_waitcnt lgkmcnt(0)\n\t"
        "v_add_f32_e32 v186, v186, v190\n\t"
        "v_add_f32_e32 v187, v187, v191\n\t"
        "v_add_f32_e32 v188, v188, v192\n\t"
        "v_add_f32_e32 v189, v189, v193\n\t"
        "v_add_f32_e32 v194, v194, v198\n\t"
        "v_add_f32_e32 v195, v195, v199\n\t"
        "v_add_f32_e32 v196, v196, v200\n\t"
        ".Lhk20%=:\n\t"
        "s_cbranch_execz .Lhk21%=\n\t"
        "v_add_f32_e32 v197, v197, v201\n\t"
        "v_add_f32_e32 v204, v186, v194\n\t"
        "v_add_f32_e32 v206, v187, v195\n\t"
        "v_add_f32_e32 v208, v188, v196\n\t"
        "v_add_f32_e32 v210, v189, v197\n\t"
        "s_add_i32 s80, s44, 0\n\t"
        "s_lshl_b32 s80, s80, 17\n\t"
        "s_or_b32 s81, s44, %[own]\n\t"
        ".Lhk21%=:\n\t"
        "s_cbranch_execz .Lhk22%=\n\t"
        "s_cmp_lt_i32 s81, 0\n\t"
        "s_cselect_b32 s82, s36, s80\n\t"
        "buffer_store_dwordx4 v[204:207], v246, %[rd], s82 offen offset:0 sc1\n\t"
        "buffer_store_dwordx4 v[208:211], v246, %[rd], s82 offen offset:1024 sc1\n\t"
        "v_mov_b32_e32 v144, v204\n\t"
        "v_mov_b32_e32 v145, v206\n\t"
        "v_mov_b32_e32 v146, v208\n\t"
        "v_mov_b32_e32 v147, v210\n\t"
        ".Lhk22%=:\n\t"
        "s_cbranch_execz .Lhk23%=\n\t"
        "s_sleep %c[xdel]\n\t"
        "s_add_i32 s80, s44, 0\n\t"
        "s_lshl_b32 s80, s80, 17\n\t"
        "s_or_b32 s81, s44, %[snd]\n\t"
        "s_cmp_lt_i32 s81, 0\n\t"
        "s_cselect_b32 s82, s36, s80\n\t"
        "buffer_load_dwordx4 v[160:163], v246, %[rd], s82 offen offset:0 sc1\n\t"
        "buffer_load_dwordx4 v[164:167], v246, %[rd], s82 offen offset:1024 sc1\n\t"
        ".Lhk23%=:\n\t"
        "s_cbranch_execz .Lhk24%=\n\t"
        "s_waitcnt vmcnt(0)\n\t"
        "s_add_i32 s80, s44, 0\n\t"
        "s_lshl_b32 s80, s80, 15\n\t"
        "s_or_b32 s81, s44, %[snd]\n\t"
        "s_cmp_lt_i32 s81, 0\n\t"
        "s_cselect_b32 s83, s36, s80\n\t"
        "s_cbranch_scc1 .Lo27%=\n\t"
        "s_mov_b32 s97, 0\n\t"
        ".Lo25%=:\n\t"
        ".Lhk24%=:\n\t"
        "s_cbranch_execz .Lhk25%=\n\t"
        "v_xor_b32_e32 v186, %[tag], v161\n\t"
        "v_xor_b32_e32 v187, %[tag], v163\n\t"
        "v_xor_b32_e32 v188, %[tag], v165\n\t"
        "v_or3_b32 v186, v186, v187, v188\n\t"
        "v_xor_b32_e32 v187, %[tag], v167\n\t"
        "v_or_b32_e32 v186, v186, v187\n\t"
        "v_cmp_ne_u32_e32 vcc, 0, v186\n\t"
        "v_sub_f32_e32 v198, 1.0, v184\n\t"
        ".Lhk25%=:\n\t"
        "s_cbranch_execz .Lhk26%=\n\t"
        "v_add_f32_e32 v190, v144, v160\n\t"
        "v_add_f32_e32 v191, v145, v162\n\t"
        "v_add_f32_e32 v192, v146, v164\n\t"
        "v_add_f32_e32 v193, v147, v166\n\t"
        "s_cbranch_vccz .Lo26%=\n\t"
        "s_sleep %c[xslp]\n\t"
        "s_lshl_b32 s82, s83, 2\n\t"
        "buffer_load_dwordx4 v[160:163], v246, %[rd], s82 offen offset:0 sc1\n\t"
        ".Lhk26%=:\n\t"
        "s_cbranch_execz .Lhk27%=\n\t"
        "buffer_load_dwordx4 v[164:167], v246, %[rd], s82 offen offset:1024 sc1\n\t"
        "s_add_u32 s97, s97, 1\n\t"
        "s_cmp_lt_u32 s97, 0x200000\n\t"
        "s_cbranch_scc0 .Lgu%=\n\t"
        "s_waitcnt vmcnt(0)\n\t"
        "s_branch .Lo25%=\n\t"
        ".Lo26%=:\n\t"
        "v_cvt_pk_bf16_f32 v198, 0, v198\n\t"
        "v_cvt_pk_bf16_f32 v190, 0, v190\n\t"
        ".Lhk27%=:\n\t"
        "s_cbranch_execz .Lhk28%=\n\t"
        "v_cvt_pk_bf16_f32 v191, 0, v191\n\t"
        "v_cvt_pk_bf16_f32 v192, 0, v192\n\t"
        "v_cvt_pk_bf16_f32 v193, 0, v193\n\t"
        "v_lshlrev_b32_e32 v186, 16, v180\n\t"
        "v_lshlrev_b32_e32 v194, 16, v176\n\t"
        "v_and_b32_e32 v187, 0xffff0000, v180\n\t"
        "v_and_b32_e32 v195, 0xffff0000, v176\n\t"
        "v_lshlrev_b32_e32 v188, 16, v181\n\t"
        ".Lhk28%=:\n\t"
        "s_cbranch_execz .Lhk29%=\n\t"
        "v_lshlrev_b32_e32 v196, 16, v177\n\t"
        "v_and_b32_e32 v189, 0xffff0000, v181\n\t"
        "v_and_b32_e32 v197, 0xffff0000, v177\n\t"
        "v_mul_f32_e32 v186, v186, v194\n\t"
        "v_mul_f32_e32 v187, v187, v195\n\t"
        "v_mul_f32_e32 v188, v188, v196\n\t"
        "v_mul_f32_e32 v189, v189, v197\n\t"
        "v_mul_f32_e32 v190, v198, v190\n\t"
        ".Lhk29%=:\n\t"
        "s_cbranch_execz .Lhk30%=\n\t"
        "v_mul_f32_e32 v191, v198, v191\n\t"
        "v_mul_f32_e32 v192, v198, v192\n\t"
        "v_mul_f32_e32 v193, v198, v193\n\t"
        "v_cvt_pk_bf16_f32 v186, 0, v186\n\t"
        "v_cvt_pk_bf16_f32 v187, 0, v187\n\t"
        "v_cvt_pk_bf16_f32 v188, 0, v188\n\t"
        "v_cvt_pk_bf16_f32 v189, 0, v189\n\t"
        "v_cvt_pk_bf16_f32 v190, 0, v190\n\t"
        ".Lhk30%=:\n\t"
        "s_cbranch_execz .Lhk31%=\n\t"
        "v_cvt_pk_bf16_f32 v191, 0, v191\n\t"
        "v_cvt_pk_bf16_f32 v192, 0, v192\n\t"
        "v_cvt_pk_bf16_f32 v193, 0, v193\n\t"
        "v_mul_f32_e32 v186, v184, v186\n\t"
        "v_mul_f32_e32 v187, v184, v187\n\t"
        "v_mul_f32_e32 v188, v184, v188\n\t"
        "v_mul_f32_e32 v189, v184, v189\n\t"
        "v_cvt_pk_bf16_f32 v186, 0, v186\n\t"
        ".Lhk31%=:\n\t"
        "s_cbranch_execz .Lhk32%=\n\t"
        "v_cvt_pk_bf16_f32 v187, 0, v187\n\t"
        "v_cvt_pk_bf16_f32 v188, 0, v188\n\t"
        "v_cvt_pk_bf16_f32 v189, 0, v189\n\t"
        "v_add_f32_e32 v186, v186, v190\n\t"
        "v_add_f32_e32 v187, v187, v191\n\t"
        "v_add_f32_e32 v188, v188, v192\n\t"
        "v_add_f32_e32 v189, v189, v193\n\t"
        "v_cvt_pk_bf16_f32 v200, v186, v187\n\t"
        ".Lhk32%=:\n\t"
        "s_cbranch_execz .Lhk33%=\n\t"
        "v_cvt_pk_bf16_f32 v201, v188, v189\n\t"
        ".Lo27%=:\n\t"
        "buffer_store_dwordx2 v[200:201], v245, %[ro], s83 offen\n\t"
        "s_add_u32 s37, s37, 1\n\t"
        "s_sub_u32 s43, 33280, s43\n\t"
        "s_cmp_lt_u32 s37, 32\n\t"
        "s_cbranch_scc1 .Lone%=\n\t"
        "buffer_store_dword v205, off, %[rd], %[ep] sc1\n\t"
        "s_endpgm\n\t"
        ".Lo13%=:\n\t"
        "s_mov_b32 s97, 0\n\t"
        "s_lshl_b32 s82, s83, 2\n\t"
        ".Lo13%=r:\n\t"
        "s_sleep %c[xslp]\n\t"
        "buffer_load_dwordx4 v[160:163], v246, %[rd], s82 offen offset:0 sc1\n\t"
        "buffer_load_dwordx4 v[164:167], v246, %[rd], s82 offen offset:1024 sc1\n\t"
        "s_add_u32 s97, s97, 1\n\t"
        "s_cmp_lt_u32 s97, 0x200000\n\t"
        "s_cbranch_scc0 .Lgu%=\n\t"
        "s_waitcnt vmcnt(0)\n\t"
        "v_xor_b32_e32 v224, %[tag], v161\n\t"
        "v_xor_b32_e32 v225, %[tag], v163\n\t"
        "v_xor_b32_e32 v226, %[tag], v165\n\t"
        "v_or3_b32 v224, v224, v225, v226\n\t"
        "v_xor_b32_e32 v225, %[tag], v167\n\t"
        "v_or_b32_e32 v224, v224, v225\n\t"
        "v_cmp_ne_u32_e32 vcc, 0, v224\n\t"
        "s_nop 3\n\t"
        "s_cbranch_vccnz .Lo13%=r\n\t"
        "s_branch .Lo14%=\n\t"
        ".Lo15%=:\n\t"
        "s_mov_b32 s97, 0\n\t"
        "s_lshl_b32 s82, s83, 2\n\t"
        ".Lo15%=r:\n\t"
        "s_sleep %c[xslp]\n\t"
        "buffer_load_dwordx4 v[168:171], v246, %[rd], s82 offen offset:0 sc1\n\t"
        "buffer_load_dwordx4 v[172:175], v246, %[rd], s82 offen offset:1024 sc1\n\t"
        "s_add_u32 s97, s97, 1\n\t"
        "s_cmp_lt_u32 s97, 0x200000\n\t"
        "s_cbranch_scc0 .Lgu%=\n\t"
        "s_waitcnt vmcnt(0)\n\t"
        "v_xor_b32_e32 v224, %[tag], v169\n\t"
        "v_xor_b32_e32 v225, %[tag], v171\n\t"
        "v_xor_b32_e32 v226, %[tag], v173\n\t"
        "v_or3_b32 v224, v224, v225, v226\n\t"
        "v_xor_b32_e32 v225, %[tag], v175\n\t"
        "v_or_b32_e32 v224, v224, v225\n\t"
        "v_cmp_ne_u32_e32 vcc, 0, v224\n\t"
        "s_nop 3\n\t"
        "s_cbranch_vccnz .Lo15%=r\n\t"
        "s_branch .Lo16%=\n\t"
        ".Lgu%=:\n\t"
        "v_mov_b32_e32 v0, 128\n\t"
        "s_mov_b32 s80, %c[ferr]\n\t"
        "buffer_store_dword v0, off, %[rd], s80 sc1\n\t"
        "s_endpgm\n\t"
        ".Lhk33%=:\n\t"
        "s_branch .Lhr%="
        :: [rd] "s"(rd), [ro] "s"(ro), [rx] "s"(rx), [w] "s"(w), [rho] "s"(rho), [tag] "s"(tag), [own] "s"(own), [snd] "s"(snd), [ep] "s"(ep), [blk] "s"(j), [t0] "s"(g_t0),
          [ferr] "i"(F_ERR), [fprep] "i"(F_PREP), [fconv] "i"(F_CONV), [fprog] "i"(F_PROG), [dbg] "i"(W_DBG), [oslp] "i"(O_SLP), [xslp] "i"(X_SLP), [xdel] "i"(X_DEL), [ydel] "i"(Y_DEL), "{v[176:191]}"(pc)
        : "memory", "vcc", "scc", "s36", "s37", "s38", "s39", "s40", "s41", "s42", "s43", "s44", "s45", "s46", "s47", "s80", "s81", "s82", "s83", "s84", "s85", "s86", "s87", "s88", "s89", "s90", "s91", "s92", "s93", "s94", "s95", "s96", "s97", "s98", "s99", WO_REGS);
}

__device__ __forceinline__ void bulk_block(const Params& p, rsrc_t r, unsigned char* lds, const int wv, const int j, const unsigned tag, const unsigned g_t0) {
    const int x = (j & 7) >> 1, i32 = (j >> 3) * 2 + (j & 1);
    auto flags = [&](int mt, int nmt, int h0, int nh, int mat) {
        DRAIN(); __syncthreads();
        const int tid = otid(); if (tid < nmt * nh) st4(r, F_KVQ + ((mt + tid / nh) * 32 + h0 + tid % nh) * 64 + mat * 4, tag); };
#pragma unroll 1
    for (int t = 0; t < 2; ++t) {
        const int mt = 2 * t + (i32 & 1), mat = (i32 >> 1) & 1, hd = 8 * x + (i32 >> 2), rot = ((i32 >> 1) & 7) * 2;
        const bf16_t* Bm = mat ? p.Wv : p.Wk;
        if (mat) gemm<1, 4, 4, 8>(r, p.Aq, Bm, mt * 128, hd * 128, lds, W_VF, wv, rot, nullptr, t == 1, t == 0 ? Bm : nullptr, (mt + 2) * 128, hd * 128);
        else gemm<0, 4, 4, 8>(r, p.Aq, Bm, mt * 128, hd * 128, lds, W_KX, wv, rot, nullptr, t == 1, t == 0 ? Bm : nullptr, (mt + 2) * 128, hd * 128);
        flags(mt, 1, hd, 1, mat);
    }
    const int hp = 4 * x + (i32 >> 3), q8 = i32 & 7, rot = ROT2 * (i32 >> 2);
    if (SKEW) wait_long(rtnow() + (unsigned)((q8 & 3) * 4 + x) * SKEW);
    {
        const int m = q8 < 4 ? 4 + (q8 & 1) : q8 - 4, mat = q8 < 4 ? q8 >> 1 : 2;
        const bf16_t* Bn = mat == 2 ? p.Wq : mat == 1 ? p.Wv : p.Wk;
        const int mt = 4 + (q8 & 3), mv = q8 >> 2;
        const bf16_t* tk = (wv == 1 && j < 32) ? p.Aq + (64 + j) * 16384 : nullptr;
        if (mv) gemm<1, 4, 8, 4, 0, NoHook, 8>(r, p.Aq, p.Wv, mt * 128, hp * 256, lds, W_VF, wv, rot, tk, false, Bn, m * 256, hp * 256);
        else gemm<0, 4, 8, 4, 0, NoHook, 8>(r, p.Aq, p.Wk, mt * 128, hp * 256, lds, W_KX, wv, rot, tk, false, Bn, m * 256, hp * 256);
        flags(mt, 1, 2 * hp, 2, mv);
        const bf16_t* tl = (wv == 1 && j < 32) ? p.Aq + (96 + j) * 16384 : nullptr;
        if (mat == 1) gemm<1, 8, 8, 4>(r, p.Aq, Bn, m * 256, hp * 256, lds, W_VF, wv, rot, tl, true);
        else gemm<0, 8, 8, 4>(r, p.Aq, Bn, m * 256, hp * 256, lds, mat == 2 ? W_QX : W_KX, wv, rot, tl, true);
        flags(2 * m, 2, 2 * hp, 2, mat);
    }
    {
        unsigned wi = 0u;
        const int l = otid() & 63; const v4u sw = mk_srd(p.Wo, 4096 * 1024 * 2); const unsigned wa = (unsigned)(((i32 & 1) * 2048 + wv * 512 + (l >> 3)) * 2048 + (i32 >> 1) * 128 + (l & 7) * 16);
        unsigned long long tab;
        asm volatile("s_getpc_b64 %0\n\ts_branch .Lwo%=\n\t"
                     "buffer_load_dwordx4 a[128:131], v191, s[92:95], s91 offen\n\ts_setpc_b64 s[96:97]\n\t" "buffer_load_dwordx4 a[132:135], v191, s[92:95], s91 offen\n\ts_setpc_b64 s[96:97]\n\t"
                     "buffer_load_dwordx4 a[136:139], v191, s[92:95], s91 offen\n\ts_setpc_b64 s[96:97]\n\t" "buffer_load_dwordx4 a[140:143], v191, s[92:95], s91 offen\n\ts_setpc_b64 s[96:97]\n\t"
                     "buffer_load_dwordx4 a[144:147], v191, s[92:95], s91 offen\n\ts_setpc_b64 s[96:97]\n\t" "buffer_load_dwordx4 a[148:151], v191, s[92:95], s91 offen\n\ts_setpc_b64 s[96:97]\n\t"
                     "buffer_load_dwordx4 a[152:155], v191, s[92:95], s91 offen\n\ts_setpc_b64 s[96:97]\n\t" "buffer_load_dwordx4 a[156:159], v191, s[92:95], s91 offen\n\ts_setpc_b64 s[96:97]\n\t"
                     "buffer_load_dwordx4 a[160:163], v191, s[92:95], s91 offen\n\ts_setpc_b64 s[96:97]\n\t" "buffer_load_dwordx4 a[164:167], v191, s[92:95], s91 offen\n\ts_setpc_b64 s[96:97]\n\t"
                     "buffer_load_dwordx4 a[168:171], v191, s[92:95], s91 offen\n\ts_setpc_b64 s[96:97]\n\t" "buffer_load_dwordx4 a[172:175], v191, s[92:95], s91 offen\n\ts_setpc_b64 s[96:97]\n\t"
                     "buffer_load_dwordx4 a[176:179], v191, s[92:95], s91 offen\n\ts_setpc_b64 s[96:97]\n\t" "buffer_load_dwordx4 a[180:183], v191, s[92:95], s91 offen\n\ts_setpc_b64 s[96:97]\n\t"
                     "buffer_load_dwordx4 a[184:187], v191, s[92:95], s91 offen\n\ts_setpc_b64 s[96:97]\n\t" "buffer_load_dwordx4 a[188:191], v191, s[92:95], s91 offen\n\ts_setpc_b64 s[96:97]\n\t"
                     "buffer_load_dwordx4 a[192:195], v191, s[92:95], s91 offen\n\ts_setpc_b64 s[96:97]\n\t" "buffer_load_dwordx4 a[196:199], v191, s[92:95], s91 offen\n\ts_setpc_b64 s[96:97]\n\t"
                     "buffer_load_dwordx4 a[200:203], v191, s[92:95], s91 offen\n\ts_setpc_b64 s[96:97]\n\t" "buffer_load_dwordx4 a[204:207], v191, s[92:95], s91 offen\n\ts_setpc_b64 s[96:97]\n\t"
                     "buffer_load_dwordx4 a[208:211], v191, s[92:95], s91 offen\n\ts_setpc_b64 s[96:97]\n\t" "buffer_load_dwordx4 a[212:215], v191, s[92:95], s91 offen\n\ts_setpc_b64 s[96:97]\n\t"
                     "buffer_load_dwordx4 a[216:219], v191, s[92:95], s91 offen\n\ts_setpc_b64 s[96:97]\n\t" "buffer_load_dwordx4 a[220:223], v191, s[92:95], s91 offen\n\ts_setpc_b64 s[96:97]\n\t"
                     "buffer_load_dwordx4 a[224:227], v191, s[92:95], s91 offen\n\ts_setpc_b64 s[96:97]\n\t" "buffer_load_dwordx4 a[228:231], v191, s[92:95], s91 offen\n\ts_setpc_b64 s[96:97]\n\t"
                     "buffer_load_dwordx4 a[232:235], v191, s[92:95], s91 offen\n\ts_setpc_b64 s[96:97]\n\t" "buffer_load_dwordx4 a[236:239], v191, s[92:95], s91 offen\n\ts_setpc_b64 s[96:97]\n\t"
                     "buffer_load_dwordx4 a[240:243], v191, s[92:95], s91 offen\n\ts_setpc_b64 s[96:97]\n\t" "buffer_load_dwordx4 a[244:247], v191, s[92:95], s91 offen\n\ts_setpc_b64 s[96:97]\n\t"
                     "buffer_load_dwordx4 a[248:251], v191, s[92:95], s91 offen\n\ts_setpc_b64 s[96:97]\n\t" "buffer_load_dwordx4 a[252:255], v191, s[92:95], s91 offen\n\ts_setpc_b64 s[96:97]\n\t"
                     "buffer_load_dwordx4 v[192:195], v191, s[92:95], s91 offen\n\ts_setpc_b64 s[96:97]\n\t" "buffer_load_dwordx4 v[196:199], v191, s[92:95], s91 offen\n\ts_setpc_b64 s[96:97]\n\t"
                     "buffer_load_dwordx4 v[200:203], v191, s[92:95], s91 offen\n\ts_setpc_b64 s[96:97]\n\t" "buffer_load_dwordx4 v[204:207], v191, s[92:95], s91 offen\n\ts_setpc_b64 s[96:97]\n\t"
                     "buffer_load_dwordx4 v[208:211], v191, s[92:95], s91 offen\n\ts_setpc_b64 s[96:97]\n\t" "buffer_load_dwordx4 v[212:215], v191, s[92:95], s91 offen\n\ts_setpc_b64 s[96:97]\n\t"
                     "buffer_load_dwordx4 v[216:219], v191, s[92:95], s91 offen\n\ts_setpc_b64 s[96:97]\n\t" "buffer_load_dwordx4 v[220:223], v191, s[92:95], s91 offen\n\ts_setpc_b64 s[96:97]\n\t"
                     "buffer_load_dwordx4 v[224:227], v191, s[92:95], s91 offen\n\ts_setpc_b64 s[96:97]\n\t" "buffer_load_dwordx4 v[228:231], v191, s[92:95], s91 offen\n\ts_setpc_b64 s[96:97]\n\t"
                     "buffer_load_dwordx4 v[232:235], v191, s[92:95], s91 offen\n\ts_setpc_b64 s[96:97]\n\t" "buffer_load_dwordx4 v[236:239], v191, s[92:95], s91 offen\n\ts_setpc_b64 s[96:97]\n\t"
                     "buffer_load_dwordx4 v[240:243], v191, s[92:95], s91 offen\n\ts_setpc_b64 s[96:97]\n\t" "buffer_load_dwordx4 v[244:247], v191, s[92:95], s91 offen\n\ts_setpc_b64 s[96:97]\n\t"
                     "buffer_load_dwordx4 v[248:251], v191, s[92:95], s91 offen\n\ts_setpc_b64 s[96:97]\n\t" "buffer_load_dwordx4 v[252:255], v191, s[92:95], s91 offen\n\ts_setpc_b64 s[96:97]\n\t"
                     "buffer_load_dwordx4 v191, s[92:95], s91 offen lds\n\ts_setpc_b64 s[96:97]\n.Lwo%=:" : "=s"(tab));
        auto hook = [&](int) {
            const unsigned q = min(wi, 63u);
            asm volatile("s_mov_b32 m0, %1\n\ts_swappc_b64 s[96:97], %0"
                         :: "s"(tab + (min(q, 48u) * 12u + 4u)), "s"(WO_L + wv * 16384 + (q - 48u) * 1024u), "{v191}"(wa), "{s[92:95]}"(sw), "{s91}"(q * 16384u) : "memory", "m0", "s96", "s97", WO_REGS);
            ++wi; };
#pragma unroll 1
        for (int t = 0; t < 2; ++t) {
            const int mt = t ? 8 + q8 : 12 + (q8 & 3), mv = t ? 2 : q8 >> 2;
            if (mv == 1) gemm<1, 4, 8, 4, 1>(r, p.Aq, p.Wv, mt * 128, hp * 256, lds, W_VF, wv, rot, nullptr, false, p.Wq, (8 + q8) * 128, hp * 256, hook);
            else gemm<0, 4, 8, 4, 1>(r, p.Aq, t ? p.Wq : p.Wk, mt * 128, hp * 256, lds, t ? W_QX : W_KX, wv, rot, nullptr, t == 1, t ? nullptr : p.Wq, (8 + q8) * 128, hp * 256, hook);
            flags(mt, 1, 2 * hp, 2, mv);
        }
        oproj_block(p, wv, i32, x, j, tag, g_t0);
    }
}

__device__ __forceinline__ void help_block(const Params& p, rsrc_t r, unsigned char* lds, const int wv, const int k, const unsigned tag, const unsigned far) {
    const int j = -32 + k; (void)j;
    {
        const unsigned th = rtnow() + HELP_D + (unsigned)k * SKEWP, OOB = 0xF0000000u;
        const int l = otid() & 63, n16 = l & 15;
        const rsrc_t rh = __builtin_amdgcn_make_buffer_rsrc(p.ws + hx_base(p.ws), 0, U_END * 16384, 0x00020000);
        const unsigned un = (unsigned)(wv * 32 + (k & 30)) * 16384u + (k & 1) * 4096u, ca = l == 0 ? un : OOB;
        v4u z[4]; unsigned best[4];
#pragma unroll
        for (int i = 0; i < 4; ++i) z[i] = (v4u){~0u, ~0u, ~0u, ~0u};
        auto probe = [&](unsigned q) -> unsigned {
            const unsigned t0 = (unsigned)__builtin_amdgcn_s_memtime(); __builtin_amdgcn_s_waitcnt(0);
            st16(rh, ca + q * 8192, z[0]); DRAIN();
            const unsigned t1 = (unsigned)__builtin_amdgcn_s_memtime(); __builtin_amdgcn_s_waitcnt(0);
            return t1 - t0; };
        st16(rh, l < 4 ? un + l * 8192 : OOB, z[0]); DRAIN();
        const unsigned o = (k >> 1 & 1) * 2;
#pragma unroll
        for (int q = 0; q < 4; ++q) best[q] = probe(q ^ o);
        const unsigned up = min(best[1], best[3]) < min(best[0], best[2]) ? 1u : 0u;
        unsigned b0 = up ? best[0] : best[1], b1 = up ? best[2] : best[3];
        for (int i = 0; i < CAL_N; ++i) { b0 = min(b0, probe((1u - up) ^ o)); b1 = min(b1, probe((3u - up) ^ o)); }
        const unsigned vote = (up ^ far) | ((b1 < b0 ? 2u : 0u) ^ o);
        if (wv == 0 && k < 8) {
#pragma unroll
            for (int i = 0; i < 4; ++i) z[i] = __builtin_elementwise_min(*(const v4u*)(p.mem0 + ((n16 & 1) + 2 * (n16 >> 2)) * 1024 + ((k & 3) * 4 + i) * 64 + (l >> 4) * 16 + (n16 & 2) * 4), (v4u){~1u, ~1u, ~1u, ~1u}); }
#pragma unroll
        for (int i = 0; i < 16; ++i) st16(rh, un + (i >> 2) * 8192 + (i & 3) * 1024 + l * 16, z[i & 3]);
        st4(r, (wv == 3 && l == 0) ? F_PROG + k * 128 : OOB, 0u);
        st16(r, HO_KVM + (63 * 32 + k) * 4096 + wv * 1024 + l * 16, (v4u){~0u, ~0u, ~0u, ~0u});
        DRAIN();
        st8(r, l == 0 ? F_CAL + k * 32 + wv * 8 : OOB, (v2u){tag, vote});
        wait_long(th);
    }
    prep_block(p, r, k, wv, tag);
    if (!conv_block(p, r, k, lds, wv, j, tag, (unsigned)k * SKEWH + (unsigned)wv * SKEWW)) return;
    attn_block(p, wv, k, tag);
}

__global__ void __launch_bounds__(256, 1) k_main(Params p) {
    extern __shared__ __attribute__((aligned(16))) unsigned char lds[];
    const unsigned g_t0 = DIAG ? rtnow() : 0u;
    rsrc_t r = __builtin_amdgcn_make_buffer_rsrc(p.ws, 0, W_TOTAL, 0x00020000);
    const int wv = __builtin_amdgcn_readfirstlane(threadIdx.x >> 6);
    int role = blockIdx.x;
    unsigned xcc; asm volatile("s_getreg_b32 %0, hwreg(HW_REG_XCC_ID)" : "=s"(xcc)); xcc &= 7u;
    unsigned old = 0u, xr = xcc, rk = blockIdx.x >> 3;
    if (MIXH) { if (xcc == 3u) { xr = 4u + (rk >> 3); rk = 24u + (rk & 7u); } else if (xcc > 3u && rk >= 24u) { xr = 3u; rk = (xcc - 4u) * 8u + (rk & 7u); } }
    unsigned slot = (xr >> 2) * 128u + (rk >> 1) * 8u + (xr & 3u) * 2u + (rk & 1u);
    const unsigned tag = (__builtin_amdgcn_readfirstlane(ld4(r, (threadIdx.x & 63) == 1 ? F_CLM + slot * 64 + 4 : F_EP)) * 2654435761u + 0x9E3779B9u) | 1u;
    if (threadIdx.x == 0) old = __hip_atomic_exchange((unsigned*)(p.ws + F_CLM + slot * 64), tag, __ATOMIC_RELAXED, __HIP_MEMORY_SCOPE_AGENT);
    if (threadIdx.x == 0) {
        for (int i = 0; i < 256 && old == tag; ++i) { slot = (slot + 1u) & 255u; old = __hip_atomic_exchange((unsigned*)(p.ws + F_CLM + slot * 64), tag, __ATOMIC_RELAXED, __HIP_MEMORY_SCOPE_AGENT); }
        *(volatile __attribute__((address_space(3))) int*)(size_t)(unsigned)(size_t)lds = (int)slot;
    }
    __syncthreads();
    role = __builtin_amdgcn_readfirstlane(*(volatile __attribute__((address_space(3))) int*)(size_t)(unsigned)(size_t)lds) & 255;
    __syncthreads();
    if (DIAG >= 10 && blockIdx.x < 8 && threadIdx.x == 0) st4(r, F_PROG + 72 + xcc * 128, g_t0);
    if (role >= 128) bulk_block(p, r, lds, wv, role - 128, tag, g_t0);
    else if ((role & 6) == 6) help_block(p, r, lds, wv, (role >> 3) * 2 + (role & 1), tag, xcc >> 2);
    else rec_block(p, wv, role, tag);
}

extern "C" void kernel_launch(void* const* d_in, const int* in_sizes, int n_in, void* d_out, int out_size, void* d_ws, size_t ws_size, hipStream_t stream) {
    Params p;
    p.Aq = (const bf16_t*)d_in[0]; p.Ak = (const bf16_t*)d_in[1]; p.Av = (const bf16_t*)d_in[2];
    p.shift_k = (const bf16_t*)d_in[3]; p.ssm_k = (const bf16_t*)d_in[4]; p.ssm_D = (const bf16_t*)d_in[5];
    p.mem0 = (const bf16_t*)d_in[7]; p.Wq = (const bf16_t*)d_in[8]; p.Wk = (const bf16_t*)d_in[9];
    p.Wv = (const bf16_t*)d_in[10]; p.Wo = (const bf16_t*)d_in[11]; p.gate_w = (const bf16_t*)d_in[13];
    p.out = (bf16_t*)d_out; p.ws = (unsigned char*)d_ws;
    (void)hipFuncSetAttribute((const void*)k_main, hipFuncAttributeMaxDynamicSharedMemorySize, L_TOTAL);
    k_main<<<256, 256, L_TOTAL, stream>>>(p);
}
```
